```python
import torch

def _softmax(x, axis=-1):
    return torch.softmax(x, dim=axis)

def _gelu(x):
    return torch.nn.functional.gelu(x, approximate='tanh')

def _silu(x):
    return torch.nn.functional.silu(x)

def _sigmoid(x):
    return torch.sigmoid(x)

def _layernorm(x, axis=-1, eps=1e-05):
    m = x.mean(dim=axis, keepdim=True)
    v = x.var(dim=axis, keepdim=True, unbiased=False)
    return (x - m) / torch.sqrt(v + eps)

def _scatter_add(tgt, idx, src, axis=0):
    return tgt.clone().index_add_(axis, idx, src)

def _segment_sum(src, seg, n):
    out = torch.zeros((n,) + src.shape[1:], dtype=src.dtype)
    return out.index_add_(0, seg, src)

def _pad(x, pw):
    flat = tuple((v for pair in reversed(pw) for v in pair))
    return torch.nn.functional.pad(x, flat)
kernel_name = 'gated_mix__memory_aug_recurrent_mem_tr__moe_arch_mod__prompt'

def setup_inputs():

    _w = lambda a: (lambda t: t.to(torch.bfloat16) if t.is_floating_point() else t)(torch.as_tensor(a))

    def _A_setup():
        B, S, Dm = (1, 2048, 1024)
        H, Dh = (32, 128)
        M, seg = (8, 32)
        seg = min(seg, S)
        S = S // seg * seg
        return {'x': _w((lambda *s: torch.randn(*s, dtype=torch.bfloat16) * 0.05)(B, S, Dm)), 'mem0': _w((lambda *s: torch.randn(*s, dtype=torch.bfloat16) * 0.8)(B, M, Dm)), 'Wq': _w((lambda *s: torch.randn(*s, dtype=torch.bfloat16) * 3.2)(Dm, H, Dh) * Dm ** (-0.5)), 'Wk': _w((lambda *s: torch.randn(*s, dtype=torch.bfloat16) * 3.2)(Dm, H, Dh) * Dm ** (-0.5)), 'Wv': _w((lambda *s: torch.randn(*s, dtype=torch.bfloat16) * 0.8)(Dm, H, Dh) * Dm ** (-0.5)), 'Wo': _w((lambda *s: torch.randn(*s, dtype=torch.bfloat16) * 0.8)(H, Dh, Dm) * (H * Dh) ** (-0.5)), 'seg_len': seg}
    _A_in = _A_setup()

    def _B_setup():
        B, S, D, F, C = (1, 2048, 1024, 3584, 8)
        return {'x': _w((lambda *s: torch.randn(*s, dtype=torch.bfloat16) * 0.05)(B, S, D)), 'w_r': _w((lambda *s: torch.randn(*s, dtype=torch.bfloat16) * 1.6)(D)), 'w_gate': _w((lambda *s: torch.randn(*s, dtype=torch.bfloat16) * 0.1)(D, F)), 'w_up': _w((lambda *s: torch.randn(*s, dtype=torch.bfloat16) * 0.1)(D, F)), 'w_down': _w((lambda *s: torch.randn(*s, dtype=torch.bfloat16) * 0.1)(F, D))}
    _B_in = _B_setup()
    return {'A_x': _A_in['x'], 'A_mem0': _A_in['mem0'], 'A_Wq': _A_in['Wq'], 'A_Wk': _A_in['Wk'], 'A_Wv': _A_in['Wv'], 'A_Wo': _A_in['Wo'], 'A_seg_len': _A_in['seg_len'], 'B_x': _B_in['x'], 'B_w_r': _B_in['w_r'], 'B_w_gate': _B_in['w_gate'], 'B_w_up': _B_in['w_up'], 'B_w_down': _B_in['w_down'], 'gate_w': _w((lambda *s: torch.randn(*s, dtype=torch.bfloat16) * 0.05)(1024) * 16.0)}

def reference(A_x, A_mem0, A_Wq, A_Wk, A_Wv, A_Wo, A_seg_len, B_x, B_w_r, B_w_gate, B_w_up, B_w_down, gate_w):

    def _A_ref(x, mem0, Wq, Wk, Wv, Wo, seg_len):
        B, S, Dm = x.shape
        H, Dh = (Wq.shape[1], Wq.shape[2])
        M = mem0.shape[1]
        scale = Dh ** (-0.5)
        n_seg = S // seg_len
        L = M + seg_len
        idx = torch.arange(L)
        mask = (idx[:, None] < M) | (idx[:, None] >= idx[None, :])
        mem = mem0
        outs = []
        for s in range(n_seg):
            xs = x[:, s * seg_len:(s + 1) * seg_len, :].clone()
            h = torch.concatenate([mem, xs], axis=1)
            q = torch.einsum('btd,dhe->bhte', h, Wq)
            k = torch.einsum('btd,dhe->bhte', h, Wk)
            v = torch.einsum('btd,dhe->bhte', h, Wv)
            scores = torch.einsum('bhqe,bhke->bhqk', q, k) * scale
            scores = torch.where(mask[None, None], scores, -1000000000.0)
            p = _softmax(scores, axis=-1)
            o = torch.einsum('bhqk,bhke->bhqe', p, v)
            o = torch.einsum('bhte,hed->btd', o, Wo)
            mem = o[:, :M, :].clone()
            outs.append(o[:, M:, :])
        return torch.concatenate(outs, axis=1)

    def _B_ref(x, w_r, w_gate, w_up, w_down):
        B, S, D = x.shape
        C = 8
        r = x @ w_r
        idx = torch.argsort(-r, axis=-1)[:, :C]
        idx_e = (lambda x, n, axis: torch.repeat_interleave(x, n, dim=axis))(idx.reshape(B, C, 1), D, axis=2)
        x_sel = (lambda x, i, axis: torch.take_along_dim(x, i, dim=axis))(x, idx_e, axis=1)
        g = _silu(x_sel @ w_gate)
        u = x_sel @ w_up
        y = g * u @ w_down
        r_sel = _sigmoid((lambda x, i, axis: torch.take_along_dim(x, i, dim=axis))(r, idx, axis=1))
        y = y * r_sel.reshape(B, C, 1)
        b_off = (torch.arange(B) * S).reshape(B, 1)
        flat_idx = (idx + b_off).reshape(B * C)
        zero = (x * 0.0).reshape(B * S, D)
        upd = _scatter_add(zero, flat_idx, y.reshape(B * C, D), axis=0)
        return x + upd.reshape(B, S, D)
    x = A_x
    g = _sigmoid((x * gate_w).sum(-1, keepdims=True))
    a = _A_ref(x, A_mem0, A_Wq, A_Wk, A_Wv, A_Wo, A_seg_len)
    b = _B_ref(x, B_w_r, B_w_gate, B_w_up, B_w_down)
    return g * a + (1 - g) * b
```

```cpp
#include <hip/hip_runtime.h>
#include <hip/hip_bf16.h>
#include <stdint.h>

typedef unsigned short bf16_t;
typedef short frag_ab __attribute__((ext_vector_type(8)));
typedef float frag_cd __attribute__((ext_vector_type(4)));

static constexpr int S_TOK = 2048, DM = 1024, NH = 32, DH = 128, MM = 8, SEG = 32, NSEG = 64, LK = 40;
static constexpr int NQKV = 3 * NH * DH;
static constexpr int HD = NH * DH;
static constexpr int FF = 3584, TOPC = 8;

__device__ __forceinline__ float bf2f(bf16_t v) { return __uint_as_float(((unsigned)v) << 16); }
__device__ __forceinline__ bf16_t f2bf(float f) {
    unsigned u = __float_as_uint(f);
    if ((u & 0x7f800000u) == 0x7f800000u) return (bf16_t)((u >> 16) | ((u & 0xffff) ? 0x40 : 0));
    u += 0x7fffu + ((u >> 16) & 1u);
    return (bf16_t)(u >> 16);
}
__device__ __forceinline__ float rbf(float f) { return bf2f(f2bf(f)); }

struct BSrc { const bf16_t* p0; const bf16_t* p1; const bf16_t* p2; int ncols_each; int pad; };

template <int EPI>
__global__ void __launch_bounds__(256, 2) k_gemm(const bf16_t* __restrict__ A, BSrc B, bf16_t* __restrict__ C, int M, int N, int K,
                                              const bf16_t* __restrict__ x, const float* __restrict__ gate, const float* __restrict__ upd, bf16_t* __restrict__ vtx) {
    constexpr int LD = 72;
    __shared__ __attribute__((aligned(16))) bf16_t sA[128 * LD];
    __shared__ __attribute__((aligned(16))) bf16_t sB[128 * LD];
    const int tid = threadIdx.x, wave = tid >> 6, lane = tid & 63;
    const int m0 = blockIdx.y * 128, n0 = blockIdx.x * 128;
    const int blk = n0 / B.ncols_each; const bf16_t* Bp = blk == 0 ? B.p0 : (blk == 1 ? B.p1 : B.p2); const int nb0 = n0 - blk * B.ncols_each; const int ldb = B.ncols_each;
    const int wm = (wave >> 1) * 64, wn = (wave & 1) * 64;
    frag_cd acc[4][4] = {};
    typedef unsigned v4u_t __attribute__((ext_vector_type(4)));
    v4u_t ra[4], rb[4];
#define gload(k0_) do { \
        _Pragma("unroll") for (int i = 0; i < 4; ++i) { int id = tid + i * 256; int r = id >> 3, c8 = (id & 7) * 8; ra[i] = *(const v4u_t*)(A + (size_t)(m0 + r) * K + (k0_) + c8); } \
        _Pragma("unroll") for (int i = 0; i < 4; ++i) { int id = tid + i * 256; int kk = id >> 4, c8 = (id & 15) * 8; rb[i] = *(const v4u_t*)(Bp + (size_t)((k0_) + kk) * ldb + nb0 + c8); } \
    } while (0)
    gload(0);
    for (int k0 = 0; k0 < K; k0 += 64) {
#pragma unroll
        for (int i = 0; i < 4; ++i) { int id = tid + i * 256; int r = id >> 3, c8 = (id & 7) * 8; *(v4u_t*)&sA[r * LD + c8] = ra[i]; }
#pragma unroll
        for (int i = 0; i < 4; ++i) { int id = tid + i * 256; int kk = id >> 4, c8 = (id & 15) * 8; unsigned wv[4] = {rb[i].x, rb[i].y, rb[i].z, rb[i].w};
#pragma unroll
            for (int e = 0; e < 4; ++e) { sB[(c8 + 2 * e) * LD + kk] = (bf16_t)(wv[e] & 0xffff); sB[(c8 + 2 * e + 1) * LD + kk] = (bf16_t)(wv[e] >> 16); } }
        __syncthreads();
        if (k0 + 64 < K) gload(k0 + 64);
#pragma unroll
        for (int ks = 0; ks < 2; ++ks) {
            frag_ab af[4], bfr[4];
#pragma unroll
            for (int i = 0; i < 4; ++i) { af[i] = *(const frag_ab*)&sA[(wm + i * 16 + (lane & 15)) * LD + ks * 32 + (lane >> 4) * 8]; bfr[i] = *(const frag_ab*)&sB[(wn + i * 16 + (lane & 15)) * LD + ks * 32 + (lane >> 4) * 8]; }
#pragma unroll
            for (int mi = 0; mi < 4; ++mi)
#pragma unroll
                for (int ni = 0; ni < 4; ++ni) acc[mi][ni] = __builtin_amdgcn_mfma_f32_16x16x32_bf16(af[mi], bfr[ni], acc[mi][ni], 0, 0, 0);
        }
        __syncthreads();
    }
#pragma unroll
    for (int mi = 0; mi < 4; ++mi)
#pragma unroll
        for (int ni = 0; ni < 4; ++ni)
#pragma unroll
            for (int r = 0; r < 4; ++r) {
                int m = m0 + wm + mi * 16 + (lane >> 4) * 4 + r, n = n0 + wn + ni * 16 + (lane & 15);
                float v = acc[mi][ni][r];
                if (EPI == 0) { C[(size_t)m * N + n] = f2bf(v); if (n >= 2 * HD) vtx[(((size_t)(m >> 5) * HD) + (n - 2 * HD)) * 32 + (m & 31)] = f2bf(v); }
                else {
                    float a = rbf(v), g = gate[m];
                    float b = rbf(bf2f(x[(size_t)m * N + n]) + upd[(size_t)m * N + n]);
                    float t1 = rbf(g * a), omg = rbf(1.0f - g), t2 = rbf(omg * b);
                    C[(size_t)m * N + n] = f2bf(t1 + t2);
                }
            }
}

__global__ void k_prep(const bf16_t* __restrict__ x, const bf16_t* __restrict__ gate_w, const bf16_t* __restrict__ w_r, float* __restrict__ gate, float* __restrict__ r) {
    int t = blockIdx.x; __shared__ float s1[256], s2[256];
    float a = 0, b = 0;
    for (int k = threadIdx.x; k < DM; k += 256) { float xv = bf2f(x[(size_t)t * DM + k]); a += rbf(xv * bf2f(gate_w[k])); b += xv * bf2f(w_r[k]); }
    s1[threadIdx.x] = a; s2[threadIdx.x] = b; __syncthreads();
    for (int o = 128; o > 0; o >>= 1) { if (threadIdx.x < o) { s1[threadIdx.x] += s1[threadIdx.x + o]; s2[threadIdx.x] += s2[threadIdx.x + o]; } __syncthreads(); }
    if (threadIdx.x == 0) { float z = rbf(s1[0]); gate[t] = rbf(1.0f / (1.0f + __expf(-z))); r[t] = rbf(s2[0]); }
}
__global__ void k_topk(const float* __restrict__ r, int* __restrict__ idx) {
    __shared__ float sv[256]; __shared__ int si[256]; __shared__ int chosen[TOPC];
    for (int c = 0; c < TOPC; ++c) {
        float bv = -INFINITY; int bi = 0x7fffffff;
        for (int t = threadIdx.x; t < S_TOK; t += 256) { bool used = false; for (int q = 0; q < c; ++q) used |= chosen[q] == t;
            float v = r[t]; if (!used && (v > bv || (v == bv && t < bi))) { bv = v; bi = t; } }
        sv[threadIdx.x] = bv; si[threadIdx.x] = bi; __syncthreads();
        for (int o = 128; o > 0; o >>= 1) { if (threadIdx.x < o) { float v2 = sv[threadIdx.x + o]; int i2 = si[threadIdx.x + o];
                if (v2 > sv[threadIdx.x] || (v2 == sv[threadIdx.x] && i2 < si[threadIdx.x])) { sv[threadIdx.x] = v2; si[threadIdx.x] = i2; } } __syncthreads(); }
        if (threadIdx.x == 0) { chosen[c] = si[0]; idx[c] = si[0]; }
        __syncthreads();
    }
}
__global__ void __launch_bounds__(256) k_moe1(const bf16_t* __restrict__ x, const int* __restrict__ idx, const bf16_t* __restrict__ wg, const bf16_t* __restrict__ wu, bf16_t* __restrict__ hbuf) {
    __shared__ float xs[TOPC][DM]; __shared__ float red[2][8][TOPC][32];
    for (int i = threadIdx.x; i < TOPC * DM; i += 256) xs[i / DM][i % DM] = bf2f(x[(size_t)idx[i / DM] * DM + i % DM]);
    __syncthreads();
    int col = threadIdx.x & 31, kp = threadIdx.x >> 5, f = blockIdx.x * 32 + col;
    float ag[TOPC] = {}, au[TOPC] = {};
    for (int k = kp * 128; k < kp * 128 + 128; ++k) { float g = bf2f(wg[(size_t)k * FF + f]), u = bf2f(wu[(size_t)k * FF + f]);
#pragma unroll
        for (int c = 0; c < TOPC; ++c) { ag[c] += xs[c][k] * g; au[c] += xs[c][k] * u; } }
#pragma unroll
    for (int c = 0; c < TOPC; ++c) { red[0][kp][c][col] = ag[c]; red[1][kp][c][col] = au[c]; }
    __syncthreads();
    { int c = threadIdx.x >> 5; float g = 0, u = 0;
#pragma unroll
      for (int q = 0; q < 8; ++q) { g += red[0][q][c][col]; u += red[1][q][c][col]; }
      float gv = rbf(g); float sl = rbf(gv / (1.0f + __expf(-gv))); hbuf[c * FF + f] = f2bf(sl * rbf(u)); }
}
__global__ void __launch_bounds__(256) k_moe2(const bf16_t* __restrict__ hbuf, const int* __restrict__ idx, const float* __restrict__ r, const bf16_t* __restrict__ wd, float* __restrict__ upd) {
    __shared__ float hs[TOPC][FF]; __shared__ float red[8][TOPC][32];
    for (int i = threadIdx.x; i < TOPC * FF; i += 256) hs[i / FF][i % FF] = bf2f(hbuf[i]);
    __syncthreads();
    int col = threadIdx.x & 31, kp = threadIdx.x >> 5, n = blockIdx.x * 32 + col;
    float acc[TOPC] = {};
    for (int k = kp * 448; k < kp * 448 + 448; ++k) { float w = bf2f(wd[(size_t)k * DM + n]);
#pragma unroll
        for (int c = 0; c < TOPC; ++c) acc[c] += hs[c][k] * w; }
#pragma unroll
    for (int c = 0; c < TOPC; ++c) red[kp][c][col] = acc[c];
    __syncthreads();
    { int c = threadIdx.x >> 5; float y = 0;
#pragma unroll
      for (int q = 0; q < 8; ++q) y += red[q][c][col];
      float rs = rbf(1.0f / (1.0f + __expf(-r[idx[c]]))); upd[(size_t)idx[c] * DM + n] = rbf(rbf(y) * rs); }
}

typedef unsigned v2u __attribute__((ext_vector_type(2)));
typedef unsigned v4u __attribute__((ext_vector_type(4)));
typedef unsigned v8u __attribute__((ext_vector_type(8)));
typedef float v2f __attribute__((ext_vector_type(2)));
typedef float v4f __attribute__((ext_vector_type(4)));
typedef __bf16 v2b __attribute__((ext_vector_type(2)));
typedef __bf16 v8b __attribute__((ext_vector_type(8)));
typedef __bf16 v16b __attribute__((ext_vector_type(16)));
typedef unsigned long long u64;
#define LAS __attribute__((address_space(3)))

static constexpr unsigned POI = 0xFFFFFFFFu;
static constexpr unsigned H2_OFF = 0, H1_OFF = 512 * 1024, H3_OFF = 768 * 1024, AN_OFF = 832 * 1024, PR_OFF = 896 * 1024;
static constexpr size_t SLOT = 2u << 20;
static constexpr int NSLOT = 4;
static constexpr unsigned SPIN_CAP = 400000u;

struct ChainArgs {
    const bf16_t* mem0; const bf16_t* Wq; const bf16_t* Wk; const bf16_t* Wv; const bf16_t* Wo; const bf16_t* qkvx; const bf16_t* vtx;
    bf16_t* ox; char* ring; unsigned* ctl; unsigned* dbg;
    int d1, d2, d3, dv, ml, slack, thr, dp;
};

typedef __amdgpu_buffer_rsrc_t rsrc_t;
#define ST16(RS, OFF, SOFF, V) __builtin_amdgcn_raw_buffer_store_b128(V, RS, OFF, SOFF, 16)
#define ST8(RS, OFF, SOFF, V) __builtin_amdgcn_raw_buffer_store_b64(V, RS, OFF, SOFF, 16)
#define ST4(RS, OFF, SOFF, V) __builtin_amdgcn_raw_buffer_store_b32(V, RS, OFF, SOFF, 16)
#define LD16(RS, OFF, SOFF) __builtin_amdgcn_raw_buffer_load_b128(RS, OFF, SOFF, 16)
#define LD8(RS, OFF, SOFF) __builtin_amdgcn_raw_buffer_load_b64(RS, OFF, SOFF, 16)
#define LD4(RS, OFF, SOFF) __builtin_amdgcn_raw_buffer_load_b32(RS, OFF, SOFF, 16)
#define FENCE() asm volatile("" ::: "memory")
#define VM0() asm volatile("s_waitcnt vmcnt(0)" ::: "memory")
#define BAR() asm volatile("s_waitcnt lgkmcnt(0)\n\ts_barrier" ::: "memory")

__device__ __forceinline__ unsigned nearo(unsigned off) { return (off >> 13) * 16384u + (off & 8191u); }
__device__ __forceinline__ unsigned rt32() { u64 t; asm volatile("s_memrealtime %0\n\ts_waitcnt lgkmcnt(0)" : "=s"(t) :: "memory"); return (unsigned)t; }
__device__ __forceinline__ unsigned cyc32() { u64 t; asm volatile("s_memtime %0\n\ts_waitcnt lgkmcnt(0)" : "=s"(t) :: "memory"); return (unsigned)t; }
__device__ __forceinline__ unsigned wait_until(unsigned T, int ml, int thr) {
    unsigned late, tmp;
    T = __builtin_amdgcn_readfirstlane(T);
    asm volatile(
        "s_memrealtime vcc\n\ts_waitcnt lgkmcnt(0)\n\t"
        "s_sub_i32 %[tmp], %[T], vcc_lo\n\t"
        "s_min_i32 %[tmp], %[tmp], 0x200\n\t"
        "s_add_i32 %[late], %[tmp], %[thr]\n\t"
        "s_cmp_lt_i32 %[late], 0\n\t"
        "s_cselect_b32 %[tmp], %[ml], %[tmp]\n\t"
        "s_cselect_b32 %[late], 1, 0\n\t"
        "s_add_i32 %[T], vcc_lo, %[tmp]\n\t"
        "s_cmp_lt_i32 %[tmp], 5\n\t"
        "s_cbranch_scc1 2f\n"
        "1:\n\t"
        "s_memrealtime vcc\n\ts_waitcnt lgkmcnt(0)\n\t"
        "s_sub_i32 %[tmp], %[T], vcc_lo\n\t"
        "s_cmp_gt_i32 %[tmp], 4\n\t"
        "s_cbranch_scc1 1b\n"
        "2:\n\t"
        "s_sub_i32 %[tmp], %[tmp], 2\n"
        "3:\n\t"
        "s_sub_i32 %[tmp], %[tmp], 1\n\t"
        "s_cmp_ge_i32 %[tmp], 0\n\t"
        "s_cbranch_scc1 3b\n\t"
        : [tmp] "=&s"(tmp), [late] "=&s"(late), [T] "+s"(T) : [ml] "s"(ml), [thr] "s"(thr) : "scc", "vcc", "memory");
    return late;
}
__device__ __forceinline__ void lds_wait(volatile LAS unsigned* p, unsigned v) { while ((int)(__builtin_amdgcn_readfirstlane(*p) - v) < 0) __builtin_amdgcn_s_sleep(1); asm volatile("" ::: "memory"); }
__device__ __forceinline__ void lds_post(volatile LAS unsigned* p, unsigned v) { asm volatile("s_waitcnt lgkmcnt(0)" ::: "memory"); *p = v; }
__device__ __forceinline__ bool spin_fail(unsigned tries, unsigned* err) {
    __builtin_amdgcn_s_sleep(1);
    if ((tries & 255u) != 255u) return false;
    unsigned e; asm volatile("global_load_dword %0, %1, off sc1\n\ts_waitcnt vmcnt(0)" : "=&v"(e) : "v"(err) : "memory");
    if (__builtin_amdgcn_readfirstlane(e) == 1u) return true;
    if (tries > SPIN_CAP) { unsigned one = 1u; asm volatile("global_store_dword %0, %1, off sc1" :: "v"(err), "v"(one) : "memory"); return true; }
    return false;
}
__device__ __forceinline__ unsigned max4(unsigned m, v4u r) { m = max(max(m, r[0]), r[1]); return max(max(m, r[2]), r[3]); }
__device__ __forceinline__ bool any_poison(unsigned mx) { return __builtin_amdgcn_ballot_w64(mx == POI) != 0; }
__device__ __forceinline__ v4f smf(v4u a, v8u b, v4f c, int idx) { return __builtin_amdgcn_smfmac_f32_16x16x64_bf16(__builtin_bit_cast(v8b, a), __builtin_bit_cast(v16b, b), c, idx, 0, 0); }
__device__ __forceinline__ v4f mf(v4u a, v4u b, v4f c) { return __builtin_amdgcn_mfma_f32_16x16x32_bf16(__builtin_bit_cast(v8b, a), __builtin_bit_cast(v8b, b), c, 0, 0, 0); }
__device__ __forceinline__ unsigned pk2(float lo, float hi) { v2f f = {lo, hi}; return __builtin_bit_cast(unsigned, __builtin_convertvector(f, v2b)); }
__device__ __forceinline__ float lo16(unsigned u) { return __uint_as_float(u << 16); }
__device__ __forceinline__ float hi16(unsigned u) { return __uint_as_float(u & 0xffff0000u); }
__device__ __forceinline__ unsigned clampp(unsigned u) { return min(u, 0xFFFFFFFEu); }
__device__ __forceinline__ float half_add(float x, float y) { v2u r = __builtin_amdgcn_permlane32_swap(__float_as_uint(x), __float_as_uint(y), false, false); return __uint_as_float(r[0]) + __uint_as_float(r[1]); }
__device__ __forceinline__ float ror8(float v) { return __int_as_float(__builtin_amdgcn_update_dpp(0, __float_as_int(v), 0x128, 0xf, 0xf, false)); }
template <bool MAX> __device__ __forceinline__ float red8(float v) {
    auto op = [](float a, float b) { return MAX ? fmaxf(a, b) : a + b; };
    v = op(v, ror8(v));
    v2u r = __builtin_amdgcn_permlane16_swap(__float_as_uint(v), __float_as_uint(v), false, false); v = op(__uint_as_float(r[0]), __uint_as_float(r[1]));
    r = __builtin_amdgcn_permlane32_swap(__float_as_uint(v), __float_as_uint(v), false, false); return op(__uint_as_float(r[0]), __uint_as_float(r[1]));
}
__device__ __forceinline__ void load_wpair(const bf16_t* W, int ld, int l, v8u& t0, v8u& t1) {
    const int c15 = l & 15, gb = l >> 4;
#pragma unroll
    for (int i = 0; i < 8; ++i) {
        const int d = 16 * (2 * (i >> 2) + (gb >> 1)) + 8 * (i & 1) + 4 * (gb & 1) + 2 * ((i >> 1) & 1);
        const unsigned lo = *(const unsigned*)(W + (size_t)d * ld + 2 * c15), hi = *(const unsigned*)(W + (size_t)(d + 1) * ld + 2 * c15);
        t0[i] = __builtin_amdgcn_perm(hi, lo, 0x05040100u); t1[i] = __builtin_amdgcn_perm(hi, lo, 0x07060302u);
    }
}

static constexpr int L_PQK = 0, L_PV = 16384, L_O = 24576, L_X = 28672, X_SZ = 18432, L_MISC = L_X + 2 * X_SZ, L_TOT = L_MISC + 64;
static constexpr int O_LD = 272;

#define STAMP(k) do {} while (0)
#define STAMP_DECL do {} while (0)
#define CYC0() do {} while (0)
#define CYC(k) do {} while (0)
#define STAMP_OUT() do {} while (0)
#define CNT(x, v) (void)(v)

template <bool CAUSAL, typename VWAIT>
__device__ __forceinline__ void attn_tail(v4f s0, v4f s1, v4f s2, v8u (&vB)[4], int idx, bool hb, int key0, int qi, unsigned (&od)[4], VWAIT vwait) {
    float sc[6];
#pragma unroll
    for (int r = 0; r < 4; ++r) sc[r] = hb ? s1[r] : s0[r];
    sc[4] = hb ? s2[2] : s2[0]; sc[5] = hb ? s2[3] : s2[1];
#pragma unroll
    for (int p = 0; p < 3; ++p) {
        unsigned u = pk2(sc[2 * p], sc[2 * p + 1]);
        u = pk2(lo16(u) * 0.08838834764831845f, hi16(u) * 0.08838834764831845f);
        sc[2 * p] = lo16(u); sc[2 * p + 1] = hi16(u);
    }
    if (CAUSAL) {
#pragma unroll
        for (int r = 0; r < 4; ++r) sc[r] = (key0 + 4 * (r >> 1) + (r & 1) <= qi) ? sc[r] : -998244352.0f;
    }
    float m = fmaxf(fmaxf(fmaxf(sc[0], sc[1]), fmaxf(sc[2], sc[3])), fmaxf(sc[4], sc[5]));
    m = red8<true>(m);
    float sum = 0;
#pragma unroll
    for (int r = 0; r < 6; ++r) { sc[r] = __builtin_amdgcn_exp2f((sc[r] - m) * 1.4426950408889634f); sum += sc[r]; }
    sum = red8<false>(sum);
    const float inv = __builtin_amdgcn_rcpf(sum);
    v4u P = {pk2(sc[0] * inv, sc[1] * inv), pk2(sc[2] * inv, sc[3] * inv), pk2(sc[4] * inv, sc[5] * inv), 0u};
    vwait();
    v4f o[4];
#pragma unroll
    for (int t = 0; t < 4; ++t) o[t] = smf(P, vB[t], v4f{0, 0, 0, 0}, idx);
#pragma unroll
    for (int r = 0; r < 4; ++r) od[r] = pk2(half_add(o[0][r], o[1][r]), half_add(o[2][r], o[3][r]));
}

__global__ void __launch_bounds__(512, 2) k_chain(ChainArgs a) {
    __shared__ __attribute__((aligned(16))) char lds[81920 + 8192];
    const int tid = threadIdx.x, w = __builtin_amdgcn_readfirstlane(tid >> 6), l = tid & 63;
    const int b = blockIdx.x;
    if ((b & 7) >= 4) return;
    const int c = (b & 3) * 32 + (b >> 3), h = c >> 2, j = c & 3;
    const int l15 = l & 15, g = l >> 4, l7 = l & 7;
    const bool hb = (l & 8) != 0;
    const int idx = hb ? 0xEEEE : 0x4444;
    unsigned* err = a.ctl;
    LAS unsigned* misc = (LAS unsigned*)(lds + L_MISC);
    STAMP_DECL;
#define TLOOP() do {} while (0)
    unsigned anc = 0;
    bool dead = false;
    const rsrc_t rr = __builtin_amdgcn_make_buffer_rsrc((void*)a.ring, (short)0, (int)(NSLOT * SLOT), 0x00020000);
#define SLOTOFF(S) (((S) & 3) * (unsigned)SLOT + ((pm >> ((S) & 3)) & 1) * 8192u)

    if (w == 6) {
        if (c == 0) {
            unsigned pm = 0x100u;
            for (int sl = 0; sl < NSLOT; ++sl) {
                unsigned t[2];
                for (int p = 0; p < 2; ++p) {
                    const unsigned base = sl * (unsigned)SLOT + p * 8192; unsigned off = nearo(PR_OFF) + (l & 31) * 4, best = 0xffffffffu;
                    for (int it = 0; it < 6; ++it) { unsigned t0 = cyc32(); ST4(rr, off, base, off); VM0(); best = min(best, cyc32() - t0); off += 256; }
                    t[p] = best;
                }
                pm |= (t[1] < t[0] ? 1u : 0u) << sl;
            }
            if (l == 0) { asm volatile("global_store_dword %0, %1, off sc1" :: "v"(a.ctl + 32), "v"(pm) : "memory"); }
        }
        unsigned pm;
        for (unsigned tries = 0;; ++tries) {
            asm volatile("global_load_dword %0, %1, off sc1\n\ts_waitcnt vmcnt(0)" : "=&v"(pm) : "v"(a.ctl + 32) : "memory");
            pm = __builtin_amdgcn_readfirstlane(pm);
            if (pm & 0x100u) break;
            __builtin_amdgcn_s_sleep(8);
            if (spin_fail(tries, err)) break;
        }
        if (l == 0) { misc[1] = pm; misc[2] = 0; misc[3] = 0; }
    }

    if (w < 4) {
        v8u wq[4][2], wk[4][2], wv[4][2];
#pragma unroll
        for (int ks = 0; ks < 4; ++ks) {
            const size_t o = (size_t)(256 * w + 64 * ks) * HD + h * DH + 32 * j;
            load_wpair(a.Wq + o, HD, l, wq[ks][0], wq[ks][1]); load_wpair(a.Wk + o, HD, l, wk[ks][0], wk[ks][1]); load_wpair(a.Wv + o, HD, l, wv[ks][0], wv[ks][1]);
        }
        const unsigned o3 = nearo(H3_OFF + (c >> 5) * 16384 + w * 4096) + l * 16, oa = nearo(AN_OFF + (c >> 5) * 128);
        const unsigned o1s = nearo(H1_OFF + h * 8192 + j * 2048) + (w >> 1) * 512 + (4 * (w & 1) + (l & 3)) * 64 + (l >> 2) * 4;
        const unsigned ovs = nearo(H1_OFF + h * 8192 + j * 2048) + 1024 + (2 * l15 + (g >> 1)) * 16 + 8 * (g & 1);
        BAR();
        const unsigned pm = __builtin_amdgcn_readfirstlane(misc[1]);
        anc = rt32();
        for (int s = 0; s < NSEG; ++s) {
            const unsigned cur = SLOTOFF(s);
            v4u m[4];
            if (s == 0) {
#pragma unroll
                for (int k = 0; k < 4; ++k) m[k] = *(const v4u*)(a.mem0 + l7 * DM + 256 * w + 64 * k + 8 * (l >> 3));
            } else {
                lds_wait(misc + 3, s);
                CNT(nlate, wait_until(anc + a.d3, a.ml, a.thr));
                unsigned an;
                auto ld = [&]() { an = LD4(rr, oa, cur);
#pragma unroll
                    for (int k = 0; k < 4; ++k) m[k] = LD16(rr, o3 + k * 1024, cur);
                    return any_poison(max4(max4(max4(max4(an, m[0]), m[1]), m[2]), m[3])); };
                if (__builtin_expect(ld() && !dead, 0)) { unsigned tries = 0; do { CNT(ntry, 1); dead = spin_fail(tries++, err); } while (ld() && !dead); }
                anc = __builtin_amdgcn_readfirstlane(an);
            }
            STAMP(0); CYC0();
            v4f q0 = {}, q1 = {}, k0 = {}, k1 = {};
#pragma unroll
            for (int ks = 0; ks < 4; ++ks) { q0 = smf(m[ks], wq[ks][0], q0, idx); q1 = smf(m[ks], wq[ks][1], q1, idx); k0 = smf(m[ks], wk[ks][0], k0, idx); k1 = smf(m[ks], wk[ks][1], k1, idx); }
            LAS v4f* pq = (LAS v4f*)(lds + L_PQK + w * 4096 + l * 16);
            CYC(3);
            pq[0] = q0; pq[64] = q1; pq[128] = k0; pq[192] = k1;
            if (w == 0) misc[0] = anc;
            CYC(4);
            BAR();
            CYC(5);
            {
                LAS float* rp = (LAS float*)(lds + L_PQK + (w >> 1) * 2048 + (w & 1) * 256 + l * 4);
                float x0 = 0, x1 = 0;
#pragma unroll
                for (int p = 0; p < 8; ++p) { x0 += rp[(p >> 1) * 1024 + (p & 1) * 128]; x1 += rp[(p >> 1) * 1024 + (p & 1) * 128 + 256]; }
                const unsigned u = clampp(pk2(x0, x1));
                ST4(rr, o1s, cur, u);
            }
            CYC(6); CYC(7); STAMP(1);
            v4f v0 = {}, v1 = {};
#pragma unroll
            for (int ks = 0; ks < 4; ++ks) { v0 = smf(m[ks], wv[ks][0], v0, idx); v1 = smf(m[ks], wv[ks][1], v1, idx); }
            LAS v4f* pv = (LAS v4f*)(lds + L_PV + w * 2048 + l * 16);
            pv[0] = v0; pv[64] = v1;
            asm volatile("s_waitcnt lgkmcnt(0)" ::: "memory");
            unsigned old = 0;
            if (l == 0) old = __hip_atomic_fetch_add(misc + 2, 1u, __ATOMIC_RELAXED, __HIP_MEMORY_SCOPE_WORKGROUP);
            old = __builtin_amdgcn_readfirstlane(old);
            asm volatile("" ::: "memory");
            if ((old & 3u) == 3u) {
                LAS v4f* rp = (LAS v4f*)(lds + L_PV + (g >> 1) * 1024 + ((g & 1) * 16 + l15) * 16);
                v4f x = {};
#pragma unroll
                for (int p = 0; p < 8; ++p) x += rp[(p >> 1) * 128 + (p & 1) * 32];
                const v2u u = {clampp(pk2(x[0], x[1])), clampp(pk2(x[2], x[3]))};
                ST8(rr, ovs, cur, u);
                STAMP(2);
            }
            BAR();
        }
    } else {
        const int bw = w - 4;
        v8u wo[2][4];
#pragma unroll
        for (int ks = 0; ks < 2; ++ks)
#pragma unroll
            for (int t = 0; t < 2; ++t) load_wpair(a.Wo + (size_t)(h * DH + 64 * ks) * DM + 256 * j + 64 * bw + 32 * t, DM, l, wo[ks][t], wo[ks][t + 2]);
        const int pi = 16 * (g >> 1) + l15;
        const unsigned o2s = nearo(H2_OFF + (32 * j + 8 * bw + (pi >> 2)) * 4096) + h * 128 + ((g & 1) * 4 + (pi & 3)) * 16;
        auto wo_stage = [&](unsigned cur) {
            const LAS v4u* op = (const LAS v4u*)(lds + L_O + l7 * O_LD + (l >> 3) * 16);
            const v4u a0 = op[0], a1 = op[8];
            v4f d[4];
#pragma unroll
            for (int t = 0; t < 4; ++t) d[t] = smf(a0, wo[0][t], v4f{0, 0, 0, 0}, idx);
#pragma unroll
            for (int t = 0; t < 4; ++t) d[t] = smf(a1, wo[1][t], d[t], idx);
            v4u u;
#pragma unroll
            for (int r = 0; r < 4; ++r) u[r] = clampp(pk2(half_add(d[0][r], d[1][r]), half_add(d[2][r], d[3][r])));
            ST16(rr, o2s, cur, u);
        };
        if (w < 6) {
            const unsigned o1 = nearo(H1_OFF + h * 8192) + l7 * 64 + g * 16;
            const unsigned ov = nearo(H1_OFF + h * 8192) + 2 * bw * 2048 + 1024 + 2 * l15 * 16 + 8 * (g >> 1);
            const int key0 = 16 * (g & 1) + 8 * (g >> 1) + (hb ? 2 : 0), qi = 8 * j + l7;
            const float cinit = g >= 2 ? -INFINITY : 0.0f;
            v8u vB[4];
#pragma unroll
            for (int t = 0; t < 4; ++t) vB[t] = v8u{0, 0, 0, 0, 0, 0, 0, 0};
            BAR();
            TLOOP();
            const unsigned pm = __builtin_amdgcn_readfirstlane(misc[1]);
            for (int s = 0; s < NSEG; ++s) {
                const unsigned cur = SLOTOFF(s);
                lds_wait(misc + 4, s);
                const LAS char* xb = (const LAS char*)(lds + L_X + (s & 1) * X_SZ);
                v4u kx[2][4], qx[4];
#pragma unroll
                for (int t = 0; t < 2; ++t)
#pragma unroll
                    for (int k = 0; k < 4; ++k) kx[t][k] = *(const LAS v4u*)(xb + (t * 4 + k) * 1024 + l * 16);
#pragma unroll
                for (int k = 0; k < 4; ++k) qx[k] = *(const LAS v4u*)(xb + 16384 + k * 512 + (g * 8 + l7) * 16);
                if (!(g & 1)) {
#pragma unroll
                    for (int t = 0; t < 4; ++t) {
                        const LAS v4u* vp = (const LAS v4u*)(xb + 8192 + (bw * 4 + t) * 1024 + ((g >> 1) * 16 + l15) * 16);
                        const v4u x0 = vp[0], x1 = vp[32];
                        vB[t] = __builtin_shufflevector(x0, x1, 0, 1, 2, 3, 4, 5, 6, 7);
                    }
                }
                v4u q[4], km[4]; v2u vm[4];
                BAR();
                anc = __builtin_amdgcn_readfirstlane(misc[0]);
                CNT(nlate, wait_until(anc + a.d1, a.ml + 10, a.thr - 10));
                auto ld_qk = [&]() {
#pragma unroll
                    for (int k = 0; k < 4; ++k) { q[k] = LD16(rr, o1 + k * 2048, cur); km[k] = LD16(rr, o1 + k * 2048 + 512, cur); } };
                auto bad_qk = [&]() { unsigned mx = max4(max4(max4(max4(0, q[0]), km[0]), q[1]), km[1]); return any_poison(max4(max4(max4(max4(mx, q[2]), km[2]), q[3]), km[3])); };
                auto ld_v = [&]() {
#pragma unroll
                    for (int t = 0; t < 4; ++t) vm[t] = LD8(rr, ov + (t & 1) * 2048 + (t >> 1) * 16, cur); };
                auto bad_v = [&]() { return any_poison(max(max(max(vm[0][0], vm[0][1]), max(vm[1][0], vm[1][1])), max(max(vm[2][0], vm[2][1]), max(vm[3][0], vm[3][1])))); };
                ld_qk();
                wait_until(anc + a.d1 + a.dv, a.dv, a.thr);
                ld_v();
                FENCE();
                if (__builtin_expect(bad_qk() && !dead, 0)) { unsigned tries = 0; do { CNT(ntry, 1); dead = spin_fail(tries++, err); ld_qk(); } while (bad_qk() && !dead); }
                STAMP(0);
                auto vwait = [&]() {
                    if (__builtin_expect(bad_v() && !dead, 0)) { unsigned tries = 0; do { CNT(ntry, 0x10000); dead = spin_fail(tries++, err); ld_v(); } while (bad_v() && !dead); }
#pragma unroll
                    for (int t = 0; t < 4; ++t) { vB[t][0] = (g & 1) ? vm[t][0] : vB[t][0]; vB[t][1] = (g & 1) ? vm[t][1] : vB[t][1]; }
                };
                unsigned od[4];
                if (s + 1 < NSEG) {
                    v4f s0 = {}, s1 = {}, s2 = {cinit, cinit, cinit, cinit};
#pragma unroll
                    for (int k = 0; k < 4; ++k) { s0 = mf(kx[0][k], q[k], s0); s1 = mf(kx[1][k], q[k], s1); s2 = mf(km[k], q[k], s2); }
                    attn_tail<false>(s0, s1, s2, vB, idx, hb, key0, qi, od, vwait);
                    LAS unsigned* op = (LAS unsigned*)(lds + L_O + 4 * (g & 1) * O_LD + (32 * bw + 16 * (g >> 1) + l15) * 4);
#pragma unroll
                    for (int r = 0; r < 4; ++r) op[r * (O_LD / 4)] = od[r];
                    STAMP(1);
                    BAR();
                    wo_stage(cur);
                    STAMP(2);
                } else { vwait(); BAR(); }
                {
                    v4f s0 = {}, s1 = {}, s2 = {cinit, cinit, cinit, cinit};
#pragma unroll
                    for (int k = 0; k < 4; ++k) { s0 = mf(kx[0][k], qx[k], s0); s1 = mf(kx[1][k], qx[k], s1); s2 = mf(km[k], qx[k], s2); }
                    attn_tail<true>(s0, s1, s2, vB, idx, hb, key0, qi, od, []() {});
                    unsigned* xp = (unsigned*)(a.ox + (size_t)(s * SEG + 8 * j + 4 * (g & 1)) * HD + h * DH) + 32 * bw + 16 * (g >> 1) + l15;
#pragma unroll
                    for (int r = 0; r < 4; ++r) xp[r * (HD / 2)] = od[r];
                    STAMP(3);
                }
            }
        } else if (w == 6) {
            const unsigned o2l = nearo(H2_OFF + c * 4096) + l * 16;
            const unsigned o3s = nearo(H3_OFF + (2 * (g >> 1) + (hb ? 1 : 0)) * 16384 + c * 128) + (4 * (l7 >> 2) + 2 * (g & 1)) * 16 + (l7 & 3) * 4;
            const unsigned oas = nearo(AN_OFF + (l & 3) * 128);
            v4u sel;
#pragma unroll
            for (int i = 0; i < 4; ++i) sel[i] = (l7 == 2 * i ? 0x3f80u : 0u) | (l7 == 2 * i + 1 ? 0x3f800000u : 0u);
            BAR();
            const unsigned pm = __builtin_amdgcn_readfirstlane(misc[1]);
            for (int s = 0; s < NSEG; ++s) {
                const unsigned cur = SLOTOFF(s);
                const unsigned nxt = SLOTOFF(s + 1);
                BAR();
                anc = __builtin_amdgcn_readfirstlane(misc[0]);
                BAR();
                if (s + 1 == NSEG) break;
                wo_stage(cur);
                STAMP(0);
                unsigned late = wait_until(anc + a.d2, a.ml, a.thr);
                CNT(nlate, late);
                v4u p[4];
                auto ld = [&]() {
#pragma unroll
                    for (int k = 0; k < 4; ++k) p[k] = LD16(rr, o2l + k * 1024, cur);
                    return any_poison(max4(max4(max4(max4(0, p[0]), p[1]), p[2]), p[3])); };
                if (__builtin_expect(ld() && !dead, 0)) { unsigned tries = 0; late = 1; do { CNT(ntry, 1); dead = spin_fail(tries++, err); } while (ld() && !dead); }
                STAMP(1);
                v4f d = {};
#pragma unroll
                for (int k = 0; k < 4; ++k) d = mf(sel, p[k], d);
#pragma unroll
                for (int r = 0; r < 4; ++r) d[r] += ror8(d[r]);
                const unsigned u0 = clampp(pk2(d[0], d[1])), u1 = clampp(pk2(d[2], d[3]));
                ST4(rr, o3s, nxt, u0); ST4(rr, o3s + 16, nxt, u1);
                if (c == 0) { const unsigned tt = clampp(rt32() + (late ? a.slack : 0)); if (l < 4) ST4(rr, oas, nxt, tt); }
                lds_post(misc + 3, s + 1);
                STAMP(2);
            }
        } else {
            const int krow = 16 * ((l15 >> 2) & 1) + 8 * (l15 >> 3) + 4 * ((l15 >> 1) & 1) + (l15 & 1);
            const unsigned gk = (krow * NQKV + HD + h * DH + 8 * g) * 2;
            const unsigned gq = ((8 * j + l7) * NQKV + h * DH + 32 * (l >> 5) + 8 * ((l >> 3) & 3)) * 2;
            const unsigned gv = ((h * DH + 2 * l15) * 32 + 16 * (g & 1) + 8 * (g >> 1)) * 2;
            const unsigned p2 = nearo(H2_OFF + c * 4096) + l * 16, p1 = nearo(H1_OFF + h * 8192 + j * 2048) + l * 16;
            const unsigned p3 = l < 32 ? nearo(H3_OFF + (l >> 3) * 16384 + c * 128) + l7 * 16 : nearo(AN_OFF) + (l - 32) * 16;
            const v4u poi4 = {POI, POI, POI, POI};
            v4u pf[18];
            const rsrc_t rq = __builtin_amdgcn_make_buffer_rsrc((void*)a.qkvx, (short)0, S_TOK * NQKV * 2, 0x00020000);
            const rsrc_t rv = __builtin_amdgcn_make_buffer_rsrc((void*)a.vtx, (short)0, S_TOK * HD * 2, 0x00020000);
            auto issue = [&](int sg) {
                const unsigned qb = sg * SEG * NQKV * 2, vb = sg * HD * 32 * 2;
#pragma unroll
                for (int t = 0; t < 2; ++t)
#pragma unroll
                    for (int k = 0; k < 4; ++k) pf[t * 4 + k] = LD16(rq, gk + t * 2 * NQKV * 2 + k * 64, qb);
#pragma unroll
                for (int i = 0; i < 8; ++i) pf[8 + i] = LD16(rv, gv + (i >> 2) * 4096 + (i & 1) * 2048 + ((i >> 1) & 1) * 64, vb);
                pf[16] = LD16(rq, gq, qb); pf[17] = LD16(rq, gq + 128, qb);
            };
            auto commit = [&](int sg) {
                LAS v4u* xb = (LAS v4u*)(lds + L_X + (sg & 1) * X_SZ + l * 16);
#pragma unroll
                for (int i = 0; i < 18; ++i) xb[i * 64] = pf[i];
                lds_post(misc + 4, sg);
                VM0();
            };
            issue(0); commit(0);
            BAR();
            const unsigned pm = __builtin_amdgcn_readfirstlane(misc[1]);
            for (int s = 0; s < NSEG; ++s) {
                const unsigned cur = SLOTOFF(s);
                const unsigned fut = SLOTOFF(s + 2);
                BAR();
                if (s + 1 == NSEG) { BAR(); break; }
                anc = __builtin_amdgcn_readfirstlane(misc[0]);
                wait_until(anc + a.d1 + a.dp, a.ml + a.dp, a.thr);
                issue(s + 1);
#pragma unroll
                for (int k = 0; k < 4; ++k) ST16(rr, p2 + k * 1024, fut, poi4);
                ST16(rr, p1, fut, poi4); ST16(rr, p1 + 1024, fut, poi4);
                if (l < 32 || c == 0) ST16(rr, p3, fut, poi4);
                BAR();
                wo_stage(cur);
                commit(s + 1);
                STAMP(0);
            }
        }
    }
    STAMP_OUT();
}

extern "C" void kernel_launch(void* const* d_in, const int* in_sizes, int n_in, void* d_out, int out_size, void* d_ws, size_t ws_size, hipStream_t stream) {
    const bf16_t* x = (const bf16_t*)d_in[0]; const bf16_t* mem0 = (const bf16_t*)d_in[1];
    const bf16_t* Wq = (const bf16_t*)d_in[2]; const bf16_t* Wk = (const bf16_t*)d_in[3]; const bf16_t* Wv = (const bf16_t*)d_in[4]; const bf16_t* Wo = (const bf16_t*)d_in[5];
    const bf16_t* w_r = (const bf16_t*)d_in[8]; const bf16_t* wg = (const bf16_t*)d_in[9]; const bf16_t* wu = (const bf16_t*)d_in[10]; const bf16_t* wd = (const bf16_t*)d_in[11];
    const bf16_t* gate_w = (const bf16_t*)d_in[12];
    bf16_t* out = (bf16_t*)d_out;
    char* ws0 = (char*)d_ws;
    char* ring = (char*)(((size_t)ws0 + SLOT - 1) & ~(size_t)(SLOT - 1));
    char* ws = ring + NSLOT * SLOT; size_t off = 0;
    auto alloc = [&](size_t bytes) { void* p = ws + off; off += (bytes + 255) & ~(size_t)255; return p; };
    unsigned* ctl = (unsigned*)alloc(1024);
    bf16_t* qkvx = (bf16_t*)alloc((size_t)S_TOK * NQKV * 2);
    bf16_t* vtx = (bf16_t*)alloc((size_t)S_TOK * HD * 2);
    bf16_t* ox = (bf16_t*)alloc((size_t)S_TOK * HD * 2);
    float* gate = (float*)alloc(S_TOK * 4); float* r = (float*)alloc(S_TOK * 4); int* idx = (int*)alloc(64 * 4);
    bf16_t* hbuf = (bf16_t*)alloc(TOPC * FF * 2);
    float* upd = (float*)alloc((size_t)S_TOK * DM * 4);
    unsigned* dbg = (unsigned*)alloc(128 * 8 * 32 * 4);

    hipMemsetAsync(ring, 0xFF, 2 * SLOT, stream);
    hipMemsetAsync(ctl, 0, 1024, stream);
    hipMemsetAsync(upd, 0, (size_t)S_TOK * DM * 4, stream);
    k_prep<<<S_TOK, 256, 0, stream>>>(x, gate_w, w_r, gate, r);
    k_topk<<<1, 256, 0, stream>>>(r, idx);
    k_moe1<<<FF / 32, 256, 0, stream>>>(x, idx, wg, wu, hbuf);
    k_moe2<<<DM / 32, 256, 0, stream>>>(hbuf, idx, r, wd, upd);
    BSrc bq{Wq, Wk, Wv, HD, 0};
    k_gemm<0><<<dim3(NQKV / 128, S_TOK / 128), 256, 0, stream>>>(x, bq, qkvx, S_TOK, NQKV, DM, nullptr, nullptr, nullptr, vtx);
    const int tn[8] = {125, 330, 400, 60, 22, 40, 8, 30};
    ChainArgs ca{mem0, Wq, Wk, Wv, Wo, qkvx, vtx, ox, ring, ctl, dbg, tn[0], tn[1], tn[2], tn[3], tn[4], tn[5], tn[6], tn[7]};
    k_chain<<<256, 512, 0, stream>>>(ca);
    BSrc bo{Wo, Wo, Wo, DM, 0};
    k_gemm<1><<<dim3(DM / 128, S_TOK / 128), 256, 0, stream>>>(ox, bo, out, S_TOK, DM, HD, x, gate, upd, nullptr);
}
```

```cpp
#include <hip/hip_runtime.h>
#include <stdint.h>

typedef unsigned short bf16_t;
typedef unsigned v2u __attribute__((ext_vector_type(2)));
typedef unsigned v4u __attribute__((ext_vector_type(4)));
typedef unsigned v8u __attribute__((ext_vector_type(8)));
typedef float v2f __attribute__((ext_vector_type(2)));
typedef float v4f __attribute__((ext_vector_type(4)));
typedef short v4s __attribute__((ext_vector_type(4)));
typedef __bf16 v2b __attribute__((ext_vector_type(2)));
typedef __bf16 v8b __attribute__((ext_vector_type(8)));
typedef __bf16 v16b __attribute__((ext_vector_type(16)));
typedef unsigned long long u64;
typedef __amdgpu_buffer_rsrc_t rsrc_t;
#define LAS __attribute__((address_space(3)))
#define DEV __device__ __forceinline__

static constexpr int S_TOK = 2048, DM = 1024, NH = 32, DH = 128, SEG = 32, NSEG = 64;
static constexpr int HD = NH * DH;
static constexpr int FF = 3584, TOPC = 8;
static constexpr float SCALE = 0.08838834764831845f, LOG2E = 1.4426950408889634f, MASKV = -998244352.0f;

DEV unsigned pk2(float lo, float hi) { v2f f = {lo, hi}; return __builtin_bit_cast(unsigned, __builtin_convertvector(f, v2b)); }
DEV float lo16(unsigned u) { return __uint_as_float(u << 16); }
DEV float hi16(unsigned u) { return __uint_as_float(u & 0xffff0000u); }
DEV float rbf(float f) { return __uint_as_float(pk2(0.0f, f)); }
DEV v4f smf(v4u a, v8u b, v4f c, int idx) { return __builtin_amdgcn_smfmac_f32_16x16x64_bf16(__builtin_bit_cast(v8b, a), __builtin_bit_cast(v16b, b), c, idx, 0, 0); }
DEV v4f mf(v4u a, v4u b, v4f c) { return __builtin_amdgcn_mfma_f32_16x16x32_bf16(__builtin_bit_cast(v8b, a), __builtin_bit_cast(v8b, b), c, 0, 0, 0); }
DEV v2u trd(const LAS char* p) { return __builtin_bit_cast(v2u, __builtin_amdgcn_ds_read_tr16_b64_v4i16((LAS v4s*)p)); }
DEV v4u cat(v2u a, v2u b) { return __builtin_shufflevector(a, b, 0, 1, 2, 3); }
DEV v2u sw16(unsigned x, unsigned y) { return __builtin_amdgcn_permlane16_swap(x, y, false, false); }
DEV v2u sw32(unsigned x, unsigned y) { return __builtin_amdgcn_permlane32_swap(x, y, false, false); }
DEV float half_add(float x, float y) { v2u r = sw32(__float_as_uint(x), __float_as_uint(y)); return __uint_as_float(r[0]) + __uint_as_float(r[1]); }
DEV float ror8(float v) { return __int_as_float(__builtin_amdgcn_update_dpp(0, __float_as_int(v), 0x128, 0xf, 0xf, false)); }
template <bool MAX> DEV float red_g(float v) {
    auto op = [](float a, float b) { return MAX ? fmaxf(a, b) : a + b; };
    v2u r = sw16(__float_as_uint(v), __float_as_uint(v)); v = op(__uint_as_float(r[0]), __uint_as_float(r[1]));
    r = sw32(__float_as_uint(v), __float_as_uint(v)); return op(__uint_as_float(r[0]), __uint_as_float(r[1]));
}
DEV float sigmoidf_(float z) { return 1.0f / (1.0f + __expf(-z)); }
DEV v4u mkrs(const void* p, unsigned bytes) { const u64 a = (u64)p; return v4u{(unsigned)a, (unsigned)(a >> 32) & 0xffffu, bytes, 0x00020000u}; }
DEV rsrc_t mkrsrc(const void* p, unsigned bytes) { return __builtin_amdgcn_make_buffer_rsrc((void*)p, (short)0, (int)bytes, 0x00020000); }

#define ST16(RS, OFF, SOFF, V) __builtin_amdgcn_raw_buffer_store_b128(V, RS, (OFF) + (SOFF), 0, 16)
#define ST4(RS, OFF, SOFF, V) __builtin_amdgcn_raw_buffer_store_b32(V, RS, OFF, SOFF, 16)
#define LD16(RS, OFF, SOFF) __builtin_amdgcn_raw_buffer_load_b128(RS, OFF, SOFF, 16)
#define LD8(RS, OFF, SOFF) __builtin_amdgcn_raw_buffer_load_b64(RS, OFF, SOFF, 16)
#define LD4(RS, OFF, SOFF) __builtin_amdgcn_raw_buffer_load_b32(RS, OFF, SOFF, 16)
#define DMA16(RS, VOFF, SOFF, LOFF) asm volatile("s_mov_b32 m0, %0\n\ts_nop 0\n\tbuffer_load_dwordx4 %1, %2, %3 offen lds" :: "s"(LOFF), "v"(VOFF), "s"(RS), "s"(SOFF) : "memory", "m0")
#define FENCE() asm volatile("" ::: "memory")
#define WAITV(n) asm volatile("s_waitcnt vmcnt(" #n ")" ::: "memory")
#define WAITL(n) asm volatile("s_waitcnt lgkmcnt(" #n ")" ::: "memory")
#define RBAR() asm volatile("s_barrier" ::: "memory")
#define BAR() asm volatile("s_waitcnt lgkmcnt(0)\n\ts_barrier" ::: "memory")
#define SCHED() __builtin_amdgcn_sched_barrier(0)

static constexpr int HTB = 16384;

#define G_LDA(dst, off) _Pragma("unroll") for (int m_ = 0; m_ < 4; ++m_) _Pragma("unroll") for (int k_ = 0; k_ < 2; ++k_) dst[m_][k_] = *(const LAS v4u*)(lds + (off) + ra[k_] + m_ * 2048)
#define G_LDB(dst, off) _Pragma("unroll") for (int n_ = 0; n_ < 2; ++n_) _Pragma("unroll") for (int k_ = 0; k_ < 2; ++k_) \
        dst[n_][k_] = cat(trd(lds + (off) + rb[n_] + k_ * 8192), trd(lds + (off) + rb[n_] + k_ * 8192 + TSTEP))
#define G_MMA(C, At, Bt) do { __builtin_amdgcn_s_setprio(1); _Pragma("unroll") for (int m_ = 0; m_ < 4; ++m_) _Pragma("unroll") for (int n_ = 0; n_ < 2; ++n_) _Pragma("unroll") for (int k_ = 0; k_ < 2; ++k_) \
        C[m_][n_] = VT ? mf(At[m_][k_], Bt[n_][k_], C[m_][n_]) : mf(Bt[n_][k_], At[m_][k_], C[m_][n_]); __builtin_amdgcn_s_setprio(0); } while (0)

template <bool VT> DEV void qkv_store(const v4f (&c)[4][2], rsrc_t ro, int row0, int h, int wc, int l) {
    const unsigned base = (unsigned)((row0 >> 5) * NH + h) * 8192u + wc * 2048u;
#pragma unroll
    for (int a = 0; a < 2; ++a)
#pragma unroll
        for (int b = 0; b < 2; ++b) {
            v4u u;
            if (VT) u = v4u{pk2(c[2 * a][b][0], c[2 * a][b][1]), pk2(c[2 * a + 1][b][0], c[2 * a + 1][b][1]), pk2(c[2 * a][b][2], c[2 * a][b][3]), pk2(c[2 * a + 1][b][2], c[2 * a + 1][b][3])};
            else u = v4u{pk2(c[2 * a + b][0][0], c[2 * a + b][0][1]), pk2(c[2 * a + b][0][2], c[2 * a + b][0][3]), pk2(c[2 * a + b][1][0], c[2 * a + b][1][1]), pk2(c[2 * a + b][1][2], c[2 * a + b][1][3])};
            ST16(ro, l * 16 + b * 1024, base + a * (NH * 8192u), u);
        }
}

template <bool VT> DEV void gemm_qkv256(LAS char* lds, v4u rx, v4u rw, rsrc_t ro, int row0, int col0) {
    const int tid = threadIdx.x, w = __builtin_amdgcn_readfirstlane(tid >> 6), l = tid & 63, wr = w >> 2, wc = w & 3, fr = l & 15, fq = l >> 4;
    constexpr int TSTEP = 1024;
    const unsigned va = (tid >> 3) * 2048 + (((tid & 7) ^ ((tid >> 4) & 7)) << 4);
    const unsigned vb = (tid >> 4) * 8192 + (((tid & 15) ^ ((((tid >> 4) & 3) << 2) | (((tid >> 7) & 1) << 1))) << 4);
    int ra[2], rb[2];
#pragma unroll
    for (int i = 0; i < 2; ++i) {
        ra[i] = (wr * 64 + fr) * 128 + (((4 * i + fq) ^ (fr >> 1)) << 4);
        rb[i] = (8 * fq + (fr >> 2)) * 256 + (((4 * wc + 2 * i + ((fr >> 1) & 1)) ^ (((fr >> 2) << 2) | ((fq & 1) << 1))) << 4) + (fr & 1) * 8;
    }
    const unsigned lb = (unsigned)(size_t)lds + w * 1024;
    const unsigned a0 = row0 * 2048, b0 = col0 * 2;
#define SA(b, hh) (((b) * 2 + (hh)) * HTB)
#define SB(b, hh) ((4 + (b) * 2 + (hh)) * HTB)
#define STA(b, hh, t) do { const unsigned s_ = a0 + (hh) * 262144 + (t) * 128; DMA16(rx, va, s_, lb + SA(b, hh)); DMA16(rx, va, s_ + 131072, lb + SA(b, hh) + 8192); } while (0)
#define STB(b, hh, t) do { const unsigned s_ = b0 + (hh) * 256 + (t) * 524288; DMA16(rw, vb, s_, lb + SB(b, hh)); DMA16(rw, vb, s_ + 262144, lb + SB(b, hh) + 8192); } while (0)
    v4f acc[2][2][4][2] = {};
    v4u At[4][2], B0[2][2], B1[2][2];
    STB(0, 0, 0); STA(0, 0, 0); STB(0, 1, 0); STA(0, 1, 0);
    if (wr == 1) RBAR();
    WAITV(4); RBAR();
    STB(1, 0, 1); STA(1, 0, 1); STB(1, 1, 1);
    WAITV(6); RBAR();
    for (int t = 0; t < 16; t += 2) {
        const int t2 = (t + 2) & 15, t3 = (t + 3) & 15;
        G_LDB(B0, SB(0, 0)); SCHED(); G_LDA(At, SA(0, 0)); STA(1, 1, t + 1);
        WAITL(8); RBAR(); WAITL(0); G_MMA(acc[0][0], At, B0); RBAR(); SCHED();
        G_LDB(B1, SB(0, 1)); STB(0, 0, t2);
        RBAR(); WAITL(0); G_MMA(acc[0][1], At, B1); RBAR();
        G_LDA(At, SA(0, 1)); STA(0, 0, t2);
        RBAR(); WAITL(0); G_MMA(acc[1][0], At, B0); RBAR(); SCHED();
        STB(0, 1, t2);
        WAITV(6); RBAR(); G_MMA(acc[1][1], At, B1); RBAR();
        G_LDB(B0, SB(1, 0)); SCHED(); G_LDA(At, SA(1, 0)); STA(0, 1, t2);
        WAITL(8); RBAR(); WAITL(0); G_MMA(acc[0][0], At, B0); RBAR(); SCHED();
        G_LDB(B1, SB(1, 1)); STB(1, 0, t3);
        RBAR(); WAITL(0); G_MMA(acc[0][1], At, B1); RBAR();
        G_LDA(At, SA(1, 1)); STA(1, 0, t3);
        RBAR(); WAITL(0); G_MMA(acc[1][0], At, B0); RBAR(); SCHED();
        STB(1, 1, t3);
        WAITV(6); RBAR(); G_MMA(acc[1][1], At, B1); RBAR();
    }
#pragma unroll
    for (int ai = 0; ai < 2; ++ai)
#pragma unroll
        for (int bj = 0; bj < 2; ++bj) qkv_store<VT>(acc[ai][bj], ro, row0 + ai * 128 + wr * 64, (col0 >> 7) + bj, wc, l);
    WAITV(0);
    if (wr == 0) RBAR();
    RBAR();
#undef STA
#undef STB
}

template <bool VT, bool OUTP> DEV void gemm128(LAS char* lds, v4u rsa, v4u rsb, int row0, int col0, v4f (&acc)[4][2]) {
    const int tid = threadIdx.x, w = __builtin_amdgcn_readfirstlane(tid >> 6), l = tid & 63, wr = w >> 2, wc = w & 3, fr = l & 15, fq = l >> 4;
    constexpr int TSTEP = OUTP ? 4096 : 1024, NT = OUTP ? 64 : 16, LDB = OUTP ? 2048 : 8192;
    const unsigned va = OUTP ? (unsigned)(w * 131072 + l * 16) : (tid >> 3) * 2048 + (((tid & 7) ^ ((tid >> 4) & 7)) << 4);
    const unsigned vb = (tid >> 4) * LDB + (((tid & 15) ^ ((((tid >> 4) & 3) << 2) | (((tid >> (OUTP ? 6 : 7)) & 1) << 1))) << 4);
    int ra[2], rb[2];
#pragma unroll
    for (int i = 0; i < 2; ++i) {
        ra[i] = OUTP ? wr * 8192 + i * 1024 + l * 16 : (wr * 64 + fr) * 128 + (((4 * i + fq) ^ (fr >> 1)) << 4);
        rb[i] = ((OUTP ? 4 : 8) * fq + (fr >> 2)) * 256 + (((4 * wc + 2 * i + ((fr >> 1) & 1)) ^ (((fr >> 2) << 2) | ((fq & 1) << 1))) << 4) + (fr & 1) * 8;
    }
    const unsigned la = (unsigned)(size_t)lds + w * (OUTP ? 2048 : 1024), lb = (unsigned)(size_t)lds + HTB + w * 1024;
    const unsigned a0 = row0 * (OUTP ? 8192 : 2048), b0 = col0 * 2;
#define STG(sl, t) do { const unsigned sa_ = a0 + (t) * (OUTP ? 2048 : 128), sb_ = b0 + (t) * (64 * LDB); \
        DMA16(rsb, vb, sb_, lb + (sl) * 2 * HTB); DMA16(rsb, vb, sb_ + 32 * LDB, lb + (sl) * 2 * HTB + 8192); \
        DMA16(rsa, va, sa_, la + (sl) * 2 * HTB); DMA16(rsa, va, sa_ + (OUTP ? 1024 : 131072), la + (sl) * 2 * HTB + (OUTP ? 1024 : 8192)); } while (0)
#pragma unroll
    for (int m = 0; m < 4; ++m) { acc[m][0] = v4f{0, 0, 0, 0}; acc[m][1] = v4f{0, 0, 0, 0}; }
    v4u At[4][2], Bt[2][2];
    STG(0, 0); STG(1, 1); STG(2, 2);
    if (wr == 1) RBAR();
    WAITV(8); RBAR(); RBAR();
    for (int t = 0; t < NT; t += 4) {
#pragma unroll
        for (int u = 0; u < 4; ++u) {
            G_LDB(Bt, u * 2 * HTB + HTB); G_LDA(At, u * 2 * HTB);
            STG((u + 3) & 3, (t + u + 3) & (NT - 1));
            WAITV(8); WAITL(0); RBAR(); G_MMA(acc, At, Bt); RBAR(); SCHED();
        }
    }
    WAITV(0);
    if (wr == 0) RBAR();
    RBAR();
#undef STG
}

DEV void xattn(rsrc_t rq, rsrc_t rk, rsrc_t rv, rsrc_t rm, rsrc_t ro, int seg, int h, int l) {
    const int fr = l & 15, g = l >> 4;
    const unsigned sb = (unsigned)(seg * NH + h) * 8192u, mb = (unsigned)(seg * NH + h) * 4096u;
    v4u q[4][2], k[4][2], km[4], v[8]; v2u vm[8];
#pragma unroll
    for (int j = 0; j < 4; ++j) {
        q[j][0] = LD16(rq, l * 16 + j * 2048, sb); q[j][1] = LD16(rq, l * 16 + j * 2048 + 1024, sb);
        k[j][0] = LD16(rk, l * 16 + j * 2048, sb); k[j][1] = LD16(rk, l * 16 + j * 2048 + 1024, sb);
        km[j] = LD16(rm, (g * 8 + (l & 7)) * 16 + j * 512, mb);
    }
#pragma unroll
    for (int d = 0; d < 8; ++d) { v[d] = LD16(rv, l * 16 + d * 1024, sb); vm[d] = LD8(rm, 2048 + fr * 16 + (g & 1) * 8 + d * 256, mb); }
#pragma unroll
    for (int u = 0; u < 2; ++u) {
        v4f s0 = {}, s1 = {}, s2 = {};
#pragma unroll
        for (int j = 0; j < 4; ++j) { s0 = mf(k[j][0], q[j][u], s0); if (u) s1 = mf(k[j][1], q[j][u], s1); s2 = mf(km[j], q[j][u], s2); }
        float e[12];
#pragma unroll
        for (int r = 0; r < 4; ++r) {
            const bool vis = 4 * g + r <= fr;
            e[r] = (u || vis) ? rbf(rbf(s0[r]) * SCALE) : MASKV;
            e[4 + r] = (u && vis) ? rbf(rbf(s1[r]) * SCALE) : MASKV;
            e[8 + r] = g < 2 ? rbf(rbf(s2[r]) * SCALE) : -INFINITY;
        }
        float m = e[0];
#pragma unroll
        for (int r = 1; r < 12; ++r) m = fmaxf(m, e[r]);
        m = red_g<true>(m);
        float sum = 0;
#pragma unroll
        for (int r = 0; r < 12; ++r) { e[r] = __builtin_amdgcn_exp2f((e[r] - m) * LOG2E); sum += e[r]; }
        const float inv = __builtin_amdgcn_rcpf(red_g<false>(sum));
        const v4u P = {pk2(e[0] * inv, e[1] * inv), pk2(e[4] * inv, e[5] * inv), pk2(e[2] * inv, e[3] * inv), pk2(e[6] * inv, e[7] * inv)};
        const unsigned pm0 = pk2(e[8] * inv, e[9] * inv), pm1 = pk2(e[10] * inv, e[11] * inv);
        const v4u Pa = {pm0, pm1, 0u, 0u}, Pb = {0u, 0u, pm0, pm1};
#pragma unroll
        for (int d = 0; d < 4; ++d) {
            const v4u vv = cat(vm[2 * d], vm[2 * d + 1]);
            const v4f o0 = mf(vv, Pa, mf(v[2 * d], P, v4f{0, 0, 0, 0})), o1 = mf(vv, Pb, mf(v[2 * d + 1], P, v4f{0, 0, 0, 0}));
            const v4u o = {pk2(o0[0], o0[1]), pk2(o0[2], o0[3]), pk2(o1[0], o1[1]), pk2(o1[2], o1[3])};
            ST16(ro, l * 16 + d * 1024, (unsigned)((seg * 2 + u) * 128 + h * 4) * 1024u, o);
        }
    }
}

DEV void prep_row(const bf16_t* x, const bf16_t* gw, const bf16_t* wr, float* gr, int row, int l) {
    float a = 0, b = 0;
#pragma unroll
    for (int i = 0; i < 2; ++i) {
        const v4u xv = *(const v4u*)(x + (size_t)row * DM + i * 512 + l * 8), gv = *(const v4u*)(gw + i * 512 + l * 8), rv = *(const v4u*)(wr + i * 512 + l * 8);
#pragma unroll
        for (int e = 0; e < 4; ++e) {
            a += rbf(lo16(xv[e]) * lo16(gv[e])) + rbf(hi16(xv[e]) * hi16(gv[e]));
            b += lo16(xv[e]) * lo16(rv[e]) + hi16(xv[e]) * hi16(rv[e]);
        }
    }
#pragma unroll
    for (int o = 1; o < 16; o <<= 1) { a += __shfl_xor(a, o); b += __shfl_xor(b, o); }
    a = red_g<false>(a); b = red_g<false>(b);
    if (l == 0) *(v2f*)(gr + 2 * row) = v2f{rbf(sigmoidf_(rbf(a))), rbf(b)};
}
DEV int top8(rsrc_t rg, int l) {
    unsigned key[32];
#pragma unroll
    for (int i = 0; i < 16; ++i) {
        const v4u t = LD16(rg, l * 16 + i * 1024, 0);
        const int row = i * 128 + l * 2;
        key[2 * i] = ((int)t[1] < 0 ? 0xffff0000u - t[1] : t[1] + 0x80000000u) + (0xffffu - row);
        key[2 * i + 1] = ((int)t[3] < 0 ? 0xffff0000u - t[3] : t[3] + 0x80000000u) + (0xffffu - row - 1);
    }
    int res = 0;
#pragma unroll 1
    for (int c = 0; c < TOPC; ++c) {
        unsigned m = 0;
#pragma unroll
        for (int i = 0; i < 32; ++i) m = max(m, key[i]);
#pragma unroll
        for (int o = 1; o < 64; o <<= 1) m = max(m, (unsigned)__shfl_xor((int)m, o));
#pragma unroll
        for (int i = 0; i < 32; ++i) key[i] = key[i] == m ? 0u : key[i];
        if (l == c) res = 0xffff - (int)(m & 0xffffu);
    }
    return res;
}
template <int NKS> DEV void skinny32(LAS char* wl, v4u rw, unsigned ldw, unsigned off, const v4u (&a)[NKS], v4f (&c)[2], int l) {
    const unsigned vo = (l >> 2) * ldw + (l & 3) * 16, lb = (unsigned)(size_t)wl;
#pragma unroll
    for (int i = 0; i < 2 * NKS; ++i) DMA16(rw, vo, off + i * 16 * ldw, lb + i * 1024);
    WAITV(0);
    const LAS char* p = wl + (8 * (l >> 4) + ((l & 15) >> 2)) * 64 + (l & 3) * 8;
    c[0] = v4f{0, 0, 0, 0}; c[1] = c[0];
#pragma unroll
    for (int k = 0; k < NKS; ++k)
#pragma unroll
        for (int n = 0; n < 2; ++n) c[n] = mf(a[k], cat(trd(p + k * 2048 + n * 32), trd(p + k * 2048 + n * 32 + 256)), c[n]);
}
DEV void moe1(LAS char* lds, const bf16_t* x, rsrc_t rg, v4u rwg, v4u rwu, rsrc_t rh, int* idxo, int cb) {
    const int tid = threadIdx.x, w = __builtin_amdgcn_readfirstlane(tid >> 6), l = tid & 63;
    const int sel = top8(rg, l);
    if (cb == 0 && tid < TOPC) idxo[tid] = sel;
    const int row = __shfl(sel, l & 7);
    v4u a[4];
#pragma unroll
    for (int k = 0; k < 4; ++k) a[k] = *(const v4u*)(x + (size_t)row * DM + w * 128 + k * 32 + (l >> 4) * 8);
    v4f cg[2], cu[2];
    skinny32<4>(lds + w * 16384, rwg, FF * 2, (w * 128 * FF + cb * 32) * 2, a, cg, l);
    skinny32<4>(lds + w * 16384 + 8192, rwu, FF * 2, (w * 128 * FF + cb * 32) * 2, a, cu, l);
    BAR();
    LAS v4f* red = (LAS v4f*)lds;
    if (l < 32) { red[(w * 4 + 0) * 32 + l] = cg[0]; red[(w * 4 + 1) * 32 + l] = cg[1]; red[(w * 4 + 2) * 32 + l] = cu[0]; red[(w * 4 + 3) * 32 + l] = cu[1]; }
    BAR();
    if (tid < 128) {
        const int tok = tid >> 4, c0 = 2 * (tid & 15);
        float hv[2];
#pragma unroll
        for (int e = 0; e < 2; ++e) {
            const int cc = c0 + e; const LAS float* p = (const LAS float*)lds + ((cc >> 4) * 32 + (tok >> 2) * 16 + (cc & 15)) * 4 + (tok & 3);
            float g = 0, u = 0;
#pragma unroll
            for (int q = 0; q < 8; ++q) { g += p[q * 512]; u += p[q * 512 + 256]; }
            g = rbf(g); hv[e] = rbf(g * sigmoidf_(g)) * rbf(u);
        }
        ST4(rh, (tok * FF + cb * 32 + c0) * 2, 0, pk2(hv[0], hv[1]));
    }
    BAR();
}
DEV void moe2(LAS char* lds, rsrc_t rh, rsrc_t rg, rsrc_t ri, v4u rwd, rsrc_t ru, int cb) {
    const int tid = threadIdx.x, w = __builtin_amdgcn_readfirstlane(tid >> 6), l = tid & 63;
    v4f c[2], c2[2];
    {
        v4u a[7];
#pragma unroll
        for (int k = 0; k < 7; ++k) a[k] = LD16(rh, ((l & 7) * FF + w * 448 + k * 32 + (l >> 4) * 8) * 2, 0);
        skinny32<7>(lds + w * 16384, rwd, DM * 2, (w * 448 * DM + cb * 32) * 2, a, c, l);
#pragma unroll
        for (int k = 0; k < 7; ++k) a[k] = LD16(rh, ((l & 7) * FF + w * 448 + 224 + k * 32 + (l >> 4) * 8) * 2, 0);
        WAITL(0);
        skinny32<7>(lds + w * 16384, rwd, DM * 2, ((w * 448 + 224) * DM + cb * 32) * 2, a, c2, l);
    }
    BAR();
    LAS v4f* red = (LAS v4f*)lds;
    if (l < 32) { red[(w * 2 + 0) * 32 + l] = c[0] + c2[0]; red[(w * 2 + 1) * 32 + l] = c[1] + c2[1]; }
    BAR();
    if (tid < 256) {
        const int tok = tid >> 5, cc = tid & 31; const LAS float* p = (const LAS float*)lds + ((cc >> 4) * 32 + (tok >> 2) * 16 + (cc & 15)) * 4 + (tok & 3);
        float y = 0;
#pragma unroll
        for (int q = 0; q < 8; ++q) y += p[q * 256];
        const int row = (int)LD4(ri, tok * 4, 0);
        const float r = __uint_as_float(LD4(rg, row * 8 + 4, 0));
        ST4(ru, (tok * DM + cb * 32 + cc) * 4, 0, __float_as_uint(rbf(rbf(y) * rbf(sigmoidf_(r)))));
    }
    BAR();
}
DEV void out_store(v4f (&c)[4][2], const bf16_t* x, rsrc_t rg, rsrc_t ri, rsrc_t ru, bf16_t* out, int row0, int col0) {
    const int tid = threadIdx.x, w = __builtin_amdgcn_readfirstlane(tid >> 6), l = tid & 63, fr = l & 15, fq = l >> 4;
    const int col = col0 + (w & 3) * 32 + (fq & 1) * 16 + (fq >> 1) * 8;
    const v4u i0 = LD16(ri, 0, 0), i1 = LD16(ri, 16, 0);
#pragma unroll
    for (int m = 0; m < 4; ++m) {
        const int row = row0 + (w >> 2) * 64 + m * 16 + fr;
        const v4u xv = *(const v4u*)(x + (size_t)row * DM + col);
        const float g = __uint_as_float(LD4(rg, row * 8, 0)), omg = rbf(1.0f - g);
        float a[8], b[8];
#pragma unroll
        for (int r = 0; r < 4; ++r) { const v2u s = sw16(__float_as_uint(c[m][0][r]), __float_as_uint(c[m][1][r])); a[r] = __uint_as_float(s[0]); a[4 + r] = __uint_as_float(s[1]);
            b[2 * r] = lo16(xv[r]); b[2 * r + 1] = hi16(xv[r]); }
        int slot = -1;
#pragma unroll
        for (int q = 0; q < 4; ++q) { slot = (int)i0[q] == row ? q : slot; slot = (int)i1[q] == row ? 4 + q : slot; }
        if (slot >= 0) {
            const v4u u0 = LD16(ru, (slot * DM + col) * 4, 0), u1 = LD16(ru, (slot * DM + col) * 4 + 16, 0);
#pragma unroll
            for (int r = 0; r < 4; ++r) { b[r] = rbf(b[r] + __uint_as_float(u0[r])); b[4 + r] = rbf(b[4 + r] + __uint_as_float(u1[r])); }
        }
        v4u o;
#pragma unroll
        for (int r = 0; r < 4; ++r) o[r] = pk2(rbf(g * rbf(a[2 * r])) + rbf(omg * b[2 * r]), rbf(g * rbf(a[2 * r + 1])) + rbf(omg * b[2 * r + 1]));
        *(v4u*)(out + (size_t)row * DM + col) = o;
    }
}

#define EXACT 0
static constexpr unsigned POI = 0xFFFFFFFFu, OOB = 0x40000000u;
static constexpr unsigned H2_OFF = 0, H1_OFF = 512 * 1024, H3_OFF = 768 * 1024, AN_OFF = 832 * 1024, PR_OFF = 896 * 1024;
static constexpr size_t SLOT = 2u << 20;
static constexpr int NSLOT = 4;
static constexpr unsigned SPIN_CAP = 200000u;

struct ChainArgs {
    const bf16_t* mem0; const bf16_t* Wq; const bf16_t* Wk; const bf16_t* Wv; const bf16_t* Wo; const char* kb; const char* vb;
    char* mb; char* ring; unsigned* ctl; unsigned* dbg;
    int d1, d2, d3, dv, ml, slack, thr, dp;
};

DEV unsigned nearo(unsigned off) { return (off >> 13) * 16384u + (off & 8191u); }
DEV unsigned rt32() { u64 t; asm volatile("s_memrealtime %0\n\ts_waitcnt lgkmcnt(0)" : "=s"(t) :: "memory"); return (unsigned)t; }
DEV unsigned cyc32() { u64 t; asm volatile("s_memtime %0\n\ts_waitcnt lgkmcnt(0)" : "=s"(t) :: "memory"); return (unsigned)t; }
DEV unsigned wait_until(unsigned T, int ml, int thr) {
    unsigned late, tmp;
    T = __builtin_amdgcn_readfirstlane(T);
    asm volatile(
        "s_memrealtime vcc\n\ts_waitcnt lgkmcnt(0)\n\t"
        "s_sub_i32 %[tmp], %[T], vcc_lo\n\t"
        "s_min_i32 %[tmp], %[tmp], 0x200\n\t"
        "s_add_i32 %[late], %[tmp], %[thr]\n\t"
        "s_cmp_lt_i32 %[late], 0\n\t"
        "s_cselect_b32 %[tmp], %[ml], %[tmp]\n\t"
        "s_cselect_b32 %[late], 1, 0\n\t"
        "s_add_i32 %[T], vcc_lo, %[tmp]\n\t"
        "s_cmp_lt_i32 %[tmp], 5\n\t"
        "s_cbranch_scc1 2f\n"
        "1:\n\t"
        "s_memrealtime vcc\n\ts_waitcnt lgkmcnt(0)\n\t"
        "s_sub_i32 %[tmp], %[T], vcc_lo\n\t"
        "s_cmp_gt_i32 %[tmp], 4\n\t"
        "s_cbranch_scc1 1b\n"
        "2:\n\t"
        "s_sub_i32 %[tmp], %[tmp], 2\n"
        "3:\n\t"
        "s_sub_i32 %[tmp], %[tmp], 1\n\t"
        "s_cmp_ge_i32 %[tmp], 0\n\t"
        "s_cbranch_scc1 3b\n\t"
        : [tmp] "=&s"(tmp), [late] "=&s"(late), [T] "+s"(T) : [ml] "s"(ml), [thr] "s"(thr) : "scc", "vcc", "memory");
    return late;
}
DEV void lds_wait(volatile LAS unsigned* p, unsigned v) { while ((int)(__builtin_amdgcn_readfirstlane(*p) - v) < 0) __builtin_amdgcn_s_sleep(1); FENCE(); }
DEV void lds_post(volatile LAS unsigned* p, unsigned v) { WAITL(0); *p = v; }
DEV bool spin_fail(unsigned tries, unsigned* err) {
    __builtin_amdgcn_s_sleep(1);
    if ((tries & 255u) != 255u) return false;
    unsigned e; asm volatile("global_load_dword %0, %1, off sc1\n\ts_waitcnt vmcnt(0)" : "=&v"(e) : "v"(err) : "memory");
    if (__builtin_amdgcn_readfirstlane(e) == 1u) return true;
    if (tries > SPIN_CAP) { unsigned one = 1u; asm volatile("global_store_dword %0, %1, off sc1" :: "v"(err), "v"(one) : "memory"); return true; }
    return false;
}
DEV unsigned max4(unsigned m, v4u r) { m = max(max(m, r[0]), r[1]); return max(max(m, r[2]), r[3]); }
DEV bool any_poison(unsigned mx) { return __builtin_amdgcn_ballot_w64(mx == POI) != 0; }
DEV bool any_nan(float a, float b) { return __builtin_amdgcn_ballot_w64(__builtin_isunordered(a, b)) != 0; }
template <int RS> DEV v8u rd_w(const LAS char* p) {
    return __builtin_shufflevector(cat(trd(p), trd(p + 32 * RS)), cat(trd(p + 8 * RS), trd(p + 40 * RS)), 0, 1, 2, 3, 4, 5, 6, 7);
}

static constexpr int PQ_LD = 68 * 4, L_PQK = 0, L_PV = 9216, L_O = 13312, L_X = 16384, X_SZ = 10240, L_MISC = 131072, L_TOT = L_MISC + 256;
static constexpr int O_LD = 272;

#define STAMP(k) do {} while (0)
#define STAMP_DECL do {} while (0)
#define STAMP_OUT() do {} while (0)
#define CNT(x, v) (void)(v)

DEV void chain(LAS char* lds, const ChainArgs& a, const int c) {
    const int tid = threadIdx.x, w = __builtin_amdgcn_readfirstlane(tid >> 6), l = tid & 63;
    const int h = c >> 2, j = c & 3;
    const int l15 = l & 15, g = l >> 4, l7 = l & 7;
    const bool hb = (l & 8) != 0;
    const int idx = hb ? 0xEEEE : 0x4444;
    unsigned* err = a.ctl;
    LAS unsigned* misc = (LAS unsigned*)(lds + L_MISC);
    STAMP_DECL;
#define TLOOP() do {} while (0)
    unsigned anc = 0;
    bool dead = false;
    const rsrc_t rr = mkrsrc(a.ring, NSLOT * SLOT);
#define SLOTOFF(S) (((S) & 3) * (unsigned)SLOT + ((pm >> ((S) & 3)) & 1) * 8192u)
    const int wrow = 4 * (g >> 1) + 2 * (l15 >> 3) + (g & 1) + 16 * ((l15 >> 2) & 1);
    const unsigned lbw = (unsigned)(size_t)lds + w * 16384;

    if (w < 4) {
        v8u wt[3][4][2];
        {
            const v4u rw[3] = {mkrs(a.Wq, DM * HD * 2), mkrs(a.Wk, DM * HD * 2), mkrs(a.Wv, DM * HD * 2)};
            const unsigned vo = (l >> 2) * (HD * 2) + (l & 3) * 16, so = (256 * w * HD + h * DH + 32 * j) * 2;
            auto issue = [&](int p) {
#pragma unroll
                for (int i = 0; i < 8; ++i) DMA16(rw[p >> 1], vo, so + ((p & 1) * 128 + i * 16) * (HD * 2), lbw + (p & 1) * 8192 + i * 1024); };
            issue(0); issue(1);
            const LAS char* pw = lds + w * 16384 + wrow * 64 + (l & 3) * 8;
#pragma unroll
            for (int p = 0; p < 6; ++p) {
                if (p < 5) WAITV(8); else WAITV(0);
#pragma unroll
                for (int k = 0; k < 2; ++k) { wt[p >> 1][2 * (p & 1) + k][0] = rd_w<64>(pw + (p & 1) * 8192 + k * 4096); wt[p >> 1][2 * (p & 1) + k][1] = rd_w<64>(pw + (p & 1) * 8192 + k * 4096 + 32); }
                WAITL(0);
                if (p < 4) issue(p + 2);
            }
        }
        const unsigned o3 = nearo(H3_OFF + (c >> 5) * 16384 + w * 4096) + l * 16, oa = nearo(AN_OFF + (c >> 5) * 128);
        v4u m[4];
#pragma unroll
        for (int k = 0; k < 4; ++k) {
            const bf16_t* mp = a.mem0 + l7 * DM + 256 * w + 64 * k + 2 * (l >> 3);
            const unsigned t0 = *(const unsigned*)mp, t1 = *(const unsigned*)(mp + 16), t2 = *(const unsigned*)(mp + 32), t3 = *(const unsigned*)(mp + 48);
            m[k] = v4u{__builtin_amdgcn_perm(t1, t0, 0x05040100u), __builtin_amdgcn_perm(t3, t2, 0x05040100u), __builtin_amdgcn_perm(t1, t0, 0x07060302u), __builtin_amdgcn_perm(t3, t2, 0x07060302u)};
        }
        BAR();
        const unsigned pm = __builtin_amdgcn_readfirstlane(misc[1]);
        anc = rt32();
        LAS float* pq = (LAS float*)(lds + L_PQK + w * 8 * PQ_LD + l * 4);
        for (int s = 0; s < NSEG; ++s) {
            const unsigned cur = SLOTOFF(s);
            unsigned an = anc;
            auto ld = [&]() { an = LD4(rr, oa, cur);
#pragma unroll
                for (int k = 0; k < 4; ++k) m[k] = LD16(rr, o3 + k * 1024, cur); };
            if (s > 0) {
                lds_wait(misc + 3, s);
                CNT(nlate, wait_until(anc + a.d3, a.ml, a.thr));
                ld();
            }
            for (unsigned tries = 0;; ++tries) {
                v4f q0 = {}, q1 = {}, k0 = {}, k1 = {};
#pragma unroll
                for (int ks = 0; ks < 4; ++ks) { q0 = smf(m[ks], wt[0][ks][0], q0, idx); q1 = smf(m[ks], wt[0][ks][1], q1, idx); k0 = smf(m[ks], wt[1][ks][0], k0, idx); k1 = smf(m[ks], wt[1][ks][1], k1, idx); }
                float hq[4], hk[4];
#pragma unroll
                for (int r = 0; r < 4; ++r) { hq[r] = half_add(q0[r], q1[r]); pq[r * 68] = hq[r]; }
#pragma unroll
                for (int r = 0; r < 4; ++r) { hk[r] = half_add(k0[r], k1[r]); pq[(4 + r) * 68] = hk[r]; }
                const bool sus = __builtin_amdgcn_ballot_w64(__builtin_isunordered(hq[0], hq[1]) || __builtin_isunordered(hq[2], hq[3]) || an == POI) != 0;
                if (__builtin_expect((!sus && !(EXACT & 1)) || dead || s == 0, 1)) break;
                if (!any_poison(max4(max4(max4(max4(an, m[0]), m[1]), m[2]), m[3]))) break;
                CNT(ntry, 1); dead = spin_fail(tries, err); ld();
            }
            anc = __builtin_amdgcn_readfirstlane(an);
            if (w == 0) misc[0] = anc;
            STAMP(0);
            BAR();
            v4f v0 = {}, v1 = {};
#pragma unroll
            for (int ks = 0; ks < 4; ++ks) { v0 = smf(m[ks], wt[2][ks][0], v0, idx); v1 = smf(m[ks], wt[2][ks][1], v1, idx); }
            *(volatile LAS v4f*)(lds + L_PV + w * 1024 + l * 16) = v4f{half_add(v0[0], v1[0]), half_add(v0[1], v1[1]), half_add(v0[2], v1[2]), half_add(v0[3], v1[3])};
            *(volatile LAS unsigned*)(misc + 8 + w) = s + 1;
            STAMP(1);
            BAR();
        }
    } else {
        const int bw = w - 4;
        v8u wo[2][4];
        {
            const v4u rw = mkrs(a.Wo, HD * DM * 2);
            const unsigned vo = (l >> 3) * (DM * 2) + (l & 7) * 16, so = (h * DH * DM + 256 * j + 64 * bw) * 2;
#pragma unroll
            for (int i = 0; i < 16; ++i) DMA16(rw, vo, so + i * 8 * (DM * 2), lbw + i * 1024);
        }
        if (w == 6) {
            if (c == 0) {
                unsigned pm = 0x100u;
                WAITV(0);
                for (int sl = 0; sl < NSLOT; ++sl) {
                    unsigned t[2];
                    for (int p = 0; p < 2; ++p) {
                        const unsigned base = sl * (unsigned)SLOT + p * 8192; unsigned off = nearo(PR_OFF) + (l & 31) * 4, best = 0xffffffffu;
                        for (int it = 0; it < 6; ++it) { unsigned t0 = cyc32(); ST4(rr, off, base, off); WAITV(0); best = min(best, cyc32() - t0); off += 256; }
                        t[p] = best;
                    }
                    pm |= (t[1] < t[0] ? 1u : 0u) << sl;
                }
                if (l == 0) { asm volatile("global_store_dword %0, %1, off sc1" :: "v"(a.ctl + 32), "v"(pm) : "memory"); }
            }
            unsigned pm;
            for (unsigned tries = 0;; ++tries) {
                asm volatile("global_load_dword %0, %1, off sc1\n\ts_waitcnt vmcnt(0)" : "=&v"(pm) : "v"(a.ctl + 32) : "memory");
                pm = __builtin_amdgcn_readfirstlane(pm);
                if (pm & 0x100u) break;
                __builtin_amdgcn_s_sleep(8);
                if (spin_fail(tries, err)) break;
            }
            if (l < 16) misc[l] = l == 1 ? pm : (l == 4 ? 0xffffffffu : 0u);
        }
        WAITV(0);
        {
            const LAS char* pw = lds + w * 16384 + wrow * 128 + (l & 3) * 8;
#pragma unroll
            for (int ks = 0; ks < 2; ++ks)
#pragma unroll
                for (int t = 0; t < 4; ++t) wo[ks][t] = rd_w<128>(pw + ks * 8192 + t * 32);
        }
        const unsigned o2s = nearo(H2_OFF + (32 * j + 8 * bw + (l15 >> 1)) * 4096) + h * 128 + (4 * (g & 1) + 2 * (g >> 1) + (l & 1)) * 16;
        auto wo_stage = [&](unsigned cur) {
            const LAS v4u* op = (const LAS v4u*)(lds + L_O + l7 * O_LD + (l >> 3) * 16);
            const v4u a0 = op[0], a1 = op[8];
            v4f d[4];
#pragma unroll
            for (int t = 0; t < 4; ++t) d[t] = smf(a1, wo[1][t], smf(a0, wo[0][t], v4f{0, 0, 0, 0}, idx), idx);
            float e[2][4];
#pragma unroll
            for (int t = 0; t < 4; ++t) { e[0][t] = half_add(d[t][0], d[t][2]); e[1][t] = half_add(d[t][1], d[t][3]); }
            ST16(rr, o2s, cur, (v4u{pk2(e[0][0], e[0][1]), pk2(e[0][2], e[0][3]), pk2(e[1][0], e[1][1]), pk2(e[1][2], e[1][3])}));
        };
        const unsigned h1 = nearo(H1_OFF + h * 8192);
        if (w < 6) {
            const unsigned o1 = h1 + ((hb ? 4 : 0) + g) * 128 + l7 * 16;
            const unsigned om = h1 + 1024 + ((g & 1) * 8 + l7) * 16 + ((hb ? 2 : 0) + (g >> 1)) * 4;
            const unsigned ov = h1 + 2 * bw * 2048 + 1280 + l15 * 16 + 8 * (g >> 1);
            const unsigned pmask = g >= 2 ? 0xffff0000u : 0x0000ffffu;
            const unsigned one2 = 0x3f803f80u;
            const v8u ones = {one2, one2, one2, one2, one2, one2, one2, one2};
            v8u vB[4];
#pragma unroll
            for (int t = 0; t < 4; ++t) vB[t] = v8u{0, 0, 0, 0, 0, 0, 0, 0};
            BAR();
            TLOOP();
            const unsigned pm = __builtin_amdgcn_readfirstlane(misc[1]);
            for (int s = 0; s + 1 < NSEG; ++s) {
                const unsigned cur = SLOTOFF(s);
                lds_wait(misc + 4, s);
                if (!(g & 1)) {
#pragma unroll
                    for (int t = 0; t < 4; ++t) {
                        const LAS v4u* vp = (const LAS v4u*)(lds + L_X + (s & 1) * X_SZ + (bw * 4 + t) * 1024 + ((g >> 1) * 16 + l15) * 16);
                        vB[t] = __builtin_shufflevector(vp[0], vp[32], 0, 1, 2, 3, 4, 5, 6, 7);
                    }
                }
                v4u sp[4]; unsigned sm[4]; v2u vm[4];
                BAR();
                anc = __builtin_amdgcn_readfirstlane(misc[0]);
                CNT(nlate, wait_until(anc + a.d1, a.ml + 10, a.thr - 10));
                auto ld_s = [&]() {
#pragma unroll
                    for (int k = 0; k < 4; ++k) { sp[k] = LD16(rr, o1 + k * 2048, cur); sm[k] = LD4(rr, om + k * 2048, cur); } };
                auto ld_v = [&]() {
#pragma unroll
                    for (int t = 0; t < 4; ++t) vm[t] = LD8(rr, ov + (t >> 1) * 2048 + (t & 1) * 256, cur); };
                ld_s();
                wait_until(anc + a.d1 + a.dv, a.dv, a.thr);
                ld_v();
                FENCE();
                for (unsigned tries = 0;; ++tries) {
                    float e[5];
#pragma unroll
                    for (int r = 0; r < 4; ++r) e[r] = (__uint_as_float(sp[0][r]) + __uint_as_float(sp[1][r])) + (__uint_as_float(sp[2][r]) + __uint_as_float(sp[3][r]));
                    e[4] = (__uint_as_float(sm[0]) + __uint_as_float(sm[1])) + (__uint_as_float(sm[2]) + __uint_as_float(sm[3]));
                    STAMP(0);
                    float m = fmaxf(fmaxf(fmaxf(e[0], e[1]), fmaxf(e[2], e[3])), e[4]);
                    m = red_g<true>(fmaxf(m, ror8(m)));
                    m = rbf(rbf(m) * SCALE) * LOG2E;
#pragma unroll
                    for (int r = 0; r < 5; ++r) e[r] = __builtin_amdgcn_exp2f(__builtin_fmaf(rbf(rbf(e[r]) * SCALE), LOG2E, -m));
                    const v4u P = {pk2(e[0], e[1]), pk2(e[2], e[3]), pk2(e[4], e[4]) & pmask, 0u};
#pragma unroll
                    for (int t = 0; t < 4; ++t) { vB[t][0] = (g & 1) ? vm[t][0] : vB[t][0]; vB[t][1] = (g & 1) ? vm[t][1] : vB[t][1]; vB[t][4] = (g & 1) ? vm[t][0] : vB[t][4]; vB[t][5] = (g & 1) ? vm[t][1] : vB[t][5]; }
                    const v4f sd = smf(P, ones, v4f{0, 0, 0, 0}, idx);
                    v4f o[4];
#pragma unroll
                    for (int t = 0; t < 4; ++t) o[t] = smf(P, vB[t], v4f{0, 0, 0, 0}, idx);
                    const float i0 = __builtin_amdgcn_rcpf(half_add(sd[0], sd[2])), i1 = __builtin_amdgcn_rcpf(half_add(sd[1], sd[3]));
                    float y[2][4];
#pragma unroll
                    for (int t = 0; t < 4; ++t) { y[0][t] = half_add(o[t][0], o[t][2]) * i0; y[1][t] = half_add(o[t][1], o[t][3]) * i1; }
                    LAS v2u* op = (LAS v2u*)(lds + L_O + (4 * (g & 1) + 2 * (g >> 1)) * O_LD + bw * 128 + l15 * 8);
                    op[0] = v2u{pk2(y[0][0], y[0][1]), pk2(y[0][2], y[0][3])}; op[O_LD / 8] = v2u{pk2(y[1][0], y[1][1]), pk2(y[1][2], y[1][3])};
                    const bool sus = __builtin_amdgcn_ballot_w64(__builtin_isunordered(y[0][0], y[0][1]) || __builtin_isunordered(y[1][2], y[1][3])) != 0;
                    if (__builtin_expect((!sus && !(EXACT & 2)) || dead, 1)) break;
                    unsigned mx = max4(max4(max4(max4(0, sp[0]), sp[1]), sp[2]), sp[3]);
                    mx = max4(max4(max4(mx, v4u{sm[0], sm[1], sm[2], sm[3]}), cat(vm[0], vm[1])), cat(vm[2], vm[3]));
                    if (!any_poison(mx)) break;
                    CNT(ntry, 1); dead = spin_fail(tries, err); ld_s(); ld_v();
                }
                STAMP(1);
                BAR();
                wo_stage(cur);
                STAMP(2);
            }
            BAR(); BAR();
        } else if (w == 6) {
            const rsrc_t rm = mkrsrc(a.mb, NSEG * NH * 4096);
            const unsigned o1s = hb ? OOB : h1 + j * 2048 + g * 128 + l7 * 16;
            const unsigned oms = (hb || g < 2) ? OOB : h1 + j * 2048 + 1024 + ((g & 1) * 8 + l7) * 16;
            const unsigned oks = hb ? h * 4096 + j * 512 + (g * 8 + l7) * 16 : OOB;
            const unsigned o2l = nearo(H2_OFF + c * 4096) + l * 16;
            const unsigned o3s = (l & 1) ? OOB : nearo(H3_OFF + (2 * (g >> 1) + (hb ? 1 : 0)) * 16384 + c * 128) + ((l7 & 6) + (g & 1)) * 16;
            const unsigned oas = nearo(AN_OFF + (l & 3) * 128);
            v4u sel;
#pragma unroll
            for (int i = 0; i < 4; ++i) sel[i] = (l7 == 2 * i ? 0x3f80u : 0u) | (l7 == 2 * i + 1 ? 0x3f800000u : 0u);
            const LAS v4f* rp = (const LAS v4f*)(lds + L_PQK + ((hb ? 4 : 0) + (l & 3)) * PQ_LD + ((l7 >> 2) * 16 + g * 4) * 4);
            BAR();
            const unsigned pm = __builtin_amdgcn_readfirstlane(misc[1]);
            for (int s = 0; s < NSEG; ++s) {
                const unsigned cur = SLOTOFF(s);
                const unsigned nxt = SLOTOFF(s + 1);
                lds_wait(misc + 4, min(s, NSEG - 2));
                const LAS v4u* kp = (const LAS v4u*)(lds + L_X + (s & 1) * X_SZ + 8192 + l * 16);
                const v4u kx0 = kp[0], kx1 = kp[64];
                BAR();
                {
                    const v4f x0 = (rp[0] + rp[8 * 17]) + (rp[16 * 17] + rp[24 * 17]), x1 = (rp[8] + rp[8 * 17 + 8]) + (rp[16 * 17 + 8] + rp[24 * 17 + 8]);
                    const v4u X = {pk2(x0[0], x0[1]), pk2(x0[2], x0[3]), pk2(x1[0], x1[1]), pk2(x1[2], x1[3])};
                    const v4f s0 = mf(kx0, X, v4f{0, 0, 0, 0}), s1 = mf(kx1, X, v4f{0, 0, 0, 0}), s2 = mf(X, X, v4f{0, 0, 0, 0});
                    if (EXACT & 16) { SCHED(); asm volatile("s_nop 7\n\ts_nop 7"); SCHED(); }
                    ST16(rr, o1s, cur, __builtin_bit_cast(v4u, s0)); ST16(rr, o1s + 512, cur, __builtin_bit_cast(v4u, s1)); ST16(rr, oms, cur, __builtin_bit_cast(v4u, s2));
                    ST16(rm, oks, s * (NH * 4096), X);
                }
                if (EXACT & 8) WAITV(0);
                anc = __builtin_amdgcn_readfirstlane(misc[0]);
                STAMP(0);
                BAR();
                if (s + 1 == NSEG) break;
                wo_stage(cur);
                STAMP(1);
                unsigned late = wait_until(anc + a.d2, a.ml, a.thr);
                CNT(nlate, late);
                v4u p[4]; v4f d;
                for (unsigned tries = 0;; ++tries) {
#pragma unroll
                    for (int k = 0; k < 4; ++k) p[k] = LD16(rr, o2l + k * 1024, cur);
                    d = v4f{0, 0, 0, 0};
#pragma unroll
                    for (int k = 0; k < 4; ++k) d = mf(sel, p[k], d);
                    if (__builtin_expect((!any_nan(d[0], d[0]) && !(EXACT & 4)) || dead, 1)) break;
                    if (!any_poison(max4(max4(max4(max4(0, p[0]), p[1]), p[2]), p[3]))) break;
                    late = 1; CNT(ntry, 1); dead = spin_fail(tries, err);
                }
                STAMP(2);
#pragma unroll
                for (int r = 0; r < 4; ++r) d[r] += ror8(d[r]);
                const unsigned u0 = pk2(d[0], d[1]), u1 = pk2(d[2], d[3]);
                ST16(rr, o3s, nxt, (v4u{u0, u1, (unsigned)__builtin_amdgcn_mov_dpp((int)u0, 0xb1, 0xf, 0xf, false), (unsigned)__builtin_amdgcn_mov_dpp((int)u1, 0xb1, 0xf, 0xf, false)}));
                if (c == 0) { const unsigned tt = min(rt32() + (late ? a.slack : 0), 0xfffffffeu); if (l < 4) ST4(rr, oas, nxt, tt); }
                lds_post(misc + 3, s + 1);
                STAMP(3);
            }
        } else {
            const rsrc_t rm = mkrsrc(a.mb, NSEG * NH * 4096);
            const unsigned ovs = (g & 1) ? OOB : h1 + j * 2048 + 1280 + ((g >> 1) * 16 + l15) * 16;
            const unsigned ovm = (g & 1) ? OOB : h * 4096 + 2048 + (32 * j + (g >> 1) * 16 + l15) * 16;
            const unsigned p2 = nearo(H2_OFF + c * 4096) + l * 16, p1 = h1 + j * 2048 + l * 16;
            const unsigned p3 = l < 32 ? nearo(H3_OFF + (l >> 3) * 16384 + c * 128) + l7 * 16 : (c == 0 ? nearo(AN_OFF) + (l - 32) * 16 : OOB);
            const v4u poi4 = {POI, POI, POI, POI};
            v4u pf[10];
            const rsrc_t rk = mkrsrc(a.kb, S_TOK * HD * 2), rv = mkrsrc(a.vb, S_TOK * HD * 2);
            auto issue = [&](int sg) {
                const unsigned sb = (unsigned)(sg * NH + h) * 8192u;
#pragma unroll
                for (int i = 0; i < 8; ++i) pf[i] = LD16(rv, l * 16 + (i & 3) * 1024, sb + (i >> 2) * 4096);
                pf[8] = LD16(rk, l * 16, sb + j * 2048); pf[9] = LD16(rk, l * 16 + 1024, sb + j * 2048);
            };
            auto commit = [&](int sg) {
                LAS v4u* xb = (LAS v4u*)(lds + L_X + (sg & 1) * X_SZ + l * 16);
#pragma unroll
                for (int i = 0; i < 10; ++i) xb[i * 64] = pf[i];
                lds_post(misc + 4, sg);
                WAITV(0);
            };
            issue(0);
            BAR();
            commit(0);
            const unsigned pm = __builtin_amdgcn_readfirstlane(misc[1]);
            for (int s = 0; s < NSEG; ++s) {
                const unsigned cur = SLOTOFF(s);
                const unsigned fut = SLOTOFF(s + 2);
                BAR();
                {
                    v4f x;
                    for (;;) {
                        const v4u f = *(volatile LAS v4u*)(misc + 8);
                        const volatile LAS v4f* rp = (const volatile LAS v4f*)(lds + L_PV + l * 16);
                        const v4f t0 = rp[0], t1 = rp[64], t2 = rp[128], t3 = rp[192];
                        x = (t0 + t1) + (t2 + t3);
                        if (__builtin_amdgcn_readfirstlane(min(min(f[0], f[1]), min(f[2], f[3]))) > (unsigned)s) break;
                    }
                    const unsigned u0 = pk2(x[0], x[1]), u1 = pk2(x[2], x[3]);
                    const v2u a0 = sw16(u0, u0), a1 = sw16(u1, u1);
                    const v4u V = {a0[0], a1[0], a0[1], a1[1]};
                    ST16(rr, ovs, cur, V); ST16(rm, ovm, s * (NH * 4096), V);
                }
                anc = __builtin_amdgcn_readfirstlane(misc[0]);
                STAMP(0);
                if (s + 1 == NSEG) { BAR(); break; }
                wait_until(anc + a.d1 + a.dp, a.ml + a.dp, a.thr);
                if (s + 2 < NSEG) issue(s + 1);
#pragma unroll
                for (int k = 0; k < 4; ++k) ST16(rr, p2 + k * 1024, fut, poi4);
                ST16(rr, p1, fut, poi4); ST16(rr, p1 + 1024, fut, poi4); ST16(rr, p3, fut, poi4);
                if (EXACT & 32) WAITV(0);
                BAR();
                wo_stage(cur);
                commit(s + 1);
                STAMP(1);
            }
        }
    }
    STAMP_OUT();
}

struct Bufs { const bf16_t* x; const bf16_t* Wq; const bf16_t* Wk; const bf16_t* Wv; const bf16_t* Wo; const bf16_t* w_r; const bf16_t* wg; const bf16_t* wu; const bf16_t* wd; const bf16_t* gw;
              char* qkv; char* mb; char* ob; float* gr; int* idx; bf16_t* hid; float* upd; bf16_t* out; };
static constexpr unsigned QSZ = S_TOK * HD * 2;

__global__ void __launch_bounds__(512, 2) k_chain(ChainArgs a) {
    __shared__ __attribute__((aligned(16))) char lds[L_TOT];
    const int b = blockIdx.x;
    if ((b & 7) >= 4) return;
    chain((LAS char*)lds, a, (b & 3) * 32 + (b >> 3));
}
__global__ void __launch_bounds__(512, 2) k_qkv(Bufs p) {
    __shared__ __attribute__((aligned(16))) char lds[131072];
    const int b = blockIdx.x, pn = (b & 7) * 6 + (b >> 3) / 8, pmm = (b >> 3) & 7;
    const int mat = pn >> 4, col0 = (pn & 15) * 256;
    const v4u rx = mkrs(p.x, S_TOK * DM * 2), rw = mkrs(mat == 0 ? p.Wq : (mat == 1 ? p.Wk : p.Wv), DM * HD * 2);
    const rsrc_t ro = mkrsrc(p.qkv + (size_t)mat * QSZ, QSZ);
    if (mat == 2) gemm_qkv256<true>((LAS char*)lds, rx, rw, ro, pmm * 256, col0);
    else gemm_qkv256<false>((LAS char*)lds, rx, rw, ro, pmm * 256, col0);
}
__global__ void __launch_bounds__(512, 2) k_xattn(Bufs p) {
    const int it = blockIdx.x * 8 + __builtin_amdgcn_readfirstlane(threadIdx.x >> 6);
    xattn(mkrsrc(p.qkv, QSZ), mkrsrc(p.qkv + QSZ, QSZ), mkrsrc(p.qkv + 2 * (size_t)QSZ, QSZ), mkrsrc(p.mb, NSEG * NH * 4096), mkrsrc(p.ob, QSZ), it >> 5, it & 31, threadIdx.x & 63);
}
__global__ void __launch_bounds__(512, 2) k_out(Bufs p) {
    __shared__ __attribute__((aligned(16))) char lds[131072];
    const int b = blockIdx.x, row0 = ((b & 7) * 2 + ((b >> 3) & 1)) * 128, col0 = (b >> 4) * 128;
    v4f acc[4][2];
    gemm128<false, true>((LAS char*)lds, mkrs(p.ob, QSZ), mkrs(p.Wo, HD * DM * 2), row0, col0, acc);
    out_store(acc, p.x, mkrsrc(p.gr, S_TOK * 8), mkrsrc(p.idx, 32), mkrsrc(p.upd, TOPC * DM * 4), p.out, row0, col0);
}
__global__ void __launch_bounds__(512, 2) k_prep(Bufs p) { prep_row(p.x, p.gw, p.w_r, p.gr, blockIdx.x * 8 + (threadIdx.x >> 6), threadIdx.x & 63); }
__global__ void __launch_bounds__(512, 2) k_moe1(Bufs p) {
    __shared__ __attribute__((aligned(16))) char lds[131072];
    moe1((LAS char*)lds, p.x, mkrsrc(p.gr, S_TOK * 8), mkrs(p.wg, DM * FF * 2), mkrs(p.wu, DM * FF * 2), mkrsrc(p.hid, TOPC * FF * 2), p.idx, blockIdx.x);
}
__global__ void __launch_bounds__(512, 2) k_moe2(Bufs p) {
    __shared__ __attribute__((aligned(16))) char lds[131072];
    moe2((LAS char*)lds, mkrsrc(p.hid, TOPC * FF * 2), mkrsrc(p.gr, S_TOK * 8), mkrsrc(p.idx, 32), mkrs(p.wd, FF * DM * 2), mkrsrc(p.upd, TOPC * DM * 4), blockIdx.x);
}

#define TUNE_DEFAULT {118, 270, 327, 10, 22, 40, 8, 30}

extern "C" void kernel_launch(void* const* d_in, const int* in_sizes, int n_in, void* d_out, int out_size, void* d_ws, size_t ws_size, hipStream_t stream) {
    char* ws0 = (char*)d_ws;
    char* ring = (char*)(((size_t)ws0 + SLOT - 1) & ~(size_t)(SLOT - 1));
    char* ws = ring + NSLOT * SLOT; size_t off = 0;
    auto alloc = [&](size_t bytes) { void* p = ws + off; off += (bytes + 255) & ~(size_t)255; return p; };
    unsigned* ctl = (unsigned*)alloc(1024);
    Bufs p;
    p.x = (const bf16_t*)d_in[0]; p.Wq = (const bf16_t*)d_in[2]; p.Wk = (const bf16_t*)d_in[3]; p.Wv = (const bf16_t*)d_in[4]; p.Wo = (const bf16_t*)d_in[5];
    p.w_r = (const bf16_t*)d_in[8]; p.wg = (const bf16_t*)d_in[9]; p.wu = (const bf16_t*)d_in[10]; p.wd = (const bf16_t*)d_in[11]; p.gw = (const bf16_t*)d_in[12];
    p.qkv = (char*)alloc(3 * (size_t)QSZ); p.mb = (char*)alloc(NSEG * NH * 4096); p.ob = (char*)alloc(QSZ);
    p.gr = (float*)alloc(S_TOK * 8); p.idx = (int*)alloc(256); p.hid = (bf16_t*)alloc(TOPC * FF * 2); p.upd = (float*)alloc(TOPC * DM * 4); p.out = (bf16_t*)d_out;
    unsigned* dbg = (unsigned*)alloc(128 * 8 * 32 * 4);

    hipMemsetAsync(ring, 0xFF, 2 * SLOT, stream);
    hipMemsetAsync(ctl, 0, 1024, stream);
    k_prep<<<S_TOK / 8, 512, 0, stream>>>(p);
    k_moe1<<<FF / 32, 512, 0, stream>>>(p);
    k_moe2<<<DM / 32, 512, 0, stream>>>(p);
    k_qkv<<<384, 512, 0, stream>>>(p);
    const int tn[8] = TUNE_DEFAULT;
    ChainArgs ca{(const bf16_t*)d_in[1], p.Wq, p.Wk, p.Wv, p.Wo, p.qkv + QSZ, p.qkv + 2 * (size_t)QSZ, p.mb, ring, ctl, dbg, tn[0], tn[1], tn[2], tn[3], tn[4], tn[5], tn[6], tn[7]};
    k_chain<<<256, 512, 0, stream>>>(ca);
    k_xattn<<<NSEG * NH / 8, 512, 0, stream>>>(p);
    k_out<<<128, 512, 0, stream>>>(p);
}
```

```cpp
#include <hip/hip_runtime.h>
#include <stdint.h>

typedef unsigned short bf16_t;
typedef unsigned v2u __attribute__((ext_vector_type(2)));
typedef unsigned v4u __attribute__((ext_vector_type(4)));
typedef unsigned v8u __attribute__((ext_vector_type(8)));
typedef float v2f __attribute__((ext_vector_type(2)));
typedef float v4f __attribute__((ext_vector_type(4)));
typedef short v4s __attribute__((ext_vector_type(4)));
typedef __bf16 v2b __attribute__((ext_vector_type(2)));
typedef __bf16 v8b __attribute__((ext_vector_type(8)));
typedef __bf16 v16b __attribute__((ext_vector_type(16)));
typedef unsigned long long u64;
typedef __amdgpu_buffer_rsrc_t rsrc_t;
#define LAS __attribute__((address_space(3)))
#define DEV __device__ __forceinline__

static constexpr int S_TOK = 2048, DM = 1024, NH = 32, DH = 128, SEG = 32, NSEG = 64;
static constexpr int HD = NH * DH;
static constexpr int FF = 3584, TOPC = 8;
static constexpr float SCALE = 0.08838834764831845f, LOG2E = 1.4426950408889634f, MASKV = -998244352.0f;

DEV unsigned pk2(float lo, float hi) { v2f f = {lo, hi}; return __builtin_bit_cast(unsigned, __builtin_convertvector(f, v2b)); }
DEV float lo16(unsigned u) { return __uint_as_float(u << 16); }
DEV float hi16(unsigned u) { return __uint_as_float(u & 0xffff0000u); }
DEV float rbf(float f) { return __uint_as_float(pk2(0.0f, f)); }
DEV v4f smf(v4u a, v8u b, v4f c, int idx) { return __builtin_amdgcn_smfmac_f32_16x16x64_bf16(__builtin_bit_cast(v8b, a), __builtin_bit_cast(v16b, b), c, idx, 0, 0); }
DEV v4f mf(v4u a, v4u b, v4f c) { return __builtin_amdgcn_mfma_f32_16x16x32_bf16(__builtin_bit_cast(v8b, a), __builtin_bit_cast(v8b, b), c, 0, 0, 0); }
DEV v2u trd(const LAS char* p) { return __builtin_bit_cast(v2u, __builtin_amdgcn_ds_read_tr16_b64_v4i16((LAS v4s*)p)); }
DEV v4u cat(v2u a, v2u b) { return __builtin_shufflevector(a, b, 0, 1, 2, 3); }
DEV v2u sw16(unsigned x, unsigned y) { return __builtin_amdgcn_permlane16_swap(x, y, false, false); }
DEV v2u sw32(unsigned x, unsigned y) { return __builtin_amdgcn_permlane32_swap(x, y, false, false); }
DEV float half_add(float x, float y) { v2u r = sw32(__float_as_uint(x), __float_as_uint(y)); return __uint_as_float(r[0]) + __uint_as_float(r[1]); }
DEV float ror8(float v) { return __int_as_float(__builtin_amdgcn_update_dpp(0, __float_as_int(v), 0x128, 0xf, 0xf, false)); }
template <bool MAX> DEV float red_g(float v) {
    auto op = [](float a, float b) { return MAX ? fmaxf(a, b) : a + b; };
    v2u r = sw16(__float_as_uint(v), __float_as_uint(v)); v = op(__uint_as_float(r[0]), __uint_as_float(r[1]));
    r = sw32(__float_as_uint(v), __float_as_uint(v)); return op(__uint_as_float(r[0]), __uint_as_float(r[1]));
}
DEV float sigmoidf_(float z) { return 1.0f / (1.0f + __expf(-z)); }
DEV v4u mkrs(const void* p, unsigned bytes) { const u64 a = (u64)p; return v4u{(unsigned)a, (unsigned)(a >> 32) & 0xffffu, bytes, 0x00020000u}; }
DEV rsrc_t mkrsrc(const void* p, unsigned bytes) { return __builtin_amdgcn_make_buffer_rsrc((void*)p, (short)0, (int)bytes, 0x00020000); }

#define ST16(RS, OFF, SOFF, V) __builtin_amdgcn_raw_buffer_store_b128(V, RS, (OFF) + (SOFF), 0, 16)
#define ST4(RS, OFF, SOFF, V) __builtin_amdgcn_raw_buffer_store_b32(V, RS, OFF, SOFF, 16)
#define LD16(RS, OFF, SOFF) __builtin_amdgcn_raw_buffer_load_b128(RS, OFF, SOFF, 16)
#define LD8(RS, OFF, SOFF) __builtin_amdgcn_raw_buffer_load_b64(RS, OFF, SOFF, 16)
#define LD4(RS, OFF, SOFF) __builtin_amdgcn_raw_buffer_load_b32(RS, OFF, SOFF, 16)
#define DMA16(RS, VOFF, SOFF, LOFF) asm volatile("s_mov_b32 m0, %0\n\ts_nop 0\n\tbuffer_load_dwordx4 %1, %2, %3 offen lds" :: "s"(LOFF), "v"(VOFF), "s"(RS), "s"(SOFF) : "memory", "m0")
#define FENCE() asm volatile("" ::: "memory")
#define WAITV(n) asm volatile("s_waitcnt vmcnt(" #n ")" ::: "memory")
#define WAITL(n) asm volatile("s_waitcnt lgkmcnt(" #n ")" ::: "memory")
#define RBAR() asm volatile("s_barrier" ::: "memory")
#define BAR() asm volatile("s_waitcnt lgkmcnt(0)\n\ts_barrier" ::: "memory")
#define SCHED() __builtin_amdgcn_sched_barrier(0)

static constexpr int HTB = 16384;

#define G_LDA(dst, off) _Pragma("unroll") for (int m_ = 0; m_ < 4; ++m_) _Pragma("unroll") for (int k_ = 0; k_ < 2; ++k_) dst[m_][k_] = *(const LAS v4u*)(lds + (off) + ra[k_] + m_ * 2048)
#define G_LDB(dst, off) _Pragma("unroll") for (int n_ = 0; n_ < 2; ++n_) _Pragma("unroll") for (int k_ = 0; k_ < 2; ++k_) \
        dst[n_][k_] = cat(trd(lds + (off) + rb[n_] + k_ * 8192), trd(lds + (off) + rb[n_] + k_ * 8192 + TSTEP))
#define G_MMA(C, At, Bt) do { __builtin_amdgcn_s_setprio(1); _Pragma("unroll") for (int m_ = 0; m_ < 4; ++m_) _Pragma("unroll") for (int n_ = 0; n_ < 2; ++n_) _Pragma("unroll") for (int k_ = 0; k_ < 2; ++k_) \
        C[m_][n_] = VT ? mf(At[m_][k_], Bt[n_][k_], C[m_][n_]) : mf(Bt[n_][k_], At[m_][k_], C[m_][n_]); __builtin_amdgcn_s_setprio(0); } while (0)

template <bool VT> DEV void qkv_store(const v4f (&c)[4][2], rsrc_t ro, int row0, int h, int wc, int l) {
    const unsigned base = (unsigned)((row0 >> 5) * NH + h) * 8192u + wc * 2048u;
#pragma unroll
    for (int a = 0; a < 2; ++a)
#pragma unroll
        for (int b = 0; b < 2; ++b) {
            v4u u;
            if (VT) u = v4u{pk2(c[2 * a][b][0], c[2 * a][b][1]), pk2(c[2 * a + 1][b][0], c[2 * a + 1][b][1]), pk2(c[2 * a][b][2], c[2 * a][b][3]), pk2(c[2 * a + 1][b][2], c[2 * a + 1][b][3])};
            else u = v4u{pk2(c[2 * a + b][0][0], c[2 * a + b][0][1]), pk2(c[2 * a + b][0][2], c[2 * a + b][0][3]), pk2(c[2 * a + b][1][0], c[2 * a + b][1][1]), pk2(c[2 * a + b][1][2], c[2 * a + b][1][3])};
            ST16(ro, l * 16 + b * 1024, base + a * (NH * 8192u), u);
        }
}

template <bool VT> DEV void gemm_qkv256(LAS char* lds, v4u rx, v4u rw, rsrc_t ro, int row0, int col0) {
    const int tid = threadIdx.x, w = __builtin_amdgcn_readfirstlane(tid >> 6), l = tid & 63, wr = w >> 2, wc = w & 3, fr = l & 15, fq = l >> 4;
    constexpr int TSTEP = 1024;
    const unsigned va = (tid >> 3) * 2048 + (((tid & 7) ^ ((tid >> 4) & 7)) << 4);
    const unsigned vb = (tid >> 4) * 8192 + (((tid & 15) ^ ((((tid >> 4) & 3) << 2) | (((tid >> 7) & 1) << 1))) << 4);
    int ra[2], rb[2];
#pragma unroll
    for (int i = 0; i < 2; ++i) {
        ra[i] = (wr * 64 + fr) * 128 + (((4 * i + fq) ^ (fr >> 1)) << 4);
        rb[i] = (8 * fq + (fr >> 2)) * 256 + (((4 * wc + 2 * i + ((fr >> 1) & 1)) ^ (((fr >> 2) << 2) | ((fq & 1) << 1))) << 4) + (fr & 1) * 8;
    }
    const unsigned lb = (unsigned)(size_t)lds + w * 1024;
    const unsigned a0 = row0 * 2048, b0 = col0 * 2;
#define SA(b, hh) (((b) * 2 + (hh)) * HTB)
#define SB(b, hh) ((4 + (b) * 2 + (hh)) * HTB)
#define STA(b, hh, t) do { const unsigned s_ = a0 + (hh) * 262144 + (t) * 128; DMA16(rx, va, s_, lb + SA(b, hh)); DMA16(rx, va, s_ + 131072, lb + SA(b, hh) + 8192); } while (0)
#define STB(b, hh, t) do { const unsigned s_ = b0 + (hh) * 256 + (t) * 524288; DMA16(rw, vb, s_, lb + SB(b, hh)); DMA16(rw, vb, s_ + 262144, lb + SB(b, hh) + 8192); } while (0)
    v4f acc[2][2][4][2] = {};
    v4u At[4][2], B0[2][2], B1[2][2];
    STB(0, 0, 0); STA(0, 0, 0); STB(0, 1, 0); STA(0, 1, 0);
    if (wr == 1) RBAR();
    WAITV(4); RBAR();
    STB(1, 0, 1); STA(1, 0, 1); STB(1, 1, 1);
    WAITV(6); RBAR();
    for (int t = 0; t < 16; t += 2) {
        const int t2 = (t + 2) & 15, t3 = (t + 3) & 15;
        G_LDB(B0, SB(0, 0)); SCHED(); G_LDA(At, SA(0, 0)); STA(1, 1, t + 1);
        WAITL(8); RBAR(); WAITL(0); G_MMA(acc[0][0], At, B0); RBAR(); SCHED();
        G_LDB(B1, SB(0, 1)); STB(0, 0, t2);
        RBAR(); WAITL(0); G_MMA(acc[0][1], At, B1); RBAR();
        G_LDA(At, SA(0, 1)); STA(0, 0, t2);
        RBAR(); WAITL(0); G_MMA(acc[1][0], At, B0); RBAR(); SCHED();
        STB(0, 1, t2);
        WAITV(6); RBAR(); G_MMA(acc[1][1], At, B1); RBAR();
        G_LDB(B0, SB(1, 0)); SCHED(); G_LDA(At, SA(1, 0)); STA(0, 1, t2);
        WAITL(8); RBAR(); WAITL(0); G_MMA(acc[0][0], At, B0); RBAR(); SCHED();
        G_LDB(B1, SB(1, 1)); STB(1, 0, t3);
        RBAR(); WAITL(0); G_MMA(acc[0][1], At, B1); RBAR();
        G_LDA(At, SA(1, 1)); STA(1, 0, t3);
        RBAR(); WAITL(0); G_MMA(acc[1][0], At, B0); RBAR(); SCHED();
        STB(1, 1, t3);
        WAITV(6); RBAR(); G_MMA(acc[1][1], At, B1); RBAR();
    }
#pragma unroll
    for (int ai = 0; ai < 2; ++ai)
#pragma unroll
        for (int bj = 0; bj < 2; ++bj) qkv_store<VT>(acc[ai][bj], ro, row0 + ai * 128 + wr * 64, (col0 >> 7) + bj, wc, l);
    WAITV(0);
    if (wr == 0) RBAR();
    RBAR();
#undef STA
#undef STB
}

template <bool VT> DEV void gemm_qkv128(LAS char* lds, v4u rsa, v4u rsb, rsrc_t ro, int row0, int col0) {
    const int tid = threadIdx.x, w = __builtin_amdgcn_readfirstlane(tid >> 6), l = tid & 63, wr = w >> 2, wc = w & 3, fr = l & 15, fq = l >> 4;
    constexpr int TSTEP = 1024, NT = 16, LDW = 8192;
    const unsigned va = (tid >> 3) * 2048 + (((tid & 7) ^ ((tid >> 4) & 7)) << 4);
    const unsigned vb = (tid >> 4) * LDW + (((tid & 15) ^ ((((tid >> 4) & 3) << 2) | (((tid >> 7) & 1) << 1))) << 4);
    int ra[2], rb[2];
#pragma unroll
    for (int i = 0; i < 2; ++i) {
        ra[i] = (wr * 64 + fr) * 128 + (((4 * i + fq) ^ (fr >> 1)) << 4);
        rb[i] = (8 * fq + (fr >> 2)) * 256 + (((4 * wc + 2 * i + ((fr >> 1) & 1)) ^ (((fr >> 2) << 2) | ((fq & 1) << 1))) << 4) + (fr & 1) * 8;
    }
    const unsigned la = (unsigned)(size_t)lds + w * 1024, lb = la + HTB;
    const unsigned a0 = row0 * 2048, b0 = col0 * 2;
#define STG(sl, t) do { const unsigned sa_ = a0 + (t) * 128, sb_ = b0 + (t) * (64 * LDW); \
        DMA16(rsb, vb, sb_, lb + (sl) * 2 * HTB); DMA16(rsb, vb, sb_ + 32 * LDW, lb + (sl) * 2 * HTB + 8192); \
        DMA16(rsa, va, sa_, la + (sl) * 2 * HTB); DMA16(rsa, va, sa_ + 131072, la + (sl) * 2 * HTB + 8192); } while (0)
    v4f acc[4][2] = {};
    v4u At[4][2], Bt[2][2];
    STG(0, 0); STG(1, 1); STG(2, 2);
    if (wr == 1) RBAR();
    WAITV(8); RBAR(); RBAR();
    for (int t = 0; t < NT; t += 4) {
#pragma unroll
        for (int u = 0; u < 4; ++u) {
            G_LDB(Bt, u * 2 * HTB + HTB); G_LDA(At, u * 2 * HTB);
            STG((u + 3) & 3, (t + u + 3) & (NT - 1));
            WAITV(8); WAITL(0); RBAR(); G_MMA(acc, At, Bt); RBAR(); SCHED();
        }
    }
    qkv_store<VT>(acc, ro, row0 + wr * 64, col0 >> 7, wc, l);
    WAITV(0);
    if (wr == 0) RBAR();
    RBAR();
#undef STG
}

static constexpr unsigned F_QKV = 1024, F_PG = 16384, F_XA = 20480, F_M1 = 24576, F_M2 = F_M1 + 256, CTL_SZ = 32768, F_PM = CTL_SZ;
static constexpr unsigned POI = 0xFFFFFFFFu, OOB = 0x40000000u, SPIN_CAP = 200000u;
DEV bool spin_fail(unsigned tries, unsigned* err) {
    __builtin_amdgcn_s_sleep(1);
    if ((tries & 255u) != 255u) return false;
    unsigned e; asm volatile("global_load_dword %0, %1, off sc1\n\ts_waitcnt vmcnt(0)" : "=&v"(e) : "v"(err) : "memory");
    if (__builtin_amdgcn_readfirstlane(e) == 1u) return true;
    if (tries > SPIN_CAP) { unsigned one = 1u; asm volatile("global_store_dword %0, %1, off sc1" :: "v"(err), "v"(one) : "memory"); return true; }
    return false;
}
DEV bool all_ge(unsigned v, unsigned t) { return __builtin_amdgcn_ballot_w64(v < t) == 0; }
DEV void wait_flags(rsrc_t rc, unsigned off, unsigned t, unsigned* err) {
    for (unsigned tries = 0;; ++tries) {
        FENCE();
        if (all_ge(LD4(rc, off, 0), t)) break;
        __builtin_amdgcn_s_sleep(6);
        if (spin_fail(tries, err)) break;
    }
    FENCE();
}
DEV unsigned qkv_flag(int m, int rb, int h) { const bool big = m == 0 || rb >= 4; return F_QKV + (((big ? rb & ~1 : rb) * NH + (big ? h & ~1 : h)) * 4 + m) * 4; }

#define LDB(RS, K, BASE) LD16(RS, l * 16 + ((K) & 3) * 1024, (BASE) + ((K) >> 2) * 4096)
DEV void xattn(rsrc_t rq, rsrc_t rk, rsrc_t rv, rsrc_t rm, rsrc_t ro, rsrc_t rc, unsigned* err, int seg, int h, int l) {
    asm volatile("" : "+v"(l));
    const int fr = l & 15, g = l >> 4;
    const unsigned sb = (unsigned)(seg * NH + h) * 8192u, mb = (unsigned)(seg * NH + h) * 4096u;
    wait_flags(rc, qkv_flag(min(l, 2), seg >> 2, h), 1, err);
    v4u q[4][2], km[4], v[8]; v2u vm[8];
    v4f sx[3] = {};
    {
        v4u k[4][2];
#pragma unroll
        for (int j = 0; j < 4; ++j) {
            q[j][0] = LDB(rq, 2 * j, sb); q[j][1] = LDB(rq, 2 * j + 1, sb);
            k[j][0] = LDB(rk, 2 * j, sb); k[j][1] = LDB(rk, 2 * j + 1, sb);
        }
#pragma unroll
        for (int d = 0; d < 8; ++d) v[d] = LDB(rv, d, sb);
#pragma unroll
        for (int j = 0; j < 4; ++j) { sx[0] = mf(k[j][0], q[j][0], sx[0]); sx[1] = mf(k[j][0], q[j][1], sx[1]); sx[2] = mf(k[j][1], q[j][1], sx[2]); }
    }
    SCHED();
    wait_flags(rc, F_PG + h * 128 + (l & 3) * 4, seg + 1, err);
#pragma unroll
    for (int j = 0; j < 4; ++j) km[j] = LD16(rm, (g * 8 + (l & 7)) * 16 + j * 512, mb);
#pragma unroll
    for (int d = 0; d < 8; ++d) vm[d] = LD8(rm, 2048 + fr * 16 + (g & 1) * 8 + d * 256, mb);
    v4f sm[2] = {};
#pragma unroll
    for (int j = 0; j < 4; ++j) { sm[0] = mf(km[j], q[j][0], sm[0]); sm[1] = mf(km[j], q[j][1], sm[1]); }
    SCHED();
#pragma unroll
    for (int u = 0; u < 2; ++u) {
        float e[12];
#pragma unroll
        for (int r = 0; r < 4; ++r) {
            const bool vis = 4 * g + r <= fr;
            e[r] = (u || vis) ? rbf(rbf(sx[u][r]) * SCALE) : MASKV;
            e[4 + r] = (u && vis) ? rbf(rbf(sx[2][r]) * SCALE) : MASKV;
            e[8 + r] = g < 2 ? rbf(rbf(sm[u][r]) * SCALE) : -INFINITY;
        }
        float m = e[0];
#pragma unroll
        for (int r = 1; r < 12; ++r) m = fmaxf(m, e[r]);
        m = red_g<true>(m);
        float sum = 0;
#pragma unroll
        for (int r = 0; r < 12; ++r) { e[r] = __builtin_amdgcn_exp2f((e[r] - m) * LOG2E); sum += e[r]; }
        const float inv = __builtin_amdgcn_rcpf(red_g<false>(sum));
        const v4u P = {pk2(e[0] * inv, e[1] * inv), pk2(e[4] * inv, e[5] * inv), pk2(e[2] * inv, e[3] * inv), pk2(e[6] * inv, e[7] * inv)};
        const unsigned pm0 = pk2(e[8] * inv, e[9] * inv), pm1 = pk2(e[10] * inv, e[11] * inv);
        const v4u Pa = {pm0, pm1, 0u, 0u}, Pb = {0u, 0u, pm0, pm1};
#pragma unroll
        for (int d = 0; d < 4; ++d) {
            const v4u vv = cat(vm[2 * d], vm[2 * d + 1]);
            const v4f o0 = mf(vv, Pa, mf(v[2 * d], P, v4f{0, 0, 0, 0})), o1 = mf(vv, Pb, mf(v[2 * d + 1], P, v4f{0, 0, 0, 0}));
            const v4u o = {pk2(o0[0], o0[1]), pk2(o0[2], o0[3]), pk2(o1[0], o1[1]), pk2(o1[2], o1[3])};
            ST16(ro, l * 16 + d * 1024, (unsigned)((seg * 2 + u) * 128 + h * 4) * 1024u, o);
        }
    }
    WAITV(0); RBAR();
    if (threadIdx.x == 0) ST4(rc, F_XA + seg * 64 + (h >> 3) * 4, 0, 1u);
}

DEV void prep_row(const bf16_t* x, const bf16_t* gw, const bf16_t* wr, float* gr, int row, int l) {
    float a = 0, b = 0;
#pragma unroll
    for (int i = 0; i < 2; ++i) {
        const v4u xv = *(const v4u*)(x + (size_t)row * DM + i * 512 + l * 8), gv = *(const v4u*)(gw + i * 512 + l * 8), rv = *(const v4u*)(wr + i * 512 + l * 8);
#pragma unroll
        for (int e = 0; e < 4; ++e) {
            a += rbf(lo16(xv[e]) * lo16(gv[e])) + rbf(hi16(xv[e]) * hi16(gv[e]));
            b += lo16(xv[e]) * lo16(rv[e]) + hi16(xv[e]) * hi16(rv[e]);
        }
    }
#pragma unroll
    for (int o = 1; o < 16; o <<= 1) { a += __shfl_xor(a, o); b += __shfl_xor(b, o); }
    a = red_g<false>(a); b = red_g<false>(b);
    if (l == 0) *(v2f*)(gr + 2 * row) = v2f{rbf(sigmoidf_(rbf(a))), rbf(b)};
}
DEV int top8(rsrc_t rg, int l) {
    unsigned key[32];
#pragma unroll
    for (int i = 0; i < 16; ++i) {
        const v4u t = LD16(rg, l * 16 + i * 1024, 0);
        const int row = i * 128 + l * 2;
        key[2 * i] = ((int)t[1] < 0 ? 0xffff0000u - t[1] : t[1] + 0x80000000u) + (0xffffu - row);
        key[2 * i + 1] = ((int)t[3] < 0 ? 0xffff0000u - t[3] : t[3] + 0x80000000u) + (0xffffu - row - 1);
    }
    int res = 0;
#pragma unroll 1
    for (int c = 0; c < TOPC; ++c) {
        unsigned m = 0;
#pragma unroll
        for (int i = 0; i < 32; ++i) m = max(m, key[i]);
#pragma unroll
        for (int o = 1; o < 64; o <<= 1) m = max(m, (unsigned)__shfl_xor((int)m, o));
#pragma unroll
        for (int i = 0; i < 32; ++i) key[i] = key[i] == m ? 0u : key[i];
        if (l == c) res = 0xffff - (int)(m & 0xffffu);
    }
    return res;
}
template <int NKS> DEV void skinny32(LAS char* wl, v4u rw, unsigned ldw, unsigned off, const v4u (&a)[NKS], v4f (&c)[2], int l) {
    const unsigned vo = (l >> 2) * ldw + (l & 3) * 16, lb = (unsigned)(size_t)wl;
#pragma unroll
    for (int i = 0; i < 2 * NKS; ++i) DMA16(rw, vo, off + i * 16 * ldw, lb + i * 1024);
    WAITV(0);
    const LAS char* p = wl + (8 * (l >> 4) + ((l & 15) >> 2)) * 64 + (l & 3) * 8;
    c[0] = v4f{0, 0, 0, 0}; c[1] = c[0];
#pragma unroll
    for (int k = 0; k < NKS; ++k)
#pragma unroll
        for (int n = 0; n < 2; ++n) c[n] = mf(a[k], cat(trd(p + k * 2048 + n * 32), trd(p + k * 2048 + n * 32 + 256)), c[n]);
}
DEV void moe1(LAS char* lds, const bf16_t* x, int sel, v4u rwg, v4u rwu, rsrc_t rh, int cb) {
    const int tid = threadIdx.x, w = __builtin_amdgcn_readfirstlane(tid >> 6), l = tid & 63;
    const int row = __shfl(sel, l & 7);
    v4u a[4];
#pragma unroll
    for (int k = 0; k < 4; ++k) a[k] = *(const v4u*)(x + (size_t)row * DM + w * 128 + k * 32 + (l >> 4) * 8);
    v4f cg[2], cu[2];
    skinny32<4>(lds + w * 16384, rwg, FF * 2, (w * 128 * FF + cb * 32) * 2, a, cg, l);
    skinny32<4>(lds + w * 16384 + 8192, rwu, FF * 2, (w * 128 * FF + cb * 32) * 2, a, cu, l);
    BAR();
    LAS v4f* red = (LAS v4f*)lds;
    if (l < 32) { red[(w * 4 + 0) * 32 + l] = cg[0]; red[(w * 4 + 1) * 32 + l] = cg[1]; red[(w * 4 + 2) * 32 + l] = cu[0]; red[(w * 4 + 3) * 32 + l] = cu[1]; }
    BAR();
    if (tid < 128) {
        const int tok = tid >> 4, c0 = 2 * (tid & 15);
        float hv[2];
#pragma unroll
        for (int e = 0; e < 2; ++e) {
            const int cc = c0 + e; const LAS float* p = (const LAS float*)lds + ((cc >> 4) * 32 + (tok >> 2) * 16 + (cc & 15)) * 4 + (tok & 3);
            float g = 0, u = 0;
#pragma unroll
            for (int q = 0; q < 8; ++q) { g += p[q * 512]; u += p[q * 512 + 256]; }
            g = rbf(g); hv[e] = rbf(g * sigmoidf_(g)) * rbf(u);
        }
        ST4(rh, (tok * FF + cb * 32 + c0) * 2, 0, pk2(hv[0], hv[1]));
    }
    BAR();
}
DEV void moe2(LAS char* lds, rsrc_t rh, rsrc_t rg, rsrc_t ri, v4u rwd, rsrc_t ru, int cb) {
    const int tid = threadIdx.x, w = __builtin_amdgcn_readfirstlane(tid >> 6), l = tid & 63;
    v4f c[2], c2[2];
    {
        v4u a[7];
#pragma unroll
        for (int k = 0; k < 7; ++k) a[k] = LD16(rh, ((l & 7) * FF + w * 448 + k * 32 + (l >> 4) * 8) * 2, 0);
        skinny32<7>(lds + w * 16384, rwd, DM * 2, (w * 448 * DM + cb * 32) * 2, a, c, l);
#pragma unroll
        for (int k = 0; k < 7; ++k) a[k] = LD16(rh, ((l & 7) * FF + w * 448 + 224 + k * 32 + (l >> 4) * 8) * 2, 0);
        WAITL(0);
        skinny32<7>(lds + w * 16384, rwd, DM * 2, ((w * 448 + 224) * DM + cb * 32) * 2, a, c2, l);
    }
    BAR();
    LAS v4f* red = (LAS v4f*)lds;
    if (l < 32) { red[(w * 2 + 0) * 32 + l] = c[0] + c2[0]; red[(w * 2 + 1) * 32 + l] = c[1] + c2[1]; }
    BAR();
    if (tid < 256) {
        const int tok = tid >> 5, cc = tid & 31; const LAS float* p = (const LAS float*)lds + ((cc >> 4) * 32 + (tok >> 2) * 16 + (cc & 15)) * 4 + (tok & 3);
        float y = 0;
#pragma unroll
        for (int q = 0; q < 8; ++q) y += p[q * 256];
        const int row = (int)LD4(ri, tok * 4, 0);
        const float r = __uint_as_float(LD4(rg, row * 8 + 4, 0));
        ST4(ru, (tok * DM + cb * 32 + cc) * 4, 0, __float_as_uint(rbf(rbf(y) * rbf(sigmoidf_(r)))));
    }
    BAR();
}

#define EXACT 0
static constexpr unsigned H2_OFF = 0, H1_OFF = 512 * 1024, H3_OFF = 768 * 1024, AN_OFF = 832 * 1024, PR_OFF = 896 * 1024;
static constexpr size_t SLOT = 2u << 20;
static constexpr int NSLOT = 4;

struct Args {
    const bf16_t* x; const bf16_t* mem0; const bf16_t* Wq; const bf16_t* Wk; const bf16_t* Wv; const bf16_t* Wo;
    const bf16_t* w_r; const bf16_t* wg; const bf16_t* wu; const bf16_t* wd; const bf16_t* gw;
    char* qkv; char* mb; char* ob; float* gr; int* idx; bf16_t* hid; float* upd; bf16_t* out;
    char* ring; unsigned* ctl; unsigned* dbg;
    int d1, d2, d3, dv, ml, slack, thr, dp;
};
static constexpr unsigned QSZ = S_TOK * HD * 2;

DEV unsigned nearo(unsigned off) { return (off >> 13) * 16384u + (off & 8191u); }
DEV unsigned rt32() { u64 t; asm volatile("s_memrealtime %0\n\ts_waitcnt lgkmcnt(0)" : "=s"(t) :: "memory"); return (unsigned)t; }
DEV unsigned cyc32() { u64 t; asm volatile("s_memtime %0\n\ts_waitcnt lgkmcnt(0)" : "=s"(t) :: "memory"); return (unsigned)t; }
DEV unsigned wait_until(unsigned T, int ml, int thr) {
    unsigned late, tmp;
    T = __builtin_amdgcn_readfirstlane(T);
    asm volatile(
        "s_memrealtime vcc\n\ts_waitcnt lgkmcnt(0)\n\t"
        "s_sub_i32 %[tmp], %[T], vcc_lo\n\t"
        "s_min_i32 %[tmp], %[tmp], 0x200\n\t"
        "s_add_i32 %[late], %[tmp], %[thr]\n\t"
        "s_cmp_lt_i32 %[late], 0\n\t"
        "s_cselect_b32 %[tmp], %[ml], %[tmp]\n\t"
        "s_cselect_b32 %[late], 1, 0\n\t"
        "s_add_i32 %[T], vcc_lo, %[tmp]\n\t"
        "s_cmp_lt_i32 %[tmp], 5\n\t"
        "s_cbranch_scc1 2f\n"
        "1:\n\t"
        "s_memrealtime vcc\n\ts_waitcnt lgkmcnt(0)\n\t"
        "s_sub_i32 %[tmp], %[T], vcc_lo\n\t"
        "s_cmp_gt_i32 %[tmp], 4\n\t"
        "s_cbranch_scc1 1b\n"
        "2:\n\t"
        "s_sub_i32 %[tmp], %[tmp], 2\n"
        "3:\n\t"
        "s_sub_i32 %[tmp], %[tmp], 1\n\t"
        "s_cmp_ge_i32 %[tmp], 0\n\t"
        "s_cbranch_scc1 3b\n\t"
        : [tmp] "=&s"(tmp), [late] "=&s"(late), [T] "+s"(T) : [ml] "s"(ml), [thr] "s"(thr) : "scc", "vcc", "memory");
    return late;
}
DEV void lds_wait(volatile LAS unsigned* p, unsigned v) { while ((int)(__builtin_amdgcn_readfirstlane(*p) - v) < 0) __builtin_amdgcn_s_sleep(1); FENCE(); }
DEV void lds_post(volatile LAS unsigned* p, unsigned v) { WAITL(0); *p = v; }
DEV unsigned max4(unsigned m, v4u r) { m = max(max(m, r[0]), r[1]); return max(max(m, r[2]), r[3]); }
DEV bool any_poison(unsigned mx) { return __builtin_amdgcn_ballot_w64(mx == POI) != 0; }
DEV bool any_nan(float a, float b) { return __builtin_amdgcn_ballot_w64(__builtin_isunordered(a, b)) != 0; }
template <int RS> DEV v8u rd_w(const LAS char* p) {
    return __builtin_shufflevector(cat(trd(p), trd(p + 32 * RS)), cat(trd(p + 8 * RS), trd(p + 40 * RS)), 0, 1, 2, 3, 4, 5, 6, 7);
}

static constexpr int PQ_LD = 68 * 4, L_PQK = 0, L_PV = 9216, L_O = 13312, L_X = 16384, X_SZ = 10240, L_MISC = 131072, L_TOT = L_MISC + 256;
static constexpr int O_LD = 272;

#define STAMP(k) do {} while (0)
#define STAMP_DECL do {} while (0)
#define STAMP_OUT() do {} while (0)
#define CNT(x, v) (void)(v)

DEV void chain(LAS char* lds, const Args& a, const int c) {
    const int tid = threadIdx.x, w = __builtin_amdgcn_readfirstlane(tid >> 6), l = tid & 63;
    const int h = c >> 2, j = c & 3;
    const int l15 = l & 15, g = l >> 4, l7 = l & 7;
    const bool hb = (l & 8) != 0;
    const int idx = hb ? 0xEEEE : 0x4444;
    unsigned* err = a.ctl;
    LAS unsigned* misc = (LAS unsigned*)(lds + L_MISC);
    STAMP_DECL;
#define TLOOP() do {} while (0)
    unsigned anc = 0;
    bool dead = false;
    const rsrc_t rr = mkrsrc(a.ring, NSLOT * SLOT);
    unsigned pm = 0;
#pragma unroll
    for (int k = 0; k < NSLOT; ++k) pm |= (a.ctl[F_PM / 4 + 2 * k + 1] < a.ctl[F_PM / 4 + 2 * k] ? 1u : 0u) << k;
    pm = __builtin_amdgcn_readfirstlane(pm);
#define SLOTOFF(S) (((S) & 3) * (unsigned)SLOT + ((pm >> ((S) & 3)) & 1) * 8192u)
    const int wrow = 4 * (g >> 1) + 2 * (l15 >> 3) + (g & 1) + 16 * ((l15 >> 2) & 1);
    const unsigned lbw = (unsigned)(size_t)lds + w * 16384;

    if (w < 4) {
        v8u wt[3][4][2];
        {
            const v4u rw[3] = {mkrs(a.Wq, DM * HD * 2), mkrs(a.Wk, DM * HD * 2), mkrs(a.Wv, DM * HD * 2)};
            const unsigned vo = (l >> 2) * (HD * 2) + (l & 3) * 16, so = (256 * w * HD + h * DH + 32 * j) * 2;
            auto issue = [&](int p) {
#pragma unroll
                for (int i = 0; i < 8; ++i) DMA16(rw[p >> 1], vo, so + ((p & 1) * 128 + i * 16) * (HD * 2), lbw + (p & 1) * 8192 + i * 1024); };
            issue(0); issue(1);
            const LAS char* pw = lds + w * 16384 + wrow * 64 + (l & 3) * 8;
#pragma unroll
            for (int p = 0; p < 6; ++p) {
                if (p < 5) WAITV(8); else WAITV(0);
#pragma unroll
                for (int k = 0; k < 2; ++k) { wt[p >> 1][2 * (p & 1) + k][0] = rd_w<64>(pw + (p & 1) * 8192 + k * 4096); wt[p >> 1][2 * (p & 1) + k][1] = rd_w<64>(pw + (p & 1) * 8192 + k * 4096 + 32); }
                WAITL(0);
                if (p < 4) issue(p + 2);
            }
        }
        const unsigned o3 = nearo(H3_OFF + (c >> 5) * 16384 + w * 4096) + l * 16, oa = nearo(AN_OFF + (c >> 5) * 128);
        v4u m[4];
#pragma unroll
        for (int k = 0; k < 4; ++k) {
            const bf16_t* mp = a.mem0 + l7 * DM + 256 * w + 64 * k + 2 * (l >> 3);
            const unsigned t0 = *(const unsigned*)mp, t1 = *(const unsigned*)(mp + 16), t2 = *(const unsigned*)(mp + 32), t3 = *(const unsigned*)(mp + 48);
            m[k] = v4u{__builtin_amdgcn_perm(t1, t0, 0x05040100u), __builtin_amdgcn_perm(t3, t2, 0x05040100u), __builtin_amdgcn_perm(t1, t0, 0x07060302u), __builtin_amdgcn_perm(t3, t2, 0x07060302u)};
        }
        BAR();
        anc = rt32();
        LAS float* pq = (LAS float*)(lds + L_PQK + w * 8 * PQ_LD + l * 4);
        for (int s = 0; s < NSEG; ++s) {
            const unsigned cur = SLOTOFF(s);
            unsigned an = anc;
            auto ld = [&]() { an = LD4(rr, oa, cur);
#pragma unroll
                for (int k = 0; k < 4; ++k) m[k] = LD16(rr, o3 + k * 1024, cur); };
            if (s > 0) {
                lds_wait(misc + 3, s);
                CNT(nlate, wait_until(anc + a.d3, a.ml, a.thr));
                ld();
            }
            for (unsigned tries = 0;; ++tries) {
                v4f q0 = {}, q1 = {}, k0 = {}, k1 = {};
#pragma unroll
                for (int ks = 0; ks < 4; ++ks) { q0 = smf(m[ks], wt[0][ks][0], q0, idx); q1 = smf(m[ks], wt[0][ks][1], q1, idx); k0 = smf(m[ks], wt[1][ks][0], k0, idx); k1 = smf(m[ks], wt[1][ks][1], k1, idx); }
                float hq[4], hk[4];
#pragma unroll
                for (int r = 0; r < 4; ++r) { hq[r] = half_add(q0[r], q1[r]); pq[r * 68] = hq[r]; }
#pragma unroll
                for (int r = 0; r < 4; ++r) { hk[r] = half_add(k0[r], k1[r]); pq[(4 + r) * 68] = hk[r]; }
                const bool sus = __builtin_amdgcn_ballot_w64(__builtin_isunordered(hq[0], hq[1]) || __builtin_isunordered(hq[2], hq[3]) || an == POI) != 0;
                if (__builtin_expect((!sus && !(EXACT & 1)) || dead || s == 0, 1)) break;
                if (!any_poison(max4(max4(max4(max4(an, m[0]), m[1]), m[2]), m[3]))) break;
                CNT(ntry, 1); dead = spin_fail(tries, err); ld();
            }
            anc = __builtin_amdgcn_readfirstlane(an);
            if (w == 0) misc[0] = anc;
            STAMP(0);
            BAR();
            v4f v0 = {}, v1 = {};
#pragma unroll
            for (int ks = 0; ks < 4; ++ks) { v0 = smf(m[ks], wt[2][ks][0], v0, idx); v1 = smf(m[ks], wt[2][ks][1], v1, idx); }
            *(volatile LAS v4f*)(lds + L_PV + w * 1024 + l * 16) = v4f{half_add(v0[0], v1[0]), half_add(v0[1], v1[1]), half_add(v0[2], v1[2]), half_add(v0[3], v1[3])};
            *(volatile LAS unsigned*)(misc + 8 + w) = s + 1;
            STAMP(1);
            BAR();
        }
    } else {
        const int bw = w - 4;
        v8u wo[2][4];
        {
            const v4u rw = mkrs(a.Wo, HD * DM * 2);
            const unsigned vo = (l >> 3) * (DM * 2) + (l & 7) * 16, so = (h * DH * DM + 256 * j + 64 * bw) * 2;
#pragma unroll
            for (int i = 0; i < 16; ++i) DMA16(rw, vo, so + i * 8 * (DM * 2), lbw + i * 1024);
        }
        if (w == 6 && l < 16) misc[l] = l == 4 ? 0xffffffffu : 0u;
        WAITV(0);
        {
            const LAS char* pw = lds + w * 16384 + wrow * 128 + (l & 3) * 8;
#pragma unroll
            for (int ks = 0; ks < 2; ++ks)
#pragma unroll
                for (int t = 0; t < 4; ++t) wo[ks][t] = rd_w<128>(pw + ks * 8192 + t * 32);
        }
        const unsigned o2s = nearo(H2_OFF + (32 * j + 8 * bw + (l15 >> 1)) * 4096) + h * 128 + (4 * (g & 1) + 2 * (g >> 1) + (l & 1)) * 16;
        auto wo_stage = [&](unsigned cur) {
            const LAS v4u* op = (const LAS v4u*)(lds + L_O + l7 * O_LD + (l >> 3) * 16);
            const v4u a0 = op[0], a1 = op[8];
            v4f d[4];
#pragma unroll
            for (int t = 0; t < 4; ++t) d[t] = smf(a1, wo[1][t], smf(a0, wo[0][t], v4f{0, 0, 0, 0}, idx), idx);
            float e[2][4];
#pragma unroll
            for (int t = 0; t < 4; ++t) { e[0][t] = half_add(d[t][0], d[t][2]); e[1][t] = half_add(d[t][1], d[t][3]); }
            ST16(rr, o2s, cur, (v4u{pk2(e[0][0], e[0][1]), pk2(e[0][2], e[0][3]), pk2(e[1][0], e[1][1]), pk2(e[1][2], e[1][3])}));
        };
        const unsigned h1 = nearo(H1_OFF + h * 8192);
        if (w < 6) {
            const unsigned o1 = h1 + ((hb ? 4 : 0) + g) * 128 + l7 * 16;
            const unsigned om = h1 + 1024 + ((g & 1) * 8 + l7) * 16 + ((hb ? 2 : 0) + (g >> 1)) * 4;
            const unsigned ov = h1 + 2 * bw * 2048 + 1280 + l15 * 16 + 8 * (g >> 1);
            const unsigned pmask = g >= 2 ? 0xffff0000u : 0x0000ffffu;
            const unsigned one2 = 0x3f803f80u;
            const v8u ones = {one2, one2, one2, one2, one2, one2, one2, one2};
            v8u vB[4];
#pragma unroll
            for (int t = 0; t < 4; ++t) vB[t] = v8u{0, 0, 0, 0, 0, 0, 0, 0};
            BAR();
            TLOOP();
            for (int s = 0; s + 1 < NSEG; ++s) {
                const unsigned cur = SLOTOFF(s);
                lds_wait(misc + 4, s);
                if (!(g & 1)) {
#pragma unroll
                    for (int t = 0; t < 4; ++t) {
                        const LAS v4u* vp = (const LAS v4u*)(lds + L_X + (s & 1) * X_SZ + (bw * 4 + t) * 1024 + ((g >> 1) * 16 + l15) * 16);
                        vB[t] = __builtin_shufflevector(vp[0], vp[32], 0, 1, 2, 3, 4, 5, 6, 7);
                    }
                }
                v4u sp[4]; unsigned sm[4]; v2u vm[4];
                BAR();
                anc = __builtin_amdgcn_readfirstlane(misc[0]);
                CNT(nlate, wait_until(anc + a.d1, a.ml + 10, a.thr - 10));
                auto ld_s = [&]() {
#pragma unroll
                    for (int k = 0; k < 4; ++k) { sp[k] = LD16(rr, o1 + k * 2048, cur); sm[k] = LD4(rr, om + k * 2048, cur); } };
                auto ld_v = [&]() {
#pragma unroll
                    for (int t = 0; t < 4; ++t) vm[t] = LD8(rr, ov + (t >> 1) * 2048 + (t & 1) * 256, cur); };
                ld_s();
                wait_until(anc + a.d1 + a.dv, a.dv, a.thr);
                ld_v();
                FENCE();
                for (unsigned tries = 0;; ++tries) {
                    float e[5];
#pragma unroll
                    for (int r = 0; r < 4; ++r) e[r] = (__uint_as_float(sp[0][r]) + __uint_as_float(sp[1][r])) + (__uint_as_float(sp[2][r]) + __uint_as_float(sp[3][r]));
                    e[4] = (__uint_as_float(sm[0]) + __uint_as_float(sm[1])) + (__uint_as_float(sm[2]) + __uint_as_float(sm[3]));
                    STAMP(0);
                    float m = fmaxf(fmaxf(fmaxf(e[0], e[1]), fmaxf(e[2], e[3])), e[4]);
                    m = red_g<true>(fmaxf(m, ror8(m)));
                    m = rbf(rbf(m) * SCALE) * LOG2E;
#pragma unroll
                    for (int r = 0; r < 5; ++r) e[r] = __builtin_amdgcn_exp2f(__builtin_fmaf(rbf(rbf(e[r]) * SCALE), LOG2E, -m));
                    const v4u P = {pk2(e[0], e[1]), pk2(e[2], e[3]), pk2(e[4], e[4]) & pmask, 0u};
#pragma unroll
                    for (int t = 0; t < 4; ++t) { vB[t][0] = (g & 1) ? vm[t][0] : vB[t][0]; vB[t][1] = (g & 1) ? vm[t][1] : vB[t][1]; vB[t][4] = (g & 1) ? vm[t][0] : vB[t][4]; vB[t][5] = (g & 1) ? vm[t][1] : vB[t][5]; }
                    const v4f sd = smf(P, ones, v4f{0, 0, 0, 0}, idx);
                    v4f o[4];
#pragma unroll
                    for (int t = 0; t < 4; ++t) o[t] = smf(P, vB[t], v4f{0, 0, 0, 0}, idx);
                    const float i0 = __builtin_amdgcn_rcpf(half_add(sd[0], sd[2])), i1 = __builtin_amdgcn_rcpf(half_add(sd[1], sd[3]));
                    float y[2][4];
#pragma unroll
                    for (int t = 0; t < 4; ++t) { y[0][t] = half_add(o[t][0], o[t][2]) * i0; y[1][t] = half_add(o[t][1], o[t][3]) * i1; }
                    LAS v2u* op = (LAS v2u*)(lds + L_O + (4 * (g & 1) + 2 * (g >> 1)) * O_LD + bw * 128 + l15 * 8);
                    op[0] = v2u{pk2(y[0][0], y[0][1]), pk2(y[0][2], y[0][3])}; op[O_LD / 8] = v2u{pk2(y[1][0], y[1][1]), pk2(y[1][2], y[1][3])};
                    const bool sus = __builtin_amdgcn_ballot_w64(__builtin_isunordered(y[0][0], y[0][1]) || __builtin_isunordered(y[1][2], y[1][3])) != 0;
                    if (__builtin_expect((!sus && !(EXACT & 2)) || dead, 1)) break;
                    unsigned mx = max4(max4(max4(max4(0, sp[0]), sp[1]), sp[2]), sp[3]);
                    mx = max4(max4(max4(mx, v4u{sm[0], sm[1], sm[2], sm[3]}), cat(vm[0], vm[1])), cat(vm[2], vm[3]));
                    if (!any_poison(mx)) break;
                    CNT(ntry, 1); dead = spin_fail(tries, err); ld_s(); ld_v();
                }
                STAMP(1);
                BAR();
                wo_stage(cur);
                STAMP(2);
            }
            BAR(); BAR();
        } else if (w == 6) {
            const rsrc_t rm = mkrsrc(a.mb, NSEG * NH * 4096);
            const unsigned o1s = hb ? OOB : h1 + j * 2048 + g * 128 + l7 * 16;
            const unsigned oms = (hb || g < 2) ? OOB : h1 + j * 2048 + 1024 + ((g & 1) * 8 + l7) * 16;
            const unsigned oks = hb ? h * 4096 + j * 512 + (g * 8 + l7) * 16 : OOB;
            const unsigned o2l = nearo(H2_OFF + c * 4096) + l * 16;
            const unsigned o3s = (l & 1) ? OOB : nearo(H3_OFF + (2 * (g >> 1) + (hb ? 1 : 0)) * 16384 + c * 128) + ((l7 & 6) + (g & 1)) * 16;
            const unsigned oas = nearo(AN_OFF + (l & 3) * 128);
            v4u sel;
#pragma unroll
            for (int i = 0; i < 4; ++i) sel[i] = (l7 == 2 * i ? 0x3f80u : 0u) | (l7 == 2 * i + 1 ? 0x3f800000u : 0u);
            const LAS v4f* rp = (const LAS v4f*)(lds + L_PQK + ((hb ? 4 : 0) + (l & 3)) * PQ_LD + ((l7 >> 2) * 16 + g * 4) * 4);
            BAR();
            for (int s = 0; s < NSEG; ++s) {
                const unsigned cur = SLOTOFF(s);
                const unsigned nxt = SLOTOFF(s + 1);
                lds_wait(misc + 4, min(s, NSEG - 2));
                const LAS v4u* kp = (const LAS v4u*)(lds + L_X + (s & 1) * X_SZ + 8192 + l * 16);
                const v4u kx0 = kp[0], kx1 = kp[64];
                BAR();
                {
                    const v4f x0 = (rp[0] + rp[8 * 17]) + (rp[16 * 17] + rp[24 * 17]), x1 = (rp[8] + rp[8 * 17 + 8]) + (rp[16 * 17 + 8] + rp[24 * 17 + 8]);
                    const v4u X = {pk2(x0[0], x0[1]), pk2(x0[2], x0[3]), pk2(x1[0], x1[1]), pk2(x1[2], x1[3])};
                    const v4f s0 = mf(kx0, X, v4f{0, 0, 0, 0}), s1 = mf(kx1, X, v4f{0, 0, 0, 0}), s2 = mf(X, X, v4f{0, 0, 0, 0});
                    if (EXACT & 16) { SCHED(); asm volatile("s_nop 7\n\ts_nop 7"); SCHED(); }
                    ST16(rr, o1s, cur, __builtin_bit_cast(v4u, s0)); ST16(rr, o1s + 512, cur, __builtin_bit_cast(v4u, s1)); ST16(rr, oms, cur, __builtin_bit_cast(v4u, s2));
                    ST16(rm, oks, s * (NH * 4096), X);
                }
                if (s + 1 == NSEG) WAITV(0);
                anc = __builtin_amdgcn_readfirstlane(misc[0]);
                STAMP(0);
                BAR();
                if (s + 1 == NSEG) break;
                wo_stage(cur);
                STAMP(1);
                unsigned late = wait_until(anc + a.d2, a.ml, a.thr);
                CNT(nlate, late);
                v4u p[4]; v4f d;
                for (unsigned tries = 0;; ++tries) {
#pragma unroll
                    for (int k = 0; k < 4; ++k) p[k] = LD16(rr, o2l + k * 1024, cur);
                    d = v4f{0, 0, 0, 0};
#pragma unroll
                    for (int k = 0; k < 4; ++k) d = mf(sel, p[k], d);
                    if (__builtin_expect((!any_nan(d[0], d[0]) && !(EXACT & 4)) || dead, 1)) break;
                    if (!any_poison(max4(max4(max4(max4(0, p[0]), p[1]), p[2]), p[3]))) break;
                    late = 1; CNT(ntry, 1); dead = spin_fail(tries, err);
                }
                STAMP(2);
#pragma unroll
                for (int r = 0; r < 4; ++r) d[r] += ror8(d[r]);
                const unsigned u0 = pk2(d[0], d[1]), u1 = pk2(d[2], d[3]);
                ST16(rr, o3s, nxt, (v4u{u0, u1, (unsigned)__builtin_amdgcn_mov_dpp((int)u0, 0xb1, 0xf, 0xf, false), (unsigned)__builtin_amdgcn_mov_dpp((int)u1, 0xb1, 0xf, 0xf, false)}));
                if (c == 0) { const unsigned tt = min(rt32() + (late ? a.slack : 0), 0xfffffffeu); if (l < 4) ST4(rr, oas, nxt, tt); }
                lds_post(misc + 3, s + 1);
                STAMP(3);
            }
        } else {
            const rsrc_t rm = mkrsrc(a.mb, NSEG * NH * 4096);
            const unsigned ovs = (g & 1) ? OOB : h1 + j * 2048 + 1280 + ((g >> 1) * 16 + l15) * 16;
            const unsigned ovm = (g & 1) ? OOB : h * 4096 + 2048 + (32 * j + (g >> 1) * 16 + l15) * 16;
            const unsigned p2 = nearo(H2_OFF + c * 4096) + l * 16, p1 = h1 + j * 2048 + l * 16;
            const unsigned p3 = l < 32 ? nearo(H3_OFF + (l >> 3) * 16384 + c * 128) + l7 * 16 : (c == 0 ? nearo(AN_OFF) + (l - 32) * 16 : OOB);
            const v4u poi4 = {POI, POI, POI, POI};
            v4u pf[10];
            const rsrc_t rk = mkrsrc(a.qkv + QSZ, QSZ), rv = mkrsrc(a.qkv + 2 * (size_t)QSZ, QSZ), rc = mkrsrc(a.ctl, CTL_SZ);
            auto issue = [&](int sg) {
                const unsigned sb = (unsigned)(sg * NH + h) * 8192u;
#pragma unroll
                for (int i = 0; i < 8; ++i) pf[i] = LD16(rv, l * 16 + (i & 3) * 1024, sb + (i >> 2) * 4096);
                pf[8] = LD16(rk, l * 16, sb + j * 2048); pf[9] = LD16(rk, l * 16 + 1024, sb + j * 2048);
            };
            auto commit = [&](int sg) {
                LAS v4u* xb = (LAS v4u*)(lds + L_X + (sg & 1) * X_SZ + l * 16);
#pragma unroll
                for (int i = 0; i < 10; ++i) xb[i * 64] = pf[i];
                lds_post(misc + 4, sg);
                WAITV(0);
            };
            BAR();
            wait_flags(rc, qkv_flag(1 + (l & 1), 0, h), 1, err);
            issue(0);
            commit(0);
            for (int s = 0; s < NSEG; ++s) {
                const unsigned cur = SLOTOFF(s);
                const unsigned fut = SLOTOFF(s + 2);
                BAR();
                {
                    v4f x;
                    for (;;) {
                        const v4u f = *(volatile LAS v4u*)(misc + 8);
                        const volatile LAS v4f* rp = (const volatile LAS v4f*)(lds + L_PV + l * 16);
                        const v4f t0 = rp[0], t1 = rp[64], t2 = rp[128], t3 = rp[192];
                        x = (t0 + t1) + (t2 + t3);
                        if (__builtin_amdgcn_readfirstlane(min(min(f[0], f[1]), min(f[2], f[3]))) > (unsigned)s) break;
                    }
                    const unsigned u0 = pk2(x[0], x[1]), u1 = pk2(x[2], x[3]);
                    const v2u a0 = sw16(u0, u0), a1 = sw16(u1, u1);
                    const v4u V = {a0[0], a1[0], a0[1], a1[1]};
                    ST16(rr, ovs, cur, V); ST16(rm, ovm, s * (NH * 4096), V);
                }
                if (l == 0) ST4(rc, F_PG + h * 128 + j * 4, 0, s);
                const unsigned fo = qkv_flag(1 + (l & 1), (s + 1) >> 2, h), fv = LD4(rc, fo, 0);
                anc = __builtin_amdgcn_readfirstlane(misc[0]);
                STAMP(0);
                if (s + 1 == NSEG) { WAITV(0); BAR(); if (l == 0) ST4(rc, F_PG + h * 128 + j * 4, 0, NSEG); break; }
                wait_until(anc + a.d1 + a.dp, a.ml + a.dp, a.thr);
                if (s + 2 < NSEG) { if (!all_ge(fv, 1)) wait_flags(rc, fo, 1, err); FENCE(); issue(s + 1); }
#pragma unroll
                for (int k = 0; k < 4; ++k) ST16(rr, p2 + k * 1024, fut, poi4);
                ST16(rr, p1, fut, poi4); ST16(rr, p1 + 1024, fut, poi4); ST16(rr, p3, fut, poi4);
                if (EXACT & 32) WAITV(0);
                BAR();
                wo_stage(cur);
                commit(s + 1);
                STAMP(1);
            }
        }
    }
    STAMP_OUT();
}

#define WSTAMP(k) do {} while (0)
#define RING_LD(K, B) asm volatile("buffer_load_dwordx4 %0, %1, %2, %3 offen offset:%4 sc1" : "+v"(ring[K]) : "v"(l16), "s"(rso), "s"((B) + ((K) >> 2) * 4096), "n"(((K) & 3) * 1024) : "memory")
#define RING_MM(N, K, C0, C1) asm volatile("s_waitcnt vmcnt(" #N ")\n\tv_mfma_f32_16x16x32_bf16 %0, %3, %2, %0\n\tv_mfma_f32_16x16x32_bf16 %1, %4, %2, %1" : "+v"(C0), "+v"(C1), "+v"(ring[K]) : "v"(wo[K][0]), "v"(wo[K][1]) : "memory")
#define RING_ALL(P) P(ring[0]), P(ring[1]), P(ring[2]), P(ring[3]), P(ring[4]), P(ring[5]), P(ring[6]), P(ring[7]), P(ring[8]), P(ring[9]), P(ring[10]), P(ring[11]), P(ring[12]), P(ring[13]), P(ring[14]), P(ring[15])
static constexpr int RED_LD = 36 * 4, RED_W = 32 * RED_LD, RED_B = 8 * RED_W;

DEV void worker(LAS char* lds, const Args& a, const int wk) {
    const int tid = threadIdx.x, w = __builtin_amdgcn_readfirstlane(tid >> 6), l = tid & 63, fr = l & 15, fq = l >> 4;
    const int g = wk >> 5, i = wk & 31;
    const rsrc_t rc = mkrsrc(a.ctl, CTL_SZ);
    unsigned* err = a.ctl;
    WSTAMP(0);
    const v4u rx = mkrs(a.x, S_TOK * DM * 2);
#pragma unroll 1
    for (int r = 0; r < 2; ++r) {
        const int m = 1 + ((i >> 3) & 1), rb = 2 * r + (i >> 4), h = 8 * g + (i & 7);
        const v4u rw = mkrs(m == 1 ? a.Wk : a.Wv, DM * HD * 2);
        const rsrc_t ro = mkrsrc(a.qkv + (size_t)m * QSZ, QSZ);
        if (m == 2) gemm_qkv128<true>(lds, rx, rw, ro, rb * 128, h * 128); else gemm_qkv128<false>(lds, rx, rw, ro, rb * 128, h * 128);
        if (tid == 0) ST4(rc, qkv_flag(m, rb, h), 0, 1u);
        WSTAMP(1 + r);
    }
#pragma unroll 1
    for (int t = i; t < 80; t += 32) {
        const int m = t < 48 ? 1 + ((t >> 2) & 1) : 0, rb = t < 48 ? 4 + 2 * (t >> 3) : 2 * ((t - 48) >> 2), h = 8 * g + 2 * (t & 3);
        const v4u rw = mkrs(m == 0 ? a.Wq : (m == 1 ? a.Wk : a.Wv), DM * HD * 2);
        const rsrc_t ro = mkrsrc(a.qkv + (size_t)m * QSZ, QSZ);
        if (m == 2) gemm_qkv256<true>(lds, rx, rw, ro, rb * 128, h * 128); else gemm_qkv256<false>(lds, rx, rw, ro, rb * 128, h * 128);
        if (tid == 0) ST4(rc, qkv_flag(m, rb, h), 0, 1u);
        WSTAMP(3 + (t >> 5));
    }
    const rsrc_t rq = mkrsrc(a.qkv, QSZ), rk = mkrsrc(a.qkv + QSZ, QSZ), rv = mkrsrc(a.qkv + 2 * (size_t)QSZ, QSZ), rm = mkrsrc(a.mb, NSEG * NH * 4096), ro = mkrsrc(a.ob, QSZ);
    const rsrc_t rg = mkrsrc(a.gr, S_TOK * 8), ri = mkrsrc(a.idx, 32), ru = mkrsrc(a.upd, TOPC * DM * 4);
    if (i >= 16) {
        const int lw = g * 16 + i - 16;
        const rsrc_t rh = mkrsrc(a.hid, TOPC * FF * 2);
        if (lw < 56) {
            const int sel = top8(rg, l);
            if (lw == 0 && tid < TOPC) ST4(ri, tid * 4, 0, sel);
#pragma unroll 1
            for (int e = 0; e < 2; ++e) moe1(lds, a.x, sel, mkrs(a.wg, DM * FF * 2), mkrs(a.wu, DM * FF * 2), rh, 2 * lw + e);
            WAITV(0); RBAR();
            if (tid == 0) ST4(rc, F_M1 + lw * 4, 0, 1u);
        }
        WSTAMP(5);
        if (lw >= 32) {
            wait_flags(rc, F_M1 + min(l, 55) * 4, 1, err);
            moe2(lds, rh, rg, ri, mkrs(a.wd, FF * DM * 2), ru, lw - 32);
            WAITV(0); RBAR();
            if (tid == 0) ST4(rc, F_M2 + (lw - 32) * 4, 0, 1u);
        }
        WSTAMP(6);
    }
    v4u wo[16][2];
    {
        const v4u rw = mkrs(a.Wo, HD * DM * 2);
        const unsigned vo = (l >> 2) * (DM * 2) + (l & 3) * 16, lb = (unsigned)(size_t)lds + w * 16384;
        const LAS char* p = lds + w * 16384 + (4 * fq + (fr >> 2)) * 64 + (l & 3) * 8;
#pragma unroll
        for (int hf = 0; hf < 2; ++hf) {
#pragma unroll
            for (int k = 0; k < 16; ++k) DMA16(rw, vo, ((512 * w + 256 * hf + 16 * k) * DM + 32 * i) * 2, lb + k * 1024);
            WAITV(0);
#pragma unroll
            for (int k = 0; k < 8; ++k)
#pragma unroll
                for (int n = 0; n < 2; ++n) wo[hf * 8 + k][n] = cat(trd(p + k * 2048 + n * 32), trd(p + k * 2048 + n * 32 + 1024));
            WAITL(0);
        }
    }
    WSTAMP(7);
    volatile LAS unsigned* prog = (volatile LAS unsigned*)(lds + 2 * RED_B);
    int xs = i >= 16 ? i - 16 : 32 + i, nx = 2;
    unsigned seen = i >= 16 ? NSEG : 0;
    int sel[8];
    bool pre = false;
    v4u ring[16];
    asm volatile("" : RING_ALL("=v"));
    const v4u rso = mkrs(a.ob, QSZ), rsc = mkrs(a.ctl, CTL_SZ), rsg = mkrs(a.gr, S_TOK * 8), rsu = mkrs(a.upd, TOPC * DM * 4);
    RBAR();
#pragma unroll 1
    for (int n = 0; n < 16; ++n) {
        const int seg = 4 * n + g;
#pragma unroll 1
        while (nx > 0 && (xs <= seg + 3 || (unsigned)xs < seen)) {
            asm volatile("s_waitcnt vmcnt(0)" :: RING_ALL("v") : "memory");
            xattn(rq, rk, rv, rm, ro, rc, err, xs, 8 * g + w, l); xs += 16; --nx; pre = false;
            asm volatile("" : RING_ALL("=v"));
        }
        if (n == 0) {
            wait_flags(rc, F_M2 + (l & 31) * 4, 1, err);
            const unsigned t = LD4(ri, (l & 7) * 4, 0);
#pragma unroll
            for (int q = 0; q < 8; ++q) sel[q] = __builtin_amdgcn_readlane(t, q);
            WSTAMP(8);
        }
        const unsigned ob = (unsigned)(seg * 256 + w * 16) * 1024u;
        int l = tid & 63; asm volatile("" : "+v"(l));
        const int fr = l & 15, fq = l >> 4, erow = w * 4 + fq, ecol = 32 * i + 2 * fr, l16 = l * 16;
        const unsigned fo = __builtin_amdgcn_readfirstlane(n < 15 ? F_XA + (seg + 4) * 64 + (w >> 1) * 4 : OOB);
        if (!pre) {
            wait_flags(rc, F_XA + seg * 64 + (w >> 1) * 4, 1, err);
#pragma unroll
            for (int k = 0; k < 16; ++k) RING_LD(k, ob);
        }
        const int row = seg * 32 + erow;
        int slot = -1;
#pragma unroll
        for (int q = 0; q < 8; ++q) slot = sel[q] == row ? q : slot;
        unsigned fn, pg, xv; float gt; v2u up;
        v4f c[2][2] = {};
        asm volatile("buffer_load_dword %[fn], off, %[rc], %[fo] sc1\n\tbuffer_load_dword %[pg], off, %[rc], %[po] sc1\n\tbuffer_load_dword %[xv], %[xo], %[rx], 0 offen\n\t"
                     "buffer_load_dword %[gt], %[go], %[rg], 0 offen\n\tbuffer_load_dwordx2 %[up], %[uo], %[ru], 0 offen sc1"
                     : [fn] "=&v"(fn), [pg] "=&v"(pg), [xv] "=&v"(xv), [gt] "=&v"(gt), [up] "=&v"(up), "+v"(c[0][0]), "+v"(c[0][1]), "+v"(c[1][0]), "+v"(c[1][1])
                     : [rc] "s"(rsc), [fo] "s"(fo), [po] "s"(F_PG + g * 1024), [rx] "s"(rx), [rg] "s"(rsg), [ru] "s"(rsu),
                       [xo] "v"((row * DM + ecol) * 2), [go] "v"(row * 8), [uo] "v"(slot < 0 ? OOB : (slot * DM + ecol) * 4) : "memory");
#pragma unroll
        for (int k = 0; k < 16; ++k) { RING_MM(20, k, c[0][0], c[0][1]); RING_LD(k, ob + 131072); }
        RING_MM(15, 0, c[1][0], c[1][1]);
        asm volatile("" : "+v"(fn), "+v"(pg), "+v"(xv), "+v"(gt), "+v"(up));
        pre = __builtin_amdgcn_readfirstlane(fn) != 0;
        const unsigned nb = pre ? ob + 4 * 262144 : OOB;
#pragma unroll
        for (int k = 0; k < 16; ++k) { if (k) RING_MM(15, k, c[1][0], c[1][1]); RING_LD(k, nb); }
        asm volatile("s_nop 7\n\ts_nop 1" : "+v"(c[0][0]), "+v"(c[0][1]), "+v"(c[1][0]), "+v"(c[1][1]));
        LAS char* rb = lds + (n & 1) * RED_B;
#pragma unroll
        for (int u = 0; u < 2; ++u)
#pragma unroll
            for (int t = 0; t < 2; ++t) *(LAS v4f*)(rb + w * RED_W + (u * 16 + fr) * RED_LD + (t * 16 + 4 * fq) * 4) = c[u][t];
        if (tid == 0) prog[n & 1] = pg;
        BAR();
        v2f acc = {0, 0};
#pragma unroll
        for (int q = 0; q < 8; ++q) acc += *(const LAS v2f*)(rb + q * RED_W + erow * RED_LD + fr * 8);
        seen = __builtin_amdgcn_readfirstlane(prog[n & 1]);
        float b0 = lo16(xv), b1 = hi16(xv);
        if (slot >= 0) { b0 = rbf(b0 + __uint_as_float(up[0])); b1 = rbf(b1 + __uint_as_float(up[1])); }
        const float omg = rbf(1.0f - gt);
        *(unsigned*)(a.out + (size_t)row * DM + ecol) = pk2(rbf(gt * rbf(acc[0])) + rbf(omg * b0), rbf(gt * rbf(acc[1])) + rbf(omg * b1));
        WSTAMP(9 + n);
    }
    asm volatile("s_waitcnt vmcnt(0)" :: RING_ALL("v") : "memory");
}

__global__ void __launch_bounds__(512, 2) k_main(Args a) {
    __shared__ __attribute__((aligned(16))) char lds[L_TOT];
    const int b = blockIdx.x, c = (b & 3) * 32 + (b >> 3);
    if ((b & 7) < 4) chain((LAS char*)lds, a, c); else worker((LAS char*)lds, a, c);
}
__global__ void __launch_bounds__(512, 2) k_init(Args a) {
    const int b = blockIdx.x, tid = threadIdx.x, w = __builtin_amdgcn_readfirstlane(tid >> 6), l = tid & 63;
    const rsrc_t rr = mkrsrc(a.ring, NSLOT * SLOT);
    if (b < 64 && (b & 7) == 0) {
        if (w == 0) {
            unsigned off = (b >> 4) * (unsigned)SLOT + ((b >> 3) & 1) * 8192 + nearo(PR_OFF) + (l & 31) * 4, t = 0xffffffffu;
            for (int it = 0; it < 4; ++it) { const unsigned t0 = cyc32(); ST4(rr, off, 0, off); WAITV(0); t = min(t, cyc32() - t0); off += 256; }
            if (l == 0) a.ctl[F_PM / 4 + (b >> 3)] = t;
        }
        RBAR();
    }
    const v4u poi4 = {POI, POI, POI, POI};
    ST16(rr, b * 16384 + tid * 16, 0, poi4); ST16(rr, b * 16384 + 8192 + tid * 16, 0, poi4);
    if (tid < 8) ST16(mkrsrc(a.ctl, CTL_SZ), b * 128 + tid * 16, 0, (v4u{0, 0, 0, 0}));
    prep_row(a.x, a.gw, a.w_r, a.gr, b * 8 + w, l);
}

#define TUNE_DEFAULT {124, 282, 342, 10, 22, 40, 8, 30}

extern "C" void kernel_launch(void* const* d_in, const int* in_sizes, int n_in, void* d_out, int out_size, void* d_ws, size_t ws_size, hipStream_t stream) {
    char* ring = (char*)(((size_t)d_ws + SLOT - 1) & ~(size_t)(SLOT - 1));
    char* ws = ring + NSLOT * SLOT; size_t off = 0;
    auto alloc = [&](size_t bytes) { void* p = ws + off; off += (bytes + 255) & ~(size_t)255; return p; };
    Args a;
    a.x = (const bf16_t*)d_in[0]; a.mem0 = (const bf16_t*)d_in[1]; a.Wq = (const bf16_t*)d_in[2]; a.Wk = (const bf16_t*)d_in[3]; a.Wv = (const bf16_t*)d_in[4]; a.Wo = (const bf16_t*)d_in[5];
    a.w_r = (const bf16_t*)d_in[8]; a.wg = (const bf16_t*)d_in[9]; a.wu = (const bf16_t*)d_in[10]; a.wd = (const bf16_t*)d_in[11]; a.gw = (const bf16_t*)d_in[12];
    a.qkv = (char*)alloc(3 * (size_t)QSZ); a.mb = (char*)alloc(NSEG * NH * 4096); a.ob = (char*)alloc(QSZ);
    a.gr = (float*)alloc(S_TOK * 8); a.idx = (int*)alloc(256); a.hid = (bf16_t*)alloc(TOPC * FF * 2); a.upd = (float*)alloc(TOPC * DM * 4); a.out = (bf16_t*)d_out;
    a.ring = ring; a.ctl = (unsigned*)alloc(CTL_SZ + 256); a.dbg = (unsigned*)alloc((32768 + 4096) * 4);
    const int tn[8] = TUNE_DEFAULT;
    a.d1 = tn[0]; a.d2 = tn[1]; a.d3 = tn[2]; a.dv = tn[3]; a.ml = tn[4]; a.slack = tn[5]; a.thr = tn[6]; a.dp = tn[7];
    k_init<<<256, 512, 0, stream>>>(a);
    k_main<<<256, 512, 0, stream>>>(a);
}
```

```cpp
#include <hip/hip_runtime.h>
#include <stdint.h>

typedef unsigned short bf16_t;
typedef unsigned v2u __attribute__((ext_vector_type(2)));
typedef unsigned v4u __attribute__((ext_vector_type(4)));
typedef unsigned v8u __attribute__((ext_vector_type(8)));
typedef float v2f __attribute__((ext_vector_type(2)));
typedef float v4f __attribute__((ext_vector_type(4)));
typedef short v4s __attribute__((ext_vector_type(4)));
typedef __bf16 v2b __attribute__((ext_vector_type(2)));
typedef __bf16 v8b __attribute__((ext_vector_type(8)));
typedef __bf16 v16b __attribute__((ext_vector_type(16)));
typedef unsigned long long u64;
typedef __amdgpu_buffer_rsrc_t rsrc_t;
#define LAS __attribute__((address_space(3)))
#define DEV __device__ __forceinline__

static constexpr int S_TOK = 2048, DM = 1024, NH = 32, DH = 128, SEG = 32, NSEG = 64;
static constexpr int HD = NH * DH;
static constexpr int FF = 3584, TOPC = 8;
static constexpr float SCALE = 0.08838834764831845f, LOG2E = 1.4426950408889634f, MASKV = -998244352.0f;

DEV unsigned pk2(float lo, float hi) { v2f f = {lo, hi}; return __builtin_bit_cast(unsigned, __builtin_convertvector(f, v2b)); }
DEV float lo16(unsigned u) { return __uint_as_float(u << 16); }
DEV float hi16(unsigned u) { return __uint_as_float(u & 0xffff0000u); }
DEV float rbf(float f) { return __uint_as_float(pk2(0.0f, f)); }
DEV v4f smf(v4u a, v8u b, v4f c, int idx) { return __builtin_amdgcn_smfmac_f32_16x16x64_bf16(__builtin_bit_cast(v8b, a), __builtin_bit_cast(v16b, b), c, idx, 0, 0); }
DEV v4f mf(v4u a, v4u b, v4f c) { return __builtin_amdgcn_mfma_f32_16x16x32_bf16(__builtin_bit_cast(v8b, a), __builtin_bit_cast(v8b, b), c, 0, 0, 0); }
DEV v2u trd(const LAS char* p) { return __builtin_bit_cast(v2u, __builtin_amdgcn_ds_read_tr16_b64_v4i16((LAS v4s*)p)); }
DEV v4u cat(v2u a, v2u b) { return __builtin_shufflevector(a, b, 0, 1, 2, 3); }
DEV v2u sw16(unsigned x, unsigned y) { return __builtin_amdgcn_permlane16_swap(x, y, false, false); }
DEV v2u sw32(unsigned x, unsigned y) { return __builtin_amdgcn_permlane32_swap(x, y, false, false); }
DEV float half_add(float x, float y) { v2u r = sw32(__float_as_uint(x), __float_as_uint(y)); return __uint_as_float(r[0]) + __uint_as_float(r[1]); }
DEV float ror8(float v) { return __int_as_float(__builtin_amdgcn_update_dpp(0, __float_as_int(v), 0x128, 0xf, 0xf, false)); }
template <bool MAX> DEV float red_g(float v) {
    auto op = [](float a, float b) { return MAX ? fmaxf(a, b) : a + b; };
    v2u r = sw16(__float_as_uint(v), __float_as_uint(v)); v = op(__uint_as_float(r[0]), __uint_as_float(r[1]));
    r = sw32(__float_as_uint(v), __float_as_uint(v)); return op(__uint_as_float(r[0]), __uint_as_float(r[1]));
}
DEV float sigmoidf_(float z) { return 1.0f / (1.0f + __expf(-z)); }
DEV v4u mkrs(const void* p, unsigned bytes) { const u64 a = (u64)p; return v4u{(unsigned)a, (unsigned)(a >> 32) & 0xffffu, bytes, 0x00020000u}; }
DEV rsrc_t mkrsrc(const void* p, unsigned bytes) { return __builtin_amdgcn_make_buffer_rsrc((void*)p, (short)0, (int)bytes, 0x00020000); }

#define ST16(RS, OFF, SOFF, V) __builtin_amdgcn_raw_buffer_store_b128(V, RS, (OFF) + (SOFF), 0, 16)
#define ST4(RS, OFF, SOFF, V) __builtin_amdgcn_raw_buffer_store_b32(V, RS, OFF, SOFF, 16)
#define LD16(RS, OFF, SOFF) __builtin_amdgcn_raw_buffer_load_b128(RS, OFF, SOFF, 16)
#define LD8(RS, OFF, SOFF) __builtin_amdgcn_raw_buffer_load_b64(RS, OFF, SOFF, 16)
#define LD4(RS, OFF, SOFF) __builtin_amdgcn_raw_buffer_load_b32(RS, OFF, SOFF, 16)
#define DMA16(RS, VOFF, SOFF, LOFF) asm volatile("s_mov_b32 m0, %0\n\ts_nop 0\n\tbuffer_load_dwordx4 %1, %2, %3 offen lds" :: "s"(LOFF), "v"(VOFF), "s"(RS), "s"(SOFF) : "memory", "m0")
#define FENCE() asm volatile("" ::: "memory")
#define WAITV(n) asm volatile("s_waitcnt vmcnt(" #n ")" ::: "memory")
#define WAITL(n) asm volatile("s_waitcnt lgkmcnt(" #n ")" ::: "memory")
#define RBAR() asm volatile("s_barrier" ::: "memory")
#define BAR() asm volatile("s_waitcnt lgkmcnt(0)\n\ts_barrier" ::: "memory")
#define SCHED() __builtin_amdgcn_sched_barrier(0)

static constexpr int HTB = 16384;

#define G_LDA(dst, off) _Pragma("unroll") for (int m_ = 0; m_ < 4; ++m_) _Pragma("unroll") for (int k_ = 0; k_ < 2; ++k_) dst[m_][k_] = *(const LAS v4u*)(lds + (off) + ra[k_] + m_ * 2048)
#define G_LDB(dst, off) _Pragma("unroll") for (int n_ = 0; n_ < 2; ++n_) _Pragma("unroll") for (int k_ = 0; k_ < 2; ++k_) \
        dst[n_][k_] = cat(trd(lds + (off) + rb[n_] + k_ * 8192), trd(lds + (off) + rb[n_] + k_ * 8192 + TSTEP))
#define G_MMA(C, At, Bt) do { __builtin_amdgcn_s_setprio(1); _Pragma("unroll") for (int m_ = 0; m_ < 4; ++m_) _Pragma("unroll") for (int n_ = 0; n_ < 2; ++n_) _Pragma("unroll") for (int k_ = 0; k_ < 2; ++k_) \
        C[m_][n_] = VT ? mf(At[m_][k_], Bt[n_][k_], C[m_][n_]) : mf(Bt[n_][k_], At[m_][k_], C[m_][n_]); __builtin_amdgcn_s_setprio(0); } while (0)

template <bool VT> DEV void qkv_store(const v4f (&c)[4][2], rsrc_t ro, int row0, int h, int wc, int l) {
    const unsigned base = (unsigned)((row0 >> 5) * NH + h) * 8192u + wc * 2048u;
#pragma unroll
    for (int a = 0; a < 2; ++a)
#pragma unroll
        for (int b = 0; b < 2; ++b) {
            v4u u;
            if (VT) u = v4u{pk2(c[2 * a][b][0], c[2 * a][b][1]), pk2(c[2 * a + 1][b][0], c[2 * a + 1][b][1]), pk2(c[2 * a][b][2], c[2 * a][b][3]), pk2(c[2 * a + 1][b][2], c[2 * a + 1][b][3])};
            else u = v4u{pk2(c[2 * a + b][0][0], c[2 * a + b][0][1]), pk2(c[2 * a + b][0][2], c[2 * a + b][0][3]), pk2(c[2 * a + b][1][0], c[2 * a + b][1][1]), pk2(c[2 * a + b][1][2], c[2 * a + b][1][3])};
            ST16(ro, l * 16 + b * 1024, base + a * (NH * 8192u), u);
        }
}

template <bool VT> DEV void gemm_qkv256(LAS char* lds, v4u rx, v4u rw, rsrc_t ro, int row0, int col0) {
    const int tid = threadIdx.x, w = __builtin_amdgcn_readfirstlane(tid >> 6), l = tid & 63, wr = w >> 2, wc = w & 3, fr = l & 15, fq = l >> 4;
    constexpr int TSTEP = 1024;
    const unsigned va = (tid >> 3) * 2048 + (((tid & 7) ^ ((tid >> 4) & 7)) << 4);
    const unsigned vb = (tid >> 4) * 8192 + (((tid & 15) ^ ((((tid >> 4) & 3) << 2) | (((tid >> 7) & 1) << 1))) << 4);
    int ra[2], rb[2];
#pragma unroll
    for (int i = 0; i < 2; ++i) {
        ra[i] = (wr * 64 + fr) * 128 + (((4 * i + fq) ^ (fr >> 1)) << 4);
        rb[i] = (8 * fq + (fr >> 2)) * 256 + (((4 * wc + 2 * i + ((fr >> 1) & 1)) ^ (((fr >> 2) << 2) | ((fq & 1) << 1))) << 4) + (fr & 1) * 8;
    }
    const unsigned lb = (unsigned)(size_t)lds + w * 1024;
    const unsigned a0 = row0 * 2048, b0 = col0 * 2;
#define SA(b, hh) (((b) * 2 + (hh)) * HTB)
#define SB(b, hh) ((4 + (b) * 2 + (hh)) * HTB)
#define STA(b, hh, t) do { const unsigned s_ = a0 + (hh) * 262144 + (t) * 128; DMA16(rx, va, s_, lb + SA(b, hh)); DMA16(rx, va, s_ + 131072, lb + SA(b, hh) + 8192); } while (0)
#define STB(b, hh, t) do { const unsigned s_ = b0 + (hh) * 256 + (t) * 524288; DMA16(rw, vb, s_, lb + SB(b, hh)); DMA16(rw, vb, s_ + 262144, lb + SB(b, hh) + 8192); } while (0)
    v4f acc[2][2][4][2] = {};
    v4u At[4][2], B0[2][2], B1[2][2];
    STB(0, 0, 0); STA(0, 0, 0); STB(0, 1, 0); STA(0, 1, 0);
    if (wr == 1) RBAR();
    WAITV(4); RBAR();
    STB(1, 0, 1); STA(1, 0, 1); STB(1, 1, 1);
    WAITV(6); RBAR();
    for (int t = 0; t < 16; t += 2) {
        const int t2 = (t + 2) & 15, t3 = (t + 3) & 15;
        G_LDB(B0, SB(0, 0)); SCHED(); G_LDA(At, SA(0, 0)); STA(1, 1, t + 1);
        WAITL(8); RBAR(); WAITL(0); G_MMA(acc[0][0], At, B0); RBAR(); SCHED();
        G_LDB(B1, SB(0, 1)); STB(0, 0, t2);
        RBAR(); WAITL(0); G_MMA(acc[0][1], At, B1); RBAR();
        G_LDA(At, SA(0, 1)); STA(0, 0, t2);
        RBAR(); WAITL(0); G_MMA(acc[1][0], At, B0); RBAR(); SCHED();
        STB(0, 1, t2);
        WAITV(6); RBAR(); G_MMA(acc[1][1], At, B1); RBAR();
        G_LDB(B0, SB(1, 0)); SCHED(); G_LDA(At, SA(1, 0)); STA(0, 1, t2);
        WAITL(8); RBAR(); WAITL(0); G_MMA(acc[0][0], At, B0); RBAR(); SCHED();
        G_LDB(B1, SB(1, 1)); STB(1, 0, t3);
        RBAR(); WAITL(0); G_MMA(acc[0][1], At, B1); RBAR();
        G_LDA(At, SA(1, 1)); STA(1, 0, t3);
        RBAR(); WAITL(0); G_MMA(acc[1][0], At, B0); RBAR(); SCHED();
        STB(1, 1, t3);
        WAITV(6); RBAR(); G_MMA(acc[1][1], At, B1); RBAR();
    }
#pragma unroll
    for (int ai = 0; ai < 2; ++ai)
#pragma unroll
        for (int bj = 0; bj < 2; ++bj) qkv_store<VT>(acc[ai][bj], ro, row0 + ai * 128 + wr * 64, (col0 >> 7) + bj, wc, l);
    WAITV(0);
    if (wr == 0) RBAR();
    RBAR();
#undef STA
#undef STB
}

template <bool VT> DEV void gemm_qkv128(LAS char* lds, v4u rsa, v4u rsb, rsrc_t ro, int row0, int col0) {
    const int tid = threadIdx.x, w = __builtin_amdgcn_readfirstlane(tid >> 6), l = tid & 63, wr = w >> 2, wc = w & 3, fr = l & 15, fq = l >> 4;
    constexpr int TSTEP = 1024, NT = 16, LDW = 8192;
    const unsigned va = (tid >> 3) * 2048 + (((tid & 7) ^ ((tid >> 4) & 7)) << 4);
    const unsigned vb = (tid >> 4) * LDW + (((tid & 15) ^ ((((tid >> 4) & 3) << 2) | (((tid >> 7) & 1) << 1))) << 4);
    int ra[2], rb[2];
#pragma unroll
    for (int i = 0; i < 2; ++i) {
        ra[i] = (wr * 64 + fr) * 128 + (((4 * i + fq) ^ (fr >> 1)) << 4);
        rb[i] = (8 * fq + (fr >> 2)) * 256 + (((4 * wc + 2 * i + ((fr >> 1) & 1)) ^ (((fr >> 2) << 2) | ((fq & 1) << 1))) << 4) + (fr & 1) * 8;
    }
    const unsigned la = (unsigned)(size_t)lds + w * 1024, lb = la + HTB;
    const unsigned a0 = row0 * 2048, b0 = col0 * 2;
#define STG(sl, t) do { const unsigned sa_ = a0 + (t) * 128, sb_ = b0 + (t) * (64 * LDW); \
        DMA16(rsb, vb, sb_, lb + (sl) * 2 * HTB); DMA16(rsb, vb, sb_ + 32 * LDW, lb + (sl) * 2 * HTB + 8192); \
        DMA16(rsa, va, sa_, la + (sl) * 2 * HTB); DMA16(rsa, va, sa_ + 131072, la + (sl) * 2 * HTB + 8192); } while (0)
    v4f acc[4][2] = {};
    v4u At[4][2], Bt[2][2];
    STG(0, 0); STG(1, 1); STG(2, 2);
    if (wr == 1) RBAR();
    WAITV(8); RBAR(); RBAR();
    for (int t = 0; t < NT; t += 4) {
#pragma unroll
        for (int u = 0; u < 4; ++u) {
            G_LDB(Bt, u * 2 * HTB + HTB); G_LDA(At, u * 2 * HTB);
            STG((u + 3) & 3, (t + u + 3) & (NT - 1));
            WAITV(8); WAITL(0); RBAR(); G_MMA(acc, At, Bt); RBAR(); SCHED();
        }
    }
    qkv_store<VT>(acc, ro, row0 + wr * 64, col0 >> 7, wc, l);
    WAITV(0);
    if (wr == 0) RBAR();
    RBAR();
#undef STG
}

static constexpr unsigned F_QKV = 1024, F_PG = 16384, F_XA = 20480, F_M1 = 24576, F_M2 = F_M1 + 256, F_LW = 25600, CTL_SZ = 32768, F_PM = CTL_SZ;
static constexpr unsigned POI = 0xFFFFFFFFu, OOB = 0x40000000u, SPIN_CAP = 200000u;
DEV bool spin_fail(unsigned tries, unsigned* err) {
    __builtin_amdgcn_s_sleep(1);
    if ((tries & 255u) != 255u) return false;
    unsigned e; asm volatile("global_load_dword %0, %1, off sc1\n\ts_waitcnt vmcnt(0)" : "=&v"(e) : "v"(err) : "memory");
    if (__builtin_amdgcn_readfirstlane(e) == 1u) return true;
    if (tries > SPIN_CAP) { unsigned one = 1u; asm volatile("global_store_dword %0, %1, off sc1" :: "v"(err), "v"(one) : "memory"); return true; }
    return false;
}
DEV bool all_ge(unsigned v, unsigned t) { return __builtin_amdgcn_ballot_w64(v < t) == 0; }
DEV void wait_flags(rsrc_t rc, unsigned off, unsigned t, unsigned* err) {
    for (unsigned tries = 0;; ++tries) {
        FENCE();
        if (all_ge(LD4(rc, off, 0), t)) break;
        __builtin_amdgcn_s_sleep(6);
        if (spin_fail(tries, err)) break;
    }
    FENCE();
}
DEV unsigned qkv_flag(int m, int rb, int h) { const bool big = m == 0 || rb >= 4; return F_QKV + (((big ? rb & ~1 : rb) * NH + (big ? h & ~1 : h)) * 4 + m) * 4; }

#define LDB(RS, K, BASE) LD16(RS, l * 16 + ((K) & 3) * 1024, (BASE) + ((K) >> 2) * 4096)
DEV void xattn(rsrc_t rq, rsrc_t rk, rsrc_t rv, rsrc_t rm, rsrc_t ro, rsrc_t rc, unsigned* err, int seg, int h, int l) {
    asm volatile("" : "+v"(l));
    const int fr = l & 15, g = l >> 4;
    const unsigned sb = (unsigned)(seg * NH + h) * 8192u, mb = (unsigned)(seg * NH + h) * 4096u;
    wait_flags(rc, qkv_flag(min(l, 2), seg >> 2, h), 1, err);
    v4u q[4][2], km[4], v[8]; v2u vm[8];
    v4f sx[3] = {};
    {
        v4u k[4][2];
#pragma unroll
        for (int j = 0; j < 4; ++j) {
            q[j][0] = LDB(rq, 2 * j, sb); q[j][1] = LDB(rq, 2 * j + 1, sb);
            k[j][0] = LDB(rk, 2 * j, sb); k[j][1] = LDB(rk, 2 * j + 1, sb);
        }
#pragma unroll
        for (int d = 0; d < 8; ++d) v[d] = LDB(rv, d, sb);
#pragma unroll
        for (int j = 0; j < 4; ++j) { sx[0] = mf(k[j][0], q[j][0], sx[0]); sx[1] = mf(k[j][0], q[j][1], sx[1]); sx[2] = mf(k[j][1], q[j][1], sx[2]); }
    }
    SCHED();
    wait_flags(rc, F_PG + h * 128 + (l & 3) * 4, seg + 1, err);
#pragma unroll
    for (int j = 0; j < 4; ++j) km[j] = LD16(rm, (g * 8 + (l & 7)) * 16 + j * 512, mb);
#pragma unroll
    for (int d = 0; d < 8; ++d) vm[d] = LD8(rm, 2048 + fr * 16 + (g & 1) * 8 + d * 256, mb);
    v4f sm[2] = {};
#pragma unroll
    for (int j = 0; j < 4; ++j) { sm[0] = mf(km[j], q[j][0], sm[0]); sm[1] = mf(km[j], q[j][1], sm[1]); }
    SCHED();
#pragma unroll
    for (int u = 0; u < 2; ++u) {
        float e[12];
#pragma unroll
        for (int r = 0; r < 4; ++r) {
            const bool vis = 4 * g + r <= fr;
            e[r] = (u || vis) ? rbf(rbf(sx[u][r]) * SCALE) : MASKV;
            e[4 + r] = (u && vis) ? rbf(rbf(sx[2][r]) * SCALE) : MASKV;
            e[8 + r] = g < 2 ? rbf(rbf(sm[u][r]) * SCALE) : -INFINITY;
        }
        float m = e[0];
#pragma unroll
        for (int r = 1; r < 12; ++r) m = fmaxf(m, e[r]);
        m = red_g<true>(m);
        float sum = 0;
#pragma unroll
        for (int r = 0; r < 12; ++r) { e[r] = __builtin_amdgcn_exp2f((e[r] - m) * LOG2E); sum += e[r]; }
        const float inv = __builtin_amdgcn_rcpf(red_g<false>(sum));
        const v4u P = {pk2(e[0] * inv, e[1] * inv), pk2(e[4] * inv, e[5] * inv), pk2(e[2] * inv, e[3] * inv), pk2(e[6] * inv, e[7] * inv)};
        const unsigned pm0 = pk2(e[8] * inv, e[9] * inv), pm1 = pk2(e[10] * inv, e[11] * inv);
        const v4u Pa = {pm0, pm1, 0u, 0u}, Pb = {0u, 0u, pm0, pm1};
#pragma unroll
        for (int d = 0; d < 4; ++d) {
            const v4u vv = cat(vm[2 * d], vm[2 * d + 1]);
            const v4f o0 = mf(vv, Pa, mf(v[2 * d], P, v4f{0, 0, 0, 0})), o1 = mf(vv, Pb, mf(v[2 * d + 1], P, v4f{0, 0, 0, 0}));
            const v4u o = {pk2(o0[0], o0[1]), pk2(o0[2], o0[3]), pk2(o1[0], o1[1]), pk2(o1[2], o1[3])};
            ST16(ro, l * 16 + d * 1024, (unsigned)((seg * 2 + u) * 128 + h * 4) * 1024u, o);
        }
    }
    WAITV(0); RBAR();
    if (threadIdx.x == 0) ST4(rc, F_XA + seg * 64 + (h >> 3) * 4, 0, 1u);
}

DEV void prep_row(const bf16_t* x, const bf16_t* gw, const bf16_t* wr, float* gr, int row, int l) {
    float a = 0, b = 0;
#pragma unroll
    for (int i = 0; i < 2; ++i) {
        const v4u xv = *(const v4u*)(x + (size_t)row * DM + i * 512 + l * 8), gv = *(const v4u*)(gw + i * 512 + l * 8), rv = *(const v4u*)(wr + i * 512 + l * 8);
#pragma unroll
        for (int e = 0; e < 4; ++e) {
            a += rbf(lo16(xv[e]) * lo16(gv[e])) + rbf(hi16(xv[e]) * hi16(gv[e]));
            b += lo16(xv[e]) * lo16(rv[e]) + hi16(xv[e]) * hi16(rv[e]);
        }
    }
#pragma unroll
    for (int o = 1; o < 16; o <<= 1) { a += __shfl_xor(a, o); b += __shfl_xor(b, o); }
    a = red_g<false>(a); b = red_g<false>(b);
    if (l == 0) *(v2f*)(gr + 2 * row) = v2f{rbf(sigmoidf_(rbf(a))), rbf(b)};
}
DEV int top8(rsrc_t rg, int l) {
    unsigned key[32];
#pragma unroll
    for (int i = 0; i < 16; ++i) {
        const v4u t = LD16(rg, l * 16 + i * 1024, 0);
        const int row = i * 128 + l * 2;
        key[2 * i] = ((int)t[1] < 0 ? 0xffff0000u - t[1] : t[1] + 0x80000000u) + (0xffffu - row);
        key[2 * i + 1] = ((int)t[3] < 0 ? 0xffff0000u - t[3] : t[3] + 0x80000000u) + (0xffffu - row - 1);
    }
    int res = 0;
#pragma unroll 1
    for (int c = 0; c < TOPC; ++c) {
        unsigned m = 0;
#pragma unroll
        for (int i = 0; i < 32; ++i) m = max(m, key[i]);
#pragma unroll
        for (int o = 1; o < 64; o <<= 1) m = max(m, (unsigned)__shfl_xor((int)m, o));
#pragma unroll
        for (int i = 0; i < 32; ++i) key[i] = key[i] == m ? 0u : key[i];
        if (l == c) res = 0xffff - (int)(m & 0xffffu);
    }
    return res;
}
template <int NKS> DEV void skinny32(LAS char* wl, v4u rw, unsigned ldw, unsigned off, const v4u (&a)[NKS], v4f (&c)[2], int l) {
    const unsigned vo = (l >> 2) * ldw + (l & 3) * 16, lb = (unsigned)(size_t)wl;
#pragma unroll
    for (int i = 0; i < 2 * NKS; ++i) DMA16(rw, vo, off + i * 16 * ldw, lb + i * 1024);
    WAITV(0);
    const LAS char* p = wl + (8 * (l >> 4) + ((l & 15) >> 2)) * 64 + (l & 3) * 8;
    c[0] = v4f{0, 0, 0, 0}; c[1] = c[0];
#pragma unroll
    for (int k = 0; k < NKS; ++k)
#pragma unroll
        for (int n = 0; n < 2; ++n) c[n] = mf(a[k], cat(trd(p + k * 2048 + n * 32), trd(p + k * 2048 + n * 32 + 256)), c[n]);
}
DEV void moe1(LAS char* lds, const bf16_t* x, int sel, v4u rwg, v4u rwu, rsrc_t rh, int cb) {
    const int tid = threadIdx.x, w = __builtin_amdgcn_readfirstlane(tid >> 6), l = tid & 63;
    const int row = __shfl(sel, l & 7);
    v4u a[4];
#pragma unroll
    for (int k = 0; k < 4; ++k) a[k] = *(const v4u*)(x + (size_t)row * DM + w * 128 + k * 32 + (l >> 4) * 8);
    v4f cg[2], cu[2];
    skinny32<4>(lds + w * 16384, rwg, FF * 2, (w * 128 * FF + cb * 32) * 2, a, cg, l);
    skinny32<4>(lds + w * 16384 + 8192, rwu, FF * 2, (w * 128 * FF + cb * 32) * 2, a, cu, l);
    BAR();
    LAS v4f* red = (LAS v4f*)lds;
    if (l < 32) { red[(w * 4 + 0) * 32 + l] = cg[0]; red[(w * 4 + 1) * 32 + l] = cg[1]; red[(w * 4 + 2) * 32 + l] = cu[0]; red[(w * 4 + 3) * 32 + l] = cu[1]; }
    BAR();
    if (tid < 128) {
        const int tok = tid >> 4, c0 = 2 * (tid & 15);
        float hv[2];
#pragma unroll
        for (int e = 0; e < 2; ++e) {
            const int cc = c0 + e; const LAS float* p = (const LAS float*)lds + ((cc >> 4) * 32 + (tok >> 2) * 16 + (cc & 15)) * 4 + (tok & 3);
            float g = 0, u = 0;
#pragma unroll
            for (int q = 0; q < 8; ++q) { g += p[q * 512]; u += p[q * 512 + 256]; }
            g = rbf(g); hv[e] = rbf(g * sigmoidf_(g)) * rbf(u);
        }
        ST4(rh, (tok * FF + cb * 32 + c0) * 2, 0, pk2(hv[0], hv[1]));
    }
    BAR();
}
DEV void moe2(LAS char* lds, rsrc_t rh, rsrc_t rg, rsrc_t ri, v4u rwd, rsrc_t ru, int cb) {
    const int tid = threadIdx.x, w = __builtin_amdgcn_readfirstlane(tid >> 6), l = tid & 63;
    v4f c[2], c2[2];
    {
        v4u a[7];
#pragma unroll
        for (int k = 0; k < 7; ++k) a[k] = LD16(rh, ((l & 7) * FF + w * 448 + k * 32 + (l >> 4) * 8) * 2, 0);
        skinny32<7>(lds + w * 16384, rwd, DM * 2, (w * 448 * DM + cb * 32) * 2, a, c, l);
#pragma unroll
        for (int k = 0; k < 7; ++k) a[k] = LD16(rh, ((l & 7) * FF + w * 448 + 224 + k * 32 + (l >> 4) * 8) * 2, 0);
        WAITL(0);
        skinny32<7>(lds + w * 16384, rwd, DM * 2, ((w * 448 + 224) * DM + cb * 32) * 2, a, c2, l);
    }
    BAR();
    LAS v4f* red = (LAS v4f*)lds;
    if (l < 32) { red[(w * 2 + 0) * 32 + l] = c[0] + c2[0]; red[(w * 2 + 1) * 32 + l] = c[1] + c2[1]; }
    BAR();
    if (tid < 256) {
        const int tok = tid >> 5, cc = tid & 31; const LAS float* p = (const LAS float*)lds + ((cc >> 4) * 32 + (tok >> 2) * 16 + (cc & 15)) * 4 + (tok & 3);
        float y = 0;
#pragma unroll
        for (int q = 0; q < 8; ++q) y += p[q * 256];
        const int row = (int)LD4(ri, tok * 4, 0);
        const float r = __uint_as_float(LD4(rg, row * 8 + 4, 0));
        ST4(ru, (tok * DM + cb * 32 + cc) * 4, 0, __float_as_uint(rbf(rbf(y) * rbf(sigmoidf_(r)))));
    }
    BAR();
}

static constexpr unsigned H2_OFF = 0, H1_OFF = 512 * 1024, H3_OFF = 768 * 1024, PR_OFF = 896 * 1024;
static constexpr size_t SLOT = 2u << 20;
static constexpr int NSLOT = 4;
struct Args {
    const bf16_t* x; const bf16_t* mem0; const bf16_t* Wq; const bf16_t* Wk; const bf16_t* Wv; const bf16_t* Wo;
    const bf16_t* w_r; const bf16_t* wg; const bf16_t* wu; const bf16_t* wd; const bf16_t* gw;
    char* qkv; char* mb; char* ob; float* gr; int* idx; bf16_t* hid; float* upd; bf16_t* out;
    char* ring; unsigned* ctl; unsigned* dbg;
    int per, d1, dv, d2, dp, slack, thr, spare;
};
static constexpr unsigned QSZ = S_TOK * HD * 2;
typedef unsigned v16u __attribute__((ext_vector_type(16)));
typedef unsigned v32u __attribute__((ext_vector_type(32)));

DEV unsigned nearo(unsigned off) { return (off >> 13) * 16384u + (off & 8191u); }
DEV unsigned rt32() { u64 t; asm volatile("s_memrealtime %0\n\ts_waitcnt lgkmcnt(0)" : "=s"(t) :: "memory"); return (unsigned)t; }
DEV unsigned cyc32() { u64 t; asm volatile("s_memtime %0\n\ts_waitcnt lgkmcnt(0)" : "=s"(t) :: "memory"); return (unsigned)t; }
template <int R1, int R2, int R3> DEV v8u rd_w(const LAS char* p) {
    return __builtin_shufflevector(cat(trd(p), trd(p + R1)), cat(trd(p + R2), trd(p + R3)), 0, 1, 2, 3, 4, 5, 6, 7);
}
DEV v16u cat8(v8u a, v8u b) { return __builtin_shufflevector(a, b, 0, 1, 2, 3, 4, 5, 6, 7, 8, 9, 10, 11, 12, 13, 14, 15); }
DEV v32u cat16(v16u a, v16u b) { return __builtin_shufflevector(a, b, 0, 1, 2, 3, 4, 5, 6, 7, 8, 9, 10, 11, 12, 13, 14, 15, 16, 17, 18, 19, 20, 21, 22, 23, 24, 25, 26, 27, 28, 29, 30, 31); }
DEV v8u cat4(v4u a, v4u b) { return __builtin_shufflevector(a, b, 0, 1, 2, 3, 4, 5, 6, 7); }

static constexpr int L_PQK = 0, L_PV = 8704, L_P = 13824, L_VT = 15360, L_X = 16384, L_K = L_X + 9216, L_MISC = 131072, L_TOT = L_MISC + 256;
static constexpr int X_FQ = 272, X_BLK = 1152;
#define ASM_A \
    "s_mov_b32 s76, 0\n" \
    "s_mov_b32 s77, 0\n" \
    "s_mov_b32 s99, 0x20000\n" \
    "s_mov_b32 s84, 0\n" \
    "s_mov_b32 s81, 0\n" \
    "s_andn2_b32 s78, s78, 3\n" \
    "s_mov_b32 s86, 0\n" \
    "s_mov_b32 s87, 0\n" \
    "s_mov_b32 s88, 0\n" \
    "s_mov_b32 s89, 0\n" \
    "s_mov_b32 s90, 0\n" \
    "s_mov_b32 s91, 0\n" \
    "s_mov_b32 s92, 0\n" \
    "s_mov_b32 s93, 0\n" \
    "s_mov_b32 s94, 0\n" \
    "s_mov_b32 s95, 0\n" \
    "s_mov_b32 s96, 0\n" \
    "s_mov_b32 s97, 0\n" \
    "s_mov_b32 s98, 0\n" \
    "v_mov_b64 v[208:209], 0\n" \
    "v_mov_b64 v[210:211], 0\n" \
    "v_mov_b64 v[212:213], 0\n" \
    "v_mov_b64 v[214:215], 0\n" \
    "v_mov_b64 v[216:217], 0\n" \
    "v_mov_b64 v[218:219], 0\n" \
    "v_mov_b64 v[220:221], 0\n" \
    "v_mov_b64 v[222:223], 0\n" \
    "v_mov_b64 v[224:225], 0\n" \
    "v_mov_b64 v[226:227], 0\n" \
    "v_mov_b64 v[228:229], 0\n" \
    "v_mov_b64 v[230:231], 0\n" \
    "s_branch L%=_Acomp\n" \
    "L%=_Aloop:\n" \
    "s_memrealtime vcc\n" \
    "s_add_u32 s80, s77, s64\n" \
    "s_waitcnt lgkmcnt(0)\n" \
    "s_sub_i32 s79, s80, vcc_lo\n" \
    "s_min_i32 s79, s79, 0x400\n" \
    "s_add_i32 s80, vcc_lo, s79\n" \
    "s_cmp_lt_i32 s79, 5\n" \
    "s_cbranch_scc1 L%=_Au1c\n" \
    "L%=_Au1a:\n" \
    "s_cmp_lt_i32 s79, 14\n" \
    "s_cbranch_scc1 L%=_Au1b\n" \
    "s_sleep 2\n" \
    "L%=_Au1b:\n" \
    "s_memrealtime vcc\n" \
    "s_waitcnt lgkmcnt(0)\n" \
    "s_sub_i32 s79, s80, vcc_lo\n" \
    "s_cmp_gt_i32 s79, 4\n" \
    "s_cbranch_scc1 L%=_Au1a\n" \
    "L%=_Au1c:\n" \
    "s_sub_i32 s79, s79, 2\n" \
    "L%=_Au1d:\n" \
    "s_sub_i32 s79, s79, 1\n" \
    "s_cmp_ge_i32 s79, 0\n" \
    "s_cbranch_scc1 L%=_Au1d\n" \
    "L%=_Apoll:\n" \
    "buffer_load_dwordx4 v[192:195], v233, s[40:43], s60 offen offset:0 sc1\n" \
    "buffer_load_dwordx4 v[196:199], v233, s[40:43], s60 offen offset:1024 sc1\n" \
    "buffer_load_dwordx4 v[200:203], v233, s[40:43], s60 offen offset:2048 sc1\n" \
    "buffer_load_dwordx4 v[204:207], v233, s[40:43], s60 offen offset:3072 sc1\n" \
    "L%=_Acomp:\n" \
    "s_waitcnt vmcnt(3)\n" \
    "v_smfmac_f32_16x16x64_bf16 v[208:211], v[192:195], v[0:7], v232\n" \
    "v_smfmac_f32_16x16x64_bf16 v[212:215], v[192:195], v[8:15], v232\n" \
    "v_smfmac_f32_16x16x64_bf16 v[216:219], v[192:195], v[64:71], v232\n" \
    "v_smfmac_f32_16x16x64_bf16 v[220:223], v[192:195], v[72:79], v232\n" \
    "s_waitcnt vmcnt(2)\n" \
    "v_smfmac_f32_16x16x64_bf16 v[208:211], v[196:199], v[16:23], v232\n" \
    "v_smfmac_f32_16x16x64_bf16 v[212:215], v[196:199], v[24:31], v232\n" \
    "v_smfmac_f32_16x16x64_bf16 v[216:219], v[196:199], v[80:87], v232\n" \
    "v_smfmac_f32_16x16x64_bf16 v[220:223], v[196:199], v[88:95], v232\n" \
    "s_waitcnt vmcnt(1)\n" \
    "v_smfmac_f32_16x16x64_bf16 v[208:211], v[200:203], v[32:39], v232\n" \
    "v_smfmac_f32_16x16x64_bf16 v[212:215], v[200:203], v[40:47], v232\n" \
    "v_smfmac_f32_16x16x64_bf16 v[216:219], v[200:203], v[96:103], v232\n" \
    "v_smfmac_f32_16x16x64_bf16 v[220:223], v[200:203], v[104:111], v232\n" \
    "s_waitcnt vmcnt(0)\n" \
    "v_smfmac_f32_16x16x64_bf16 v[208:211], v[204:207], v[48:55], v232\n" \
    "v_smfmac_f32_16x16x64_bf16 v[212:215], v[204:207], v[56:63], v232\n" \
    "v_smfmac_f32_16x16x64_bf16 v[216:219], v[204:207], v[112:119], v232\n" \
    "v_smfmac_f32_16x16x64_bf16 v[220:223], v[204:207], v[120:127], v232\n" \
    "v_add_f32 v208, v208, v210\n" \
    "v_add_f32 v209, v209, v211\n" \
    "ds_write2_b32 v234, v208, v209 offset0:0 offset1:68\n" \
    "v_add_f32 v212, v212, v214\n" \
    "v_add_f32 v213, v213, v215\n" \
    "ds_write2_b32 v238, v212, v213 offset0:0 offset1:68\n" \
    "v_add_f32 v216, v216, v218\n" \
    "v_add_f32 v217, v217, v219\n" \
    "ds_write2_b32 v234, v216, v217 offset0:136 offset1:204\n" \
    "v_add_f32 v220, v220, v222\n" \
    "v_add_f32 v221, v221, v223\n" \
    "ds_write2_b32 v238, v220, v221 offset0:136 offset1:204\n" \
    "v_cmp_u_f32 vcc, v208, v209\n" \
    "v_smfmac_f32_16x16x64_bf16 v[224:227], v[192:195], v[128:135], v232\n" \
    "v_smfmac_f32_16x16x64_bf16 v[228:231], v[192:195], v[136:143], v232\n" \
    "v_smfmac_f32_16x16x64_bf16 v[224:227], v[196:199], v[144:151], v232\n" \
    "v_smfmac_f32_16x16x64_bf16 v[228:231], v[196:199], v[152:159], v232\n" \
    "s_cbranch_vccnz L%=_Aslow\n" \
    "L%=_Aok:\n" \
    "s_waitcnt lgkmcnt(0)\n" \
    "s_barrier\n" \
    "v_smfmac_f32_16x16x64_bf16 v[224:227], v[200:203], v[160:167], v232\n" \
    "v_smfmac_f32_16x16x64_bf16 v[228:231], v[200:203], v[168:175], v232\n" \
    "v_smfmac_f32_16x16x64_bf16 v[224:227], v[204:207], v[176:183], v232\n" \
    "v_smfmac_f32_16x16x64_bf16 v[228:231], v[204:207], v[184:191], v232\n" \
    "s_add_u32 s79, s76, 1\n" \
    "v_mov_b32 v241, s79\n" \
    "s_nop 1\n" \
    "v_add_f32 v224, v224, v226\n" \
    "v_add_f32 v225, v225, v227\n" \
    "ds_write2_b32 v235, v224, v225 offset0:0 offset1:72\n" \
    "s_nop 0\n" \
    "v_add_f32 v228, v228, v230\n" \
    "v_add_f32 v229, v229, v231\n" \
    "ds_write2_b32 v235, v228, v229 offset0:160 offset1:232\n" \
    "ds_write_b32 v236, v241 offset:16\n" \
    "ds_read_b32 v244, v237\n" \
    "s_waitcnt lgkmcnt(0)\n" \
    "v_readfirstlane_b32 s79, v244\n" \
    "s_cmp_gt_u32 s79, s76\n" \
    "s_cbranch_scc1 L%=_Au2s\n" \
    "L%=_Au2k:\n" \
    "v_mov_b64 v[208:209], 0\n" \
    "v_mov_b64 v[210:211], 0\n" \
    "v_mov_b64 v[212:213], 0\n" \
    "v_mov_b64 v[214:215], 0\n" \
    "v_mov_b64 v[216:217], 0\n" \
    "v_mov_b64 v[218:219], 0\n" \
    "v_mov_b64 v[220:221], 0\n" \
    "v_mov_b64 v[222:223], 0\n" \
    "v_mov_b64 v[224:225], 0\n" \
    "v_mov_b64 v[226:227], 0\n" \
    "v_mov_b64 v[228:229], 0\n" \
    "v_mov_b64 v[230:231], 0\n" \
    "s_mov_b32 s79, s60\n" \
    "s_mov_b32 s60, s61\n" \
    "s_mov_b32 s61, s62\n" \
    "s_mov_b32 s62, s63\n" \
    "s_mov_b32 s63, s79\n" \
    "s_add_u32 s76, s76, 1\n" \
    "s_add_u32 s77, s77, s65\n" \
    "s_add_u32 s84, s84, 0x20000\n" \
    "s_add_u32 s81, s81, s65\n" \
    "s_barrier\n" \
    "s_cmp_lt_u32 s76, 64\n" \
    "s_cbranch_scc1 L%=_Aloop\n" \
    "s_branch L%=_end\n" \
    "L%=_Aslow:\n" \
    "s_nop 7\n" \
    "s_nop 0\n" \
    "v_max_u32 v244, v192, v193\n" \
    "v_max3_u32 v244, v244, v194, v195\n" \
    "v_max3_u32 v244, v244, v196, v197\n" \
    "v_max3_u32 v244, v244, v198, v199\n" \
    "v_max3_u32 v244, v244, v200, v201\n" \
    "v_max3_u32 v244, v244, v202, v203\n" \
    "v_max3_u32 v244, v244, v204, v205\n" \
    "v_max3_u32 v244, v244, v206, v207\n" \
    "v_cmp_eq_u32 vcc, -1, v244\n" \
    "s_cbranch_vccz L%=_Aok\n" \
    "s_cmp_eq_u32 s99, 0\n" \
    "s_cbranch_scc1 L%=_Aok\n" \
    "s_sub_u32 s99, s99, 1\n" \
    "s_cmp_lg_u32 s99, 0\n" \
    "s_cbranch_scc1 L%=_Au3\n" \
    "v_mov_b32 v244, 1\n" \
    "buffer_store_dword v244, off, s[44:47], 0 sc1\n" \
    "L%=_Au3:\n" \
    "s_add_u32 s79, s76, 1\n" \
    "v_mov_b32 v244, s79\n" \
    "ds_write_b32 v237, v244\n" \
    "s_sleep 1\n" \
    "v_mov_b64 v[208:209], 0\n" \
    "v_mov_b64 v[210:211], 0\n" \
    "v_mov_b64 v[212:213], 0\n" \
    "v_mov_b64 v[214:215], 0\n" \
    "v_mov_b64 v[216:217], 0\n" \
    "v_mov_b64 v[218:219], 0\n" \
    "v_mov_b64 v[220:221], 0\n" \
    "v_mov_b64 v[222:223], 0\n" \
    "v_mov_b64 v[224:225], 0\n" \
    "v_mov_b64 v[226:227], 0\n" \
    "v_mov_b64 v[228:229], 0\n" \
    "v_mov_b64 v[230:231], 0\n" \
    "s_branch L%=_Apoll\n" \
    "L%=_Au2s:\n" \
    "buffer_load_dword v244, off, s[44:47], s78 sc1\n" \
    "s_waitcnt vmcnt(0)\n" \
    "v_readfirstlane_b32 s79, v244\n" \
    "s_lshr_b32 s80, s79, 24\n" \
    "s_cmp_gt_u32 s80, s76\n" \
    "s_cbranch_scc1 L%=_Au2h\n" \
    "s_cmp_eq_u32 s99, 0\n" \
    "s_cbranch_scc1 L%=_Au2k\n" \
    "s_sub_u32 s99, s99, 1\n" \
    "s_sleep 2\n" \
    "s_branch L%=_Au2s\n" \
    "L%=_Au2h:\n" \
    "s_and_b32 s79, s79, 0xffffff\n" \
    "s_add_u32 s79, s79, s81\n" \
    "s_max_u32 s77, s77, s79\n" \
    "s_branch L%=_Au2k\n" \
    "L%=_end:\n"
#define ASM_B \
    "s_and_b32 s79, s78, 3\n" \
    "s_cmp_eq_u32 s79, 1\n" \
    "s_cbranch_scc1 L%=_S\n" \
    "s_cmp_eq_u32 s79, 2\n" \
    "s_cbranch_scc1 L%=_R\n" \
    "s_cmp_eq_u32 s79, 3\n" \
    "s_cbranch_scc1 L%=_P\n" \
    "L%=_T:\n" \
    "s_mov_b32 s76, 0\n" \
    "s_mov_b32 s77, 0\n" \
    "s_mov_b32 s99, 0x20000\n" \
    "s_mov_b32 s84, 0\n" \
    "s_mov_b32 s81, 0\n" \
    "s_andn2_b32 s78, s78, 3\n" \
    "s_mov_b32 s86, 0\n" \
    "s_mov_b32 s87, 0\n" \
    "s_mov_b32 s88, 0\n" \
    "s_mov_b32 s89, 0\n" \
    "s_mov_b32 s90, 0\n" \
    "s_mov_b32 s91, 0\n" \
    "s_mov_b32 s92, 0\n" \
    "s_mov_b32 s93, 0\n" \
    "s_mov_b32 s94, 0\n" \
    "s_mov_b32 s95, 0\n" \
    "s_mov_b32 s96, 0\n" \
    "s_mov_b32 s97, 0\n" \
    "s_mov_b32 s98, 0\n" \
    "v_mov_b32 v96, 0x3f803f80\n" \
    "v_mov_b32 v97, 0x3f803f80\n" \
    "v_mov_b32 v98, 0x3f803f80\n" \
    "v_mov_b32 v99, 0x3f803f80\n" \
    "v_mov_b32 v100, 0x3f803f80\n" \
    "v_mov_b32 v101, 0x3f803f80\n" \
    "v_mov_b64 v[102:103], 0\n" \
    "v_mov_b64 v[64:65], 0\n" \
    "v_mov_b64 v[66:67], 0\n" \
    "v_mov_b64 v[68:69], 0\n" \
    "v_mov_b64 v[70:71], 0\n" \
    "v_mov_b64 v[72:73], 0\n" \
    "v_mov_b64 v[74:75], 0\n" \
    "v_mov_b64 v[76:77], 0\n" \
    "v_mov_b64 v[78:79], 0\n" \
    "v_mov_b64 v[80:81], 0\n" \
    "v_mov_b64 v[82:83], 0\n" \
    "v_mov_b64 v[84:85], 0\n" \
    "v_mov_b64 v[86:87], 0\n" \
    "v_mov_b64 v[88:89], 0\n" \
    "v_mov_b64 v[90:91], 0\n" \
    "v_mov_b64 v[92:93], 0\n" \
    "v_mov_b64 v[94:95], 0\n" \
    "v_mov_b64 v[192:193], 0\n" \
    "v_mov_b64 v[194:195], 0\n" \
    "L%=_Tloop:\n" \
    "ds_read_b64_tr_b16 v[104:105], v228 offset:16384\n" \
    "ds_read_b64_tr_b16 v[106:107], v228 offset:16416\n" \
    "ds_read_b64_tr_b16 v[108:109], v228 offset:18688\n" \
    "ds_read_b64_tr_b16 v[110:111], v228 offset:18720\n" \
    "ds_read_b64_tr_b16 v[112:113], v228 offset:20992\n" \
    "ds_read_b64_tr_b16 v[114:115], v228 offset:21024\n" \
    "ds_read_b64_tr_b16 v[116:117], v228 offset:23296\n" \
    "ds_read_b64_tr_b16 v[118:119], v228 offset:23328\n" \
    "ds_read_b64_tr_b16 v[120:121], v228 offset:16656\n" \
    "ds_read_b64_tr_b16 v[122:123], v228 offset:16688\n" \
    "ds_read_b64_tr_b16 v[124:125], v228 offset:18960\n" \
    "ds_read_b64_tr_b16 v[126:127], v228 offset:18992\n" \
    "ds_read_b64_tr_b16 v[128:129], v228 offset:21264\n" \
    "ds_read_b64_tr_b16 v[130:131], v228 offset:21296\n" \
    "ds_read_b64_tr_b16 v[132:133], v228 offset:23568\n" \
    "ds_read_b64_tr_b16 v[134:135], v228 offset:23600\n" \
    "s_waitcnt lgkmcnt(14)\n" \
    "v_mfma_f32_16x16x32_bf16 v[136:139], v[104:107], v[0:3], 0\n" \
    "v_mfma_f32_16x16x32_bf16 v[144:147], v[104:107], v[8:11], 0\n" \
    "v_mfma_f32_16x16x32_bf16 v[152:155], v[104:107], v[16:19], 0\n" \
    "v_mfma_f32_16x16x32_bf16 v[160:163], v[104:107], v[24:27], 0\n" \
    "s_waitcnt lgkmcnt(12)\n" \
    "v_mfma_f32_16x16x32_bf16 v[136:139], v[108:111], v[4:7], v[136:139]\n" \
    "v_mfma_f32_16x16x32_bf16 v[144:147], v[108:111], v[12:15], v[144:147]\n" \
    "v_mfma_f32_16x16x32_bf16 v[152:155], v[108:111], v[20:23], v[152:155]\n" \
    "v_mfma_f32_16x16x32_bf16 v[160:163], v[108:111], v[28:31], v[160:163]\n" \
    "s_waitcnt lgkmcnt(10)\n" \
    "v_mfma_f32_16x16x32_bf16 v[136:139], v[112:115], v[32:35], v[136:139]\n" \
    "v_mfma_f32_16x16x32_bf16 v[144:147], v[112:115], v[40:43], v[144:147]\n" \
    "v_mfma_f32_16x16x32_bf16 v[152:155], v[112:115], v[48:51], v[152:155]\n" \
    "v_mfma_f32_16x16x32_bf16 v[160:163], v[112:115], v[56:59], v[160:163]\n" \
    "s_waitcnt lgkmcnt(8)\n" \
    "v_mfma_f32_16x16x32_bf16 v[136:139], v[116:119], v[36:39], v[136:139]\n" \
    "v_mfma_f32_16x16x32_bf16 v[144:147], v[116:119], v[44:47], v[144:147]\n" \
    "v_mfma_f32_16x16x32_bf16 v[152:155], v[116:119], v[52:55], v[152:155]\n" \
    "v_mfma_f32_16x16x32_bf16 v[160:163], v[116:119], v[60:63], v[160:163]\n" \
    "s_waitcnt lgkmcnt(6)\n" \
    "v_mfma_f32_16x16x32_bf16 v[140:143], v[120:123], v[0:3], 0\n" \
    "v_mfma_f32_16x16x32_bf16 v[148:151], v[120:123], v[8:11], 0\n" \
    "v_mfma_f32_16x16x32_bf16 v[156:159], v[120:123], v[16:19], 0\n" \
    "v_mfma_f32_16x16x32_bf16 v[164:167], v[120:123], v[24:27], 0\n" \
    "s_waitcnt lgkmcnt(4)\n" \
    "v_mfma_f32_16x16x32_bf16 v[140:143], v[124:127], v[4:7], v[140:143]\n" \
    "v_mfma_f32_16x16x32_bf16 v[148:151], v[124:127], v[12:15], v[148:151]\n" \
    "v_mfma_f32_16x16x32_bf16 v[156:159], v[124:127], v[20:23], v[156:159]\n" \
    "v_mfma_f32_16x16x32_bf16 v[164:167], v[124:127], v[28:31], v[164:167]\n" \
    "s_waitcnt lgkmcnt(2)\n" \
    "v_mfma_f32_16x16x32_bf16 v[140:143], v[128:131], v[32:35], v[140:143]\n" \
    "v_mfma_f32_16x16x32_bf16 v[148:151], v[128:131], v[40:43], v[148:151]\n" \
    "v_mfma_f32_16x16x32_bf16 v[156:159], v[128:131], v[48:51], v[156:159]\n" \
    "v_mfma_f32_16x16x32_bf16 v[164:167], v[128:131], v[56:59], v[164:167]\n" \
    "s_waitcnt lgkmcnt(0)\n" \
    "v_mfma_f32_16x16x32_bf16 v[140:143], v[132:135], v[36:39], v[140:143]\n" \
    "v_mfma_f32_16x16x32_bf16 v[148:151], v[132:135], v[44:47], v[148:151]\n" \
    "v_mfma_f32_16x16x32_bf16 v[156:159], v[132:135], v[52:55], v[156:159]\n" \
    "v_mfma_f32_16x16x32_bf16 v[164:167], v[132:135], v[60:63], v[164:167]\n" \
    "v_cvt_pk_bf16_f32 v65, v136, v137\n" \
    "v_cvt_pk_bf16_f32 v68, v138, v139\n" \
    "v_cvt_pk_bf16_f32 v73, v144, v145\n" \
    "v_cvt_pk_bf16_f32 v76, v146, v147\n" \
    "v_cvt_pk_bf16_f32 v81, v152, v153\n" \
    "v_cvt_pk_bf16_f32 v84, v154, v155\n" \
    "v_cvt_pk_bf16_f32 v89, v160, v161\n" \
    "v_cvt_pk_bf16_f32 v92, v162, v163\n" \
    "v_cvt_pk_bf16_f32 v66, v142, v143\n" \
    "v_cvt_pk_bf16_f32 v67, v142, v143\n" \
    "v_cvt_pk_bf16_f32 v69, v140, v141\n" \
    "v_cvt_pk_bf16_f32 v74, v150, v151\n" \
    "v_cvt_pk_bf16_f32 v75, v150, v151\n" \
    "v_cvt_pk_bf16_f32 v77, v148, v149\n" \
    "v_cvt_pk_bf16_f32 v82, v158, v159\n" \
    "v_cvt_pk_bf16_f32 v83, v158, v159\n" \
    "v_cvt_pk_bf16_f32 v85, v156, v157\n" \
    "v_cvt_pk_bf16_f32 v90, v166, v167\n" \
    "v_cvt_pk_bf16_f32 v91, v166, v167\n" \
    "v_cvt_pk_bf16_f32 v93, v164, v165\n" \
    "v_permlane16_swap_b32 v66, v67\n" \
    "v_permlane16_swap_b32 v74, v75\n" \
    "v_permlane16_swap_b32 v82, v83\n" \
    "v_permlane16_swap_b32 v90, v91\n" \
    "v_mov_b64 v[176:177], 0\n" \
    "v_mov_b64 v[178:179], 0\n" \
    "v_mov_b64 v[180:181], 0\n" \
    "v_mov_b64 v[182:183], 0\n" \
    "v_mov_b64 v[184:185], 0\n" \
    "v_mov_b64 v[186:187], 0\n" \
    "v_mov_b64 v[188:189], 0\n" \
    "v_mov_b64 v[190:191], 0\n" \
    "v_mov_b64 v[196:197], 0\n" \
    "v_mov_b64 v[198:199], 0\n" \
    "v_mov_b64 v[200:201], 0\n" \
    "v_mov_b64 v[202:203], 0\n" \
    "v_mov_b64 v[204:205], 0\n" \
    "v_mov_b64 v[206:207], 0\n" \
    "v_mov_b64 v[208:209], 0\n" \
    "v_mov_b64 v[210:211], 0\n" \
    "v_mov_b64 v[212:213], 0\n" \
    "v_mov_b64 v[214:215], 0\n" \
    "s_add_u32 s85, s60, 0x1000\n" \
    "s_barrier\n" \
    "s_cmp_eq_u32 s74, 0\n" \
    "s_cbranch_scc1 L%=_Tlead\n" \
    "ds_read_b32 v216, v229\n" \
    "s_waitcnt lgkmcnt(0)\n" \
    "v_readfirstlane_b32 s79, v216\n" \
    "s_cmp_gt_u32 s79, s76\n" \
    "s_cbranch_scc1 L%=_Tu1s\n" \
    "L%=_Tu1k:\n" \
    "L%=_Tled:\n" \
    "s_cmp_eq_u32 s76, 63\n" \
    "s_cbranch_scc1 L%=_Tfin\n" \
    "s_memrealtime vcc\n" \
    "s_add_u32 s80, s77, s64\n" \
    "s_add_u32 s80, s80, s66\n" \
    "s_waitcnt lgkmcnt(0)\n" \
    "s_sub_i32 s79, s80, vcc_lo\n" \
    "s_min_i32 s79, s79, 0x400\n" \
    "s_add_i32 s80, vcc_lo, s79\n" \
    "s_cmp_lt_i32 s79, 5\n" \
    "s_cbranch_scc1 L%=_Tu2c\n" \
    "L%=_Tu2a:\n" \
    "s_cmp_lt_i32 s79, 14\n" \
    "s_cbranch_scc1 L%=_Tu2b\n" \
    "s_sleep 2\n" \
    "L%=_Tu2b:\n" \
    "s_memrealtime vcc\n" \
    "s_waitcnt lgkmcnt(0)\n" \
    "s_sub_i32 s79, s80, vcc_lo\n" \
    "s_cmp_gt_i32 s79, 4\n" \
    "s_cbranch_scc1 L%=_Tu2a\n" \
    "L%=_Tu2c:\n" \
    "s_sub_i32 s79, s79, 2\n" \
    "L%=_Tu2d:\n" \
    "s_sub_i32 s79, s79, 1\n" \
    "s_cmp_ge_i32 s79, 0\n" \
    "s_cbranch_scc1 L%=_Tu2d\n" \
    "buffer_load_dwordx4 v[104:107], v230, s[40:43], s60 offen offset:0 sc1\n" \
    "buffer_load_dwordx2 v[120:121], v231, s[40:43], s60 offen offset:0 sc1\n" \
    "buffer_load_dwordx4 v[108:111], v230, s[40:43], s60 offen offset:2048 sc1\n" \
    "buffer_load_dwordx2 v[122:123], v231, s[40:43], s60 offen offset:2048 sc1\n" \
    "buffer_load_dwordx4 v[112:115], v230, s[40:43], s85 offen offset:0 sc1\n" \
    "buffer_load_dwordx2 v[124:125], v231, s[40:43], s85 offen offset:0 sc1\n" \
    "buffer_load_dwordx4 v[116:119], v230, s[40:43], s85 offen offset:2048 sc1\n" \
    "buffer_load_dwordx2 v[126:127], v231, s[40:43], s85 offen offset:2048 sc1\n" \
    "s_memrealtime vcc\n" \
    "s_add_u32 s80, s77, s64\n" \
    "s_add_u32 s80, s80, s67\n" \
    "s_waitcnt lgkmcnt(0)\n" \
    "s_sub_i32 s79, s80, vcc_lo\n" \
    "s_min_i32 s79, s79, 0x400\n" \
    "s_add_i32 s80, vcc_lo, s79\n" \
    "s_cmp_lt_i32 s79, 5\n" \
    "s_cbranch_scc1 L%=_Tu3c\n" \
    "L%=_Tu3a:\n" \
    "s_cmp_lt_i32 s79, 14\n" \
    "s_cbranch_scc1 L%=_Tu3b\n" \
    "s_sleep 2\n" \
    "L%=_Tu3b:\n" \
    "s_memrealtime vcc\n" \
    "s_waitcnt lgkmcnt(0)\n" \
    "s_sub_i32 s79, s80, vcc_lo\n" \
    "s_cmp_gt_i32 s79, 4\n" \
    "s_cbranch_scc1 L%=_Tu3a\n" \
    "L%=_Tu3c:\n" \
    "s_sub_i32 s79, s79, 2\n" \
    "L%=_Tu3d:\n" \
    "s_sub_i32 s79, s79, 1\n" \
    "s_cmp_ge_i32 s79, 0\n" \
    "s_cbranch_scc1 L%=_Tu3d\n" \
    "buffer_load_dwordx4 v[168:171], v225, s[40:43], s60 offen sc1\n" \
    "buffer_load_dwordx4 v[172:175], v225, s[40:43], s85 offen sc1\n" \
    "L%=_Tsc:\n" \
    "s_waitcnt vmcnt(6)\n" \
    "v_pk_add_f32 v[128:129], v[104:105], v[108:109]\n" \
    "v_pk_add_f32 v[130:131], v[106:107], v[110:111]\n" \
    "v_pk_add_f32 v[132:133], v[120:121], v[122:123]\n" \
    "s_waitcnt vmcnt(4)\n" \
    "v_pk_add_f32 v[128:129], v[128:129], v[112:113]\n" \
    "v_pk_add_f32 v[130:131], v[130:131], v[114:115]\n" \
    "v_pk_add_f32 v[132:133], v[132:133], v[124:125]\n" \
    "s_waitcnt vmcnt(2)\n" \
    "v_pk_add_f32 v[128:129], v[128:129], v[116:117]\n" \
    "v_pk_add_f32 v[130:131], v[130:131], v[118:119]\n" \
    "v_pk_add_f32 v[132:133], v[132:133], v[126:127]\n" \
    "v_max3_f32 v136, v128, v129, v130\n" \
    "v_max3_f32 v137, v131, v132, v133\n" \
    "v_max_f32 v136, v136, v137\n" \
    "s_nop 1\n" \
    "v_max_f32_dpp v136, v136, v136 quad_perm:[1,0,3,2] row_mask:0xf bank_mask:0xf\n" \
    "s_nop 1\n" \
    "v_max_f32_dpp v136, v136, v136 quad_perm:[2,3,0,1] row_mask:0xf bank_mask:0xf\n" \
    "s_nop 1\n" \
    "v_max_f32_dpp v136, v136, v136 row_half_mirror row_mask:0xf bank_mask:0xf\n" \
    "v_mul_f32 v136, s72, v136\n" \
    "v_pk_fma_f32 v[128:129], v[128:129], s[72:73], v[136:137] op_sel_hi:[1,1,0] neg_lo:[0,0,1] neg_hi:[0,0,1]\n" \
    "v_pk_fma_f32 v[132:133], v[132:133], s[72:73], v[136:137] op_sel_hi:[1,1,0] neg_lo:[0,0,1] neg_hi:[0,0,1]\n" \
    "v_pk_fma_f32 v[130:131], v[130:131], s[72:73], v[136:137] op_sel_hi:[1,1,0] neg_lo:[0,0,1] neg_hi:[0,0,1]\n" \
    "v_exp_f32 v128, v128\n" \
    "v_exp_f32 v129, v129\n" \
    "v_exp_f32 v132, v132\n" \
    "v_exp_f32 v133, v133\n" \
    "v_exp_f32 v130, v130\n" \
    "v_exp_f32 v131, v131\n" \
    "v_cvt_pk_bf16_f32 v140, v128, v129\n" \
    "v_cvt_pk_bf16_f32 v141, v132, v133\n" \
    "v_cvt_pk_bf16_f32 v142, v130, v131\n" \
    "ds_write_b96 v232, v[140:142]\n" \
    "v_or3_b32 v136, v140, v141, v142\n" \
    "v_and_b32 v136, 0x40004000, v136\n" \
    "v_cmp_ne_u32 vcc, 0, v136\n" \
    "s_waitcnt vmcnt(0)\n" \
    "v_smfmac_f32_16x16x64_bf16 v[176:179], v[168:171], v[0:7], v224\n" \
    "v_smfmac_f32_16x16x64_bf16 v[180:183], v[168:171], v[8:15], v224\n" \
    "v_smfmac_f32_16x16x64_bf16 v[176:179], v[172:175], v[32:39], v224\n" \
    "s_cbranch_vccnz L%=_Tslow\n" \
    "L%=_Tok:\n" \
    "s_waitcnt lgkmcnt(0)\n" \
    "s_barrier\n" \
    "ds_read_b96 v[192:194], v227\n" \
    "L%=_Tvc:\n" \
    "v_smfmac_f32_16x16x64_bf16 v[180:183], v[172:175], v[40:47], v224\n" \
    "v_smfmac_f32_16x16x64_bf16 v[184:187], v[168:171], v[16:23], v224\n" \
    "v_add_f32 v176, v176, v178\n" \
    "v_smfmac_f32_16x16x64_bf16 v[188:191], v[168:171], v[24:31], v224\n" \
    "v_add_f32 v177, v177, v179\n" \
    "v_smfmac_f32_16x16x64_bf16 v[184:187], v[172:175], v[48:55], v224\n" \
    "v_cvt_pk_bf16_f32 v64, v176, v177\n" \
    "v_smfmac_f32_16x16x64_bf16 v[188:191], v[172:175], v[56:63], v224\n" \
    "v_add_f32 v180, v180, v182\n" \
    "s_waitcnt lgkmcnt(0)\n" \
    "v_smfmac_f32_16x16x64_bf16 v[212:215], v[192:195], v[96:103], v224\n" \
    "v_add_f32 v181, v181, v183\n" \
    "v_smfmac_f32_16x16x64_bf16 v[196:199], v[192:195], v[64:71], v224\n" \
    "v_cvt_pk_bf16_f32 v72, v180, v181\n" \
    "v_add_f32 v184, v184, v186\n" \
    "v_add_f32 v185, v185, v187\n" \
    "v_smfmac_f32_16x16x64_bf16 v[200:203], v[192:195], v[72:79], v224\n" \
    "v_cvt_pk_bf16_f32 v80, v184, v185\n" \
    "v_add_f32 v188, v188, v190\n" \
    "v_add_f32 v189, v189, v191\n" \
    "v_smfmac_f32_16x16x64_bf16 v[204:207], v[192:195], v[80:87], v224\n" \
    "v_cvt_pk_bf16_f32 v88, v188, v189\n" \
    "v_add_f32 v212, v212, v214\n" \
    "v_add_f32 v213, v213, v215\n" \
    "v_smfmac_f32_16x16x64_bf16 v[208:211], v[192:195], v[88:95], v224\n" \
    "v_cmp_u_f32 vcc, v176, v177\n" \
    "v_rcp_f32 v220, v212\n" \
    "v_rcp_f32 v221, v213\n" \
    "v_add_f32 v196, v196, v198\n" \
    "v_add_f32 v197, v197, v199\n" \
    "v_pk_add_f32 v[200:201], v[200:201], v[202:203]\n" \
    "v_pk_add_f32 v[204:205], v[204:205], v[206:207]\n" \
    "s_nop 0\n" \
    "v_pk_add_f32 v[208:209], v[208:209], v[210:211]\n" \
    "v_pk_mul_f32 v[196:197], v[196:197], v[220:221]\n" \
    "v_pk_mul_f32 v[200:201], v[200:201], v[220:221]\n" \
    "v_pk_mul_f32 v[204:205], v[204:205], v[220:221]\n" \
    "v_pk_mul_f32 v[208:209], v[208:209], v[220:221]\n" \
    "v_cvt_pk_bf16_f32 v216, v196, v200\n" \
    "v_cvt_pk_bf16_f32 v217, v204, v208\n" \
    "v_cvt_pk_bf16_f32 v218, v197, v201\n" \
    "v_cvt_pk_bf16_f32 v219, v205, v209\n" \
    "s_cbranch_vccnz L%=_Tvslow\n" \
    "L%=_Tvok:\n" \
    "buffer_store_dwordx4 v[216:219], v226, s[40:43], s60 offen sc1\n" \
    "s_mov_b32 s79, s60\n" \
    "s_mov_b32 s60, s61\n" \
    "s_mov_b32 s61, s62\n" \
    "s_mov_b32 s62, s63\n" \
    "s_mov_b32 s63, s79\n" \
    "s_add_u32 s76, s76, 1\n" \
    "s_add_u32 s77, s77, s65\n" \
    "s_add_u32 s84, s84, 0x20000\n" \
    "s_add_u32 s81, s81, s65\n" \
    "s_branch L%=_Tloop\n" \
    "L%=_Tslow:\n" \
    "s_nop 7\n" \
    "s_nop 0\n" \
    "v_max_u32 v136, v104, v105\n" \
    "v_max3_u32 v136, v136, v106, v107\n" \
    "v_max3_u32 v136, v136, v108, v109\n" \
    "v_max3_u32 v136, v136, v110, v111\n" \
    "v_max3_u32 v136, v136, v112, v113\n" \
    "v_max3_u32 v136, v136, v114, v115\n" \
    "v_max3_u32 v136, v136, v116, v117\n" \
    "v_max3_u32 v136, v136, v118, v119\n" \
    "v_max3_u32 v136, v136, v120, v121\n" \
    "v_max3_u32 v136, v136, v122, v123\n" \
    "v_max3_u32 v136, v136, v124, v125\n" \
    "v_max3_u32 v136, v136, v126, v127\n" \
    "v_cmp_eq_u32 vcc, -1, v136\n" \
    "s_cbranch_vccz L%=_Tok\n" \
    "s_cmp_eq_u32 s99, 0\n" \
    "s_cbranch_scc1 L%=_Tok\n" \
    "s_sub_u32 s99, s99, 1\n" \
    "s_cmp_lg_u32 s99, 0\n" \
    "s_cbranch_scc1 L%=_Tu4\n" \
    "v_mov_b32 v136, 1\n" \
    "buffer_store_dword v136, off, s[44:47], 0 sc1\n" \
    "L%=_Tu4:\n" \
    "s_add_u32 s79, s76, 2\n" \
    "v_mov_b32 v136, s79\n" \
    "ds_write_b32 v229, v136\n" \
    "s_sleep 1\n" \
    "buffer_load_dwordx4 v[104:107], v230, s[40:43], s60 offen offset:0 sc1\n" \
    "buffer_load_dwordx2 v[120:121], v231, s[40:43], s60 offen offset:0 sc1\n" \
    "buffer_load_dwordx4 v[108:111], v230, s[40:43], s60 offen offset:2048 sc1\n" \
    "buffer_load_dwordx2 v[122:123], v231, s[40:43], s60 offen offset:2048 sc1\n" \
    "buffer_load_dwordx4 v[112:115], v230, s[40:43], s85 offen offset:0 sc1\n" \
    "buffer_load_dwordx2 v[124:125], v231, s[40:43], s85 offen offset:0 sc1\n" \
    "buffer_load_dwordx4 v[116:119], v230, s[40:43], s85 offen offset:2048 sc1\n" \
    "buffer_load_dwordx2 v[126:127], v231, s[40:43], s85 offen offset:2048 sc1\n" \
    "buffer_load_dwordx4 v[168:171], v225, s[40:43], s60 offen sc1\n" \
    "buffer_load_dwordx4 v[172:175], v225, s[40:43], s85 offen sc1\n" \
    "v_mov_b64 v[176:177], 0\n" \
    "v_mov_b64 v[178:179], 0\n" \
    "v_mov_b64 v[180:181], 0\n" \
    "v_mov_b64 v[182:183], 0\n" \
    "v_mov_b64 v[184:185], 0\n" \
    "v_mov_b64 v[186:187], 0\n" \
    "v_mov_b64 v[188:189], 0\n" \
    "v_mov_b64 v[190:191], 0\n" \
    "s_branch L%=_Tsc\n" \
    "L%=_Tvslow:\n" \
    "s_nop 7\n" \
    "s_nop 0\n" \
    "v_max_u32 v136, v168, v169\n" \
    "v_max3_u32 v136, v136, v170, v171\n" \
    "v_max3_u32 v136, v136, v172, v173\n" \
    "v_max3_u32 v136, v136, v174, v175\n" \
    "v_cmp_eq_u32 vcc, -1, v136\n" \
    "s_cbranch_vccz L%=_Tvok\n" \
    "s_cmp_eq_u32 s99, 0\n" \
    "s_cbranch_scc1 L%=_Tvok\n" \
    "s_sub_u32 s99, s99, 1\n" \
    "s_cmp_lg_u32 s99, 0\n" \
    "s_cbranch_scc1 L%=_Tu5\n" \
    "v_mov_b32 v136, 1\n" \
    "buffer_store_dword v136, off, s[44:47], 0 sc1\n" \
    "L%=_Tu5:\n" \
    "s_add_u32 s79, s76, 2\n" \
    "v_mov_b32 v136, s79\n" \
    "ds_write_b32 v229, v136\n" \
    "s_sleep 1\n" \
    "buffer_load_dwordx4 v[168:171], v225, s[40:43], s60 offen sc1\n" \
    "buffer_load_dwordx4 v[172:175], v225, s[40:43], s85 offen sc1\n" \
    "v_mov_b64 v[176:177], 0\n" \
    "v_mov_b64 v[178:179], 0\n" \
    "v_mov_b64 v[180:181], 0\n" \
    "v_mov_b64 v[182:183], 0\n" \
    "v_mov_b64 v[184:185], 0\n" \
    "v_mov_b64 v[186:187], 0\n" \
    "v_mov_b64 v[188:189], 0\n" \
    "v_mov_b64 v[190:191], 0\n" \
    "v_mov_b64 v[196:197], 0\n" \
    "v_mov_b64 v[198:199], 0\n" \
    "v_mov_b64 v[200:201], 0\n" \
    "v_mov_b64 v[202:203], 0\n" \
    "v_mov_b64 v[204:205], 0\n" \
    "v_mov_b64 v[206:207], 0\n" \
    "v_mov_b64 v[208:209], 0\n" \
    "v_mov_b64 v[210:211], 0\n" \
    "v_mov_b64 v[212:213], 0\n" \
    "v_mov_b64 v[214:215], 0\n" \
    "s_waitcnt vmcnt(0)\n" \
    "v_smfmac_f32_16x16x64_bf16 v[176:179], v[168:171], v[0:7], v224\n" \
    "v_smfmac_f32_16x16x64_bf16 v[180:183], v[168:171], v[8:15], v224\n" \
    "v_smfmac_f32_16x16x64_bf16 v[176:179], v[172:175], v[32:39], v224\n" \
    "s_nop 7\n" \
    "s_nop 0\n" \
    "s_branch L%=_Tvc\n" \
    "L%=_Tlead:\n" \
    "s_memrealtime vcc\n" \
    "s_add_u32 s80, s77, s64\n" \
    "s_add_u32 s80, s80, s71\n" \
    "s_waitcnt lgkmcnt(0)\n" \
    "s_sub_i32 s79, vcc_lo, s80\n" \
    "s_cmp_gt_i32 s79, 0\n" \
    "s_addc_u32 s98, s98, 0\n" \
    "s_cmp_gt_i32 s79, 0\n" \
    "s_cselect_b32 s80, s70, 0\n" \
    "s_max_i32 s79, s79, 0\n" \
    "s_add_u32 s77, s77, s79\n" \
    "s_add_u32 s77, s77, s80\n" \
    "s_sub_u32 s79, s77, s81\n" \
    "s_and_b32 s79, s79, 0xffffff\n" \
    "s_add_u32 s80, s76, 1\n" \
    "s_lshl_b32 s80, s80, 24\n" \
    "s_or_b32 s79, s79, s80\n" \
    "v_mov_b32 v216, s79\n" \
    "buffer_store_dword v216, v233, s[44:47], 0 offen sc1\n" \
    "s_branch L%=_Tled\n" \
    "L%=_Tfin:\n" \
    "s_barrier\n" \
    "s_branch L%=_end\n" \
    "L%=_Tu1s:\n" \
    "buffer_load_dword v216, off, s[44:47], s78 sc1\n" \
    "s_waitcnt vmcnt(0)\n" \
    "v_readfirstlane_b32 s79, v216\n" \
    "s_lshr_b32 s80, s79, 24\n" \
    "s_cmp_gt_u32 s80, s76\n" \
    "s_cbranch_scc1 L%=_Tu1h\n" \
    "s_cmp_eq_u32 s99, 0\n" \
    "s_cbranch_scc1 L%=_Tu1k\n" \
    "s_sub_u32 s99, s99, 1\n" \
    "s_sleep 2\n" \
    "s_branch L%=_Tu1s\n" \
    "L%=_Tu1h:\n" \
    "s_and_b32 s79, s79, 0xffffff\n" \
    "s_add_u32 s79, s79, s81\n" \
    "s_max_u32 s77, s77, s79\n" \
    "s_branch L%=_Tu1k\n" \
    "L%=_S:\n" \
    "s_mov_b32 s76, 0\n" \
    "s_mov_b32 s77, 0\n" \
    "s_mov_b32 s99, 0x20000\n" \
    "s_mov_b32 s84, 0\n" \
    "s_mov_b32 s81, 0\n" \
    "s_andn2_b32 s78, s78, 3\n" \
    "s_mov_b32 s86, 0\n" \
    "s_mov_b32 s87, 0\n" \
    "s_mov_b32 s88, 0\n" \
    "s_mov_b32 s89, 0\n" \
    "s_mov_b32 s90, 0\n" \
    "s_mov_b32 s91, 0\n" \
    "s_mov_b32 s92, 0\n" \
    "s_mov_b32 s93, 0\n" \
    "s_mov_b32 s94, 0\n" \
    "s_mov_b32 s95, 0\n" \
    "s_mov_b32 s96, 0\n" \
    "s_mov_b32 s97, 0\n" \
    "s_mov_b32 s98, 0\n" \
    "v_mov_b32 v96, 0x3f803f80\n" \
    "v_mov_b32 v97, 0x3f803f80\n" \
    "v_mov_b32 v98, 0x3f803f80\n" \
    "v_mov_b32 v99, 0x3f803f80\n" \
    "v_mov_b32 v100, 0x3f803f80\n" \
    "v_mov_b32 v101, 0x3f803f80\n" \
    "v_mov_b64 v[102:103], 0\n" \
    "v_mov_b64 v[64:65], 0\n" \
    "v_mov_b64 v[66:67], 0\n" \
    "v_mov_b64 v[68:69], 0\n" \
    "v_mov_b64 v[70:71], 0\n" \
    "v_mov_b64 v[72:73], 0\n" \
    "v_mov_b64 v[74:75], 0\n" \
    "v_mov_b64 v[76:77], 0\n" \
    "v_mov_b64 v[78:79], 0\n" \
    "v_mov_b64 v[80:81], 0\n" \
    "v_mov_b64 v[82:83], 0\n" \
    "v_mov_b64 v[84:85], 0\n" \
    "v_mov_b64 v[86:87], 0\n" \
    "v_mov_b64 v[88:89], 0\n" \
    "v_mov_b64 v[90:91], 0\n" \
    "v_mov_b64 v[92:93], 0\n" \
    "v_mov_b64 v[94:95], 0\n" \
    "v_mov_b64 v[192:193], 0\n" \
    "v_mov_b64 v[194:195], 0\n" \
    "L%=_Sloop:\n" \
    "ds_read_b64_tr_b16 v[104:105], v228 offset:16384\n" \
    "ds_read_b64_tr_b16 v[106:107], v228 offset:16416\n" \
    "ds_read_b64_tr_b16 v[108:109], v228 offset:18688\n" \
    "ds_read_b64_tr_b16 v[110:111], v228 offset:18720\n" \
    "ds_read_b64_tr_b16 v[112:113], v228 offset:20992\n" \
    "ds_read_b64_tr_b16 v[114:115], v228 offset:21024\n" \
    "ds_read_b64_tr_b16 v[116:117], v228 offset:23296\n" \
    "ds_read_b64_tr_b16 v[118:119], v228 offset:23328\n" \
    "ds_read_b64_tr_b16 v[120:121], v228 offset:16656\n" \
    "ds_read_b64_tr_b16 v[122:123], v228 offset:16688\n" \
    "ds_read_b64_tr_b16 v[124:125], v228 offset:18960\n" \
    "ds_read_b64_tr_b16 v[126:127], v228 offset:18992\n" \
    "ds_read_b64_tr_b16 v[128:129], v228 offset:21264\n" \
    "ds_read_b64_tr_b16 v[130:131], v228 offset:21296\n" \
    "ds_read_b64_tr_b16 v[132:133], v228 offset:23568\n" \
    "ds_read_b64_tr_b16 v[134:135], v228 offset:23600\n" \
    "s_waitcnt lgkmcnt(14)\n" \
    "v_mfma_f32_16x16x32_bf16 v[136:139], v[104:107], v[0:3], 0\n" \
    "v_mfma_f32_16x16x32_bf16 v[144:147], v[104:107], v[8:11], 0\n" \
    "v_mfma_f32_16x16x32_bf16 v[152:155], v[104:107], v[16:19], 0\n" \
    "v_mfma_f32_16x16x32_bf16 v[160:163], v[104:107], v[24:27], 0\n" \
    "s_waitcnt lgkmcnt(12)\n" \
    "v_mfma_f32_16x16x32_bf16 v[136:139], v[108:111], v[4:7], v[136:139]\n" \
    "v_mfma_f32_16x16x32_bf16 v[144:147], v[108:111], v[12:15], v[144:147]\n" \
    "v_mfma_f32_16x16x32_bf16 v[152:155], v[108:111], v[20:23], v[152:155]\n" \
    "v_mfma_f32_16x16x32_bf16 v[160:163], v[108:111], v[28:31], v[160:163]\n" \
    "s_waitcnt lgkmcnt(10)\n" \
    "v_mfma_f32_16x16x32_bf16 v[136:139], v[112:115], v[32:35], v[136:139]\n" \
    "v_mfma_f32_16x16x32_bf16 v[144:147], v[112:115], v[40:43], v[144:147]\n" \
    "v_mfma_f32_16x16x32_bf16 v[152:155], v[112:115], v[48:51], v[152:155]\n" \
    "v_mfma_f32_16x16x32_bf16 v[160:163], v[112:115], v[56:59], v[160:163]\n" \
    "s_waitcnt lgkmcnt(8)\n" \
    "v_mfma_f32_16x16x32_bf16 v[136:139], v[116:119], v[36:39], v[136:139]\n" \
    "v_mfma_f32_16x16x32_bf16 v[144:147], v[116:119], v[44:47], v[144:147]\n" \
    "v_mfma_f32_16x16x32_bf16 v[152:155], v[116:119], v[52:55], v[152:155]\n" \
    "v_mfma_f32_16x16x32_bf16 v[160:163], v[116:119], v[60:63], v[160:163]\n" \
    "s_waitcnt lgkmcnt(6)\n" \
    "v_mfma_f32_16x16x32_bf16 v[140:143], v[120:123], v[0:3], 0\n" \
    "v_mfma_f32_16x16x32_bf16 v[148:151], v[120:123], v[8:11], 0\n" \
    "v_mfma_f32_16x16x32_bf16 v[156:159], v[120:123], v[16:19], 0\n" \
    "v_mfma_f32_16x16x32_bf16 v[164:167], v[120:123], v[24:27], 0\n" \
    "s_waitcnt lgkmcnt(4)\n" \
    "v_mfma_f32_16x16x32_bf16 v[140:143], v[124:127], v[4:7], v[140:143]\n" \
    "v_mfma_f32_16x16x32_bf16 v[148:151], v[124:127], v[12:15], v[148:151]\n" \
    "v_mfma_f32_16x16x32_bf16 v[156:159], v[124:127], v[20:23], v[156:159]\n" \
    "v_mfma_f32_16x16x32_bf16 v[164:167], v[124:127], v[28:31], v[164:167]\n" \
    "s_waitcnt lgkmcnt(2)\n" \
    "v_mfma_f32_16x16x32_bf16 v[140:143], v[128:131], v[32:35], v[140:143]\n" \
    "v_mfma_f32_16x16x32_bf16 v[148:151], v[128:131], v[40:43], v[148:151]\n" \
    "v_mfma_f32_16x16x32_bf16 v[156:159], v[128:131], v[48:51], v[156:159]\n" \
    "v_mfma_f32_16x16x32_bf16 v[164:167], v[128:131], v[56:59], v[164:167]\n" \
    "s_waitcnt lgkmcnt(0)\n" \
    "v_mfma_f32_16x16x32_bf16 v[140:143], v[132:135], v[36:39], v[140:143]\n" \
    "v_mfma_f32_16x16x32_bf16 v[148:151], v[132:135], v[44:47], v[148:151]\n" \
    "v_mfma_f32_16x16x32_bf16 v[156:159], v[132:135], v[52:55], v[156:159]\n" \
    "v_mfma_f32_16x16x32_bf16 v[164:167], v[132:135], v[60:63], v[164:167]\n" \
    "v_cvt_pk_bf16_f32 v65, v136, v137\n" \
    "v_cvt_pk_bf16_f32 v68, v138, v139\n" \
    "v_cvt_pk_bf16_f32 v73, v144, v145\n" \
    "v_cvt_pk_bf16_f32 v76, v146, v147\n" \
    "v_cvt_pk_bf16_f32 v81, v152, v153\n" \
    "v_cvt_pk_bf16_f32 v84, v154, v155\n" \
    "v_cvt_pk_bf16_f32 v89, v160, v161\n" \
    "v_cvt_pk_bf16_f32 v92, v162, v163\n" \
    "v_cvt_pk_bf16_f32 v66, v142, v143\n" \
    "v_cvt_pk_bf16_f32 v67, v142, v143\n" \
    "v_cvt_pk_bf16_f32 v69, v140, v141\n" \
    "v_cvt_pk_bf16_f32 v74, v150, v151\n" \
    "v_cvt_pk_bf16_f32 v75, v150, v151\n" \
    "v_cvt_pk_bf16_f32 v77, v148, v149\n" \
    "v_cvt_pk_bf16_f32 v82, v158, v159\n" \
    "v_cvt_pk_bf16_f32 v83, v158, v159\n" \
    "v_cvt_pk_bf16_f32 v85, v156, v157\n" \
    "v_cvt_pk_bf16_f32 v90, v166, v167\n" \
    "v_cvt_pk_bf16_f32 v91, v166, v167\n" \
    "v_cvt_pk_bf16_f32 v93, v164, v165\n" \
    "v_permlane16_swap_b32 v66, v67\n" \
    "v_permlane16_swap_b32 v74, v75\n" \
    "v_permlane16_swap_b32 v82, v83\n" \
    "v_permlane16_swap_b32 v90, v91\n" \
    "ds_read_b128 v[240:243], v234 offset:25600\n" \
    "ds_read_b128 v[244:247], v234 offset:26624\n" \
    "v_mov_b64 v[176:177], 0\n" \
    "v_mov_b64 v[178:179], 0\n" \
    "v_mov_b64 v[180:181], 0\n" \
    "v_mov_b64 v[182:183], 0\n" \
    "v_mov_b64 v[184:185], 0\n" \
    "v_mov_b64 v[186:187], 0\n" \
    "v_mov_b64 v[188:189], 0\n" \
    "v_mov_b64 v[190:191], 0\n" \
    "v_mov_b64 v[196:197], 0\n" \
    "v_mov_b64 v[198:199], 0\n" \
    "v_mov_b64 v[200:201], 0\n" \
    "v_mov_b64 v[202:203], 0\n" \
    "v_mov_b64 v[204:205], 0\n" \
    "v_mov_b64 v[206:207], 0\n" \
    "v_mov_b64 v[208:209], 0\n" \
    "v_mov_b64 v[210:211], 0\n" \
    "v_mov_b64 v[212:213], 0\n" \
    "v_mov_b64 v[214:215], 0\n" \
    "s_add_u32 s85, s60, 0x1000\n" \
    "s_waitcnt vmcnt(0) lgkmcnt(0)\n" \
    "s_barrier\n" \
    "ds_read_b128 v[104:107], v230 offset:0\n" \
    "ds_read_b128 v[108:111], v230 offset:2176\n" \
    "ds_read_b128 v[112:115], v230 offset:4352\n" \
    "ds_read_b128 v[116:119], v230 offset:6528\n" \
    "ds_read_b128 v[120:123], v230 offset:1088\n" \
    "ds_read_b128 v[124:127], v230 offset:3264\n" \
    "ds_read_b128 v[128:131], v230 offset:5440\n" \
    "ds_read_b128 v[132:135], v230 offset:7616\n" \
    "s_waitcnt lgkmcnt(6)\n" \
    "v_pk_add_f32 v[104:105], v[104:105], v[108:109]\n" \
    "v_pk_add_f32 v[106:107], v[106:107], v[110:111]\n" \
    "s_waitcnt lgkmcnt(4)\n" \
    "v_pk_add_f32 v[112:113], v[112:113], v[116:117]\n" \
    "v_pk_add_f32 v[114:115], v[114:115], v[118:119]\n" \
    "v_pk_add_f32 v[104:105], v[104:105], v[112:113]\n" \
    "v_pk_add_f32 v[106:107], v[106:107], v[114:115]\n" \
    "v_cvt_pk_bf16_f32 v136, v104, v105\n" \
    "v_cvt_pk_bf16_f32 v137, v106, v107\n" \
    "s_waitcnt lgkmcnt(2)\n" \
    "v_pk_add_f32 v[120:121], v[120:121], v[124:125]\n" \
    "v_pk_add_f32 v[122:123], v[122:123], v[126:127]\n" \
    "s_waitcnt lgkmcnt(0)\n" \
    "v_pk_add_f32 v[128:129], v[128:129], v[132:133]\n" \
    "v_pk_add_f32 v[130:131], v[130:131], v[134:135]\n" \
    "v_pk_add_f32 v[120:121], v[120:121], v[128:129]\n" \
    "v_pk_add_f32 v[122:123], v[122:123], v[130:131]\n" \
    "v_cvt_pk_bf16_f32 v138, v120, v121\n" \
    "v_cvt_pk_bf16_f32 v139, v122, v123\n" \
    "s_nop 1\n" \
    "v_mfma_f32_16x16x32_bf16 v[140:143], v[240:243], v[136:139], 0\n" \
    "v_mfma_f32_16x16x32_bf16 v[144:147], v[244:247], v[136:139], 0\n" \
    "v_mfma_f32_16x16x32_bf16 v[148:151], v[136:139], v[136:139], 0\n" \
    "buffer_store_dwordx4 v[140:143], v231, s[40:43], s60 offen sc1\n" \
    "s_nop 2\n" \
    "buffer_store_dwordx4 v[144:147], v231, s[40:43], s60 offen offset:512 sc1\n" \
    "s_nop 2\n" \
    "buffer_store_dwordx4 v[148:151], v232, s[40:43], s60 offen sc1\n" \
    "s_cmp_eq_u32 s76, 63\n" \
    "s_cbranch_scc1 L%=_Sfin\n" \
    "ds_read_b32 v216, v229\n" \
    "s_waitcnt lgkmcnt(0)\n" \
    "v_readfirstlane_b32 s79, v216\n" \
    "s_cmp_gt_u32 s79, s76\n" \
    "s_cbranch_scc1 L%=_Su1s\n" \
    "L%=_Su1k:\n" \
    "s_memrealtime vcc\n" \
    "s_add_u32 s80, s77, s64\n" \
    "s_add_u32 s80, s80, s67\n" \
    "s_waitcnt lgkmcnt(0)\n" \
    "s_sub_i32 s79, s80, vcc_lo\n" \
    "s_min_i32 s79, s79, 0x400\n" \
    "s_add_i32 s80, vcc_lo, s79\n" \
    "s_cmp_lt_i32 s79, 5\n" \
    "s_cbranch_scc1 L%=_Su2c\n" \
    "L%=_Su2a:\n" \
    "s_cmp_lt_i32 s79, 14\n" \
    "s_cbranch_scc1 L%=_Su2b\n" \
    "s_sleep 2\n" \
    "L%=_Su2b:\n" \
    "s_memrealtime vcc\n" \
    "s_waitcnt lgkmcnt(0)\n" \
    "s_sub_i32 s79, s80, vcc_lo\n" \
    "s_cmp_gt_i32 s79, 4\n" \
    "s_cbranch_scc1 L%=_Su2a\n" \
    "L%=_Su2c:\n" \
    "s_sub_i32 s79, s79, 2\n" \
    "L%=_Su2d:\n" \
    "s_sub_i32 s79, s79, 1\n" \
    "s_cmp_ge_i32 s79, 0\n" \
    "s_cbranch_scc1 L%=_Su2d\n" \
    "buffer_load_dwordx4 v[168:171], v225, s[40:43], s60 offen sc1\n" \
    "buffer_load_dwordx4 v[172:175], v225, s[40:43], s85 offen sc1\n" \
    "buffer_store_dwordx4 v[136:139], v233, s[48:51], s84 offen sc1\n" \
    "L%=_Svc:\n" \
    "s_waitcnt vmcnt(2)\n" \
    "v_smfmac_f32_16x16x64_bf16 v[176:179], v[168:171], v[0:7], v224\n" \
    "v_smfmac_f32_16x16x64_bf16 v[180:183], v[168:171], v[8:15], v224\n" \
    "s_waitcnt vmcnt(1)\n" \
    "v_smfmac_f32_16x16x64_bf16 v[176:179], v[172:175], v[32:39], v224\n" \
    "v_smfmac_f32_16x16x64_bf16 v[180:183], v[172:175], v[40:47], v224\n" \
    "v_smfmac_f32_16x16x64_bf16 v[184:187], v[168:171], v[16:23], v224\n" \
    "v_smfmac_f32_16x16x64_bf16 v[188:191], v[168:171], v[24:31], v224\n" \
    "v_add_f32 v176, v176, v178\n" \
    "v_smfmac_f32_16x16x64_bf16 v[184:187], v[172:175], v[48:55], v224\n" \
    "v_add_f32 v177, v177, v179\n" \
    "v_smfmac_f32_16x16x64_bf16 v[188:191], v[172:175], v[56:63], v224\n" \
    "v_cmp_u_f32 vcc, v176, v177\n" \
    "v_cvt_pk_bf16_f32 v64, v176, v177\n" \
    "v_add_f32 v180, v180, v182\n" \
    "v_add_f32 v181, v181, v183\n" \
    "v_cvt_pk_bf16_f32 v72, v180, v181\n" \
    "v_add_f32 v184, v184, v186\n" \
    "v_add_f32 v185, v185, v187\n" \
    "v_cvt_pk_bf16_f32 v80, v184, v185\n" \
    "v_add_f32 v188, v188, v190\n" \
    "v_add_f32 v189, v189, v191\n" \
    "v_cvt_pk_bf16_f32 v88, v188, v189\n" \
    "s_cbranch_vccnz L%=_Svslow\n" \
    "L%=_Svok:\n" \
    "s_barrier\n" \
    "ds_read_b96 v[192:194], v227\n" \
    "s_waitcnt lgkmcnt(0)\n" \
    "v_smfmac_f32_16x16x64_bf16 v[212:215], v[192:195], v[96:103], v224\n" \
    "v_smfmac_f32_16x16x64_bf16 v[196:199], v[192:195], v[64:71], v224\n" \
    "v_smfmac_f32_16x16x64_bf16 v[200:203], v[192:195], v[72:79], v224\n" \
    "v_smfmac_f32_16x16x64_bf16 v[204:207], v[192:195], v[80:87], v224\n" \
    "v_add_f32 v212, v212, v214\n" \
    "v_smfmac_f32_16x16x64_bf16 v[208:211], v[192:195], v[88:95], v224\n" \
    "v_add_f32 v213, v213, v215\n" \
    "v_rcp_f32 v220, v212\n" \
    "v_rcp_f32 v221, v213\n" \
    "v_add_f32 v196, v196, v198\n" \
    "v_add_f32 v197, v197, v199\n" \
    "v_pk_add_f32 v[200:201], v[200:201], v[202:203]\n" \
    "v_pk_add_f32 v[204:205], v[204:205], v[206:207]\n" \
    "s_nop 0\n" \
    "v_pk_add_f32 v[208:209], v[208:209], v[210:211]\n" \
    "v_pk_mul_f32 v[196:197], v[196:197], v[220:221]\n" \
    "v_pk_mul_f32 v[200:201], v[200:201], v[220:221]\n" \
    "v_pk_mul_f32 v[204:205], v[204:205], v[220:221]\n" \
    "v_pk_mul_f32 v[208:209], v[208:209], v[220:221]\n" \
    "v_cvt_pk_bf16_f32 v216, v196, v200\n" \
    "v_cvt_pk_bf16_f32 v217, v204, v208\n" \
    "v_cvt_pk_bf16_f32 v218, v197, v201\n" \
    "v_cvt_pk_bf16_f32 v219, v205, v209\n" \
    "buffer_store_dwordx4 v[216:219], v226, s[40:43], s60 offen sc1\n" \
    "s_mov_b32 s79, s60\n" \
    "s_mov_b32 s60, s61\n" \
    "s_mov_b32 s61, s62\n" \
    "s_mov_b32 s62, s63\n" \
    "s_mov_b32 s63, s79\n" \
    "s_add_u32 s76, s76, 1\n" \
    "s_add_u32 s77, s77, s65\n" \
    "s_add_u32 s84, s84, 0x20000\n" \
    "s_add_u32 s81, s81, s65\n" \
    "s_branch L%=_Sloop\n" \
    "L%=_Sfin:\n" \
    "buffer_store_dwordx4 v[136:139], v233, s[48:51], s84 offen sc1\n" \
    "s_waitcnt vmcnt(0)\n" \
    "s_barrier\n" \
    "s_branch L%=_end\n" \
    "L%=_Su1s:\n" \
    "buffer_load_dword v216, off, s[44:47], s78 sc1\n" \
    "s_waitcnt vmcnt(0)\n" \
    "v_readfirstlane_b32 s79, v216\n" \
    "s_lshr_b32 s80, s79, 24\n" \
    "s_cmp_gt_u32 s80, s76\n" \
    "s_cbranch_scc1 L%=_Su1h\n" \
    "s_cmp_eq_u32 s99, 0\n" \
    "s_cbranch_scc1 L%=_Su1k\n" \
    "s_sub_u32 s99, s99, 1\n" \
    "s_sleep 2\n" \
    "s_branch L%=_Su1s\n" \
    "L%=_Su1h:\n" \
    "s_and_b32 s79, s79, 0xffffff\n" \
    "s_add_u32 s79, s79, s81\n" \
    "s_max_u32 s77, s77, s79\n" \
    "s_branch L%=_Su1k\n" \
    "L%=_Svslow:\n" \
    "s_nop 7\n" \
    "s_nop 0\n" \
    "v_max_u32 v216, v168, v169\n" \
    "v_max3_u32 v216, v216, v170, v171\n" \
    "v_max3_u32 v216, v216, v172, v173\n" \
    "v_max3_u32 v216, v216, v174, v175\n" \
    "v_cmp_eq_u32 vcc, -1, v216\n" \
    "s_cbranch_vccz L%=_Svok\n" \
    "s_cmp_eq_u32 s99, 0\n" \
    "s_cbranch_scc1 L%=_Svok\n" \
    "s_sub_u32 s99, s99, 1\n" \
    "s_cmp_lg_u32 s99, 0\n" \
    "s_cbranch_scc1 L%=_Su3\n" \
    "v_mov_b32 v216, 1\n" \
    "buffer_store_dword v216, off, s[44:47], 0 sc1\n" \
    "L%=_Su3:\n" \
    "s_add_u32 s79, s76, 2\n" \
    "v_mov_b32 v216, s79\n" \
    "ds_write_b32 v229, v216\n" \
    "s_sleep 1\n" \
    "buffer_load_dwordx4 v[168:171], v225, s[40:43], s60 offen sc1\n" \
    "buffer_load_dwordx4 v[172:175], v225, s[40:43], s85 offen sc1\n" \
    "v_mov_b64 v[176:177], 0\n" \
    "v_mov_b64 v[178:179], 0\n" \
    "v_mov_b64 v[180:181], 0\n" \
    "v_mov_b64 v[182:183], 0\n" \
    "v_mov_b64 v[184:185], 0\n" \
    "v_mov_b64 v[186:187], 0\n" \
    "v_mov_b64 v[188:189], 0\n" \
    "v_mov_b64 v[190:191], 0\n" \
    "s_waitcnt vmcnt(0)\n" \
    "s_branch L%=_Svc\n" \
    "L%=_R:\n" \
    "s_mov_b32 s76, 0\n" \
    "s_mov_b32 s77, 0\n" \
    "s_mov_b32 s99, 0x20000\n" \
    "s_mov_b32 s84, 0\n" \
    "s_mov_b32 s81, 0\n" \
    "s_andn2_b32 s78, s78, 3\n" \
    "s_mov_b32 s86, 0\n" \
    "s_mov_b32 s87, 0\n" \
    "s_mov_b32 s88, 0\n" \
    "s_mov_b32 s89, 0\n" \
    "s_mov_b32 s90, 0\n" \
    "s_mov_b32 s91, 0\n" \
    "s_mov_b32 s92, 0\n" \
    "s_mov_b32 s93, 0\n" \
    "s_mov_b32 s94, 0\n" \
    "s_mov_b32 s95, 0\n" \
    "s_mov_b32 s96, 0\n" \
    "s_mov_b32 s97, 0\n" \
    "s_mov_b32 s98, 0\n" \
    "v_mov_b32 v96, 0x3f803f80\n" \
    "v_mov_b32 v97, 0x3f803f80\n" \
    "v_mov_b32 v98, 0x3f803f80\n" \
    "v_mov_b32 v99, 0x3f803f80\n" \
    "v_mov_b32 v100, 0x3f803f80\n" \
    "v_mov_b32 v101, 0x3f803f80\n" \
    "v_mov_b64 v[102:103], 0\n" \
    "v_mov_b64 v[64:65], 0\n" \
    "v_mov_b64 v[66:67], 0\n" \
    "v_mov_b64 v[68:69], 0\n" \
    "v_mov_b64 v[70:71], 0\n" \
    "v_mov_b64 v[72:73], 0\n" \
    "v_mov_b64 v[74:75], 0\n" \
    "v_mov_b64 v[76:77], 0\n" \
    "v_mov_b64 v[78:79], 0\n" \
    "v_mov_b64 v[80:81], 0\n" \
    "v_mov_b64 v[82:83], 0\n" \
    "v_mov_b64 v[84:85], 0\n" \
    "v_mov_b64 v[86:87], 0\n" \
    "v_mov_b64 v[88:89], 0\n" \
    "v_mov_b64 v[90:91], 0\n" \
    "v_mov_b64 v[92:93], 0\n" \
    "v_mov_b64 v[94:95], 0\n" \
    "v_mov_b64 v[192:193], 0\n" \
    "v_mov_b64 v[194:195], 0\n" \
    "ds_read_b64_tr_b16 v[104:105], v228 offset:16384\n" \
    "ds_read_b64_tr_b16 v[106:107], v228 offset:16416\n" \
    "ds_read_b64_tr_b16 v[108:109], v228 offset:18688\n" \
    "ds_read_b64_tr_b16 v[110:111], v228 offset:18720\n" \
    "ds_read_b64_tr_b16 v[112:113], v228 offset:20992\n" \
    "ds_read_b64_tr_b16 v[114:115], v228 offset:21024\n" \
    "ds_read_b64_tr_b16 v[116:117], v228 offset:23296\n" \
    "ds_read_b64_tr_b16 v[118:119], v228 offset:23328\n" \
    "ds_read_b64_tr_b16 v[120:121], v228 offset:16656\n" \
    "ds_read_b64_tr_b16 v[122:123], v228 offset:16688\n" \
    "ds_read_b64_tr_b16 v[124:125], v228 offset:18960\n" \
    "ds_read_b64_tr_b16 v[126:127], v228 offset:18992\n" \
    "ds_read_b64_tr_b16 v[128:129], v228 offset:21264\n" \
    "ds_read_b64_tr_b16 v[130:131], v228 offset:21296\n" \
    "ds_read_b64_tr_b16 v[132:133], v228 offset:23568\n" \
    "ds_read_b64_tr_b16 v[134:135], v228 offset:23600\n" \
    "s_waitcnt lgkmcnt(14)\n" \
    "v_mfma_f32_16x16x32_bf16 v[136:139], v[104:107], v[0:3], 0\n" \
    "v_mfma_f32_16x16x32_bf16 v[144:147], v[104:107], v[8:11], 0\n" \
    "v_mfma_f32_16x16x32_bf16 v[152:155], v[104:107], v[16:19], 0\n" \
    "v_mfma_f32_16x16x32_bf16 v[160:163], v[104:107], v[24:27], 0\n" \
    "s_waitcnt lgkmcnt(12)\n" \
    "v_mfma_f32_16x16x32_bf16 v[136:139], v[108:111], v[4:7], v[136:139]\n" \
    "v_mfma_f32_16x16x32_bf16 v[144:147], v[108:111], v[12:15], v[144:147]\n" \
    "v_mfma_f32_16x16x32_bf16 v[152:155], v[108:111], v[20:23], v[152:155]\n" \
    "v_mfma_f32_16x16x32_bf16 v[160:163], v[108:111], v[28:31], v[160:163]\n" \
    "s_waitcnt lgkmcnt(10)\n" \
    "v_mfma_f32_16x16x32_bf16 v[136:139], v[112:115], v[32:35], v[136:139]\n" \
    "v_mfma_f32_16x16x32_bf16 v[144:147], v[112:115], v[40:43], v[144:147]\n" \
    "v_mfma_f32_16x16x32_bf16 v[152:155], v[112:115], v[48:51], v[152:155]\n" \
    "v_mfma_f32_16x16x32_bf16 v[160:163], v[112:115], v[56:59], v[160:163]\n" \
    "s_waitcnt lgkmcnt(8)\n" \
    "v_mfma_f32_16x16x32_bf16 v[136:139], v[116:119], v[36:39], v[136:139]\n" \
    "v_mfma_f32_16x16x32_bf16 v[144:147], v[116:119], v[44:47], v[144:147]\n" \
    "v_mfma_f32_16x16x32_bf16 v[152:155], v[116:119], v[52:55], v[152:155]\n" \
    "v_mfma_f32_16x16x32_bf16 v[160:163], v[116:119], v[60:63], v[160:163]\n" \
    "s_waitcnt lgkmcnt(6)\n" \
    "v_mfma_f32_16x16x32_bf16 v[140:143], v[120:123], v[0:3], 0\n" \
    "v_mfma_f32_16x16x32_bf16 v[148:151], v[120:123], v[8:11], 0\n" \
    "v_mfma_f32_16x16x32_bf16 v[156:159], v[120:123], v[16:19], 0\n" \
    "v_mfma_f32_16x16x32_bf16 v[164:167], v[120:123], v[24:27], 0\n" \
    "s_waitcnt lgkmcnt(4)\n" \
    "v_mfma_f32_16x16x32_bf16 v[140:143], v[124:127], v[4:7], v[140:143]\n" \
    "v_mfma_f32_16x16x32_bf16 v[148:151], v[124:127], v[12:15], v[148:151]\n" \
    "v_mfma_f32_16x16x32_bf16 v[156:159], v[124:127], v[20:23], v[156:159]\n" \
    "v_mfma_f32_16x16x32_bf16 v[164:167], v[124:127], v[28:31], v[164:167]\n" \
    "s_waitcnt lgkmcnt(2)\n" \
    "v_mfma_f32_16x16x32_bf16 v[140:143], v[128:131], v[32:35], v[140:143]\n" \
    "v_mfma_f32_16x16x32_bf16 v[148:151], v[128:131], v[40:43], v[148:151]\n" \
    "v_mfma_f32_16x16x32_bf16 v[156:159], v[128:131], v[48:51], v[156:159]\n" \
    "v_mfma_f32_16x16x32_bf16 v[164:167], v[128:131], v[56:59], v[164:167]\n" \
    "s_waitcnt lgkmcnt(0)\n" \
    "v_mfma_f32_16x16x32_bf16 v[140:143], v[132:135], v[36:39], v[140:143]\n" \
    "v_mfma_f32_16x16x32_bf16 v[148:151], v[132:135], v[44:47], v[148:151]\n" \
    "v_mfma_f32_16x16x32_bf16 v[156:159], v[132:135], v[52:55], v[156:159]\n" \
    "v_mfma_f32_16x16x32_bf16 v[164:167], v[132:135], v[60:63], v[164:167]\n" \
    "v_cvt_pk_bf16_f32 v65, v136, v137\n" \
    "v_cvt_pk_bf16_f32 v68, v138, v139\n" \
    "v_cvt_pk_bf16_f32 v73, v144, v145\n" \
    "v_cvt_pk_bf16_f32 v76, v146, v147\n" \
    "v_cvt_pk_bf16_f32 v81, v152, v153\n" \
    "v_cvt_pk_bf16_f32 v84, v154, v155\n" \
    "v_cvt_pk_bf16_f32 v89, v160, v161\n" \
    "v_cvt_pk_bf16_f32 v92, v162, v163\n" \
    "v_cvt_pk_bf16_f32 v66, v142, v143\n" \
    "v_cvt_pk_bf16_f32 v67, v142, v143\n" \
    "v_cvt_pk_bf16_f32 v69, v140, v141\n" \
    "v_cvt_pk_bf16_f32 v74, v150, v151\n" \
    "v_cvt_pk_bf16_f32 v75, v150, v151\n" \
    "v_cvt_pk_bf16_f32 v77, v148, v149\n" \
    "v_cvt_pk_bf16_f32 v82, v158, v159\n" \
    "v_cvt_pk_bf16_f32 v83, v158, v159\n" \
    "v_cvt_pk_bf16_f32 v85, v156, v157\n" \
    "v_cvt_pk_bf16_f32 v90, v166, v167\n" \
    "v_cvt_pk_bf16_f32 v91, v166, v167\n" \
    "v_cvt_pk_bf16_f32 v93, v164, v165\n" \
    "v_permlane16_swap_b32 v66, v67\n" \
    "v_permlane16_swap_b32 v74, v75\n" \
    "v_permlane16_swap_b32 v82, v83\n" \
    "v_permlane16_swap_b32 v90, v91\n" \
    "L%=_Rloop:\n" \
    "v_mov_b64 v[176:177], 0\n" \
    "v_mov_b64 v[178:179], 0\n" \
    "v_mov_b64 v[180:181], 0\n" \
    "v_mov_b64 v[182:183], 0\n" \
    "v_mov_b64 v[184:185], 0\n" \
    "v_mov_b64 v[186:187], 0\n" \
    "v_mov_b64 v[188:189], 0\n" \
    "v_mov_b64 v[190:191], 0\n" \
    "v_mov_b64 v[196:197], 0\n" \
    "v_mov_b64 v[198:199], 0\n" \
    "v_mov_b64 v[200:201], 0\n" \
    "v_mov_b64 v[202:203], 0\n" \
    "v_mov_b64 v[204:205], 0\n" \
    "v_mov_b64 v[206:207], 0\n" \
    "v_mov_b64 v[208:209], 0\n" \
    "v_mov_b64 v[210:211], 0\n" \
    "v_mov_b64 v[212:213], 0\n" \
    "v_mov_b64 v[214:215], 0\n" \
    "s_add_u32 s85, s60, 0x1000\n" \
    "s_waitcnt vmcnt(0)\n" \
    "s_barrier\n" \
    "s_nop 7\n" \
    "s_nop 7\n" \
    "s_nop 7\n" \
    "L%=_Rvs:\n" \
    "ds_read_b128 v[104:107], v229 offset:16\n" \
    "ds_read_b128 v[108:111], v230 offset:8704\n" \
    "ds_read_b128 v[112:115], v230 offset:9984\n" \
    "ds_read_b128 v[116:119], v230 offset:11264\n" \
    "ds_read_b128 v[120:123], v230 offset:12544\n" \
    "s_waitcnt lgkmcnt(4)\n" \
    "v_min3_u32 v104, v104, v105, v106\n" \
    "v_min_u32 v104, v104, v107\n" \
    "s_nop 0\n" \
    "v_readfirstlane_b32 s79, v104\n" \
    "s_cmp_le_u32 s79, s76\n" \
    "s_cbranch_scc1 L%=_Rvs\n" \
    "s_waitcnt lgkmcnt(2)\n" \
    "v_pk_add_f32 v[108:109], v[108:109], v[112:113]\n" \
    "v_pk_add_f32 v[110:111], v[110:111], v[114:115]\n" \
    "s_waitcnt lgkmcnt(0)\n" \
    "v_pk_add_f32 v[116:117], v[116:117], v[120:121]\n" \
    "v_pk_add_f32 v[118:119], v[118:119], v[122:123]\n" \
    "v_pk_add_f32 v[108:109], v[108:109], v[116:117]\n" \
    "v_pk_add_f32 v[110:111], v[110:111], v[118:119]\n" \
    "v_cvt_pk_bf16_f32 v124, v108, v109\n" \
    "v_cvt_pk_bf16_f32 v125, v110, v111\n" \
    "s_nop 0\n" \
    "v_mov_b32_dpp v126, v124 quad_perm:[1,0,3,2] row_mask:0xf bank_mask:0xf\n" \
    "v_mov_b32_dpp v127, v125 quad_perm:[1,0,3,2] row_mask:0xf bank_mask:0xf\n" \
    "buffer_store_dwordx4 v[124:127], v231, s[40:43], s60 offen sc1\n" \
    "ds_write_b64 v232, v[124:125]\n" \
    "ds_read_b64_tr_b16 v[128:129], v233\n" \
    "ds_read_b64_tr_b16 v[130:131], v233 offset:256\n" \
    "s_cmp_eq_u32 s76, 63\n" \
    "s_cbranch_scc1 L%=_Rfin\n" \
    "ds_read_b32 v216, v229\n" \
    "s_waitcnt lgkmcnt(0)\n" \
    "v_readfirstlane_b32 s79, v216\n" \
    "s_cmp_gt_u32 s79, s76\n" \
    "s_cbranch_scc1 L%=_Ru1s\n" \
    "L%=_Ru1k:\n" \
    "s_memrealtime vcc\n" \
    "s_add_u32 s80, s77, s64\n" \
    "s_add_u32 s80, s80, s67\n" \
    "s_waitcnt lgkmcnt(0)\n" \
    "s_sub_i32 s79, s80, vcc_lo\n" \
    "s_min_i32 s79, s79, 0x400\n" \
    "s_add_i32 s80, vcc_lo, s79\n" \
    "s_cmp_lt_i32 s79, 5\n" \
    "s_cbranch_scc1 L%=_Ru2c\n" \
    "L%=_Ru2a:\n" \
    "s_cmp_lt_i32 s79, 14\n" \
    "s_cbranch_scc1 L%=_Ru2b\n" \
    "s_sleep 2\n" \
    "L%=_Ru2b:\n" \
    "s_memrealtime vcc\n" \
    "s_waitcnt lgkmcnt(0)\n" \
    "s_sub_i32 s79, s80, vcc_lo\n" \
    "s_cmp_gt_i32 s79, 4\n" \
    "s_cbranch_scc1 L%=_Ru2a\n" \
    "L%=_Ru2c:\n" \
    "s_sub_i32 s79, s79, 2\n" \
    "L%=_Ru2d:\n" \
    "s_sub_i32 s79, s79, 1\n" \
    "s_cmp_ge_i32 s79, 0\n" \
    "s_cbranch_scc1 L%=_Ru2d\n" \
    "buffer_load_dwordx4 v[168:171], v225, s[40:43], s60 offen sc1\n" \
    "buffer_load_dwordx4 v[172:175], v225, s[40:43], s85 offen sc1\n" \
    "buffer_store_dwordx4 v[128:131], v234, s[48:51], s84 offen sc1\n" \
    "L%=_Rvc:\n" \
    "s_waitcnt vmcnt(2)\n" \
    "v_smfmac_f32_16x16x64_bf16 v[176:179], v[168:171], v[0:7], v224\n" \
    "v_smfmac_f32_16x16x64_bf16 v[180:183], v[168:171], v[8:15], v224\n" \
    "s_waitcnt vmcnt(1)\n" \
    "v_smfmac_f32_16x16x64_bf16 v[176:179], v[172:175], v[32:39], v224\n" \
    "v_smfmac_f32_16x16x64_bf16 v[180:183], v[172:175], v[40:47], v224\n" \
    "v_smfmac_f32_16x16x64_bf16 v[184:187], v[168:171], v[16:23], v224\n" \
    "v_smfmac_f32_16x16x64_bf16 v[188:191], v[168:171], v[24:31], v224\n" \
    "v_add_f32 v176, v176, v178\n" \
    "v_smfmac_f32_16x16x64_bf16 v[184:187], v[172:175], v[48:55], v224\n" \
    "v_add_f32 v177, v177, v179\n" \
    "v_smfmac_f32_16x16x64_bf16 v[188:191], v[172:175], v[56:63], v224\n" \
    "v_cmp_u_f32 vcc, v176, v177\n" \
    "v_cvt_pk_bf16_f32 v64, v176, v177\n" \
    "v_add_f32 v180, v180, v182\n" \
    "v_add_f32 v181, v181, v183\n" \
    "v_cvt_pk_bf16_f32 v72, v180, v181\n" \
    "v_add_f32 v184, v184, v186\n" \
    "v_add_f32 v185, v185, v187\n" \
    "v_cvt_pk_bf16_f32 v80, v184, v185\n" \
    "v_add_f32 v188, v188, v190\n" \
    "v_add_f32 v189, v189, v191\n" \
    "v_cvt_pk_bf16_f32 v88, v188, v189\n" \
    "s_cbranch_vccnz L%=_Rvslow\n" \
    "L%=_Rvok:\n" \
    "s_barrier\n" \
    "ds_read_b96 v[192:194], v227\n" \
    "s_waitcnt lgkmcnt(0)\n" \
    "v_smfmac_f32_16x16x64_bf16 v[212:215], v[192:195], v[96:103], v224\n" \
    "v_smfmac_f32_16x16x64_bf16 v[196:199], v[192:195], v[64:71], v224\n" \
    "v_smfmac_f32_16x16x64_bf16 v[200:203], v[192:195], v[72:79], v224\n" \
    "v_smfmac_f32_16x16x64_bf16 v[204:207], v[192:195], v[80:87], v224\n" \
    "v_add_f32 v212, v212, v214\n" \
    "v_smfmac_f32_16x16x64_bf16 v[208:211], v[192:195], v[88:95], v224\n" \
    "v_add_f32 v213, v213, v215\n" \
    "v_rcp_f32 v220, v212\n" \
    "v_rcp_f32 v221, v213\n" \
    "v_add_f32 v196, v196, v198\n" \
    "v_add_f32 v197, v197, v199\n" \
    "v_pk_add_f32 v[200:201], v[200:201], v[202:203]\n" \
    "v_pk_add_f32 v[204:205], v[204:205], v[206:207]\n" \
    "s_nop 0\n" \
    "v_pk_add_f32 v[208:209], v[208:209], v[210:211]\n" \
    "v_pk_mul_f32 v[196:197], v[196:197], v[220:221]\n" \
    "v_pk_mul_f32 v[200:201], v[200:201], v[220:221]\n" \
    "v_pk_mul_f32 v[204:205], v[204:205], v[220:221]\n" \
    "v_pk_mul_f32 v[208:209], v[208:209], v[220:221]\n" \
    "v_cvt_pk_bf16_f32 v216, v196, v200\n" \
    "v_cvt_pk_bf16_f32 v217, v204, v208\n" \
    "v_cvt_pk_bf16_f32 v218, v197, v201\n" \
    "v_cvt_pk_bf16_f32 v219, v205, v209\n" \
    "buffer_store_dwordx4 v[216:219], v226, s[40:43], s60 offen sc1\n" \
    "ds_read_b64_tr_b16 v[104:105], v228 offset:16384\n" \
    "ds_read_b64_tr_b16 v[106:107], v228 offset:16416\n" \
    "ds_read_b64_tr_b16 v[108:109], v228 offset:18688\n" \
    "ds_read_b64_tr_b16 v[110:111], v228 offset:18720\n" \
    "ds_read_b64_tr_b16 v[112:113], v228 offset:20992\n" \
    "ds_read_b64_tr_b16 v[114:115], v228 offset:21024\n" \
    "ds_read_b64_tr_b16 v[116:117], v228 offset:23296\n" \
    "ds_read_b64_tr_b16 v[118:119], v228 offset:23328\n" \
    "ds_read_b64_tr_b16 v[120:121], v228 offset:16656\n" \
    "ds_read_b64_tr_b16 v[122:123], v228 offset:16688\n" \
    "ds_read_b64_tr_b16 v[124:125], v228 offset:18960\n" \
    "ds_read_b64_tr_b16 v[126:127], v228 offset:18992\n" \
    "ds_read_b64_tr_b16 v[128:129], v228 offset:21264\n" \
    "ds_read_b64_tr_b16 v[130:131], v228 offset:21296\n" \
    "ds_read_b64_tr_b16 v[132:133], v228 offset:23568\n" \
    "ds_read_b64_tr_b16 v[134:135], v228 offset:23600\n" \
    "s_waitcnt lgkmcnt(14)\n" \
    "v_mfma_f32_16x16x32_bf16 v[136:139], v[104:107], v[0:3], 0\n" \
    "v_mfma_f32_16x16x32_bf16 v[144:147], v[104:107], v[8:11], 0\n" \
    "v_mfma_f32_16x16x32_bf16 v[152:155], v[104:107], v[16:19], 0\n" \
    "v_mfma_f32_16x16x32_bf16 v[160:163], v[104:107], v[24:27], 0\n" \
    "s_waitcnt lgkmcnt(12)\n" \
    "v_mfma_f32_16x16x32_bf16 v[136:139], v[108:111], v[4:7], v[136:139]\n" \
    "v_mfma_f32_16x16x32_bf16 v[144:147], v[108:111], v[12:15], v[144:147]\n" \
    "v_mfma_f32_16x16x32_bf16 v[152:155], v[108:111], v[20:23], v[152:155]\n" \
    "v_mfma_f32_16x16x32_bf16 v[160:163], v[108:111], v[28:31], v[160:163]\n" \
    "s_waitcnt lgkmcnt(10)\n" \
    "v_mfma_f32_16x16x32_bf16 v[136:139], v[112:115], v[32:35], v[136:139]\n" \
    "v_mfma_f32_16x16x32_bf16 v[144:147], v[112:115], v[40:43], v[144:147]\n" \
    "v_mfma_f32_16x16x32_bf16 v[152:155], v[112:115], v[48:51], v[152:155]\n" \
    "v_mfma_f32_16x16x32_bf16 v[160:163], v[112:115], v[56:59], v[160:163]\n" \
    "s_waitcnt lgkmcnt(8)\n" \
    "v_mfma_f32_16x16x32_bf16 v[136:139], v[116:119], v[36:39], v[136:139]\n" \
    "v_mfma_f32_16x16x32_bf16 v[144:147], v[116:119], v[44:47], v[144:147]\n" \
    "v_mfma_f32_16x16x32_bf16 v[152:155], v[116:119], v[52:55], v[152:155]\n" \
    "v_mfma_f32_16x16x32_bf16 v[160:163], v[116:119], v[60:63], v[160:163]\n" \
    "s_memrealtime vcc\n" \
    "s_add_u32 s80, s77, s64\n" \
    "s_add_u32 s80, s80, s68\n" \
    "s_waitcnt lgkmcnt(0)\n" \
    "s_sub_i32 s79, s80, vcc_lo\n" \
    "s_min_i32 s79, s79, 0x400\n" \
    "s_add_i32 s80, vcc_lo, s79\n" \
    "s_cmp_lt_i32 s79, 5\n" \
    "s_cbranch_scc1 L%=_Ru3c\n" \
    "L%=_Ru3a:\n" \
    "s_cmp_lt_i32 s79, 14\n" \
    "s_cbranch_scc1 L%=_Ru3b\n" \
    "s_sleep 2\n" \
    "L%=_Ru3b:\n" \
    "s_memrealtime vcc\n" \
    "s_waitcnt lgkmcnt(0)\n" \
    "s_sub_i32 s79, s80, vcc_lo\n" \
    "s_cmp_gt_i32 s79, 4\n" \
    "s_cbranch_scc1 L%=_Ru3a\n" \
    "L%=_Ru3c:\n" \
    "s_sub_i32 s79, s79, 2\n" \
    "L%=_Ru3d:\n" \
    "s_sub_i32 s79, s79, 1\n" \
    "s_cmp_ge_i32 s79, 0\n" \
    "s_cbranch_scc1 L%=_Ru3d\n" \
    "buffer_load_dwordx4 v[176:179], v235, s[40:43], s60 offen offset:0 sc1\n" \
    "buffer_load_dwordx4 v[180:183], v235, s[40:43], s60 offen offset:1024 sc1\n" \
    "buffer_load_dwordx4 v[184:187], v235, s[40:43], s60 offen offset:2048 sc1\n" \
    "buffer_load_dwordx4 v[188:191], v235, s[40:43], s60 offen offset:3072 sc1\n" \
    "v_mfma_f32_16x16x32_bf16 v[140:143], v[120:123], v[0:3], 0\n" \
    "v_mfma_f32_16x16x32_bf16 v[148:151], v[120:123], v[8:11], 0\n" \
    "v_mfma_f32_16x16x32_bf16 v[156:159], v[120:123], v[16:19], 0\n" \
    "v_mfma_f32_16x16x32_bf16 v[164:167], v[120:123], v[24:27], 0\n" \
    "v_mfma_f32_16x16x32_bf16 v[140:143], v[124:127], v[4:7], v[140:143]\n" \
    "v_mfma_f32_16x16x32_bf16 v[148:151], v[124:127], v[12:15], v[148:151]\n" \
    "v_mfma_f32_16x16x32_bf16 v[156:159], v[124:127], v[20:23], v[156:159]\n" \
    "v_mfma_f32_16x16x32_bf16 v[164:167], v[124:127], v[28:31], v[164:167]\n" \
    "v_mfma_f32_16x16x32_bf16 v[140:143], v[128:131], v[32:35], v[140:143]\n" \
    "v_mfma_f32_16x16x32_bf16 v[148:151], v[128:131], v[40:43], v[148:151]\n" \
    "v_mfma_f32_16x16x32_bf16 v[156:159], v[128:131], v[48:51], v[156:159]\n" \
    "v_mfma_f32_16x16x32_bf16 v[164:167], v[128:131], v[56:59], v[164:167]\n" \
    "v_mfma_f32_16x16x32_bf16 v[140:143], v[132:135], v[36:39], v[140:143]\n" \
    "v_mfma_f32_16x16x32_bf16 v[148:151], v[132:135], v[44:47], v[148:151]\n" \
    "v_mfma_f32_16x16x32_bf16 v[156:159], v[132:135], v[52:55], v[156:159]\n" \
    "v_mfma_f32_16x16x32_bf16 v[164:167], v[132:135], v[60:63], v[164:167]\n" \
    "v_cvt_pk_bf16_f32 v65, v136, v137\n" \
    "v_cvt_pk_bf16_f32 v68, v138, v139\n" \
    "v_cvt_pk_bf16_f32 v73, v144, v145\n" \
    "v_cvt_pk_bf16_f32 v76, v146, v147\n" \
    "v_cvt_pk_bf16_f32 v81, v152, v153\n" \
    "v_cvt_pk_bf16_f32 v84, v154, v155\n" \
    "v_cvt_pk_bf16_f32 v89, v160, v161\n" \
    "v_cvt_pk_bf16_f32 v92, v162, v163\n" \
    "L%=_Rred:\n" \
    "s_waitcnt vmcnt(3)\n" \
    "v_mfma_f32_16x16x32_bf16 v[240:243], v[244:247], v[176:179], 0\n" \
    "s_waitcnt vmcnt(2)\n" \
    "v_mfma_f32_16x16x32_bf16 v[240:243], v[244:247], v[180:183], v[240:243]\n" \
    "s_waitcnt vmcnt(1)\n" \
    "v_mfma_f32_16x16x32_bf16 v[240:243], v[244:247], v[184:187], v[240:243]\n" \
    "s_waitcnt vmcnt(0)\n" \
    "v_mfma_f32_16x16x32_bf16 v[240:243], v[244:247], v[188:191], v[240:243]\n" \
    "s_nop 7\n" \
    "v_cmp_u_f32 vcc, v240, v240\n" \
    "v_add_f32_dpp v240, v240, v240 row_ror:8 row_mask:0xf bank_mask:0xf\n" \
    "v_add_f32_dpp v241, v241, v241 row_ror:8 row_mask:0xf bank_mask:0xf\n" \
    "v_add_f32_dpp v242, v242, v242 row_ror:8 row_mask:0xf bank_mask:0xf\n" \
    "v_add_f32_dpp v243, v243, v243 row_ror:8 row_mask:0xf bank_mask:0xf\n" \
    "v_cvt_pk_bf16_f32 v216, v240, v241\n" \
    "v_cvt_pk_bf16_f32 v217, v242, v243\n" \
    "s_nop 0\n" \
    "v_mov_b32_dpp v218, v216 quad_perm:[1,0,3,2] row_mask:0xf bank_mask:0xf\n" \
    "v_mov_b32_dpp v219, v217 quad_perm:[1,0,3,2] row_mask:0xf bank_mask:0xf\n" \
    "s_cbranch_vccnz L%=_Rslow\n" \
    "L%=_Rok:\n" \
    "buffer_store_dwordx4 v[216:219], v236, s[40:43], s61 offen sc1\n" \
    "v_cvt_pk_bf16_f32 v66, v142, v143\n" \
    "v_cvt_pk_bf16_f32 v67, v142, v143\n" \
    "v_cvt_pk_bf16_f32 v69, v140, v141\n" \
    "v_cvt_pk_bf16_f32 v74, v150, v151\n" \
    "v_cvt_pk_bf16_f32 v75, v150, v151\n" \
    "v_cvt_pk_bf16_f32 v77, v148, v149\n" \
    "v_cvt_pk_bf16_f32 v82, v158, v159\n" \
    "v_cvt_pk_bf16_f32 v83, v158, v159\n" \
    "v_cvt_pk_bf16_f32 v85, v156, v157\n" \
    "v_cvt_pk_bf16_f32 v90, v166, v167\n" \
    "v_cvt_pk_bf16_f32 v91, v166, v167\n" \
    "v_cvt_pk_bf16_f32 v93, v164, v165\n" \
    "v_permlane16_swap_b32 v66, v67\n" \
    "v_permlane16_swap_b32 v74, v75\n" \
    "v_permlane16_swap_b32 v82, v83\n" \
    "v_permlane16_swap_b32 v90, v91\n" \
    "s_mov_b32 s79, s60\n" \
    "s_mov_b32 s60, s61\n" \
    "s_mov_b32 s61, s62\n" \
    "s_mov_b32 s62, s63\n" \
    "s_mov_b32 s63, s79\n" \
    "s_add_u32 s76, s76, 1\n" \
    "s_add_u32 s77, s77, s65\n" \
    "s_add_u32 s84, s84, 0x20000\n" \
    "s_add_u32 s81, s81, s65\n" \
    "s_branch L%=_Rloop\n" \
    "L%=_Rslow:\n" \
    "s_nop 7\n" \
    "s_nop 0\n" \
    "v_max_u32 v220, v176, v177\n" \
    "v_max3_u32 v220, v220, v178, v179\n" \
    "v_max3_u32 v220, v220, v180, v181\n" \
    "v_max3_u32 v220, v220, v182, v183\n" \
    "v_max3_u32 v220, v220, v184, v185\n" \
    "v_max3_u32 v220, v220, v186, v187\n" \
    "v_max3_u32 v220, v220, v188, v189\n" \
    "v_max3_u32 v220, v220, v190, v191\n" \
    "v_cmp_eq_u32 vcc, -1, v220\n" \
    "s_cbranch_vccz L%=_Rok\n" \
    "s_cmp_eq_u32 s99, 0\n" \
    "s_cbranch_scc1 L%=_Rok\n" \
    "s_sub_u32 s99, s99, 1\n" \
    "s_cmp_lg_u32 s99, 0\n" \
    "s_cbranch_scc1 L%=_Ru4\n" \
    "v_mov_b32 v220, 1\n" \
    "buffer_store_dword v220, off, s[44:47], 0 sc1\n" \
    "L%=_Ru4:\n" \
    "s_add_u32 s79, s76, 2\n" \
    "v_mov_b32 v220, s79\n" \
    "ds_write_b32 v229, v220\n" \
    "s_sleep 1\n" \
    "buffer_load_dwordx4 v[176:179], v235, s[40:43], s60 offen offset:0 sc1\n" \
    "buffer_load_dwordx4 v[180:183], v235, s[40:43], s60 offen offset:1024 sc1\n" \
    "buffer_load_dwordx4 v[184:187], v235, s[40:43], s60 offen offset:2048 sc1\n" \
    "buffer_load_dwordx4 v[188:191], v235, s[40:43], s60 offen offset:3072 sc1\n" \
    "s_branch L%=_Rred\n" \
    "L%=_Rfin:\n" \
    "s_waitcnt lgkmcnt(0)\n" \
    "buffer_store_dwordx4 v[128:131], v234, s[48:51], s84 offen sc1\n" \
    "s_waitcnt vmcnt(0)\n" \
    "s_barrier\n" \
    "s_branch L%=_end\n" \
    "L%=_Ru1s:\n" \
    "buffer_load_dword v216, off, s[44:47], s78 sc1\n" \
    "s_waitcnt vmcnt(0)\n" \
    "v_readfirstlane_b32 s79, v216\n" \
    "s_lshr_b32 s80, s79, 24\n" \
    "s_cmp_gt_u32 s80, s76\n" \
    "s_cbranch_scc1 L%=_Ru1h\n" \
    "s_cmp_eq_u32 s99, 0\n" \
    "s_cbranch_scc1 L%=_Ru1k\n" \
    "s_sub_u32 s99, s99, 1\n" \
    "s_sleep 2\n" \
    "s_branch L%=_Ru1s\n" \
    "L%=_Ru1h:\n" \
    "s_and_b32 s79, s79, 0xffffff\n" \
    "s_add_u32 s79, s79, s81\n" \
    "s_max_u32 s77, s77, s79\n" \
    "s_branch L%=_Ru1k\n" \
    "L%=_Rvslow:\n" \
    "s_nop 7\n" \
    "s_nop 0\n" \
    "v_max_u32 v216, v168, v169\n" \
    "v_max3_u32 v216, v216, v170, v171\n" \
    "v_max3_u32 v216, v216, v172, v173\n" \
    "v_max3_u32 v216, v216, v174, v175\n" \
    "v_cmp_eq_u32 vcc, -1, v216\n" \
    "s_cbranch_vccz L%=_Rvok\n" \
    "s_cmp_eq_u32 s99, 0\n" \
    "s_cbranch_scc1 L%=_Rvok\n" \
    "s_sub_u32 s99, s99, 1\n" \
    "s_cmp_lg_u32 s99, 0\n" \
    "s_cbranch_scc1 L%=_Ru5\n" \
    "v_mov_b32 v216, 1\n" \
    "buffer_store_dword v216, off, s[44:47], 0 sc1\n" \
    "L%=_Ru5:\n" \
    "s_add_u32 s79, s76, 2\n" \
    "v_mov_b32 v216, s79\n" \
    "ds_write_b32 v229, v216\n" \
    "s_sleep 1\n" \
    "buffer_load_dwordx4 v[168:171], v225, s[40:43], s60 offen sc1\n" \
    "buffer_load_dwordx4 v[172:175], v225, s[40:43], s85 offen sc1\n" \
    "v_mov_b64 v[176:177], 0\n" \
    "v_mov_b64 v[178:179], 0\n" \
    "v_mov_b64 v[180:181], 0\n" \
    "v_mov_b64 v[182:183], 0\n" \
    "v_mov_b64 v[184:185], 0\n" \
    "v_mov_b64 v[186:187], 0\n" \
    "v_mov_b64 v[188:189], 0\n" \
    "v_mov_b64 v[190:191], 0\n" \
    "s_waitcnt vmcnt(0)\n" \
    "s_branch L%=_Rvc\n" \
    "L%=_P:\n" \
    "s_mov_b32 s76, 0\n" \
    "s_mov_b32 s77, 0\n" \
    "s_mov_b32 s99, 0x20000\n" \
    "s_mov_b32 s84, 0\n" \
    "s_mov_b32 s81, 0\n" \
    "s_andn2_b32 s78, s78, 3\n" \
    "s_mov_b32 s86, 0\n" \
    "s_mov_b32 s87, 0\n" \
    "s_mov_b32 s88, 0\n" \
    "s_mov_b32 s89, 0\n" \
    "s_mov_b32 s90, 0\n" \
    "s_mov_b32 s91, 0\n" \
    "s_mov_b32 s92, 0\n" \
    "s_mov_b32 s93, 0\n" \
    "s_mov_b32 s94, 0\n" \
    "s_mov_b32 s95, 0\n" \
    "s_mov_b32 s96, 0\n" \
    "s_mov_b32 s97, 0\n" \
    "s_mov_b32 s98, 0\n" \
    "v_mov_b32 v96, 0x3f803f80\n" \
    "v_mov_b32 v97, 0x3f803f80\n" \
    "v_mov_b32 v98, 0x3f803f80\n" \
    "v_mov_b32 v99, 0x3f803f80\n" \
    "v_mov_b32 v100, 0x3f803f80\n" \
    "v_mov_b32 v101, 0x3f803f80\n" \
    "v_mov_b64 v[102:103], 0\n" \
    "v_mov_b64 v[64:65], 0\n" \
    "v_mov_b64 v[66:67], 0\n" \
    "v_mov_b64 v[68:69], 0\n" \
    "v_mov_b64 v[70:71], 0\n" \
    "v_mov_b64 v[72:73], 0\n" \
    "v_mov_b64 v[74:75], 0\n" \
    "v_mov_b64 v[76:77], 0\n" \
    "v_mov_b64 v[78:79], 0\n" \
    "v_mov_b64 v[80:81], 0\n" \
    "v_mov_b64 v[82:83], 0\n" \
    "v_mov_b64 v[84:85], 0\n" \
    "v_mov_b64 v[86:87], 0\n" \
    "v_mov_b64 v[88:89], 0\n" \
    "v_mov_b64 v[90:91], 0\n" \
    "v_mov_b64 v[92:93], 0\n" \
    "v_mov_b64 v[94:95], 0\n" \
    "v_mov_b64 v[192:193], 0\n" \
    "v_mov_b64 v[194:195], 0\n" \
    "v_mov_b32 v237, 1\n" \
    "v_mov_b32 v244, -1\n" \
    "v_mov_b32 v245, -1\n" \
    "v_mov_b32 v246, -1\n" \
    "v_mov_b32 v247, -1\n" \
    "L%=_Ploop:\n" \
    "ds_read_b64_tr_b16 v[104:105], v228 offset:16384\n" \
    "ds_read_b64_tr_b16 v[106:107], v228 offset:16416\n" \
    "ds_read_b64_tr_b16 v[108:109], v228 offset:18688\n" \
    "ds_read_b64_tr_b16 v[110:111], v228 offset:18720\n" \
    "ds_read_b64_tr_b16 v[112:113], v228 offset:20992\n" \
    "ds_read_b64_tr_b16 v[114:115], v228 offset:21024\n" \
    "ds_read_b64_tr_b16 v[116:117], v228 offset:23296\n" \
    "ds_read_b64_tr_b16 v[118:119], v228 offset:23328\n" \
    "ds_read_b64_tr_b16 v[120:121], v228 offset:16656\n" \
    "ds_read_b64_tr_b16 v[122:123], v228 offset:16688\n" \
    "ds_read_b64_tr_b16 v[124:125], v228 offset:18960\n" \
    "ds_read_b64_tr_b16 v[126:127], v228 offset:18992\n" \
    "ds_read_b64_tr_b16 v[128:129], v228 offset:21264\n" \
    "ds_read_b64_tr_b16 v[130:131], v228 offset:21296\n" \
    "ds_read_b64_tr_b16 v[132:133], v228 offset:23568\n" \
    "ds_read_b64_tr_b16 v[134:135], v228 offset:23600\n" \
    "s_waitcnt lgkmcnt(14)\n" \
    "v_mfma_f32_16x16x32_bf16 v[136:139], v[104:107], v[0:3], 0\n" \
    "v_mfma_f32_16x16x32_bf16 v[144:147], v[104:107], v[8:11], 0\n" \
    "v_mfma_f32_16x16x32_bf16 v[152:155], v[104:107], v[16:19], 0\n" \
    "v_mfma_f32_16x16x32_bf16 v[160:163], v[104:107], v[24:27], 0\n" \
    "s_waitcnt lgkmcnt(12)\n" \
    "v_mfma_f32_16x16x32_bf16 v[136:139], v[108:111], v[4:7], v[136:139]\n" \
    "v_mfma_f32_16x16x32_bf16 v[144:147], v[108:111], v[12:15], v[144:147]\n" \
    "v_mfma_f32_16x16x32_bf16 v[152:155], v[108:111], v[20:23], v[152:155]\n" \
    "v_mfma_f32_16x16x32_bf16 v[160:163], v[108:111], v[28:31], v[160:163]\n" \
    "s_waitcnt lgkmcnt(10)\n" \
    "v_mfma_f32_16x16x32_bf16 v[136:139], v[112:115], v[32:35], v[136:139]\n" \
    "v_mfma_f32_16x16x32_bf16 v[144:147], v[112:115], v[40:43], v[144:147]\n" \
    "v_mfma_f32_16x16x32_bf16 v[152:155], v[112:115], v[48:51], v[152:155]\n" \
    "v_mfma_f32_16x16x32_bf16 v[160:163], v[112:115], v[56:59], v[160:163]\n" \
    "s_waitcnt lgkmcnt(8)\n" \
    "v_mfma_f32_16x16x32_bf16 v[136:139], v[116:119], v[36:39], v[136:139]\n" \
    "v_mfma_f32_16x16x32_bf16 v[144:147], v[116:119], v[44:47], v[144:147]\n" \
    "v_mfma_f32_16x16x32_bf16 v[152:155], v[116:119], v[52:55], v[152:155]\n" \
    "v_mfma_f32_16x16x32_bf16 v[160:163], v[116:119], v[60:63], v[160:163]\n" \
    "s_waitcnt lgkmcnt(6)\n" \
    "v_mfma_f32_16x16x32_bf16 v[140:143], v[120:123], v[0:3], 0\n" \
    "v_mfma_f32_16x16x32_bf16 v[148:151], v[120:123], v[8:11], 0\n" \
    "v_mfma_f32_16x16x32_bf16 v[156:159], v[120:123], v[16:19], 0\n" \
    "v_mfma_f32_16x16x32_bf16 v[164:167], v[120:123], v[24:27], 0\n" \
    "s_waitcnt lgkmcnt(4)\n" \
    "v_mfma_f32_16x16x32_bf16 v[140:143], v[124:127], v[4:7], v[140:143]\n" \
    "v_mfma_f32_16x16x32_bf16 v[148:151], v[124:127], v[12:15], v[148:151]\n" \
    "v_mfma_f32_16x16x32_bf16 v[156:159], v[124:127], v[20:23], v[156:159]\n" \
    "v_mfma_f32_16x16x32_bf16 v[164:167], v[124:127], v[28:31], v[164:167]\n" \
    "s_waitcnt lgkmcnt(2)\n" \
    "v_mfma_f32_16x16x32_bf16 v[140:143], v[128:131], v[32:35], v[140:143]\n" \
    "v_mfma_f32_16x16x32_bf16 v[148:151], v[128:131], v[40:43], v[148:151]\n" \
    "v_mfma_f32_16x16x32_bf16 v[156:159], v[128:131], v[48:51], v[156:159]\n" \
    "v_mfma_f32_16x16x32_bf16 v[164:167], v[128:131], v[56:59], v[164:167]\n" \
    "s_waitcnt lgkmcnt(0)\n" \
    "v_mfma_f32_16x16x32_bf16 v[140:143], v[132:135], v[36:39], v[140:143]\n" \
    "v_mfma_f32_16x16x32_bf16 v[148:151], v[132:135], v[44:47], v[148:151]\n" \
    "v_mfma_f32_16x16x32_bf16 v[156:159], v[132:135], v[52:55], v[156:159]\n" \
    "v_mfma_f32_16x16x32_bf16 v[164:167], v[132:135], v[60:63], v[164:167]\n" \
    "v_cvt_pk_bf16_f32 v65, v136, v137\n" \
    "v_cvt_pk_bf16_f32 v68, v138, v139\n" \
    "v_cvt_pk_bf16_f32 v73, v144, v145\n" \
    "v_cvt_pk_bf16_f32 v76, v146, v147\n" \
    "v_cvt_pk_bf16_f32 v81, v152, v153\n" \
    "v_cvt_pk_bf16_f32 v84, v154, v155\n" \
    "v_cvt_pk_bf16_f32 v89, v160, v161\n" \
    "v_cvt_pk_bf16_f32 v92, v162, v163\n" \
    "v_cvt_pk_bf16_f32 v66, v142, v143\n" \
    "v_cvt_pk_bf16_f32 v67, v142, v143\n" \
    "v_cvt_pk_bf16_f32 v69, v140, v141\n" \
    "v_cvt_pk_bf16_f32 v74, v150, v151\n" \
    "v_cvt_pk_bf16_f32 v75, v150, v151\n" \
    "v_cvt_pk_bf16_f32 v77, v148, v149\n" \
    "v_cvt_pk_bf16_f32 v82, v158, v159\n" \
    "v_cvt_pk_bf16_f32 v83, v158, v159\n" \
    "v_cvt_pk_bf16_f32 v85, v156, v157\n" \
    "v_cvt_pk_bf16_f32 v90, v166, v167\n" \
    "v_cvt_pk_bf16_f32 v91, v166, v167\n" \
    "v_cvt_pk_bf16_f32 v93, v164, v165\n" \
    "v_permlane16_swap_b32 v66, v67\n" \
    "v_permlane16_swap_b32 v74, v75\n" \
    "v_permlane16_swap_b32 v82, v83\n" \
    "v_permlane16_swap_b32 v90, v91\n" \
    "v_mov_b64 v[176:177], 0\n" \
    "v_mov_b64 v[178:179], 0\n" \
    "v_mov_b64 v[180:181], 0\n" \
    "v_mov_b64 v[182:183], 0\n" \
    "v_mov_b64 v[184:185], 0\n" \
    "v_mov_b64 v[186:187], 0\n" \
    "v_mov_b64 v[188:189], 0\n" \
    "v_mov_b64 v[190:191], 0\n" \
    "v_mov_b64 v[196:197], 0\n" \
    "v_mov_b64 v[198:199], 0\n" \
    "v_mov_b64 v[200:201], 0\n" \
    "v_mov_b64 v[202:203], 0\n" \
    "v_mov_b64 v[204:205], 0\n" \
    "v_mov_b64 v[206:207], 0\n" \
    "v_mov_b64 v[208:209], 0\n" \
    "v_mov_b64 v[210:211], 0\n" \
    "v_mov_b64 v[212:213], 0\n" \
    "v_mov_b64 v[214:215], 0\n" \
    "s_add_u32 s85, s60, 0x1000\n" \
    "s_add_u32 s83, s76, 1\n" \
    "s_min_u32 s83, s83, 63\n" \
    "s_lshl_b32 s83, s83, 18\n" \
    "s_add_u32 s83, s83, s72\n" \
    "v_cmp_eq_u32 vcc, 0, v237\n" \
    "s_cbranch_vccnz L%=_Pfslow\n" \
    "L%=_Pfok:\n" \
    "s_add_u32 s82, s76, 2\n" \
    "s_min_u32 s82, s82, 63\n" \
    "s_lshr_b32 s82, s82, 2\n" \
    "s_and_b32 s79, s82, 14\n" \
    "s_cmp_lt_u32 s82, 4\n" \
    "s_cselect_b32 s82, s82, s79\n" \
    "s_cselect_b32 s79, s73, s74\n" \
    "s_lshl_b32 s82, s82, 9\n" \
    "s_add_u32 s82, s82, s79\n" \
    "s_memrealtime vcc\n" \
    "s_add_u32 s80, s77, s64\n" \
    "s_add_u32 s80, s80, s69\n" \
    "s_waitcnt lgkmcnt(0)\n" \
    "s_sub_i32 s79, s80, vcc_lo\n" \
    "s_min_i32 s79, s79, 0x400\n" \
    "s_add_i32 s80, vcc_lo, s79\n" \
    "s_cmp_lt_i32 s79, 5\n" \
    "s_cbranch_scc1 L%=_Pu1c\n" \
    "L%=_Pu1a:\n" \
    "s_cmp_lt_i32 s79, 14\n" \
    "s_cbranch_scc1 L%=_Pu1b\n" \
    "s_sleep 2\n" \
    "L%=_Pu1b:\n" \
    "s_memrealtime vcc\n" \
    "s_waitcnt lgkmcnt(0)\n" \
    "s_sub_i32 s79, s80, vcc_lo\n" \
    "s_cmp_gt_i32 s79, 4\n" \
    "s_cbranch_scc1 L%=_Pu1a\n" \
    "L%=_Pu1c:\n" \
    "s_sub_i32 s79, s79, 2\n" \
    "L%=_Pu1d:\n" \
    "s_sub_i32 s79, s79, 1\n" \
    "s_cmp_ge_i32 s79, 0\n" \
    "s_cbranch_scc1 L%=_Pu1d\n" \
    "buffer_load_dwordx4 v[104:107], v230, s[56:59], s83 offen offset:0 sc1\n" \
    "buffer_load_dwordx4 v[108:111], v230, s[56:59], s83 offen offset:1024 sc1\n" \
    "buffer_load_dwordx4 v[112:115], v230, s[56:59], s83 offen offset:2048 sc1\n" \
    "buffer_load_dwordx4 v[116:119], v230, s[56:59], s83 offen offset:3072 sc1\n" \
    "s_add_u32 s79, s83, 0x1000\n" \
    "buffer_load_dwordx4 v[120:123], v230, s[56:59], s79 offen offset:0 sc1\n" \
    "buffer_load_dwordx4 v[124:127], v230, s[56:59], s79 offen offset:1024 sc1\n" \
    "buffer_load_dwordx4 v[128:131], v230, s[56:59], s79 offen offset:2048 sc1\n" \
    "buffer_load_dwordx4 v[132:135], v230, s[56:59], s79 offen offset:3072 sc1\n" \
    "s_add_u32 s79, s83, s75\n" \
    "buffer_load_dwordx4 v[136:139], v230, s[52:55], s79 offen offset:0 sc1\n" \
    "buffer_load_dwordx4 v[140:143], v230, s[52:55], s79 offen offset:1024 sc1\n" \
    "buffer_load_dword v237, v235, s[44:47], s82 offen sc1\n" \
    "buffer_store_dwordx4 v[244:247], v232, s[40:43], s62 offen offset:0 sc1\n" \
    "buffer_store_dwordx4 v[244:247], v232, s[40:43], s62 offen offset:1024 sc1\n" \
    "buffer_store_dwordx4 v[244:247], v232, s[40:43], s62 offen offset:2048 sc1\n" \
    "buffer_store_dwordx4 v[244:247], v232, s[40:43], s62 offen offset:3072 sc1\n" \
    "buffer_store_dwordx4 v[244:247], v233, s[40:43], s62 offen offset:0 sc1\n" \
    "buffer_store_dwordx4 v[244:247], v233, s[40:43], s62 offen offset:1024 sc1\n" \
    "buffer_store_dwordx4 v[244:247], v234, s[40:43], s62 offen sc1\n" \
    "v_mov_b32 v238, s76\n" \
    "s_barrier\n" \
    "s_cmp_eq_u32 s76, 63\n" \
    "s_cbranch_scc1 L%=_Pfin\n" \
    "ds_read_b32 v216, v229\n" \
    "s_waitcnt lgkmcnt(0)\n" \
    "v_readfirstlane_b32 s79, v216\n" \
    "s_cmp_gt_u32 s79, s76\n" \
    "s_cbranch_scc1 L%=_Pu2s\n" \
    "L%=_Pu2k:\n" \
    "s_waitcnt vmcnt(0)\n" \
    "ds_write_b128 v231, v[104:107] offset:16384\n" \
    "ds_write_b128 v231, v[108:111] offset:17536\n" \
    "ds_write_b128 v231, v[112:115] offset:18688\n" \
    "ds_write_b128 v231, v[116:119] offset:19840\n" \
    "ds_write_b128 v231, v[120:123] offset:20992\n" \
    "ds_write_b128 v231, v[124:127] offset:22144\n" \
    "ds_write_b128 v231, v[128:131] offset:23296\n" \
    "ds_write_b128 v231, v[132:135] offset:24448\n" \
    "ds_write_b128 v230, v[136:139] offset:25600\n" \
    "ds_write_b128 v230, v[140:143] offset:26624\n" \
    "s_memrealtime vcc\n" \
    "s_add_u32 s80, s77, s64\n" \
    "s_add_u32 s80, s80, s67\n" \
    "s_waitcnt lgkmcnt(0)\n" \
    "s_sub_i32 s79, s80, vcc_lo\n" \
    "s_min_i32 s79, s79, 0x400\n" \
    "s_add_i32 s80, vcc_lo, s79\n" \
    "s_cmp_lt_i32 s79, 5\n" \
    "s_cbranch_scc1 L%=_Pu3c\n" \
    "L%=_Pu3a:\n" \
    "s_cmp_lt_i32 s79, 14\n" \
    "s_cbranch_scc1 L%=_Pu3b\n" \
    "s_sleep 2\n" \
    "L%=_Pu3b:\n" \
    "s_memrealtime vcc\n" \
    "s_waitcnt lgkmcnt(0)\n" \
    "s_sub_i32 s79, s80, vcc_lo\n" \
    "s_cmp_gt_i32 s79, 4\n" \
    "s_cbranch_scc1 L%=_Pu3a\n" \
    "L%=_Pu3c:\n" \
    "s_sub_i32 s79, s79, 2\n" \
    "L%=_Pu3d:\n" \
    "s_sub_i32 s79, s79, 1\n" \
    "s_cmp_ge_i32 s79, 0\n" \
    "s_cbranch_scc1 L%=_Pu3d\n" \
    "buffer_load_dwordx4 v[168:171], v225, s[40:43], s60 offen sc1\n" \
    "buffer_load_dwordx4 v[172:175], v225, s[40:43], s85 offen sc1\n" \
    "buffer_store_dword v238, v236, s[44:47], 0 offen sc1\n" \
    "L%=_Pvc:\n" \
    "s_waitcnt vmcnt(2)\n" \
    "v_smfmac_f32_16x16x64_bf16 v[176:179], v[168:171], v[0:7], v224\n" \
    "v_smfmac_f32_16x16x64_bf16 v[180:183], v[168:171], v[8:15], v224\n" \
    "s_waitcnt vmcnt(1)\n" \
    "v_smfmac_f32_16x16x64_bf16 v[176:179], v[172:175], v[32:39], v224\n" \
    "v_smfmac_f32_16x16x64_bf16 v[180:183], v[172:175], v[40:47], v224\n" \
    "v_smfmac_f32_16x16x64_bf16 v[184:187], v[168:171], v[16:23], v224\n" \
    "v_smfmac_f32_16x16x64_bf16 v[188:191], v[168:171], v[24:31], v224\n" \
    "v_add_f32 v176, v176, v178\n" \
    "v_smfmac_f32_16x16x64_bf16 v[184:187], v[172:175], v[48:55], v224\n" \
    "v_add_f32 v177, v177, v179\n" \
    "v_smfmac_f32_16x16x64_bf16 v[188:191], v[172:175], v[56:63], v224\n" \
    "v_cmp_u_f32 vcc, v176, v177\n" \
    "v_cvt_pk_bf16_f32 v64, v176, v177\n" \
    "v_add_f32 v180, v180, v182\n" \
    "v_add_f32 v181, v181, v183\n" \
    "v_cvt_pk_bf16_f32 v72, v180, v181\n" \
    "v_add_f32 v184, v184, v186\n" \
    "v_add_f32 v185, v185, v187\n" \
    "v_cvt_pk_bf16_f32 v80, v184, v185\n" \
    "v_add_f32 v188, v188, v190\n" \
    "v_add_f32 v189, v189, v191\n" \
    "v_cvt_pk_bf16_f32 v88, v188, v189\n" \
    "s_cbranch_vccnz L%=_Pvslow\n" \
    "L%=_Pvok:\n" \
    "s_barrier\n" \
    "ds_read_b96 v[192:194], v227\n" \
    "s_waitcnt lgkmcnt(0)\n" \
    "v_smfmac_f32_16x16x64_bf16 v[212:215], v[192:195], v[96:103], v224\n" \
    "v_smfmac_f32_16x16x64_bf16 v[196:199], v[192:195], v[64:71], v224\n" \
    "v_smfmac_f32_16x16x64_bf16 v[200:203], v[192:195], v[72:79], v224\n" \
    "v_smfmac_f32_16x16x64_bf16 v[204:207], v[192:195], v[80:87], v224\n" \
    "v_add_f32 v212, v212, v214\n" \
    "v_smfmac_f32_16x16x64_bf16 v[208:211], v[192:195], v[88:95], v224\n" \
    "v_add_f32 v213, v213, v215\n" \
    "v_rcp_f32 v220, v212\n" \
    "v_rcp_f32 v221, v213\n" \
    "v_add_f32 v196, v196, v198\n" \
    "v_add_f32 v197, v197, v199\n" \
    "v_pk_add_f32 v[200:201], v[200:201], v[202:203]\n" \
    "v_pk_add_f32 v[204:205], v[204:205], v[206:207]\n" \
    "s_nop 0\n" \
    "v_pk_add_f32 v[208:209], v[208:209], v[210:211]\n" \
    "v_pk_mul_f32 v[196:197], v[196:197], v[220:221]\n" \
    "v_pk_mul_f32 v[200:201], v[200:201], v[220:221]\n" \
    "v_pk_mul_f32 v[204:205], v[204:205], v[220:221]\n" \
    "v_pk_mul_f32 v[208:209], v[208:209], v[220:221]\n" \
    "v_cvt_pk_bf16_f32 v216, v196, v200\n" \
    "v_cvt_pk_bf16_f32 v217, v204, v208\n" \
    "v_cvt_pk_bf16_f32 v218, v197, v201\n" \
    "v_cvt_pk_bf16_f32 v219, v205, v209\n" \
    "buffer_store_dwordx4 v[216:219], v226, s[40:43], s60 offen sc1\n" \
    "s_mov_b32 s79, s60\n" \
    "s_mov_b32 s60, s61\n" \
    "s_mov_b32 s61, s62\n" \
    "s_mov_b32 s62, s63\n" \
    "s_mov_b32 s63, s79\n" \
    "s_add_u32 s76, s76, 1\n" \
    "s_add_u32 s77, s77, s65\n" \
    "s_add_u32 s84, s84, 0x20000\n" \
    "s_add_u32 s81, s81, s65\n" \
    "s_branch L%=_Ploop\n" \
    "L%=_Pfslow:\n" \
    "s_sleep 4\n" \
    "buffer_load_dword v237, v235, s[44:47], s82 offen sc1\n" \
    "s_waitcnt vmcnt(0)\n" \
    "v_cmp_eq_u32 vcc, 0, v237\n" \
    "s_cbranch_vccz L%=_Pfok\n" \
    "s_cmp_eq_u32 s99, 0\n" \
    "s_cbranch_scc1 L%=_Pfok\n" \
    "s_sub_u32 s99, s99, 1\n" \
    "s_branch L%=_Pfslow\n" \
    "L%=_Pfin:\n" \
    "buffer_store_dword v238, v236, s[44:47], 0 offen sc1\n" \
    "s_waitcnt vmcnt(0)\n" \
    "s_barrier\n" \
    "v_mov_b32 v238, 64\n" \
    "buffer_store_dword v238, v236, s[44:47], 0 offen sc1\n" \
    "s_branch L%=_end\n" \
    "L%=_Pu2s:\n" \
    "buffer_load_dword v216, off, s[44:47], s78 sc1\n" \
    "s_waitcnt vmcnt(0)\n" \
    "v_readfirstlane_b32 s79, v216\n" \
    "s_lshr_b32 s80, s79, 24\n" \
    "s_cmp_gt_u32 s80, s76\n" \
    "s_cbranch_scc1 L%=_Pu2h\n" \
    "s_cmp_eq_u32 s99, 0\n" \
    "s_cbranch_scc1 L%=_Pu2k\n" \
    "s_sub_u32 s99, s99, 1\n" \
    "s_sleep 2\n" \
    "s_branch L%=_Pu2s\n" \
    "L%=_Pu2h:\n" \
    "s_and_b32 s79, s79, 0xffffff\n" \
    "s_add_u32 s79, s79, s81\n" \
    "s_max_u32 s77, s77, s79\n" \
    "s_branch L%=_Pu2k\n" \
    "L%=_Pvslow:\n" \
    "s_nop 7\n" \
    "s_nop 0\n" \
    "v_max_u32 v216, v168, v169\n" \
    "v_max3_u32 v216, v216, v170, v171\n" \
    "v_max3_u32 v216, v216, v172, v173\n" \
    "v_max3_u32 v216, v216, v174, v175\n" \
    "v_cmp_eq_u32 vcc, -1, v216\n" \
    "s_cbranch_vccz L%=_Pvok\n" \
    "s_cmp_eq_u32 s99, 0\n" \
    "s_cbranch_scc1 L%=_Pvok\n" \
    "s_sub_u32 s99, s99, 1\n" \
    "s_cmp_lg_u32 s99, 0\n" \
    "s_cbranch_scc1 L%=_Pu4\n" \
    "v_mov_b32 v216, 1\n" \
    "buffer_store_dword v216, off, s[44:47], 0 sc1\n" \
    "L%=_Pu4:\n" \
    "s_add_u32 s79, s76, 2\n" \
    "v_mov_b32 v216, s79\n" \
    "ds_write_b32 v229, v216\n" \
    "s_sleep 1\n" \
    "buffer_load_dwordx4 v[168:171], v225, s[40:43], s60 offen sc1\n" \
    "buffer_load_dwordx4 v[172:175], v225, s[40:43], s85 offen sc1\n" \
    "v_mov_b64 v[176:177], 0\n" \
    "v_mov_b64 v[178:179], 0\n" \
    "v_mov_b64 v[180:181], 0\n" \
    "v_mov_b64 v[182:183], 0\n" \
    "v_mov_b64 v[184:185], 0\n" \
    "v_mov_b64 v[186:187], 0\n" \
    "v_mov_b64 v[188:189], 0\n" \
    "v_mov_b64 v[190:191], 0\n" \
    "s_waitcnt vmcnt(0)\n" \
    "s_branch L%=_Pvc\n" \
    "L%=_end:\n"
#define V8(b) "v" #b "0", "v" #b "1", "v" #b "2", "v" #b "3", "v" #b "4", "v" #b "5", "v" #b "6", "v" #b "7", "v" #b "8", "v" #b "9"
#define S_IO "+{s[40:55]}"(sA), "+{s[56:71]}"(sB), "+{s[72:75]}"(sC), "+{s78}"(lwo), "={s[92:99]}"(so)
#define S_CLOB "memory", "vcc", "scc", "s76", "s77", "s79", "s80", "s81", "s82", "s83", "s84", "s85", "s86", "s87", "s88", "s89", "s90", "s91"
#define B_CLOB S_CLOB, "v64", "v65", "v66", "v67", "v68", "v69", V8(7), V8(8), V8(9), V8(10), V8(11), V8(12), V8(13), V8(14), V8(15), V8(16), V8(17), V8(18), V8(19), V8(20), V8(21), \
    "v220", "v221", "v240", "v241", "v242", "v243"
#define FIN() do {} while (0)

#define MK_S() \
    unsigned so4[NSLOT]; \
_Pragma("unroll") \
    for (int k = 0; k < NSLOT; ++k) so4[k] = __builtin_amdgcn_readfirstlane(k * (unsigned)SLOT + (a.ctl[F_PM / 4 + 2 * k + 1] < a.ctl[F_PM / 4 + 2 * k] ? 8192u : 0u)); \
    v16u sA = cat8(cat4(mkrs(a.ring, NSLOT * SLOT), mkrs(a.ctl, CTL_SZ)), cat4(mkrs(a.mb, NSEG * NH * 4096), mkrs(a.qkv + QSZ, QSZ))); \
    v16u sB = cat8(cat4(mkrs(a.qkv + 2 * (size_t)QSZ, QSZ), v4u{so4[0], so4[1], so4[2], so4[3]}), \
                   v8u{(unsigned)__builtin_amdgcn_readfirstlane(a.ctl[F_PM / 4 + 8]), (unsigned)a.per, (unsigned)a.d1, (unsigned)a.dv, (unsigned)a.d2, (unsigned)a.dp, (unsigned)a.slack, (unsigned)a.thr});

DEV void chain(LAS char* lds, const Args& a, const int c) {
    const int w = __builtin_amdgcn_readfirstlane(threadIdx.x >> 6), h = c >> 2, j = c & 3;
    const unsigned lb = (unsigned)(size_t)lds, lbw = lb + w * 16384;
#define LANE() int l = threadIdx.x & 63; asm volatile("" : "+v"(l)); const int l15 = l & 15, g = l >> 4, hb = (l >> 1) & 1, q4 = l15 >> 2; const unsigned idx = hb ? 0xEEEE : 0x4444
    v4u sC; v8u so;
    unsigned lwo = F_LW + (c >> 5) * 128 + (w & 3);
    if (w < 4) {
        LANE();
        v16u wt[12];
        {
            const v4u rw[3] = {mkrs(a.Wq, DM * HD * 2), mkrs(a.Wk, DM * HD * 2), mkrs(a.Wv, DM * HD * 2)};
            const unsigned vo = (l >> 2) * (HD * 2) + (l & 3) * 16, sw = (256 * w * HD + h * DH + 32 * j) * 2;
            auto issue = [&](int p) {
#pragma unroll
                for (int i = 0; i < 8; ++i) DMA16(rw[p >> 1], vo, sw + ((p & 1) * 128 + i * 16) * (HD * 2), lbw + (p & 1) * 8192 + i * 1024); };
            issue(0); issue(1);
            const LAS char* pw = lds + w * 16384 + (4 * (g >> 1) + (g & 1) + 2 * (q4 >> 1) + 16 * (q4 & 1)) * 64 + (l & 3) * 8;
#pragma unroll
            for (int p = 0; p < 6; ++p) {
                if (p < 5) WAITV(8); else WAITV(0);
#pragma unroll
                for (int k = 0; k < 2; ++k) wt[2 * p + k] = cat8(rd_w<2048, 512, 2560>(pw + (p & 1) * 8192 + k * 4096), rd_w<2048, 512, 2560>(pw + (p & 1) * 8192 + k * 4096 + 32));
                WAITL(0);
                if (p < 4) issue(p + 2);
            }
        }
        v16u m;
#pragma unroll
        for (int k = 0; k < 4; ++k) {
            const bf16_t* mp = a.mem0 + (2 * q4 + (l & 1)) * DM + 256 * w + 64 * k + 4 * g + 2 * hb;
            const unsigned t0 = *(const unsigned*)mp, t1 = *(const unsigned*)(mp + 16), t2 = *(const unsigned*)(mp + 32), t3 = *(const unsigned*)(mp + 48);
            m[4 * k] = __builtin_amdgcn_perm(t1, t0, 0x05040100u); m[4 * k + 1] = __builtin_amdgcn_perm(t3, t2, 0x05040100u);
            m[4 * k + 2] = __builtin_amdgcn_perm(t1, t0, 0x07060302u); m[4 * k + 3] = __builtin_amdgcn_perm(t3, t2, 0x07060302u);
        }
        const unsigned pq = lb + L_PQK + w * 2176 + l * 4;
        const v8u va = {idx, nearo(H3_OFF + (c >> 5) * 16384 + w * 4096) + l * 16, pq, lb + L_PV + w * 1280 + l * 4, lb + L_MISC + w * 4, lb + L_MISC, pq + 1088, 0};
        sC = v4u{0, 0, 0, 0};
        MK_S();
        BAR(); BAR();
        asm volatile(ASM_A : S_IO, "+{v[192:207]}"(m)
                     : "{v[0:31]}"(cat16(wt[0], wt[1])), "{v[32:63]}"(cat16(wt[2], wt[3])), "{v[64:95]}"(cat16(wt[4], wt[5])), "{v[96:127]}"(cat16(wt[6], wt[7])),
                       "{v[128:159]}"(cat16(wt[8], wt[9])), "{v[160:191]}"(cat16(wt[10], wt[11])), "{v[232:239]}"(va)
                     : S_CLOB, "v208", "v209", V8(21), V8(22), "v230", "v231", "v240", "v241", "v244", "v245", "v246", "v247");
        FIN();
    } else {
        LANE();
        const int bw = w - 4, l7 = l & 7;
        {
            const v4u rw = mkrs(a.Wo, HD * DM * 2);
            const unsigned vo = (l >> 3) * (DM * 2) + l7 * 16, sw = (h * DH * DM + 256 * j + 64 * bw) * 2;
#pragma unroll
            for (int i = 0; i < 16; ++i) DMA16(rw, vo, sw + i * 8 * (DM * 2), lbw + i * 1024);
        }
        if (w == 6 && l < 16) ((LAS unsigned*)(lds + L_MISC))[l] = l == 0;
        WAITV(0);
        const LAS char* pw = lds + w * 16384 + (16 * (g & 1) + 8 * (g >> 1) + (q4 & 1) + 4 * (q4 >> 1)) * 128 + (l & 3) * 8;
#define RDO(o) rd_w<256, 4096, 4352>(pw + (o))
        const v32u wo0 = cat16(cat8(RDO(0), RDO(32)), cat8(RDO(64), RDO(96))), wo1 = cat16(cat8(RDO(8192), RDO(8224)), cat8(RDO(8256), RDO(8288)));
        BAR();
        const unsigned h1 = nearo(H1_OFF + h * 8192), h1j = h1 + j * 2048;
        v16u va = {};
        va[0] = idx; va[1] = h1 + (g >> 1) * 2048 + 1280 + (l & 31) * 16;
        va[2] = nearo(H2_OFF + (32 * j + 8 * bw + q4 * 2 + (g >> 1)) * 4096) + h * 128 + ((g & 1) * 4 + (l & 3)) * 16;
        va[3] = lb + L_P + g * 320 + l15 * 16;
        va[4] = lb + (g & 1) * X_BLK + (l & 2) * X_FQ + (8 * (g >> 1) + (q4 & 1) + 4 * (q4 >> 1)) * 16 + (l & 1) * 8;
        va[5] = lb + L_MISC;
        v4u sel = {};
        if (w == 4) {
            const int tok = l >> 3, u = (l >> 2) & 1, z = l & 3;
            va[6] = h1 + (z == 0 ? 8 + u : (z == 1 ? 2 * u : (z == 2 ? 4 + 2 * u : 2 * u + 1))) * 128 + tok * 16;
            va[7] = h1 + (5 + 2 * u) * 128 + tok * 16 + (z & 1) * 8;
            va[8] = lb + L_P + ((z & 2) + u) * 320 + (4 * (tok >> 1) + 2 * (z & 1) + (tok & 1)) * 16;
            sC = v4u{__float_as_uint(SCALE * LOG2E), __float_as_uint(SCALE * LOG2E), (unsigned)c, 0};
            va[9] = l < 4 ? F_LW + l * 128 : OOB;
        } else if (w == 5) {
            va[6] = lb + L_PQK + ((l15 >> 3) * 2 + (l & 1)) * 272 + (l7 >> 1) * 64 + g * 16;
            va[7] = (l & 8) ? OOB : h1j + g * 128 + l7 * 16;
            va[8] = ((l & 8) || g < 2) ? OOB : h1j + 1024 + ((g & 1) * 8 + l7) * 16;
            va[9] = (l & 8) ? h * 4096 + j * 512 + (g * 8 + l7) * 16 : OOB;
            va[10] = lb + l * 16;
            sC = v4u{0, 0, 0, 0};
        } else if (w == 6) {
            const int rho = (l >> 1) & 15, tok = 2 * (rho >> 2) + (rho & 1), c4 = 2 * (l >> 5) + ((rho >> 1) & 1);
            va[6] = lb + ((l & 1) * 160 + (tok & 1) * 72 + (tok >> 1) * 16 + c4 * 4) * 4;
            va[7] = (l & 1) ? OOB : h1j + 1280 + (l >> 1) * 16;
            va[8] = lb + L_VT + tok * 64 + (l & 1) * 32 + c4 * 8;
            va[9] = lb + L_VT + q4 * 64 + (g & 1) * 32 + (l & 3) * 8;
            va[10] = l < 32 ? h * 4096 + 2048 + (32 * j + l) * 16 : OOB;
            va[11] = nearo(H2_OFF + c * 4096) + l * 16;
            va[12] = (l & 1) ? OOB : nearo(H3_OFF + (2 * (g >> 1) + (l15 >> 3)) * 16384 + c * 128) + ((l7 & 6) + (g & 1)) * 16;
#pragma unroll
            for (int i = 0; i < 4; ++i) sel[i] = (l7 == 2 * i ? 0x3f80u : 0u) | (l7 == 2 * i + 1 ? 0x3f800000u : 0u);
            sC = v4u{0, 0, 0, 0};
        } else {
            const rsrc_t rk = mkrsrc(a.qkv + QSZ, QSZ), rv = mkrsrc(a.qkv + 2 * (size_t)QSZ, QSZ);
            wait_flags(mkrsrc(a.ctl, CTL_SZ), qkv_flag(1 + (l & 1), 0, h), 1, a.ctl);
            v4u pf[10];
#pragma unroll
            for (int i = 0; i < 8; ++i) pf[i] = LDB(rv, i, h * 8192);
            pf[8] = LDB(rk, 0, h * 8192 + j * 2048); pf[9] = LDB(rk, 1, h * 8192 + j * 2048);
#pragma unroll
            for (int i = 0; i < 8; ++i) *(LAS v4u*)(lds + L_X + i * X_BLK + g * X_FQ + l15 * 16) = pf[i];
            *(LAS v4u*)(lds + L_K + l * 16) = pf[8]; *(LAS v4u*)(lds + L_K + 1024 + l * 16) = pf[9];
            va[6] = l * 16; va[7] = lb + g * X_FQ + l15 * 16;
            va[8] = nearo(H2_OFF + c * 4096) + l * 16; va[9] = h1j + l * 16;
            va[10] = l < 32 ? nearo(H3_OFF + (l >> 3) * 16384 + c * 128) + l7 * 16 : OOB;
            va[11] = 4 + (l & 1) * 4; va[12] = l == 0 ? F_PG + h * 128 + j * 4 : OOB;
            sC = v4u{h * 8192u, F_QKV + h * 16u, F_QKV + (h & ~1) * 16u, j * 2048u};
        }
        MK_S();
        BAR();
        asm volatile(ASM_B : S_IO, "+{v[224:239]}"(va), "+{v[244:247]}"(sel) : "{v[0:31]}"(wo0), "{v[32:63]}"(wo1) : B_CLOB);
        FIN();
    }
}

#define WSTAMP(k) do {} while (0)
#define RING_LD(K, B) asm volatile("buffer_load_dwordx4 %0, %1, %2, %3 offen offset:%4 sc1" : "+v"(ring[K]) : "v"(l16), "s"(rso), "s"((B) + ((K) >> 2) * 4096), "n"(((K) & 3) * 1024) : "memory")
#define RING_MM(N, K, C0, C1) asm volatile("s_waitcnt vmcnt(" #N ")\n\tv_mfma_f32_16x16x32_bf16 %0, %3, %2, %0\n\tv_mfma_f32_16x16x32_bf16 %1, %4, %2, %1" : "+v"(C0), "+v"(C1), "+v"(ring[K]) : "v"(wo[K][0]), "v"(wo[K][1]) : "memory")
#define RING_ALL(P) P(ring[0]), P(ring[1]), P(ring[2]), P(ring[3]), P(ring[4]), P(ring[5]), P(ring[6]), P(ring[7]), P(ring[8]), P(ring[9]), P(ring[10]), P(ring[11]), P(ring[12]), P(ring[13]), P(ring[14]), P(ring[15])
static constexpr int RED_LD = 36 * 4, RED_W = 32 * RED_LD, RED_B = 8 * RED_W;

DEV void worker(LAS char* lds, const Args& a, const int wk) {
    const int tid = threadIdx.x, w = __builtin_amdgcn_readfirstlane(tid >> 6), l = tid & 63, fr = l & 15, fq = l >> 4;
    const int g = wk >> 5, i = wk & 31;
    const rsrc_t rc = mkrsrc(a.ctl, CTL_SZ);
    unsigned* err = a.ctl;
    WSTAMP(0);
    const v4u rx = mkrs(a.x, S_TOK * DM * 2);
#pragma unroll 1
    for (int r = 0; r < 2; ++r) {
        const int m = 1 + ((i >> 3) & 1), rb = 2 * r + (i >> 4), h = 8 * g + (i & 7);
        const v4u rw = mkrs(m == 1 ? a.Wk : a.Wv, DM * HD * 2);
        const rsrc_t ro = mkrsrc(a.qkv + (size_t)m * QSZ, QSZ);
        if (m == 2) gemm_qkv128<true>(lds, rx, rw, ro, rb * 128, h * 128); else gemm_qkv128<false>(lds, rx, rw, ro, rb * 128, h * 128);
        if (tid == 0) ST4(rc, qkv_flag(m, rb, h), 0, 1u);
        WSTAMP(1 + r);
    }
#pragma unroll 1
    for (int t = i; t < 80; t += 32) {
        const int m = t < 48 ? 1 + ((t >> 2) & 1) : 0, rb = t < 48 ? 4 + 2 * (t >> 3) : 2 * ((t - 48) >> 2), h = 8 * g + 2 * (t & 3);
        const v4u rw = mkrs(m == 0 ? a.Wq : (m == 1 ? a.Wk : a.Wv), DM * HD * 2);
        const rsrc_t ro = mkrsrc(a.qkv + (size_t)m * QSZ, QSZ);
        if (m == 2) gemm_qkv256<true>(lds, rx, rw, ro, rb * 128, h * 128); else gemm_qkv256<false>(lds, rx, rw, ro, rb * 128, h * 128);
        if (tid == 0) ST4(rc, qkv_flag(m, rb, h), 0, 1u);
        WSTAMP(3 + (t >> 5));
    }
    const rsrc_t rq = mkrsrc(a.qkv, QSZ), rk = mkrsrc(a.qkv + QSZ, QSZ), rv = mkrsrc(a.qkv + 2 * (size_t)QSZ, QSZ), rm = mkrsrc(a.mb, NSEG * NH * 4096), ro = mkrsrc(a.ob, QSZ);
    const rsrc_t rg = mkrsrc(a.gr, S_TOK * 8), ri = mkrsrc(a.idx, 32), ru = mkrsrc(a.upd, TOPC * DM * 4);
    if (i >= 16) {
        const int lw = g * 16 + i - 16;
        const rsrc_t rh = mkrsrc(a.hid, TOPC * FF * 2);
        if (lw < 56) {
            const int sel = top8(rg, l);
            if (lw == 0 && tid < TOPC) ST4(ri, tid * 4, 0, sel);
#pragma unroll 1
            for (int e = 0; e < 2; ++e) moe1(lds, a.x, sel, mkrs(a.wg, DM * FF * 2), mkrs(a.wu, DM * FF * 2), rh, 2 * lw + e);
            WAITV(0); RBAR();
            if (tid == 0) ST4(rc, F_M1 + lw * 4, 0, 1u);
        }
        WSTAMP(5);
        if (lw >= 32) {
            wait_flags(rc, F_M1 + min(l, 55) * 4, 1, err);
            moe2(lds, rh, rg, ri, mkrs(a.wd, FF * DM * 2), ru, lw - 32);
            WAITV(0); RBAR();
            if (tid == 0) ST4(rc, F_M2 + (lw - 32) * 4, 0, 1u);
        }
        WSTAMP(6);
    }
    v4u wo[16][2];
    {
        const v4u rw = mkrs(a.Wo, HD * DM * 2);
        const unsigned vo = (l >> 2) * (DM * 2) + (l & 3) * 16, lb = (unsigned)(size_t)lds + w * 16384;
        const LAS char* p = lds + w * 16384 + (4 * fq + (fr >> 2)) * 64 + (l & 3) * 8;
#pragma unroll
        for (int hf = 0; hf < 2; ++hf) {
#pragma unroll
            for (int k = 0; k < 16; ++k) DMA16(rw, vo, ((512 * w + 256 * hf + 16 * k) * DM + 32 * i) * 2, lb + k * 1024);
            WAITV(0);
#pragma unroll
            for (int k = 0; k < 8; ++k)
#pragma unroll
                for (int n = 0; n < 2; ++n) wo[hf * 8 + k][n] = cat(trd(p + k * 2048 + n * 32), trd(p + k * 2048 + n * 32 + 1024));
            WAITL(0);
        }
    }
    WSTAMP(7);
    volatile LAS unsigned* prog = (volatile LAS unsigned*)(lds + 2 * RED_B);
    int xs = i >= 16 ? i - 16 : 32 + i, nx = 2;
    unsigned seen = i >= 16 ? NSEG : 0;
    int sel[8];
    bool pre = false;
    v4u ring[16];
    asm volatile("" : RING_ALL("=v"));
    const v4u rso = mkrs(a.ob, QSZ), rsc = mkrs(a.ctl, CTL_SZ), rsg = mkrs(a.gr, S_TOK * 8), rsu = mkrs(a.upd, TOPC * DM * 4);
    RBAR();
#pragma unroll 1
    for (int n = 0; n < 16; ++n) {
        const int seg = 4 * n + g;
#pragma unroll 1
        while (nx > 0 && (xs <= seg + 3 || (unsigned)xs < seen)) {
            asm volatile("s_waitcnt vmcnt(0)" :: RING_ALL("v") : "memory");
            xattn(rq, rk, rv, rm, ro, rc, err, xs, 8 * g + w, l); xs += 16; --nx; pre = false;
            asm volatile("" : RING_ALL("=v"));
        }
        if (n == 0) {
            wait_flags(rc, F_M2 + (l & 31) * 4, 1, err);
            const unsigned t = LD4(ri, (l & 7) * 4, 0);
#pragma unroll
            for (int q = 0; q < 8; ++q) sel[q] = __builtin_amdgcn_readlane(t, q);
            WSTAMP(8);
        }
        const unsigned ob = (unsigned)(seg * 256 + w * 16) * 1024u;
        int l = tid & 63; asm volatile("" : "+v"(l));
        const int fr = l & 15, fq = l >> 4, erow = w * 4 + fq, ecol = 32 * i + 2 * fr, l16 = l * 16;
        const unsigned fo = __builtin_amdgcn_readfirstlane(n < 15 ? F_XA + (seg + 4) * 64 + (w >> 1) * 4 : OOB);
        if (!pre) {
            wait_flags(rc, F_XA + seg * 64 + (w >> 1) * 4, 1, err);
#pragma unroll
            for (int k = 0; k < 16; ++k) RING_LD(k, ob);
        }
        const int row = seg * 32 + erow;
        int slot = -1;
#pragma unroll
        for (int q = 0; q < 8; ++q) slot = sel[q] == row ? q : slot;
        unsigned fn, pg, xv; float gt; v2u up;
        v4f c[2][2] = {};
        asm volatile("buffer_load_dword %[fn], off, %[rc], %[fo] sc1\n\tbuffer_load_dword %[pg], off, %[rc], %[po] sc1\n\tbuffer_load_dword %[xv], %[xo], %[rx], 0 offen\n\t"
                     "buffer_load_dword %[gt], %[go], %[rg], 0 offen\n\tbuffer_load_dwordx2 %[up], %[uo], %[ru], 0 offen sc1"
                     : [fn] "=&v"(fn), [pg] "=&v"(pg), [xv] "=&v"(xv), [gt] "=&v"(gt), [up] "=&v"(up), "+v"(c[0][0]), "+v"(c[0][1]), "+v"(c[1][0]), "+v"(c[1][1])
                     : [rc] "s"(rsc), [fo] "s"(fo), [po] "s"(F_PG + g * 1024), [rx] "s"(rx), [rg] "s"(rsg), [ru] "s"(rsu),
                       [xo] "v"((row * DM + ecol) * 2), [go] "v"(row * 8), [uo] "v"(slot < 0 ? OOB : (slot * DM + ecol) * 4) : "memory");
#pragma unroll
        for (int k = 0; k < 16; ++k) { RING_MM(20, k, c[0][0], c[0][1]); RING_LD(k, ob + 131072); }
        RING_MM(15, 0, c[1][0], c[1][1]);
        asm volatile("" : "+v"(fn), "+v"(pg), "+v"(xv), "+v"(gt), "+v"(up));
        pre = __builtin_amdgcn_readfirstlane(fn) != 0;
        const unsigned nb = pre ? ob + 4 * 262144 : OOB;
#pragma unroll
        for (int k = 0; k < 16; ++k) { if (k) RING_MM(15, k, c[1][0], c[1][1]); RING_LD(k, nb); }
        asm volatile("s_nop 7\n\ts_nop 1" : "+v"(c[0][0]), "+v"(c[0][1]), "+v"(c[1][0]), "+v"(c[1][1]));
        LAS char* rb = lds + (n & 1) * RED_B;
#pragma unroll
        for (int u = 0; u < 2; ++u)
#pragma unroll
            for (int t = 0; t < 2; ++t) *(LAS v4f*)(rb + w * RED_W + (u * 16 + fr) * RED_LD + (t * 16 + 4 * fq) * 4) = c[u][t];
        if (tid == 0) prog[n & 1] = pg;
        BAR();
        v2f acc = {0, 0};
#pragma unroll
        for (int q = 0; q < 8; ++q) acc += *(const LAS v2f*)(rb + q * RED_W + erow * RED_LD + fr * 8);
        seen = __builtin_amdgcn_readfirstlane(prog[n & 1]);
        float b0 = lo16(xv), b1 = hi16(xv);
        if (slot >= 0) { b0 = rbf(b0 + __uint_as_float(up[0])); b1 = rbf(b1 + __uint_as_float(up[1])); }
        const float omg = rbf(1.0f - gt);
        *(unsigned*)(a.out + (size_t)row * DM + ecol) = pk2(rbf(gt * rbf(acc[0])) + rbf(omg * b0), rbf(gt * rbf(acc[1])) + rbf(omg * b1));
        WSTAMP(9 + n);
    }
    asm volatile("s_waitcnt vmcnt(0)" :: RING_ALL("v") : "memory");
}

__global__ void __launch_bounds__(512, 2) k_main(Args a) {
    __shared__ __attribute__((aligned(16))) char lds[L_TOT];
    const int b = blockIdx.x, c = (b & 3) * 32 + (b >> 3);
    if ((b & 7) < 4) chain((LAS char*)lds, a, c); else worker((LAS char*)lds, a, c);
}
__global__ void __launch_bounds__(512, 2) k_init(Args a) {
    const int b = blockIdx.x, tid = threadIdx.x, w = __builtin_amdgcn_readfirstlane(tid >> 6), l = tid & 63;
    const rsrc_t rr = mkrsrc(a.ring, NSLOT * SLOT);
    if (b < 64 && (b & 7) == 0) {
        if (w == 0) {
            unsigned off = (b >> 4) * (unsigned)SLOT + ((b >> 3) & 1) * 8192 + nearo(PR_OFF) + (l & 31) * 4, t = 0xffffffffu;
            for (int it = 0; it < 4; ++it) { const unsigned t0 = cyc32(); ST4(rr, off, 0, off); WAITV(0); t = min(t, cyc32() - t0); off += 256; }
            if (l == 0) a.ctl[F_PM / 4 + (b >> 3)] = t;
            if (l == 0 && b == 0) a.ctl[F_PM / 4 + 8] = rt32();
        }
        RBAR();
    }
    const v4u poi4 = {POI, POI, POI, POI};
    ST16(rr, b * 16384 + tid * 16, 0, poi4); ST16(rr, b * 16384 + 8192 + tid * 16, 0, poi4);
    if (tid < 8) ST16(mkrsrc(a.ctl, CTL_SZ), b * 128 + tid * 16, 0, (v4u{0, 0, 0, 0}));
    prep_row(a.x, a.gw, a.w_r, a.gr, b * 8 + w, l);
}

#define TUNE_DEFAULT {238, 87, 91, 190, 5, 40, 60, 0}

extern "C" void kernel_launch(void* const* d_in, const int* in_sizes, int n_in, void* d_out, int out_size, void* d_ws, size_t ws_size, hipStream_t stream) {
    char* ring = (char*)(((size_t)d_ws + SLOT - 1) & ~(size_t)(SLOT - 1));
    char* ws = ring + NSLOT * SLOT; size_t off = 0;
    auto alloc = [&](size_t bytes) { void* p = ws + off; off += (bytes + 255) & ~(size_t)255; return p; };
    Args a;
    a.x = (const bf16_t*)d_in[0]; a.mem0 = (const bf16_t*)d_in[1]; a.Wq = (const bf16_t*)d_in[2]; a.Wk = (const bf16_t*)d_in[3]; a.Wv = (const bf16_t*)d_in[4]; a.Wo = (const bf16_t*)d_in[5];
    a.w_r = (const bf16_t*)d_in[8]; a.wg = (const bf16_t*)d_in[9]; a.wu = (const bf16_t*)d_in[10]; a.wd = (const bf16_t*)d_in[11]; a.gw = (const bf16_t*)d_in[12];
    a.qkv = (char*)alloc(3 * (size_t)QSZ); a.mb = (char*)alloc(NSEG * NH * 4096); a.ob = (char*)alloc(QSZ);
    a.gr = (float*)alloc(S_TOK * 8); a.idx = (int*)alloc(256); a.hid = (bf16_t*)alloc(TOPC * FF * 2); a.upd = (float*)alloc(TOPC * DM * 4); a.out = (bf16_t*)d_out;
    a.ring = ring; a.ctl = (unsigned*)alloc(CTL_SZ + 256); a.dbg = (unsigned*)alloc((32768 + 4096) * 4);
    const int tn[8] = TUNE_DEFAULT;
    a.per = tn[0]; a.d1 = tn[1]; a.dv = tn[2]; a.d2 = tn[3]; a.dp = tn[4]; a.slack = tn[5]; a.thr = tn[6]; a.spare = tn[7];
    k_init<<<256, 512, 0, stream>>>(a);
    k_main<<<256, 512, 0, stream>>>(a);
}
```

```cpp
#include <hip/hip_runtime.h>
#include <stdint.h>

typedef unsigned short bf16_t;
typedef unsigned v2u __attribute__((ext_vector_type(2)));
typedef unsigned v4u __attribute__((ext_vector_type(4)));
typedef unsigned v8u __attribute__((ext_vector_type(8)));
typedef float v2f __attribute__((ext_vector_type(2)));
typedef float v4f __attribute__((ext_vector_type(4)));
typedef short v4s __attribute__((ext_vector_type(4)));
typedef __bf16 v2b __attribute__((ext_vector_type(2)));
typedef __bf16 v8b __attribute__((ext_vector_type(8)));
typedef __bf16 v16b __attribute__((ext_vector_type(16)));
typedef unsigned long long u64;
typedef __amdgpu_buffer_rsrc_t rsrc_t;
#define LAS __attribute__((address_space(3)))
#define DEV __device__ __forceinline__

static constexpr int S_TOK = 2048, DM = 1024, NH = 32, DH = 128, SEG = 32, NSEG = 64;
static constexpr int HD = NH * DH;
static constexpr int FF = 3584, TOPC = 8;
static constexpr float SCALE = 0.08838834764831845f, LOG2E = 1.4426950408889634f, MASKV = -998244352.0f;

DEV unsigned pk2(float lo, float hi) { v2f f = {lo, hi}; return __builtin_bit_cast(unsigned, __builtin_convertvector(f, v2b)); }
DEV float lo16(unsigned u) { return __uint_as_float(u << 16); }
DEV float hi16(unsigned u) { return __uint_as_float(u & 0xffff0000u); }
DEV float rbf(float f) { return __uint_as_float(pk2(0.0f, f)); }
DEV v4f smf(v4u a, v8u b, v4f c, int idx) { return __builtin_amdgcn_smfmac_f32_16x16x64_bf16(__builtin_bit_cast(v8b, a), __builtin_bit_cast(v16b, b), c, idx, 0, 0); }
DEV v4f mf(v4u a, v4u b, v4f c) { return __builtin_amdgcn_mfma_f32_16x16x32_bf16(__builtin_bit_cast(v8b, a), __builtin_bit_cast(v8b, b), c, 0, 0, 0); }
DEV v2u trd(const LAS char* p) { return __builtin_bit_cast(v2u, __builtin_amdgcn_ds_read_tr16_b64_v4i16((LAS v4s*)p)); }
DEV v4u cat(v2u a, v2u b) { return __builtin_shufflevector(a, b, 0, 1, 2, 3); }
DEV v2u sw16(unsigned x, unsigned y) { return __builtin_amdgcn_permlane16_swap(x, y, false, false); }
DEV v2u sw32(unsigned x, unsigned y) { return __builtin_amdgcn_permlane32_swap(x, y, false, false); }
DEV float half_add(float x, float y) { v2u r = sw32(__float_as_uint(x), __float_as_uint(y)); return __uint_as_float(r[0]) + __uint_as_float(r[1]); }
DEV float ror8(float v) { return __int_as_float(__builtin_amdgcn_update_dpp(0, __float_as_int(v), 0x128, 0xf, 0xf, false)); }
template <bool MAX> DEV float red_g(float v) {
    auto op = [](float a, float b) { return MAX ? fmaxf(a, b) : a + b; };
    v2u r = sw16(__float_as_uint(v), __float_as_uint(v)); v = op(__uint_as_float(r[0]), __uint_as_float(r[1]));
    r = sw32(__float_as_uint(v), __float_as_uint(v)); return op(__uint_as_float(r[0]), __uint_as_float(r[1]));
}
DEV float sigmoidf_(float z) { return 1.0f / (1.0f + __expf(-z)); }
DEV v4u mkrs(const void* p, unsigned bytes) { const u64 a = (u64)p; return v4u{(unsigned)a, (unsigned)(a >> 32) & 0xffffu, bytes, 0x00020000u}; }
DEV rsrc_t mkrsrc(const void* p, unsigned bytes) { return __builtin_amdgcn_make_buffer_rsrc((void*)p, (short)0, (int)bytes, 0x00020000); }

#define ST16(RS, OFF, SOFF, V) __builtin_amdgcn_raw_buffer_store_b128(V, RS, (OFF) + (SOFF), 0, 16)
#define ST4(RS, OFF, SOFF, V) __builtin_amdgcn_raw_buffer_store_b32(V, RS, OFF, SOFF, 16)
#define LD16(RS, OFF, SOFF) __builtin_amdgcn_raw_buffer_load_b128(RS, OFF, SOFF, 16)
#define LD8(RS, OFF, SOFF) __builtin_amdgcn_raw_buffer_load_b64(RS, OFF, SOFF, 16)
#define LD4(RS, OFF, SOFF) __builtin_amdgcn_raw_buffer_load_b32(RS, OFF, SOFF, 16)
#define DMA16(RS, VOFF, SOFF, LOFF) asm volatile("s_mov_b32 m0, %0\n\ts_nop 0\n\tbuffer_load_dwordx4 %1, %2, %3 offen lds" :: "s"(LOFF), "v"(VOFF), "s"(RS), "s"(SOFF) : "memory", "m0")
#define DMA16N(RS, VOFF, SOFF, LOFF) asm volatile("s_mov_b32 m0, %0\n\ts_nop 0\n\tbuffer_load_dwordx4 %1, %2, %3 offen nt lds" :: "s"(LOFF), "v"(VOFF), "s"(RS), "s"(SOFF) : "memory", "m0")
#define WSTAMP(k) do {} while (0)
#define FENCE() asm volatile("" ::: "memory")
#define WAITV(n) asm volatile("s_waitcnt vmcnt(" #n ")" ::: "memory")
#define WAITL(n) asm volatile("s_waitcnt lgkmcnt(" #n ")" ::: "memory")
#define RBAR() asm volatile("s_barrier" ::: "memory")
#define BAR() asm volatile("s_waitcnt lgkmcnt(0)\n\ts_barrier" ::: "memory")
#define SCHED() __builtin_amdgcn_sched_barrier(0)

static constexpr int XP = 1056, XS = 32 * XP, L_RED = 4 * XS;

DEV unsigned wt_src(int tid, unsigned ldw) { return (tid >> 4) * ldw + (((tid & 15) ^ ((((tid >> 4) & 3) << 2) | (((tid >> 7) & 1) << 1))) << 4); }
DEV int wt_rd(int w, int l, int n) { const int fr = l & 15, fq = l >> 4; return (w >> 2) * 16384 + (8 * fq + (fr >> 2)) * 256 + (((4 * (w & 3) + 2 * n + ((fr >> 1) & 1)) ^ (((fr >> 2) << 2) | ((fq & 1) << 1))) << 4) + (fr & 1) * 8; }
DEV v4u wt_frag(const LAS char* p) { return cat(trd(p), trd(p + 1024)); }
#define WT_ISSUE(R0, R1, VB, S0, S1, LDW, LB) do { DMA16N(R0, VB, S0, LB); DMA16N(R0, VB, (S0) + 32 * (LDW), (LB) + 8192); DMA16N(R1, VB, S1, (LB) + 16384); DMA16N(R1, VB, (S1) + 32 * (LDW), (LB) + 24576); } while (0)

static constexpr unsigned F_QKV = 1024, F_PG = 16384, F_XA = 20480, F_M1 = 24576, F_CK = 2048, CTL_SZ = 32768, F_PM = CTL_SZ;
static constexpr unsigned POI = 0xFFFFFFFFu, OOB = 0x40000000u, SPIN_CAP = 200000u;
DEV bool spin_fail(unsigned tries, unsigned* err) {
    __builtin_amdgcn_s_sleep(1);
    if ((tries & 255u) != 255u) return false;
    unsigned e; asm volatile("global_load_dword %0, %1, off sc1\n\ts_waitcnt vmcnt(0)" : "=&v"(e) : "v"(err) : "memory");
    if (__builtin_amdgcn_readfirstlane(e) == 1u) return true;
    if (tries > SPIN_CAP) { unsigned one = 1u; asm volatile("global_store_dword %0, %1, off sc1" :: "v"(err), "v"(one) : "memory"); return true; }
    return false;
}
DEV bool all_ge(unsigned v, unsigned t) { return __builtin_amdgcn_ballot_w64(v < t) == 0; }
DEV void wait_flags(rsrc_t rc, unsigned off, unsigned t, unsigned* err) {
    for (unsigned tries = 0;; ++tries) {
        FENCE();
        if (all_ge(LD4(rc, off, 0), t)) break;
        __builtin_amdgcn_s_sleep(6);
        if (spin_fail(tries, err)) break;
    }
    FENCE();
}

template <bool VT> DEV void stream_qkv(LAS char* lds, v4u rx, v4u rw, rsrc_t ro, rsrc_t rc, unsigned fo, int h, unsigned* dbg) {
    const int tid = threadIdx.x, w = __builtin_amdgcn_readfirstlane(tid >> 6), l = tid & 63, kh = w >> 2, cg = w & 3;
    const unsigned lb = (unsigned)(size_t)lds, vb = wt_src(tid, HD * 2), l16 = l * 16;
    const unsigned x0 = (4 * cg + (VT ? 2 : 16) * (1 - kh)) * 2048 + kh * 1024, x1 = (4 * cg + (VT ? 2 : 16) * kh) * 2048 + kh * 1024;
#define STW(s) WT_ISSUE(rw, rw, vb, h * 256 + (s) * (64 * HD * 2), h * 256 + ((s) + 8) * (64 * HD * 2), HD * 2, lb + w * 1024 + ((s) & 3) * XS)
#define STX(u, q, e) DMA16(rx, l16, ((u) >> 1) * 65536 + ((q) & 1 ? x1 : x0) + (VT ? ((e) & 1) + 16 * ((e) >> 1) : (e)) * 2048, lb + (w * 4 + (e)) * XP + (q) * XS)
    v4u W[16][2];
    STW(0); STW(1); STW(2); STW(3);
#pragma unroll
    for (int s = 0; s < 8; ++s) {
        WAITV(12); RBAR();
#pragma unroll
        for (int n = 0; n < 2; ++n) { const LAS char* p = lds + wt_rd(w, l, n) + (s & 3) * XS; W[2 * s][n] = wt_frag(p); W[2 * s + 1][n] = wt_frag(p + 8192); }
        BAR();
        if (s < 4) STW(s + 4); else { STX(s - 4, s - 4, 0); STX(s - 4, s - 4, 1); STX(s - 4, s - 4, 2); STX(s - 4, s - 4, 3); }
    }
    const LAS char* px = lds + (kh * 16 + (l & 15)) * XP + (l >> 4) * 16;
    LAS v4f* ps = (LAS v4f*)(lds + L_RED + w * 2048 + l16);
    const LAS v4f* pr = (const LAS v4f*)(lds + L_RED + (w ^ 4) * 2048 + l16);
    unsigned oo = OOB, on = (unsigned)h * 8192u + cg * 2048u + kh * 1024u + l16;
    v4f c[2][2] = {}, r0, r1;
    v4u f[4];
    auto fin = [&]() { r0 += c[1][0]; r1 += c[1][1]; ST16(ro, oo, 0, (v4u{pk2(r0[0], r0[1]), pk2(r0[2], r0[3]), pk2(r1[0], r1[1]), pk2(r1[2], r1[3])})); oo = on; on += NH * 8192u; };
    WSTAMP(3);
    WAITV(8); RBAR();
    WSTAMP(4);
#pragma unroll
    for (int k = 0; k < 4; ++k) f[k] = *(const LAS v4u*)(px + k * 64);
#pragma unroll 1
    for (int u0 = 0; u0 < 2 * NSEG; u0 += 4) {
        if (u0 == 4) WSTAMP(5);
        if (u0 == 64) WSTAMP(6);
#pragma unroll
        for (int q = 0; q < 4; ++q) {
            if (!(q & 1)) { r0 = pr[0]; r1 = pr[64]; }
#pragma unroll
            for (int k = 0; k < 16; ++k) {
                const v4u x = f[k & 3];
#pragma unroll
                for (int n = 0; n < 2; ++n) { const v4f z = k ? c[q & 1][n] : v4f{0, 0, 0, 0}; c[q & 1][n] = VT ? mf(x, W[k][n], z) : mf(W[k][n], x, z); }
                f[k & 3] = *(const LAS v4u*)(px + (k < 12 ? q * XS + (k + 4) * 64 : ((q + 1) & 3) * XS + (k - 12) * 64));
                if (k == 2) {
                    if (q & 1) { ps[0] = c[0][0]; ps[64] = c[0][1]; if (tid == 0) ST4(rc, fo, 0, (u0 + q) >> 1); }
                    else fin();
                }
                if (k >= 3 && k < 7 && u0 + q > 0) STX(u0 + q + 3, (q + 3) & 3, k - 3);
                SCHED();
            }
            WAITV(4); RBAR(); SCHED();
        }
    }
    r0 = pr[0]; r1 = pr[64]; fin();
    WAITV(0); BAR();
    if (tid == 0) ST4(rc, fo, 0, NSEG);
#undef STW
#undef STX
}

#define LDB(RS, K, BASE) LD16(RS, l * 16 + ((K) & 3) * 1024, (BASE) + ((K) >> 2) * 4096)
DEV void xattn(rsrc_t rq, rsrc_t rk, rsrc_t rv, rsrc_t rm, rsrc_t ro, rsrc_t rc, unsigned* err, int seg, int h, int l) {
    asm volatile("" : "+v"(l));
    const int fr = l & 15, g = l >> 4;
    const unsigned sb = (unsigned)(seg * NH + h) * 8192u, mb = (unsigned)(seg * NH + h) * 4096u;
    wait_flags(rc, F_QKV + h * 16 + min(l, 2) * 4, seg + 1, err);
    v4u q[4][2], km[4], v[8]; v2u vm[8];
    v4f sx[3] = {};
    {
        v4u k[4][2];
#pragma unroll
        for (int j = 0; j < 4; ++j) {
            q[j][0] = LDB(rq, 2 * j, sb); q[j][1] = LDB(rq, 2 * j + 1, sb);
            k[j][0] = LDB(rk, 2 * j, sb); k[j][1] = LDB(rk, 2 * j + 1, sb);
        }
#pragma unroll
        for (int d = 0; d < 8; ++d) v[d] = LDB(rv, d, sb);
#pragma unroll
        for (int j = 0; j < 4; ++j) { sx[0] = mf(k[j][0], q[j][0], sx[0]); sx[1] = mf(k[j][0], q[j][1], sx[1]); sx[2] = mf(k[j][1], q[j][1], sx[2]); }
    }
    SCHED();
    wait_flags(rc, F_PG + h * 128 + (l & 3) * 4, seg + 1, err);
#pragma unroll
    for (int j = 0; j < 4; ++j) km[j] = LD16(rm, (g * 8 + (l & 7)) * 16 + j * 512, mb);
#pragma unroll
    for (int d = 0; d < 8; ++d) vm[d] = LD8(rm, 2048 + fr * 16 + (g & 1) * 8 + d * 256, mb);
    v4f sm[2] = {};
#pragma unroll
    for (int j = 0; j < 4; ++j) { sm[0] = mf(km[j], q[j][0], sm[0]); sm[1] = mf(km[j], q[j][1], sm[1]); }
    SCHED();
#pragma unroll
    for (int u = 0; u < 2; ++u) {
        float e[12];
#pragma unroll
        for (int r = 0; r < 4; ++r) {
            const bool vis = 4 * g + r <= fr;
            e[r] = (u || vis) ? rbf(rbf(sx[u][r]) * SCALE) : MASKV;
            e[4 + r] = (u && vis) ? rbf(rbf(sx[2][r]) * SCALE) : MASKV;
            e[8 + r] = g < 2 ? rbf(rbf(sm[u][r]) * SCALE) : -INFINITY;
        }
        float m = e[0];
#pragma unroll
        for (int r = 1; r < 12; ++r) m = fmaxf(m, e[r]);
        m = red_g<true>(m);
        float sum = 0;
#pragma unroll
        for (int r = 0; r < 12; ++r) { e[r] = __builtin_amdgcn_exp2f((e[r] - m) * LOG2E); sum += e[r]; }
        const float inv = __builtin_amdgcn_rcpf(red_g<false>(sum));
        const v4u P = {pk2(e[0] * inv, e[1] * inv), pk2(e[4] * inv, e[5] * inv), pk2(e[2] * inv, e[3] * inv), pk2(e[6] * inv, e[7] * inv)};
        const unsigned pm0 = pk2(e[8] * inv, e[9] * inv), pm1 = pk2(e[10] * inv, e[11] * inv);
        const v4u Pa = {pm0, pm1, 0u, 0u}, Pb = {0u, 0u, pm0, pm1};
#pragma unroll
        for (int d = 0; d < 4; ++d) {
            const v4u vv = cat(vm[2 * d], vm[2 * d + 1]);
            const v4u va = __builtin_shufflevector(v[2 * d], v[2 * d + 1], 0, 1, 4, 5), vb = __builtin_shufflevector(v[2 * d], v[2 * d + 1], 2, 3, 6, 7);
            const v4f o0 = mf(vv, Pa, mf(va, P, v4f{0, 0, 0, 0})), o1 = mf(vv, Pb, mf(vb, P, v4f{0, 0, 0, 0}));
            const v4u o = {pk2(o0[0], o0[1]), pk2(o0[2], o0[3]), pk2(o1[0], o1[1]), pk2(o1[2], o1[3])};
            ST16(ro, l * 16 + d * 1024, (unsigned)((seg * 2 + u) * 128 + h * 4) * 1024u, o);
        }
    }
    WAITV(0); RBAR();
    if (threadIdx.x == 0) ST4(rc, F_XA + seg * 64 + (h >> 3) * 4, 0, 1u);
}

DEV void prep_row(const bf16_t* x, const bf16_t* gw, const bf16_t* wr, float* gr, int row, int l) {
    float a = 0, b = 0;
#pragma unroll
    for (int i = 0; i < 2; ++i) {
        const v4u xv = *(const v4u*)(x + (size_t)row * DM + i * 512 + l * 8), gv = *(const v4u*)(gw + i * 512 + l * 8), rv = *(const v4u*)(wr + i * 512 + l * 8);
#pragma unroll
        for (int e = 0; e < 4; ++e) {
            a += rbf(lo16(xv[e]) * lo16(gv[e])) + rbf(hi16(xv[e]) * hi16(gv[e]));
            b += lo16(xv[e]) * lo16(rv[e]) + hi16(xv[e]) * hi16(rv[e]);
        }
    }
#pragma unroll
    for (int o = 1; o < 16; o <<= 1) { a += __shfl_xor(a, o); b += __shfl_xor(b, o); }
    a = red_g<false>(a); b = red_g<false>(b);
    if (l == 0) *(v2f*)(gr + 2 * row) = v2f{rbf(sigmoidf_(rbf(a))), rbf(b)};
}
DEV int top8(rsrc_t rg, int l) {
    unsigned key[32];
#pragma unroll
    for (int i = 0; i < 16; ++i) {
        const v4u t = LD16(rg, l * 16 + i * 1024, 0);
        const int row = i * 128 + l * 2;
        key[2 * i] = ((int)t[1] < 0 ? 0xffff0000u - t[1] : t[1] + 0x80000000u) + (0xffffu - row);
        key[2 * i + 1] = ((int)t[3] < 0 ? 0xffff0000u - t[3] : t[3] + 0x80000000u) + (0xffffu - row - 1);
    }
    int res = 0;
#pragma unroll 1
    for (int c = 0; c < TOPC; ++c) {
        unsigned m = 0;
#pragma unroll
        for (int i = 0; i < 32; ++i) m = max(m, key[i]);
#pragma unroll
        for (int o = 1; o < 64; o <<= 1) m = max(m, (unsigned)__shfl_xor((int)m, o));
#pragma unroll
        for (int i = 0; i < 32; ++i) key[i] = key[i] == m ? 0u : key[i];
        if (l == c) res = 0xffff - (int)(m & 0xffffu);
    }
    return res;
}
static constexpr int NMOE = FF / 128, XR = 2080;
DEV void moe(LAS char* lds, v4u rx, int sel, v4u rg, v4u ru, v4u rd, rsrc_t rp, int cu) {
    const int tid = threadIdx.x, w = __builtin_amdgcn_readfirstlane(tid >> 6), l = tid & 63, kh = w >> 2, cg = w & 3, fr = l & 15, fq = l >> 4;
    const unsigned lb = (unsigned)(size_t)lds, v1 = wt_src(tid, FF * 2), v2 = wt_src(tid, DM * 2);
    const unsigned xo = __builtin_amdgcn_readlane(sel, w) * 2048;
    DMA16(rx, l * 16, xo, lb + L_RED + w * XR); DMA16(rx, l * 16, xo + 1024, lb + L_RED + w * XR + 1024);
    auto issue = [&](int s) {
        const unsigned d = lb + w * 1024 + (s & 3) * XS;
        if (s < 16) { const unsigned o = s * (64 * FF * 2) + cu * 256; WT_ISSUE(rg, ru, v1, o, o, FF * 2, d); }
        else { const unsigned o = s < 24 ? (cu * 128 + (s & 1) * 64) * (DM * 2) + (((s >> 1) + cu) & 3) * 512 : OOB; WT_ISSUE(rd, rd, v2, o, o + 256, DM * 2, d); } };
    issue(0); issue(1); issue(2); issue(3);
    const int p0 = wt_rd(w, l, 0), p1 = wt_rd(w, l, 1);
    v4f c[2] = {};
    auto stage = [&](int s, const LAS char* pa) {
        WAITV(12); RBAR();
        const LAS char* p = lds + (s & 3) * XS;
        const v4u a0 = *(const LAS v4u*)pa, a1 = *(const LAS v4u*)(pa + 64), b0 = wt_frag(p + p0), b1 = wt_frag(p + p1), b2 = wt_frag(p + p0 + 8192), b3 = wt_frag(p + p1 + 8192);
        BAR(); issue(s + 4);
        c[0] = mf(b2, a1, mf(b0, a0, c[0])); c[1] = mf(b3, a1, mf(b1, a0, c[1])); };
#pragma unroll 1
    for (int s = 0; s < 16; ++s) stage(s, lds + L_RED + (l & 7) * XR + fq * 16 + s * 128);
    LAS v4f* pu = (LAS v4f*)(lds + L_RED + cg * 2048 + l * 16);
    if (kh) { pu[0] = c[0]; pu[64] = c[1]; }
    BAR();
    if (!kh && fr < 8) {
#pragma unroll
        for (int n = 0; n < 2; ++n) {
            const v4f u = pu[64 * n]; float hv[4];
#pragma unroll
            for (int e = 0; e < 4; ++e) { const float g = rbf(c[n][e]); hv[e] = rbf(g * sigmoidf_(g)) * rbf(u[e]); }
            *(LAS v2u*)(lds + L_RED + 8192 + fr * 288 + cg * 64 + n * 32 + fq * 8) = v2u{pk2(hv[0], hv[1]), pk2(hv[2], hv[3])};
        }
    }
    WAITL(0);
#pragma unroll 1
    for (int s = 16; s < 24; s += 2) {
        c[0] = v4f{0, 0, 0, 0}; c[1] = c[0];
        const LAS char* pa = lds + L_RED + 8192 + (l & 7) * 288 + fq * 16;
        stage(s, pa); stage(s + 1, pa + 128);
        ST16(rp, cu * 32768 + (l & 7) * 4096 + ((((s >> 1) + cu) & 3) * 256 + kh * 128 + cg * 32 + (fr >> 3) * 16 + fq * 4) * 4, 0, __builtin_bit_cast(v4u, fr < 8 ? c[0] : c[1]));
    }
    WAITV(0); RBAR();
}

static constexpr unsigned H2_OFF = 0, H1_OFF = 512 * 1024, H3_OFF = 768 * 1024, PR_OFF = 896 * 1024;
static constexpr size_t SLOT = 2u << 20;
static constexpr int NSLOT = 4;
struct Args {
    const bf16_t* x; const bf16_t* mem0; const bf16_t* Wq; const bf16_t* Wk; const bf16_t* Wv; const bf16_t* Wo;
    const bf16_t* w_r; const bf16_t* wg; const bf16_t* wu; const bf16_t* wd; const bf16_t* gw;
    char* qkv; char* mb; char* ob; float* gr; int* idx; float* part; bf16_t* out;
    char* ring; unsigned* ctl; unsigned* dbg;
    float per; unsigned per0, d1, dv, d2, dp, slack, thr, spare, wq, wx, pf;
};
typedef const __attribute__((address_space(4))) Args* KArgs;
DEV KArgs args() { KArgs p = (KArgs)__builtin_amdgcn_kernarg_segment_ptr(); asm volatile("" : "+s"(p)); return p; }
static constexpr unsigned QSZ = S_TOK * HD * 2;
typedef unsigned v16u __attribute__((ext_vector_type(16)));
typedef unsigned v32u __attribute__((ext_vector_type(32)));

DEV unsigned nearo(unsigned off) { return (off >> 13) * 16384u + (off & 8191u); }
DEV unsigned rt32() { u64 t; asm volatile("s_memrealtime %0\n\ts_waitcnt lgkmcnt(0)" : "=s"(t) :: "memory"); return (unsigned)t; }
DEV unsigned cyc32() { u64 t; asm volatile("s_memtime %0\n\ts_waitcnt lgkmcnt(0)" : "=s"(t) :: "memory"); return (unsigned)t; }
template <int R1, int R2, int R3> DEV v8u rd_w(const LAS char* p) {
    return __builtin_shufflevector(cat(trd(p), trd(p + R1)), cat(trd(p + R2), trd(p + R3)), 0, 1, 2, 3, 4, 5, 6, 7);
}
DEV v16u cat8(v8u a, v8u b) { return __builtin_shufflevector(a, b, 0, 1, 2, 3, 4, 5, 6, 7, 8, 9, 10, 11, 12, 13, 14, 15); }
DEV v32u cat16(v16u a, v16u b) { return __builtin_shufflevector(a, b, 0, 1, 2, 3, 4, 5, 6, 7, 8, 9, 10, 11, 12, 13, 14, 15, 16, 17, 18, 19, 20, 21, 22, 23, 24, 25, 26, 27, 28, 29, 30, 31); }
DEV v8u cat4(v4u a, v4u b) { return __builtin_shufflevector(a, b, 0, 1, 2, 3, 4, 5, 6, 7); }

static constexpr int L_PQK = 0, L_PV = 8704, L_P = 13824, L_VT = 15360, L_X = 16384, L_K = L_X + 9216, L_MISC = 28672, L_PF = 29696, L_STG = 32768, L_TOT = 163840;
static constexpr int X_FQ = 272, X_BLK = 1152;
#define ASM_A \
    "s_setprio 1\n" \
    "s_mov_b32 s76, 0\n" \
    "s_mov_b32 s99, 0x20000\n" \
    "s_mov_b32 s84, 0\n" \
    "s_mov_b32 s81, 0\n" \
    "s_mov_b32 s86, 0\n" \
    "s_mov_b32 s87, 0\n" \
    "s_mov_b32 s88, 0\n" \
    "s_mov_b32 s89, 0\n" \
    "s_mov_b32 s90, 0\n" \
    "s_mov_b32 s91, 0\n" \
    "s_mov_b32 s92, 0\n" \
    "s_mov_b32 s93, 0\n" \
    "s_mov_b32 s94, 0\n" \
    "s_mov_b32 s95, 0\n" \
    "s_mov_b32 s96, 0\n" \
    "s_mov_b32 s97, 0\n" \
    "s_mov_b32 s98, 0\n" \
    "ds_read_b32 v244, v237\n" \
    "s_waitcnt lgkmcnt(0)\n" \
    "v_readfirstlane_b32 s94, v244\n" \
    "s_mul_i32 s79, s94, s70\n" \
    "s_add_u32 s79, s79, s81\n" \
    "s_mov_b32 s77, s79\n" \
    "s_mul_i32 s96, s65, s71\n" \
    "s_lshr_b32 s96, s96, 4\n" \
    "v_mov_b64 v[208:209], 0\n" \
    "v_mov_b64 v[210:211], 0\n" \
    "v_mov_b64 v[212:213], 0\n" \
    "v_mov_b64 v[214:215], 0\n" \
    "v_mov_b64 v[216:217], 0\n" \
    "v_mov_b64 v[218:219], 0\n" \
    "v_mov_b64 v[220:221], 0\n" \
    "v_mov_b64 v[222:223], 0\n" \
    "v_mov_b64 v[224:225], 0\n" \
    "v_mov_b64 v[226:227], 0\n" \
    "v_mov_b64 v[228:229], 0\n" \
    "v_mov_b64 v[230:231], 0\n" \
    "v_mov_b32 v241, 1\n" \
    "s_branch L%=_Acomp\n" \
    "L%=_Aloop:\n" \
    "s_and_b32 s95, s94, 1\n" \
    "s_lshl_b32 s95, s95, 17\n" \
    "s_add_u32 s95, s95, s60\n" \
    "s_add_u32 s85, s95, 0x1000\n" \
    "s_branch L%=_Au2b\n" \
    "L%=_Au2a:\n" \
    "s_sleep 4\n" \
    "L%=_Au2b:\n" \
    "s_memrealtime vcc\n" \
    "s_add_u32 s80, s77, s64\n" \
    "s_add_u32 s80, s80, -3\n" \
    "s_waitcnt lgkmcnt(0)\n" \
    "s_sub_i32 s79, s80, vcc_lo\n" \
    "s_sub_u32 s79, s79, 40\n" \
    "s_cmp_lt_u32 s79, 0x400\n" \
    "s_cbranch_scc1 L%=_Au2a\n" \
    "s_memtime s[38:39]\n" \
    "s_memrealtime vcc\n" \
    "s_nop 4\n" \
    "s_memrealtime s[32:33]\n" \
    "s_nop 4\n" \
    "s_memrealtime s[34:35]\n" \
    "s_nop 4\n" \
    "s_memrealtime s[36:37]\n" \
    "s_add_u32 s80, s77, s64\n" \
    "s_lshl_b32 s80, s80, 2\n" \
    "s_waitcnt lgkmcnt(0)\n" \
    "s_sub_u32 s80, s80, vcc_lo\n" \
    "s_sub_u32 s80, s80, s32\n" \
    "s_sub_u32 s80, s80, s34\n" \
    "s_sub_u32 s80, s80, s36\n" \
    "s_mul_i32 s80, s80, s71\n" \
    "s_ashr_i32 s80, s80, 6\n" \
    "s_add_u32 s78, s80, s38\n" \
    "L%=_Au3a:\n" \
    "s_memtime s[38:39]\n" \
    "s_mul_i32 s80, -3, 23\n" \
    "s_add_u32 s80, s80, s78\n" \
    "s_waitcnt lgkmcnt(0)\n" \
    "s_sub_i32 s79, s80, s38\n" \
    "s_max_i32 s79, s79, 0\n" \
    "s_cmp_gt_u32 s79, 320\n" \
    "s_cbranch_scc1 L%=_Au3s\n" \
    ".p2align 4\n" \
    "L%=_Au3d:\n" \
    "s_sub_u32 s79, s79, 28\n" \
    "s_cbranch_scc0 L%=_Au3d\n" \
    "L%=_Au3e:\n" \
    "L%=_Apoll:\n" \
    "ds_read_b32 v244, v237 offset:4\n" \
    "s_waitcnt lgkmcnt(0)\n" \
    "v_readfirstlane_b32 s79, v244\n" \
    "s_cmp_lt_u32 s79, s76\n" \
    "s_cbranch_scc1 L%=_Askip\n" \
    "L%=_Ago:\n" \
    "buffer_load_dwordx4 v[192:195], v233, s[40:43], s95 offen sc1\n" \
    "s_nop 5\n" \
    "buffer_load_dwordx4 v[196:199], v233, s[40:43], s85 offen sc1\n" \
    "s_nop 5\n" \
    "buffer_load_dwordx4 v[200:203], v239, s[40:43], s95 offen sc1\n" \
    "s_nop 5\n" \
    "buffer_load_dwordx4 v[204:207], v239, s[40:43], s85 offen sc1\n" \
    "L%=_Acomp:\n" \
    "s_mov_b32 m0, s72\n" \
    "s_waitcnt vmcnt(3)\n" \
    "v_smfmac_f32_16x16x64_bf16 v[208:211], v[192:195], v[0:7], v232\n" \
    "v_smfmac_f32_16x16x64_bf16 v[212:215], v[192:195], v[8:15], v232\n" \
    "v_smfmac_f32_16x16x64_bf16 v[216:219], v[192:195], v[64:71], v232\n" \
    "v_smfmac_f32_16x16x64_bf16 v[220:223], v[192:195], v[72:79], v232\n" \
    "v_smfmac_f32_16x16x64_bf16 v[224:227], v[192:195], v[128:135], v232\n" \
    "s_waitcnt vmcnt(2)\n" \
    "v_smfmac_f32_16x16x64_bf16 v[208:211], v[196:199], v[16:23], v232\n" \
    "v_smfmac_f32_16x16x64_bf16 v[212:215], v[196:199], v[24:31], v232\n" \
    "v_smfmac_f32_16x16x64_bf16 v[216:219], v[196:199], v[80:87], v232\n" \
    "v_smfmac_f32_16x16x64_bf16 v[220:223], v[196:199], v[88:95], v232\n" \
    "v_smfmac_f32_16x16x64_bf16 v[228:231], v[192:195], v[136:143], v232\n" \
    "s_waitcnt vmcnt(1)\n" \
    "v_smfmac_f32_16x16x64_bf16 v[208:211], v[200:203], v[32:39], v232\n" \
    "v_smfmac_f32_16x16x64_bf16 v[212:215], v[200:203], v[40:47], v232\n" \
    "v_smfmac_f32_16x16x64_bf16 v[216:219], v[200:203], v[96:103], v232\n" \
    "v_smfmac_f32_16x16x64_bf16 v[220:223], v[200:203], v[104:111], v232\n" \
    "v_smfmac_f32_16x16x64_bf16 v[224:227], v[196:199], v[144:151], v232\n" \
    "s_waitcnt vmcnt(0)\n" \
    "v_smfmac_f32_16x16x64_bf16 v[208:211], v[204:207], v[48:55], v232\n" \
    "v_smfmac_f32_16x16x64_bf16 v[212:215], v[204:207], v[56:63], v232\n" \
    "v_smfmac_f32_16x16x64_bf16 v[216:219], v[204:207], v[112:119], v232\n" \
    "v_add_f32 v208, v208, v210\n" \
    "v_smfmac_f32_16x16x64_bf16 v[220:223], v[204:207], v[120:127], v232\n" \
    "v_add_f32 v209, v209, v211\n" \
    "v_cmp_u_f32 vcc, v208, v209\n" \
    "v_add_f32 v212, v212, v214\n" \
    "v_add_f32 v213, v213, v215\n" \
    "v_add_f32 v216, v216, v218\n" \
    "v_add_f32 v217, v217, v219\n" \
    "s_nop 1\n" \
    "v_add_f32 v220, v220, v222\n" \
    "v_add_f32 v221, v221, v223\n" \
    "v_smfmac_f32_16x16x64_bf16 v[224:227], v[200:203], v[160:167], v232\n" \
    "s_cbranch_vccnz L%=_Aslow\n" \
    "L%=_Aok:\n" \
    "ds_write_addtid_b32 v208 offset:0\n" \
    "ds_write_addtid_b32 v209 offset:544\n" \
    "v_smfmac_f32_16x16x64_bf16 v[228:231], v[196:199], v[152:159], v232\n" \
    "ds_write_addtid_b32 v216 offset:256\n" \
    "ds_write_addtid_b32 v217 offset:800\n" \
    "v_smfmac_f32_16x16x64_bf16 v[224:227], v[204:207], v[176:183], v232\n" \
    "ds_write_addtid_b32 v212 offset:1088\n" \
    "ds_write_addtid_b32 v213 offset:1632\n" \
    "v_smfmac_f32_16x16x64_bf16 v[228:231], v[200:203], v[168:175], v232\n" \
    "ds_write_addtid_b32 v220 offset:1344\n" \
    "ds_write_addtid_b32 v221 offset:1888\n" \
    "ds_write_b32 v236, v241 offset:32\n" \
    "v_smfmac_f32_16x16x64_bf16 v[228:231], v[204:207], v[184:191], v232\n" \
    "s_barrier\n" \
    "s_mov_b32 m0, s73\n" \
    "v_add_f32 v224, v224, v226\n" \
    "v_add_f32 v225, v225, v227\n" \
    "ds_write_addtid_b32 v224 offset:8704\n" \
    "ds_write_addtid_b32 v225 offset:8992\n" \
    "s_nop 1\n" \
    "v_add_f32 v228, v228, v230\n" \
    "v_add_f32 v229, v229, v231\n" \
    "ds_write_addtid_b32 v228 offset:9344\n" \
    "ds_write_addtid_b32 v229 offset:9632\n" \
    "ds_write_b32 v236, v241 offset:16\n" \
    "v_mov_b64 v[208:209], 0\n" \
    "v_mov_b64 v[210:211], 0\n" \
    "v_mov_b64 v[212:213], 0\n" \
    "v_mov_b64 v[214:215], 0\n" \
    "v_mov_b64 v[216:217], 0\n" \
    "v_mov_b64 v[218:219], 0\n" \
    "v_mov_b64 v[220:221], 0\n" \
    "v_mov_b64 v[222:223], 0\n" \
    "v_mov_b64 v[224:225], 0\n" \
    "v_mov_b64 v[226:227], 0\n" \
    "v_mov_b64 v[228:229], 0\n" \
    "v_mov_b64 v[230:231], 0\n" \
    "v_add_u32 v241, 1, v241\n" \
    "ds_read_b32 v244, v237\n" \
    "s_waitcnt lgkmcnt(0)\n" \
    "v_readfirstlane_b32 s94, v244\n" \
    "s_mul_i32 s79, s94, s70\n" \
    "s_add_u32 s79, s79, s81\n" \
    "s_mov_b32 s77, s79\n" \
    "s_barrier\n" \
    "ds_read_b64 v[244:245], v237 offset:64\n" \
    "s_mov_b32 s79, s60\n" \
    "s_mov_b32 s60, s61\n" \
    "s_mov_b32 s61, s62\n" \
    "s_mov_b32 s62, s63\n" \
    "s_mov_b32 s63, s79\n" \
    "s_add_u32 s76, s76, 1\n" \
    "s_add_u32 s77, s77, s65\n" \
    "s_add_u32 s84, s84, 0x20000\n" \
    "s_add_u32 s81, s81, s65\n" \
    "s_waitcnt lgkmcnt(0)\n" \
    "v_readfirstlane_b32 s65, v244\n" \
    "v_readfirstlane_b32 s71, v245\n" \
    "s_mul_i32 s96, s65, s71\n" \
    "s_lshr_b32 s96, s96, 4\n" \
    "s_cmp_lt_u32 s76, 64\n" \
    "s_cbranch_scc1 L%=_Aloop\n" \
    "s_branch L%=_end\n" \
    "L%=_Aslow:\n" \
    "s_nop 7\n" \
    "s_nop 0\n" \
    "v_max_u32 v244, v192, v193\n" \
    "v_max3_u32 v244, v244, v194, v195\n" \
    "v_max3_u32 v244, v244, v196, v197\n" \
    "v_max3_u32 v244, v244, v198, v199\n" \
    "v_max3_u32 v244, v244, v200, v201\n" \
    "v_max3_u32 v244, v244, v202, v203\n" \
    "v_max3_u32 v244, v244, v204, v205\n" \
    "v_max3_u32 v244, v244, v206, v207\n" \
    "v_cmp_eq_u32 vcc, -1, v244\n" \
    "s_cbranch_vccz L%=_Aok\n" \
    "s_cmp_eq_u32 s99, 0\n" \
    "s_cbranch_scc1 L%=_Aok\n" \
    "s_sub_u32 s99, s99, 1\n" \
    "s_cmp_lg_u32 s99, 0\n" \
    "s_cbranch_scc1 L%=_Au5\n" \
    "v_mov_b32 v244, 1\n" \
    "buffer_store_dword v244, off, s[44:47], 0 sc1\n" \
    "L%=_Au5:\n" \
    "v_mov_b64 v[208:209], 0\n" \
    "v_mov_b64 v[210:211], 0\n" \
    "v_mov_b64 v[212:213], 0\n" \
    "v_mov_b64 v[214:215], 0\n" \
    "v_mov_b64 v[216:217], 0\n" \
    "v_mov_b64 v[218:219], 0\n" \
    "v_mov_b64 v[220:221], 0\n" \
    "v_mov_b64 v[222:223], 0\n" \
    "v_mov_b64 v[224:225], 0\n" \
    "v_mov_b64 v[226:227], 0\n" \
    "v_mov_b64 v[228:229], 0\n" \
    "v_mov_b64 v[230:231], 0\n" \
    "L%=_Ash:\n" \
    "s_memrealtime vcc\n" \
    "s_waitcnt lgkmcnt(0)\n" \
    "s_sub_u32 s80, vcc_lo, s64\n" \
    "s_sub_u32 s80, s80, 4\n" \
    "s_mul_i32 s32, s70, s71\n" \
    "s_lshr_b32 s32, s32, 4\n" \
    "L%=_Au6g:\n" \
    "s_add_u32 s77, s77, s70\n" \
    "s_add_u32 s78, s78, s32\n" \
    "s_add_u32 s94, s94, 1\n" \
    "s_sub_u32 s79, s80, s77\n" \
    "s_cmp_lt_u32 s79, 0x1000000\n" \
    "s_cbranch_scc1 L%=_Au6g\n" \
    "v_mov_b32 v244, s94\n" \
    "ds_max_u32 v237, v244\n" \
    "s_cmp_gt_i32 s79, -20\n" \
    "s_cbranch_scc1 L%=_Au6c\n" \
    "s_branch L%=_Au7b\n" \
    "L%=_Au7a:\n" \
    "s_sleep 4\n" \
    "L%=_Au7b:\n" \
    "s_memrealtime vcc\n" \
    "s_add_u32 s80, s77, s64\n" \
    "s_add_u32 s80, s80, -3\n" \
    "s_waitcnt lgkmcnt(0)\n" \
    "s_sub_i32 s79, s80, vcc_lo\n" \
    "s_sub_u32 s79, s79, 40\n" \
    "s_cmp_lt_u32 s79, 0x400\n" \
    "s_cbranch_scc1 L%=_Au7a\n" \
    "s_memtime s[38:39]\n" \
    "s_memrealtime vcc\n" \
    "s_nop 4\n" \
    "s_memrealtime s[32:33]\n" \
    "s_nop 4\n" \
    "s_memrealtime s[34:35]\n" \
    "s_nop 4\n" \
    "s_memrealtime s[36:37]\n" \
    "s_add_u32 s80, s77, s64\n" \
    "s_lshl_b32 s80, s80, 2\n" \
    "s_waitcnt lgkmcnt(0)\n" \
    "s_sub_u32 s80, s80, vcc_lo\n" \
    "s_sub_u32 s80, s80, s32\n" \
    "s_sub_u32 s80, s80, s34\n" \
    "s_sub_u32 s80, s80, s36\n" \
    "s_mul_i32 s80, s80, s71\n" \
    "s_ashr_i32 s80, s80, 6\n" \
    "s_add_u32 s78, s80, s38\n" \
    "L%=_Au6c:\n" \
    "s_and_b32 s95, s94, 1\n" \
    "s_lshl_b32 s95, s95, 17\n" \
    "s_add_u32 s95, s95, s60\n" \
    "s_add_u32 s85, s95, 0x1000\n" \
    "L%=_Au8a:\n" \
    "s_memtime s[38:39]\n" \
    "s_mul_i32 s80, -3, 23\n" \
    "s_add_u32 s80, s80, s78\n" \
    "s_waitcnt lgkmcnt(0)\n" \
    "s_sub_i32 s79, s80, s38\n" \
    "s_max_i32 s79, s79, 0\n" \
    "s_cmp_gt_u32 s79, 320\n" \
    "s_cbranch_scc1 L%=_Au8s\n" \
    ".p2align 4\n" \
    "L%=_Au8d:\n" \
    "s_sub_u32 s79, s79, 28\n" \
    "s_cbranch_scc0 L%=_Au8d\n" \
    "L%=_Au8e:\n" \
    "s_branch L%=_Apoll\n" \
    "L%=_Askip:\n" \
    "s_cmp_lt_u32 s99, 2\n" \
    "s_cbranch_scc1 L%=_Ago\n" \
    "s_sub_u32 s99, s99, 1\n" \
    "s_branch L%=_Ash\n" \
    "L%=_Au3s:\n" \
    "s_cmp_gt_u32 s79, 0x8000\n" \
    "s_cbranch_scc1 L%=_Au3e\n" \
    "s_sleep 2\n" \
    "s_branch L%=_Au3a\n" \
    "L%=_Au8s:\n" \
    "s_cmp_gt_u32 s79, 0x8000\n" \
    "s_cbranch_scc1 L%=_Au8e\n" \
    "s_sleep 2\n" \
    "s_branch L%=_Au8a\n" \
    "L%=_end:\n"
#define ASM_B \
    "s_and_b32 s79, s78, 3\n" \
    "s_cmp_eq_u32 s79, 1\n" \
    "s_cbranch_scc1 L%=_S\n" \
    "s_cmp_eq_u32 s79, 2\n" \
    "s_cbranch_scc1 L%=_R\n" \
    "s_cmp_eq_u32 s79, 3\n" \
    "s_cbranch_scc1 L%=_P\n" \
    "L%=_T:\n" \
    "s_mov_b32 s76, 0\n" \
    "s_mov_b32 s99, 0x20000\n" \
    "s_mov_b32 s84, 0\n" \
    "s_mov_b32 s81, 0\n" \
    "s_mov_b32 s86, 0\n" \
    "s_mov_b32 s87, 0\n" \
    "s_mov_b32 s88, 0\n" \
    "s_mov_b32 s89, 0\n" \
    "s_mov_b32 s90, 0\n" \
    "s_mov_b32 s91, 0\n" \
    "s_mov_b32 s92, 0\n" \
    "s_mov_b32 s93, 0\n" \
    "s_mov_b32 s94, 0\n" \
    "s_mov_b32 s95, 0\n" \
    "s_mov_b32 s96, 0\n" \
    "s_mov_b32 s97, 0\n" \
    "s_mov_b32 s98, 0\n" \
    "ds_read_b32 v216, v229\n" \
    "s_waitcnt lgkmcnt(0)\n" \
    "v_readfirstlane_b32 s94, v216\n" \
    "s_mul_i32 s79, s94, s70\n" \
    "s_add_u32 s79, s79, s81\n" \
    "s_mov_b32 s77, s79\n" \
    "s_mul_i32 s96, s65, s71\n" \
    "s_lshr_b32 s96, s96, 4\n" \
    "v_mov_b32 v96, 0x3f803f80\n" \
    "v_mov_b32 v97, 0x3f803f80\n" \
    "v_mov_b32 v98, 0x3f803f80\n" \
    "v_mov_b32 v99, 0x3f803f80\n" \
    "v_mov_b32 v100, 0x3f803f80\n" \
    "v_mov_b32 v101, 0x3f803f80\n" \
    "v_mov_b64 v[102:103], 0\n" \
    "v_mov_b64 v[64:65], 0\n" \
    "v_mov_b64 v[66:67], 0\n" \
    "v_mov_b64 v[68:69], 0\n" \
    "v_mov_b64 v[70:71], 0\n" \
    "v_mov_b64 v[72:73], 0\n" \
    "v_mov_b64 v[74:75], 0\n" \
    "v_mov_b64 v[76:77], 0\n" \
    "v_mov_b64 v[78:79], 0\n" \
    "v_mov_b64 v[80:81], 0\n" \
    "v_mov_b64 v[82:83], 0\n" \
    "v_mov_b64 v[84:85], 0\n" \
    "v_mov_b64 v[86:87], 0\n" \
    "v_mov_b64 v[88:89], 0\n" \
    "v_mov_b64 v[90:91], 0\n" \
    "v_mov_b64 v[92:93], 0\n" \
    "v_mov_b64 v[94:95], 0\n" \
    "v_mov_b64 v[192:193], 0\n" \
    "v_mov_b64 v[194:195], 0\n" \
    "L%=_Tloop:\n" \
    "ds_read_b64_tr_b16 v[104:105], v228 offset:16384\n" \
    "ds_read_b64_tr_b16 v[106:107], v228 offset:16416\n" \
    "ds_read_b64_tr_b16 v[108:109], v228 offset:18688\n" \
    "ds_read_b64_tr_b16 v[110:111], v228 offset:18720\n" \
    "ds_read_b64_tr_b16 v[112:113], v228 offset:20992\n" \
    "ds_read_b64_tr_b16 v[114:115], v228 offset:21024\n" \
    "ds_read_b64_tr_b16 v[116:117], v228 offset:23296\n" \
    "ds_read_b64_tr_b16 v[118:119], v228 offset:23328\n" \
    "ds_read_b64_tr_b16 v[120:121], v228 offset:16656\n" \
    "ds_read_b64_tr_b16 v[122:123], v228 offset:16688\n" \
    "ds_read_b64_tr_b16 v[124:125], v228 offset:18960\n" \
    "ds_read_b64_tr_b16 v[126:127], v228 offset:18992\n" \
    "ds_read_b64_tr_b16 v[128:129], v228 offset:21264\n" \
    "ds_read_b64_tr_b16 v[130:131], v228 offset:21296\n" \
    "ds_read_b64_tr_b16 v[132:133], v228 offset:23568\n" \
    "ds_read_b64_tr_b16 v[134:135], v228 offset:23600\n" \
    "s_waitcnt lgkmcnt(14)\n" \
    "v_mfma_f32_16x16x32_bf16 v[136:139], v[104:107], v[0:3], 0\n" \
    "v_mfma_f32_16x16x32_bf16 v[144:147], v[104:107], v[8:11], 0\n" \
    "v_mfma_f32_16x16x32_bf16 v[152:155], v[104:107], v[16:19], 0\n" \
    "v_mfma_f32_16x16x32_bf16 v[160:163], v[104:107], v[24:27], 0\n" \
    "s_waitcnt lgkmcnt(12)\n" \
    "v_mfma_f32_16x16x32_bf16 v[136:139], v[108:111], v[4:7], v[136:139]\n" \
    "v_mfma_f32_16x16x32_bf16 v[144:147], v[108:111], v[12:15], v[144:147]\n" \
    "v_mfma_f32_16x16x32_bf16 v[152:155], v[108:111], v[20:23], v[152:155]\n" \
    "v_mfma_f32_16x16x32_bf16 v[160:163], v[108:111], v[28:31], v[160:163]\n" \
    "s_waitcnt lgkmcnt(10)\n" \
    "v_mfma_f32_16x16x32_bf16 v[136:139], v[112:115], v[32:35], v[136:139]\n" \
    "v_mfma_f32_16x16x32_bf16 v[144:147], v[112:115], v[40:43], v[144:147]\n" \
    "v_mfma_f32_16x16x32_bf16 v[152:155], v[112:115], v[48:51], v[152:155]\n" \
    "v_mfma_f32_16x16x32_bf16 v[160:163], v[112:115], v[56:59], v[160:163]\n" \
    "s_waitcnt lgkmcnt(8)\n" \
    "v_mfma_f32_16x16x32_bf16 v[136:139], v[116:119], v[36:39], v[136:139]\n" \
    "v_mfma_f32_16x16x32_bf16 v[144:147], v[116:119], v[44:47], v[144:147]\n" \
    "v_mfma_f32_16x16x32_bf16 v[152:155], v[116:119], v[52:55], v[152:155]\n" \
    "v_mfma_f32_16x16x32_bf16 v[160:163], v[116:119], v[60:63], v[160:163]\n" \
    "s_waitcnt lgkmcnt(6)\n" \
    "v_mfma_f32_16x16x32_bf16 v[140:143], v[120:123], v[0:3], 0\n" \
    "v_mfma_f32_16x16x32_bf16 v[148:151], v[120:123], v[8:11], 0\n" \
    "v_mfma_f32_16x16x32_bf16 v[156:159], v[120:123], v[16:19], 0\n" \
    "v_mfma_f32_16x16x32_bf16 v[164:167], v[120:123], v[24:27], 0\n" \
    "s_waitcnt lgkmcnt(4)\n" \
    "v_mfma_f32_16x16x32_bf16 v[140:143], v[124:127], v[4:7], v[140:143]\n" \
    "v_mfma_f32_16x16x32_bf16 v[148:151], v[124:127], v[12:15], v[148:151]\n" \
    "v_mfma_f32_16x16x32_bf16 v[156:159], v[124:127], v[20:23], v[156:159]\n" \
    "v_mfma_f32_16x16x32_bf16 v[164:167], v[124:127], v[28:31], v[164:167]\n" \
    "s_waitcnt lgkmcnt(2)\n" \
    "v_mfma_f32_16x16x32_bf16 v[140:143], v[128:131], v[32:35], v[140:143]\n" \
    "v_mfma_f32_16x16x32_bf16 v[148:151], v[128:131], v[40:43], v[148:151]\n" \
    "v_mfma_f32_16x16x32_bf16 v[156:159], v[128:131], v[48:51], v[156:159]\n" \
    "v_mfma_f32_16x16x32_bf16 v[164:167], v[128:131], v[56:59], v[164:167]\n" \
    "s_waitcnt lgkmcnt(0)\n" \
    "v_mfma_f32_16x16x32_bf16 v[140:143], v[132:135], v[36:39], v[140:143]\n" \
    "v_mfma_f32_16x16x32_bf16 v[148:151], v[132:135], v[44:47], v[148:151]\n" \
    "v_mfma_f32_16x16x32_bf16 v[156:159], v[132:135], v[52:55], v[156:159]\n" \
    "v_mfma_f32_16x16x32_bf16 v[164:167], v[132:135], v[60:63], v[164:167]\n" \
    "v_cvt_pk_bf16_f32 v65, v136, v137\n" \
    "v_cvt_pk_bf16_f32 v68, v138, v139\n" \
    "v_cvt_pk_bf16_f32 v73, v144, v145\n" \
    "v_cvt_pk_bf16_f32 v76, v146, v147\n" \
    "v_cvt_pk_bf16_f32 v81, v152, v153\n" \
    "v_cvt_pk_bf16_f32 v84, v154, v155\n" \
    "v_cvt_pk_bf16_f32 v89, v160, v161\n" \
    "v_cvt_pk_bf16_f32 v92, v162, v163\n" \
    "v_cvt_pk_bf16_f32 v66, v142, v143\n" \
    "v_cvt_pk_bf16_f32 v67, v142, v143\n" \
    "v_cvt_pk_bf16_f32 v69, v140, v141\n" \
    "v_cvt_pk_bf16_f32 v74, v150, v151\n" \
    "v_cvt_pk_bf16_f32 v75, v150, v151\n" \
    "v_cvt_pk_bf16_f32 v77, v148, v149\n" \
    "v_cvt_pk_bf16_f32 v82, v158, v159\n" \
    "v_cvt_pk_bf16_f32 v83, v158, v159\n" \
    "v_cvt_pk_bf16_f32 v85, v156, v157\n" \
    "v_cvt_pk_bf16_f32 v90, v166, v167\n" \
    "v_cvt_pk_bf16_f32 v91, v166, v167\n" \
    "v_cvt_pk_bf16_f32 v93, v164, v165\n" \
    "v_permlane16_swap_b32 v66, v67\n" \
    "v_permlane16_swap_b32 v74, v75\n" \
    "v_permlane16_swap_b32 v82, v83\n" \
    "v_permlane16_swap_b32 v90, v91\n" \
    "v_mov_b64 v[176:177], 0\n" \
    "v_mov_b64 v[178:179], 0\n" \
    "v_mov_b64 v[180:181], 0\n" \
    "v_mov_b64 v[182:183], 0\n" \
    "v_mov_b64 v[184:185], 0\n" \
    "v_mov_b64 v[186:187], 0\n" \
    "v_mov_b64 v[188:189], 0\n" \
    "v_mov_b64 v[190:191], 0\n" \
    "v_mov_b64 v[196:197], 0\n" \
    "v_mov_b64 v[198:199], 0\n" \
    "v_mov_b64 v[200:201], 0\n" \
    "v_mov_b64 v[202:203], 0\n" \
    "v_mov_b64 v[204:205], 0\n" \
    "v_mov_b64 v[206:207], 0\n" \
    "v_mov_b64 v[208:209], 0\n" \
    "v_mov_b64 v[210:211], 0\n" \
    "v_mov_b64 v[212:213], 0\n" \
    "v_mov_b64 v[214:215], 0\n" \
    "s_add_u32 s85, s60, 0x1000\n" \
    "s_branch L%=_Tu2b\n" \
    "L%=_Tu2a:\n" \
    "s_sleep 4\n" \
    "L%=_Tu2b:\n" \
    "s_memrealtime vcc\n" \
    "s_add_u32 s80, s77, s64\n" \
    "s_add_u32 s80, s80, s69\n" \
    "s_waitcnt lgkmcnt(0)\n" \
    "s_sub_i32 s79, s80, vcc_lo\n" \
    "s_sub_u32 s79, s79, 40\n" \
    "s_cmp_lt_u32 s79, 0x400\n" \
    "s_cbranch_scc1 L%=_Tu2a\n" \
    "s_memtime s[38:39]\n" \
    "s_memrealtime vcc\n" \
    "s_nop 4\n" \
    "s_memrealtime s[32:33]\n" \
    "s_nop 4\n" \
    "s_memrealtime s[34:35]\n" \
    "s_nop 4\n" \
    "s_memrealtime s[36:37]\n" \
    "s_add_u32 s80, s77, s64\n" \
    "s_lshl_b32 s80, s80, 2\n" \
    "s_waitcnt lgkmcnt(0)\n" \
    "s_sub_u32 s80, s80, vcc_lo\n" \
    "s_sub_u32 s80, s80, s32\n" \
    "s_sub_u32 s80, s80, s34\n" \
    "s_sub_u32 s80, s80, s36\n" \
    "s_mul_i32 s80, s80, s71\n" \
    "s_ashr_i32 s80, s80, 6\n" \
    "s_add_u32 s78, s80, s38\n" \
    "v_mov_b32 v143, s76\n" \
    "s_barrier\n" \
    "s_cmp_eq_u32 s76, 63\n" \
    "s_cbranch_scc1 L%=_Tfin\n" \
    "ds_read_b32 v216, v229\n" \
    "s_waitcnt lgkmcnt(0)\n" \
    "v_readfirstlane_b32 s94, v216\n" \
    "s_mul_i32 s79, s94, s70\n" \
    "s_add_u32 s79, s79, s81\n" \
    "s_cmp_lg_u32 s79, s77\n" \
    "s_cbranch_scc1 L%=_Tu3s\n" \
    "L%=_Tu3k:\n" \
    "L%=_Tu4a:\n" \
    "s_memtime s[38:39]\n" \
    "s_mul_hi_u32 s80, s66, s96\n" \
    "s_add_u32 s80, s80, s78\n" \
    "s_waitcnt lgkmcnt(0)\n" \
    "s_sub_i32 s79, s80, s38\n" \
    "s_max_i32 s79, s79, 0\n" \
    "s_cmp_gt_u32 s79, 320\n" \
    "s_cbranch_scc1 L%=_Tu4s\n" \
    ".p2align 4\n" \
    "L%=_Tu4d:\n" \
    "s_sub_u32 s79, s79, 28\n" \
    "s_cbranch_scc0 L%=_Tu4d\n" \
    "L%=_Tu4e:\n" \
    "buffer_load_dwordx4 v[104:107], v230, s[40:43], s60 offen offset:0 sc1\n" \
    "buffer_load_dwordx2 v[120:121], v231, s[40:43], s60 offen offset:0 sc1\n" \
    "buffer_load_dwordx4 v[108:111], v230, s[40:43], s60 offen offset:2048 sc1\n" \
    "buffer_load_dwordx2 v[122:123], v231, s[40:43], s60 offen offset:2048 sc1\n" \
    "buffer_load_dwordx4 v[112:115], v230, s[40:43], s85 offen offset:0 sc1\n" \
    "buffer_load_dwordx2 v[124:125], v231, s[40:43], s85 offen offset:0 sc1\n" \
    "buffer_load_dwordx4 v[116:119], v230, s[40:43], s85 offen offset:2048 sc1\n" \
    "buffer_load_dwordx2 v[126:127], v231, s[40:43], s85 offen offset:2048 sc1\n" \
    "s_setprio 3\n" \
    "buffer_load_dwordx4 v[168:171], v225, s[40:43], s60 offen sc1\n" \
    "buffer_load_dwordx4 v[172:175], v225, s[40:43], s85 offen sc1\n" \
    "L%=_Tsc:\n" \
    "s_waitcnt vmcnt(6)\n" \
    "v_pk_add_f32 v[128:129], v[104:105], v[108:109]\n" \
    "v_pk_add_f32 v[130:131], v[106:107], v[110:111]\n" \
    "v_pk_add_f32 v[132:133], v[120:121], v[122:123]\n" \
    "s_waitcnt vmcnt(4)\n" \
    "v_pk_add_f32 v[128:129], v[128:129], v[112:113]\n" \
    "v_pk_add_f32 v[130:131], v[130:131], v[114:115]\n" \
    "v_pk_add_f32 v[132:133], v[132:133], v[124:125]\n" \
    "s_waitcnt vmcnt(2)\n" \
    "v_pk_add_f32 v[128:129], v[128:129], v[116:117]\n" \
    "v_pk_add_f32 v[130:131], v[130:131], v[118:119]\n" \
    "v_pk_add_f32 v[132:133], v[132:133], v[126:127]\n" \
    "v_max3_f32 v136, v128, v129, v130\n" \
    "v_max3_f32 v137, v131, v132, v133\n" \
    "v_max_f32 v136, v136, v137\n" \
    "v_fma_f32 v141, v131, v132, v133\n" \
    "v_fma_f32 v140, v128, v129, v130\n" \
    "v_max_f32_dpp v138, v136, v136 row_ror:8 row_mask:0xf bank_mask:0xf\n" \
    "v_max_f32_dpp v139, v136, v136 row_ror:8 row_mask:0xf bank_mask:0xf\n" \
    "s_waitcnt vmcnt(0)\n" \
    "v_smfmac_f32_16x16x64_bf16 v[176:179], v[168:171], v[0:7], v224\n" \
    "v_cmp_u_f32 vcc, v140, v141\n" \
    "v_permlane16_swap_b32 v138, v139\n" \
    "v_max_f32 v136, v138, v139\n" \
    "v_max_f32 v137, v138, v139\n" \
    "v_smfmac_f32_16x16x64_bf16 v[180:183], v[168:171], v[8:15], v224\n" \
    "s_cbranch_vccnz L%=_Tslow\n" \
    "L%=_Tok:\n" \
    "v_permlane32_swap_b32 v136, v137\n" \
    "v_max_f32 v136, v136, v137\n" \
    "v_mul_f32 v136, s72, v136\n" \
    "v_pk_fma_f32 v[128:129], v[128:129], s[72:73], v[136:137] op_sel_hi:[1,1,0] neg_lo:[0,0,1] neg_hi:[0,0,1]\n" \
    "v_pk_fma_f32 v[132:133], v[132:133], s[72:73], v[136:137] op_sel_hi:[1,1,0] neg_lo:[0,0,1] neg_hi:[0,0,1]\n" \
    "v_pk_fma_f32 v[130:131], v[130:131], s[72:73], v[136:137] op_sel_hi:[1,1,0] neg_lo:[0,0,1] neg_hi:[0,0,1]\n" \
    "v_exp_f32 v128, v128\n" \
    "v_exp_f32 v129, v129\n" \
    "v_exp_f32 v132, v132\n" \
    "v_exp_f32 v133, v133\n" \
    "v_exp_f32 v130, v130\n" \
    "v_exp_f32 v131, v131\n" \
    "v_cvt_pk_bf16_f32 v140, v128, v129\n" \
    "v_cvt_pk_bf16_f32 v141, v132, v133\n" \
    "v_cvt_pk_bf16_f32 v142, v130, v131\n" \
    "ds_write_b128 v232, v[140:143]\n" \
    "s_barrier\n" \
    "ds_read_b128 v[192:195], v227\n" \
    "s_waitcnt vmcnt(0)\n" \
    "v_smfmac_f32_16x16x64_bf16 v[176:179], v[172:175], v[32:39], v224\n" \
    "L%=_Tvc:\n" \
    "v_smfmac_f32_16x16x64_bf16 v[180:183], v[172:175], v[40:47], v224\n" \
    "v_smfmac_f32_16x16x64_bf16 v[184:187], v[168:171], v[16:23], v224\n" \
    "v_add_f32 v176, v176, v178\n" \
    "v_smfmac_f32_16x16x64_bf16 v[188:191], v[168:171], v[24:31], v224\n" \
    "v_add_f32 v177, v177, v179\n" \
    "v_smfmac_f32_16x16x64_bf16 v[184:187], v[172:175], v[48:55], v224\n" \
    "v_cmp_u_f32 vcc, v176, v177\n" \
    "v_smfmac_f32_16x16x64_bf16 v[188:191], v[172:175], v[56:63], v224\n" \
    "s_cbranch_vccnz L%=_Tvslow\n" \
    "L%=_Tvok:\n" \
    "v_cvt_pk_bf16_f32 v64, v176, v177\n" \
    "v_add_f32 v180, v180, v182\n" \
    "s_waitcnt lgkmcnt(0)\n" \
    "v_smfmac_f32_16x16x64_bf16 v[212:215], v[192:195], v[96:103], v224\n" \
    "v_add_f32 v181, v181, v183\n" \
    "v_smfmac_f32_16x16x64_bf16 v[196:199], v[192:195], v[64:71], v224\n" \
    "v_cvt_pk_bf16_f32 v72, v180, v181\n" \
    "v_add_f32 v184, v184, v186\n" \
    "v_add_f32 v185, v185, v187\n" \
    "v_smfmac_f32_16x16x64_bf16 v[200:203], v[192:195], v[72:79], v224\n" \
    "v_cvt_pk_bf16_f32 v80, v184, v185\n" \
    "v_add_f32 v188, v188, v190\n" \
    "v_add_f32 v189, v189, v191\n" \
    "v_smfmac_f32_16x16x64_bf16 v[204:207], v[192:195], v[80:87], v224\n" \
    "v_cvt_pk_bf16_f32 v88, v188, v189\n" \
    "v_add_f32 v212, v212, v214\n" \
    "v_add_f32 v213, v213, v215\n" \
    "v_smfmac_f32_16x16x64_bf16 v[208:211], v[192:195], v[88:95], v224\n" \
    "v_rcp_f32 v220, v212\n" \
    "v_rcp_f32 v221, v213\n" \
    "v_add_f32 v196, v196, v198\n" \
    "v_add_f32 v197, v197, v199\n" \
    "v_pk_add_f32 v[200:201], v[200:201], v[202:203]\n" \
    "v_pk_add_f32 v[204:205], v[204:205], v[206:207]\n" \
    "s_nop 1\n" \
    "v_pk_add_f32 v[208:209], v[208:209], v[210:211]\n" \
    "v_pk_mul_f32 v[196:197], v[196:197], v[220:221]\n" \
    "v_pk_mul_f32 v[200:201], v[200:201], v[220:221]\n" \
    "v_pk_mul_f32 v[204:205], v[204:205], v[220:221]\n" \
    "v_pk_mul_f32 v[208:209], v[208:209], v[220:221]\n" \
    "v_cvt_pk_bf16_f32 v216, v196, v200\n" \
    "v_cvt_pk_bf16_f32 v217, v204, v208\n" \
    "v_cvt_pk_bf16_f32 v218, v197, v201\n" \
    "v_cvt_pk_bf16_f32 v219, v205, v209\n" \
    "buffer_store_dwordx4 v[216:219], v226, s[40:43], s60 offen sc1\n" \
    "s_setprio 0\n" \
    "ds_read_b64 v[220:221], v229 offset:64\n" \
    "s_mov_b32 s79, s60\n" \
    "s_mov_b32 s60, s61\n" \
    "s_mov_b32 s61, s62\n" \
    "s_mov_b32 s62, s63\n" \
    "s_mov_b32 s63, s79\n" \
    "s_add_u32 s76, s76, 1\n" \
    "s_add_u32 s77, s77, s65\n" \
    "s_add_u32 s84, s84, 0x20000\n" \
    "s_add_u32 s81, s81, s65\n" \
    "s_waitcnt lgkmcnt(0)\n" \
    "v_readfirstlane_b32 s65, v220\n" \
    "v_readfirstlane_b32 s71, v221\n" \
    "s_mul_i32 s96, s65, s71\n" \
    "s_lshr_b32 s96, s96, 4\n" \
    "s_branch L%=_Tloop\n" \
    "L%=_Tslow:\n" \
    "s_nop 7\n" \
    "s_nop 0\n" \
    "v_max_u32 v138, v104, v105\n" \
    "v_max3_u32 v138, v138, v106, v107\n" \
    "v_max3_u32 v138, v138, v108, v109\n" \
    "v_max3_u32 v138, v138, v110, v111\n" \
    "v_max3_u32 v138, v138, v112, v113\n" \
    "v_max3_u32 v138, v138, v114, v115\n" \
    "v_max3_u32 v138, v138, v116, v117\n" \
    "v_max3_u32 v138, v138, v118, v119\n" \
    "v_max3_u32 v138, v138, v120, v121\n" \
    "v_max3_u32 v138, v138, v122, v123\n" \
    "v_max3_u32 v138, v138, v124, v125\n" \
    "v_max3_u32 v138, v138, v126, v127\n" \
    "v_cmp_eq_u32 vcc, -1, v138\n" \
    "s_cbranch_vccz L%=_Tok\n" \
    "s_cmp_eq_u32 s99, 0\n" \
    "s_cbranch_scc1 L%=_Tok\n" \
    "s_sub_u32 s99, s99, 1\n" \
    "s_cmp_lg_u32 s99, 0\n" \
    "s_cbranch_scc1 L%=_Tu5\n" \
    "v_mov_b32 v138, 1\n" \
    "buffer_store_dword v138, off, s[44:47], 0 sc1\n" \
    "L%=_Tu5:\n" \
    "s_sleep 1\n" \
    "buffer_load_dwordx4 v[104:107], v230, s[40:43], s60 offen offset:0 sc1\n" \
    "buffer_load_dwordx2 v[120:121], v231, s[40:43], s60 offen offset:0 sc1\n" \
    "buffer_load_dwordx4 v[108:111], v230, s[40:43], s60 offen offset:2048 sc1\n" \
    "buffer_load_dwordx2 v[122:123], v231, s[40:43], s60 offen offset:2048 sc1\n" \
    "buffer_load_dwordx4 v[112:115], v230, s[40:43], s85 offen offset:0 sc1\n" \
    "buffer_load_dwordx2 v[124:125], v231, s[40:43], s85 offen offset:0 sc1\n" \
    "buffer_load_dwordx4 v[116:119], v230, s[40:43], s85 offen offset:2048 sc1\n" \
    "buffer_load_dwordx2 v[126:127], v231, s[40:43], s85 offen offset:2048 sc1\n" \
    "buffer_load_dwordx4 v[168:171], v225, s[40:43], s60 offen sc1\n" \
    "buffer_load_dwordx4 v[172:175], v225, s[40:43], s85 offen sc1\n" \
    "v_mov_b64 v[176:177], 0\n" \
    "v_mov_b64 v[178:179], 0\n" \
    "v_mov_b64 v[180:181], 0\n" \
    "v_mov_b64 v[182:183], 0\n" \
    "v_mov_b64 v[184:185], 0\n" \
    "v_mov_b64 v[186:187], 0\n" \
    "v_mov_b64 v[188:189], 0\n" \
    "v_mov_b64 v[190:191], 0\n" \
    "s_branch L%=_Tsc\n" \
    "L%=_Tvslow:\n" \
    "s_nop 7\n" \
    "s_nop 0\n" \
    "v_max_u32 v136, v168, v169\n" \
    "v_max3_u32 v136, v136, v170, v171\n" \
    "v_max3_u32 v136, v136, v172, v173\n" \
    "v_max3_u32 v136, v136, v174, v175\n" \
    "v_cmp_eq_u32 vcc, -1, v136\n" \
    "s_cbranch_vccz L%=_Tvok\n" \
    "s_cmp_eq_u32 s99, 0\n" \
    "s_cbranch_scc1 L%=_Tvok\n" \
    "s_sub_u32 s99, s99, 1\n" \
    "s_cmp_lg_u32 s99, 0\n" \
    "s_cbranch_scc1 L%=_Tu6\n" \
    "v_mov_b32 v136, 1\n" \
    "buffer_store_dword v136, off, s[44:47], 0 sc1\n" \
    "L%=_Tu6:\n" \
    "s_sleep 1\n" \
    "buffer_load_dwordx4 v[168:171], v225, s[40:43], s60 offen sc1\n" \
    "buffer_load_dwordx4 v[172:175], v225, s[40:43], s85 offen sc1\n" \
    "v_mov_b64 v[176:177], 0\n" \
    "v_mov_b64 v[178:179], 0\n" \
    "v_mov_b64 v[180:181], 0\n" \
    "v_mov_b64 v[182:183], 0\n" \
    "v_mov_b64 v[184:185], 0\n" \
    "v_mov_b64 v[186:187], 0\n" \
    "v_mov_b64 v[188:189], 0\n" \
    "v_mov_b64 v[190:191], 0\n" \
    "v_mov_b64 v[196:197], 0\n" \
    "v_mov_b64 v[198:199], 0\n" \
    "v_mov_b64 v[200:201], 0\n" \
    "v_mov_b64 v[202:203], 0\n" \
    "v_mov_b64 v[204:205], 0\n" \
    "v_mov_b64 v[206:207], 0\n" \
    "v_mov_b64 v[208:209], 0\n" \
    "v_mov_b64 v[210:211], 0\n" \
    "v_mov_b64 v[212:213], 0\n" \
    "v_mov_b64 v[214:215], 0\n" \
    "s_waitcnt vmcnt(0)\n" \
    "v_smfmac_f32_16x16x64_bf16 v[176:179], v[168:171], v[0:7], v224\n" \
    "v_smfmac_f32_16x16x64_bf16 v[180:183], v[168:171], v[8:15], v224\n" \
    "v_smfmac_f32_16x16x64_bf16 v[176:179], v[172:175], v[32:39], v224\n" \
    "s_nop 7\n" \
    "s_nop 0\n" \
    "s_branch L%=_Tvc\n" \
    "L%=_Tfin:\n" \
    "s_barrier\n" \
    "s_branch L%=_end\n" \
    "L%=_Tu3s:\n" \
    "s_mov_b32 s77, s79\n" \
    "s_memtime s[38:39]\n" \
    "s_memrealtime vcc\n" \
    "s_nop 4\n" \
    "s_memrealtime s[32:33]\n" \
    "s_nop 4\n" \
    "s_memrealtime s[34:35]\n" \
    "s_nop 4\n" \
    "s_memrealtime s[36:37]\n" \
    "s_add_u32 s80, s77, s64\n" \
    "s_lshl_b32 s80, s80, 2\n" \
    "s_waitcnt lgkmcnt(0)\n" \
    "s_sub_u32 s80, s80, vcc_lo\n" \
    "s_sub_u32 s80, s80, s32\n" \
    "s_sub_u32 s80, s80, s34\n" \
    "s_sub_u32 s80, s80, s36\n" \
    "s_mul_i32 s80, s80, s71\n" \
    "s_ashr_i32 s80, s80, 6\n" \
    "s_add_u32 s78, s80, s38\n" \
    "s_branch L%=_Tu3k\n" \
    "L%=_Tu4s:\n" \
    "s_cmp_gt_u32 s79, 0x8000\n" \
    "s_cbranch_scc1 L%=_Tu4e\n" \
    "s_sleep 2\n" \
    "s_branch L%=_Tu4a\n" \
    "L%=_S:\n" \
    "s_mov_b32 s76, 0\n" \
    "s_mov_b32 s99, 0x20000\n" \
    "s_mov_b32 s84, 0\n" \
    "s_mov_b32 s81, 0\n" \
    "s_mov_b32 s86, 0\n" \
    "s_mov_b32 s87, 0\n" \
    "s_mov_b32 s88, 0\n" \
    "s_mov_b32 s89, 0\n" \
    "s_mov_b32 s90, 0\n" \
    "s_mov_b32 s91, 0\n" \
    "s_mov_b32 s92, 0\n" \
    "s_mov_b32 s93, 0\n" \
    "s_mov_b32 s94, 0\n" \
    "s_mov_b32 s95, 0\n" \
    "s_mov_b32 s96, 0\n" \
    "s_mov_b32 s97, 0\n" \
    "s_mov_b32 s98, 0\n" \
    "ds_read_b32 v216, v229\n" \
    "s_waitcnt lgkmcnt(0)\n" \
    "v_readfirstlane_b32 s94, v216\n" \
    "s_mul_i32 s79, s94, s70\n" \
    "s_add_u32 s79, s79, s81\n" \
    "s_mov_b32 s77, s79\n" \
    "s_mul_i32 s96, s65, s71\n" \
    "s_lshr_b32 s96, s96, 4\n" \
    "v_mov_b32 v96, 0x3f803f80\n" \
    "v_mov_b32 v97, 0x3f803f80\n" \
    "v_mov_b32 v98, 0x3f803f80\n" \
    "v_mov_b32 v99, 0x3f803f80\n" \
    "v_mov_b32 v100, 0x3f803f80\n" \
    "v_mov_b32 v101, 0x3f803f80\n" \
    "v_mov_b64 v[102:103], 0\n" \
    "v_mov_b64 v[64:65], 0\n" \
    "v_mov_b64 v[66:67], 0\n" \
    "v_mov_b64 v[68:69], 0\n" \
    "v_mov_b64 v[70:71], 0\n" \
    "v_mov_b64 v[72:73], 0\n" \
    "v_mov_b64 v[74:75], 0\n" \
    "v_mov_b64 v[76:77], 0\n" \
    "v_mov_b64 v[78:79], 0\n" \
    "v_mov_b64 v[80:81], 0\n" \
    "v_mov_b64 v[82:83], 0\n" \
    "v_mov_b64 v[84:85], 0\n" \
    "v_mov_b64 v[86:87], 0\n" \
    "v_mov_b64 v[88:89], 0\n" \
    "v_mov_b64 v[90:91], 0\n" \
    "v_mov_b64 v[92:93], 0\n" \
    "v_mov_b64 v[94:95], 0\n" \
    "v_mov_b64 v[192:193], 0\n" \
    "v_mov_b64 v[194:195], 0\n" \
    "L%=_Sloop:\n" \
    "ds_read_b64_tr_b16 v[104:105], v228 offset:16384\n" \
    "ds_read_b64_tr_b16 v[106:107], v228 offset:16416\n" \
    "ds_read_b64_tr_b16 v[108:109], v228 offset:18688\n" \
    "ds_read_b64_tr_b16 v[110:111], v228 offset:18720\n" \
    "ds_read_b64_tr_b16 v[112:113], v228 offset:20992\n" \
    "ds_read_b64_tr_b16 v[114:115], v228 offset:21024\n" \
    "ds_read_b64_tr_b16 v[116:117], v228 offset:23296\n" \
    "ds_read_b64_tr_b16 v[118:119], v228 offset:23328\n" \
    "ds_read_b64_tr_b16 v[120:121], v228 offset:16656\n" \
    "ds_read_b64_tr_b16 v[122:123], v228 offset:16688\n" \
    "ds_read_b64_tr_b16 v[124:125], v228 offset:18960\n" \
    "ds_read_b64_tr_b16 v[126:127], v228 offset:18992\n" \
    "ds_read_b64_tr_b16 v[128:129], v228 offset:21264\n" \
    "ds_read_b64_tr_b16 v[130:131], v228 offset:21296\n" \
    "ds_read_b64_tr_b16 v[132:133], v228 offset:23568\n" \
    "ds_read_b64_tr_b16 v[134:135], v228 offset:23600\n" \
    "s_waitcnt lgkmcnt(14)\n" \
    "v_mfma_f32_16x16x32_bf16 v[136:139], v[104:107], v[0:3], 0\n" \
    "v_mfma_f32_16x16x32_bf16 v[144:147], v[104:107], v[8:11], 0\n" \
    "v_mfma_f32_16x16x32_bf16 v[152:155], v[104:107], v[16:19], 0\n" \
    "v_mfma_f32_16x16x32_bf16 v[160:163], v[104:107], v[24:27], 0\n" \
    "s_waitcnt lgkmcnt(12)\n" \
    "v_mfma_f32_16x16x32_bf16 v[136:139], v[108:111], v[4:7], v[136:139]\n" \
    "v_mfma_f32_16x16x32_bf16 v[144:147], v[108:111], v[12:15], v[144:147]\n" \
    "v_mfma_f32_16x16x32_bf16 v[152:155], v[108:111], v[20:23], v[152:155]\n" \
    "v_mfma_f32_16x16x32_bf16 v[160:163], v[108:111], v[28:31], v[160:163]\n" \
    "s_waitcnt lgkmcnt(10)\n" \
    "v_mfma_f32_16x16x32_bf16 v[136:139], v[112:115], v[32:35], v[136:139]\n" \
    "v_mfma_f32_16x16x32_bf16 v[144:147], v[112:115], v[40:43], v[144:147]\n" \
    "v_mfma_f32_16x16x32_bf16 v[152:155], v[112:115], v[48:51], v[152:155]\n" \
    "v_mfma_f32_16x16x32_bf16 v[160:163], v[112:115], v[56:59], v[160:163]\n" \
    "s_waitcnt lgkmcnt(8)\n" \
    "v_mfma_f32_16x16x32_bf16 v[136:139], v[116:119], v[36:39], v[136:139]\n" \
    "v_mfma_f32_16x16x32_bf16 v[144:147], v[116:119], v[44:47], v[144:147]\n" \
    "v_mfma_f32_16x16x32_bf16 v[152:155], v[116:119], v[52:55], v[152:155]\n" \
    "v_mfma_f32_16x16x32_bf16 v[160:163], v[116:119], v[60:63], v[160:163]\n" \
    "s_waitcnt lgkmcnt(6)\n" \
    "v_mfma_f32_16x16x32_bf16 v[140:143], v[120:123], v[0:3], 0\n" \
    "v_mfma_f32_16x16x32_bf16 v[148:151], v[120:123], v[8:11], 0\n" \
    "v_mfma_f32_16x16x32_bf16 v[156:159], v[120:123], v[16:19], 0\n" \
    "v_mfma_f32_16x16x32_bf16 v[164:167], v[120:123], v[24:27], 0\n" \
    "s_waitcnt lgkmcnt(4)\n" \
    "v_mfma_f32_16x16x32_bf16 v[140:143], v[124:127], v[4:7], v[140:143]\n" \
    "v_mfma_f32_16x16x32_bf16 v[148:151], v[124:127], v[12:15], v[148:151]\n" \
    "v_mfma_f32_16x16x32_bf16 v[156:159], v[124:127], v[20:23], v[156:159]\n" \
    "v_mfma_f32_16x16x32_bf16 v[164:167], v[124:127], v[28:31], v[164:167]\n" \
    "s_waitcnt lgkmcnt(2)\n" \
    "v_mfma_f32_16x16x32_bf16 v[140:143], v[128:131], v[32:35], v[140:143]\n" \
    "v_mfma_f32_16x16x32_bf16 v[148:151], v[128:131], v[40:43], v[148:151]\n" \
    "v_mfma_f32_16x16x32_bf16 v[156:159], v[128:131], v[48:51], v[156:159]\n" \
    "v_mfma_f32_16x16x32_bf16 v[164:167], v[128:131], v[56:59], v[164:167]\n" \
    "s_waitcnt lgkmcnt(0)\n" \
    "v_mfma_f32_16x16x32_bf16 v[140:143], v[132:135], v[36:39], v[140:143]\n" \
    "v_mfma_f32_16x16x32_bf16 v[148:151], v[132:135], v[44:47], v[148:151]\n" \
    "v_mfma_f32_16x16x32_bf16 v[156:159], v[132:135], v[52:55], v[156:159]\n" \
    "v_mfma_f32_16x16x32_bf16 v[164:167], v[132:135], v[60:63], v[164:167]\n" \
    "v_cvt_pk_bf16_f32 v65, v136, v137\n" \
    "v_cvt_pk_bf16_f32 v68, v138, v139\n" \
    "v_cvt_pk_bf16_f32 v73, v144, v145\n" \
    "v_cvt_pk_bf16_f32 v76, v146, v147\n" \
    "v_cvt_pk_bf16_f32 v81, v152, v153\n" \
    "v_cvt_pk_bf16_f32 v84, v154, v155\n" \
    "v_cvt_pk_bf16_f32 v89, v160, v161\n" \
    "v_cvt_pk_bf16_f32 v92, v162, v163\n" \
    "v_cvt_pk_bf16_f32 v66, v142, v143\n" \
    "v_cvt_pk_bf16_f32 v67, v142, v143\n" \
    "v_cvt_pk_bf16_f32 v69, v140, v141\n" \
    "v_cvt_pk_bf16_f32 v74, v150, v151\n" \
    "v_cvt_pk_bf16_f32 v75, v150, v151\n" \
    "v_cvt_pk_bf16_f32 v77, v148, v149\n" \
    "v_cvt_pk_bf16_f32 v82, v158, v159\n" \
    "v_cvt_pk_bf16_f32 v83, v158, v159\n" \
    "v_cvt_pk_bf16_f32 v85, v156, v157\n" \
    "v_cvt_pk_bf16_f32 v90, v166, v167\n" \
    "v_cvt_pk_bf16_f32 v91, v166, v167\n" \
    "v_cvt_pk_bf16_f32 v93, v164, v165\n" \
    "v_permlane16_swap_b32 v66, v67\n" \
    "v_permlane16_swap_b32 v74, v75\n" \
    "v_permlane16_swap_b32 v82, v83\n" \
    "v_permlane16_swap_b32 v90, v91\n" \
    "ds_read_b128 v[240:243], v234 offset:25600\n" \
    "ds_read_b128 v[244:247], v234 offset:26624\n" \
    "v_mov_b64 v[176:177], 0\n" \
    "v_mov_b64 v[178:179], 0\n" \
    "v_mov_b64 v[180:181], 0\n" \
    "v_mov_b64 v[182:183], 0\n" \
    "v_mov_b64 v[184:185], 0\n" \
    "v_mov_b64 v[186:187], 0\n" \
    "v_mov_b64 v[188:189], 0\n" \
    "v_mov_b64 v[190:191], 0\n" \
    "v_mov_b64 v[196:197], 0\n" \
    "v_mov_b64 v[198:199], 0\n" \
    "v_mov_b64 v[200:201], 0\n" \
    "v_mov_b64 v[202:203], 0\n" \
    "v_mov_b64 v[204:205], 0\n" \
    "v_mov_b64 v[206:207], 0\n" \
    "v_mov_b64 v[208:209], 0\n" \
    "v_mov_b64 v[210:211], 0\n" \
    "v_mov_b64 v[212:213], 0\n" \
    "v_mov_b64 v[214:215], 0\n" \
    "s_add_u32 s85, s60, 0x1000\n" \
    "s_branch L%=_Su2b\n" \
    "L%=_Su2a:\n" \
    "s_sleep 4\n" \
    "L%=_Su2b:\n" \
    "s_memrealtime vcc\n" \
    "s_add_u32 s80, s77, s64\n" \
    "s_add_u32 s80, s80, s69\n" \
    "s_waitcnt lgkmcnt(0)\n" \
    "s_sub_i32 s79, s80, vcc_lo\n" \
    "s_sub_u32 s79, s79, 40\n" \
    "s_cmp_lt_u32 s79, 0x400\n" \
    "s_cbranch_scc1 L%=_Su2a\n" \
    "s_memtime s[38:39]\n" \
    "s_memrealtime vcc\n" \
    "s_nop 4\n" \
    "s_memrealtime s[32:33]\n" \
    "s_nop 4\n" \
    "s_memrealtime s[34:35]\n" \
    "s_nop 4\n" \
    "s_memrealtime s[36:37]\n" \
    "s_add_u32 s80, s77, s64\n" \
    "s_lshl_b32 s80, s80, 2\n" \
    "s_waitcnt lgkmcnt(0)\n" \
    "s_sub_u32 s80, s80, vcc_lo\n" \
    "s_sub_u32 s80, s80, s32\n" \
    "s_sub_u32 s80, s80, s34\n" \
    "s_sub_u32 s80, s80, s36\n" \
    "s_mul_i32 s80, s80, s71\n" \
    "s_ashr_i32 s80, s80, 6\n" \
    "s_add_u32 s78, s80, s38\n" \
    "L%=_Su3a:\n" \
    "s_memtime s[38:39]\n" \
    "s_mul_i32 s80, s69, 23\n" \
    "s_add_u32 s80, s80, s78\n" \
    "s_waitcnt lgkmcnt(0)\n" \
    "s_sub_i32 s79, s80, s38\n" \
    "s_max_i32 s79, s79, 0\n" \
    "s_cmp_gt_u32 s79, 320\n" \
    "s_cbranch_scc1 L%=_Su3s\n" \
    ".p2align 4\n" \
    "L%=_Su3d:\n" \
    "s_sub_u32 s79, s79, 28\n" \
    "s_cbranch_scc0 L%=_Su3d\n" \
    "L%=_Su3e:\n" \
    "s_sub_u32 s79, s84, 0x20000\n" \
    "s_cselect_b32 s79, 0x40000000, s79\n" \
    "buffer_store_dwordx4 v[236:239], v233, s[48:51], s79 offen sc1\n" \
    "s_setprio 3\n" \
    "s_barrier\n" \
    "L%=_Srd:\n" \
    "ds_read_b128 v[136:139], v229 offset:32\n" \
    "ds_read_b128 v[104:107], v230 offset:0\n" \
    "ds_read_b128 v[108:111], v230 offset:2176\n" \
    "ds_read_b128 v[112:115], v230 offset:4352\n" \
    "ds_read_b128 v[116:119], v230 offset:6528\n" \
    "ds_read_b128 v[120:123], v230 offset:1088\n" \
    "ds_read_b128 v[124:127], v230 offset:3264\n" \
    "ds_read_b128 v[128:131], v230 offset:5440\n" \
    "ds_read_b128 v[132:135], v230 offset:7616\n" \
    "s_waitcnt lgkmcnt(8)\n" \
    "v_min3_u32 v136, v136, v137, v138\n" \
    "v_min_u32 v136, v136, v139\n" \
    "v_cmp_ge_u32 vcc, s76, v136\n" \
    "s_cbranch_vccnz L%=_Srd\n" \
    "s_waitcnt lgkmcnt(6)\n" \
    "v_pk_add_f32 v[104:105], v[104:105], v[108:109]\n" \
    "v_pk_add_f32 v[106:107], v[106:107], v[110:111]\n" \
    "s_waitcnt lgkmcnt(4)\n" \
    "v_pk_add_f32 v[112:113], v[112:113], v[116:117]\n" \
    "v_pk_add_f32 v[114:115], v[114:115], v[118:119]\n" \
    "v_pk_add_f32 v[104:105], v[104:105], v[112:113]\n" \
    "v_pk_add_f32 v[106:107], v[106:107], v[114:115]\n" \
    "v_cvt_pk_bf16_f32 v236, v104, v105\n" \
    "v_cvt_pk_bf16_f32 v237, v106, v107\n" \
    "s_waitcnt lgkmcnt(2)\n" \
    "v_pk_add_f32 v[120:121], v[120:121], v[124:125]\n" \
    "v_pk_add_f32 v[122:123], v[122:123], v[126:127]\n" \
    "s_waitcnt lgkmcnt(0)\n" \
    "v_pk_add_f32 v[128:129], v[128:129], v[132:133]\n" \
    "v_pk_add_f32 v[130:131], v[130:131], v[134:135]\n" \
    "v_pk_add_f32 v[120:121], v[120:121], v[128:129]\n" \
    "v_pk_add_f32 v[122:123], v[122:123], v[130:131]\n" \
    "v_cvt_pk_bf16_f32 v238, v120, v121\n" \
    "v_cvt_pk_bf16_f32 v239, v122, v123\n" \
    "s_nop 1\n" \
    "v_mfma_f32_16x16x32_bf16 v[140:143], v[240:243], v[236:239], 0\n" \
    "v_mfma_f32_16x16x32_bf16 v[144:147], v[244:247], v[236:239], 0\n" \
    "v_mfma_f32_16x16x32_bf16 v[148:151], v[236:239], v[236:239], 0\n" \
    "buffer_store_dwordx4 v[140:143], v231, s[40:43], s60 offen sc1\n" \
    "s_nop 2\n" \
    "buffer_store_dwordx4 v[144:147], v231, s[40:43], s60 offen offset:512 sc1\n" \
    "s_nop 2\n" \
    "buffer_store_dwordx4 v[148:151], v232, s[40:43], s60 offen sc1\n" \
    "s_cmp_eq_u32 s76, 63\n" \
    "s_cbranch_scc1 L%=_Sfin\n" \
    "ds_read_b32 v216, v229\n" \
    "s_waitcnt lgkmcnt(0)\n" \
    "v_readfirstlane_b32 s94, v216\n" \
    "s_mul_i32 s79, s94, s70\n" \
    "s_add_u32 s79, s79, s81\n" \
    "s_cmp_lg_u32 s79, s77\n" \
    "s_cbranch_scc1 L%=_Su4s\n" \
    "L%=_Su4k:\n" \
    "L%=_Su5a:\n" \
    "s_memtime s[38:39]\n" \
    "s_mul_hi_u32 s80, s67, s96\n" \
    "s_add_u32 s80, s80, s78\n" \
    "s_waitcnt lgkmcnt(0)\n" \
    "s_sub_i32 s79, s80, s38\n" \
    "s_max_i32 s79, s79, 0\n" \
    "s_cmp_gt_u32 s79, 320\n" \
    "s_cbranch_scc1 L%=_Su5s\n" \
    ".p2align 4\n" \
    "L%=_Su5d:\n" \
    "s_sub_u32 s79, s79, 28\n" \
    "s_cbranch_scc0 L%=_Su5d\n" \
    "L%=_Su5e:\n" \
    "buffer_load_dwordx4 v[168:171], v225, s[40:43], s60 offen sc1\n" \
    "buffer_load_dwordx4 v[172:175], v225, s[40:43], s85 offen sc1\n" \
    "L%=_Svc:\n" \
    "s_waitcnt vmcnt(1)\n" \
    "v_smfmac_f32_16x16x64_bf16 v[176:179], v[168:171], v[0:7], v224\n" \
    "v_smfmac_f32_16x16x64_bf16 v[180:183], v[168:171], v[8:15], v224\n" \
    "s_waitcnt vmcnt(0)\n" \
    "v_smfmac_f32_16x16x64_bf16 v[176:179], v[172:175], v[32:39], v224\n" \
    "v_smfmac_f32_16x16x64_bf16 v[180:183], v[172:175], v[40:47], v224\n" \
    "v_smfmac_f32_16x16x64_bf16 v[184:187], v[168:171], v[16:23], v224\n" \
    "v_smfmac_f32_16x16x64_bf16 v[188:191], v[168:171], v[24:31], v224\n" \
    "v_add_f32 v176, v176, v178\n" \
    "v_smfmac_f32_16x16x64_bf16 v[184:187], v[172:175], v[48:55], v224\n" \
    "v_add_f32 v177, v177, v179\n" \
    "v_smfmac_f32_16x16x64_bf16 v[188:191], v[172:175], v[56:63], v224\n" \
    "v_cmp_u_f32 vcc, v176, v177\n" \
    "v_cvt_pk_bf16_f32 v64, v176, v177\n" \
    "v_add_f32 v180, v180, v182\n" \
    "v_add_f32 v181, v181, v183\n" \
    "v_cvt_pk_bf16_f32 v72, v180, v181\n" \
    "s_cbranch_vccnz L%=_Svslow\n" \
    "L%=_Svok:\n" \
    "v_add_f32 v184, v184, v186\n" \
    "v_add_f32 v185, v185, v187\n" \
    "v_cvt_pk_bf16_f32 v80, v184, v185\n" \
    "v_add_f32 v188, v188, v190\n" \
    "v_add_f32 v189, v189, v191\n" \
    "v_cvt_pk_bf16_f32 v88, v188, v189\n" \
    "s_barrier\n" \
    "L%=_Sfr:\n" \
    "ds_read_b128 v[192:195], v227\n" \
    "s_waitcnt lgkmcnt(0)\n" \
    "v_smfmac_f32_16x16x64_bf16 v[212:215], v[192:195], v[96:103], v224\n" \
    "v_cmp_ne_u32 vcc, s76, v195\n" \
    "v_smfmac_f32_16x16x64_bf16 v[196:199], v[192:195], v[64:71], v224\n" \
    "s_cbranch_vccnz L%=_Sfs\n" \
    "v_smfmac_f32_16x16x64_bf16 v[200:203], v[192:195], v[72:79], v224\n" \
    "v_smfmac_f32_16x16x64_bf16 v[204:207], v[192:195], v[80:87], v224\n" \
    "v_add_f32 v212, v212, v214\n" \
    "v_smfmac_f32_16x16x64_bf16 v[208:211], v[192:195], v[88:95], v224\n" \
    "v_add_f32 v213, v213, v215\n" \
    "v_rcp_f32 v220, v212\n" \
    "v_rcp_f32 v221, v213\n" \
    "v_add_f32 v196, v196, v198\n" \
    "v_add_f32 v197, v197, v199\n" \
    "v_pk_add_f32 v[200:201], v[200:201], v[202:203]\n" \
    "v_pk_add_f32 v[204:205], v[204:205], v[206:207]\n" \
    "s_nop 0\n" \
    "v_pk_add_f32 v[208:209], v[208:209], v[210:211]\n" \
    "v_pk_mul_f32 v[196:197], v[196:197], v[220:221]\n" \
    "v_pk_mul_f32 v[200:201], v[200:201], v[220:221]\n" \
    "v_pk_mul_f32 v[204:205], v[204:205], v[220:221]\n" \
    "v_pk_mul_f32 v[208:209], v[208:209], v[220:221]\n" \
    "v_cvt_pk_bf16_f32 v216, v196, v200\n" \
    "v_cvt_pk_bf16_f32 v217, v204, v208\n" \
    "v_cvt_pk_bf16_f32 v218, v197, v201\n" \
    "v_cvt_pk_bf16_f32 v219, v205, v209\n" \
    "buffer_store_dwordx4 v[216:219], v226, s[40:43], s60 offen sc1\n" \
    "s_setprio 0\n" \
    "ds_read_b64 v[220:221], v229 offset:64\n" \
    "s_mov_b32 s79, s60\n" \
    "s_mov_b32 s60, s61\n" \
    "s_mov_b32 s61, s62\n" \
    "s_mov_b32 s62, s63\n" \
    "s_mov_b32 s63, s79\n" \
    "s_add_u32 s76, s76, 1\n" \
    "s_add_u32 s77, s77, s65\n" \
    "s_add_u32 s84, s84, 0x20000\n" \
    "s_add_u32 s81, s81, s65\n" \
    "s_waitcnt lgkmcnt(0)\n" \
    "v_readfirstlane_b32 s65, v220\n" \
    "v_readfirstlane_b32 s71, v221\n" \
    "s_mul_i32 s96, s65, s71\n" \
    "s_lshr_b32 s96, s96, 4\n" \
    "s_branch L%=_Sloop\n" \
    "L%=_Sfin:\n" \
    "buffer_store_dwordx4 v[236:239], v233, s[48:51], s84 offen sc1\n" \
    "s_waitcnt vmcnt(0)\n" \
    "s_barrier\n" \
    "s_branch L%=_end\n" \
    "L%=_Su3s:\n" \
    "s_cmp_gt_u32 s79, 0x8000\n" \
    "s_cbranch_scc1 L%=_Su3e\n" \
    "s_sleep 2\n" \
    "s_branch L%=_Su3a\n" \
    "L%=_Su4s:\n" \
    "s_mov_b32 s77, s79\n" \
    "s_memtime s[38:39]\n" \
    "s_memrealtime vcc\n" \
    "s_nop 4\n" \
    "s_memrealtime s[32:33]\n" \
    "s_nop 4\n" \
    "s_memrealtime s[34:35]\n" \
    "s_nop 4\n" \
    "s_memrealtime s[36:37]\n" \
    "s_add_u32 s80, s77, s64\n" \
    "s_lshl_b32 s80, s80, 2\n" \
    "s_waitcnt lgkmcnt(0)\n" \
    "s_sub_u32 s80, s80, vcc_lo\n" \
    "s_sub_u32 s80, s80, s32\n" \
    "s_sub_u32 s80, s80, s34\n" \
    "s_sub_u32 s80, s80, s36\n" \
    "s_mul_i32 s80, s80, s71\n" \
    "s_ashr_i32 s80, s80, 6\n" \
    "s_add_u32 s78, s80, s38\n" \
    "s_branch L%=_Su4k\n" \
    "L%=_Su5s:\n" \
    "s_cmp_gt_u32 s79, 0x8000\n" \
    "s_cbranch_scc1 L%=_Su5e\n" \
    "s_sleep 2\n" \
    "s_branch L%=_Su5a\n" \
    "L%=_Svslow:\n" \
    "s_nop 7\n" \
    "s_nop 0\n" \
    "v_max_u32 v216, v168, v169\n" \
    "v_max3_u32 v216, v216, v170, v171\n" \
    "v_max3_u32 v216, v216, v172, v173\n" \
    "v_max3_u32 v216, v216, v174, v175\n" \
    "v_cmp_eq_u32 vcc, -1, v216\n" \
    "s_cbranch_vccz L%=_Svok\n" \
    "s_cmp_eq_u32 s99, 0\n" \
    "s_cbranch_scc1 L%=_Svok\n" \
    "s_sub_u32 s99, s99, 1\n" \
    "s_cmp_lg_u32 s99, 0\n" \
    "s_cbranch_scc1 L%=_Su6\n" \
    "v_mov_b32 v216, 1\n" \
    "buffer_store_dword v216, off, s[44:47], 0 sc1\n" \
    "L%=_Su6:\n" \
    "s_sleep 1\n" \
    "buffer_load_dwordx4 v[168:171], v225, s[40:43], s60 offen sc1\n" \
    "buffer_load_dwordx4 v[172:175], v225, s[40:43], s85 offen sc1\n" \
    "v_mov_b64 v[176:177], 0\n" \
    "v_mov_b64 v[178:179], 0\n" \
    "v_mov_b64 v[180:181], 0\n" \
    "v_mov_b64 v[182:183], 0\n" \
    "v_mov_b64 v[184:185], 0\n" \
    "v_mov_b64 v[186:187], 0\n" \
    "v_mov_b64 v[188:189], 0\n" \
    "v_mov_b64 v[190:191], 0\n" \
    "s_waitcnt vmcnt(0)\n" \
    "s_branch L%=_Svc\n" \
    "L%=_Sfs:\n" \
    "s_nop 7\n" \
    "s_nop 0\n" \
    "v_mov_b64 v[196:197], 0\n" \
    "v_mov_b64 v[198:199], 0\n" \
    "v_mov_b64 v[200:201], 0\n" \
    "v_mov_b64 v[202:203], 0\n" \
    "v_mov_b64 v[204:205], 0\n" \
    "v_mov_b64 v[206:207], 0\n" \
    "v_mov_b64 v[208:209], 0\n" \
    "v_mov_b64 v[210:211], 0\n" \
    "v_mov_b64 v[212:213], 0\n" \
    "v_mov_b64 v[214:215], 0\n" \
    "s_branch L%=_Sfr\n" \
    "L%=_R:\n" \
    "s_mov_b32 s76, 0\n" \
    "s_mov_b32 s99, 0x20000\n" \
    "s_mov_b32 s84, 0\n" \
    "s_mov_b32 s81, 0\n" \
    "s_mov_b32 s86, 0\n" \
    "s_mov_b32 s87, 0\n" \
    "s_mov_b32 s88, 0\n" \
    "s_mov_b32 s89, 0\n" \
    "s_mov_b32 s90, 0\n" \
    "s_mov_b32 s91, 0\n" \
    "s_mov_b32 s92, 0\n" \
    "s_mov_b32 s93, 0\n" \
    "s_mov_b32 s94, 0\n" \
    "s_mov_b32 s95, 0\n" \
    "s_mov_b32 s96, 0\n" \
    "s_mov_b32 s97, 0\n" \
    "s_mov_b32 s98, 0\n" \
    "ds_read_b32 v216, v229\n" \
    "s_waitcnt lgkmcnt(0)\n" \
    "v_readfirstlane_b32 s94, v216\n" \
    "s_mul_i32 s79, s94, s70\n" \
    "s_add_u32 s79, s79, s81\n" \
    "s_mov_b32 s77, s79\n" \
    "s_mul_i32 s96, s65, s71\n" \
    "s_lshr_b32 s96, s96, 4\n" \
    "v_mov_b32 v96, 0x3f803f80\n" \
    "v_mov_b32 v97, 0x3f803f80\n" \
    "v_mov_b32 v98, 0x3f803f80\n" \
    "v_mov_b32 v99, 0x3f803f80\n" \
    "v_mov_b32 v100, 0x3f803f80\n" \
    "v_mov_b32 v101, 0x3f803f80\n" \
    "v_mov_b64 v[102:103], 0\n" \
    "v_mov_b64 v[64:65], 0\n" \
    "v_mov_b64 v[66:67], 0\n" \
    "v_mov_b64 v[68:69], 0\n" \
    "v_mov_b64 v[70:71], 0\n" \
    "v_mov_b64 v[72:73], 0\n" \
    "v_mov_b64 v[74:75], 0\n" \
    "v_mov_b64 v[76:77], 0\n" \
    "v_mov_b64 v[78:79], 0\n" \
    "v_mov_b64 v[80:81], 0\n" \
    "v_mov_b64 v[82:83], 0\n" \
    "v_mov_b64 v[84:85], 0\n" \
    "v_mov_b64 v[86:87], 0\n" \
    "v_mov_b64 v[88:89], 0\n" \
    "v_mov_b64 v[90:91], 0\n" \
    "v_mov_b64 v[92:93], 0\n" \
    "v_mov_b64 v[94:95], 0\n" \
    "v_mov_b64 v[192:193], 0\n" \
    "v_mov_b64 v[194:195], 0\n" \
    "v_mov_b32 v222, v236\n" \
    "v_mov_b32 v223, 0\n" \
    "ds_read_b64_tr_b16 v[104:105], v228 offset:16384\n" \
    "ds_read_b64_tr_b16 v[106:107], v228 offset:16416\n" \
    "ds_read_b64_tr_b16 v[108:109], v228 offset:18688\n" \
    "ds_read_b64_tr_b16 v[110:111], v228 offset:18720\n" \
    "ds_read_b64_tr_b16 v[112:113], v228 offset:20992\n" \
    "ds_read_b64_tr_b16 v[114:115], v228 offset:21024\n" \
    "ds_read_b64_tr_b16 v[116:117], v228 offset:23296\n" \
    "ds_read_b64_tr_b16 v[118:119], v228 offset:23328\n" \
    "ds_read_b64_tr_b16 v[120:121], v228 offset:16656\n" \
    "ds_read_b64_tr_b16 v[122:123], v228 offset:16688\n" \
    "ds_read_b64_tr_b16 v[124:125], v228 offset:18960\n" \
    "ds_read_b64_tr_b16 v[126:127], v228 offset:18992\n" \
    "ds_read_b64_tr_b16 v[128:129], v228 offset:21264\n" \
    "ds_read_b64_tr_b16 v[130:131], v228 offset:21296\n" \
    "ds_read_b64_tr_b16 v[132:133], v228 offset:23568\n" \
    "ds_read_b64_tr_b16 v[134:135], v228 offset:23600\n" \
    "s_waitcnt lgkmcnt(14)\n" \
    "v_mfma_f32_16x16x32_bf16 v[136:139], v[104:107], v[0:3], 0\n" \
    "v_mfma_f32_16x16x32_bf16 v[144:147], v[104:107], v[8:11], 0\n" \
    "v_mfma_f32_16x16x32_bf16 v[152:155], v[104:107], v[16:19], 0\n" \
    "v_mfma_f32_16x16x32_bf16 v[160:163], v[104:107], v[24:27], 0\n" \
    "s_waitcnt lgkmcnt(12)\n" \
    "v_mfma_f32_16x16x32_bf16 v[136:139], v[108:111], v[4:7], v[136:139]\n" \
    "v_mfma_f32_16x16x32_bf16 v[144:147], v[108:111], v[12:15], v[144:147]\n" \
    "v_mfma_f32_16x16x32_bf16 v[152:155], v[108:111], v[20:23], v[152:155]\n" \
    "v_mfma_f32_16x16x32_bf16 v[160:163], v[108:111], v[28:31], v[160:163]\n" \
    "s_waitcnt lgkmcnt(10)\n" \
    "v_mfma_f32_16x16x32_bf16 v[136:139], v[112:115], v[32:35], v[136:139]\n" \
    "v_mfma_f32_16x16x32_bf16 v[144:147], v[112:115], v[40:43], v[144:147]\n" \
    "v_mfma_f32_16x16x32_bf16 v[152:155], v[112:115], v[48:51], v[152:155]\n" \
    "v_mfma_f32_16x16x32_bf16 v[160:163], v[112:115], v[56:59], v[160:163]\n" \
    "s_waitcnt lgkmcnt(8)\n" \
    "v_mfma_f32_16x16x32_bf16 v[136:139], v[116:119], v[36:39], v[136:139]\n" \
    "v_mfma_f32_16x16x32_bf16 v[144:147], v[116:119], v[44:47], v[144:147]\n" \
    "v_mfma_f32_16x16x32_bf16 v[152:155], v[116:119], v[52:55], v[152:155]\n" \
    "v_mfma_f32_16x16x32_bf16 v[160:163], v[116:119], v[60:63], v[160:163]\n" \
    "s_waitcnt lgkmcnt(6)\n" \
    "v_mfma_f32_16x16x32_bf16 v[140:143], v[120:123], v[0:3], 0\n" \
    "v_mfma_f32_16x16x32_bf16 v[148:151], v[120:123], v[8:11], 0\n" \
    "v_mfma_f32_16x16x32_bf16 v[156:159], v[120:123], v[16:19], 0\n" \
    "v_mfma_f32_16x16x32_bf16 v[164:167], v[120:123], v[24:27], 0\n" \
    "s_waitcnt lgkmcnt(4)\n" \
    "v_mfma_f32_16x16x32_bf16 v[140:143], v[124:127], v[4:7], v[140:143]\n" \
    "v_mfma_f32_16x16x32_bf16 v[148:151], v[124:127], v[12:15], v[148:151]\n" \
    "v_mfma_f32_16x16x32_bf16 v[156:159], v[124:127], v[20:23], v[156:159]\n" \
    "v_mfma_f32_16x16x32_bf16 v[164:167], v[124:127], v[28:31], v[164:167]\n" \
    "s_waitcnt lgkmcnt(2)\n" \
    "v_mfma_f32_16x16x32_bf16 v[140:143], v[128:131], v[32:35], v[140:143]\n" \
    "v_mfma_f32_16x16x32_bf16 v[148:151], v[128:131], v[40:43], v[148:151]\n" \
    "v_mfma_f32_16x16x32_bf16 v[156:159], v[128:131], v[48:51], v[156:159]\n" \
    "v_mfma_f32_16x16x32_bf16 v[164:167], v[128:131], v[56:59], v[164:167]\n" \
    "s_waitcnt lgkmcnt(0)\n" \
    "v_mfma_f32_16x16x32_bf16 v[140:143], v[132:135], v[36:39], v[140:143]\n" \
    "v_mfma_f32_16x16x32_bf16 v[148:151], v[132:135], v[44:47], v[148:151]\n" \
    "v_mfma_f32_16x16x32_bf16 v[156:159], v[132:135], v[52:55], v[156:159]\n" \
    "v_mfma_f32_16x16x32_bf16 v[164:167], v[132:135], v[60:63], v[164:167]\n" \
    "v_cvt_pk_bf16_f32 v65, v136, v137\n" \
    "v_cvt_pk_bf16_f32 v68, v138, v139\n" \
    "v_cvt_pk_bf16_f32 v73, v144, v145\n" \
    "v_cvt_pk_bf16_f32 v76, v146, v147\n" \
    "v_cvt_pk_bf16_f32 v81, v152, v153\n" \
    "v_cvt_pk_bf16_f32 v84, v154, v155\n" \
    "v_cvt_pk_bf16_f32 v89, v160, v161\n" \
    "v_cvt_pk_bf16_f32 v92, v162, v163\n" \
    "v_cvt_pk_bf16_f32 v66, v142, v143\n" \
    "v_cvt_pk_bf16_f32 v67, v142, v143\n" \
    "v_cvt_pk_bf16_f32 v69, v140, v141\n" \
    "v_cvt_pk_bf16_f32 v74, v150, v151\n" \
    "v_cvt_pk_bf16_f32 v75, v150, v151\n" \
    "v_cvt_pk_bf16_f32 v77, v148, v149\n" \
    "v_cvt_pk_bf16_f32 v82, v158, v159\n" \
    "v_cvt_pk_bf16_f32 v83, v158, v159\n" \
    "v_cvt_pk_bf16_f32 v85, v156, v157\n" \
    "v_cvt_pk_bf16_f32 v90, v166, v167\n" \
    "v_cvt_pk_bf16_f32 v91, v166, v167\n" \
    "v_cvt_pk_bf16_f32 v93, v164, v165\n" \
    "v_permlane16_swap_b32 v66, v67\n" \
    "v_permlane16_swap_b32 v74, v75\n" \
    "v_permlane16_swap_b32 v82, v83\n" \
    "v_permlane16_swap_b32 v90, v91\n" \
    "L%=_Rloop:\n" \
    "v_mov_b64 v[176:177], 0\n" \
    "v_mov_b64 v[178:179], 0\n" \
    "v_mov_b64 v[180:181], 0\n" \
    "v_mov_b64 v[182:183], 0\n" \
    "v_mov_b64 v[184:185], 0\n" \
    "v_mov_b64 v[186:187], 0\n" \
    "v_mov_b64 v[188:189], 0\n" \
    "v_mov_b64 v[190:191], 0\n" \
    "v_mov_b64 v[196:197], 0\n" \
    "v_mov_b64 v[198:199], 0\n" \
    "v_mov_b64 v[200:201], 0\n" \
    "v_mov_b64 v[202:203], 0\n" \
    "v_mov_b64 v[204:205], 0\n" \
    "v_mov_b64 v[206:207], 0\n" \
    "v_mov_b64 v[208:209], 0\n" \
    "v_mov_b64 v[210:211], 0\n" \
    "v_mov_b64 v[212:213], 0\n" \
    "v_mov_b64 v[214:215], 0\n" \
    "s_add_u32 s85, s60, 0x1000\n" \
    "s_branch L%=_Ru2b\n" \
    "L%=_Ru2a:\n" \
    "s_sleep 4\n" \
    "L%=_Ru2b:\n" \
    "s_memrealtime vcc\n" \
    "s_add_u32 s80, s77, s64\n" \
    "s_add_u32 s80, s80, s69\n" \
    "s_waitcnt lgkmcnt(0)\n" \
    "s_sub_i32 s79, s80, vcc_lo\n" \
    "s_sub_u32 s79, s79, 40\n" \
    "s_cmp_lt_u32 s79, 0x400\n" \
    "s_cbranch_scc1 L%=_Ru2a\n" \
    "s_memtime s[38:39]\n" \
    "s_memrealtime vcc\n" \
    "s_nop 4\n" \
    "s_memrealtime s[32:33]\n" \
    "s_nop 4\n" \
    "s_memrealtime s[34:35]\n" \
    "s_nop 4\n" \
    "s_memrealtime s[36:37]\n" \
    "s_add_u32 s80, s77, s64\n" \
    "s_lshl_b32 s80, s80, 2\n" \
    "s_waitcnt lgkmcnt(0)\n" \
    "s_sub_u32 s80, s80, vcc_lo\n" \
    "s_sub_u32 s80, s80, s32\n" \
    "s_sub_u32 s80, s80, s34\n" \
    "s_sub_u32 s80, s80, s36\n" \
    "s_mul_i32 s80, s80, s71\n" \
    "s_ashr_i32 s80, s80, 6\n" \
    "s_add_u32 s78, s80, s38\n" \
    "L%=_Ru3a:\n" \
    "s_memtime s[38:39]\n" \
    "s_mul_i32 s80, s69, 23\n" \
    "s_add_u32 s80, s80, s78\n" \
    "s_waitcnt lgkmcnt(0)\n" \
    "s_sub_i32 s79, s80, s38\n" \
    "s_max_i32 s79, s79, 0\n" \
    "s_cmp_gt_u32 s79, 320\n" \
    "s_cbranch_scc1 L%=_Ru3s\n" \
    ".p2align 4\n" \
    "L%=_Ru3d:\n" \
    "s_sub_u32 s79, s79, 28\n" \
    "s_cbranch_scc0 L%=_Ru3d\n" \
    "L%=_Ru3e:\n" \
    "s_sub_u32 s79, s84, 0x20000\n" \
    "s_cselect_b32 s79, 0x40000000, s79\n" \
    "buffer_store_dwordx4 v[236:239], v234, s[48:51], s79 offen sc1\n" \
    "s_setprio 3\n" \
    "s_barrier\n" \
    "s_nop 14\n" \
    "L%=_Rvs:\n" \
    "ds_read_b128 v[104:107], v229 offset:16\n" \
    "ds_read_b128 v[108:111], v230 offset:8704\n" \
    "ds_read_b128 v[112:115], v230 offset:9984\n" \
    "ds_read_b128 v[116:119], v230 offset:11264\n" \
    "ds_read_b128 v[120:123], v230 offset:12544\n" \
    "s_waitcnt lgkmcnt(4)\n" \
    "v_min3_u32 v104, v104, v105, v106\n" \
    "v_min_u32 v104, v104, v107\n" \
    "s_nop 0\n" \
    "v_readfirstlane_b32 s79, v104\n" \
    "s_cmp_le_u32 s79, s76\n" \
    "s_cbranch_scc1 L%=_Rvs\n" \
    "s_waitcnt lgkmcnt(2)\n" \
    "v_pk_add_f32 v[108:109], v[108:109], v[112:113]\n" \
    "v_pk_add_f32 v[110:111], v[110:111], v[114:115]\n" \
    "s_waitcnt lgkmcnt(0)\n" \
    "v_pk_add_f32 v[116:117], v[116:117], v[120:121]\n" \
    "v_pk_add_f32 v[118:119], v[118:119], v[122:123]\n" \
    "v_pk_add_f32 v[108:109], v[108:109], v[116:117]\n" \
    "v_pk_add_f32 v[110:111], v[110:111], v[118:119]\n" \
    "v_cvt_pk_bf16_f32 v124, v108, v109\n" \
    "v_cvt_pk_bf16_f32 v125, v110, v111\n" \
    "s_nop 0\n" \
    "v_mov_b32_dpp v126, v124 quad_perm:[1,0,3,2] row_mask:0xf bank_mask:0xf\n" \
    "v_mov_b32_dpp v127, v125 quad_perm:[1,0,3,2] row_mask:0xf bank_mask:0xf\n" \
    "buffer_store_dwordx4 v[124:127], v231, s[40:43], s60 offen sc1\n" \
    "ds_write_b64 v232, v[124:125]\n" \
    "ds_read_b64_tr_b16 v[236:237], v233\n" \
    "ds_read_b64_tr_b16 v[238:239], v233 offset:256\n" \
    "s_cmp_eq_u32 s76, 63\n" \
    "s_cbranch_scc1 L%=_Rfin\n" \
    "ds_read_b32 v216, v229\n" \
    "s_waitcnt lgkmcnt(0)\n" \
    "v_readfirstlane_b32 s94, v216\n" \
    "s_mul_i32 s79, s94, s70\n" \
    "s_add_u32 s79, s79, s81\n" \
    "s_cmp_lg_u32 s79, s77\n" \
    "s_cbranch_scc1 L%=_Ru4s\n" \
    "L%=_Ru4k:\n" \
    "L%=_Ru5a:\n" \
    "s_memtime s[38:39]\n" \
    "s_mul_hi_u32 s80, s67, s96\n" \
    "s_add_u32 s80, s80, s78\n" \
    "s_waitcnt lgkmcnt(0)\n" \
    "s_sub_i32 s79, s80, s38\n" \
    "s_max_i32 s79, s79, 0\n" \
    "s_cmp_gt_u32 s79, 320\n" \
    "s_cbranch_scc1 L%=_Ru5s\n" \
    ".p2align 4\n" \
    "L%=_Ru5d:\n" \
    "s_sub_u32 s79, s79, 28\n" \
    "s_cbranch_scc0 L%=_Ru5d\n" \
    "L%=_Ru5e:\n" \
    "buffer_load_dwordx4 v[168:171], v225, s[40:43], s60 offen sc1\n" \
    "buffer_load_dwordx4 v[172:175], v225, s[40:43], s85 offen sc1\n" \
    "s_memtime s[38:39]\n" \
    "s_memrealtime vcc\n" \
    "s_nop 4\n" \
    "s_memrealtime s[32:33]\n" \
    "s_nop 4\n" \
    "s_memrealtime s[34:35]\n" \
    "s_nop 4\n" \
    "s_memrealtime s[36:37]\n" \
    "s_add_u32 s80, s77, s64\n" \
    "s_lshl_b32 s80, s80, 2\n" \
    "s_waitcnt lgkmcnt(0)\n" \
    "s_sub_u32 s80, s80, vcc_lo\n" \
    "s_sub_u32 s80, s80, s32\n" \
    "s_sub_u32 s80, s80, s34\n" \
    "s_sub_u32 s80, s80, s36\n" \
    "s_mul_i32 s80, s80, s71\n" \
    "s_ashr_i32 s80, s80, 6\n" \
    "s_add_u32 s78, s80, s38\n" \
    "L%=_Rvc:\n" \
    "s_waitcnt vmcnt(1)\n" \
    "v_smfmac_f32_16x16x64_bf16 v[176:179], v[168:171], v[0:7], v224\n" \
    "v_smfmac_f32_16x16x64_bf16 v[180:183], v[168:171], v[8:15], v224\n" \
    "s_waitcnt vmcnt(0)\n" \
    "v_smfmac_f32_16x16x64_bf16 v[176:179], v[172:175], v[32:39], v224\n" \
    "v_smfmac_f32_16x16x64_bf16 v[180:183], v[172:175], v[40:47], v224\n" \
    "v_smfmac_f32_16x16x64_bf16 v[184:187], v[168:171], v[16:23], v224\n" \
    "v_smfmac_f32_16x16x64_bf16 v[188:191], v[168:171], v[24:31], v224\n" \
    "v_add_f32 v176, v176, v178\n" \
    "v_smfmac_f32_16x16x64_bf16 v[184:187], v[172:175], v[48:55], v224\n" \
    "v_add_f32 v177, v177, v179\n" \
    "v_smfmac_f32_16x16x64_bf16 v[188:191], v[172:175], v[56:63], v224\n" \
    "v_cmp_u_f32 vcc, v176, v177\n" \
    "v_cvt_pk_bf16_f32 v64, v176, v177\n" \
    "v_add_f32 v180, v180, v182\n" \
    "v_add_f32 v181, v181, v183\n" \
    "v_cvt_pk_bf16_f32 v72, v180, v181\n" \
    "s_cbranch_vccnz L%=_Rvslow\n" \
    "L%=_Rvok:\n" \
    "v_add_f32 v184, v184, v186\n" \
    "v_add_f32 v185, v185, v187\n" \
    "v_cvt_pk_bf16_f32 v80, v184, v185\n" \
    "v_add_f32 v188, v188, v190\n" \
    "v_add_f32 v189, v189, v191\n" \
    "v_cvt_pk_bf16_f32 v88, v188, v189\n" \
    "s_barrier\n" \
    "L%=_Rfr:\n" \
    "ds_read_b128 v[192:195], v227\n" \
    "s_waitcnt lgkmcnt(0)\n" \
    "v_smfmac_f32_16x16x64_bf16 v[212:215], v[192:195], v[96:103], v224\n" \
    "v_cmp_ne_u32 vcc, s76, v195\n" \
    "v_smfmac_f32_16x16x64_bf16 v[196:199], v[192:195], v[64:71], v224\n" \
    "s_cbranch_vccnz L%=_Rfs\n" \
    "v_smfmac_f32_16x16x64_bf16 v[200:203], v[192:195], v[72:79], v224\n" \
    "v_smfmac_f32_16x16x64_bf16 v[204:207], v[192:195], v[80:87], v224\n" \
    "v_add_f32 v212, v212, v214\n" \
    "v_smfmac_f32_16x16x64_bf16 v[208:211], v[192:195], v[88:95], v224\n" \
    "v_add_f32 v213, v213, v215\n" \
    "v_rcp_f32 v220, v212\n" \
    "v_rcp_f32 v221, v213\n" \
    "v_add_f32 v196, v196, v198\n" \
    "v_add_f32 v197, v197, v199\n" \
    "v_pk_add_f32 v[200:201], v[200:201], v[202:203]\n" \
    "v_pk_add_f32 v[204:205], v[204:205], v[206:207]\n" \
    "s_nop 0\n" \
    "v_pk_add_f32 v[208:209], v[208:209], v[210:211]\n" \
    "v_pk_mul_f32 v[196:197], v[196:197], v[220:221]\n" \
    "v_pk_mul_f32 v[200:201], v[200:201], v[220:221]\n" \
    "v_pk_mul_f32 v[204:205], v[204:205], v[220:221]\n" \
    "v_pk_mul_f32 v[208:209], v[208:209], v[220:221]\n" \
    "v_cvt_pk_bf16_f32 v216, v196, v200\n" \
    "v_cvt_pk_bf16_f32 v217, v204, v208\n" \
    "v_cvt_pk_bf16_f32 v218, v197, v201\n" \
    "v_cvt_pk_bf16_f32 v219, v205, v209\n" \
    "buffer_store_dwordx4 v[216:219], v226, s[40:43], s60 offen sc1\n" \
    "ds_read_b64_tr_b16 v[104:105], v228 offset:16384\n" \
    "ds_read_b64_tr_b16 v[106:107], v228 offset:16416\n" \
    "ds_read_b64_tr_b16 v[108:109], v228 offset:18688\n" \
    "ds_read_b64_tr_b16 v[110:111], v228 offset:18720\n" \
    "ds_read_b64_tr_b16 v[112:113], v228 offset:20992\n" \
    "ds_read_b64_tr_b16 v[114:115], v228 offset:21024\n" \
    "ds_read_b64_tr_b16 v[116:117], v228 offset:23296\n" \
    "ds_read_b64_tr_b16 v[118:119], v228 offset:23328\n" \
    "ds_read_b64_tr_b16 v[120:121], v228 offset:16656\n" \
    "ds_read_b64_tr_b16 v[122:123], v228 offset:16688\n" \
    "ds_read_b64_tr_b16 v[124:125], v228 offset:18960\n" \
    "ds_read_b64_tr_b16 v[126:127], v228 offset:18992\n" \
    "ds_read_b64_tr_b16 v[128:129], v228 offset:21264\n" \
    "ds_read_b64_tr_b16 v[130:131], v228 offset:21296\n" \
    "ds_read_b64_tr_b16 v[132:133], v228 offset:23568\n" \
    "ds_read_b64_tr_b16 v[134:135], v228 offset:23600\n" \
    "s_waitcnt lgkmcnt(14)\n" \
    "v_mfma_f32_16x16x32_bf16 v[136:139], v[104:107], v[0:3], 0\n" \
    "v_mfma_f32_16x16x32_bf16 v[144:147], v[104:107], v[8:11], 0\n" \
    "v_mfma_f32_16x16x32_bf16 v[152:155], v[104:107], v[16:19], 0\n" \
    "v_mfma_f32_16x16x32_bf16 v[160:163], v[104:107], v[24:27], 0\n" \
    "s_waitcnt lgkmcnt(12)\n" \
    "v_mfma_f32_16x16x32_bf16 v[136:139], v[108:111], v[4:7], v[136:139]\n" \
    "v_mfma_f32_16x16x32_bf16 v[144:147], v[108:111], v[12:15], v[144:147]\n" \
    "v_mfma_f32_16x16x32_bf16 v[152:155], v[108:111], v[20:23], v[152:155]\n" \
    "v_mfma_f32_16x16x32_bf16 v[160:163], v[108:111], v[28:31], v[160:163]\n" \
    "s_waitcnt lgkmcnt(10)\n" \
    "v_mfma_f32_16x16x32_bf16 v[136:139], v[112:115], v[32:35], v[136:139]\n" \
    "v_mfma_f32_16x16x32_bf16 v[144:147], v[112:115], v[40:43], v[144:147]\n" \
    "v_mfma_f32_16x16x32_bf16 v[152:155], v[112:115], v[48:51], v[152:155]\n" \
    "v_mfma_f32_16x16x32_bf16 v[160:163], v[112:115], v[56:59], v[160:163]\n" \
    "s_waitcnt lgkmcnt(8)\n" \
    "v_mfma_f32_16x16x32_bf16 v[136:139], v[116:119], v[36:39], v[136:139]\n" \
    "v_mfma_f32_16x16x32_bf16 v[144:147], v[116:119], v[44:47], v[144:147]\n" \
    "v_mfma_f32_16x16x32_bf16 v[152:155], v[116:119], v[52:55], v[152:155]\n" \
    "v_mfma_f32_16x16x32_bf16 v[160:163], v[116:119], v[60:63], v[160:163]\n" \
    "v_add_u32 v223, 1, v223\n" \
    "s_and_b32 s95, s94, 1\n" \
    "s_lshl_b32 s95, s95, 17\n" \
    "s_add_u32 s95, s95, s61\n" \
    "s_add_u32 s85, s95, 0x1000\n" \
    "s_xor_b32 s85, s95, 0x20000\n" \
    "L%=_Ru6a:\n" \
    "s_memtime s[38:39]\n" \
    "s_mul_hi_u32 s80, s68, s96\n" \
    "s_add_u32 s80, s80, s78\n" \
    "s_waitcnt lgkmcnt(0)\n" \
    "s_sub_i32 s79, s80, s38\n" \
    "s_max_i32 s79, s79, 0\n" \
    "s_cmp_gt_u32 s79, 320\n" \
    "s_cbranch_scc1 L%=_Ru6s\n" \
    ".p2align 4\n" \
    "L%=_Ru6d:\n" \
    "s_sub_u32 s79, s79, 28\n" \
    "s_cbranch_scc0 L%=_Ru6d\n" \
    "L%=_Ru6e:\n" \
    "buffer_load_dwordx4 v[176:179], v235, s[40:43], s60 offen offset:0 sc1\n" \
    "buffer_load_dwordx4 v[180:183], v235, s[40:43], s60 offen offset:1024 sc1\n" \
    "buffer_load_dwordx4 v[184:187], v235, s[40:43], s60 offen offset:2048 sc1\n" \
    "buffer_load_dwordx4 v[188:191], v235, s[40:43], s60 offen offset:3072 sc1\n" \
    "v_mfma_f32_16x16x32_bf16 v[140:143], v[120:123], v[0:3], 0\n" \
    "v_mfma_f32_16x16x32_bf16 v[148:151], v[120:123], v[8:11], 0\n" \
    "v_mfma_f32_16x16x32_bf16 v[156:159], v[120:123], v[16:19], 0\n" \
    "v_mfma_f32_16x16x32_bf16 v[164:167], v[120:123], v[24:27], 0\n" \
    "v_mfma_f32_16x16x32_bf16 v[140:143], v[124:127], v[4:7], v[140:143]\n" \
    "v_mfma_f32_16x16x32_bf16 v[148:151], v[124:127], v[12:15], v[148:151]\n" \
    "v_mfma_f32_16x16x32_bf16 v[156:159], v[124:127], v[20:23], v[156:159]\n" \
    "v_mfma_f32_16x16x32_bf16 v[164:167], v[124:127], v[28:31], v[164:167]\n" \
    "v_mfma_f32_16x16x32_bf16 v[140:143], v[128:131], v[32:35], v[140:143]\n" \
    "v_mfma_f32_16x16x32_bf16 v[148:151], v[128:131], v[40:43], v[148:151]\n" \
    "v_mfma_f32_16x16x32_bf16 v[156:159], v[128:131], v[48:51], v[156:159]\n" \
    "v_mfma_f32_16x16x32_bf16 v[164:167], v[128:131], v[56:59], v[164:167]\n" \
    "v_mfma_f32_16x16x32_bf16 v[140:143], v[132:135], v[36:39], v[140:143]\n" \
    "v_mfma_f32_16x16x32_bf16 v[148:151], v[132:135], v[44:47], v[148:151]\n" \
    "v_mfma_f32_16x16x32_bf16 v[156:159], v[132:135], v[52:55], v[156:159]\n" \
    "v_mfma_f32_16x16x32_bf16 v[164:167], v[132:135], v[60:63], v[164:167]\n" \
    "v_cvt_pk_bf16_f32 v65, v136, v137\n" \
    "v_cvt_pk_bf16_f32 v68, v138, v139\n" \
    "v_cvt_pk_bf16_f32 v73, v144, v145\n" \
    "v_cvt_pk_bf16_f32 v76, v146, v147\n" \
    "v_cvt_pk_bf16_f32 v81, v152, v153\n" \
    "v_cvt_pk_bf16_f32 v84, v154, v155\n" \
    "v_cvt_pk_bf16_f32 v89, v160, v161\n" \
    "v_cvt_pk_bf16_f32 v92, v162, v163\n" \
    "L%=_Rred:\n" \
    "s_waitcnt vmcnt(3)\n" \
    "v_mfma_f32_16x16x32_bf16 v[240:243], v[244:247], v[176:179], 0\n" \
    "s_waitcnt vmcnt(2)\n" \
    "v_mfma_f32_16x16x32_bf16 v[240:243], v[244:247], v[180:183], v[240:243]\n" \
    "s_waitcnt vmcnt(1)\n" \
    "v_mfma_f32_16x16x32_bf16 v[240:243], v[244:247], v[184:187], v[240:243]\n" \
    "s_waitcnt vmcnt(0)\n" \
    "v_mfma_f32_16x16x32_bf16 v[240:243], v[244:247], v[188:191], v[240:243]\n" \
    "s_nop 7\n" \
    "v_cmp_u_f32 vcc, v240, v240\n" \
    "v_add_f32_dpp v240, v240, v240 row_ror:8 row_mask:0xf bank_mask:0xf\n" \
    "v_add_f32_dpp v241, v241, v241 row_ror:8 row_mask:0xf bank_mask:0xf\n" \
    "v_add_f32_dpp v242, v242, v242 row_ror:8 row_mask:0xf bank_mask:0xf\n" \
    "v_add_f32_dpp v243, v243, v243 row_ror:8 row_mask:0xf bank_mask:0xf\n" \
    "v_cvt_pk_bf16_f32 v216, v240, v241\n" \
    "v_cvt_pk_bf16_f32 v217, v242, v243\n" \
    "s_cbranch_vccnz L%=_Rslow\n" \
    "L%=_Rok:\n" \
    "v_mov_b32_dpp v218, v216 quad_perm:[1,0,3,2] row_mask:0xf bank_mask:0xf\n" \
    "v_mov_b32_dpp v219, v217 quad_perm:[1,0,3,2] row_mask:0xf bank_mask:0xf\n" \
    "buffer_store_dwordx4 v[216:219], v222, s[40:43], s95 offen sc1\n" \
    "ds_write_b32 v229, v223 offset:4\n" \
    "buffer_store_dwordx4 v[216:219], v222, s[40:43], s85 offen sc1\n" \
    "s_setprio 0\n" \
    "v_cvt_pk_bf16_f32 v66, v142, v143\n" \
    "v_cvt_pk_bf16_f32 v67, v142, v143\n" \
    "v_cvt_pk_bf16_f32 v69, v140, v141\n" \
    "v_cvt_pk_bf16_f32 v74, v150, v151\n" \
    "v_cvt_pk_bf16_f32 v75, v150, v151\n" \
    "v_cvt_pk_bf16_f32 v77, v148, v149\n" \
    "v_cvt_pk_bf16_f32 v82, v158, v159\n" \
    "v_cvt_pk_bf16_f32 v83, v158, v159\n" \
    "v_cvt_pk_bf16_f32 v85, v156, v157\n" \
    "v_cvt_pk_bf16_f32 v90, v166, v167\n" \
    "v_cvt_pk_bf16_f32 v91, v166, v167\n" \
    "v_cvt_pk_bf16_f32 v93, v164, v165\n" \
    "v_permlane16_swap_b32 v66, v67\n" \
    "v_permlane16_swap_b32 v74, v75\n" \
    "v_permlane16_swap_b32 v82, v83\n" \
    "v_permlane16_swap_b32 v90, v91\n" \
    "ds_read_b64 v[220:221], v229 offset:64\n" \
    "s_mov_b32 s79, s60\n" \
    "s_mov_b32 s60, s61\n" \
    "s_mov_b32 s61, s62\n" \
    "s_mov_b32 s62, s63\n" \
    "s_mov_b32 s63, s79\n" \
    "s_add_u32 s76, s76, 1\n" \
    "s_add_u32 s77, s77, s65\n" \
    "s_add_u32 s84, s84, 0x20000\n" \
    "s_add_u32 s81, s81, s65\n" \
    "s_waitcnt lgkmcnt(0)\n" \
    "v_readfirstlane_b32 s65, v220\n" \
    "v_readfirstlane_b32 s71, v221\n" \
    "s_mul_i32 s96, s65, s71\n" \
    "s_lshr_b32 s96, s96, 4\n" \
    "s_branch L%=_Rloop\n" \
    "L%=_Rslow:\n" \
    "s_nop 7\n" \
    "s_nop 0\n" \
    "v_max_u32 v220, v176, v177\n" \
    "v_max3_u32 v220, v220, v178, v179\n" \
    "v_max3_u32 v220, v220, v180, v181\n" \
    "v_max3_u32 v220, v220, v182, v183\n" \
    "v_max3_u32 v220, v220, v184, v185\n" \
    "v_max3_u32 v220, v220, v186, v187\n" \
    "v_max3_u32 v220, v220, v188, v189\n" \
    "v_max3_u32 v220, v220, v190, v191\n" \
    "v_cmp_eq_u32 vcc, -1, v220\n" \
    "s_cbranch_vccz L%=_Rok\n" \
    "s_cmp_eq_u32 s99, 0\n" \
    "s_cbranch_scc1 L%=_Rok\n" \
    "s_sub_u32 s99, s99, 1\n" \
    "s_cmp_lg_u32 s99, 0\n" \
    "s_cbranch_scc1 L%=_Ru7\n" \
    "v_mov_b32 v220, 1\n" \
    "buffer_store_dword v220, off, s[44:47], 0 sc1\n" \
    "L%=_Ru7:\n" \
    "s_sleep 1\n" \
    "buffer_load_dwordx4 v[176:179], v235, s[40:43], s60 offen offset:0 sc1\n" \
    "buffer_load_dwordx4 v[180:183], v235, s[40:43], s60 offen offset:1024 sc1\n" \
    "buffer_load_dwordx4 v[184:187], v235, s[40:43], s60 offen offset:2048 sc1\n" \
    "buffer_load_dwordx4 v[188:191], v235, s[40:43], s60 offen offset:3072 sc1\n" \
    "s_branch L%=_Rred\n" \
    "L%=_Rfin:\n" \
    "s_waitcnt lgkmcnt(0)\n" \
    "buffer_store_dwordx4 v[236:239], v234, s[48:51], s84 offen sc1\n" \
    "s_waitcnt vmcnt(0)\n" \
    "s_barrier\n" \
    "s_branch L%=_end\n" \
    "L%=_Ru3s:\n" \
    "s_cmp_gt_u32 s79, 0x8000\n" \
    "s_cbranch_scc1 L%=_Ru3e\n" \
    "s_sleep 2\n" \
    "s_branch L%=_Ru3a\n" \
    "L%=_Ru4s:\n" \
    "s_mov_b32 s77, s79\n" \
    "s_memtime s[38:39]\n" \
    "s_memrealtime vcc\n" \
    "s_nop 4\n" \
    "s_memrealtime s[32:33]\n" \
    "s_nop 4\n" \
    "s_memrealtime s[34:35]\n" \
    "s_nop 4\n" \
    "s_memrealtime s[36:37]\n" \
    "s_add_u32 s80, s77, s64\n" \
    "s_lshl_b32 s80, s80, 2\n" \
    "s_waitcnt lgkmcnt(0)\n" \
    "s_sub_u32 s80, s80, vcc_lo\n" \
    "s_sub_u32 s80, s80, s32\n" \
    "s_sub_u32 s80, s80, s34\n" \
    "s_sub_u32 s80, s80, s36\n" \
    "s_mul_i32 s80, s80, s71\n" \
    "s_ashr_i32 s80, s80, 6\n" \
    "s_add_u32 s78, s80, s38\n" \
    "s_branch L%=_Ru4k\n" \
    "L%=_Ru5s:\n" \
    "s_cmp_gt_u32 s79, 0x8000\n" \
    "s_cbranch_scc1 L%=_Ru5e\n" \
    "s_sleep 2\n" \
    "s_branch L%=_Ru5a\n" \
    "L%=_Rvslow:\n" \
    "s_nop 7\n" \
    "s_nop 0\n" \
    "v_max_u32 v216, v168, v169\n" \
    "v_max3_u32 v216, v216, v170, v171\n" \
    "v_max3_u32 v216, v216, v172, v173\n" \
    "v_max3_u32 v216, v216, v174, v175\n" \
    "v_cmp_eq_u32 vcc, -1, v216\n" \
    "s_cbranch_vccz L%=_Rvok\n" \
    "s_cmp_eq_u32 s99, 0\n" \
    "s_cbranch_scc1 L%=_Rvok\n" \
    "s_sub_u32 s99, s99, 1\n" \
    "s_cmp_lg_u32 s99, 0\n" \
    "s_cbranch_scc1 L%=_Ru8\n" \
    "v_mov_b32 v216, 1\n" \
    "buffer_store_dword v216, off, s[44:47], 0 sc1\n" \
    "L%=_Ru8:\n" \
    "s_sleep 1\n" \
    "buffer_load_dwordx4 v[168:171], v225, s[40:43], s60 offen sc1\n" \
    "buffer_load_dwordx4 v[172:175], v225, s[40:43], s85 offen sc1\n" \
    "v_mov_b64 v[176:177], 0\n" \
    "v_mov_b64 v[178:179], 0\n" \
    "v_mov_b64 v[180:181], 0\n" \
    "v_mov_b64 v[182:183], 0\n" \
    "v_mov_b64 v[184:185], 0\n" \
    "v_mov_b64 v[186:187], 0\n" \
    "v_mov_b64 v[188:189], 0\n" \
    "v_mov_b64 v[190:191], 0\n" \
    "s_waitcnt vmcnt(0)\n" \
    "s_branch L%=_Rvc\n" \
    "L%=_Rfs:\n" \
    "s_nop 7\n" \
    "s_nop 0\n" \
    "v_mov_b64 v[196:197], 0\n" \
    "v_mov_b64 v[198:199], 0\n" \
    "v_mov_b64 v[200:201], 0\n" \
    "v_mov_b64 v[202:203], 0\n" \
    "v_mov_b64 v[204:205], 0\n" \
    "v_mov_b64 v[206:207], 0\n" \
    "v_mov_b64 v[208:209], 0\n" \
    "v_mov_b64 v[210:211], 0\n" \
    "v_mov_b64 v[212:213], 0\n" \
    "v_mov_b64 v[214:215], 0\n" \
    "s_branch L%=_Rfr\n" \
    "L%=_Ru6s:\n" \
    "s_cmp_gt_u32 s79, 0x8000\n" \
    "s_cbranch_scc1 L%=_Ru6e\n" \
    "s_sleep 2\n" \
    "s_branch L%=_Ru6a\n" \
    "L%=_P:\n" \
    "s_mov_b32 s76, 0\n" \
    "s_mov_b32 s99, 0x20000\n" \
    "s_mov_b32 s84, 0\n" \
    "s_mov_b32 s81, 0\n" \
    "s_mov_b32 s86, 0\n" \
    "s_mov_b32 s87, 0\n" \
    "s_mov_b32 s88, 0\n" \
    "s_mov_b32 s89, 0\n" \
    "s_mov_b32 s90, 0\n" \
    "s_mov_b32 s91, 0\n" \
    "s_mov_b32 s92, 0\n" \
    "s_mov_b32 s93, 0\n" \
    "s_mov_b32 s94, 0\n" \
    "s_mov_b32 s95, 0\n" \
    "s_mov_b32 s96, 0\n" \
    "s_mov_b32 s97, 0\n" \
    "s_mov_b32 s98, 0\n" \
    "ds_read_b32 v216, v229\n" \
    "s_waitcnt lgkmcnt(0)\n" \
    "v_readfirstlane_b32 s94, v216\n" \
    "s_mul_i32 s79, s94, s70\n" \
    "s_add_u32 s79, s79, s81\n" \
    "s_mov_b32 s77, s79\n" \
    "s_mul_i32 s96, s65, s71\n" \
    "s_lshr_b32 s96, s96, 4\n" \
    "v_mov_b32 v96, 0x3f803f80\n" \
    "v_mov_b32 v97, 0x3f803f80\n" \
    "v_mov_b32 v98, 0x3f803f80\n" \
    "v_mov_b32 v99, 0x3f803f80\n" \
    "v_mov_b32 v100, 0x3f803f80\n" \
    "v_mov_b32 v101, 0x3f803f80\n" \
    "v_mov_b64 v[102:103], 0\n" \
    "v_mov_b64 v[64:65], 0\n" \
    "v_mov_b64 v[66:67], 0\n" \
    "v_mov_b64 v[68:69], 0\n" \
    "v_mov_b64 v[70:71], 0\n" \
    "v_mov_b64 v[72:73], 0\n" \
    "v_mov_b64 v[74:75], 0\n" \
    "v_mov_b64 v[76:77], 0\n" \
    "v_mov_b64 v[78:79], 0\n" \
    "v_mov_b64 v[80:81], 0\n" \
    "v_mov_b64 v[82:83], 0\n" \
    "v_mov_b64 v[84:85], 0\n" \
    "v_mov_b64 v[86:87], 0\n" \
    "v_mov_b64 v[88:89], 0\n" \
    "v_mov_b64 v[90:91], 0\n" \
    "v_mov_b64 v[92:93], 0\n" \
    "v_mov_b64 v[94:95], 0\n" \
    "v_mov_b64 v[192:193], 0\n" \
    "v_mov_b64 v[194:195], 0\n" \
    "v_mov_b32 v238, 0\n" \
    "v_cvt_f32_u32 v240, s71\n" \
    "v_mov_b32 v241, 1.0\n" \
    "v_mov_b32 v242, 0\n" \
    "v_mov_b32 v243, s65\n" \
    "s_mov_b32 s74, s94\n" \
    "v_mov_b32 v244, -1\n" \
    "v_mov_b32 v245, -1\n" \
    "v_mov_b32 v246, -1\n" \
    "v_mov_b32 v247, -1\n" \
    "L%=_Ploop:\n" \
    "ds_read_b64_tr_b16 v[104:105], v228 offset:16384\n" \
    "ds_read_b64_tr_b16 v[106:107], v228 offset:16416\n" \
    "ds_read_b64_tr_b16 v[108:109], v228 offset:18688\n" \
    "ds_read_b64_tr_b16 v[110:111], v228 offset:18720\n" \
    "ds_read_b64_tr_b16 v[112:113], v228 offset:20992\n" \
    "ds_read_b64_tr_b16 v[114:115], v228 offset:21024\n" \
    "ds_read_b64_tr_b16 v[116:117], v228 offset:23296\n" \
    "ds_read_b64_tr_b16 v[118:119], v228 offset:23328\n" \
    "ds_read_b64_tr_b16 v[120:121], v228 offset:16656\n" \
    "ds_read_b64_tr_b16 v[122:123], v228 offset:16688\n" \
    "ds_read_b64_tr_b16 v[124:125], v228 offset:18960\n" \
    "ds_read_b64_tr_b16 v[126:127], v228 offset:18992\n" \
    "ds_read_b64_tr_b16 v[128:129], v228 offset:21264\n" \
    "ds_read_b64_tr_b16 v[130:131], v228 offset:21296\n" \
    "ds_read_b64_tr_b16 v[132:133], v228 offset:23568\n" \
    "ds_read_b64_tr_b16 v[134:135], v228 offset:23600\n" \
    "s_waitcnt lgkmcnt(14)\n" \
    "v_mfma_f32_16x16x32_bf16 v[136:139], v[104:107], v[0:3], 0\n" \
    "v_mfma_f32_16x16x32_bf16 v[144:147], v[104:107], v[8:11], 0\n" \
    "v_mfma_f32_16x16x32_bf16 v[152:155], v[104:107], v[16:19], 0\n" \
    "v_mfma_f32_16x16x32_bf16 v[160:163], v[104:107], v[24:27], 0\n" \
    "s_waitcnt lgkmcnt(12)\n" \
    "v_mfma_f32_16x16x32_bf16 v[136:139], v[108:111], v[4:7], v[136:139]\n" \
    "v_mfma_f32_16x16x32_bf16 v[144:147], v[108:111], v[12:15], v[144:147]\n" \
    "v_mfma_f32_16x16x32_bf16 v[152:155], v[108:111], v[20:23], v[152:155]\n" \
    "v_mfma_f32_16x16x32_bf16 v[160:163], v[108:111], v[28:31], v[160:163]\n" \
    "s_waitcnt lgkmcnt(10)\n" \
    "v_mfma_f32_16x16x32_bf16 v[136:139], v[112:115], v[32:35], v[136:139]\n" \
    "v_mfma_f32_16x16x32_bf16 v[144:147], v[112:115], v[40:43], v[144:147]\n" \
    "v_mfma_f32_16x16x32_bf16 v[152:155], v[112:115], v[48:51], v[152:155]\n" \
    "v_mfma_f32_16x16x32_bf16 v[160:163], v[112:115], v[56:59], v[160:163]\n" \
    "s_waitcnt lgkmcnt(8)\n" \
    "v_mfma_f32_16x16x32_bf16 v[136:139], v[116:119], v[36:39], v[136:139]\n" \
    "v_mfma_f32_16x16x32_bf16 v[144:147], v[116:119], v[44:47], v[144:147]\n" \
    "v_mfma_f32_16x16x32_bf16 v[152:155], v[116:119], v[52:55], v[152:155]\n" \
    "v_mfma_f32_16x16x32_bf16 v[160:163], v[116:119], v[60:63], v[160:163]\n" \
    "s_waitcnt lgkmcnt(6)\n" \
    "v_mfma_f32_16x16x32_bf16 v[140:143], v[120:123], v[0:3], 0\n" \
    "v_mfma_f32_16x16x32_bf16 v[148:151], v[120:123], v[8:11], 0\n" \
    "v_mfma_f32_16x16x32_bf16 v[156:159], v[120:123], v[16:19], 0\n" \
    "v_mfma_f32_16x16x32_bf16 v[164:167], v[120:123], v[24:27], 0\n" \
    "s_waitcnt lgkmcnt(4)\n" \
    "v_mfma_f32_16x16x32_bf16 v[140:143], v[124:127], v[4:7], v[140:143]\n" \
    "v_mfma_f32_16x16x32_bf16 v[148:151], v[124:127], v[12:15], v[148:151]\n" \
    "v_mfma_f32_16x16x32_bf16 v[156:159], v[124:127], v[20:23], v[156:159]\n" \
    "v_mfma_f32_16x16x32_bf16 v[164:167], v[124:127], v[28:31], v[164:167]\n" \
    "s_waitcnt lgkmcnt(2)\n" \
    "v_mfma_f32_16x16x32_bf16 v[140:143], v[128:131], v[32:35], v[140:143]\n" \
    "v_mfma_f32_16x16x32_bf16 v[148:151], v[128:131], v[40:43], v[148:151]\n" \
    "v_mfma_f32_16x16x32_bf16 v[156:159], v[128:131], v[48:51], v[156:159]\n" \
    "v_mfma_f32_16x16x32_bf16 v[164:167], v[128:131], v[56:59], v[164:167]\n" \
    "s_waitcnt lgkmcnt(0)\n" \
    "v_mfma_f32_16x16x32_bf16 v[140:143], v[132:135], v[36:39], v[140:143]\n" \
    "v_mfma_f32_16x16x32_bf16 v[148:151], v[132:135], v[44:47], v[148:151]\n" \
    "v_mfma_f32_16x16x32_bf16 v[156:159], v[132:135], v[52:55], v[156:159]\n" \
    "v_mfma_f32_16x16x32_bf16 v[164:167], v[132:135], v[60:63], v[164:167]\n" \
    "v_cvt_pk_bf16_f32 v65, v136, v137\n" \
    "v_cvt_pk_bf16_f32 v68, v138, v139\n" \
    "v_cvt_pk_bf16_f32 v73, v144, v145\n" \
    "v_cvt_pk_bf16_f32 v76, v146, v147\n" \
    "v_cvt_pk_bf16_f32 v81, v152, v153\n" \
    "v_cvt_pk_bf16_f32 v84, v154, v155\n" \
    "v_cvt_pk_bf16_f32 v89, v160, v161\n" \
    "v_cvt_pk_bf16_f32 v92, v162, v163\n" \
    "v_cvt_pk_bf16_f32 v66, v142, v143\n" \
    "v_cvt_pk_bf16_f32 v67, v142, v143\n" \
    "v_cvt_pk_bf16_f32 v69, v140, v141\n" \
    "v_cvt_pk_bf16_f32 v74, v150, v151\n" \
    "v_cvt_pk_bf16_f32 v75, v150, v151\n" \
    "v_cvt_pk_bf16_f32 v77, v148, v149\n" \
    "v_cvt_pk_bf16_f32 v82, v158, v159\n" \
    "v_cvt_pk_bf16_f32 v83, v158, v159\n" \
    "v_cvt_pk_bf16_f32 v85, v156, v157\n" \
    "v_cvt_pk_bf16_f32 v90, v166, v167\n" \
    "v_cvt_pk_bf16_f32 v91, v166, v167\n" \
    "v_cvt_pk_bf16_f32 v93, v164, v165\n" \
    "v_permlane16_swap_b32 v66, v67\n" \
    "v_permlane16_swap_b32 v74, v75\n" \
    "v_permlane16_swap_b32 v82, v83\n" \
    "v_permlane16_swap_b32 v90, v91\n" \
    "v_mov_b64 v[176:177], 0\n" \
    "v_mov_b64 v[178:179], 0\n" \
    "v_mov_b64 v[180:181], 0\n" \
    "v_mov_b64 v[182:183], 0\n" \
    "v_mov_b64 v[184:185], 0\n" \
    "v_mov_b64 v[186:187], 0\n" \
    "v_mov_b64 v[188:189], 0\n" \
    "v_mov_b64 v[190:191], 0\n" \
    "v_mov_b64 v[196:197], 0\n" \
    "v_mov_b64 v[198:199], 0\n" \
    "v_mov_b64 v[200:201], 0\n" \
    "v_mov_b64 v[202:203], 0\n" \
    "v_mov_b64 v[204:205], 0\n" \
    "v_mov_b64 v[206:207], 0\n" \
    "v_mov_b64 v[208:209], 0\n" \
    "v_mov_b64 v[210:211], 0\n" \
    "v_mov_b64 v[212:213], 0\n" \
    "v_mov_b64 v[214:215], 0\n" \
    "s_add_u32 s85, s60, 0x1000\n" \
    "s_add_u32 s82, s76, 1\n" \
    "s_min_u32 s82, s82, 63\n" \
    "s_lshl_b32 s83, s82, 18\n" \
    "s_add_u32 s83, s83, s72\n" \
    "v_cmp_ge_u32 vcc, s82, v237\n" \
    "s_cbranch_vccnz L%=_Pfslow\n" \
    "L%=_Pfok:\n" \
    "s_branch L%=_Pu2b\n" \
    "L%=_Pu2a:\n" \
    "s_sleep 4\n" \
    "L%=_Pu2b:\n" \
    "s_memrealtime vcc\n" \
    "s_add_u32 s80, s77, s64\n" \
    "s_add_u32 s80, s80, s69\n" \
    "s_waitcnt lgkmcnt(0)\n" \
    "s_sub_i32 s79, s80, vcc_lo\n" \
    "s_sub_u32 s79, s79, 40\n" \
    "s_cmp_lt_u32 s79, 0x400\n" \
    "s_cbranch_scc1 L%=_Pu2a\n" \
    "s_memtime s[38:39]\n" \
    "s_memrealtime vcc\n" \
    "s_nop 4\n" \
    "s_memrealtime s[32:33]\n" \
    "s_nop 4\n" \
    "s_memrealtime s[34:35]\n" \
    "s_nop 4\n" \
    "s_memrealtime s[36:37]\n" \
    "s_add_u32 s80, s77, s64\n" \
    "s_lshl_b32 s80, s80, 2\n" \
    "s_waitcnt lgkmcnt(0)\n" \
    "s_sub_u32 s80, s80, vcc_lo\n" \
    "s_sub_u32 s80, s80, s32\n" \
    "s_sub_u32 s80, s80, s34\n" \
    "s_sub_u32 s80, s80, s36\n" \
    "s_mul_i32 s80, s80, s71\n" \
    "s_ashr_i32 s80, s80, 6\n" \
    "s_add_u32 s78, s80, s38\n" \
    "L%=_Pu3a:\n" \
    "s_memtime s[38:39]\n" \
    "s_mul_i32 s80, s69, 23\n" \
    "s_add_u32 s80, s80, s78\n" \
    "s_waitcnt lgkmcnt(0)\n" \
    "s_sub_i32 s79, s80, s38\n" \
    "s_max_i32 s79, s79, 0\n" \
    "s_cmp_gt_u32 s79, 320\n" \
    "s_cbranch_scc1 L%=_Pu3s\n" \
    ".p2align 4\n" \
    "L%=_Pu3d:\n" \
    "s_sub_u32 s79, s79, 28\n" \
    "s_cbranch_scc0 L%=_Pu3d\n" \
    "L%=_Pu3e:\n" \
    "buffer_load_dwordx4 v[104:107], v230, s[56:59], s83 offen offset:0 sc1\n" \
    "buffer_load_dwordx4 v[108:111], v230, s[56:59], s83 offen offset:1024 sc1\n" \
    "buffer_load_dwordx4 v[112:115], v230, s[56:59], s83 offen offset:2048 sc1\n" \
    "buffer_load_dwordx4 v[116:119], v230, s[56:59], s83 offen offset:3072 sc1\n" \
    "s_add_u32 s79, s83, 0x1000\n" \
    "buffer_load_dwordx4 v[120:123], v230, s[56:59], s79 offen offset:0 sc1\n" \
    "buffer_load_dwordx4 v[124:127], v230, s[56:59], s79 offen offset:1024 sc1\n" \
    "buffer_load_dwordx4 v[128:131], v230, s[56:59], s79 offen offset:2048 sc1\n" \
    "buffer_load_dwordx4 v[132:135], v230, s[56:59], s79 offen offset:3072 sc1\n" \
    "s_add_u32 s79, s83, s75\n" \
    "buffer_load_dwordx4 v[136:139], v230, s[52:55], s79 offen offset:0 sc1\n" \
    "buffer_load_dwordx4 v[140:143], v230, s[52:55], s79 offen offset:1024 sc1\n" \
    "buffer_load_dword v237, v235, s[44:47], 0 offen sc1\n" \
    "s_lshl_b32 s79, s76, 7\n" \
    "buffer_load_dwordx4 v[144:147], off, s[44:47], s79 offset:2176 sc1\n" \
    "buffer_store_dword v243, v239, s[44:47], s79 offen sc1\n" \
    "buffer_store_dwordx4 v[244:247], v232, s[40:43], s62 offen offset:0 sc1\n" \
    "buffer_store_dwordx4 v[244:247], v232, s[40:43], s62 offen offset:1024 sc1\n" \
    "buffer_store_dwordx4 v[244:247], v232, s[40:43], s62 offen offset:2048 sc1\n" \
    "buffer_store_dwordx4 v[244:247], v232, s[40:43], s62 offen offset:3072 sc1\n" \
    "buffer_store_dwordx4 v[244:247], v233, s[40:43], s62 offen offset:0 sc1\n" \
    "buffer_store_dwordx4 v[244:247], v233, s[40:43], s62 offen offset:1024 sc1\n" \
    "buffer_store_dwordx4 v[244:247], v234, s[40:43], s62 offen sc1\n" \
    "buffer_store_dword v238, v236, s[44:47], 0 offen sc1\n" \
    "s_barrier\n" \
    "s_cmp_eq_u32 s76, 63\n" \
    "s_cbranch_scc1 L%=_Pfin\n" \
    "ds_read_b32 v216, v229\n" \
    "s_waitcnt lgkmcnt(0)\n" \
    "v_readfirstlane_b32 s94, v216\n" \
    "s_mul_i32 s79, s94, s70\n" \
    "s_add_u32 s79, s79, s81\n" \
    "s_cmp_lg_u32 s79, s77\n" \
    "s_cbranch_scc1 L%=_Pu4s\n" \
    "L%=_Pu4k:\n" \
    "s_sub_u32 s32, s78, s97\n" \
    "s_sub_u32 s33, s77, s98\n" \
    "s_mov_b32 s97, s78\n" \
    "s_mov_b32 s98, s77\n" \
    "s_lshl_b32 s32, s32, 4\n" \
    "s_cmp_eq_u32 s76, 0\n" \
    "s_cbranch_scc1 L%=_Pck\n" \
    "v_cvt_f32_u32 v148, s33\n" \
    "v_cvt_f32_u32 v149, s32\n" \
    "v_rcp_f32 v148, v148\n" \
    "s_cmp_lt_u32 s76, 4\n" \
    "s_cselect_b32 s34, 0, 0x3c449ba6\n" \
    "s_cmp_eq_u32 s94, s74\n" \
    "s_mov_b32 s74, s94\n" \
    "s_cselect_b32 s32, 0x7f000000, 0\n" \
    "s_cselect_b32 s33, 0.5, 1.0\n" \
    "s_cselect_b32 s34, 0, s34\n" \
    "v_fma_f32 v148, v148, v149, -v240\n" \
    "v_max_f32 v149, 0.5, v241\n" \
    "v_min_f32 v148, s32, v148\n" \
    "v_cmp_gt_f32 vcc, 0, v148\n" \
    "v_mul_f32 v242, 0x3f7851ec, v242\n" \
    "v_add_f32 v242, s34, v242\n" \
    "v_cndmask_b32 v149, v241, v149, vcc\n" \
    "v_fma_f32 v240, v148, v149, v240\n" \
    "v_mul_f32 v241, s33, v241\n" \
    "v_max_f32 v240, 0x43400000, v240\n" \
    "v_max_f32 v241, 0x3e000000, v241\n" \
    "v_min_f32 v240, 0x43e00000, v240\n" \
    "v_fma_f32 v148, v242, s73, s73\n" \
    "v_rcp_f32 v149, v240\n" \
    "s_nop 0\n" \
    "v_fma_f32 v148, v148, v149, 0.5\n" \
    "v_cvt_u32_f32 v243, v148\n" \
    "L%=_Pck:\n" \
    "v_add_f32 v149, 0.5, v240\n" \
    "v_mov_b32 v148, s65\n" \
    "v_cvt_u32_f32 v149, v149\n" \
    "s_waitcnt vmcnt(9)\n" \
    "ds_write_b128 v231, v[104:107] offset:16384\n" \
    "ds_write_b128 v231, v[108:111] offset:17536\n" \
    "ds_write_b128 v231, v[112:115] offset:18688\n" \
    "ds_write_b128 v231, v[116:119] offset:19840\n" \
    "ds_write_b128 v231, v[120:123] offset:20992\n" \
    "ds_write_b128 v231, v[124:127] offset:22144\n" \
    "ds_write_b128 v231, v[128:131] offset:23296\n" \
    "ds_write_b128 v231, v[132:135] offset:24448\n" \
    "ds_write_b128 v230, v[136:139] offset:25600\n" \
    "ds_write_b128 v230, v[140:143] offset:26624\n" \
    "v_max3_u32 v144, v144, v145, v146\n" \
    "v_max_u32 v144, v144, v147\n" \
    "v_cmp_eq_u32 vcc, 0, v144\n" \
    "s_nop 1\n" \
    "v_cndmask_b32 v148, v144, v148, vcc\n" \
    "ds_write_b64 v229, v[148:149] offset:64\n" \
    "v_mov_b32 v238, s76\n" \
    "s_setprio 3\n" \
    "L%=_Pu5a:\n" \
    "s_memtime s[38:39]\n" \
    "s_mul_hi_u32 s80, s67, s96\n" \
    "s_add_u32 s80, s80, s78\n" \
    "s_waitcnt lgkmcnt(0)\n" \
    "s_sub_i32 s79, s80, s38\n" \
    "s_max_i32 s79, s79, 0\n" \
    "s_cmp_gt_u32 s79, 320\n" \
    "s_cbranch_scc1 L%=_Pu5s\n" \
    ".p2align 4\n" \
    "L%=_Pu5d:\n" \
    "s_sub_u32 s79, s79, 28\n" \
    "s_cbranch_scc0 L%=_Pu5d\n" \
    "L%=_Pu5e:\n" \
    "buffer_load_dwordx4 v[168:171], v225, s[40:43], s60 offen sc1\n" \
    "buffer_load_dwordx4 v[172:175], v225, s[40:43], s85 offen sc1\n" \
    "L%=_Pvc:\n" \
    "s_waitcnt vmcnt(1)\n" \
    "v_smfmac_f32_16x16x64_bf16 v[176:179], v[168:171], v[0:7], v224\n" \
    "v_smfmac_f32_16x16x64_bf16 v[180:183], v[168:171], v[8:15], v224\n" \
    "s_waitcnt vmcnt(0)\n" \
    "v_smfmac_f32_16x16x64_bf16 v[176:179], v[172:175], v[32:39], v224\n" \
    "v_smfmac_f32_16x16x64_bf16 v[180:183], v[172:175], v[40:47], v224\n" \
    "v_smfmac_f32_16x16x64_bf16 v[184:187], v[168:171], v[16:23], v224\n" \
    "v_smfmac_f32_16x16x64_bf16 v[188:191], v[168:171], v[24:31], v224\n" \
    "v_add_f32 v176, v176, v178\n" \
    "v_smfmac_f32_16x16x64_bf16 v[184:187], v[172:175], v[48:55], v224\n" \
    "v_add_f32 v177, v177, v179\n" \
    "v_smfmac_f32_16x16x64_bf16 v[188:191], v[172:175], v[56:63], v224\n" \
    "v_cmp_u_f32 vcc, v176, v177\n" \
    "v_cvt_pk_bf16_f32 v64, v176, v177\n" \
    "v_add_f32 v180, v180, v182\n" \
    "v_add_f32 v181, v181, v183\n" \
    "v_cvt_pk_bf16_f32 v72, v180, v181\n" \
    "s_cbranch_vccnz L%=_Pvslow\n" \
    "L%=_Pvok:\n" \
    "v_add_f32 v184, v184, v186\n" \
    "v_add_f32 v185, v185, v187\n" \
    "v_cvt_pk_bf16_f32 v80, v184, v185\n" \
    "v_add_f32 v188, v188, v190\n" \
    "v_add_f32 v189, v189, v191\n" \
    "v_cvt_pk_bf16_f32 v88, v188, v189\n" \
    "s_barrier\n" \
    "L%=_Pfr:\n" \
    "ds_read_b128 v[192:195], v227\n" \
    "s_waitcnt lgkmcnt(0)\n" \
    "v_smfmac_f32_16x16x64_bf16 v[212:215], v[192:195], v[96:103], v224\n" \
    "v_cmp_ne_u32 vcc, s76, v195\n" \
    "v_smfmac_f32_16x16x64_bf16 v[196:199], v[192:195], v[64:71], v224\n" \
    "s_cbranch_vccnz L%=_Pfs\n" \
    "v_smfmac_f32_16x16x64_bf16 v[200:203], v[192:195], v[72:79], v224\n" \
    "v_smfmac_f32_16x16x64_bf16 v[204:207], v[192:195], v[80:87], v224\n" \
    "v_add_f32 v212, v212, v214\n" \
    "v_smfmac_f32_16x16x64_bf16 v[208:211], v[192:195], v[88:95], v224\n" \
    "v_add_f32 v213, v213, v215\n" \
    "v_rcp_f32 v220, v212\n" \
    "v_rcp_f32 v221, v213\n" \
    "v_add_f32 v196, v196, v198\n" \
    "v_add_f32 v197, v197, v199\n" \
    "v_pk_add_f32 v[200:201], v[200:201], v[202:203]\n" \
    "v_pk_add_f32 v[204:205], v[204:205], v[206:207]\n" \
    "s_nop 0\n" \
    "v_pk_add_f32 v[208:209], v[208:209], v[210:211]\n" \
    "v_pk_mul_f32 v[196:197], v[196:197], v[220:221]\n" \
    "v_pk_mul_f32 v[200:201], v[200:201], v[220:221]\n" \
    "v_pk_mul_f32 v[204:205], v[204:205], v[220:221]\n" \
    "v_pk_mul_f32 v[208:209], v[208:209], v[220:221]\n" \
    "v_cvt_pk_bf16_f32 v216, v196, v200\n" \
    "v_cvt_pk_bf16_f32 v217, v204, v208\n" \
    "v_cvt_pk_bf16_f32 v218, v197, v201\n" \
    "v_cvt_pk_bf16_f32 v219, v205, v209\n" \
    "buffer_store_dwordx4 v[216:219], v226, s[40:43], s60 offen sc1\n" \
    "s_setprio 0\n" \
    "ds_read_b64 v[220:221], v229 offset:64\n" \
    "s_mov_b32 s79, s60\n" \
    "s_mov_b32 s60, s61\n" \
    "s_mov_b32 s61, s62\n" \
    "s_mov_b32 s62, s63\n" \
    "s_mov_b32 s63, s79\n" \
    "s_add_u32 s76, s76, 1\n" \
    "s_add_u32 s77, s77, s65\n" \
    "s_add_u32 s84, s84, 0x20000\n" \
    "s_add_u32 s81, s81, s65\n" \
    "s_waitcnt lgkmcnt(0)\n" \
    "v_readfirstlane_b32 s65, v220\n" \
    "v_readfirstlane_b32 s71, v221\n" \
    "s_mul_i32 s96, s65, s71\n" \
    "s_lshr_b32 s96, s96, 4\n" \
    "s_branch L%=_Ploop\n" \
    "L%=_Pfslow:\n" \
    "s_sleep 4\n" \
    "buffer_load_dword v237, v235, s[44:47], 0 offen sc1\n" \
    "s_waitcnt vmcnt(0)\n" \
    "v_cmp_ge_u32 vcc, s82, v237\n" \
    "s_cbranch_vccz L%=_Pfok\n" \
    "s_cmp_eq_u32 s99, 0\n" \
    "s_cbranch_scc1 L%=_Pfok\n" \
    "s_sub_u32 s99, s99, 1\n" \
    "s_branch L%=_Pfslow\n" \
    "L%=_Pfin:\n" \
    "s_waitcnt vmcnt(0)\n" \
    "s_barrier\n" \
    "v_mov_b32 v238, 64\n" \
    "buffer_store_dword v238, v236, s[44:47], 0 offen sc1\n" \
    "s_branch L%=_end\n" \
    "L%=_Pu3s:\n" \
    "s_cmp_gt_u32 s79, 0x8000\n" \
    "s_cbranch_scc1 L%=_Pu3e\n" \
    "s_sleep 2\n" \
    "s_branch L%=_Pu3a\n" \
    "L%=_Pu4s:\n" \
    "s_mov_b32 s77, s79\n" \
    "s_memtime s[38:39]\n" \
    "s_memrealtime vcc\n" \
    "s_nop 4\n" \
    "s_memrealtime s[32:33]\n" \
    "s_nop 4\n" \
    "s_memrealtime s[34:35]\n" \
    "s_nop 4\n" \
    "s_memrealtime s[36:37]\n" \
    "s_add_u32 s80, s77, s64\n" \
    "s_lshl_b32 s80, s80, 2\n" \
    "s_waitcnt lgkmcnt(0)\n" \
    "s_sub_u32 s80, s80, vcc_lo\n" \
    "s_sub_u32 s80, s80, s32\n" \
    "s_sub_u32 s80, s80, s34\n" \
    "s_sub_u32 s80, s80, s36\n" \
    "s_mul_i32 s80, s80, s71\n" \
    "s_ashr_i32 s80, s80, 6\n" \
    "s_add_u32 s78, s80, s38\n" \
    "s_branch L%=_Pu4k\n" \
    "L%=_Pu5s:\n" \
    "s_cmp_gt_u32 s79, 0x8000\n" \
    "s_cbranch_scc1 L%=_Pu5e\n" \
    "s_sleep 2\n" \
    "s_branch L%=_Pu5a\n" \
    "L%=_Pvslow:\n" \
    "s_nop 7\n" \
    "s_nop 0\n" \
    "v_max_u32 v216, v168, v169\n" \
    "v_max3_u32 v216, v216, v170, v171\n" \
    "v_max3_u32 v216, v216, v172, v173\n" \
    "v_max3_u32 v216, v216, v174, v175\n" \
    "v_cmp_eq_u32 vcc, -1, v216\n" \
    "s_cbranch_vccz L%=_Pvok\n" \
    "s_cmp_eq_u32 s99, 0\n" \
    "s_cbranch_scc1 L%=_Pvok\n" \
    "s_sub_u32 s99, s99, 1\n" \
    "s_cmp_lg_u32 s99, 0\n" \
    "s_cbranch_scc1 L%=_Pu6\n" \
    "v_mov_b32 v216, 1\n" \
    "buffer_store_dword v216, off, s[44:47], 0 sc1\n" \
    "L%=_Pu6:\n" \
    "s_sleep 1\n" \
    "buffer_load_dwordx4 v[168:171], v225, s[40:43], s60 offen sc1\n" \
    "buffer_load_dwordx4 v[172:175], v225, s[40:43], s85 offen sc1\n" \
    "v_mov_b64 v[176:177], 0\n" \
    "v_mov_b64 v[178:179], 0\n" \
    "v_mov_b64 v[180:181], 0\n" \
    "v_mov_b64 v[182:183], 0\n" \
    "v_mov_b64 v[184:185], 0\n" \
    "v_mov_b64 v[186:187], 0\n" \
    "v_mov_b64 v[188:189], 0\n" \
    "v_mov_b64 v[190:191], 0\n" \
    "s_waitcnt vmcnt(0)\n" \
    "s_branch L%=_Pvc\n" \
    "L%=_Pfs:\n" \
    "s_nop 7\n" \
    "s_nop 0\n" \
    "v_mov_b64 v[196:197], 0\n" \
    "v_mov_b64 v[198:199], 0\n" \
    "v_mov_b64 v[200:201], 0\n" \
    "v_mov_b64 v[202:203], 0\n" \
    "v_mov_b64 v[204:205], 0\n" \
    "v_mov_b64 v[206:207], 0\n" \
    "v_mov_b64 v[208:209], 0\n" \
    "v_mov_b64 v[210:211], 0\n" \
    "v_mov_b64 v[212:213], 0\n" \
    "v_mov_b64 v[214:215], 0\n" \
    "s_branch L%=_Pfr\n" \
    "L%=_end:\n"
#define V8(b) "v" #b "0", "v" #b "1", "v" #b "2", "v" #b "3", "v" #b "4", "v" #b "5", "v" #b "6", "v" #b "7", "v" #b "8", "v" #b "9"
#define S_IO "+{s[40:55]}"(sA), "+{s[56:71]}"(sB), "+{s[72:75]}"(sC), "+{s78}"(lwo), "={s[92:99]}"(so)
#define FIN2() do {} while (0)
#define S_CLOB "memory", "vcc", "scc", "m0", "s32", "s33", "s34", "s35", "s36", "s37", "s38", "s39", "s76", "s77", "s79", "s80", "s81", "s82", "s83", "s84", "s85", "s86", "s87", "s88", "s89", "s90", "s91"
#define B_CLOB S_CLOB, "v64", "v65", "v66", "v67", "v68", "v69", V8(7), V8(8), V8(9), V8(10), V8(11), V8(12), V8(13), V8(14), V8(15), V8(16), V8(17), V8(18), V8(19), V8(20), V8(21), \
    "v220", "v221", "v222", "v223", "v240", "v241", "v242", "v243"
#define FIN() do {} while (0)

#define MK_S() \
    a = args(); \
    unsigned so4[NSLOT]; \
    const __attribute__((address_space(4))) unsigned* pm = (const __attribute__((address_space(4))) unsigned*)(size_t)(a->ctl + F_PM / 4);     \
_Pragma("unroll") \
    for (int k = 0; k < NSLOT; ++k) so4[k] = k * (unsigned)SLOT + (pm[2 * k + 1] < pm[2 * k] ? 8192u : 0u); \
    v16u sA = cat8(cat4(mkrs(a->ring, NSLOT * SLOT), mkrs(a->ctl, CTL_SZ)), cat4(mkrs(a->mb, NSEG * NH * 4096), mkrs(a->qkv + QSZ, QSZ))); \
    v16u sB = cat8(cat4(mkrs(a->qkv + 2 * (size_t)QSZ, QSZ), v4u{so4[0], so4[1], so4[2], so4[3]}), \
                   v8u{pm[8], (unsigned)a->per0, (unsigned)a->d1, (unsigned)a->dv, (unsigned)a->d2, (unsigned)a->dp, (unsigned)a->slack, (unsigned)a->thr});

#define START() __hip_atomic_fetch_max((LAS unsigned*)(lds + L_MISC), (rt32() - sB[8] - spare) / sB[14] + 1, __ATOMIC_RELAXED, __HIP_MEMORY_SCOPE_WORKGROUP)

DEV void chain(LAS char* lds, const int c) {
    KArgs a = args();
    const int w = __builtin_amdgcn_readfirstlane(threadIdx.x >> 6), h = c >> 2, j = c & 3;
    const unsigned lb = (unsigned)(size_t)lds, lbw = lb + L_STG + w * 16384;
#define LANE() int l = threadIdx.x & 63; asm volatile("" : "+v"(l)); const int l15 = l & 15, g = l >> 4, hb = (l >> 1) & 1, q4 = l15 >> 2; const unsigned idx = hb ? 0xEEEE : 0x4444
    v4u sC, stv; v2u stw; unsigned stx; v8u so;
    unsigned lwo = w & 3;
    if (w < 4) {
        LANE();
        v16u wt[12], m;
            const v4u rw[3] = {mkrs(a->Wk, DM * HD * 2), mkrs(a->Wv, DM * HD * 2), mkrs(a->Wq, DM * HD * 2)};
            const unsigned vo = (l >> 2) * (HD * 2) + (l & 3) * 16, sw = (256 * w * HD + h * DH + 32 * j) * 2;
            auto issue = [&](int p) {
#pragma unroll
                for (int i = 0; i < 8; ++i) DMA16N(rw[p >> 1], vo, sw + ((p & 1) * 128 + i * 16) * (HD * 2), lbw + (p & 1) * 8192 + i * 1024); };
            {
                const v4u rp = w & 1 ? rw[2] : rw[1];
                const unsigned ps = ((rp[0] >> 7) + (c >> 5) * 16) & 63, po = w < 2 ? ((3 - ps) & 7) * 128 + (l & 1) * 1024 : (63 - ps < 16 && !(l & 1) ? (63 - ps) * 128 : OOB);
                if (a->pf) asm volatile("s_mov_b32 m0, %0\n\ts_nop 0\n\tbuffer_load_dword %1, %2, 0 offen lds" :: "s"(lb + L_PF + w * 256), "v"(((c & 31) * 32 + (l >> 1)) * (HD * 2) + (c >> 5) * 2048 + po), "s"(rp) : "memory", "m0");
            }
            unsigned mr[16];
#pragma unroll
            for (int k = 0; k < 16; ++k) mr[k] = *(const unsigned*)(a->mem0 + (2 * q4 + (l & 1)) * DM + 256 * w + 16 * k + 4 * g + 2 * hb);
            SCHED();
            issue(0); issue(1);
            RBAR();
            MK_S();
            unsigned spare = a->spare; asm volatile("" : "+s"(sA), "+s"(sB), "+s"(spare));
            const LAS char* pw = lds + L_STG + w * 16384 + (4 * (g >> 1) + (g & 1) + 2 * (q4 >> 1) + 16 * (q4 & 1)) * 64 + (l & 3) * 8;
#pragma unroll
            for (int p = 0; p < 6; ++p) {
                if (p < 5) WAITV(8); else WAITV(0);
#pragma unroll
                for (int k = 0; k < 2; ++k) wt[(2 * p + k + 4) % 12] = cat8(rd_w<2048, 512, 2560>(pw + (p & 1) * 8192 + k * 4096), rd_w<2048, 512, 2560>(pw + (p & 1) * 8192 + k * 4096 + 32));
                WAITL(0);
                if (p < 4) issue(p + 2);
            }
#pragma unroll
            for (int k = 0; k < 16; k += 4) {
                m[k] = __builtin_amdgcn_perm(mr[k + 1], mr[k], 0x05040100u); m[k + 1] = __builtin_amdgcn_perm(mr[k + 3], mr[k + 2], 0x05040100u);
                m[k + 2] = __builtin_amdgcn_perm(mr[k + 1], mr[k], 0x07060302u); m[k + 3] = __builtin_amdgcn_perm(mr[k + 3], mr[k + 2], 0x07060302u);
            }
        const unsigned pq = nearo(H3_OFF + (c >> 5) * 16384) + w * 1024 + l * 16;
        const v8u va = {idx, pq, 0, 0, lb + L_MISC + w * 4, lb + L_MISC, 0, pq + 16384};
        sC = v4u{lb + w * 2176u, lb + w * 1280u, 0, 0};
        START();
        asm volatile(ASM_A : S_IO, "+{v[192:207]}"(m)
                     : "{v[0:31]}"(cat16(wt[0], wt[1])), "{v[32:63]}"(cat16(wt[2], wt[3])), "{v[64:95]}"(cat16(wt[4], wt[5])), "{v[96:127]}"(cat16(wt[6], wt[7])),
                       "{v[128:159]}"(cat16(wt[8], wt[9])), "{v[160:191]}"(cat16(wt[10], wt[11])), "{v[232:239]}"(va)
                     : S_CLOB, "v208", "v209", V8(21), V8(22), "v230", "v231", "v240", "v241", "v244", "v245", "v246", "v247");
        FIN();
    } else {
        LANE();
        const int bw = w - 4, l7 = l & 7;
        if (w == 6 && l < 16) ((LAS unsigned*)(lds + L_MISC))[l] = 0;
        BAR();
        MK_S();
        {
            const v4u rw = mkrs(a->Wo, HD * DM * 2);
            const unsigned vo = (l >> 3) * (DM * 2) + l7 * 16, sw = (h * DH * DM + 256 * j + 64 * bw) * 2;
#pragma unroll
            for (int i = 0; i < 16; ++i) DMA16N(rw, vo, sw + i * 8 * (DM * 2), lbw + i * 1024);
        }
        WAITV(0);
        const LAS char* pw = lds + L_STG + w * 16384 + (16 * (g & 1) + 8 * (g >> 1) + (q4 & 1) + 4 * (q4 >> 1)) * 128 + (l & 3) * 8;
#define RDO(o) rd_w<256, 4096, 4352>(pw + (o))
        const v32u wo0 = cat16(cat8(RDO(0), RDO(32)), cat8(RDO(64), RDO(96))), wo1 = cat16(cat8(RDO(8192), RDO(8224)), cat8(RDO(8256), RDO(8288)));
        const unsigned c3 = ((c >> 3) & 3) * 4096 + (c >> 5) * 1024 + (c & 7) * 128, h1 = nearo(H1_OFF + h * 8192), h1j = h1 + j * 2048;
        v16u va = {};
        va[0] = idx; va[1] = h1 + (g >> 1) * 2048 + 1280 + (l & 31) * 16;
        va[2] = nearo(H2_OFF + (32 * j + 8 * bw + q4 * 2 + (g >> 1)) * 4096) + h * 128 + ((g & 1) * 4 + (l & 3)) * 16;
        va[3] = lb + L_P + g * 320 + l15 * 16;
        va[4] = lb + (l & 1) * X_BLK + (l & 2) * X_FQ + (8 * (g >> 1) + (q4 & 1) + 4 * (q4 >> 1)) * 16 + (g & 1) * 8;
        va[5] = lb + L_MISC;
        v4u sel = {};
        if (w == 4) {
            const int tok = l & 7, u = l >> 5, z = (l >> 3) & 3;
            va[6] = h1 + (z == 0 ? 8 + u : (z == 1 ? 2 * u : (z == 2 ? 4 + 2 * u : 2 * u + 1))) * 128 + tok * 16;
            va[7] = h1 + (5 + 2 * u) * 128 + tok * 16 + (z & 1) * 8;
            va[8] = lb + L_P + ((z & 2) + u) * 320 + (4 * (tok >> 1) + 2 * (z & 1) + (tok & 1)) * 16;
            *(LAS v4u*)(lds + va[8]) = v4u{POI, POI, POI, POI};
            sC = v4u{__float_as_uint(SCALE * LOG2E), __float_as_uint(SCALE * LOG2E), 0, 0};
        } else if (w == 5) {
            va[6] = lb + L_PQK + (l15 >> 3) * 256 + (l & 1) * 544 + (l7 >> 1) * 64 + g * 16;
            va[7] = (l & 8) ? OOB : h1j + g * 128 + l7 * 16;
            va[8] = ((l & 8) || g < 2) ? OOB : h1j + 1024 + ((g & 1) * 8 + l7) * 16;
            va[9] = (l & 8) ? h * 4096 + j * 512 + (g * 8 + l7) * 16 : OOB;
            va[10] = lb + l * 16;
            sC = v4u{0, 0, 0, 0};
        } else if (w == 6) {
            const int rho = (l >> 1) & 15, tok = 2 * (rho >> 2) + (rho & 1), c4 = 2 * (l >> 5) + ((rho >> 1) & 1);
            va[6] = lb + ((l & 1) * 160 + (tok & 1) * 72 + (tok >> 1) * 16 + c4 * 4) * 4;
            va[7] = (l & 1) ? OOB : h1j + 1280 + (l >> 1) * 16;
            va[8] = lb + L_VT + tok * 64 + (l & 1) * 32 + c4 * 8;
            va[9] = lb + L_VT + q4 * 64 + (g & 1) * 32 + (l & 3) * 8;
            va[10] = l < 32 ? h * 4096 + 2048 + (32 * j + l) * 16 : OOB;
            va[11] = nearo(H2_OFF + c * 4096) + l * 16;
            va[12] = (l & 1) ? OOB : nearo(H3_OFF + (2 * (g >> 1) + (l15 >> 3)) * 16384 + c3) + ((l7 & 6) + (g & 1)) * 16;
#pragma unroll
            for (int i = 0; i < 4; ++i) sel[i] = (l7 == 2 * i ? 0x3f80u : 0u) | (l7 == 2 * i + 1 ? 0x3f800000u : 0u);
            sC = v4u{0, 0, 0, 0};
        } else {
            const rsrc_t rk = mkrsrc(a->qkv + QSZ, QSZ), rv = mkrsrc(a->qkv + 2 * (size_t)QSZ, QSZ);
            wait_flags(mkrsrc(a->ctl, CTL_SZ), F_QKV + h * 16 + 4 + (l & 1) * 4, 1, a->ctl);
            v4u pf[10];
#pragma unroll
            for (int i = 0; i < 8; ++i) pf[i] = LDB(rv, i, h * 8192);
            pf[8] = LDB(rk, 0, h * 8192 + j * 2048); pf[9] = LDB(rk, 1, h * 8192 + j * 2048);
            va[13] = LD4(mkrsrc(a->ctl, CTL_SZ), F_QKV + h * 16 + 4 + (l & 1) * 4, 0);
#pragma unroll
            for (int i = 0; i < 8; ++i) *(LAS v4u*)(lds + L_X + i * X_BLK + g * X_FQ + l15 * 16) = pf[i];
            *(LAS v4u*)(lds + L_K + l * 16) = pf[8]; *(LAS v4u*)(lds + L_K + 1024 + l * 16) = pf[9];
            FENCE(); ((volatile LAS unsigned*)(lds + L_MISC))[2] = 1;
            va[6] = l * 16; va[7] = lb + g * X_FQ + l15 * 16;
            va[8] = nearo(H2_OFF + c * 4096) + l * 16; va[9] = h1j + l * 16;
            va[10] = nearo(H3_OFF + (l >> 3) * 16384 + c3) + l7 * 16;
            va[11] = F_QKV + h * 16 + 4 + (l & 1) * 4; va[12] = l == 0 ? F_PG + h * 128 + j * 4 : OOB;
            va[15] = l == 0 && (c & 31) == 0 ? F_CK + 384 + (c >> 5) * 4 : OOB;
            sC = v4u{h * 8192u, __float_as_uint(a->per), 0, j * 2048u};
        }
        while (((volatile LAS unsigned*)(lds + L_MISC))[2] == 0) __builtin_amdgcn_s_sleep(1);
        asm volatile(ASM_B : S_IO, "+{v[224:239]}"(va), "+{v[244:247]}"(sel) : "{v[0:31]}"(wo0), "{v[32:63]}"(wo1) : B_CLOB);
        FIN();
    }
}

#define RING_LD(K, B) asm volatile("buffer_load_dwordx4 %0, %1, %2, %3 offen offset:%4 sc1" : "+v"(ring[K]) : "v"(l16), "s"(rso), "s"((B) + ((K) >> 2) * 4096), "n"(((K) & 3) * 1024) : "memory")
#define RING_MM(N, K, C0, C1) asm volatile("s_waitcnt vmcnt(" #N ")\n\tv_mfma_f32_16x16x32_bf16 %0, %3, %2, %0\n\tv_mfma_f32_16x16x32_bf16 %1, %4, %2, %1" : "+v"(C0), "+v"(C1), "+v"(ring[K]) : "v"(wo[K][0]), "v"(wo[K][1]) : "memory")
#define RING_ALL(P) P(ring[0]), P(ring[1]), P(ring[2]), P(ring[3]), P(ring[4]), P(ring[5]), P(ring[6]), P(ring[7]), P(ring[8]), P(ring[9]), P(ring[10]), P(ring[11]), P(ring[12]), P(ring[13]), P(ring[14]), P(ring[15])
static constexpr int RED_LD = 36 * 4, RED_W = 32 * RED_LD, RED_B = 8 * RED_W;

DEV void worker(LAS char* lds, const int wk) {
    KArgs a = args();
    const int tid = threadIdx.x, w = __builtin_amdgcn_readfirstlane(tid >> 6), l = tid & 63, fr = l & 15, fq = l >> 4;
    const int g = wk >> 5, i = wk & 31;
    const rsrc_t rc = mkrsrc(a->ctl, CTL_SZ);
    unsigned* err = a->ctl;
    unsigned* dbg = a->dbg + 32768 + wk * 32;
    WSTAMP(0);
    const v4u rx = mkrs(a->x, S_TOK * DM * 2);
    const int role = i >> 3;
    const int h = 8 * g + (i & 7);
    if (role >= 2) {
        const unsigned t0 = rt32(), d = role == 2 ? a->wq : a->wx;
        while (rt32() - t0 < d) __builtin_amdgcn_s_sleep(8);
    }
    if (role == 1) stream_qkv<true>(lds, rx, mkrs(a->Wv, DM * HD * 2), mkrsrc(a->qkv + 2 * (size_t)QSZ, QSZ), rc, F_QKV + h * 16 + 8, h, dbg);
    else if (role < 3) stream_qkv<false>(lds, rx, mkrs(role ? a->Wq : a->Wk, DM * HD * 2), mkrsrc(a->qkv + (role ? 0 : QSZ), QSZ), rc, F_QKV + h * 16 + (role ? 0 : 4), h, dbg);
    else if (h < NMOE) {
        const int sel = top8(mkrsrc(a->gr, S_TOK * 8), l);
        if (h == 0 && tid < TOPC) ST4(mkrsrc(a->idx, 32), tid * 4, 0, sel);
        WSTAMP(1);
        moe(lds, rx, sel, mkrs(a->wg, DM * FF * 2), mkrs(a->wu, DM * FF * 2), mkrs(a->wd, FF * DM * 2), mkrsrc(a->part, NMOE * TOPC * DM * 4), h);
        if (tid == 0) ST4(rc, F_M1 + h * 4, 0, 1u);
    }
    WSTAMP(2);
    a = args();
    const rsrc_t rq = mkrsrc(a->qkv, QSZ), rk = mkrsrc(a->qkv + QSZ, QSZ), rv = mkrsrc(a->qkv + 2 * (size_t)QSZ, QSZ), rm = mkrsrc(a->mb, NSEG * NH * 4096), ro = mkrsrc(a->ob, QSZ);
    const rsrc_t ri = mkrsrc(a->idx, 32), rp = mkrsrc(a->part, NMOE * TOPC * DM * 4);
    const int cgp = role == 3 ? h : (g < 3 ? i : 24 + (i & 7)), seg0 = role == 3 ? 0 : 16 + (g < 3 ? g : role), sst = role == 3 ? 1 : 3;
    v4u wo[16][2];
    {
        const v4u rw = mkrs(a->Wo, HD * DM * 2);
        const unsigned vo = (l >> 2) * (DM * 2) + (l & 3) * 16, lb = (unsigned)(size_t)lds + w * 16384;
        const LAS char* p = lds + w * 16384 + (4 * fq + (fr >> 2)) * 64 + (l & 3) * 8;
#pragma unroll
        for (int hf = 0; hf < 2; ++hf) {
#pragma unroll
            for (int k = 0; k < 16; ++k) DMA16(rw, vo, ((512 * w + 256 * hf + 16 * k) * DM + 32 * cgp) * 2, lb + k * 1024);
            WAITV(0);
#pragma unroll
            for (int k = 0; k < 8; ++k)
#pragma unroll
                for (int n = 0; n < 2; ++n) wo[hf * 8 + k][n] = cat(trd(p + k * 2048 + n * 32), trd(p + k * 2048 + n * 32 + 1024));
            WAITL(0);
        }
    }
    WSTAMP(7);
    volatile LAS unsigned* prog = (volatile LAS unsigned*)(lds + 2 * RED_B);
    LAS float* upd = (LAS float*)(lds + 2 * RED_B + 64);
    int xs = i & 7, nx = role == 3 ? 8 : 0;
    unsigned seen = 0;
    int sel[8];
    {
        wait_flags(rc, F_M1 + min(l, NMOE - 1) * 4, 1, err);
        const unsigned t = LD4(ri, (l >> 3) * 4, 0);
        v4f p = {0, 0, 0, 0};
#pragma unroll
        for (int q = 0; q < 4; ++q) p += __builtin_bit_cast(v4f, LD16(rp, (w * 4 + q) * 32768 + (l >> 3) * 4096 + cgp * 128 + (l & 7) * 16, 0));
#pragma unroll
        for (int q = 0; q < 8; ++q) sel[q] = __builtin_amdgcn_readlane(t, q * 8);
        const float r = rbf(sigmoidf_(a->gr[2 * t + 1]));
        RBAR();
        ((LAS v4f*)lds)[tid] = p;
        BAR();
        if (w == 0) {
#pragma unroll
            for (int q = 1; q < 7; ++q) p += ((LAS v4f*)lds)[q * 64 + l];
            ((LAS v4f*)upd)[l] = v4f{rbf(rbf(p[0]) * r), rbf(rbf(p[1]) * r), rbf(rbf(p[2]) * r), rbf(rbf(p[3]) * r)};
        }
    }
    bool pre = false;
    v4u ring[16];
    asm volatile("" : RING_ALL("=v"));
    const v4u rso = mkrs(a->ob, QSZ), rsc = mkrs(a->ctl, CTL_SZ), rsg = mkrs(a->gr, S_TOK * 8);
    BAR();
    WSTAMP(8);
#pragma unroll 1
    for (int n = 0;; ++n) {
        const int seg = seg0 + n * sst;
#pragma unroll 1
        while (nx > 0 && (xs <= seg + 7 || (unsigned)xs < seen || n == 16)) {
            asm volatile("s_waitcnt vmcnt(0)" :: RING_ALL("v") : "memory");
            xattn(rq, rk, rv, rm, ro, rc, err, xs, 8 * g + w, l); xs += 8; --nx; pre = false;
            asm volatile("" : RING_ALL("=v"));
        }
        if (n == 16) break;
        const unsigned ob = (unsigned)(seg * 256 + w * 16) * 1024u;
        int l = tid & 63; asm volatile("" : "+v"(l));
        const int fr = l & 15, fq = l >> 4, erow = w * 4 + fq, ecol = 32 * cgp + 2 * fr, l16 = l * 16;
        const unsigned fo = __builtin_amdgcn_readfirstlane(n < 15 ? F_XA + (seg + sst) * 64 + (w >> 1) * 4 : OOB);
        if (!pre) {
            wait_flags(rc, F_XA + seg * 64 + (w >> 1) * 4, 1, err);
#pragma unroll
            for (int k = 0; k < 16; ++k) RING_LD(k, ob);
        }
        const int row = seg * 32 + erow;
        int slot = -1;
#pragma unroll
        for (int q = 0; q < 8; ++q) slot = sel[q] == row ? q : slot;
        unsigned fn, pg, xv; float gt;
        v4f c[2][2] = {};
        asm volatile("buffer_load_dword %[fn], off, %[rc], %[fo] sc1\n\tbuffer_load_dword %[pg], off, %[rc], %[po] sc1\n\tbuffer_load_dword %[xv], %[xo], %[rx], 0 offen\n\t"
                     "buffer_load_dword %[gt], %[go], %[rg], 0 offen"
                     : [fn] "=&v"(fn), [pg] "=&v"(pg), [xv] "=&v"(xv), [gt] "=&v"(gt), "+v"(c[0][0]), "+v"(c[0][1]), "+v"(c[1][0]), "+v"(c[1][1])
                     : [rc] "s"(rsc), [fo] "s"(fo), [po] "s"(F_PG + g * 1024), [rx] "s"(rx), [rg] "s"(rsg),
                       [xo] "v"((row * DM + ecol) * 2), [go] "v"(row * 8) : "memory");
#pragma unroll
        for (int k = 0; k < 16; ++k) { RING_MM(19, k, c[0][0], c[0][1]); RING_LD(k, ob + 131072); }
        RING_MM(15, 0, c[1][0], c[1][1]);
        asm volatile("" : "+v"(fn), "+v"(pg), "+v"(xv), "+v"(gt));
        pre = __builtin_amdgcn_readfirstlane(fn) != 0;
        const unsigned nb = pre ? ob + sst * 262144 : OOB;
#pragma unroll
        for (int k = 0; k < 16; ++k) { if (k) RING_MM(15, k, c[1][0], c[1][1]); RING_LD(k, nb); }
        asm volatile("s_nop 7\n\ts_nop 1" : "+v"(c[0][0]), "+v"(c[0][1]), "+v"(c[1][0]), "+v"(c[1][1]));
        LAS char* rb = lds + (n & 1) * RED_B;
#pragma unroll
        for (int u = 0; u < 2; ++u)
#pragma unroll
            for (int t = 0; t < 2; ++t) *(LAS v4f*)(rb + w * RED_W + (u * 16 + fr) * RED_LD + (t * 16 + 4 * fq) * 4) = c[u][t];
        if (tid == 0) prog[n & 1] = pg;
        BAR();
        v2f acc = {0, 0};
#pragma unroll
        for (int q = 0; q < 8; ++q) acc += *(const LAS v2f*)(rb + q * RED_W + erow * RED_LD + fr * 8);
        seen = __builtin_amdgcn_readfirstlane(prog[n & 1]);
        float b0 = lo16(xv), b1 = hi16(xv);
        if (slot >= 0) { b0 = rbf(b0 + upd[slot * 32 + 2 * fr]); b1 = rbf(b1 + upd[slot * 32 + 2 * fr + 1]); }
        const float omg = rbf(1.0f - gt);
        *(unsigned*)(a->out + (size_t)row * DM + ecol) = pk2(rbf(gt * rbf(acc[0])) + rbf(omg * b0), rbf(gt * rbf(acc[1])) + rbf(omg * b1));
        WSTAMP(9 + n);
    }
    asm volatile("s_waitcnt vmcnt(0)" :: RING_ALL("v") : "memory");
    WSTAMP(25);
}

__global__ void __launch_bounds__(512, 2) k_main(Args) {
    __shared__ __attribute__((aligned(16))) char lds[L_TOT];
    const int b = blockIdx.x, c = (b & 3) * 32 + (b >> 3);
    if ((b & 7) < 4) chain((LAS char*)lds, c); else worker((LAS char*)lds, c);
}
__global__ void __launch_bounds__(512, 2) k_init(Args a) {
    const int b = blockIdx.x, tid = threadIdx.x, w = __builtin_amdgcn_readfirstlane(tid >> 6), l = tid & 63;
    const rsrc_t rr = mkrsrc(a.ring, NSLOT * SLOT);
    if (b < 64 && (b & 7) == 0) {
        if (w == 0) {
            unsigned off = (b >> 4) * (unsigned)SLOT + ((b >> 3) & 1) * 8192 + nearo(PR_OFF) + (l & 31) * 4, t = 0xffffffffu;
            for (int it = 0; it < 4; ++it) { const unsigned t0 = cyc32(); ST4(rr, off, 0, off); WAITV(0); t = min(t, cyc32() - t0); off += 256; }
            if (l == 0) a.ctl[F_PM / 4 + (b >> 3)] = t;
            if (l == 0 && b == 0) a.ctl[F_PM / 4 + 8] = rt32();
        }
        RBAR();
    }
    const v4u poi4 = {POI, POI, POI, POI};
    ST16(rr, b * 16384 + tid * 16, 0, poi4); ST16(rr, b * 16384 + 8192 + tid * 16, 0, poi4);
    if (tid < 8) ST16(mkrsrc(a.ctl, CTL_SZ), b * 128 + tid * 16, 0, (v4u{0, 0, 0, 0}));
    prep_row(a.x, a.gw, a.w_r, a.gr, b * 8 + w, l);
}

#define TUNE_DEFAULT {206, 78, 80, 156, 8, 80, 352, 100, 400, 600, 1}

extern "C" void kernel_launch(void* const* d_in, const int* in_sizes, int n_in, void* d_out, int out_size, void* d_ws, size_t ws_size, hipStream_t stream) {
    char* ring = (char*)(((size_t)d_ws + SLOT - 1) & ~(size_t)(SLOT - 1));
    char* ws = ring + NSLOT * SLOT; size_t off = 0;
    auto alloc = [&](size_t bytes) { void* p = ws + off; off += (bytes + 255) & ~(size_t)255; return p; };
    Args a;
    a.x = (const bf16_t*)d_in[0]; a.mem0 = (const bf16_t*)d_in[1]; a.Wq = (const bf16_t*)d_in[2]; a.Wk = (const bf16_t*)d_in[3]; a.Wv = (const bf16_t*)d_in[4]; a.Wo = (const bf16_t*)d_in[5];
    a.w_r = (const bf16_t*)d_in[8]; a.wg = (const bf16_t*)d_in[9]; a.wu = (const bf16_t*)d_in[10]; a.wd = (const bf16_t*)d_in[11]; a.gw = (const bf16_t*)d_in[12];
    a.qkv = (char*)alloc(3 * (size_t)QSZ); a.mb = (char*)alloc(NSEG * NH * 4096); a.ob = (char*)alloc(QSZ);
    a.gr = (float*)alloc(S_TOK * 8); a.idx = (int*)alloc(256); a.part = (float*)alloc(NMOE * TOPC * DM * 4); a.out = (bf16_t*)d_out;
    a.ring = ring; a.ctl = (unsigned*)alloc(CTL_SZ + 256); a.dbg = (unsigned*)alloc((65536 + 1024 * 512) * 4);
    const int tn[11] = TUNE_DEFAULT;
    a.per = tn[0] * 368.0f; a.per0 = tn[0] * 368 / tn[6]; a.pf = tn[10];
    a.d1 = ((u64)tn[1] << 32) / tn[0]; a.dv = ((u64)tn[2] << 32) / tn[0]; a.d2 = ((u64)tn[3] << 32) / tn[0]; a.dp = tn[4]; a.slack = tn[5]; a.thr = tn[6]; a.spare = tn[7]; a.wq = tn[8]; a.wx = tn[9];
    k_init<<<256, 512, 0, stream>>>(a);
    k_main<<<256, 512, 0, stream>>>(a);
}
```

```cpp
#include <hip/hip_runtime.h>
#include <stdint.h>

typedef unsigned short bf16_t;
typedef unsigned v2u __attribute__((ext_vector_type(2)));
typedef unsigned v4u __attribute__((ext_vector_type(4)));
typedef unsigned v8u __attribute__((ext_vector_type(8)));
typedef float v2f __attribute__((ext_vector_type(2)));
typedef float v4f __attribute__((ext_vector_type(4)));
typedef short v4s __attribute__((ext_vector_type(4)));
typedef __bf16 v2b __attribute__((ext_vector_type(2)));
typedef __bf16 v8b __attribute__((ext_vector_type(8)));
typedef __bf16 v16b __attribute__((ext_vector_type(16)));
typedef unsigned long long u64;
typedef __amdgpu_buffer_rsrc_t rsrc_t;
#define LAS __attribute__((address_space(3)))
#define DEV __device__ __forceinline__

static constexpr int S_TOK = 2048, DM = 1024, NH = 32, DH = 128, SEG = 32, NSEG = 64;
static constexpr int HD = NH * DH;
static constexpr int FF = 3584, TOPC = 8;
static constexpr float SCALE = 0.08838834764831845f, LOG2E = 1.4426950408889634f, MASKV = -998244352.0f;

DEV unsigned pk2(float lo, float hi) { v2f f = {lo, hi}; return __builtin_bit_cast(unsigned, __builtin_convertvector(f, v2b)); }
DEV float lo16(unsigned u) { return __uint_as_float(u << 16); }
DEV float hi16(unsigned u) { return __uint_as_float(u & 0xffff0000u); }
DEV float rbf(float f) { return __uint_as_float(pk2(0.0f, f)); }
DEV v4f smf(v4u a, v8u b, v4f c, int idx) { return __builtin_amdgcn_smfmac_f32_16x16x64_bf16(__builtin_bit_cast(v8b, a), __builtin_bit_cast(v16b, b), c, idx, 0, 0); }
DEV v4f mf(v4u a, v4u b, v4f c) { return __builtin_amdgcn_mfma_f32_16x16x32_bf16(__builtin_bit_cast(v8b, a), __builtin_bit_cast(v8b, b), c, 0, 0, 0); }
DEV v2u trd(const LAS char* p) { return __builtin_bit_cast(v2u, __builtin_amdgcn_ds_read_tr16_b64_v4i16((LAS v4s*)p)); }
DEV v4u cat(v2u a, v2u b) { return __builtin_shufflevector(a, b, 0, 1, 2, 3); }
DEV v2u sw16(unsigned x, unsigned y) { return __builtin_amdgcn_permlane16_swap(x, y, false, false); }
DEV v2u sw32(unsigned x, unsigned y) { return __builtin_amdgcn_permlane32_swap(x, y, false, false); }
DEV float half_add(float x, float y) { v2u r = sw32(__float_as_uint(x), __float_as_uint(y)); return __uint_as_float(r[0]) + __uint_as_float(r[1]); }
DEV float ror8(float v) { return __int_as_float(__builtin_amdgcn_update_dpp(0, __float_as_int(v), 0x128, 0xf, 0xf, false)); }
template <bool MAX> DEV float red_g(float v) {
    auto op = [](float a, float b) { return MAX ? fmaxf(a, b) : a + b; };
    v2u r = sw16(__float_as_uint(v), __float_as_uint(v)); v = op(__uint_as_float(r[0]), __uint_as_float(r[1]));
    r = sw32(__float_as_uint(v), __float_as_uint(v)); return op(__uint_as_float(r[0]), __uint_as_float(r[1]));
}
DEV float sigmoidf_(float z) { return 1.0f / (1.0f + __expf(-z)); }
DEV v4u mkrs(const void* p, unsigned bytes) { const u64 a = (u64)p; return v4u{(unsigned)a, (unsigned)(a >> 32) & 0xffffu, bytes, 0x00020000u}; }
DEV rsrc_t mkrsrc(const void* p, unsigned bytes) { return __builtin_amdgcn_make_buffer_rsrc((void*)p, (short)0, (int)bytes, 0x00020000); }

#define ST16(RS, OFF, SOFF, V) __builtin_amdgcn_raw_buffer_store_b128(V, RS, (OFF) + (SOFF), 0, 16)
#define NTS 0
#define ST16X(RS, OFF, V, NT) __builtin_amdgcn_raw_buffer_store_b128(V, RS, OFF, 0, (NT) ? 18 : 16)
#define LD16X(RS, OFF, SOFF, NT) __builtin_amdgcn_raw_buffer_load_b128(RS, OFF, SOFF, (NT) ? 18 : 16)
#define ST4(RS, OFF, SOFF, V) __builtin_amdgcn_raw_buffer_store_b32(V, RS, OFF, SOFF, 16)
#define LD16(RS, OFF, SOFF) __builtin_amdgcn_raw_buffer_load_b128(RS, OFF, SOFF, 16)
#define LD8(RS, OFF, SOFF) __builtin_amdgcn_raw_buffer_load_b64(RS, OFF, SOFF, 16)
#define LD4(RS, OFF, SOFF) __builtin_amdgcn_raw_buffer_load_b32(RS, OFF, SOFF, 16)
#define DMA16(RS, VOFF, SOFF, LOFF) asm volatile("s_mov_b32 m0, %0\n\ts_nop 0\n\tbuffer_load_dwordx4 %1, %2, %3 offen lds" :: "s"(LOFF), "v"(VOFF), "s"(RS), "s"(SOFF) : "memory", "m0")
#define DMA16N(RS, VOFF, SOFF, LOFF) asm volatile("s_mov_b32 m0, %0\n\ts_nop 0\n\tbuffer_load_dwordx4 %1, %2, %3 offen nt lds" :: "s"(LOFF), "v"(VOFF), "s"(RS), "s"(SOFF) : "memory", "m0")
#define WSTAMP(k) do {} while (0)
#define EXP 0
#define XI 3
#define BK 9
#define FENCE() asm volatile("" ::: "memory")
#define WAITV(n) asm volatile("s_waitcnt vmcnt(" #n ")" ::: "memory")
#define WAITL(n) asm volatile("s_waitcnt lgkmcnt(" #n ")" ::: "memory")
#define RBAR() asm volatile("s_barrier" ::: "memory")
#define BAR() asm volatile("s_waitcnt lgkmcnt(0)\n\ts_barrier" ::: "memory")
#define SCHED() __builtin_amdgcn_sched_barrier(0)

static constexpr int XP = 1056, XS = 32 * XP, L_RED = 4 * XS;

DEV unsigned wt_src(int tid, unsigned ldw) { return (tid >> 4) * ldw + (((tid & 15) ^ ((((tid >> 4) & 3) << 2) | (((tid >> 7) & 1) << 1))) << 4); }
DEV int wt_rd(int w, int l, int n) { const int fr = l & 15, fq = l >> 4; return (w >> 2) * 16384 + (8 * fq + (fr >> 2)) * 256 + (((4 * (w & 3) + 2 * n + ((fr >> 1) & 1)) ^ (((fr >> 2) << 2) | ((fq & 1) << 1))) << 4) + (fr & 1) * 8; }
DEV v4u wt_frag(const LAS char* p) { return cat(trd(p), trd(p + 1024)); }
#define WT_ISSUE(R0, R1, VB, S0, S1, LDW, LB) do { DMA16N(R0, VB, S0, LB); DMA16N(R0, VB, (S0) + 32 * (LDW), (LB) + 8192); DMA16N(R1, VB, S1, (LB) + 16384); DMA16N(R1, VB, (S1) + 32 * (LDW), (LB) + 24576); } while (0)

static constexpr unsigned F_BC = 512, F_CK = 2048, F_EX = 12288, F_W = 16384, F_PG = 32768, F_GR = 49152, CTL_SZ = 65536;
DEV unsigned fw(int role, int h) { return F_W + ((h >> 3) * 32 + role * 8 + (h & 7)) * 128; }
static constexpr unsigned POI = 0xFFFFFFFFu, OOB = 0x40000000u, SPIN_CAP = 200000u;
DEV bool spin_fail(unsigned tries, unsigned* err) {
    __builtin_amdgcn_s_sleep(1);
    if ((tries & 255u) != 255u) return false;
    unsigned e; asm volatile("global_load_dword %0, %1, off sc1\n\ts_waitcnt vmcnt(0)" : "=&v"(e) : "v"(err) : "memory");
    if (__builtin_amdgcn_readfirstlane(e) == 1u) return true;
    if (tries > SPIN_CAP) { unsigned one = 1u; asm volatile("global_store_dword %0, %1, off sc1" :: "v"(err), "v"(one) : "memory"); return true; }
    return false;
}
DEV bool all_ge(unsigned v, unsigned t) { return __builtin_amdgcn_ballot_w64(v < t) == 0; }
template <int Z = 6> DEV void wait_flags(rsrc_t rc, unsigned off, unsigned t, unsigned* err) {
    for (unsigned tries = 0;; ++tries) {
        FENCE();
        if (all_ge(LD4(rc, off, 0), t)) break;
        __builtin_amdgcn_s_sleep(Z);
        if (spin_fail(tries, err)) break;
    }
    FENCE();
}

#define BC_LD(B, RS) asm volatile("buffer_load_dword %0, off, %1, 0 offset:512 sc1" : "=&v"(B) : "s"(RS) : "memory")
DEV unsigned beacon(v4u rs, unsigned b0, unsigned b1, unsigned need) {
    asm volatile("" : "+v"(b0), "+v"(b1));
    for (unsigned tries = 0;; ++tries) {
        const unsigned u = __builtin_amdgcn_readfirstlane(b1);
        if ((u != __builtin_amdgcn_readfirstlane(b0) && (u & need) == need) || tries > SPIN_CAP) return u;
        __builtin_amdgcn_s_sleep(4);
        BC_LD(b1, rs); asm volatile("s_waitcnt vmcnt(0)" : "+v"(b1) :: "memory");
    }
}

template <bool VT> DEV void stream_qkv(LAS char* lds, v4u rx, v4u rw, rsrc_t ro, rsrc_t rc, v4u rsc, bool hs, unsigned fo, int h, unsigned* dbg) {
    const int tid = threadIdx.x, w = __builtin_amdgcn_readfirstlane(tid >> 6), l = tid & 63, kh = w >> 2, cg = w & 3;
    const unsigned lb = (unsigned)(size_t)lds, vb = wt_src(tid, HD * 2), l16 = l * 16;
    const unsigned x0 = (4 * cg + (VT ? 2 : 16) * (1 - kh)) * 2048 + kh * 1024, x1 = (4 * cg + (VT ? 2 : 16) * kh) * 2048 + kh * 1024;
#define STW(s) WT_ISSUE(rw, rw, vb, h * 256 + (s) * (64 * HD * 2), h * 256 + ((s) + 8) * (64 * HD * 2), HD * 2, lb + w * 1024 + ((s) & 3) * XS)
#define STX(u, q, e) DMA16(rx, l16, ((u) >> 1) * 65536 + ((q) & 1 ? x1 : x0) + (VT ? ((e) & 1) + 16 * ((e) >> 1) : (e)) * 2048, lb + (w * 4 + (e)) * XP + (q) * XS)
    const unsigned fl = tid == 0 ? fo : OOB;
    v4u W[16][2];
    unsigned b0, b1;
    STW(0); STW(1); STW(2); STW(3);
    BC_LD(b0, rsc);
#pragma unroll
    for (int s = 0; s < 8; ++s) {
        WAITV(12); RBAR();
#pragma unroll
        for (int n = 0; n < 2; ++n) { const LAS char* p = lds + wt_rd(w, l, n) + (s & 3) * XS; W[2 * s][n] = wt_frag(p); W[2 * s + 1][n] = wt_frag(p + 8192); }
        BAR();
        if (s == 4) BC_LD(b1, rsc);
        if (s == 7) { if (hs) beacon(rsc, b0, b1, 0); ST4(rc, OOB, 0, 0u); FENCE(); }
        if (s < 4) STW(s + 4); else { STX(s - 4, s - 4, 0); STX(s - 4, s - 4, 1); if (s < 7) { STX(s - 4, s - 4, 2); STX(s - 4, s - 4, 3); } }
    }
    const LAS char* px = lds + (kh * 16 + (l & 15)) * XP + (l >> 4) * 16;
    LAS v4f* ps = (LAS v4f*)(lds + L_RED + w * 2048 + l16);
    const LAS v4f* pr = (const LAS v4f*)(lds + L_RED + (w ^ 4) * 2048 + l16);
    unsigned oo = OOB, on = (unsigned)h * 8192u + cg * 2048u + kh * 1024u + l16;
    v4f c[2][2] = {}, r0 = {}, r1 = {};
    v4u f[8];
    auto fin = [&]() { r0 += c[1][0]; r1 += c[1][1]; ST16X(ro, oo, (v4u{pk2(r0[0], r0[1]), pk2(r0[2], r0[3]), pk2(r1[0], r1[1]), pk2(r1[2], r1[3])}), NTS & 1); oo = on; on += NH * 8192u; };
    WSTAMP(3);
    WAITV(10); RBAR();
    WSTAMP(4);
    const LAS char* px2 = px + 2 * XS; asm volatile("" : "+v"(px2));
#define RDX(q, k) f[(k) & 7] = *(const LAS v4u*)(((q) & 2 ? px2 : px) + ((q) & 1) * XS + (k) * 64)
#pragma unroll
    for (int k = 0; k < 8; ++k) RDX(0, k);
#pragma unroll 1
    for (int u0 = 0; u0 < 2 * NSEG; u0 += 4) {
        if (u0 == 4) WSTAMP(5);
        if (u0 == 64) WSTAMP(6);
#pragma unroll
        for (int q = 0; q < 4; ++q) {
#pragma unroll
            for (int k = 0; k < 16; ++k) {
                const v4u x = f[k & 7];
#pragma unroll
                for (int n = 0; n < 2; ++n) { const v4f z = k ? c[q & 1][n] : v4f{0, 0, 0, 0}; c[q & 1][n] = VT ? mf(x, W[k][n], z) : mf(W[k][n], x, z); }
                if (k < 8) RDX(q, k + 8);
                if (k == BK) { SCHED(); WAITV(10); BAR(); for (int e = 0; e < BK - 8; ++e) RDX(q + 1, e); }
                if (k >= BK) RDX(q + 1, k - 8);
                if (((k + 16 - BK) & 3) == 0) asm volatile("s_setprio %0" :: "n"(3 - (((k + 16 - BK) >> 2) & 3)));
                if (k == 2) { if (q & 1) { ps[0] = c[0][0]; ps[64] = c[0][1]; } else fin(); }
                if (k == 13) { if (q & 1) { r0 = pr[0]; r1 = pr[64]; } else ST4(rc, fl, 0, max((u0 + q - 2) >> 1, 0)); }
                if (!(EXP & 1) && !(k & 1) && kh == (((k >> 1) ^ ((BK + 1) >> 1)) & 1)) { if (k > BK) STX(u0 + q + 4, q, (k - BK) >> 2); else STX(u0 + q + 3, (q + 3) & 3, (k + 16 - BK) >> 2); }
                SCHED();
            }
        }
    }
    fin(); __builtin_amdgcn_s_setprio(0);
    WAITV(0); BAR();
    if (tid == 0) ST4(rc, fo, 0, NSEG);
#undef STW
#undef STX
}

#define LDBX(RS, K, BASE, NT) LD16X(RS, l * 16 + ((K) & 3) * 1024, (BASE) + ((K) >> 2) * 4096, NT)
#define LDB(RS, K, BASE) LDBX(RS, K, BASE, 0)
DEV unsigned xattn(rsrc_t rq, rsrc_t rk, rsrc_t rv, rsrc_t rm, rsrc_t ro, rsrc_t rc, unsigned* err, int seg, int h, int l, unsigned fl, unsigned* dbg) {
#define XST(k) do {} while (0)
    XST(3);
    asm volatile("" : "+v"(l));
    const int fr = l & 15, g = l >> 4;
    const unsigned sb = (unsigned)(seg * NH + h) * 8192u, mb = (unsigned)(seg * NH + h) * 4096u;
    const unsigned f0 = fw(min(l & 3, 2), h) + 12, f1 = F_PG + h * 512 + (l & 3) * 128;
    const unsigned old = __builtin_amdgcn_ballot_w64(fl <= (unsigned)seg);
    if (old & 15) wait_flags(rc, f0, seg + 1, err);
    XST(4);
    v4u q[4][2], k[4][2], km[4], v[8]; v2u vm[8];
    auto ldm = [&]() {
#pragma unroll
        for (int j = 0; j < 4; ++j) km[j] = LD16(rm, (g * 8 + (l & 7)) * 16 + j * 512, mb);
#pragma unroll
        for (int d = 0; d < 8; ++d) vm[d] = LD8(rm, 2048 + fr * 16 + (g & 1) * 8 + d * 256, mb); };
#pragma unroll
    for (int j = 0; j < 8; ++j) q[j >> 1][j & 1] = LDBX(rq, j, sb, 1);
    if (!(old & 240)) {
#pragma unroll
        for (int j = 0; j < 8; ++j) k[j >> 1][j & 1] = LDBX(rk, j, sb, 1);
#pragma unroll
        for (int d = 0; d < 8; ++d) v[d] = LDBX(rv, d, sb, 1);
        ldm();
    } else {
#pragma unroll
        for (int j = 0; j < 8; ++j) k[j >> 1][j & 1] = LDB(rk, j, sb);
#pragma unroll
        for (int d = 0; d < 8; ++d) v[d] = LDB(rv, d, sb);
    }
    v4f sx[3] = {}, o[2][8];
    float mx[2], zx[2];
    constexpr float C = SCALE * LOG2E, MR = MASKV / SCALE;
#pragma unroll
    for (int j = 0; j < 4; ++j) { sx[0] = mf(k[j][0], q[j][0], sx[0]); sx[1] = mf(k[j][0], q[j][1], sx[1]); sx[2] = mf(k[j][1], q[j][1], sx[2]); }
#pragma unroll
    for (int u = 0; u < 2; ++u) {
        float e[8];
#pragma unroll
        for (int r = 0; r < 4; ++r) { const bool vis = 4 * g + r <= fr; e[r] = (u || vis) ? sx[u][r] : MR; e[4 + r] = (u && vis) ? sx[2][r] : MR; }
        float m = e[0];
#pragma unroll
        for (int r = 1; r < 8; ++r) m = fmaxf(m, e[r]);
        mx[u] = m = red_g<true>(m); zx[u] = 0;
#pragma unroll
        for (int r = 0; r < 8; ++r) { e[r] = __builtin_amdgcn_exp2f((e[r] - m) * C); zx[u] += e[r]; }
        const v4u P = {pk2(e[0], e[1]), pk2(e[4], e[5]), pk2(e[2], e[3]), pk2(e[6], e[7])};
#pragma unroll
        for (int d = 0; d < 8; d += 2) {
            o[u][d] = mf(__builtin_shufflevector(v[d], v[d + 1], 0, 1, 4, 5), P, v4f{0, 0, 0, 0});
            o[u][d + 1] = mf(__builtin_shufflevector(v[d], v[d + 1], 2, 3, 6, 7), P, v4f{0, 0, 0, 0});
        }
    }
    asm volatile("" : "+v"(zx[0]), "+v"(zx[1]));
    SCHED(); XST(5);
    if (old & 240) { wait_flags<2>(rc, f1, seg + 1, err); ldm(); }
    XST(6);
    v4f sm[2] = {};
#pragma unroll
    for (int j = 0; j < 4; ++j) { sm[0] = mf(km[j], q[j][0], sm[0]); sm[1] = mf(km[j], q[j][1], sm[1]); }
    XST(28);
#pragma unroll
    for (int u = 0; u < 2; ++u) {
        const float m = red_g<true>(fmaxf(fmaxf(fmaxf(sm[u][0], sm[u][1]), fmaxf(sm[u][2], sm[u][3])), mx[u])), al = __builtin_amdgcn_exp2f((mx[u] - m) * C);
        float e[4];
#pragma unroll
        for (int r = 0; r < 4; ++r) e[r] = __builtin_amdgcn_exp2f((sm[u][r] - m) * C);
        const float inv = __builtin_amdgcn_rcpf(red_g<false>(al * zx[u] + 0.5f * (e[0] + e[1] + e[2] + e[3]))), f = al * inv, im = g < 2 ? inv : 0.0f;
        const unsigned pm0 = pk2(e[0] * im, e[1] * im), pm1 = pk2(e[2] * im, e[3] * im);
        const v4u Pa = {pm0, pm1, 0u, 0u}, Pb = {0u, 0u, pm0, pm1};
#pragma unroll
        for (int d = 0; d < 4; ++d) {
            const v4u vv = cat(vm[2 * d], vm[2 * d + 1]);
            const v4f o0 = mf(vv, Pa, o[u][2 * d] * f), o1 = mf(vv, Pb, o[u][2 * d + 1] * f);
            ST16(ro, l * 16 + d * 1024, (unsigned)((seg * 2 + u) * 128 + h * 4) * 1024u, (v4u{pk2(o0[0], o0[1]), pk2(o0[2], o0[3]), pk2(o1[0], o1[1]), pk2(o1[2], o1[3])}));
        }
    }
    XST(29);
    FENCE(); const unsigned fn = LD4(rc, fl > seg + 4u ? OOB : (l & 4 ? f1 : f0), 0);
    WAITV(1); XST(30); RBAR(); XST(31);
    if (threadIdx.x == 0) ST4(rc, fw(3, (h & 24) + (seg & 3)) + 64 + (seg & 60), 0, 1u);
    FENCE();
    return max(fl, fn);
}

template <int N> DEV unsigned ror(unsigned v) { return __builtin_amdgcn_update_dpp(0u, v, 0x120 + N, 0xf, 0xf, false); }
DEV void prep_rows(const bf16_t* x, const bf16_t* gw, const bf16_t* wr, rsrc_t rg, int row, int l) {
    v4u xv[16], gv[2], rv[2];
#pragma unroll
    for (int i = 0; i < 2; ++i) { gv[i] = *(const v4u*)(gw + i * 512 + l * 8); rv[i] = *(const v4u*)(wr + i * 512 + l * 8); }
#pragma unroll
    for (int i = 0; i < 16; ++i) xv[i] = *(const v4u*)(x + (size_t)row * DM + i * 512 + l * 8);
    float ga = 0, gb = 0;
#pragma unroll
    for (int k = 0; k < 8; ++k) {
        float a = 0, b = 0;
#pragma unroll
        for (int i = 0; i < 8; ++i) {
            const unsigned u = xv[2 * k + (i >> 2)][i & 3], g = gv[i >> 2][i & 3], r = rv[i >> 2][i & 3];
            a += rbf(lo16(u) * lo16(g)) + rbf(hi16(u) * hi16(g));
            b += lo16(u) * lo16(r) + hi16(u) * hi16(r);
        }
        a = half_add(a, b);
        a += __uint_as_float(ror<8>(__float_as_uint(a))); a += __uint_as_float(ror<4>(__float_as_uint(a)));
        a += __uint_as_float(ror<2>(__float_as_uint(a))); a += __uint_as_float(ror<1>(__float_as_uint(a)));
        const v2u s = sw16(__float_as_uint(a), __float_as_uint(a)); a = __uint_as_float(s[0]) + __uint_as_float(s[1]);
        if ((l & 31) == k) ga = a;
    }
    gb = rbf(ga);
    const unsigned u = __float_as_uint(l < 32 ? rbf(sigmoidf_(gb)) : gb) | 1u;
    const v2u e = sw32(u, u);
    ST16(rg, l < 8 && !(l & 1) ? F_GR + (row + l) * 8 : OOB, 0, (v4u{e[0], e[1], ror<15>(e[0]), ror<15>(e[1])}));
}
template <int N> DEV unsigned top_keys(unsigned* key, int l) {
    unsigned res = 0;
#pragma unroll 1
    for (int c = 0; c < TOPC; ++c) {
        unsigned m = key[0];
#pragma unroll
        for (int i = 1; i < N; ++i) m = max(m, key[i]);
        m = max(m, ror<8>(m)); m = max(m, ror<4>(m)); m = max(m, ror<2>(m)); m = max(m, ror<1>(m));
        v2u s = sw16(m, m); m = max(s[0], s[1]); s = sw32(m, m); m = max(s[0], s[1]);
#pragma unroll
        for (int i = 0; i < N; ++i) key[i] = key[i] == m ? 0u : key[i];
        if (l == c) res = m;
    }
    return res;
}
DEV int top8(LAS unsigned* cand, rsrc_t rc, unsigned* err, int w, int l) {
    v4u t[2];
    for (unsigned tries = 0;; ++tries) {
        FENCE();
        t[0] = LD16(rc, F_GR + w * 2048 + l * 16, 0); t[1] = LD16(rc, F_GR + w * 2048 + 1024 + l * 16, 0);
        const v4u v = t[0] & t[1];
        if (all_ge(v[0] & v[1] & v[2] & v[3] & 1u, 1u)) break;
        __builtin_amdgcn_s_sleep(8);
        if (spin_fail(tries, err)) break;
    }
    unsigned key[4];
#pragma unroll
    for (int i = 0; i < 4; ++i) { const unsigned r = t[i >> 1][(i & 1) * 2 + 1] - 1u; key[i] = ((int)r < 0 ? 0xffff0000u - r : r + 0x80000000u) + (0xffffu - (w * 256 + (i >> 1) * 128 + l * 2 + (i & 1))); }
    key[0] = top_keys<4>(key, l);
    if (l < TOPC) cand[w * TOPC + l] = key[0];
    BAR();
    key[0] = cand[l];
    return 0xffff - (int)(top_keys<1>(key, l) & 0xffffu);
}
static constexpr int NMOE = FF / 128, XR = 2080;
DEV void moe(LAS char* lds, v4u rx, int sel, v4u rg, v4u ru, v4u rd, rsrc_t rp, int cu) {
    const int tid = threadIdx.x, w = __builtin_amdgcn_readfirstlane(tid >> 6), l = tid & 63, kh = w >> 2, cg = w & 3, fr = l & 15, fq = l >> 4;
    const unsigned lb = (unsigned)(size_t)lds, v1 = wt_src(tid, FF * 2), v2 = wt_src(tid, DM * 2);
    const unsigned xo = __builtin_amdgcn_readlane(sel, w) * 2048;
    DMA16(rx, l * 16, xo, lb + L_RED + w * XR); DMA16(rx, l * 16, xo + 1024, lb + L_RED + w * XR + 1024);
    auto issue = [&](int s) {
        const unsigned d = lb + w * 1024 + (s & 3) * XS;
        if (s < 16) { const unsigned o = s * (64 * FF * 2) + cu * 256; WT_ISSUE(rg, ru, v1, o, o, FF * 2, d); }
        else { const unsigned o = s < 24 ? (cu * 128 + (s & 1) * 64) * (DM * 2) + (((s >> 1) + cu) & 3) * 512 : OOB; WT_ISSUE(rd, rd, v2, o, o + 256, DM * 2, d); } };
    issue(0); issue(1); issue(2); issue(3);
    const int p0 = wt_rd(w, l, 0), p1 = wt_rd(w, l, 1);
    v4f c[2] = {};
    auto stage = [&](int s, const LAS char* pa) {
        WAITV(12); RBAR();
        const LAS char* p = lds + (s & 3) * XS;
        const v4u a0 = *(const LAS v4u*)pa, a1 = *(const LAS v4u*)(pa + 64), b0 = wt_frag(p + p0), b1 = wt_frag(p + p1), b2 = wt_frag(p + p0 + 8192), b3 = wt_frag(p + p1 + 8192);
        BAR();
        issue(s + 4);
        c[0] = mf(b2, a1, mf(b0, a0, c[0])); c[1] = mf(b3, a1, mf(b1, a0, c[1])); };
#pragma unroll 1
    for (int s = 0; s < 16; ++s) stage(s, lds + L_RED + (l & 7) * XR + fq * 16 + s * 128);
    LAS v4f* pu = (LAS v4f*)(lds + L_RED + cg * 2048 + l * 16);
    if (kh) { pu[0] = c[0]; pu[64] = c[1]; }
    BAR();
    if (!kh && fr < 8) {
#pragma unroll
        for (int n = 0; n < 2; ++n) {
            const v4f u = pu[64 * n]; float hv[4];
#pragma unroll
            for (int e = 0; e < 4; ++e) { const float g = rbf(c[n][e]); hv[e] = rbf(g * sigmoidf_(g)) * rbf(u[e]); }
            *(LAS v2u*)(lds + L_RED + 8192 + fr * 288 + cg * 64 + n * 32 + fq * 8) = v2u{pk2(hv[0], hv[1]), pk2(hv[2], hv[3])};
        }
    }
    WAITL(0);
#pragma unroll 1
    for (int s = 16; s < 24; s += 2) {
        c[0] = v4f{0, 0, 0, 0}; c[1] = c[0];
        const LAS char* pa = lds + L_RED + 8192 + (l & 7) * 288 + fq * 16;
        stage(s, pa); stage(s + 1, pa + 128);
        ST16(rp, cu * 32768 + (l & 7) * 4096 + ((((s >> 1) + cu) & 3) * 256 + kh * 128 + cg * 32 + (fr >> 3) * 16 + fq * 4) * 4, 0, __builtin_bit_cast(v4u, fr < 8 ? c[0] : c[1]));
    }
    WAITV(0); RBAR();
}

static constexpr unsigned H2_OFF = 0, H1_OFF = 512 * 1024, H3_OFF = 768 * 1024, PR_OFF = 896 * 1024;
static constexpr size_t SLOT = 2u << 20;
static constexpr int NSLOT = 4;
struct Args {
    const bf16_t* x; const bf16_t* mem0; const bf16_t* Wq; const bf16_t* Wk; const bf16_t* Wv; const bf16_t* Wo;
    const bf16_t* w_r; const bf16_t* wg; const bf16_t* wu; const bf16_t* wd; const bf16_t* gw;
    char* qkv; char* mb; char* ob; int* idx; float* part; float* upd; float* ex; char* wt; bf16_t* out;
    char* ring; unsigned* ctl; unsigned* dbg;
    float per; unsigned per0, d1, dv, d2, dp, slack, thr, spare, wq, wx, pf, ms, ws;
};
typedef const __attribute__((address_space(4))) Args* KArgs;
DEV KArgs args() { KArgs p = (KArgs)__builtin_amdgcn_kernarg_segment_ptr(); asm volatile("" : "+s"(p)); return p; }
static constexpr unsigned QSZ = S_TOK * HD * 2;
typedef unsigned v16u __attribute__((ext_vector_type(16)));
typedef unsigned v32u __attribute__((ext_vector_type(32)));

DEV unsigned nearo(unsigned off) { return (off >> 13) * 16384u + (off & 8191u); }
DEV unsigned rt32() { u64 t; asm volatile("s_memrealtime %0\n\ts_waitcnt lgkmcnt(0)" : "=s"(t) :: "memory"); return (unsigned)t; }
DEV unsigned cyc32() { u64 t; asm volatile("s_memtime %0\n\ts_waitcnt lgkmcnt(0)" : "=s"(t) :: "memory"); return (unsigned)t; }
template <int R1, int R2, int R3> DEV v8u rd_w(const LAS char* p) {
    return __builtin_shufflevector(cat(trd(p), trd(p + R1)), cat(trd(p + R2), trd(p + R3)), 0, 1, 2, 3, 4, 5, 6, 7);
}
DEV v16u cat8(v8u a, v8u b) { return __builtin_shufflevector(a, b, 0, 1, 2, 3, 4, 5, 6, 7, 8, 9, 10, 11, 12, 13, 14, 15); }
DEV v32u cat16(v16u a, v16u b) { return __builtin_shufflevector(a, b, 0, 1, 2, 3, 4, 5, 6, 7, 8, 9, 10, 11, 12, 13, 14, 15, 16, 17, 18, 19, 20, 21, 22, 23, 24, 25, 26, 27, 28, 29, 30, 31); }
DEV v8u cat4(v4u a, v4u b) { return __builtin_shufflevector(a, b, 0, 1, 2, 3, 4, 5, 6, 7); }

static constexpr int L_PQK = 0, L_PV = 8704, L_P = 13824, L_VT = 15360, L_X = 16384, L_K = L_X + 9216, L_MISC = 28672, L_PF = 29696, L_STG = 32768, L_TOT = 163840;
static constexpr int X_FQ = 272, X_BLK = 1152;
#define ASM_A \
    "s_setprio 1\n" \
    "s_mov_b32 s76, 0\n" \
    "s_mov_b32 s99, 0x20000\n" \
    "s_mov_b32 s84, 0\n" \
    "s_mov_b32 s81, 0\n" \
    "s_mov_b32 s86, 0\n" \
    "s_mov_b32 s87, 0\n" \
    "s_mov_b32 s88, 0\n" \
    "s_mov_b32 s89, 0\n" \
    "s_mov_b32 s90, 0\n" \
    "s_mov_b32 s91, 0\n" \
    "s_mov_b32 s92, 0\n" \
    "s_mov_b32 s93, 0\n" \
    "s_mov_b32 s94, 0\n" \
    "s_mov_b32 s95, 0\n" \
    "s_mov_b32 s96, 0\n" \
    "s_mov_b32 s97, 0\n" \
    "s_mov_b32 s98, 0\n" \
    "ds_read_b32 v244, v237\n" \
    "s_waitcnt lgkmcnt(0)\n" \
    "v_readfirstlane_b32 s94, v244\n" \
    "s_mul_i32 s79, s94, s70\n" \
    "s_add_u32 s79, s79, s81\n" \
    "s_mov_b32 s77, s79\n" \
    "s_mul_i32 s96, s65, s71\n" \
    "s_lshr_b32 s96, s96, 4\n" \
    "v_mov_b64 v[208:209], 0\n" \
    "v_mov_b64 v[210:211], 0\n" \
    "v_mov_b64 v[212:213], 0\n" \
    "v_mov_b64 v[214:215], 0\n" \
    "v_mov_b64 v[216:217], 0\n" \
    "v_mov_b64 v[218:219], 0\n" \
    "v_mov_b64 v[220:221], 0\n" \
    "v_mov_b64 v[222:223], 0\n" \
    "v_mov_b64 v[224:225], 0\n" \
    "v_mov_b64 v[226:227], 0\n" \
    "v_mov_b64 v[228:229], 0\n" \
    "v_mov_b64 v[230:231], 0\n" \
    "v_mov_b32 v241, 1\n" \
    "s_branch L%=_Acomp\n" \
    "L%=_Aloop:\n" \
    "s_and_b32 s95, s94, 1\n" \
    "s_lshl_b32 s95, s95, 17\n" \
    "s_add_u32 s95, s95, s60\n" \
    "s_add_u32 s85, s95, 0x1000\n" \
    "s_branch L%=_Au2b\n" \
    "L%=_Au2a:\n" \
    "s_sleep 4\n" \
    "L%=_Au2b:\n" \
    "s_memrealtime vcc\n" \
    "s_add_u32 s80, s77, s64\n" \
    "s_add_u32 s80, s80, -3\n" \
    "s_waitcnt lgkmcnt(0)\n" \
    "s_sub_i32 s79, s80, vcc_lo\n" \
    "s_sub_u32 s79, s79, 40\n" \
    "s_cmp_lt_u32 s79, 0x400\n" \
    "s_cbranch_scc1 L%=_Au2a\n" \
    "s_memtime s[38:39]\n" \
    "s_memrealtime vcc\n" \
    "s_nop 4\n" \
    "s_memrealtime s[32:33]\n" \
    "s_nop 4\n" \
    "s_memrealtime s[34:35]\n" \
    "s_nop 4\n" \
    "s_memrealtime s[36:37]\n" \
    "s_add_u32 s80, s77, s64\n" \
    "s_lshl_b32 s80, s80, 2\n" \
    "s_waitcnt lgkmcnt(0)\n" \
    "s_sub_u32 s80, s80, vcc_lo\n" \
    "s_sub_u32 s80, s80, s32\n" \
    "s_sub_u32 s80, s80, s34\n" \
    "s_sub_u32 s80, s80, s36\n" \
    "s_mul_i32 s80, s80, s71\n" \
    "s_ashr_i32 s80, s80, 6\n" \
    "s_add_u32 s78, s80, s38\n" \
    "L%=_Au3a:\n" \
    "s_memtime s[38:39]\n" \
    "s_mul_i32 s80, -3, 23\n" \
    "s_add_u32 s80, s80, s78\n" \
    "s_waitcnt lgkmcnt(0)\n" \
    "s_sub_i32 s79, s80, s38\n" \
    "s_max_i32 s79, s79, 0\n" \
    "s_cmp_gt_u32 s79, 320\n" \
    "s_cbranch_scc1 L%=_Au3s\n" \
    ".p2align 4\n" \
    "L%=_Au3d:\n" \
    "s_sub_u32 s79, s79, 28\n" \
    "s_cbranch_scc0 L%=_Au3d\n" \
    "L%=_Au3e:\n" \
    "L%=_Apoll:\n" \
    "ds_read_b32 v244, v237 offset:4\n" \
    "s_waitcnt lgkmcnt(0)\n" \
    "v_readfirstlane_b32 s79, v244\n" \
    "s_cmp_lt_u32 s79, s76\n" \
    "s_cbranch_scc1 L%=_Askip\n" \
    "L%=_Ago:\n" \
    "buffer_load_dwordx4 v[192:195], v233, s[40:43], s95 offen sc1\n" \
    "s_nop 5\n" \
    "buffer_load_dwordx4 v[196:199], v233, s[40:43], s85 offen sc1\n" \
    "s_nop 5\n" \
    "buffer_load_dwordx4 v[200:203], v239, s[40:43], s95 offen sc1\n" \
    "s_nop 5\n" \
    "buffer_load_dwordx4 v[204:207], v239, s[40:43], s85 offen sc1\n" \
    "L%=_Acomp:\n" \
    "s_mov_b32 m0, s72\n" \
    "s_waitcnt vmcnt(3)\n" \
    "v_smfmac_f32_16x16x64_bf16 v[208:211], v[192:195], v[0:7], v232\n" \
    "v_smfmac_f32_16x16x64_bf16 v[212:215], v[192:195], v[8:15], v232\n" \
    "v_smfmac_f32_16x16x64_bf16 v[216:219], v[192:195], v[64:71], v232\n" \
    "v_smfmac_f32_16x16x64_bf16 v[220:223], v[192:195], v[72:79], v232\n" \
    "v_smfmac_f32_16x16x64_bf16 v[224:227], v[192:195], v[128:135], v232\n" \
    "s_waitcnt vmcnt(2)\n" \
    "v_smfmac_f32_16x16x64_bf16 v[208:211], v[196:199], v[16:23], v232\n" \
    "v_smfmac_f32_16x16x64_bf16 v[212:215], v[196:199], v[24:31], v232\n" \
    "v_smfmac_f32_16x16x64_bf16 v[216:219], v[196:199], v[80:87], v232\n" \
    "v_smfmac_f32_16x16x64_bf16 v[220:223], v[196:199], v[88:95], v232\n" \
    "v_smfmac_f32_16x16x64_bf16 v[228:231], v[192:195], v[136:143], v232\n" \
    "s_waitcnt vmcnt(1)\n" \
    "v_smfmac_f32_16x16x64_bf16 v[208:211], v[200:203], v[32:39], v232\n" \
    "v_smfmac_f32_16x16x64_bf16 v[212:215], v[200:203], v[40:47], v232\n" \
    "v_smfmac_f32_16x16x64_bf16 v[216:219], v[200:203], v[96:103], v232\n" \
    "v_smfmac_f32_16x16x64_bf16 v[220:223], v[200:203], v[104:111], v232\n" \
    "v_smfmac_f32_16x16x64_bf16 v[224:227], v[196:199], v[144:151], v232\n" \
    "s_waitcnt vmcnt(0)\n" \
    "v_smfmac_f32_16x16x64_bf16 v[208:211], v[204:207], v[48:55], v232\n" \
    "v_smfmac_f32_16x16x64_bf16 v[212:215], v[204:207], v[56:63], v232\n" \
    "v_smfmac_f32_16x16x64_bf16 v[216:219], v[204:207], v[112:119], v232\n" \
    "v_add_f32 v208, v208, v210\n" \
    "v_smfmac_f32_16x16x64_bf16 v[220:223], v[204:207], v[120:127], v232\n" \
    "v_add_f32 v209, v209, v211\n" \
    "v_cmp_u_f32 vcc, v208, v209\n" \
    "v_add_f32 v212, v212, v214\n" \
    "v_add_f32 v213, v213, v215\n" \
    "v_add_f32 v216, v216, v218\n" \
    "v_add_f32 v217, v217, v219\n" \
    "s_nop 1\n" \
    "v_add_f32 v220, v220, v222\n" \
    "v_add_f32 v221, v221, v223\n" \
    "v_smfmac_f32_16x16x64_bf16 v[224:227], v[200:203], v[160:167], v232\n" \
    "s_cbranch_vccnz L%=_Aslow\n" \
    "L%=_Aok:\n" \
    "ds_write_addtid_b32 v208 offset:0\n" \
    "ds_write_addtid_b32 v209 offset:544\n" \
    "v_smfmac_f32_16x16x64_bf16 v[228:231], v[196:199], v[152:159], v232\n" \
    "ds_write_addtid_b32 v216 offset:256\n" \
    "ds_write_addtid_b32 v217 offset:800\n" \
    "v_smfmac_f32_16x16x64_bf16 v[224:227], v[204:207], v[176:183], v232\n" \
    "ds_write_addtid_b32 v212 offset:1088\n" \
    "ds_write_addtid_b32 v213 offset:1632\n" \
    "v_smfmac_f32_16x16x64_bf16 v[228:231], v[200:203], v[168:175], v232\n" \
    "ds_write_addtid_b32 v220 offset:1344\n" \
    "ds_write_addtid_b32 v221 offset:1888\n" \
    "ds_write_b32 v236, v241 offset:32\n" \
    "v_smfmac_f32_16x16x64_bf16 v[228:231], v[204:207], v[184:191], v232\n" \
    "s_barrier\n" \
    "s_mov_b32 m0, s73\n" \
    "v_add_f32 v224, v224, v226\n" \
    "v_add_f32 v225, v225, v227\n" \
    "ds_write_addtid_b32 v224 offset:8704\n" \
    "ds_write_addtid_b32 v225 offset:8992\n" \
    "s_nop 1\n" \
    "v_add_f32 v228, v228, v230\n" \
    "v_add_f32 v229, v229, v231\n" \
    "ds_write_addtid_b32 v228 offset:9344\n" \
    "ds_write_addtid_b32 v229 offset:9632\n" \
    "ds_write_b32 v236, v241 offset:16\n" \
    "v_mov_b64 v[208:209], 0\n" \
    "v_mov_b64 v[210:211], 0\n" \
    "v_mov_b64 v[212:213], 0\n" \
    "v_mov_b64 v[214:215], 0\n" \
    "v_mov_b64 v[216:217], 0\n" \
    "v_mov_b64 v[218:219], 0\n" \
    "v_mov_b64 v[220:221], 0\n" \
    "v_mov_b64 v[222:223], 0\n" \
    "v_mov_b64 v[224:225], 0\n" \
    "v_mov_b64 v[226:227], 0\n" \
    "v_mov_b64 v[228:229], 0\n" \
    "v_mov_b64 v[230:231], 0\n" \
    "v_add_u32 v241, 1, v241\n" \
    "ds_read_b32 v244, v237\n" \
    "s_waitcnt lgkmcnt(0)\n" \
    "v_readfirstlane_b32 s94, v244\n" \
    "s_mul_i32 s79, s94, s70\n" \
    "s_add_u32 s79, s79, s81\n" \
    "s_mov_b32 s77, s79\n" \
    "s_barrier\n" \
    "ds_read_b64 v[244:245], v237 offset:64\n" \
    "s_mov_b32 s79, s60\n" \
    "s_mov_b32 s60, s61\n" \
    "s_mov_b32 s61, s62\n" \
    "s_mov_b32 s62, s63\n" \
    "s_mov_b32 s63, s79\n" \
    "s_add_u32 s76, s76, 1\n" \
    "s_add_u32 s77, s77, s65\n" \
    "s_add_u32 s84, s84, 0x20000\n" \
    "s_add_u32 s81, s81, s65\n" \
    "s_waitcnt lgkmcnt(0)\n" \
    "v_readfirstlane_b32 s65, v244\n" \
    "v_readfirstlane_b32 s71, v245\n" \
    "s_mul_i32 s96, s65, s71\n" \
    "s_lshr_b32 s96, s96, 4\n" \
    "s_cmp_lt_u32 s76, 64\n" \
    "s_cbranch_scc1 L%=_Aloop\n" \
    "s_branch L%=_end\n" \
    "L%=_Aslow:\n" \
    "s_nop 7\n" \
    "s_nop 0\n" \
    "v_max_u32 v244, v192, v193\n" \
    "v_max3_u32 v244, v244, v194, v195\n" \
    "v_max3_u32 v244, v244, v196, v197\n" \
    "v_max3_u32 v244, v244, v198, v199\n" \
    "v_max3_u32 v244, v244, v200, v201\n" \
    "v_max3_u32 v244, v244, v202, v203\n" \
    "v_max3_u32 v244, v244, v204, v205\n" \
    "v_max3_u32 v244, v244, v206, v207\n" \
    "v_cmp_eq_u32 vcc, -1, v244\n" \
    "s_cbranch_vccz L%=_Aok\n" \
    "s_cmp_eq_u32 s99, 0\n" \
    "s_cbranch_scc1 L%=_Aok\n" \
    "s_sub_u32 s99, s99, 1\n" \
    "s_cmp_lg_u32 s99, 0\n" \
    "s_cbranch_scc1 L%=_Au5\n" \
    "v_mov_b32 v244, 1\n" \
    "buffer_store_dword v244, off, s[44:47], 0 sc1\n" \
    "L%=_Au5:\n" \
    "v_mov_b64 v[208:209], 0\n" \
    "v_mov_b64 v[210:211], 0\n" \
    "v_mov_b64 v[212:213], 0\n" \
    "v_mov_b64 v[214:215], 0\n" \
    "v_mov_b64 v[216:217], 0\n" \
    "v_mov_b64 v[218:219], 0\n" \
    "v_mov_b64 v[220:221], 0\n" \
    "v_mov_b64 v[222:223], 0\n" \
    "v_mov_b64 v[224:225], 0\n" \
    "v_mov_b64 v[226:227], 0\n" \
    "v_mov_b64 v[228:229], 0\n" \
    "v_mov_b64 v[230:231], 0\n" \
    "L%=_Ash:\n" \
    "s_memrealtime vcc\n" \
    "s_waitcnt lgkmcnt(0)\n" \
    "s_sub_u32 s80, vcc_lo, s64\n" \
    "s_sub_u32 s80, s80, 4\n" \
    "s_mul_i32 s32, s70, s71\n" \
    "s_lshr_b32 s32, s32, 4\n" \
    "L%=_Au6g:\n" \
    "s_add_u32 s77, s77, s70\n" \
    "s_add_u32 s78, s78, s32\n" \
    "s_add_u32 s94, s94, 1\n" \
    "s_sub_u32 s79, s80, s77\n" \
    "s_cmp_lt_u32 s79, 0x1000000\n" \
    "s_cbranch_scc1 L%=_Au6g\n" \
    "v_mov_b32 v244, s94\n" \
    "ds_max_u32 v237, v244\n" \
    "s_cmp_gt_i32 s79, -20\n" \
    "s_cbranch_scc1 L%=_Au6c\n" \
    "s_branch L%=_Au7b\n" \
    "L%=_Au7a:\n" \
    "s_sleep 4\n" \
    "L%=_Au7b:\n" \
    "s_memrealtime vcc\n" \
    "s_add_u32 s80, s77, s64\n" \
    "s_add_u32 s80, s80, -3\n" \
    "s_waitcnt lgkmcnt(0)\n" \
    "s_sub_i32 s79, s80, vcc_lo\n" \
    "s_sub_u32 s79, s79, 40\n" \
    "s_cmp_lt_u32 s79, 0x400\n" \
    "s_cbranch_scc1 L%=_Au7a\n" \
    "s_memtime s[38:39]\n" \
    "s_memrealtime vcc\n" \
    "s_nop 4\n" \
    "s_memrealtime s[32:33]\n" \
    "s_nop 4\n" \
    "s_memrealtime s[34:35]\n" \
    "s_nop 4\n" \
    "s_memrealtime s[36:37]\n" \
    "s_add_u32 s80, s77, s64\n" \
    "s_lshl_b32 s80, s80, 2\n" \
    "s_waitcnt lgkmcnt(0)\n" \
    "s_sub_u32 s80, s80, vcc_lo\n" \
    "s_sub_u32 s80, s80, s32\n" \
    "s_sub_u32 s80, s80, s34\n" \
    "s_sub_u32 s80, s80, s36\n" \
    "s_mul_i32 s80, s80, s71\n" \
    "s_ashr_i32 s80, s80, 6\n" \
    "s_add_u32 s78, s80, s38\n" \
    "L%=_Au6c:\n" \
    "s_and_b32 s95, s94, 1\n" \
    "s_lshl_b32 s95, s95, 17\n" \
    "s_add_u32 s95, s95, s60\n" \
    "s_add_u32 s85, s95, 0x1000\n" \
    "L%=_Au8a:\n" \
    "s_memtime s[38:39]\n" \
    "s_mul_i32 s80, -3, 23\n" \
    "s_add_u32 s80, s80, s78\n" \
    "s_waitcnt lgkmcnt(0)\n" \
    "s_sub_i32 s79, s80, s38\n" \
    "s_max_i32 s79, s79, 0\n" \
    "s_cmp_gt_u32 s79, 320\n" \
    "s_cbranch_scc1 L%=_Au8s\n" \
    ".p2align 4\n" \
    "L%=_Au8d:\n" \
    "s_sub_u32 s79, s79, 28\n" \
    "s_cbranch_scc0 L%=_Au8d\n" \
    "L%=_Au8e:\n" \
    "s_branch L%=_Apoll\n" \
    "L%=_Askip:\n" \
    "s_cmp_lt_u32 s99, 2\n" \
    "s_cbranch_scc1 L%=_Ago\n" \
    "s_sub_u32 s99, s99, 1\n" \
    "s_branch L%=_Ash\n" \
    "L%=_Au3s:\n" \
    "s_cmp_gt_u32 s79, 0x8000\n" \
    "s_cbranch_scc1 L%=_Au3e\n" \
    "s_sleep 2\n" \
    "s_branch L%=_Au3a\n" \
    "L%=_Au8s:\n" \
    "s_cmp_gt_u32 s79, 0x8000\n" \
    "s_cbranch_scc1 L%=_Au8e\n" \
    "s_sleep 2\n" \
    "s_branch L%=_Au8a\n" \
    "L%=_end:\n"
#define ASM_B \
    "s_and_b32 s79, s78, 3\n" \
    "s_cmp_eq_u32 s79, 1\n" \
    "s_cbranch_scc1 L%=_S\n" \
    "s_cmp_eq_u32 s79, 2\n" \
    "s_cbranch_scc1 L%=_R\n" \
    "s_cmp_eq_u32 s79, 3\n" \
    "s_cbranch_scc1 L%=_P\n" \
    "L%=_T:\n" \
    "s_mov_b32 s76, 0\n" \
    "s_mov_b32 s99, 0x20000\n" \
    "s_mov_b32 s84, 0\n" \
    "s_mov_b32 s81, 0\n" \
    "s_mov_b32 s86, 0\n" \
    "s_mov_b32 s87, 0\n" \
    "s_mov_b32 s88, 0\n" \
    "s_mov_b32 s89, 0\n" \
    "s_mov_b32 s90, 0\n" \
    "s_mov_b32 s91, 0\n" \
    "s_mov_b32 s92, 0\n" \
    "s_mov_b32 s93, 0\n" \
    "s_mov_b32 s94, 0\n" \
    "s_mov_b32 s95, 0\n" \
    "s_mov_b32 s96, 0\n" \
    "s_mov_b32 s97, 0\n" \
    "s_mov_b32 s98, 0\n" \
    "ds_read_b32 v216, v229\n" \
    "s_waitcnt lgkmcnt(0)\n" \
    "v_readfirstlane_b32 s94, v216\n" \
    "s_mul_i32 s79, s94, s70\n" \
    "s_add_u32 s79, s79, s81\n" \
    "s_mov_b32 s77, s79\n" \
    "s_mul_i32 s96, s65, s71\n" \
    "s_lshr_b32 s96, s96, 4\n" \
    "v_mov_b32 v96, 0x3f803f80\n" \
    "v_mov_b32 v97, 0x3f803f80\n" \
    "v_mov_b32 v98, 0x3f803f80\n" \
    "v_mov_b32 v99, 0x3f803f80\n" \
    "v_mov_b32 v100, 0x3f803f80\n" \
    "v_mov_b32 v101, 0x3f803f80\n" \
    "v_mov_b64 v[102:103], 0\n" \
    "v_mov_b64 v[64:65], 0\n" \
    "v_mov_b64 v[66:67], 0\n" \
    "v_mov_b64 v[68:69], 0\n" \
    "v_mov_b64 v[70:71], 0\n" \
    "v_mov_b64 v[72:73], 0\n" \
    "v_mov_b64 v[74:75], 0\n" \
    "v_mov_b64 v[76:77], 0\n" \
    "v_mov_b64 v[78:79], 0\n" \
    "v_mov_b64 v[80:81], 0\n" \
    "v_mov_b64 v[82:83], 0\n" \
    "v_mov_b64 v[84:85], 0\n" \
    "v_mov_b64 v[86:87], 0\n" \
    "v_mov_b64 v[88:89], 0\n" \
    "v_mov_b64 v[90:91], 0\n" \
    "v_mov_b64 v[92:93], 0\n" \
    "v_mov_b64 v[94:95], 0\n" \
    "v_mov_b64 v[192:193], 0\n" \
    "v_mov_b64 v[194:195], 0\n" \
    "L%=_Tloop:\n" \
    "ds_read_b64_tr_b16 v[104:105], v228 offset:16384\n" \
    "ds_read_b64_tr_b16 v[106:107], v228 offset:16416\n" \
    "ds_read_b64_tr_b16 v[108:109], v228 offset:18688\n" \
    "ds_read_b64_tr_b16 v[110:111], v228 offset:18720\n" \
    "ds_read_b64_tr_b16 v[112:113], v228 offset:20992\n" \
    "ds_read_b64_tr_b16 v[114:115], v228 offset:21024\n" \
    "ds_read_b64_tr_b16 v[116:117], v228 offset:23296\n" \
    "ds_read_b64_tr_b16 v[118:119], v228 offset:23328\n" \
    "ds_read_b64_tr_b16 v[120:121], v228 offset:16656\n" \
    "ds_read_b64_tr_b16 v[122:123], v228 offset:16688\n" \
    "ds_read_b64_tr_b16 v[124:125], v228 offset:18960\n" \
    "ds_read_b64_tr_b16 v[126:127], v228 offset:18992\n" \
    "ds_read_b64_tr_b16 v[128:129], v228 offset:21264\n" \
    "ds_read_b64_tr_b16 v[130:131], v228 offset:21296\n" \
    "ds_read_b64_tr_b16 v[132:133], v228 offset:23568\n" \
    "ds_read_b64_tr_b16 v[134:135], v228 offset:23600\n" \
    "s_waitcnt lgkmcnt(14)\n" \
    "v_mfma_f32_16x16x32_bf16 v[136:139], v[104:107], v[0:3], 0\n" \
    "v_mfma_f32_16x16x32_bf16 v[144:147], v[104:107], v[8:11], 0\n" \
    "v_mfma_f32_16x16x32_bf16 v[152:155], v[104:107], v[16:19], 0\n" \
    "v_mfma_f32_16x16x32_bf16 v[160:163], v[104:107], v[24:27], 0\n" \
    "s_waitcnt lgkmcnt(12)\n" \
    "v_mfma_f32_16x16x32_bf16 v[136:139], v[108:111], v[4:7], v[136:139]\n" \
    "v_mfma_f32_16x16x32_bf16 v[144:147], v[108:111], v[12:15], v[144:147]\n" \
    "v_mfma_f32_16x16x32_bf16 v[152:155], v[108:111], v[20:23], v[152:155]\n" \
    "v_mfma_f32_16x16x32_bf16 v[160:163], v[108:111], v[28:31], v[160:163]\n" \
    "s_waitcnt lgkmcnt(10)\n" \
    "v_mfma_f32_16x16x32_bf16 v[136:139], v[112:115], v[32:35], v[136:139]\n" \
    "v_mfma_f32_16x16x32_bf16 v[144:147], v[112:115], v[40:43], v[144:147]\n" \
    "v_mfma_f32_16x16x32_bf16 v[152:155], v[112:115], v[48:51], v[152:155]\n" \
    "v_mfma_f32_16x16x32_bf16 v[160:163], v[112:115], v[56:59], v[160:163]\n" \
    "s_waitcnt lgkmcnt(8)\n" \
    "v_mfma_f32_16x16x32_bf16 v[136:139], v[116:119], v[36:39], v[136:139]\n" \
    "v_mfma_f32_16x16x32_bf16 v[144:147], v[116:119], v[44:47], v[144:147]\n" \
    "v_mfma_f32_16x16x32_bf16 v[152:155], v[116:119], v[52:55], v[152:155]\n" \
    "v_mfma_f32_16x16x32_bf16 v[160:163], v[116:119], v[60:63], v[160:163]\n" \
    "s_waitcnt lgkmcnt(6)\n" \
    "v_mfma_f32_16x16x32_bf16 v[140:143], v[120:123], v[0:3], 0\n" \
    "v_mfma_f32_16x16x32_bf16 v[148:151], v[120:123], v[8:11], 0\n" \
    "v_mfma_f32_16x16x32_bf16 v[156:159], v[120:123], v[16:19], 0\n" \
    "v_mfma_f32_16x16x32_bf16 v[164:167], v[120:123], v[24:27], 0\n" \
    "s_waitcnt lgkmcnt(4)\n" \
    "v_mfma_f32_16x16x32_bf16 v[140:143], v[124:127], v[4:7], v[140:143]\n" \
    "v_mfma_f32_16x16x32_bf16 v[148:151], v[124:127], v[12:15], v[148:151]\n" \
    "v_mfma_f32_16x16x32_bf16 v[156:159], v[124:127], v[20:23], v[156:159]\n" \
    "v_mfma_f32_16x16x32_bf16 v[164:167], v[124:127], v[28:31], v[164:167]\n" \
    "s_waitcnt lgkmcnt(2)\n" \
    "v_mfma_f32_16x16x32_bf16 v[140:143], v[128:131], v[32:35], v[140:143]\n" \
    "v_mfma_f32_16x16x32_bf16 v[148:151], v[128:131], v[40:43], v[148:151]\n" \
    "v_mfma_f32_16x16x32_bf16 v[156:159], v[128:131], v[48:51], v[156:159]\n" \
    "v_mfma_f32_16x16x32_bf16 v[164:167], v[128:131], v[56:59], v[164:167]\n" \
    "s_waitcnt lgkmcnt(0)\n" \
    "v_mfma_f32_16x16x32_bf16 v[140:143], v[132:135], v[36:39], v[140:143]\n" \
    "v_mfma_f32_16x16x32_bf16 v[148:151], v[132:135], v[44:47], v[148:151]\n" \
    "v_mfma_f32_16x16x32_bf16 v[156:159], v[132:135], v[52:55], v[156:159]\n" \
    "v_mfma_f32_16x16x32_bf16 v[164:167], v[132:135], v[60:63], v[164:167]\n" \
    "v_cvt_pk_bf16_f32 v65, v136, v137\n" \
    "v_cvt_pk_bf16_f32 v68, v138, v139\n" \
    "v_cvt_pk_bf16_f32 v73, v144, v145\n" \
    "v_cvt_pk_bf16_f32 v76, v146, v147\n" \
    "v_cvt_pk_bf16_f32 v81, v152, v153\n" \
    "v_cvt_pk_bf16_f32 v84, v154, v155\n" \
    "v_cvt_pk_bf16_f32 v89, v160, v161\n" \
    "v_cvt_pk_bf16_f32 v92, v162, v163\n" \
    "v_cvt_pk_bf16_f32 v66, v142, v143\n" \
    "v_cvt_pk_bf16_f32 v67, v142, v143\n" \
    "v_cvt_pk_bf16_f32 v69, v140, v141\n" \
    "v_cvt_pk_bf16_f32 v74, v150, v151\n" \
    "v_cvt_pk_bf16_f32 v75, v150, v151\n" \
    "v_cvt_pk_bf16_f32 v77, v148, v149\n" \
    "v_cvt_pk_bf16_f32 v82, v158, v159\n" \
    "v_cvt_pk_bf16_f32 v83, v158, v159\n" \
    "v_cvt_pk_bf16_f32 v85, v156, v157\n" \
    "v_cvt_pk_bf16_f32 v90, v166, v167\n" \
    "v_cvt_pk_bf16_f32 v91, v166, v167\n" \
    "v_cvt_pk_bf16_f32 v93, v164, v165\n" \
    "v_permlane16_swap_b32 v66, v67\n" \
    "v_permlane16_swap_b32 v74, v75\n" \
    "v_permlane16_swap_b32 v82, v83\n" \
    "v_permlane16_swap_b32 v90, v91\n" \
    "v_mov_b64 v[176:177], 0\n" \
    "v_mov_b64 v[178:179], 0\n" \
    "v_mov_b64 v[180:181], 0\n" \
    "v_mov_b64 v[182:183], 0\n" \
    "v_mov_b64 v[184:185], 0\n" \
    "v_mov_b64 v[186:187], 0\n" \
    "v_mov_b64 v[188:189], 0\n" \
    "v_mov_b64 v[190:191], 0\n" \
    "v_mov_b64 v[196:197], 0\n" \
    "v_mov_b64 v[198:199], 0\n" \
    "v_mov_b64 v[200:201], 0\n" \
    "v_mov_b64 v[202:203], 0\n" \
    "v_mov_b64 v[204:205], 0\n" \
    "v_mov_b64 v[206:207], 0\n" \
    "v_mov_b64 v[208:209], 0\n" \
    "v_mov_b64 v[210:211], 0\n" \
    "v_mov_b64 v[212:213], 0\n" \
    "v_mov_b64 v[214:215], 0\n" \
    "s_add_u32 s85, s60, 0x1000\n" \
    "s_branch L%=_Tu2b\n" \
    "L%=_Tu2a:\n" \
    "s_sleep 4\n" \
    "L%=_Tu2b:\n" \
    "s_memrealtime vcc\n" \
    "s_add_u32 s80, s77, s64\n" \
    "s_add_u32 s80, s80, s69\n" \
    "s_waitcnt lgkmcnt(0)\n" \
    "s_sub_i32 s79, s80, vcc_lo\n" \
    "s_sub_u32 s79, s79, 40\n" \
    "s_cmp_lt_u32 s79, 0x400\n" \
    "s_cbranch_scc1 L%=_Tu2a\n" \
    "s_memtime s[38:39]\n" \
    "s_memrealtime vcc\n" \
    "s_nop 4\n" \
    "s_memrealtime s[32:33]\n" \
    "s_nop 4\n" \
    "s_memrealtime s[34:35]\n" \
    "s_nop 4\n" \
    "s_memrealtime s[36:37]\n" \
    "s_add_u32 s80, s77, s64\n" \
    "s_lshl_b32 s80, s80, 2\n" \
    "s_waitcnt lgkmcnt(0)\n" \
    "s_sub_u32 s80, s80, vcc_lo\n" \
    "s_sub_u32 s80, s80, s32\n" \
    "s_sub_u32 s80, s80, s34\n" \
    "s_sub_u32 s80, s80, s36\n" \
    "s_mul_i32 s80, s80, s71\n" \
    "s_ashr_i32 s80, s80, 6\n" \
    "s_add_u32 s78, s80, s38\n" \
    "v_mov_b32 v143, s76\n" \
    "s_barrier\n" \
    "s_cmp_eq_u32 s76, 63\n" \
    "s_cbranch_scc1 L%=_Tfin\n" \
    "ds_read_b32 v216, v229\n" \
    "s_waitcnt lgkmcnt(0)\n" \
    "v_readfirstlane_b32 s94, v216\n" \
    "s_mul_i32 s79, s94, s70\n" \
    "s_add_u32 s79, s79, s81\n" \
    "s_cmp_lg_u32 s79, s77\n" \
    "s_cbranch_scc1 L%=_Tu3s\n" \
    "L%=_Tu3k:\n" \
    "L%=_Tu4a:\n" \
    "s_memtime s[38:39]\n" \
    "s_mul_hi_u32 s80, s66, s96\n" \
    "s_add_u32 s80, s80, s78\n" \
    "s_waitcnt lgkmcnt(0)\n" \
    "s_sub_i32 s79, s80, s38\n" \
    "s_max_i32 s79, s79, 0\n" \
    "s_cmp_gt_u32 s79, 320\n" \
    "s_cbranch_scc1 L%=_Tu4s\n" \
    ".p2align 4\n" \
    "L%=_Tu4d:\n" \
    "s_sub_u32 s79, s79, 28\n" \
    "s_cbranch_scc0 L%=_Tu4d\n" \
    "L%=_Tu4e:\n" \
    "buffer_load_dwordx4 v[104:107], v230, s[40:43], s60 offen offset:0 sc1\n" \
    "buffer_load_dwordx2 v[120:121], v231, s[40:43], s60 offen offset:0 sc1\n" \
    "buffer_load_dwordx4 v[108:111], v230, s[40:43], s60 offen offset:2048 sc1\n" \
    "buffer_load_dwordx2 v[122:123], v231, s[40:43], s60 offen offset:2048 sc1\n" \
    "buffer_load_dwordx4 v[112:115], v230, s[40:43], s85 offen offset:0 sc1\n" \
    "buffer_load_dwordx2 v[124:125], v231, s[40:43], s85 offen offset:0 sc1\n" \
    "buffer_load_dwordx4 v[116:119], v230, s[40:43], s85 offen offset:2048 sc1\n" \
    "buffer_load_dwordx2 v[126:127], v231, s[40:43], s85 offen offset:2048 sc1\n" \
    "s_setprio 3\n" \
    "buffer_load_dwordx4 v[168:171], v225, s[40:43], s60 offen sc1\n" \
    "buffer_load_dwordx4 v[172:175], v225, s[40:43], s85 offen sc1\n" \
    "L%=_Tsc:\n" \
    "s_waitcnt vmcnt(6)\n" \
    "v_pk_add_f32 v[128:129], v[104:105], v[108:109]\n" \
    "v_pk_add_f32 v[130:131], v[106:107], v[110:111]\n" \
    "v_pk_add_f32 v[132:133], v[120:121], v[122:123]\n" \
    "s_waitcnt vmcnt(4)\n" \
    "v_pk_add_f32 v[128:129], v[128:129], v[112:113]\n" \
    "v_pk_add_f32 v[130:131], v[130:131], v[114:115]\n" \
    "v_pk_add_f32 v[132:133], v[132:133], v[124:125]\n" \
    "s_waitcnt vmcnt(2)\n" \
    "v_pk_add_f32 v[128:129], v[128:129], v[116:117]\n" \
    "v_pk_add_f32 v[130:131], v[130:131], v[118:119]\n" \
    "v_pk_add_f32 v[132:133], v[132:133], v[126:127]\n" \
    "v_max3_f32 v136, v128, v129, v130\n" \
    "v_max3_f32 v137, v131, v132, v133\n" \
    "v_max_f32 v136, v136, v137\n" \
    "v_fma_f32 v141, v131, v132, v133\n" \
    "v_fma_f32 v140, v128, v129, v130\n" \
    "v_max_f32_dpp v138, v136, v136 row_ror:8 row_mask:0xf bank_mask:0xf\n" \
    "v_max_f32_dpp v139, v136, v136 row_ror:8 row_mask:0xf bank_mask:0xf\n" \
    "s_waitcnt vmcnt(0)\n" \
    "v_smfmac_f32_16x16x64_bf16 v[176:179], v[168:171], v[0:7], v224\n" \
    "v_cmp_u_f32 vcc, v140, v141\n" \
    "v_permlane16_swap_b32 v138, v139\n" \
    "v_max_f32 v136, v138, v139\n" \
    "v_max_f32 v137, v138, v139\n" \
    "v_smfmac_f32_16x16x64_bf16 v[180:183], v[168:171], v[8:15], v224\n" \
    "s_cbranch_vccnz L%=_Tslow\n" \
    "L%=_Tok:\n" \
    "v_permlane32_swap_b32 v136, v137\n" \
    "v_max_f32 v136, v136, v137\n" \
    "v_mul_f32 v136, s72, v136\n" \
    "v_pk_fma_f32 v[128:129], v[128:129], s[72:73], v[136:137] op_sel_hi:[1,1,0] neg_lo:[0,0,1] neg_hi:[0,0,1]\n" \
    "v_pk_fma_f32 v[132:133], v[132:133], s[72:73], v[136:137] op_sel_hi:[1,1,0] neg_lo:[0,0,1] neg_hi:[0,0,1]\n" \
    "v_pk_fma_f32 v[130:131], v[130:131], s[72:73], v[136:137] op_sel_hi:[1,1,0] neg_lo:[0,0,1] neg_hi:[0,0,1]\n" \
    "v_exp_f32 v128, v128\n" \
    "v_exp_f32 v129, v129\n" \
    "v_exp_f32 v132, v132\n" \
    "v_exp_f32 v133, v133\n" \
    "v_exp_f32 v130, v130\n" \
    "v_exp_f32 v131, v131\n" \
    "v_cvt_pk_bf16_f32 v140, v128, v129\n" \
    "v_cvt_pk_bf16_f32 v141, v132, v133\n" \
    "v_cvt_pk_bf16_f32 v142, v130, v131\n" \
    "ds_write_b128 v232, v[140:143]\n" \
    "s_barrier\n" \
    "ds_read_b128 v[192:195], v227\n" \
    "s_waitcnt vmcnt(0)\n" \
    "v_smfmac_f32_16x16x64_bf16 v[176:179], v[172:175], v[32:39], v224\n" \
    "L%=_Tvc:\n" \
    "v_smfmac_f32_16x16x64_bf16 v[180:183], v[172:175], v[40:47], v224\n" \
    "v_smfmac_f32_16x16x64_bf16 v[184:187], v[168:171], v[16:23], v224\n" \
    "v_add_f32 v176, v176, v178\n" \
    "v_smfmac_f32_16x16x64_bf16 v[188:191], v[168:171], v[24:31], v224\n" \
    "v_add_f32 v177, v177, v179\n" \
    "v_smfmac_f32_16x16x64_bf16 v[184:187], v[172:175], v[48:55], v224\n" \
    "v_cmp_u_f32 vcc, v176, v177\n" \
    "v_smfmac_f32_16x16x64_bf16 v[188:191], v[172:175], v[56:63], v224\n" \
    "s_cbranch_vccnz L%=_Tvslow\n" \
    "L%=_Tvok:\n" \
    "v_cvt_pk_bf16_f32 v64, v176, v177\n" \
    "v_add_f32 v180, v180, v182\n" \
    "s_waitcnt lgkmcnt(0)\n" \
    "v_smfmac_f32_16x16x64_bf16 v[212:215], v[192:195], v[96:103], v224\n" \
    "v_add_f32 v181, v181, v183\n" \
    "v_smfmac_f32_16x16x64_bf16 v[196:199], v[192:195], v[64:71], v224\n" \
    "v_cvt_pk_bf16_f32 v72, v180, v181\n" \
    "v_add_f32 v184, v184, v186\n" \
    "v_add_f32 v185, v185, v187\n" \
    "v_smfmac_f32_16x16x64_bf16 v[200:203], v[192:195], v[72:79], v224\n" \
    "v_cvt_pk_bf16_f32 v80, v184, v185\n" \
    "v_add_f32 v188, v188, v190\n" \
    "v_add_f32 v189, v189, v191\n" \
    "v_smfmac_f32_16x16x64_bf16 v[204:207], v[192:195], v[80:87], v224\n" \
    "v_cvt_pk_bf16_f32 v88, v188, v189\n" \
    "v_add_f32 v212, v212, v214\n" \
    "v_add_f32 v213, v213, v215\n" \
    "v_smfmac_f32_16x16x64_bf16 v[208:211], v[192:195], v[88:95], v224\n" \
    "v_rcp_f32 v220, v212\n" \
    "v_rcp_f32 v221, v213\n" \
    "v_add_f32 v196, v196, v198\n" \
    "v_add_f32 v197, v197, v199\n" \
    "v_pk_add_f32 v[200:201], v[200:201], v[202:203]\n" \
    "v_pk_add_f32 v[204:205], v[204:205], v[206:207]\n" \
    "s_nop 1\n" \
    "v_pk_add_f32 v[208:209], v[208:209], v[210:211]\n" \
    "v_pk_mul_f32 v[196:197], v[196:197], v[220:221]\n" \
    "v_pk_mul_f32 v[200:201], v[200:201], v[220:221]\n" \
    "v_pk_mul_f32 v[204:205], v[204:205], v[220:221]\n" \
    "v_pk_mul_f32 v[208:209], v[208:209], v[220:221]\n" \
    "v_cvt_pk_bf16_f32 v216, v196, v200\n" \
    "v_cvt_pk_bf16_f32 v217, v204, v208\n" \
    "v_cvt_pk_bf16_f32 v218, v197, v201\n" \
    "v_cvt_pk_bf16_f32 v219, v205, v209\n" \
    "buffer_store_dwordx4 v[216:219], v226, s[40:43], s60 offen sc1\n" \
    "s_setprio 0\n" \
    "buffer_store_dword v143, v233, s[44:47], 0 offen sc1\n" \
    "ds_read_b64 v[220:221], v229 offset:64\n" \
    "s_mov_b32 s79, s60\n" \
    "s_mov_b32 s60, s61\n" \
    "s_mov_b32 s61, s62\n" \
    "s_mov_b32 s62, s63\n" \
    "s_mov_b32 s63, s79\n" \
    "s_add_u32 s76, s76, 1\n" \
    "s_add_u32 s77, s77, s65\n" \
    "s_add_u32 s84, s84, 0x20000\n" \
    "s_add_u32 s81, s81, s65\n" \
    "s_waitcnt lgkmcnt(0)\n" \
    "v_readfirstlane_b32 s65, v220\n" \
    "v_readfirstlane_b32 s71, v221\n" \
    "s_mul_i32 s96, s65, s71\n" \
    "s_lshr_b32 s96, s96, 4\n" \
    "s_branch L%=_Tloop\n" \
    "L%=_Tslow:\n" \
    "s_nop 7\n" \
    "s_nop 0\n" \
    "v_max_u32 v138, v104, v105\n" \
    "v_max3_u32 v138, v138, v106, v107\n" \
    "v_max3_u32 v138, v138, v108, v109\n" \
    "v_max3_u32 v138, v138, v110, v111\n" \
    "v_max3_u32 v138, v138, v112, v113\n" \
    "v_max3_u32 v138, v138, v114, v115\n" \
    "v_max3_u32 v138, v138, v116, v117\n" \
    "v_max3_u32 v138, v138, v118, v119\n" \
    "v_max3_u32 v138, v138, v120, v121\n" \
    "v_max3_u32 v138, v138, v122, v123\n" \
    "v_max3_u32 v138, v138, v124, v125\n" \
    "v_max3_u32 v138, v138, v126, v127\n" \
    "v_cmp_eq_u32 vcc, -1, v138\n" \
    "s_cbranch_vccz L%=_Tok\n" \
    "s_cmp_eq_u32 s99, 0\n" \
    "s_cbranch_scc1 L%=_Tok\n" \
    "s_sub_u32 s99, s99, 1\n" \
    "s_cmp_lg_u32 s99, 0\n" \
    "s_cbranch_scc1 L%=_Tu5\n" \
    "v_mov_b32 v138, 1\n" \
    "buffer_store_dword v138, off, s[44:47], 0 sc1\n" \
    "L%=_Tu5:\n" \
    "s_sleep 1\n" \
    "buffer_load_dwordx4 v[104:107], v230, s[40:43], s60 offen offset:0 sc1\n" \
    "buffer_load_dwordx2 v[120:121], v231, s[40:43], s60 offen offset:0 sc1\n" \
    "buffer_load_dwordx4 v[108:111], v230, s[40:43], s60 offen offset:2048 sc1\n" \
    "buffer_load_dwordx2 v[122:123], v231, s[40:43], s60 offen offset:2048 sc1\n" \
    "buffer_load_dwordx4 v[112:115], v230, s[40:43], s85 offen offset:0 sc1\n" \
    "buffer_load_dwordx2 v[124:125], v231, s[40:43], s85 offen offset:0 sc1\n" \
    "buffer_load_dwordx4 v[116:119], v230, s[40:43], s85 offen offset:2048 sc1\n" \
    "buffer_load_dwordx2 v[126:127], v231, s[40:43], s85 offen offset:2048 sc1\n" \
    "buffer_load_dwordx4 v[168:171], v225, s[40:43], s60 offen sc1\n" \
    "buffer_load_dwordx4 v[172:175], v225, s[40:43], s85 offen sc1\n" \
    "v_mov_b64 v[176:177], 0\n" \
    "v_mov_b64 v[178:179], 0\n" \
    "v_mov_b64 v[180:181], 0\n" \
    "v_mov_b64 v[182:183], 0\n" \
    "v_mov_b64 v[184:185], 0\n" \
    "v_mov_b64 v[186:187], 0\n" \
    "v_mov_b64 v[188:189], 0\n" \
    "v_mov_b64 v[190:191], 0\n" \
    "s_branch L%=_Tsc\n" \
    "L%=_Tvslow:\n" \
    "s_nop 7\n" \
    "s_nop 0\n" \
    "v_max_u32 v136, v168, v169\n" \
    "v_max3_u32 v136, v136, v170, v171\n" \
    "v_max3_u32 v136, v136, v172, v173\n" \
    "v_max3_u32 v136, v136, v174, v175\n" \
    "v_cmp_eq_u32 vcc, -1, v136\n" \
    "s_cbranch_vccz L%=_Tvok\n" \
    "s_cmp_eq_u32 s99, 0\n" \
    "s_cbranch_scc1 L%=_Tvok\n" \
    "s_sub_u32 s99, s99, 1\n" \
    "s_cmp_lg_u32 s99, 0\n" \
    "s_cbranch_scc1 L%=_Tu6\n" \
    "v_mov_b32 v136, 1\n" \
    "buffer_store_dword v136, off, s[44:47], 0 sc1\n" \
    "L%=_Tu6:\n" \
    "s_sleep 1\n" \
    "buffer_load_dwordx4 v[168:171], v225, s[40:43], s60 offen sc1\n" \
    "buffer_load_dwordx4 v[172:175], v225, s[40:43], s85 offen sc1\n" \
    "v_mov_b64 v[176:177], 0\n" \
    "v_mov_b64 v[178:179], 0\n" \
    "v_mov_b64 v[180:181], 0\n" \
    "v_mov_b64 v[182:183], 0\n" \
    "v_mov_b64 v[184:185], 0\n" \
    "v_mov_b64 v[186:187], 0\n" \
    "v_mov_b64 v[188:189], 0\n" \
    "v_mov_b64 v[190:191], 0\n" \
    "v_mov_b64 v[196:197], 0\n" \
    "v_mov_b64 v[198:199], 0\n" \
    "v_mov_b64 v[200:201], 0\n" \
    "v_mov_b64 v[202:203], 0\n" \
    "v_mov_b64 v[204:205], 0\n" \
    "v_mov_b64 v[206:207], 0\n" \
    "v_mov_b64 v[208:209], 0\n" \
    "v_mov_b64 v[210:211], 0\n" \
    "v_mov_b64 v[212:213], 0\n" \
    "v_mov_b64 v[214:215], 0\n" \
    "s_waitcnt vmcnt(0)\n" \
    "v_smfmac_f32_16x16x64_bf16 v[176:179], v[168:171], v[0:7], v224\n" \
    "v_smfmac_f32_16x16x64_bf16 v[180:183], v[168:171], v[8:15], v224\n" \
    "v_smfmac_f32_16x16x64_bf16 v[176:179], v[172:175], v[32:39], v224\n" \
    "s_nop 7\n" \
    "s_nop 0\n" \
    "s_branch L%=_Tvc\n" \
    "L%=_Tfin:\n" \
    "v_mov_b32 v143, 64\n" \
    "s_barrier\n" \
    "buffer_store_dword v143, v233, s[44:47], 0 offen sc1\n" \
    "s_branch L%=_end\n" \
    "L%=_Tu3s:\n" \
    "s_mov_b32 s77, s79\n" \
    "s_memtime s[38:39]\n" \
    "s_memrealtime vcc\n" \
    "s_nop 4\n" \
    "s_memrealtime s[32:33]\n" \
    "s_nop 4\n" \
    "s_memrealtime s[34:35]\n" \
    "s_nop 4\n" \
    "s_memrealtime s[36:37]\n" \
    "s_add_u32 s80, s77, s64\n" \
    "s_lshl_b32 s80, s80, 2\n" \
    "s_waitcnt lgkmcnt(0)\n" \
    "s_sub_u32 s80, s80, vcc_lo\n" \
    "s_sub_u32 s80, s80, s32\n" \
    "s_sub_u32 s80, s80, s34\n" \
    "s_sub_u32 s80, s80, s36\n" \
    "s_mul_i32 s80, s80, s71\n" \
    "s_ashr_i32 s80, s80, 6\n" \
    "s_add_u32 s78, s80, s38\n" \
    "s_branch L%=_Tu3k\n" \
    "L%=_Tu4s:\n" \
    "s_cmp_gt_u32 s79, 0x8000\n" \
    "s_cbranch_scc1 L%=_Tu4e\n" \
    "s_sleep 2\n" \
    "s_branch L%=_Tu4a\n" \
    "L%=_S:\n" \
    "s_mov_b32 s76, 0\n" \
    "s_mov_b32 s99, 0x20000\n" \
    "s_mov_b32 s84, 0\n" \
    "s_mov_b32 s81, 0\n" \
    "s_mov_b32 s86, 0\n" \
    "s_mov_b32 s87, 0\n" \
    "s_mov_b32 s88, 0\n" \
    "s_mov_b32 s89, 0\n" \
    "s_mov_b32 s90, 0\n" \
    "s_mov_b32 s91, 0\n" \
    "s_mov_b32 s92, 0\n" \
    "s_mov_b32 s93, 0\n" \
    "s_mov_b32 s94, 0\n" \
    "s_mov_b32 s95, 0\n" \
    "s_mov_b32 s96, 0\n" \
    "s_mov_b32 s97, 0\n" \
    "s_mov_b32 s98, 0\n" \
    "ds_read_b32 v216, v229\n" \
    "s_waitcnt lgkmcnt(0)\n" \
    "v_readfirstlane_b32 s94, v216\n" \
    "s_mul_i32 s79, s94, s70\n" \
    "s_add_u32 s79, s79, s81\n" \
    "s_mov_b32 s77, s79\n" \
    "s_mul_i32 s96, s65, s71\n" \
    "s_lshr_b32 s96, s96, 4\n" \
    "v_mov_b32 v96, 0x3f803f80\n" \
    "v_mov_b32 v97, 0x3f803f80\n" \
    "v_mov_b32 v98, 0x3f803f80\n" \
    "v_mov_b32 v99, 0x3f803f80\n" \
    "v_mov_b32 v100, 0x3f803f80\n" \
    "v_mov_b32 v101, 0x3f803f80\n" \
    "v_mov_b64 v[102:103], 0\n" \
    "v_mov_b64 v[64:65], 0\n" \
    "v_mov_b64 v[66:67], 0\n" \
    "v_mov_b64 v[68:69], 0\n" \
    "v_mov_b64 v[70:71], 0\n" \
    "v_mov_b64 v[72:73], 0\n" \
    "v_mov_b64 v[74:75], 0\n" \
    "v_mov_b64 v[76:77], 0\n" \
    "v_mov_b64 v[78:79], 0\n" \
    "v_mov_b64 v[80:81], 0\n" \
    "v_mov_b64 v[82:83], 0\n" \
    "v_mov_b64 v[84:85], 0\n" \
    "v_mov_b64 v[86:87], 0\n" \
    "v_mov_b64 v[88:89], 0\n" \
    "v_mov_b64 v[90:91], 0\n" \
    "v_mov_b64 v[92:93], 0\n" \
    "v_mov_b64 v[94:95], 0\n" \
    "v_mov_b64 v[192:193], 0\n" \
    "v_mov_b64 v[194:195], 0\n" \
    "L%=_Sloop:\n" \
    "ds_read_b64_tr_b16 v[104:105], v228 offset:16384\n" \
    "ds_read_b64_tr_b16 v[106:107], v228 offset:16416\n" \
    "ds_read_b64_tr_b16 v[108:109], v228 offset:18688\n" \
    "ds_read_b64_tr_b16 v[110:111], v228 offset:18720\n" \
    "ds_read_b64_tr_b16 v[112:113], v228 offset:20992\n" \
    "ds_read_b64_tr_b16 v[114:115], v228 offset:21024\n" \
    "ds_read_b64_tr_b16 v[116:117], v228 offset:23296\n" \
    "ds_read_b64_tr_b16 v[118:119], v228 offset:23328\n" \
    "ds_read_b64_tr_b16 v[120:121], v228 offset:16656\n" \
    "ds_read_b64_tr_b16 v[122:123], v228 offset:16688\n" \
    "ds_read_b64_tr_b16 v[124:125], v228 offset:18960\n" \
    "ds_read_b64_tr_b16 v[126:127], v228 offset:18992\n" \
    "ds_read_b64_tr_b16 v[128:129], v228 offset:21264\n" \
    "ds_read_b64_tr_b16 v[130:131], v228 offset:21296\n" \
    "ds_read_b64_tr_b16 v[132:133], v228 offset:23568\n" \
    "ds_read_b64_tr_b16 v[134:135], v228 offset:23600\n" \
    "s_waitcnt lgkmcnt(14)\n" \
    "v_mfma_f32_16x16x32_bf16 v[136:139], v[104:107], v[0:3], 0\n" \
    "v_mfma_f32_16x16x32_bf16 v[144:147], v[104:107], v[8:11], 0\n" \
    "v_mfma_f32_16x16x32_bf16 v[152:155], v[104:107], v[16:19], 0\n" \
    "v_mfma_f32_16x16x32_bf16 v[160:163], v[104:107], v[24:27], 0\n" \
    "s_waitcnt lgkmcnt(12)\n" \
    "v_mfma_f32_16x16x32_bf16 v[136:139], v[108:111], v[4:7], v[136:139]\n" \
    "v_mfma_f32_16x16x32_bf16 v[144:147], v[108:111], v[12:15], v[144:147]\n" \
    "v_mfma_f32_16x16x32_bf16 v[152:155], v[108:111], v[20:23], v[152:155]\n" \
    "v_mfma_f32_16x16x32_bf16 v[160:163], v[108:111], v[28:31], v[160:163]\n" \
    "s_waitcnt lgkmcnt(10)\n" \
    "v_mfma_f32_16x16x32_bf16 v[136:139], v[112:115], v[32:35], v[136:139]\n" \
    "v_mfma_f32_16x16x32_bf16 v[144:147], v[112:115], v[40:43], v[144:147]\n" \
    "v_mfma_f32_16x16x32_bf16 v[152:155], v[112:115], v[48:51], v[152:155]\n" \
    "v_mfma_f32_16x16x32_bf16 v[160:163], v[112:115], v[56:59], v[160:163]\n" \
    "s_waitcnt lgkmcnt(8)\n" \
    "v_mfma_f32_16x16x32_bf16 v[136:139], v[116:119], v[36:39], v[136:139]\n" \
    "v_mfma_f32_16x16x32_bf16 v[144:147], v[116:119], v[44:47], v[144:147]\n" \
    "v_mfma_f32_16x16x32_bf16 v[152:155], v[116:119], v[52:55], v[152:155]\n" \
    "v_mfma_f32_16x16x32_bf16 v[160:163], v[116:119], v[60:63], v[160:163]\n" \
    "s_waitcnt lgkmcnt(6)\n" \
    "v_mfma_f32_16x16x32_bf16 v[140:143], v[120:123], v[0:3], 0\n" \
    "v_mfma_f32_16x16x32_bf16 v[148:151], v[120:123], v[8:11], 0\n" \
    "v_mfma_f32_16x16x32_bf16 v[156:159], v[120:123], v[16:19], 0\n" \
    "v_mfma_f32_16x16x32_bf16 v[164:167], v[120:123], v[24:27], 0\n" \
    "s_waitcnt lgkmcnt(4)\n" \
    "v_mfma_f32_16x16x32_bf16 v[140:143], v[124:127], v[4:7], v[140:143]\n" \
    "v_mfma_f32_16x16x32_bf16 v[148:151], v[124:127], v[12:15], v[148:151]\n" \
    "v_mfma_f32_16x16x32_bf16 v[156:159], v[124:127], v[20:23], v[156:159]\n" \
    "v_mfma_f32_16x16x32_bf16 v[164:167], v[124:127], v[28:31], v[164:167]\n" \
    "s_waitcnt lgkmcnt(2)\n" \
    "v_mfma_f32_16x16x32_bf16 v[140:143], v[128:131], v[32:35], v[140:143]\n" \
    "v_mfma_f32_16x16x32_bf16 v[148:151], v[128:131], v[40:43], v[148:151]\n" \
    "v_mfma_f32_16x16x32_bf16 v[156:159], v[128:131], v[48:51], v[156:159]\n" \
    "v_mfma_f32_16x16x32_bf16 v[164:167], v[128:131], v[56:59], v[164:167]\n" \
    "s_waitcnt lgkmcnt(0)\n" \
    "v_mfma_f32_16x16x32_bf16 v[140:143], v[132:135], v[36:39], v[140:143]\n" \
    "v_mfma_f32_16x16x32_bf16 v[148:151], v[132:135], v[44:47], v[148:151]\n" \
    "v_mfma_f32_16x16x32_bf16 v[156:159], v[132:135], v[52:55], v[156:159]\n" \
    "v_mfma_f32_16x16x32_bf16 v[164:167], v[132:135], v[60:63], v[164:167]\n" \
    "v_cvt_pk_bf16_f32 v65, v136, v137\n" \
    "v_cvt_pk_bf16_f32 v68, v138, v139\n" \
    "v_cvt_pk_bf16_f32 v73, v144, v145\n" \
    "v_cvt_pk_bf16_f32 v76, v146, v147\n" \
    "v_cvt_pk_bf16_f32 v81, v152, v153\n" \
    "v_cvt_pk_bf16_f32 v84, v154, v155\n" \
    "v_cvt_pk_bf16_f32 v89, v160, v161\n" \
    "v_cvt_pk_bf16_f32 v92, v162, v163\n" \
    "v_cvt_pk_bf16_f32 v66, v142, v143\n" \
    "v_cvt_pk_bf16_f32 v67, v142, v143\n" \
    "v_cvt_pk_bf16_f32 v69, v140, v141\n" \
    "v_cvt_pk_bf16_f32 v74, v150, v151\n" \
    "v_cvt_pk_bf16_f32 v75, v150, v151\n" \
    "v_cvt_pk_bf16_f32 v77, v148, v149\n" \
    "v_cvt_pk_bf16_f32 v82, v158, v159\n" \
    "v_cvt_pk_bf16_f32 v83, v158, v159\n" \
    "v_cvt_pk_bf16_f32 v85, v156, v157\n" \
    "v_cvt_pk_bf16_f32 v90, v166, v167\n" \
    "v_cvt_pk_bf16_f32 v91, v166, v167\n" \
    "v_cvt_pk_bf16_f32 v93, v164, v165\n" \
    "v_permlane16_swap_b32 v66, v67\n" \
    "v_permlane16_swap_b32 v74, v75\n" \
    "v_permlane16_swap_b32 v82, v83\n" \
    "v_permlane16_swap_b32 v90, v91\n" \
    "ds_read_b128 v[240:243], v234 offset:25600\n" \
    "ds_read_b128 v[244:247], v234 offset:26624\n" \
    "v_mov_b64 v[176:177], 0\n" \
    "v_mov_b64 v[178:179], 0\n" \
    "v_mov_b64 v[180:181], 0\n" \
    "v_mov_b64 v[182:183], 0\n" \
    "v_mov_b64 v[184:185], 0\n" \
    "v_mov_b64 v[186:187], 0\n" \
    "v_mov_b64 v[188:189], 0\n" \
    "v_mov_b64 v[190:191], 0\n" \
    "v_mov_b64 v[196:197], 0\n" \
    "v_mov_b64 v[198:199], 0\n" \
    "v_mov_b64 v[200:201], 0\n" \
    "v_mov_b64 v[202:203], 0\n" \
    "v_mov_b64 v[204:205], 0\n" \
    "v_mov_b64 v[206:207], 0\n" \
    "v_mov_b64 v[208:209], 0\n" \
    "v_mov_b64 v[210:211], 0\n" \
    "v_mov_b64 v[212:213], 0\n" \
    "v_mov_b64 v[214:215], 0\n" \
    "s_add_u32 s85, s60, 0x1000\n" \
    "s_branch L%=_Su2b\n" \
    "L%=_Su2a:\n" \
    "s_sleep 4\n" \
    "L%=_Su2b:\n" \
    "s_memrealtime vcc\n" \
    "s_add_u32 s80, s77, s64\n" \
    "s_add_u32 s80, s80, s69\n" \
    "s_waitcnt lgkmcnt(0)\n" \
    "s_sub_i32 s79, s80, vcc_lo\n" \
    "s_sub_u32 s79, s79, 40\n" \
    "s_cmp_lt_u32 s79, 0x400\n" \
    "s_cbranch_scc1 L%=_Su2a\n" \
    "s_memtime s[38:39]\n" \
    "s_memrealtime vcc\n" \
    "s_nop 4\n" \
    "s_memrealtime s[32:33]\n" \
    "s_nop 4\n" \
    "s_memrealtime s[34:35]\n" \
    "s_nop 4\n" \
    "s_memrealtime s[36:37]\n" \
    "s_add_u32 s80, s77, s64\n" \
    "s_lshl_b32 s80, s80, 2\n" \
    "s_waitcnt lgkmcnt(0)\n" \
    "s_sub_u32 s80, s80, vcc_lo\n" \
    "s_sub_u32 s80, s80, s32\n" \
    "s_sub_u32 s80, s80, s34\n" \
    "s_sub_u32 s80, s80, s36\n" \
    "s_mul_i32 s80, s80, s71\n" \
    "s_ashr_i32 s80, s80, 6\n" \
    "s_add_u32 s78, s80, s38\n" \
    "L%=_Su3a:\n" \
    "s_memtime s[38:39]\n" \
    "s_mul_i32 s80, s69, 23\n" \
    "s_add_u32 s80, s80, s78\n" \
    "s_waitcnt lgkmcnt(0)\n" \
    "s_sub_i32 s79, s80, s38\n" \
    "s_max_i32 s79, s79, 0\n" \
    "s_cmp_gt_u32 s79, 320\n" \
    "s_cbranch_scc1 L%=_Su3s\n" \
    ".p2align 4\n" \
    "L%=_Su3d:\n" \
    "s_sub_u32 s79, s79, 28\n" \
    "s_cbranch_scc0 L%=_Su3d\n" \
    "L%=_Su3e:\n" \
    "s_sub_u32 s79, s84, 0x20000\n" \
    "s_cselect_b32 s79, 0x40000000, s79\n" \
    "buffer_store_dwordx4 v[236:239], v233, s[48:51], s79 offen sc1\n" \
    "s_setprio 3\n" \
    "s_barrier\n" \
    "L%=_Srd:\n" \
    "ds_read_b128 v[136:139], v229 offset:32\n" \
    "ds_read_b128 v[104:107], v230 offset:0\n" \
    "ds_read_b128 v[108:111], v230 offset:2176\n" \
    "ds_read_b128 v[112:115], v230 offset:4352\n" \
    "ds_read_b128 v[116:119], v230 offset:6528\n" \
    "ds_read_b128 v[120:123], v230 offset:1088\n" \
    "ds_read_b128 v[124:127], v230 offset:3264\n" \
    "ds_read_b128 v[128:131], v230 offset:5440\n" \
    "ds_read_b128 v[132:135], v230 offset:7616\n" \
    "s_waitcnt lgkmcnt(8)\n" \
    "v_min3_u32 v136, v136, v137, v138\n" \
    "v_min_u32 v136, v136, v139\n" \
    "v_cmp_ge_u32 vcc, s76, v136\n" \
    "s_cbranch_vccnz L%=_Srd\n" \
    "s_waitcnt lgkmcnt(6)\n" \
    "v_pk_add_f32 v[104:105], v[104:105], v[108:109]\n" \
    "v_pk_add_f32 v[106:107], v[106:107], v[110:111]\n" \
    "s_waitcnt lgkmcnt(4)\n" \
    "v_pk_add_f32 v[112:113], v[112:113], v[116:117]\n" \
    "v_pk_add_f32 v[114:115], v[114:115], v[118:119]\n" \
    "v_pk_add_f32 v[104:105], v[104:105], v[112:113]\n" \
    "v_pk_add_f32 v[106:107], v[106:107], v[114:115]\n" \
    "v_cvt_pk_bf16_f32 v236, v104, v105\n" \
    "v_cvt_pk_bf16_f32 v237, v106, v107\n" \
    "s_waitcnt lgkmcnt(2)\n" \
    "v_pk_add_f32 v[120:121], v[120:121], v[124:125]\n" \
    "v_pk_add_f32 v[122:123], v[122:123], v[126:127]\n" \
    "s_waitcnt lgkmcnt(0)\n" \
    "v_pk_add_f32 v[128:129], v[128:129], v[132:133]\n" \
    "v_pk_add_f32 v[130:131], v[130:131], v[134:135]\n" \
    "v_pk_add_f32 v[120:121], v[120:121], v[128:129]\n" \
    "v_pk_add_f32 v[122:123], v[122:123], v[130:131]\n" \
    "v_cvt_pk_bf16_f32 v238, v120, v121\n" \
    "v_cvt_pk_bf16_f32 v239, v122, v123\n" \
    "s_nop 1\n" \
    "v_mfma_f32_16x16x32_bf16 v[140:143], v[240:243], v[236:239], 0\n" \
    "v_mfma_f32_16x16x32_bf16 v[144:147], v[244:247], v[236:239], 0\n" \
    "v_mfma_f32_16x16x32_bf16 v[148:151], v[236:239], v[236:239], 0\n" \
    "buffer_store_dwordx4 v[140:143], v231, s[40:43], s60 offen sc1\n" \
    "s_nop 2\n" \
    "buffer_store_dwordx4 v[144:147], v231, s[40:43], s60 offen offset:512 sc1\n" \
    "s_nop 2\n" \
    "buffer_store_dwordx4 v[148:151], v232, s[40:43], s60 offen sc1\n" \
    "s_cmp_eq_u32 s76, 63\n" \
    "s_cbranch_scc1 L%=_Sfin\n" \
    "ds_read_b32 v216, v229\n" \
    "s_waitcnt lgkmcnt(0)\n" \
    "v_readfirstlane_b32 s94, v216\n" \
    "s_mul_i32 s79, s94, s70\n" \
    "s_add_u32 s79, s79, s81\n" \
    "s_cmp_lg_u32 s79, s77\n" \
    "s_cbranch_scc1 L%=_Su4s\n" \
    "L%=_Su4k:\n" \
    "L%=_Su5a:\n" \
    "s_memtime s[38:39]\n" \
    "s_mul_hi_u32 s80, s67, s96\n" \
    "s_add_u32 s80, s80, s78\n" \
    "s_waitcnt lgkmcnt(0)\n" \
    "s_sub_i32 s79, s80, s38\n" \
    "s_max_i32 s79, s79, 0\n" \
    "s_cmp_gt_u32 s79, 320\n" \
    "s_cbranch_scc1 L%=_Su5s\n" \
    ".p2align 4\n" \
    "L%=_Su5d:\n" \
    "s_sub_u32 s79, s79, 28\n" \
    "s_cbranch_scc0 L%=_Su5d\n" \
    "L%=_Su5e:\n" \
    "buffer_load_dwordx4 v[168:171], v225, s[40:43], s60 offen sc1\n" \
    "buffer_load_dwordx4 v[172:175], v225, s[40:43], s85 offen sc1\n" \
    "L%=_Svc:\n" \
    "s_waitcnt vmcnt(1)\n" \
    "v_smfmac_f32_16x16x64_bf16 v[176:179], v[168:171], v[0:7], v224\n" \
    "v_smfmac_f32_16x16x64_bf16 v[180:183], v[168:171], v[8:15], v224\n" \
    "s_waitcnt vmcnt(0)\n" \
    "v_smfmac_f32_16x16x64_bf16 v[176:179], v[172:175], v[32:39], v224\n" \
    "v_smfmac_f32_16x16x64_bf16 v[180:183], v[172:175], v[40:47], v224\n" \
    "v_smfmac_f32_16x16x64_bf16 v[184:187], v[168:171], v[16:23], v224\n" \
    "v_smfmac_f32_16x16x64_bf16 v[188:191], v[168:171], v[24:31], v224\n" \
    "v_add_f32 v176, v176, v178\n" \
    "v_smfmac_f32_16x16x64_bf16 v[184:187], v[172:175], v[48:55], v224\n" \
    "v_add_f32 v177, v177, v179\n" \
    "v_smfmac_f32_16x16x64_bf16 v[188:191], v[172:175], v[56:63], v224\n" \
    "v_cmp_u_f32 vcc, v176, v177\n" \
    "v_cvt_pk_bf16_f32 v64, v176, v177\n" \
    "v_add_f32 v180, v180, v182\n" \
    "v_add_f32 v181, v181, v183\n" \
    "v_cvt_pk_bf16_f32 v72, v180, v181\n" \
    "s_cbranch_vccnz L%=_Svslow\n" \
    "L%=_Svok:\n" \
    "v_add_f32 v184, v184, v186\n" \
    "v_add_f32 v185, v185, v187\n" \
    "v_cvt_pk_bf16_f32 v80, v184, v185\n" \
    "v_add_f32 v188, v188, v190\n" \
    "v_add_f32 v189, v189, v191\n" \
    "v_cvt_pk_bf16_f32 v88, v188, v189\n" \
    "s_barrier\n" \
    "L%=_Sfr:\n" \
    "ds_read_b128 v[192:195], v227\n" \
    "s_waitcnt lgkmcnt(0)\n" \
    "v_smfmac_f32_16x16x64_bf16 v[212:215], v[192:195], v[96:103], v224\n" \
    "v_cmp_ne_u32 vcc, s76, v195\n" \
    "v_smfmac_f32_16x16x64_bf16 v[196:199], v[192:195], v[64:71], v224\n" \
    "s_cbranch_vccnz L%=_Sfs\n" \
    "v_smfmac_f32_16x16x64_bf16 v[200:203], v[192:195], v[72:79], v224\n" \
    "v_smfmac_f32_16x16x64_bf16 v[204:207], v[192:195], v[80:87], v224\n" \
    "v_add_f32 v212, v212, v214\n" \
    "v_smfmac_f32_16x16x64_bf16 v[208:211], v[192:195], v[88:95], v224\n" \
    "v_add_f32 v213, v213, v215\n" \
    "v_rcp_f32 v220, v212\n" \
    "v_rcp_f32 v221, v213\n" \
    "v_add_f32 v196, v196, v198\n" \
    "v_add_f32 v197, v197, v199\n" \
    "v_pk_add_f32 v[200:201], v[200:201], v[202:203]\n" \
    "v_pk_add_f32 v[204:205], v[204:205], v[206:207]\n" \
    "s_nop 0\n" \
    "v_pk_add_f32 v[208:209], v[208:209], v[210:211]\n" \
    "v_pk_mul_f32 v[196:197], v[196:197], v[220:221]\n" \
    "v_pk_mul_f32 v[200:201], v[200:201], v[220:221]\n" \
    "v_pk_mul_f32 v[204:205], v[204:205], v[220:221]\n" \
    "v_pk_mul_f32 v[208:209], v[208:209], v[220:221]\n" \
    "v_cvt_pk_bf16_f32 v216, v196, v200\n" \
    "v_cvt_pk_bf16_f32 v217, v204, v208\n" \
    "v_cvt_pk_bf16_f32 v218, v197, v201\n" \
    "v_cvt_pk_bf16_f32 v219, v205, v209\n" \
    "buffer_store_dwordx4 v[216:219], v226, s[40:43], s60 offen sc1\n" \
    "s_setprio 0\n" \
    "ds_read_b64 v[220:221], v229 offset:64\n" \
    "s_mov_b32 s79, s60\n" \
    "s_mov_b32 s60, s61\n" \
    "s_mov_b32 s61, s62\n" \
    "s_mov_b32 s62, s63\n" \
    "s_mov_b32 s63, s79\n" \
    "s_add_u32 s76, s76, 1\n" \
    "s_add_u32 s77, s77, s65\n" \
    "s_add_u32 s84, s84, 0x20000\n" \
    "s_add_u32 s81, s81, s65\n" \
    "s_waitcnt lgkmcnt(0)\n" \
    "v_readfirstlane_b32 s65, v220\n" \
    "v_readfirstlane_b32 s71, v221\n" \
    "s_mul_i32 s96, s65, s71\n" \
    "s_lshr_b32 s96, s96, 4\n" \
    "s_branch L%=_Sloop\n" \
    "L%=_Sfin:\n" \
    "buffer_store_dwordx4 v[236:239], v233, s[48:51], s84 offen sc1\n" \
    "s_waitcnt vmcnt(0)\n" \
    "s_barrier\n" \
    "s_branch L%=_end\n" \
    "L%=_Su3s:\n" \
    "s_cmp_gt_u32 s79, 0x8000\n" \
    "s_cbranch_scc1 L%=_Su3e\n" \
    "s_sleep 2\n" \
    "s_branch L%=_Su3a\n" \
    "L%=_Su4s:\n" \
    "s_mov_b32 s77, s79\n" \
    "s_memtime s[38:39]\n" \
    "s_memrealtime vcc\n" \
    "s_nop 4\n" \
    "s_memrealtime s[32:33]\n" \
    "s_nop 4\n" \
    "s_memrealtime s[34:35]\n" \
    "s_nop 4\n" \
    "s_memrealtime s[36:37]\n" \
    "s_add_u32 s80, s77, s64\n" \
    "s_lshl_b32 s80, s80, 2\n" \
    "s_waitcnt lgkmcnt(0)\n" \
    "s_sub_u32 s80, s80, vcc_lo\n" \
    "s_sub_u32 s80, s80, s32\n" \
    "s_sub_u32 s80, s80, s34\n" \
    "s_sub_u32 s80, s80, s36\n" \
    "s_mul_i32 s80, s80, s71\n" \
    "s_ashr_i32 s80, s80, 6\n" \
    "s_add_u32 s78, s80, s38\n" \
    "s_branch L%=_Su4k\n" \
    "L%=_Su5s:\n" \
    "s_cmp_gt_u32 s79, 0x8000\n" \
    "s_cbranch_scc1 L%=_Su5e\n" \
    "s_sleep 2\n" \
    "s_branch L%=_Su5a\n" \
    "L%=_Svslow:\n" \
    "s_nop 7\n" \
    "s_nop 0\n" \
    "v_max_u32 v216, v168, v169\n" \
    "v_max3_u32 v216, v216, v170, v171\n" \
    "v_max3_u32 v216, v216, v172, v173\n" \
    "v_max3_u32 v216, v216, v174, v175\n" \
    "v_cmp_eq_u32 vcc, -1, v216\n" \
    "s_cbranch_vccz L%=_Svok\n" \
    "s_cmp_eq_u32 s99, 0\n" \
    "s_cbranch_scc1 L%=_Svok\n" \
    "s_sub_u32 s99, s99, 1\n" \
    "s_cmp_lg_u32 s99, 0\n" \
    "s_cbranch_scc1 L%=_Su6\n" \
    "v_mov_b32 v216, 1\n" \
    "buffer_store_dword v216, off, s[44:47], 0 sc1\n" \
    "L%=_Su6:\n" \
    "s_sleep 1\n" \
    "buffer_load_dwordx4 v[168:171], v225, s[40:43], s60 offen sc1\n" \
    "buffer_load_dwordx4 v[172:175], v225, s[40:43], s85 offen sc1\n" \
    "v_mov_b64 v[176:177], 0\n" \
    "v_mov_b64 v[178:179], 0\n" \
    "v_mov_b64 v[180:181], 0\n" \
    "v_mov_b64 v[182:183], 0\n" \
    "v_mov_b64 v[184:185], 0\n" \
    "v_mov_b64 v[186:187], 0\n" \
    "v_mov_b64 v[188:189], 0\n" \
    "v_mov_b64 v[190:191], 0\n" \
    "s_waitcnt vmcnt(0)\n" \
    "s_branch L%=_Svc\n" \
    "L%=_Sfs:\n" \
    "s_nop 7\n" \
    "s_nop 0\n" \
    "v_mov_b64 v[196:197], 0\n" \
    "v_mov_b64 v[198:199], 0\n" \
    "v_mov_b64 v[200:201], 0\n" \
    "v_mov_b64 v[202:203], 0\n" \
    "v_mov_b64 v[204:205], 0\n" \
    "v_mov_b64 v[206:207], 0\n" \
    "v_mov_b64 v[208:209], 0\n" \
    "v_mov_b64 v[210:211], 0\n" \
    "v_mov_b64 v[212:213], 0\n" \
    "v_mov_b64 v[214:215], 0\n" \
    "s_branch L%=_Sfr\n" \
    "L%=_R:\n" \
    "s_mov_b32 s76, 0\n" \
    "s_mov_b32 s99, 0x20000\n" \
    "s_mov_b32 s84, 0\n" \
    "s_mov_b32 s81, 0\n" \
    "s_mov_b32 s86, 0\n" \
    "s_mov_b32 s87, 0\n" \
    "s_mov_b32 s88, 0\n" \
    "s_mov_b32 s89, 0\n" \
    "s_mov_b32 s90, 0\n" \
    "s_mov_b32 s91, 0\n" \
    "s_mov_b32 s92, 0\n" \
    "s_mov_b32 s93, 0\n" \
    "s_mov_b32 s94, 0\n" \
    "s_mov_b32 s95, 0\n" \
    "s_mov_b32 s96, 0\n" \
    "s_mov_b32 s97, 0\n" \
    "s_mov_b32 s98, 0\n" \
    "ds_read_b32 v216, v229\n" \
    "s_waitcnt lgkmcnt(0)\n" \
    "v_readfirstlane_b32 s94, v216\n" \
    "s_mul_i32 s79, s94, s70\n" \
    "s_add_u32 s79, s79, s81\n" \
    "s_mov_b32 s77, s79\n" \
    "s_mul_i32 s96, s65, s71\n" \
    "s_lshr_b32 s96, s96, 4\n" \
    "v_mov_b32 v96, 0x3f803f80\n" \
    "v_mov_b32 v97, 0x3f803f80\n" \
    "v_mov_b32 v98, 0x3f803f80\n" \
    "v_mov_b32 v99, 0x3f803f80\n" \
    "v_mov_b32 v100, 0x3f803f80\n" \
    "v_mov_b32 v101, 0x3f803f80\n" \
    "v_mov_b64 v[102:103], 0\n" \
    "v_mov_b64 v[64:65], 0\n" \
    "v_mov_b64 v[66:67], 0\n" \
    "v_mov_b64 v[68:69], 0\n" \
    "v_mov_b64 v[70:71], 0\n" \
    "v_mov_b64 v[72:73], 0\n" \
    "v_mov_b64 v[74:75], 0\n" \
    "v_mov_b64 v[76:77], 0\n" \
    "v_mov_b64 v[78:79], 0\n" \
    "v_mov_b64 v[80:81], 0\n" \
    "v_mov_b64 v[82:83], 0\n" \
    "v_mov_b64 v[84:85], 0\n" \
    "v_mov_b64 v[86:87], 0\n" \
    "v_mov_b64 v[88:89], 0\n" \
    "v_mov_b64 v[90:91], 0\n" \
    "v_mov_b64 v[92:93], 0\n" \
    "v_mov_b64 v[94:95], 0\n" \
    "v_mov_b64 v[192:193], 0\n" \
    "v_mov_b64 v[194:195], 0\n" \
    "v_mov_b32 v222, v236\n" \
    "v_mov_b32 v223, 0\n" \
    "ds_read_b64_tr_b16 v[104:105], v228 offset:16384\n" \
    "ds_read_b64_tr_b16 v[106:107], v228 offset:16416\n" \
    "ds_read_b64_tr_b16 v[108:109], v228 offset:18688\n" \
    "ds_read_b64_tr_b16 v[110:111], v228 offset:18720\n" \
    "ds_read_b64_tr_b16 v[112:113], v228 offset:20992\n" \
    "ds_read_b64_tr_b16 v[114:115], v228 offset:21024\n" \
    "ds_read_b64_tr_b16 v[116:117], v228 offset:23296\n" \
    "ds_read_b64_tr_b16 v[118:119], v228 offset:23328\n" \
    "ds_read_b64_tr_b16 v[120:121], v228 offset:16656\n" \
    "ds_read_b64_tr_b16 v[122:123], v228 offset:16688\n" \
    "ds_read_b64_tr_b16 v[124:125], v228 offset:18960\n" \
    "ds_read_b64_tr_b16 v[126:127], v228 offset:18992\n" \
    "ds_read_b64_tr_b16 v[128:129], v228 offset:21264\n" \
    "ds_read_b64_tr_b16 v[130:131], v228 offset:21296\n" \
    "ds_read_b64_tr_b16 v[132:133], v228 offset:23568\n" \
    "ds_read_b64_tr_b16 v[134:135], v228 offset:23600\n" \
    "s_waitcnt lgkmcnt(14)\n" \
    "v_mfma_f32_16x16x32_bf16 v[136:139], v[104:107], v[0:3], 0\n" \
    "v_mfma_f32_16x16x32_bf16 v[144:147], v[104:107], v[8:11], 0\n" \
    "v_mfma_f32_16x16x32_bf16 v[152:155], v[104:107], v[16:19], 0\n" \
    "v_mfma_f32_16x16x32_bf16 v[160:163], v[104:107], v[24:27], 0\n" \
    "s_waitcnt lgkmcnt(12)\n" \
    "v_mfma_f32_16x16x32_bf16 v[136:139], v[108:111], v[4:7], v[136:139]\n" \
    "v_mfma_f32_16x16x32_bf16 v[144:147], v[108:111], v[12:15], v[144:147]\n" \
    "v_mfma_f32_16x16x32_bf16 v[152:155], v[108:111], v[20:23], v[152:155]\n" \
    "v_mfma_f32_16x16x32_bf16 v[160:163], v[108:111], v[28:31], v[160:163]\n" \
    "s_waitcnt lgkmcnt(10)\n" \
    "v_mfma_f32_16x16x32_bf16 v[136:139], v[112:115], v[32:35], v[136:139]\n" \
    "v_mfma_f32_16x16x32_bf16 v[144:147], v[112:115], v[40:43], v[144:147]\n" \
    "v_mfma_f32_16x16x32_bf16 v[152:155], v[112:115], v[48:51], v[152:155]\n" \
    "v_mfma_f32_16x16x32_bf16 v[160:163], v[112:115], v[56:59], v[160:163]\n" \
    "s_waitcnt lgkmcnt(8)\n" \
    "v_mfma_f32_16x16x32_bf16 v[136:139], v[116:119], v[36:39], v[136:139]\n" \
    "v_mfma_f32_16x16x32_bf16 v[144:147], v[116:119], v[44:47], v[144:147]\n" \
    "v_mfma_f32_16x16x32_bf16 v[152:155], v[116:119], v[52:55], v[152:155]\n" \
    "v_mfma_f32_16x16x32_bf16 v[160:163], v[116:119], v[60:63], v[160:163]\n" \
    "s_waitcnt lgkmcnt(6)\n" \
    "v_mfma_f32_16x16x32_bf16 v[140:143], v[120:123], v[0:3], 0\n" \
    "v_mfma_f32_16x16x32_bf16 v[148:151], v[120:123], v[8:11], 0\n" \
    "v_mfma_f32_16x16x32_bf16 v[156:159], v[120:123], v[16:19], 0\n" \
    "v_mfma_f32_16x16x32_bf16 v[164:167], v[120:123], v[24:27], 0\n" \
    "s_waitcnt lgkmcnt(4)\n" \
    "v_mfma_f32_16x16x32_bf16 v[140:143], v[124:127], v[4:7], v[140:143]\n" \
    "v_mfma_f32_16x16x32_bf16 v[148:151], v[124:127], v[12:15], v[148:151]\n" \
    "v_mfma_f32_16x16x32_bf16 v[156:159], v[124:127], v[20:23], v[156:159]\n" \
    "v_mfma_f32_16x16x32_bf16 v[164:167], v[124:127], v[28:31], v[164:167]\n" \
    "s_waitcnt lgkmcnt(2)\n" \
    "v_mfma_f32_16x16x32_bf16 v[140:143], v[128:131], v[32:35], v[140:143]\n" \
    "v_mfma_f32_16x16x32_bf16 v[148:151], v[128:131], v[40:43], v[148:151]\n" \
    "v_mfma_f32_16x16x32_bf16 v[156:159], v[128:131], v[48:51], v[156:159]\n" \
    "v_mfma_f32_16x16x32_bf16 v[164:167], v[128:131], v[56:59], v[164:167]\n" \
    "s_waitcnt lgkmcnt(0)\n" \
    "v_mfma_f32_16x16x32_bf16 v[140:143], v[132:135], v[36:39], v[140:143]\n" \
    "v_mfma_f32_16x16x32_bf16 v[148:151], v[132:135], v[44:47], v[148:151]\n" \
    "v_mfma_f32_16x16x32_bf16 v[156:159], v[132:135], v[52:55], v[156:159]\n" \
    "v_mfma_f32_16x16x32_bf16 v[164:167], v[132:135], v[60:63], v[164:167]\n" \
    "v_cvt_pk_bf16_f32 v65, v136, v137\n" \
    "v_cvt_pk_bf16_f32 v68, v138, v139\n" \
    "v_cvt_pk_bf16_f32 v73, v144, v145\n" \
    "v_cvt_pk_bf16_f32 v76, v146, v147\n" \
    "v_cvt_pk_bf16_f32 v81, v152, v153\n" \
    "v_cvt_pk_bf16_f32 v84, v154, v155\n" \
    "v_cvt_pk_bf16_f32 v89, v160, v161\n" \
    "v_cvt_pk_bf16_f32 v92, v162, v163\n" \
    "v_cvt_pk_bf16_f32 v66, v142, v143\n" \
    "v_cvt_pk_bf16_f32 v67, v142, v143\n" \
    "v_cvt_pk_bf16_f32 v69, v140, v141\n" \
    "v_cvt_pk_bf16_f32 v74, v150, v151\n" \
    "v_cvt_pk_bf16_f32 v75, v150, v151\n" \
    "v_cvt_pk_bf16_f32 v77, v148, v149\n" \
    "v_cvt_pk_bf16_f32 v82, v158, v159\n" \
    "v_cvt_pk_bf16_f32 v83, v158, v159\n" \
    "v_cvt_pk_bf16_f32 v85, v156, v157\n" \
    "v_cvt_pk_bf16_f32 v90, v166, v167\n" \
    "v_cvt_pk_bf16_f32 v91, v166, v167\n" \
    "v_cvt_pk_bf16_f32 v93, v164, v165\n" \
    "v_permlane16_swap_b32 v66, v67\n" \
    "v_permlane16_swap_b32 v74, v75\n" \
    "v_permlane16_swap_b32 v82, v83\n" \
    "v_permlane16_swap_b32 v90, v91\n" \
    "L%=_Rloop:\n" \
    "v_mov_b64 v[176:177], 0\n" \
    "v_mov_b64 v[178:179], 0\n" \
    "v_mov_b64 v[180:181], 0\n" \
    "v_mov_b64 v[182:183], 0\n" \
    "v_mov_b64 v[184:185], 0\n" \
    "v_mov_b64 v[186:187], 0\n" \
    "v_mov_b64 v[188:189], 0\n" \
    "v_mov_b64 v[190:191], 0\n" \
    "v_mov_b64 v[196:197], 0\n" \
    "v_mov_b64 v[198:199], 0\n" \
    "v_mov_b64 v[200:201], 0\n" \
    "v_mov_b64 v[202:203], 0\n" \
    "v_mov_b64 v[204:205], 0\n" \
    "v_mov_b64 v[206:207], 0\n" \
    "v_mov_b64 v[208:209], 0\n" \
    "v_mov_b64 v[210:211], 0\n" \
    "v_mov_b64 v[212:213], 0\n" \
    "v_mov_b64 v[214:215], 0\n" \
    "s_add_u32 s85, s60, 0x1000\n" \
    "s_branch L%=_Ru2b\n" \
    "L%=_Ru2a:\n" \
    "s_sleep 4\n" \
    "L%=_Ru2b:\n" \
    "s_memrealtime vcc\n" \
    "s_add_u32 s80, s77, s64\n" \
    "s_add_u32 s80, s80, s69\n" \
    "s_waitcnt lgkmcnt(0)\n" \
    "s_sub_i32 s79, s80, vcc_lo\n" \
    "s_sub_u32 s79, s79, 40\n" \
    "s_cmp_lt_u32 s79, 0x400\n" \
    "s_cbranch_scc1 L%=_Ru2a\n" \
    "s_memtime s[38:39]\n" \
    "s_memrealtime vcc\n" \
    "s_nop 4\n" \
    "s_memrealtime s[32:33]\n" \
    "s_nop 4\n" \
    "s_memrealtime s[34:35]\n" \
    "s_nop 4\n" \
    "s_memrealtime s[36:37]\n" \
    "s_add_u32 s80, s77, s64\n" \
    "s_lshl_b32 s80, s80, 2\n" \
    "s_waitcnt lgkmcnt(0)\n" \
    "s_sub_u32 s80, s80, vcc_lo\n" \
    "s_sub_u32 s80, s80, s32\n" \
    "s_sub_u32 s80, s80, s34\n" \
    "s_sub_u32 s80, s80, s36\n" \
    "s_mul_i32 s80, s80, s71\n" \
    "s_ashr_i32 s80, s80, 6\n" \
    "s_add_u32 s78, s80, s38\n" \
    "L%=_Ru3a:\n" \
    "s_memtime s[38:39]\n" \
    "s_mul_i32 s80, s69, 23\n" \
    "s_add_u32 s80, s80, s78\n" \
    "s_waitcnt lgkmcnt(0)\n" \
    "s_sub_i32 s79, s80, s38\n" \
    "s_max_i32 s79, s79, 0\n" \
    "s_cmp_gt_u32 s79, 320\n" \
    "s_cbranch_scc1 L%=_Ru3s\n" \
    ".p2align 4\n" \
    "L%=_Ru3d:\n" \
    "s_sub_u32 s79, s79, 28\n" \
    "s_cbranch_scc0 L%=_Ru3d\n" \
    "L%=_Ru3e:\n" \
    "s_sub_u32 s79, s84, 0x20000\n" \
    "s_cselect_b32 s79, 0x40000000, s79\n" \
    "buffer_store_dwordx4 v[236:239], v234, s[48:51], s79 offen sc1\n" \
    "s_setprio 3\n" \
    "s_barrier\n" \
    "s_nop 14\n" \
    "L%=_Rvs:\n" \
    "ds_read_b128 v[104:107], v229 offset:16\n" \
    "ds_read_b128 v[108:111], v230 offset:8704\n" \
    "ds_read_b128 v[112:115], v230 offset:9984\n" \
    "ds_read_b128 v[116:119], v230 offset:11264\n" \
    "ds_read_b128 v[120:123], v230 offset:12544\n" \
    "s_waitcnt lgkmcnt(4)\n" \
    "v_min3_u32 v104, v104, v105, v106\n" \
    "v_min_u32 v104, v104, v107\n" \
    "s_nop 0\n" \
    "v_readfirstlane_b32 s79, v104\n" \
    "s_cmp_le_u32 s79, s76\n" \
    "s_cbranch_scc1 L%=_Rvs\n" \
    "s_waitcnt lgkmcnt(2)\n" \
    "v_pk_add_f32 v[108:109], v[108:109], v[112:113]\n" \
    "v_pk_add_f32 v[110:111], v[110:111], v[114:115]\n" \
    "s_waitcnt lgkmcnt(0)\n" \
    "v_pk_add_f32 v[116:117], v[116:117], v[120:121]\n" \
    "v_pk_add_f32 v[118:119], v[118:119], v[122:123]\n" \
    "v_pk_add_f32 v[108:109], v[108:109], v[116:117]\n" \
    "v_pk_add_f32 v[110:111], v[110:111], v[118:119]\n" \
    "v_cvt_pk_bf16_f32 v124, v108, v109\n" \
    "v_cvt_pk_bf16_f32 v125, v110, v111\n" \
    "s_nop 0\n" \
    "v_mov_b32_dpp v126, v124 quad_perm:[1,0,3,2] row_mask:0xf bank_mask:0xf\n" \
    "v_mov_b32_dpp v127, v125 quad_perm:[1,0,3,2] row_mask:0xf bank_mask:0xf\n" \
    "buffer_store_dwordx4 v[124:127], v231, s[40:43], s60 offen sc1\n" \
    "ds_write_b64 v232, v[124:125]\n" \
    "ds_read_b64_tr_b16 v[236:237], v233\n" \
    "ds_read_b64_tr_b16 v[238:239], v233 offset:256\n" \
    "s_cmp_eq_u32 s76, 63\n" \
    "s_cbranch_scc1 L%=_Rfin\n" \
    "ds_read_b32 v216, v229\n" \
    "s_waitcnt lgkmcnt(0)\n" \
    "v_readfirstlane_b32 s94, v216\n" \
    "s_mul_i32 s79, s94, s70\n" \
    "s_add_u32 s79, s79, s81\n" \
    "s_cmp_lg_u32 s79, s77\n" \
    "s_cbranch_scc1 L%=_Ru4s\n" \
    "L%=_Ru4k:\n" \
    "L%=_Ru5a:\n" \
    "s_memtime s[38:39]\n" \
    "s_mul_hi_u32 s80, s67, s96\n" \
    "s_add_u32 s80, s80, s78\n" \
    "s_waitcnt lgkmcnt(0)\n" \
    "s_sub_i32 s79, s80, s38\n" \
    "s_max_i32 s79, s79, 0\n" \
    "s_cmp_gt_u32 s79, 320\n" \
    "s_cbranch_scc1 L%=_Ru5s\n" \
    ".p2align 4\n" \
    "L%=_Ru5d:\n" \
    "s_sub_u32 s79, s79, 28\n" \
    "s_cbranch_scc0 L%=_Ru5d\n" \
    "L%=_Ru5e:\n" \
    "buffer_load_dwordx4 v[168:171], v225, s[40:43], s60 offen sc1\n" \
    "buffer_load_dwordx4 v[172:175], v225, s[40:43], s85 offen sc1\n" \
    "s_memtime s[38:39]\n" \
    "s_memrealtime vcc\n" \
    "s_nop 4\n" \
    "s_memrealtime s[32:33]\n" \
    "s_nop 4\n" \
    "s_memrealtime s[34:35]\n" \
    "s_nop 4\n" \
    "s_memrealtime s[36:37]\n" \
    "s_add_u32 s80, s77, s64\n" \
    "s_lshl_b32 s80, s80, 2\n" \
    "s_waitcnt lgkmcnt(0)\n" \
    "s_sub_u32 s80, s80, vcc_lo\n" \
    "s_sub_u32 s80, s80, s32\n" \
    "s_sub_u32 s80, s80, s34\n" \
    "s_sub_u32 s80, s80, s36\n" \
    "s_mul_i32 s80, s80, s71\n" \
    "s_ashr_i32 s80, s80, 6\n" \
    "s_add_u32 s78, s80, s38\n" \
    "L%=_Rvc:\n" \
    "s_waitcnt vmcnt(1)\n" \
    "v_smfmac_f32_16x16x64_bf16 v[176:179], v[168:171], v[0:7], v224\n" \
    "v_smfmac_f32_16x16x64_bf16 v[180:183], v[168:171], v[8:15], v224\n" \
    "s_waitcnt vmcnt(0)\n" \
    "v_smfmac_f32_16x16x64_bf16 v[176:179], v[172:175], v[32:39], v224\n" \
    "v_smfmac_f32_16x16x64_bf16 v[180:183], v[172:175], v[40:47], v224\n" \
    "v_smfmac_f32_16x16x64_bf16 v[184:187], v[168:171], v[16:23], v224\n" \
    "v_smfmac_f32_16x16x64_bf16 v[188:191], v[168:171], v[24:31], v224\n" \
    "v_add_f32 v176, v176, v178\n" \
    "v_smfmac_f32_16x16x64_bf16 v[184:187], v[172:175], v[48:55], v224\n" \
    "v_add_f32 v177, v177, v179\n" \
    "v_smfmac_f32_16x16x64_bf16 v[188:191], v[172:175], v[56:63], v224\n" \
    "v_cmp_u_f32 vcc, v176, v177\n" \
    "v_cvt_pk_bf16_f32 v64, v176, v177\n" \
    "v_add_f32 v180, v180, v182\n" \
    "v_add_f32 v181, v181, v183\n" \
    "v_cvt_pk_bf16_f32 v72, v180, v181\n" \
    "s_cbranch_vccnz L%=_Rvslow\n" \
    "L%=_Rvok:\n" \
    "v_add_f32 v184, v184, v186\n" \
    "v_add_f32 v185, v185, v187\n" \
    "v_cvt_pk_bf16_f32 v80, v184, v185\n" \
    "v_add_f32 v188, v188, v190\n" \
    "v_add_f32 v189, v189, v191\n" \
    "v_cvt_pk_bf16_f32 v88, v188, v189\n" \
    "s_barrier\n" \
    "L%=_Rfr:\n" \
    "ds_read_b128 v[192:195], v227\n" \
    "s_waitcnt lgkmcnt(0)\n" \
    "v_smfmac_f32_16x16x64_bf16 v[212:215], v[192:195], v[96:103], v224\n" \
    "v_cmp_ne_u32 vcc, s76, v195\n" \
    "v_smfmac_f32_16x16x64_bf16 v[196:199], v[192:195], v[64:71], v224\n" \
    "s_cbranch_vccnz L%=_Rfs\n" \
    "v_smfmac_f32_16x16x64_bf16 v[200:203], v[192:195], v[72:79], v224\n" \
    "v_smfmac_f32_16x16x64_bf16 v[204:207], v[192:195], v[80:87], v224\n" \
    "v_add_f32 v212, v212, v214\n" \
    "v_smfmac_f32_16x16x64_bf16 v[208:211], v[192:195], v[88:95], v224\n" \
    "v_add_f32 v213, v213, v215\n" \
    "v_rcp_f32 v220, v212\n" \
    "v_rcp_f32 v221, v213\n" \
    "v_add_f32 v196, v196, v198\n" \
    "v_add_f32 v197, v197, v199\n" \
    "v_pk_add_f32 v[200:201], v[200:201], v[202:203]\n" \
    "v_pk_add_f32 v[204:205], v[204:205], v[206:207]\n" \
    "s_nop 0\n" \
    "v_pk_add_f32 v[208:209], v[208:209], v[210:211]\n" \
    "v_pk_mul_f32 v[196:197], v[196:197], v[220:221]\n" \
    "v_pk_mul_f32 v[200:201], v[200:201], v[220:221]\n" \
    "v_pk_mul_f32 v[204:205], v[204:205], v[220:221]\n" \
    "v_pk_mul_f32 v[208:209], v[208:209], v[220:221]\n" \
    "v_cvt_pk_bf16_f32 v216, v196, v200\n" \
    "v_cvt_pk_bf16_f32 v217, v204, v208\n" \
    "v_cvt_pk_bf16_f32 v218, v197, v201\n" \
    "v_cvt_pk_bf16_f32 v219, v205, v209\n" \
    "buffer_store_dwordx4 v[216:219], v226, s[40:43], s60 offen sc1\n" \
    "ds_read_b64_tr_b16 v[104:105], v228 offset:16384\n" \
    "ds_read_b64_tr_b16 v[106:107], v228 offset:16416\n" \
    "ds_read_b64_tr_b16 v[108:109], v228 offset:18688\n" \
    "ds_read_b64_tr_b16 v[110:111], v228 offset:18720\n" \
    "ds_read_b64_tr_b16 v[112:113], v228 offset:20992\n" \
    "ds_read_b64_tr_b16 v[114:115], v228 offset:21024\n" \
    "ds_read_b64_tr_b16 v[116:117], v228 offset:23296\n" \
    "ds_read_b64_tr_b16 v[118:119], v228 offset:23328\n" \
    "ds_read_b64_tr_b16 v[120:121], v228 offset:16656\n" \
    "ds_read_b64_tr_b16 v[122:123], v228 offset:16688\n" \
    "ds_read_b64_tr_b16 v[124:125], v228 offset:18960\n" \
    "ds_read_b64_tr_b16 v[126:127], v228 offset:18992\n" \
    "ds_read_b64_tr_b16 v[128:129], v228 offset:21264\n" \
    "ds_read_b64_tr_b16 v[130:131], v228 offset:21296\n" \
    "ds_read_b64_tr_b16 v[132:133], v228 offset:23568\n" \
    "ds_read_b64_tr_b16 v[134:135], v228 offset:23600\n" \
    "s_waitcnt lgkmcnt(14)\n" \
    "v_mfma_f32_16x16x32_bf16 v[136:139], v[104:107], v[0:3], 0\n" \
    "v_mfma_f32_16x16x32_bf16 v[144:147], v[104:107], v[8:11], 0\n" \
    "v_mfma_f32_16x16x32_bf16 v[152:155], v[104:107], v[16:19], 0\n" \
    "v_mfma_f32_16x16x32_bf16 v[160:163], v[104:107], v[24:27], 0\n" \
    "s_waitcnt lgkmcnt(12)\n" \
    "v_mfma_f32_16x16x32_bf16 v[136:139], v[108:111], v[4:7], v[136:139]\n" \
    "v_mfma_f32_16x16x32_bf16 v[144:147], v[108:111], v[12:15], v[144:147]\n" \
    "v_mfma_f32_16x16x32_bf16 v[152:155], v[108:111], v[20:23], v[152:155]\n" \
    "v_mfma_f32_16x16x32_bf16 v[160:163], v[108:111], v[28:31], v[160:163]\n" \
    "s_waitcnt lgkmcnt(10)\n" \
    "v_mfma_f32_16x16x32_bf16 v[136:139], v[112:115], v[32:35], v[136:139]\n" \
    "v_mfma_f32_16x16x32_bf16 v[144:147], v[112:115], v[40:43], v[144:147]\n" \
    "v_mfma_f32_16x16x32_bf16 v[152:155], v[112:115], v[48:51], v[152:155]\n" \
    "v_mfma_f32_16x16x32_bf16 v[160:163], v[112:115], v[56:59], v[160:163]\n" \
    "s_waitcnt lgkmcnt(8)\n" \
    "v_mfma_f32_16x16x32_bf16 v[136:139], v[116:119], v[36:39], v[136:139]\n" \
    "v_mfma_f32_16x16x32_bf16 v[144:147], v[116:119], v[44:47], v[144:147]\n" \
    "v_mfma_f32_16x16x32_bf16 v[152:155], v[116:119], v[52:55], v[152:155]\n" \
    "v_mfma_f32_16x16x32_bf16 v[160:163], v[116:119], v[60:63], v[160:163]\n" \
    "v_add_u32 v223, 1, v223\n" \
    "s_and_b32 s95, s94, 1\n" \
    "s_lshl_b32 s95, s95, 17\n" \
    "s_add_u32 s95, s95, s61\n" \
    "s_add_u32 s85, s95, 0x1000\n" \
    "s_xor_b32 s85, s95, 0x20000\n" \
    "L%=_Ru6a:\n" \
    "s_memtime s[38:39]\n" \
    "s_mul_hi_u32 s80, s68, s96\n" \
    "s_add_u32 s80, s80, s78\n" \
    "s_waitcnt lgkmcnt(0)\n" \
    "s_sub_i32 s79, s80, s38\n" \
    "s_max_i32 s79, s79, 0\n" \
    "s_cmp_gt_u32 s79, 320\n" \
    "s_cbranch_scc1 L%=_Ru6s\n" \
    ".p2align 4\n" \
    "L%=_Ru6d:\n" \
    "s_sub_u32 s79, s79, 28\n" \
    "s_cbranch_scc0 L%=_Ru6d\n" \
    "L%=_Ru6e:\n" \
    "buffer_load_dwordx4 v[176:179], v235, s[40:43], s60 offen offset:0 sc1\n" \
    "buffer_load_dwordx4 v[180:183], v235, s[40:43], s60 offen offset:1024 sc1\n" \
    "buffer_load_dwordx4 v[184:187], v235, s[40:43], s60 offen offset:2048 sc1\n" \
    "buffer_load_dwordx4 v[188:191], v235, s[40:43], s60 offen offset:3072 sc1\n" \
    "v_mfma_f32_16x16x32_bf16 v[140:143], v[120:123], v[0:3], 0\n" \
    "v_mfma_f32_16x16x32_bf16 v[148:151], v[120:123], v[8:11], 0\n" \
    "v_mfma_f32_16x16x32_bf16 v[156:159], v[120:123], v[16:19], 0\n" \
    "v_mfma_f32_16x16x32_bf16 v[164:167], v[120:123], v[24:27], 0\n" \
    "v_mfma_f32_16x16x32_bf16 v[140:143], v[124:127], v[4:7], v[140:143]\n" \
    "v_mfma_f32_16x16x32_bf16 v[148:151], v[124:127], v[12:15], v[148:151]\n" \
    "v_mfma_f32_16x16x32_bf16 v[156:159], v[124:127], v[20:23], v[156:159]\n" \
    "v_mfma_f32_16x16x32_bf16 v[164:167], v[124:127], v[28:31], v[164:167]\n" \
    "v_mfma_f32_16x16x32_bf16 v[140:143], v[128:131], v[32:35], v[140:143]\n" \
    "v_mfma_f32_16x16x32_bf16 v[148:151], v[128:131], v[40:43], v[148:151]\n" \
    "v_mfma_f32_16x16x32_bf16 v[156:159], v[128:131], v[48:51], v[156:159]\n" \
    "v_mfma_f32_16x16x32_bf16 v[164:167], v[128:131], v[56:59], v[164:167]\n" \
    "v_mfma_f32_16x16x32_bf16 v[140:143], v[132:135], v[36:39], v[140:143]\n" \
    "v_mfma_f32_16x16x32_bf16 v[148:151], v[132:135], v[44:47], v[148:151]\n" \
    "v_mfma_f32_16x16x32_bf16 v[156:159], v[132:135], v[52:55], v[156:159]\n" \
    "v_mfma_f32_16x16x32_bf16 v[164:167], v[132:135], v[60:63], v[164:167]\n" \
    "v_cvt_pk_bf16_f32 v65, v136, v137\n" \
    "v_cvt_pk_bf16_f32 v68, v138, v139\n" \
    "v_cvt_pk_bf16_f32 v73, v144, v145\n" \
    "v_cvt_pk_bf16_f32 v76, v146, v147\n" \
    "v_cvt_pk_bf16_f32 v81, v152, v153\n" \
    "v_cvt_pk_bf16_f32 v84, v154, v155\n" \
    "v_cvt_pk_bf16_f32 v89, v160, v161\n" \
    "v_cvt_pk_bf16_f32 v92, v162, v163\n" \
    "L%=_Rred:\n" \
    "s_waitcnt vmcnt(3)\n" \
    "v_mfma_f32_16x16x32_bf16 v[240:243], v[244:247], v[176:179], 0\n" \
    "s_waitcnt vmcnt(2)\n" \
    "v_mfma_f32_16x16x32_bf16 v[240:243], v[244:247], v[180:183], v[240:243]\n" \
    "s_waitcnt vmcnt(1)\n" \
    "v_mfma_f32_16x16x32_bf16 v[240:243], v[244:247], v[184:187], v[240:243]\n" \
    "s_waitcnt vmcnt(0)\n" \
    "v_mfma_f32_16x16x32_bf16 v[240:243], v[244:247], v[188:191], v[240:243]\n" \
    "s_nop 7\n" \
    "v_cmp_u_f32 vcc, v240, v240\n" \
    "v_add_f32_dpp v240, v240, v240 row_ror:8 row_mask:0xf bank_mask:0xf\n" \
    "v_add_f32_dpp v241, v241, v241 row_ror:8 row_mask:0xf bank_mask:0xf\n" \
    "v_add_f32_dpp v242, v242, v242 row_ror:8 row_mask:0xf bank_mask:0xf\n" \
    "v_add_f32_dpp v243, v243, v243 row_ror:8 row_mask:0xf bank_mask:0xf\n" \
    "v_cvt_pk_bf16_f32 v216, v240, v241\n" \
    "v_cvt_pk_bf16_f32 v217, v242, v243\n" \
    "s_cbranch_vccnz L%=_Rslow\n" \
    "L%=_Rok:\n" \
    "v_mov_b32_dpp v218, v216 quad_perm:[1,0,3,2] row_mask:0xf bank_mask:0xf\n" \
    "v_mov_b32_dpp v219, v217 quad_perm:[1,0,3,2] row_mask:0xf bank_mask:0xf\n" \
    "buffer_store_dwordx4 v[216:219], v222, s[40:43], s95 offen sc1\n" \
    "ds_write_b32 v229, v223 offset:4\n" \
    "buffer_store_dwordx4 v[216:219], v222, s[40:43], s85 offen sc1\n" \
    "s_setprio 0\n" \
    "v_cvt_pk_bf16_f32 v66, v142, v143\n" \
    "v_cvt_pk_bf16_f32 v67, v142, v143\n" \
    "v_cvt_pk_bf16_f32 v69, v140, v141\n" \
    "v_cvt_pk_bf16_f32 v74, v150, v151\n" \
    "v_cvt_pk_bf16_f32 v75, v150, v151\n" \
    "v_cvt_pk_bf16_f32 v77, v148, v149\n" \
    "v_cvt_pk_bf16_f32 v82, v158, v159\n" \
    "v_cvt_pk_bf16_f32 v83, v158, v159\n" \
    "v_cvt_pk_bf16_f32 v85, v156, v157\n" \
    "v_cvt_pk_bf16_f32 v90, v166, v167\n" \
    "v_cvt_pk_bf16_f32 v91, v166, v167\n" \
    "v_cvt_pk_bf16_f32 v93, v164, v165\n" \
    "v_permlane16_swap_b32 v66, v67\n" \
    "v_permlane16_swap_b32 v74, v75\n" \
    "v_permlane16_swap_b32 v82, v83\n" \
    "v_permlane16_swap_b32 v90, v91\n" \
    "ds_read_b64 v[220:221], v229 offset:64\n" \
    "s_mov_b32 s79, s60\n" \
    "s_mov_b32 s60, s61\n" \
    "s_mov_b32 s61, s62\n" \
    "s_mov_b32 s62, s63\n" \
    "s_mov_b32 s63, s79\n" \
    "s_add_u32 s76, s76, 1\n" \
    "s_add_u32 s77, s77, s65\n" \
    "s_add_u32 s84, s84, 0x20000\n" \
    "s_add_u32 s81, s81, s65\n" \
    "s_waitcnt lgkmcnt(0)\n" \
    "v_readfirstlane_b32 s65, v220\n" \
    "v_readfirstlane_b32 s71, v221\n" \
    "s_mul_i32 s96, s65, s71\n" \
    "s_lshr_b32 s96, s96, 4\n" \
    "s_branch L%=_Rloop\n" \
    "L%=_Rslow:\n" \
    "s_nop 7\n" \
    "s_nop 0\n" \
    "v_max_u32 v220, v176, v177\n" \
    "v_max3_u32 v220, v220, v178, v179\n" \
    "v_max3_u32 v220, v220, v180, v181\n" \
    "v_max3_u32 v220, v220, v182, v183\n" \
    "v_max3_u32 v220, v220, v184, v185\n" \
    "v_max3_u32 v220, v220, v186, v187\n" \
    "v_max3_u32 v220, v220, v188, v189\n" \
    "v_max3_u32 v220, v220, v190, v191\n" \
    "v_cmp_eq_u32 vcc, -1, v220\n" \
    "s_cbranch_vccz L%=_Rok\n" \
    "s_cmp_eq_u32 s99, 0\n" \
    "s_cbranch_scc1 L%=_Rok\n" \
    "s_sub_u32 s99, s99, 1\n" \
    "s_cmp_lg_u32 s99, 0\n" \
    "s_cbranch_scc1 L%=_Ru7\n" \
    "v_mov_b32 v220, 1\n" \
    "buffer_store_dword v220, off, s[44:47], 0 sc1\n" \
    "L%=_Ru7:\n" \
    "s_sleep 1\n" \
    "buffer_load_dwordx4 v[176:179], v235, s[40:43], s60 offen offset:0 sc1\n" \
    "buffer_load_dwordx4 v[180:183], v235, s[40:43], s60 offen offset:1024 sc1\n" \
    "buffer_load_dwordx4 v[184:187], v235, s[40:43], s60 offen offset:2048 sc1\n" \
    "buffer_load_dwordx4 v[188:191], v235, s[40:43], s60 offen offset:3072 sc1\n" \
    "s_branch L%=_Rred\n" \
    "L%=_Rfin:\n" \
    "s_waitcnt lgkmcnt(0)\n" \
    "buffer_store_dwordx4 v[236:239], v234, s[48:51], s84 offen sc1\n" \
    "s_waitcnt vmcnt(0)\n" \
    "s_barrier\n" \
    "s_branch L%=_end\n" \
    "L%=_Ru3s:\n" \
    "s_cmp_gt_u32 s79, 0x8000\n" \
    "s_cbranch_scc1 L%=_Ru3e\n" \
    "s_sleep 2\n" \
    "s_branch L%=_Ru3a\n" \
    "L%=_Ru4s:\n" \
    "s_mov_b32 s77, s79\n" \
    "s_memtime s[38:39]\n" \
    "s_memrealtime vcc\n" \
    "s_nop 4\n" \
    "s_memrealtime s[32:33]\n" \
    "s_nop 4\n" \
    "s_memrealtime s[34:35]\n" \
    "s_nop 4\n" \
    "s_memrealtime s[36:37]\n" \
    "s_add_u32 s80, s77, s64\n" \
    "s_lshl_b32 s80, s80, 2\n" \
    "s_waitcnt lgkmcnt(0)\n" \
    "s_sub_u32 s80, s80, vcc_lo\n" \
    "s_sub_u32 s80, s80, s32\n" \
    "s_sub_u32 s80, s80, s34\n" \
    "s_sub_u32 s80, s80, s36\n" \
    "s_mul_i32 s80, s80, s71\n" \
    "s_ashr_i32 s80, s80, 6\n" \
    "s_add_u32 s78, s80, s38\n" \
    "s_branch L%=_Ru4k\n" \
    "L%=_Ru5s:\n" \
    "s_cmp_gt_u32 s79, 0x8000\n" \
    "s_cbranch_scc1 L%=_Ru5e\n" \
    "s_sleep 2\n" \
    "s_branch L%=_Ru5a\n" \
    "L%=_Rvslow:\n" \
    "s_nop 7\n" \
    "s_nop 0\n" \
    "v_max_u32 v216, v168, v169\n" \
    "v_max3_u32 v216, v216, v170, v171\n" \
    "v_max3_u32 v216, v216, v172, v173\n" \
    "v_max3_u32 v216, v216, v174, v175\n" \
    "v_cmp_eq_u32 vcc, -1, v216\n" \
    "s_cbranch_vccz L%=_Rvok\n" \
    "s_cmp_eq_u32 s99, 0\n" \
    "s_cbranch_scc1 L%=_Rvok\n" \
    "s_sub_u32 s99, s99, 1\n" \
    "s_cmp_lg_u32 s99, 0\n" \
    "s_cbranch_scc1 L%=_Ru8\n" \
    "v_mov_b32 v216, 1\n" \
    "buffer_store_dword v216, off, s[44:47], 0 sc1\n" \
    "L%=_Ru8:\n" \
    "s_sleep 1\n" \
    "buffer_load_dwordx4 v[168:171], v225, s[40:43], s60 offen sc1\n" \
    "buffer_load_dwordx4 v[172:175], v225, s[40:43], s85 offen sc1\n" \
    "v_mov_b64 v[176:177], 0\n" \
    "v_mov_b64 v[178:179], 0\n" \
    "v_mov_b64 v[180:181], 0\n" \
    "v_mov_b64 v[182:183], 0\n" \
    "v_mov_b64 v[184:185], 0\n" \
    "v_mov_b64 v[186:187], 0\n" \
    "v_mov_b64 v[188:189], 0\n" \
    "v_mov_b64 v[190:191], 0\n" \
    "s_waitcnt vmcnt(0)\n" \
    "s_branch L%=_Rvc\n" \
    "L%=_Rfs:\n" \
    "s_nop 7\n" \
    "s_nop 0\n" \
    "v_mov_b64 v[196:197], 0\n" \
    "v_mov_b64 v[198:199], 0\n" \
    "v_mov_b64 v[200:201], 0\n" \
    "v_mov_b64 v[202:203], 0\n" \
    "v_mov_b64 v[204:205], 0\n" \
    "v_mov_b64 v[206:207], 0\n" \
    "v_mov_b64 v[208:209], 0\n" \
    "v_mov_b64 v[210:211], 0\n" \
    "v_mov_b64 v[212:213], 0\n" \
    "v_mov_b64 v[214:215], 0\n" \
    "s_branch L%=_Rfr\n" \
    "L%=_Ru6s:\n" \
    "s_cmp_gt_u32 s79, 0x8000\n" \
    "s_cbranch_scc1 L%=_Ru6e\n" \
    "s_sleep 2\n" \
    "s_branch L%=_Ru6a\n" \
    "L%=_P:\n" \
    "s_mov_b32 s76, 0\n" \
    "s_mov_b32 s99, 0x20000\n" \
    "s_mov_b32 s84, 0\n" \
    "s_mov_b32 s81, 0\n" \
    "s_mov_b32 s86, 0\n" \
    "s_mov_b32 s87, 0\n" \
    "s_mov_b32 s88, 0\n" \
    "s_mov_b32 s89, 0\n" \
    "s_mov_b32 s90, 0\n" \
    "s_mov_b32 s91, 0\n" \
    "s_mov_b32 s92, 0\n" \
    "s_mov_b32 s93, 0\n" \
    "s_mov_b32 s94, 0\n" \
    "s_mov_b32 s95, 0\n" \
    "s_mov_b32 s96, 0\n" \
    "s_mov_b32 s97, 0\n" \
    "s_mov_b32 s98, 0\n" \
    "ds_read_b32 v216, v229\n" \
    "s_waitcnt lgkmcnt(0)\n" \
    "v_readfirstlane_b32 s94, v216\n" \
    "s_mul_i32 s79, s94, s70\n" \
    "s_add_u32 s79, s79, s81\n" \
    "s_mov_b32 s77, s79\n" \
    "s_mul_i32 s96, s65, s71\n" \
    "s_lshr_b32 s96, s96, 4\n" \
    "v_mov_b32 v96, 0x3f803f80\n" \
    "v_mov_b32 v97, 0x3f803f80\n" \
    "v_mov_b32 v98, 0x3f803f80\n" \
    "v_mov_b32 v99, 0x3f803f80\n" \
    "v_mov_b32 v100, 0x3f803f80\n" \
    "v_mov_b32 v101, 0x3f803f80\n" \
    "v_mov_b64 v[102:103], 0\n" \
    "v_mov_b64 v[64:65], 0\n" \
    "v_mov_b64 v[66:67], 0\n" \
    "v_mov_b64 v[68:69], 0\n" \
    "v_mov_b64 v[70:71], 0\n" \
    "v_mov_b64 v[72:73], 0\n" \
    "v_mov_b64 v[74:75], 0\n" \
    "v_mov_b64 v[76:77], 0\n" \
    "v_mov_b64 v[78:79], 0\n" \
    "v_mov_b64 v[80:81], 0\n" \
    "v_mov_b64 v[82:83], 0\n" \
    "v_mov_b64 v[84:85], 0\n" \
    "v_mov_b64 v[86:87], 0\n" \
    "v_mov_b64 v[88:89], 0\n" \
    "v_mov_b64 v[90:91], 0\n" \
    "v_mov_b64 v[92:93], 0\n" \
    "v_mov_b64 v[94:95], 0\n" \
    "v_mov_b64 v[192:193], 0\n" \
    "v_mov_b64 v[194:195], 0\n" \
    "v_cvt_f32_u32 v240, s71\n" \
    "v_mov_b32 v241, 1.0\n" \
    "v_mov_b32 v242, 0\n" \
    "v_mov_b32 v243, s65\n" \
    "s_mov_b32 s74, s94\n" \
    "v_mov_b32 v244, -1\n" \
    "v_mov_b32 v245, -1\n" \
    "v_mov_b32 v246, -1\n" \
    "v_mov_b32 v247, -1\n" \
    "L%=_Ploop:\n" \
    "ds_read_b64_tr_b16 v[104:105], v228 offset:16384\n" \
    "ds_read_b64_tr_b16 v[106:107], v228 offset:16416\n" \
    "ds_read_b64_tr_b16 v[108:109], v228 offset:18688\n" \
    "ds_read_b64_tr_b16 v[110:111], v228 offset:18720\n" \
    "ds_read_b64_tr_b16 v[112:113], v228 offset:20992\n" \
    "ds_read_b64_tr_b16 v[114:115], v228 offset:21024\n" \
    "ds_read_b64_tr_b16 v[116:117], v228 offset:23296\n" \
    "ds_read_b64_tr_b16 v[118:119], v228 offset:23328\n" \
    "ds_read_b64_tr_b16 v[120:121], v228 offset:16656\n" \
    "ds_read_b64_tr_b16 v[122:123], v228 offset:16688\n" \
    "ds_read_b64_tr_b16 v[124:125], v228 offset:18960\n" \
    "ds_read_b64_tr_b16 v[126:127], v228 offset:18992\n" \
    "ds_read_b64_tr_b16 v[128:129], v228 offset:21264\n" \
    "ds_read_b64_tr_b16 v[130:131], v228 offset:21296\n" \
    "ds_read_b64_tr_b16 v[132:133], v228 offset:23568\n" \
    "ds_read_b64_tr_b16 v[134:135], v228 offset:23600\n" \
    "s_waitcnt lgkmcnt(14)\n" \
    "v_mfma_f32_16x16x32_bf16 v[136:139], v[104:107], v[0:3], 0\n" \
    "v_mfma_f32_16x16x32_bf16 v[144:147], v[104:107], v[8:11], 0\n" \
    "v_mfma_f32_16x16x32_bf16 v[152:155], v[104:107], v[16:19], 0\n" \
    "v_mfma_f32_16x16x32_bf16 v[160:163], v[104:107], v[24:27], 0\n" \
    "s_waitcnt lgkmcnt(12)\n" \
    "v_mfma_f32_16x16x32_bf16 v[136:139], v[108:111], v[4:7], v[136:139]\n" \
    "v_mfma_f32_16x16x32_bf16 v[144:147], v[108:111], v[12:15], v[144:147]\n" \
    "v_mfma_f32_16x16x32_bf16 v[152:155], v[108:111], v[20:23], v[152:155]\n" \
    "v_mfma_f32_16x16x32_bf16 v[160:163], v[108:111], v[28:31], v[160:163]\n" \
    "s_waitcnt lgkmcnt(10)\n" \
    "v_mfma_f32_16x16x32_bf16 v[136:139], v[112:115], v[32:35], v[136:139]\n" \
    "v_mfma_f32_16x16x32_bf16 v[144:147], v[112:115], v[40:43], v[144:147]\n" \
    "v_mfma_f32_16x16x32_bf16 v[152:155], v[112:115], v[48:51], v[152:155]\n" \
    "v_mfma_f32_16x16x32_bf16 v[160:163], v[112:115], v[56:59], v[160:163]\n" \
    "s_waitcnt lgkmcnt(8)\n" \
    "v_mfma_f32_16x16x32_bf16 v[136:139], v[116:119], v[36:39], v[136:139]\n" \
    "v_mfma_f32_16x16x32_bf16 v[144:147], v[116:119], v[44:47], v[144:147]\n" \
    "v_mfma_f32_16x16x32_bf16 v[152:155], v[116:119], v[52:55], v[152:155]\n" \
    "v_mfma_f32_16x16x32_bf16 v[160:163], v[116:119], v[60:63], v[160:163]\n" \
    "s_waitcnt lgkmcnt(6)\n" \
    "v_mfma_f32_16x16x32_bf16 v[140:143], v[120:123], v[0:3], 0\n" \
    "v_mfma_f32_16x16x32_bf16 v[148:151], v[120:123], v[8:11], 0\n" \
    "v_mfma_f32_16x16x32_bf16 v[156:159], v[120:123], v[16:19], 0\n" \
    "v_mfma_f32_16x16x32_bf16 v[164:167], v[120:123], v[24:27], 0\n" \
    "s_waitcnt lgkmcnt(4)\n" \
    "v_mfma_f32_16x16x32_bf16 v[140:143], v[124:127], v[4:7], v[140:143]\n" \
    "v_mfma_f32_16x16x32_bf16 v[148:151], v[124:127], v[12:15], v[148:151]\n" \
    "v_mfma_f32_16x16x32_bf16 v[156:159], v[124:127], v[20:23], v[156:159]\n" \
    "v_mfma_f32_16x16x32_bf16 v[164:167], v[124:127], v[28:31], v[164:167]\n" \
    "s_waitcnt lgkmcnt(2)\n" \
    "v_mfma_f32_16x16x32_bf16 v[140:143], v[128:131], v[32:35], v[140:143]\n" \
    "v_mfma_f32_16x16x32_bf16 v[148:151], v[128:131], v[40:43], v[148:151]\n" \
    "v_mfma_f32_16x16x32_bf16 v[156:159], v[128:131], v[48:51], v[156:159]\n" \
    "v_mfma_f32_16x16x32_bf16 v[164:167], v[128:131], v[56:59], v[164:167]\n" \
    "s_waitcnt lgkmcnt(0)\n" \
    "v_mfma_f32_16x16x32_bf16 v[140:143], v[132:135], v[36:39], v[140:143]\n" \
    "v_mfma_f32_16x16x32_bf16 v[148:151], v[132:135], v[44:47], v[148:151]\n" \
    "v_mfma_f32_16x16x32_bf16 v[156:159], v[132:135], v[52:55], v[156:159]\n" \
    "v_mfma_f32_16x16x32_bf16 v[164:167], v[132:135], v[60:63], v[164:167]\n" \
    "v_cvt_pk_bf16_f32 v65, v136, v137\n" \
    "v_cvt_pk_bf16_f32 v68, v138, v139\n" \
    "v_cvt_pk_bf16_f32 v73, v144, v145\n" \
    "v_cvt_pk_bf16_f32 v76, v146, v147\n" \
    "v_cvt_pk_bf16_f32 v81, v152, v153\n" \
    "v_cvt_pk_bf16_f32 v84, v154, v155\n" \
    "v_cvt_pk_bf16_f32 v89, v160, v161\n" \
    "v_cvt_pk_bf16_f32 v92, v162, v163\n" \
    "v_cvt_pk_bf16_f32 v66, v142, v143\n" \
    "v_cvt_pk_bf16_f32 v67, v142, v143\n" \
    "v_cvt_pk_bf16_f32 v69, v140, v141\n" \
    "v_cvt_pk_bf16_f32 v74, v150, v151\n" \
    "v_cvt_pk_bf16_f32 v75, v150, v151\n" \
    "v_cvt_pk_bf16_f32 v77, v148, v149\n" \
    "v_cvt_pk_bf16_f32 v82, v158, v159\n" \
    "v_cvt_pk_bf16_f32 v83, v158, v159\n" \
    "v_cvt_pk_bf16_f32 v85, v156, v157\n" \
    "v_cvt_pk_bf16_f32 v90, v166, v167\n" \
    "v_cvt_pk_bf16_f32 v91, v166, v167\n" \
    "v_cvt_pk_bf16_f32 v93, v164, v165\n" \
    "v_permlane16_swap_b32 v66, v67\n" \
    "v_permlane16_swap_b32 v74, v75\n" \
    "v_permlane16_swap_b32 v82, v83\n" \
    "v_permlane16_swap_b32 v90, v91\n" \
    "v_mov_b64 v[176:177], 0\n" \
    "v_mov_b64 v[178:179], 0\n" \
    "v_mov_b64 v[180:181], 0\n" \
    "v_mov_b64 v[182:183], 0\n" \
    "v_mov_b64 v[184:185], 0\n" \
    "v_mov_b64 v[186:187], 0\n" \
    "v_mov_b64 v[188:189], 0\n" \
    "v_mov_b64 v[190:191], 0\n" \
    "v_mov_b64 v[196:197], 0\n" \
    "v_mov_b64 v[198:199], 0\n" \
    "v_mov_b64 v[200:201], 0\n" \
    "v_mov_b64 v[202:203], 0\n" \
    "v_mov_b64 v[204:205], 0\n" \
    "v_mov_b64 v[206:207], 0\n" \
    "v_mov_b64 v[208:209], 0\n" \
    "v_mov_b64 v[210:211], 0\n" \
    "v_mov_b64 v[212:213], 0\n" \
    "v_mov_b64 v[214:215], 0\n" \
    "s_add_u32 s85, s60, 0x1000\n" \
    "s_add_u32 s82, s76, 1\n" \
    "s_min_u32 s82, s82, 63\n" \
    "s_lshl_b32 s83, s82, 18\n" \
    "s_add_u32 s83, s83, s72\n" \
    "v_cmp_ge_u32 vcc, s82, v237\n" \
    "s_cbranch_vccnz L%=_Pfslow\n" \
    "L%=_Pfok:\n" \
    "s_add_u32 s79, s82, 3\n" \
    "v_mov_b32 v150, 0x40000000\n" \
    "v_cmp_lt_u32 vcc, s79, v237\n" \
    "s_nop 1\n" \
    "v_cndmask_b32 v150, v235, v150, vcc\n" \
    "s_branch L%=_Pu2b\n" \
    "L%=_Pu2a:\n" \
    "s_sleep 4\n" \
    "L%=_Pu2b:\n" \
    "s_memrealtime vcc\n" \
    "s_add_u32 s80, s77, s64\n" \
    "s_add_u32 s80, s80, s69\n" \
    "s_waitcnt lgkmcnt(0)\n" \
    "s_sub_i32 s79, s80, vcc_lo\n" \
    "s_sub_u32 s79, s79, 40\n" \
    "s_cmp_lt_u32 s79, 0x400\n" \
    "s_cbranch_scc1 L%=_Pu2a\n" \
    "s_memtime s[38:39]\n" \
    "s_memrealtime vcc\n" \
    "s_nop 4\n" \
    "s_memrealtime s[32:33]\n" \
    "s_nop 4\n" \
    "s_memrealtime s[34:35]\n" \
    "s_nop 4\n" \
    "s_memrealtime s[36:37]\n" \
    "s_add_u32 s80, s77, s64\n" \
    "s_lshl_b32 s80, s80, 2\n" \
    "s_waitcnt lgkmcnt(0)\n" \
    "s_sub_u32 s80, s80, vcc_lo\n" \
    "s_sub_u32 s80, s80, s32\n" \
    "s_sub_u32 s80, s80, s34\n" \
    "s_sub_u32 s80, s80, s36\n" \
    "s_mul_i32 s80, s80, s71\n" \
    "s_ashr_i32 s80, s80, 6\n" \
    "s_add_u32 s78, s80, s38\n" \
    "L%=_Pu3a:\n" \
    "s_memtime s[38:39]\n" \
    "s_mul_i32 s80, s69, 23\n" \
    "s_add_u32 s80, s80, s78\n" \
    "s_waitcnt lgkmcnt(0)\n" \
    "s_sub_i32 s79, s80, s38\n" \
    "s_max_i32 s79, s79, 0\n" \
    "s_cmp_gt_u32 s79, 320\n" \
    "s_cbranch_scc1 L%=_Pu3s\n" \
    ".p2align 4\n" \
    "L%=_Pu3d:\n" \
    "s_sub_u32 s79, s79, 28\n" \
    "s_cbranch_scc0 L%=_Pu3d\n" \
    "L%=_Pu3e:\n" \
    "buffer_load_dwordx4 v[104:107], v230, s[56:59], s83 offen offset:0 sc1\n" \
    "buffer_load_dwordx4 v[108:111], v230, s[56:59], s83 offen offset:1024 sc1\n" \
    "buffer_load_dwordx4 v[112:115], v230, s[56:59], s83 offen offset:2048 sc1\n" \
    "buffer_load_dwordx4 v[116:119], v230, s[56:59], s83 offen offset:3072 sc1\n" \
    "s_add_u32 s79, s83, 0x1000\n" \
    "buffer_load_dwordx4 v[120:123], v230, s[56:59], s79 offen offset:0 sc1\n" \
    "buffer_load_dwordx4 v[124:127], v230, s[56:59], s79 offen offset:1024 sc1\n" \
    "buffer_load_dwordx4 v[128:131], v230, s[56:59], s79 offen offset:2048 sc1\n" \
    "buffer_load_dwordx4 v[132:135], v230, s[56:59], s79 offen offset:3072 sc1\n" \
    "s_add_u32 s79, s83, s75\n" \
    "buffer_load_dwordx4 v[136:139], v230, s[52:55], s79 offen offset:0 sc1\n" \
    "buffer_load_dwordx4 v[140:143], v230, s[52:55], s79 offen offset:1024 sc1\n" \
    "buffer_load_dword v151, v150, s[44:47], 0 offen sc1\n" \
    "s_lshl_b32 s79, s76, 7\n" \
    "buffer_load_dwordx4 v[144:147], off, s[44:47], s79 offset:2176 sc1\n" \
    "buffer_store_dword v243, v239, s[44:47], s79 offen sc1\n" \
    "buffer_store_dwordx4 v[244:247], v232, s[40:43], s62 offen offset:0 sc1\n" \
    "buffer_store_dwordx4 v[244:247], v232, s[40:43], s62 offen offset:1024 sc1\n" \
    "buffer_store_dwordx4 v[244:247], v232, s[40:43], s62 offen offset:2048 sc1\n" \
    "buffer_store_dwordx4 v[244:247], v232, s[40:43], s62 offen offset:3072 sc1\n" \
    "buffer_store_dwordx4 v[244:247], v233, s[40:43], s62 offen offset:0 sc1\n" \
    "buffer_store_dwordx4 v[244:247], v233, s[40:43], s62 offen offset:1024 sc1\n" \
    "buffer_store_dwordx4 v[244:247], v234, s[40:43], s62 offen sc1\n" \
    "s_barrier\n" \
    "s_cmp_eq_u32 s76, 63\n" \
    "s_cbranch_scc1 L%=_Pfin\n" \
    "ds_read_b32 v216, v229\n" \
    "s_waitcnt lgkmcnt(0)\n" \
    "v_readfirstlane_b32 s94, v216\n" \
    "s_mul_i32 s79, s94, s70\n" \
    "s_add_u32 s79, s79, s81\n" \
    "s_cmp_lg_u32 s79, s77\n" \
    "s_cbranch_scc1 L%=_Pu4s\n" \
    "L%=_Pu4k:\n" \
    "s_sub_u32 s32, s78, s97\n" \
    "s_sub_u32 s33, s77, s98\n" \
    "s_mov_b32 s97, s78\n" \
    "s_mov_b32 s98, s77\n" \
    "s_lshl_b32 s32, s32, 4\n" \
    "s_cmp_eq_u32 s76, 0\n" \
    "s_cbranch_scc1 L%=_Pck\n" \
    "v_cvt_f32_u32 v148, s33\n" \
    "v_cvt_f32_u32 v149, s32\n" \
    "v_rcp_f32 v148, v148\n" \
    "s_cmp_lt_u32 s76, 4\n" \
    "s_cselect_b32 s34, 0, 0x3c449ba6\n" \
    "s_cmp_eq_u32 s94, s74\n" \
    "s_mov_b32 s74, s94\n" \
    "s_cselect_b32 s32, 0x7f000000, 0\n" \
    "s_cselect_b32 s33, 0.5, 1.0\n" \
    "s_cselect_b32 s34, 0, s34\n" \
    "v_fma_f32 v148, v148, v149, -v240\n" \
    "v_max_f32 v149, 0.5, v241\n" \
    "v_min_f32 v148, s32, v148\n" \
    "v_cmp_gt_f32 vcc, 0, v148\n" \
    "v_mul_f32 v242, 0x3f7851ec, v242\n" \
    "v_add_f32 v242, s34, v242\n" \
    "v_cndmask_b32 v149, v241, v149, vcc\n" \
    "v_fma_f32 v240, v148, v149, v240\n" \
    "v_mul_f32 v241, s33, v241\n" \
    "v_max_f32 v240, 0x43400000, v240\n" \
    "v_max_f32 v241, 0x3e000000, v241\n" \
    "v_min_f32 v240, 0x43e00000, v240\n" \
    "v_fma_f32 v148, v242, s73, s73\n" \
    "v_rcp_f32 v149, v240\n" \
    "s_nop 0\n" \
    "v_fma_f32 v148, v148, v149, 0.5\n" \
    "v_cvt_u32_f32 v243, v148\n" \
    "L%=_Pck:\n" \
    "v_add_f32 v149, 0.5, v240\n" \
    "v_mov_b32 v148, s65\n" \
    "v_cvt_u32_f32 v149, v149\n" \
    "s_waitcnt vmcnt(8)\n" \
    "ds_write_b128 v231, v[104:107] offset:16384\n" \
    "ds_write_b128 v231, v[108:111] offset:17536\n" \
    "ds_write_b128 v231, v[112:115] offset:18688\n" \
    "ds_write_b128 v231, v[116:119] offset:19840\n" \
    "ds_write_b128 v231, v[120:123] offset:20992\n" \
    "ds_write_b128 v231, v[124:127] offset:22144\n" \
    "ds_write_b128 v231, v[128:131] offset:23296\n" \
    "ds_write_b128 v231, v[132:135] offset:24448\n" \
    "ds_write_b128 v230, v[136:139] offset:25600\n" \
    "ds_write_b128 v230, v[140:143] offset:26624\n" \
    "v_max3_u32 v144, v144, v145, v146\n" \
    "v_max_u32 v144, v144, v147\n" \
    "v_cmp_eq_u32 vcc, 0, v144\n" \
    "s_nop 1\n" \
    "v_cndmask_b32 v148, v144, v148, vcc\n" \
    "ds_write_b64 v229, v[148:149] offset:64\n" \
    "v_max_u32 v237, v237, v151\n" \
    "s_setprio 3\n" \
    "L%=_Pu5a:\n" \
    "s_memtime s[38:39]\n" \
    "s_mul_hi_u32 s80, s67, s96\n" \
    "s_add_u32 s80, s80, s78\n" \
    "s_waitcnt lgkmcnt(0)\n" \
    "s_sub_i32 s79, s80, s38\n" \
    "s_max_i32 s79, s79, 0\n" \
    "s_cmp_gt_u32 s79, 320\n" \
    "s_cbranch_scc1 L%=_Pu5s\n" \
    ".p2align 4\n" \
    "L%=_Pu5d:\n" \
    "s_sub_u32 s79, s79, 28\n" \
    "s_cbranch_scc0 L%=_Pu5d\n" \
    "L%=_Pu5e:\n" \
    "buffer_load_dwordx4 v[168:171], v225, s[40:43], s60 offen sc1\n" \
    "buffer_load_dwordx4 v[172:175], v225, s[40:43], s85 offen sc1\n" \
    "L%=_Pvc:\n" \
    "s_waitcnt vmcnt(1)\n" \
    "v_smfmac_f32_16x16x64_bf16 v[176:179], v[168:171], v[0:7], v224\n" \
    "v_smfmac_f32_16x16x64_bf16 v[180:183], v[168:171], v[8:15], v224\n" \
    "s_waitcnt vmcnt(0)\n" \
    "v_smfmac_f32_16x16x64_bf16 v[176:179], v[172:175], v[32:39], v224\n" \
    "v_smfmac_f32_16x16x64_bf16 v[180:183], v[172:175], v[40:47], v224\n" \
    "v_smfmac_f32_16x16x64_bf16 v[184:187], v[168:171], v[16:23], v224\n" \
    "v_smfmac_f32_16x16x64_bf16 v[188:191], v[168:171], v[24:31], v224\n" \
    "v_add_f32 v176, v176, v178\n" \
    "v_smfmac_f32_16x16x64_bf16 v[184:187], v[172:175], v[48:55], v224\n" \
    "v_add_f32 v177, v177, v179\n" \
    "v_smfmac_f32_16x16x64_bf16 v[188:191], v[172:175], v[56:63], v224\n" \
    "v_cmp_u_f32 vcc, v176, v177\n" \
    "v_cvt_pk_bf16_f32 v64, v176, v177\n" \
    "v_add_f32 v180, v180, v182\n" \
    "v_add_f32 v181, v181, v183\n" \
    "v_cvt_pk_bf16_f32 v72, v180, v181\n" \
    "s_cbranch_vccnz L%=_Pvslow\n" \
    "L%=_Pvok:\n" \
    "v_add_f32 v184, v184, v186\n" \
    "v_add_f32 v185, v185, v187\n" \
    "v_cvt_pk_bf16_f32 v80, v184, v185\n" \
    "v_add_f32 v188, v188, v190\n" \
    "v_add_f32 v189, v189, v191\n" \
    "v_cvt_pk_bf16_f32 v88, v188, v189\n" \
    "s_barrier\n" \
    "L%=_Pfr:\n" \
    "ds_read_b128 v[192:195], v227\n" \
    "s_waitcnt lgkmcnt(0)\n" \
    "v_smfmac_f32_16x16x64_bf16 v[212:215], v[192:195], v[96:103], v224\n" \
    "v_cmp_ne_u32 vcc, s76, v195\n" \
    "v_smfmac_f32_16x16x64_bf16 v[196:199], v[192:195], v[64:71], v224\n" \
    "s_cbranch_vccnz L%=_Pfs\n" \
    "v_smfmac_f32_16x16x64_bf16 v[200:203], v[192:195], v[72:79], v224\n" \
    "v_smfmac_f32_16x16x64_bf16 v[204:207], v[192:195], v[80:87], v224\n" \
    "v_add_f32 v212, v212, v214\n" \
    "v_smfmac_f32_16x16x64_bf16 v[208:211], v[192:195], v[88:95], v224\n" \
    "v_add_f32 v213, v213, v215\n" \
    "v_rcp_f32 v220, v212\n" \
    "v_rcp_f32 v221, v213\n" \
    "v_add_f32 v196, v196, v198\n" \
    "v_add_f32 v197, v197, v199\n" \
    "v_pk_add_f32 v[200:201], v[200:201], v[202:203]\n" \
    "v_pk_add_f32 v[204:205], v[204:205], v[206:207]\n" \
    "s_nop 0\n" \
    "v_pk_add_f32 v[208:209], v[208:209], v[210:211]\n" \
    "v_pk_mul_f32 v[196:197], v[196:197], v[220:221]\n" \
    "v_pk_mul_f32 v[200:201], v[200:201], v[220:221]\n" \
    "v_pk_mul_f32 v[204:205], v[204:205], v[220:221]\n" \
    "v_pk_mul_f32 v[208:209], v[208:209], v[220:221]\n" \
    "v_cvt_pk_bf16_f32 v216, v196, v200\n" \
    "v_cvt_pk_bf16_f32 v217, v204, v208\n" \
    "v_cvt_pk_bf16_f32 v218, v197, v201\n" \
    "v_cvt_pk_bf16_f32 v219, v205, v209\n" \
    "buffer_store_dwordx4 v[216:219], v226, s[40:43], s60 offen sc1\n" \
    "s_setprio 0\n" \
    "ds_read_b64 v[220:221], v229 offset:64\n" \
    "s_mov_b32 s79, s60\n" \
    "s_mov_b32 s60, s61\n" \
    "s_mov_b32 s61, s62\n" \
    "s_mov_b32 s62, s63\n" \
    "s_mov_b32 s63, s79\n" \
    "s_add_u32 s76, s76, 1\n" \
    "s_add_u32 s77, s77, s65\n" \
    "s_add_u32 s84, s84, 0x20000\n" \
    "s_add_u32 s81, s81, s65\n" \
    "s_waitcnt lgkmcnt(0)\n" \
    "v_readfirstlane_b32 s65, v220\n" \
    "v_readfirstlane_b32 s71, v221\n" \
    "s_mul_i32 s96, s65, s71\n" \
    "s_lshr_b32 s96, s96, 4\n" \
    "s_branch L%=_Ploop\n" \
    "L%=_Pfslow:\n" \
    "s_sleep 4\n" \
    "buffer_load_dword v237, v235, s[44:47], 0 offen sc1\n" \
    "s_waitcnt vmcnt(0)\n" \
    "v_cmp_ge_u32 vcc, s82, v237\n" \
    "s_cbranch_vccz L%=_Pfok\n" \
    "s_cmp_eq_u32 s99, 0\n" \
    "s_cbranch_scc1 L%=_Pfok\n" \
    "s_sub_u32 s99, s99, 1\n" \
    "s_branch L%=_Pfslow\n" \
    "L%=_Pfin:\n" \
    "s_waitcnt vmcnt(0)\n" \
    "s_barrier\n" \
    "s_branch L%=_end\n" \
    "L%=_Pu3s:\n" \
    "s_cmp_gt_u32 s79, 0x8000\n" \
    "s_cbranch_scc1 L%=_Pu3e\n" \
    "s_sleep 2\n" \
    "s_branch L%=_Pu3a\n" \
    "L%=_Pu4s:\n" \
    "s_mov_b32 s77, s79\n" \
    "s_memtime s[38:39]\n" \
    "s_memrealtime vcc\n" \
    "s_nop 4\n" \
    "s_memrealtime s[32:33]\n" \
    "s_nop 4\n" \
    "s_memrealtime s[34:35]\n" \
    "s_nop 4\n" \
    "s_memrealtime s[36:37]\n" \
    "s_add_u32 s80, s77, s64\n" \
    "s_lshl_b32 s80, s80, 2\n" \
    "s_waitcnt lgkmcnt(0)\n" \
    "s_sub_u32 s80, s80, vcc_lo\n" \
    "s_sub_u32 s80, s80, s32\n" \
    "s_sub_u32 s80, s80, s34\n" \
    "s_sub_u32 s80, s80, s36\n" \
    "s_mul_i32 s80, s80, s71\n" \
    "s_ashr_i32 s80, s80, 6\n" \
    "s_add_u32 s78, s80, s38\n" \
    "s_branch L%=_Pu4k\n" \
    "L%=_Pu5s:\n" \
    "s_cmp_gt_u32 s79, 0x8000\n" \
    "s_cbranch_scc1 L%=_Pu5e\n" \
    "s_sleep 2\n" \
    "s_branch L%=_Pu5a\n" \
    "L%=_Pvslow:\n" \
    "s_nop 7\n" \
    "s_nop 0\n" \
    "v_max_u32 v216, v168, v169\n" \
    "v_max3_u32 v216, v216, v170, v171\n" \
    "v_max3_u32 v216, v216, v172, v173\n" \
    "v_max3_u32 v216, v216, v174, v175\n" \
    "v_cmp_eq_u32 vcc, -1, v216\n" \
    "s_cbranch_vccz L%=_Pvok\n" \
    "s_cmp_eq_u32 s99, 0\n" \
    "s_cbranch_scc1 L%=_Pvok\n" \
    "s_sub_u32 s99, s99, 1\n" \
    "s_cmp_lg_u32 s99, 0\n" \
    "s_cbranch_scc1 L%=_Pu6\n" \
    "v_mov_b32 v216, 1\n" \
    "buffer_store_dword v216, off, s[44:47], 0 sc1\n" \
    "L%=_Pu6:\n" \
    "s_sleep 1\n" \
    "buffer_load_dwordx4 v[168:171], v225, s[40:43], s60 offen sc1\n" \
    "buffer_load_dwordx4 v[172:175], v225, s[40:43], s85 offen sc1\n" \
    "v_mov_b64 v[176:177], 0\n" \
    "v_mov_b64 v[178:179], 0\n" \
    "v_mov_b64 v[180:181], 0\n" \
    "v_mov_b64 v[182:183], 0\n" \
    "v_mov_b64 v[184:185], 0\n" \
    "v_mov_b64 v[186:187], 0\n" \
    "v_mov_b64 v[188:189], 0\n" \
    "v_mov_b64 v[190:191], 0\n" \
    "s_waitcnt vmcnt(0)\n" \
    "s_branch L%=_Pvc\n" \
    "L%=_Pfs:\n" \
    "s_nop 7\n" \
    "s_nop 0\n" \
    "v_mov_b64 v[196:197], 0\n" \
    "v_mov_b64 v[198:199], 0\n" \
    "v_mov_b64 v[200:201], 0\n" \
    "v_mov_b64 v[202:203], 0\n" \
    "v_mov_b64 v[204:205], 0\n" \
    "v_mov_b64 v[206:207], 0\n" \
    "v_mov_b64 v[208:209], 0\n" \
    "v_mov_b64 v[210:211], 0\n" \
    "v_mov_b64 v[212:213], 0\n" \
    "v_mov_b64 v[214:215], 0\n" \
    "s_branch L%=_Pfr\n" \
    "L%=_end:\n"
#define V8(b) "v" #b "0", "v" #b "1", "v" #b "2", "v" #b "3", "v" #b "4", "v" #b "5", "v" #b "6", "v" #b "7", "v" #b "8", "v" #b "9"
#define S_IO "+{s[40:55]}"(sA), "+{s[56:71]}"(sB), "+{s[72:75]}"(sC), "+{s78}"(lwo), "={s[92:99]}"(so)
#define FIN2() do {} while (0)
#define S_CLOB "memory", "vcc", "scc", "m0", "s32", "s33", "s34", "s35", "s36", "s37", "s38", "s39", "s76", "s77", "s79", "s80", "s81", "s82", "s83", "s84", "s85", "s86", "s87", "s88", "s89", "s90", "s91"
#define B_CLOB S_CLOB, "v64", "v65", "v66", "v67", "v68", "v69", V8(7), V8(8), V8(9), V8(10), V8(11), V8(12), V8(13), V8(14), V8(15), V8(16), V8(17), V8(18), V8(19), V8(20), V8(21), \
    "v220", "v221", "v222", "v223", "v240", "v241", "v242", "v243"
#define FIN() do {} while (0)

#define MK_S() \
    a = args(); \
    v16u sA = cat8(cat4(mkrs(a->ring, NSLOT * SLOT), mkrs(a->ctl, CTL_SZ)), cat4(mkrs(a->mb, NSEG * NH * 4096), mkrs(a->qkv + QSZ, QSZ))); \
    v16u sB = cat8(cat4(mkrs(a->qkv + 2 * (size_t)QSZ, QSZ), v4u{0, 0, 0, 0}), \
                   v8u{0, (unsigned)a->per0, (unsigned)a->d1, (unsigned)a->dv, (unsigned)a->d2, (unsigned)a->dp, (unsigned)a->slack, (unsigned)a->thr}); \
    const v4u rsc = __builtin_shufflevector(sA, sA, 4, 5, 6, 7); \
    unsigned b0, b1
#define HALVES() do { const unsigned u_ = beacon(rsc, b0, b1, 16); \
_Pragma("unroll") \
    for (int k = 0; k < NSLOT; ++k) sB[4 + k] = k * (unsigned)SLOT + ((u_ >> k) & 1) * 8192u; } while (0)

#define START() __hip_atomic_fetch_max((LAS unsigned*)(lds + L_MISC), (rt32() - spare) / sB[14] + 1, __ATOMIC_RELAXED, __HIP_MEMORY_SCOPE_WORKGROUP)

DEV void chain(LAS char* lds, const int c) {
    KArgs a = args();
    const int w = __builtin_amdgcn_readfirstlane(threadIdx.x >> 6), h = c >> 2, j = c & 3;
    const unsigned lb = (unsigned)(size_t)lds, lbw = lb + L_STG + w * 16384;
#define LANE() int l = threadIdx.x & 63; asm volatile("" : "+v"(l)); const int l15 = l & 15, g = l >> 4, hb = (l >> 1) & 1, q4 = l15 >> 2; const unsigned idx = hb ? 0xEEEE : 0x4444
    v4u sC, stv; v2u stw; unsigned stx; v8u so;
    unsigned lwo = w & 3;
    if (w < 4) {
        LANE();
        v16u wt[12], m;
            const v4u rw[3] = {mkrs(a->Wk, DM * HD * 2), mkrs(a->Wv, DM * HD * 2), mkrs(a->Wq, DM * HD * 2)};
            const unsigned vo = (l >> 2) * (HD * 2) + (l & 3) * 16, sw = (256 * w * HD + h * DH + 32 * j) * 2;
            auto issue = [&](int p) {
#pragma unroll
                for (int i = 0; i < 8; ++i) DMA16N(rw[p >> 1], vo, sw + ((p & 1) * 128 + i * 16) * (HD * 2), lbw + (p & 1) * 8192 + i * 1024); };
            {
                const v4u rp = w & 1 ? rw[2] : rw[1];
                const unsigned ps = ((rp[0] >> 7) + (c >> 5) * 16) & 63, po = w < 2 ? ((3 - ps) & 7) * 128 + (l & 1) * 1024 : (63 - ps < 16 && !(l & 1) ? (63 - ps) * 128 : OOB);
                if (a->pf) asm volatile("s_mov_b32 m0, %0\n\ts_nop 0\n\tbuffer_load_dword %1, %2, 0 offen lds" :: "s"(lb + L_PF + w * 256), "v"(((c & 31) * 32 + (l >> 1)) * (HD * 2) + (c >> 5) * 2048 + po), "s"(rp) : "memory", "m0");
            }
            unsigned mr[16];
#pragma unroll
            for (int k = 0; k < 16; ++k) mr[k] = *(const unsigned*)(a->mem0 + (2 * q4 + (l & 1)) * DM + 256 * w + 16 * k + 4 * g + 2 * hb);
            SCHED();
            issue(0); issue(1);
            RBAR();
            MK_S();
            unsigned spare = a->spare; asm volatile("" : "+s"(sA), "+s"(sB), "+s"(spare));
            BC_LD(b0, rsc);
            const LAS char* pw = lds + L_STG + w * 16384 + (4 * (g >> 1) + (g & 1) + 2 * (q4 >> 1) + 16 * (q4 & 1)) * 64 + (l & 3) * 8;
#pragma unroll
            for (int p = 0; p < 6; ++p) {
                if (p < 5) WAITV(8); else WAITV(0);
#pragma unroll
                for (int k = 0; k < 2; ++k) wt[(2 * p + k + 4) % 12] = cat8(rd_w<2048, 512, 2560>(pw + (p & 1) * 8192 + k * 4096), rd_w<2048, 512, 2560>(pw + (p & 1) * 8192 + k * 4096 + 32));
                WAITL(0);
                if (p < 4) issue(p + 2);
                if (p == 3) BC_LD(b1, rsc);
            }
#pragma unroll
            for (int k = 0; k < 16; k += 4) {
                m[k] = __builtin_amdgcn_perm(mr[k + 1], mr[k], 0x05040100u); m[k + 1] = __builtin_amdgcn_perm(mr[k + 3], mr[k + 2], 0x05040100u);
                m[k + 2] = __builtin_amdgcn_perm(mr[k + 1], mr[k], 0x07060302u); m[k + 3] = __builtin_amdgcn_perm(mr[k + 3], mr[k + 2], 0x07060302u);
            }
        const unsigned pq = nearo(H3_OFF + (c >> 5) * 16384) + w * 1024 + l * 16;
        const v8u va = {idx, pq, 0, 0, lb + L_MISC + w * 4, lb + L_MISC, 0, pq + 16384};
        sC = v4u{lb + w * 2176u, lb + w * 1280u, 0, 0};
        HALVES();
        START();
        asm volatile(ASM_A : S_IO, "+{v[192:207]}"(m)
                     : "{v[0:31]}"(cat16(wt[0], wt[1])), "{v[32:63]}"(cat16(wt[2], wt[3])), "{v[64:95]}"(cat16(wt[4], wt[5])), "{v[96:127]}"(cat16(wt[6], wt[7])),
                       "{v[128:159]}"(cat16(wt[8], wt[9])), "{v[160:191]}"(cat16(wt[10], wt[11])), "{v[232:239]}"(va)
                     : S_CLOB, "v208", "v209", V8(21), V8(22), "v230", "v231", "v240", "v241", "v244", "v245", "v246", "v247");
        FIN();
    } else {
        LANE();
        const int bw = w - 4, l7 = l & 7;
        MK_S();
        if (w == 6 && l < 16) ((LAS unsigned*)(lds + L_MISC))[l] = l ? 0 : rt32() / sB[14];
        BAR();
        BC_LD(b0, rsc);
        {
            const v4u rw = mkrs(a->Wo, HD * DM * 2);
            const unsigned vo = (l >> 3) * (DM * 2) + l7 * 16, sw = (h * DH * DM + 256 * j + 64 * bw) * 2;
#pragma unroll
            for (int i = 0; i < 16; ++i) DMA16N(rw, vo, sw + i * 8 * (DM * 2), lbw + i * 1024);
        }
        BC_LD(b1, rsc);
        WAITV(0);
        HALVES();
        const LAS char* pw = lds + L_STG + w * 16384 + (16 * (g & 1) + 8 * (g >> 1) + (q4 & 1) + 4 * (q4 >> 1)) * 128 + (l & 3) * 8;
#define RDO(o) rd_w<256, 4096, 4352>(pw + (o))
        const v32u wo0 = cat16(cat8(RDO(0), RDO(32)), cat8(RDO(64), RDO(96))), wo1 = cat16(cat8(RDO(8192), RDO(8224)), cat8(RDO(8256), RDO(8288)));
        const unsigned c3 = ((c >> 3) & 3) * 4096 + (c >> 5) * 1024 + (c & 7) * 128, h1 = nearo(H1_OFF + h * 8192), h1j = h1 + j * 2048;
        v16u va = {};
        va[0] = idx; va[1] = h1 + (g >> 1) * 2048 + 1280 + (l & 31) * 16;
        va[2] = nearo(H2_OFF + (32 * j + 8 * bw + q4 * 2 + (g >> 1)) * 4096) + h * 128 + ((g & 1) * 4 + (l & 3)) * 16;
        va[3] = lb + L_P + g * 320 + l15 * 16;
        va[4] = lb + (l & 1) * X_BLK + (l & 2) * X_FQ + (8 * (g >> 1) + (q4 & 1) + 4 * (q4 >> 1)) * 16 + (g & 1) * 8;
        va[5] = lb + L_MISC;
        v4u sel = {};
        if (w == 4) {
            const int tok = l & 7, u = l >> 5, z = (l >> 3) & 3;
            va[6] = h1 + (z == 0 ? 8 + u : (z == 1 ? 2 * u : (z == 2 ? 4 + 2 * u : 2 * u + 1))) * 128 + tok * 16;
            va[7] = h1 + (5 + 2 * u) * 128 + tok * 16 + (z & 1) * 8;
            va[8] = lb + L_P + ((z & 2) + u) * 320 + (4 * (tok >> 1) + 2 * (z & 1) + (tok & 1)) * 16;
            va[9] = l == 0 ? F_PG + c * 128 : OOB;
            *(LAS v4u*)(lds + va[8]) = v4u{POI, POI, POI, POI};
            sC = v4u{__float_as_uint(SCALE * LOG2E), __float_as_uint(SCALE * LOG2E), 0, 0};
        } else if (w == 5) {
            va[6] = lb + L_PQK + (l15 >> 3) * 256 + (l & 1) * 544 + (l7 >> 1) * 64 + g * 16;
            va[7] = (l & 8) ? OOB : h1j + g * 128 + l7 * 16;
            va[8] = ((l & 8) || g < 2) ? OOB : h1j + 1024 + ((g & 1) * 8 + l7) * 16;
            va[9] = (l & 8) ? h * 4096 + j * 512 + (g * 8 + l7) * 16 : OOB;
            va[10] = lb + l * 16;
            sC = v4u{0, 0, 0, 0};
        } else if (w == 6) {
            const int rho = (l >> 1) & 15, tok = 2 * (rho >> 2) + (rho & 1), c4 = 2 * (l >> 5) + ((rho >> 1) & 1);
            va[6] = lb + ((l & 1) * 160 + (tok & 1) * 72 + (tok >> 1) * 16 + c4 * 4) * 4;
            va[7] = (l & 1) ? OOB : h1j + 1280 + (l >> 1) * 16;
            va[8] = lb + L_VT + tok * 64 + (l & 1) * 32 + c4 * 8;
            va[9] = lb + L_VT + q4 * 64 + (g & 1) * 32 + (l & 3) * 8;
            va[10] = l < 32 ? h * 4096 + 2048 + (32 * j + l) * 16 : OOB;
            va[11] = nearo(H2_OFF + c * 4096) + l * 16;
            va[12] = (l & 1) ? OOB : nearo(H3_OFF + (2 * (g >> 1) + (l15 >> 3)) * 16384 + c3) + ((l7 & 6) + (g & 1)) * 16;
#pragma unroll
            for (int i = 0; i < 4; ++i) sel[i] = (l7 == 2 * i ? 0x3f80u : 0u) | (l7 == 2 * i + 1 ? 0x3f800000u : 0u);
            sC = v4u{0, 0, 0, 0};
        } else {
            const rsrc_t rk = mkrsrc(a->qkv + QSZ, QSZ), rv = mkrsrc(a->qkv + 2 * (size_t)QSZ, QSZ);
            wait_flags(mkrsrc(a->ctl, CTL_SZ), fw(l & 1, h) + 12, 1, a->ctl);
            v4u pf[10];
#pragma unroll
            for (int i = 0; i < 8; ++i) pf[i] = LDB(rv, i, h * 8192);
            pf[8] = LDB(rk, 0, h * 8192 + j * 2048); pf[9] = LDB(rk, 1, h * 8192 + j * 2048);
            va[13] = LD4(mkrsrc(a->ctl, CTL_SZ), fw(l & 1, h) + 12, 0);
#pragma unroll
            for (int i = 0; i < 8; ++i) *(LAS v4u*)(lds + L_X + i * X_BLK + g * X_FQ + l15 * 16) = pf[i];
            *(LAS v4u*)(lds + L_K + l * 16) = pf[8]; *(LAS v4u*)(lds + L_K + 1024 + l * 16) = pf[9];
            FENCE(); ((volatile LAS unsigned*)(lds + L_MISC))[2] = 1;
            va[6] = l * 16; va[7] = lb + g * X_FQ + l15 * 16;
            va[8] = nearo(H2_OFF + c * 4096) + l * 16; va[9] = h1j + l * 16;
            va[10] = nearo(H3_OFF + (l >> 3) * 16384 + c3) + l7 * 16;
            va[11] = fw(l & 1, h) + 12;
            va[15] = l == 0 && (c & 31) == 0 ? F_CK + 384 + (c >> 5) * 4 : OOB;
            sC = v4u{h * 8192u, __float_as_uint(a->per), 0, j * 2048u};
        }
        while (((volatile LAS unsigned*)(lds + L_MISC))[2] == 0) __builtin_amdgcn_s_sleep(1);
        asm volatile(ASM_B : S_IO, "+{v[224:239]}"(va), "+{v[244:247]}"(sel) : "{v[0:31]}"(wo0), "{v[32:63]}"(wo1) : B_CLOB);
        FIN();
    }
}

#define RING_LD(K, B) asm volatile("buffer_load_dwordx4 %0, %1, %2, %3 offen offset:%4 sc1" : "+v"(ring[K]) : "v"(l16), "s"(rso), "s"((B) + ((K) >> 3) * 131072 + (((K) >> 2) & 1) * 4096), "n"(((K) & 3) * 1024) : "memory")
#define RING_MM(K, M, C0, C1, C2, C3) asm volatile("s_waitcnt vmcnt(19)\n\tv_mfma_f32_16x16x32_bf16 %0, %5, %4, " C0 "\n\tv_mfma_f32_16x16x32_bf16 %1, %6, %4, " C1 "\n\tv_mfma_f32_16x16x32_bf16 %2, %7, %4, " C2 "\n\tv_mfma_f32_16x16x32_bf16 %3, %8, %4, " C3 \
    : M(c[0]), M(c[1]), M(c[2]), M(c[3]), "+v"(ring[K]) : "v"(wo[(K) & 7][0]), "v"(wo[(K) & 7][1]), "v"(wo[(K) & 7][2]), "v"(wo[(K) & 7][3]) : "memory")
#define RING_1(K, B, O) "buffer_load_dwordx4 %" #K ", %16, %17, %" #B " offen offset:" #O " sc1\n\t"
#define RING_4(K0, K1, K2, K3, B) RING_1(K0, B, 0) RING_1(K1, B, 1024) RING_1(K2, B, 2048) RING_1(K3, B, 3072)
#define RING_ALL(P) P(ring[0]), P(ring[1]), P(ring[2]), P(ring[3]), P(ring[4]), P(ring[5]), P(ring[6]), P(ring[7]), P(ring[8]), P(ring[9]), P(ring[10]), P(ring[11]), P(ring[12]), P(ring[13]), P(ring[14]), P(ring[15])
#define NPROBE 3
#define TSEG 34
static constexpr int RED_B = 65536;

DEV void worker(LAS char* lds, const int wk) {
    KArgs a = args();
    const int tid = threadIdx.x, w = __builtin_amdgcn_readfirstlane(tid >> 6), l = tid & 63, fr = l & 15, fq = l >> 4;
    const int g = wk >> 5, i = wk & 31;
    const rsrc_t rc = mkrsrc(a->ctl, CTL_SZ);
    unsigned* err = a->ctl;
    unsigned* dbg = a->dbg + 32768 + wk * 32;
    WSTAMP(0);
    const v4u rx = mkrs(a->x, S_TOK * DM * 2);
    const int role = i >> 3;
    const int h = 8 * g + (i & 7);
    const unsigned me = F_W + wk * 128;
    v4u rsc = mkrs(a->ctl, CTL_SZ);
    if (role >= 2) {
        const unsigned t0 = rt32();
        const rsrc_t rr = mkrsrc(a->ring, NSLOT * SLOT);
        unsigned b0, ak = 1;
        BC_LD(b0, rsc);
        if (role == 2) {
#pragma unroll
            for (int k = 0; k < 14; ++k) ST16(rr, (h >> 4) * (unsigned)SLOT + ((h & 15) * 14 + k) * 8192 + tid * 16, 0, (v4u{POI, POI, POI, POI}));
        } else if (wk == 127) {
#pragma unroll
            for (int k = 0; k < 8; ++k) ST16(rc, k == 0 && (tid >> 3) == F_BC / 128 ? OOB : k * 8192 + tid * 16, 0, (v4u{0, 0, 0, 0}));
        } else if (i < 28 && w == 0) {
            unsigned t[2] = {~0u, ~0u};
            for (int it = 0; it < NPROBE; ++it) {
#pragma unroll
                for (int p = 0; p < 2; ++p) { const unsigned c0 = cyc32(); ST4(rr, (i & 3) * (unsigned)SLOT + p * 8192 + nearo(PR_OFF) + g * 256 + (l & 31) * 4, 0, c0); WAITV(0); t[p] = min(t[p], cyc32() - c0); }
            }
            ak = 256 + min(max((int)(t[1] - t[0]), -100), 100);
        }
        WAITV(0); RBAR();
        WSTAMP(1);
        if (w == 0) {
            if (wk != 127) beacon(rsc, b0, b0, 0);
            ST4(rc, l ? OOB : (wk == 127 ? F_BC : me), 0, ak);
        }
        RBAR();
        const unsigned d = role == 2 ? a->wq : (wk == 127 ? 0 : a->wx);
        while (rt32() - t0 < d) __builtin_amdgcn_s_sleep(8);
        if (role == 3) prep_rows(a->x, a->gw, a->w_r, rc, h * 64 + w * 8, l);
        if (w == 0 && wk == 127) {
            unsigned bc = 0;
            for (unsigned tries = 0; tries < SPIN_CAP; ++tries) {
                bc += 32; ST4(rc, l ? OOB : F_BC, 0, bc);
                FENCE();
                if (!(bc & 16)) {
                    unsigned v = LD4(rc, F_W + ((l >> 4) * 32 + 16 + fr) * 128, 0);
                    if (__builtin_amdgcn_ballot_w64(v == 0) << 1 == 0) {
                        v += __shfl_xor((int)v, 16); v += __shfl_xor((int)v, 32);
                        bc |= 16 | ((unsigned)(__builtin_amdgcn_ballot_w64(v > 1024) >> 8) & 15);
                        continue;
                    }
                } else if (rt32() - t0 > 2400 && all_ge(LD4(rc, F_PG, 0), 1)) break;
                __builtin_amdgcn_s_sleep(16);
            }
            WSTAMP(5);
        }
    }
    if (role == 1) stream_qkv<true>(lds, rx, mkrs(a->Wv, DM * HD * 2), mkrsrc(a->qkv + 2 * (size_t)QSZ, QSZ), rc, rsc, true, me + 12, h, dbg);
    else if (role < 3) stream_qkv<false>(lds, rx, mkrs(role ? a->Wq : a->Wk, DM * HD * 2), mkrsrc(a->qkv + (role ? 0 : QSZ), QSZ), rc, rsc, role == 0, me + 12, h, dbg);
    else if (h < NMOE) {
        const int sel = top8((LAS unsigned*)(lds + L_TOT - 256), rc, err, w, l);
        WSTAMP(26);
        if (h == 0 && tid < TOPC) ST4(mkrsrc(a->idx, 32), tid * 4, 0, sel);
        moe(lds, rx, sel, mkrs(a->wg, DM * FF * 2), mkrs(a->wu, DM * FF * 2), mkrs(a->wd, FF * DM * 2), mkrsrc(a->part, NMOE * TOPC * DM * 4), h);
        if (tid == 0) ST4(rc, me + 4, 0, 1u);
    }
    WSTAMP(2);
    a = args();
    const rsrc_t ri = mkrsrc(a->idx, 32), ru = mkrsrc(a->upd, TOPC * DM * 4), rt = mkrsrc(a->wt, HD * DM * 2);
    const bool rcv = role == 3 ? i < 28 : role == 2 || g >= 2;
    const int cg = role == 3 ? 4 * g + (i & 3) : (role == 2 ? h & 15 : i), k0 = (rcv ? 0 : 64) + 8 * w;
    const int seg0 = role == 3 ? 0 : (role == 2 ? 2 + (g >> 1) : (rcv ? g - 2 : TSEG + g)), t1 = role == 3 ? TSEG - 1 : 0, sst = rcv ? 4 : 3;
    auto next = [&](int s) { return s + (s < t1 ? 1 : sst); };
    auto fx = [&](int s, int l) { return fw(3, (rcv ? 0 : 16) + (l & 1) * 8 + (s & 3)) + 64 + (s & 60); };
    const unsigned fe = F_EX + cg * 256, wb = (cg * 128 + k0) * 4096;
    volatile LAS unsigned* prog = (volatile LAS unsigned*)(lds + 2 * RED_B);
    LAS v4f* upd = (LAS v4f*)(lds + 2 * RED_B + 64);
    v4u wo[8][4];
    int sel[8];
    if (role == 3) {
        {
            const v4u rw = mkrs(a->Wo, HD * DM * 2);
            const unsigned vo = (l >> 3) * (DM * 2) + (((l & 7) ^ ((l >> 3) & 6)) << 4), lb = (unsigned)(size_t)lds + w * 16384;
            auto issue = [&](int p) {
#pragma unroll
                for (int k = 0; k < 8; ++k) DMA16N(rw, vo, ((32 * k0 + 64 * p + 8 * k) * DM + 64 * cg) * 2, lb + (p & 1) * 8192 + k * 1024); };
            issue(0); issue(1);
            const int row = 4 * fq + (fr >> 2);
#pragma unroll
            for (int p = 0; p < 4; ++p) {
                if (p < 3) WAITV(8); else WAITV(0);
#pragma unroll
                for (int t = 0; t < 4; ++t) {
                    const LAS char* q = lds + w * 16384 + (p & 1) * 8192 + row * 128 + (((2 * t + ((l >> 1) & 1)) ^ (row & 6)) << 4) + (l & 1) * 8;
                    wo[2 * p][t] = cat(trd(q), trd(q + 2048)); wo[2 * p + 1][t] = cat(trd(q + 4096), trd(q + 6144));
                }
                WAITL(0);
                if (p < 2) issue(p + 2);
            }
#pragma unroll
            for (int k = 0; k < 32; ++k) ST16(rt, wb + l * 16 + k * 1024, 0, wo[k >> 2][k & 3]);
        }
        WSTAMP(7);
        {
            wait_flags<16>(rc, fw(3, min(l, NMOE - 1)) + 4, 1, err);
            const rsrc_t rp = mkrsrc(a->part, NMOE * TOPC * DM * 4);
            const unsigned t = LD4(ri, (l >> 3) * 4, 0), o = (l >> 3) * 4096 + h * 128 + (l & 7) * 16;
            v4f p = {0, 0, 0, 0};
#pragma unroll
            for (int q = 0; q < 4; ++q) p += __builtin_bit_cast(v4f, LD16(rp, (w * 4 + q) * 32768 + o, 0));
            const float r = rbf(sigmoidf_(hi16(LD4(rc, F_GR + t * 8 + 4, 0))));
            upd[tid] = p;
            WAITV(0); BAR();
            if (w == 0) {
#pragma unroll
                for (int q = 1; q < 7; ++q) p += upd[q * 64 + l];
                ST16(ru, o, 0, __builtin_bit_cast(v4u, (v4f{rbf(rbf(p[0]) * r), rbf(rbf(p[1]) * r), rbf(rbf(p[2]) * r), rbf(rbf(p[3]) * r)})));
                WAITV(0);
                if (l == 0) ST4(rc, me + 8, 0, 1u);
                prog[1] = 0;
            }
        }
        if (rcv) {
            WSTAMP(8);
            const rsrc_t rq = mkrsrc(a->qkv, QSZ), rk = mkrsrc(a->qkv + QSZ, QSZ), rv = mkrsrc(a->qkv + 2 * (size_t)QSZ, QSZ), rm = mkrsrc(a->mb, NSEG * NH * 4096), ro = mkrsrc(a->ob, QSZ);
            unsigned fl = 0;
#pragma unroll 1
            for (int xs = i & 3; xs < NSEG; xs += 4) { fl = xattn(rq, rk, rv, rm, ro, rc, err, xs, 8 * g + w, l, fl, dbg); WSTAMP(9 + (xs >> 2)); }
            WSTAMP(25);
            return;
        }
#pragma unroll
        for (int q = 0; q < 8; ++q) sel[q] = 0;
    } else {
        wait_flags(rc, fw(3, l < 2 ? 2 * cg + l : 2 * (cg & 12) + (cg & 3) + (rcv ? 0 : 4)) + 8, 1, err);
        const unsigned t = LD4(ri, (l & 7) * 4, 0), n1 = LD4(rc, l < 2 ? fx(next(seg0), l) : fe + seg0 * 4, 0);
        const v4u u = LD16(ru, w < 2 ? (tid >> 4) * 4096 + cg * 256 + fr * 16 : OOB, 0);
#pragma unroll
        for (int k = 0; k < 32; ++k) wo[k >> 2][k & 3] = LDBX(rt, k, wb, NTS & 8);
        if (w < 2) upd[tid] = __builtin_bit_cast(v4f, u);
#pragma unroll
        for (int q = 0; q < 8; ++q) sel[q] = __builtin_amdgcn_readlane(t, q);
        if (w == 0) prog[1] = (__builtin_amdgcn_readlane(n1, 0) & __builtin_amdgcn_readlane(n1, 1)) | __builtin_amdgcn_readlane(n1, 2) << 1;
        WAITV(0);
        WSTAMP(7);
    }
    unsigned pre = 0;
    v4u ring[16];
    asm volatile("" : RING_ALL("=v"));
    const v4u rso = mkrs(a->ob, QSZ), rse = mkrs(a->ex, S_TOK * DM * 4);
    rsc = mkrs(a->ctl, CTL_SZ);
    const rsrc_t re = mkrsrc(a->ex, S_TOK * DM * 4), rout = mkrsrc(a->out, S_TOK * DM * 2);
    BAR();
    unsigned st = __builtin_amdgcn_readfirstlane(prog[1]);
    WSTAMP(8);
#pragma unroll 1
    for (int n = 0, seg = seg0, prv = OOB / 4; seg < NSEG; ++n, prv = seg, seg = next(seg)) {
        const int s1 = next(seg), s2 = next(s1);
        const unsigned ob = (unsigned)(seg * 256 + k0) * 1024u;
        int l = tid & 63; asm volatile("" : "+v"(l));
        const int fr = l & 15, fq = l >> 4, l16 = l * 16, row = seg * 32 + (w & 1) * 16 + fr, col = (w >> 1) * 16 + 4 * fq;
        if (!pre) wait_flags(rc, fx(seg, l), 1, err);
        asm volatile("s_cmp_lg_u32 %18, 0\n\ts_cbranch_scc1 L%=\n\t" RING_4(0, 1, 2, 3, 19) RING_4(4, 5, 6, 7, 20) RING_4(8, 9, 10, 11, 21) RING_4(12, 13, 14, 15, 22) "L%=:"
                     : RING_ALL("+v") : "v"(l16), "s"(rso), "s"(pre), "s"(ob), "s"(ob + 4096), "s"(ob + 131072), "s"(ob + 135168) : "memory", "scc");
        pre = st & 1;
        const bool got = rcv && (st & 2);
        const unsigned nb = pre ? ob + (s1 - seg) * 262144 : OOB, eo = (unsigned)(seg * 16 + cg) * 8192u + tid * 16, oo = rcv ? (row * DM + 64 * cg + col) * 2 : OOB;
        unsigned sm; v2u xv; float gt; v4f pv;
        v4f c[4];
        asm volatile("buffer_load_dword %[sm], %[so], %[rc], 0 offen sc1\n\tbuffer_load_dwordx4 %[pv], %[eo], %[re], 0 offen sc1\n\tbuffer_load_dwordx2 %[xv], %[xo], %[rx], 0 offen\n\t"
                     "buffer_load_dword %[gt], %[go], %[rc], 0 offen sc1"
                     : [sm] "=&v"(sm), [pv] "=&v"(pv), [xv] "=&v"(xv), [gt] "=&v"(gt)
                     : [rc] "s"(rsc), [re] "s"(rse), [rx] "s"(rx), [so] "v"(w ? OOB : (l < 2 ? (s2 < NSEG ? fx(s2, l) : OOB) : (rcv ? fe + s1 * 4 : OOB))),
                       [eo] "v"(got ? eo : OOB), [xo] "v"(oo), [go] "v"(F_GR + row * 8) : "memory");
        LAS v4f* rb = (LAS v4f*)(lds + (n & 1) * RED_B) + l;
#pragma unroll
        for (int k = 0; k < 16; ++k) {
            if (k & 7) RING_MM(k, "+v", "%0", "%1", "%2", "%3"); else RING_MM(k, "=&v", "0", "0", "0", "0");
            RING_LD(k, nb);
            if ((k & 7) == 7) {
#pragma unroll
                for (int t = 0; t < 4; ++t) { asm volatile("s_nop 3" : "+v"(c[t]) :: "memory"); rb[((2 * t + (k >> 3)) * 8 + w) * 64] = c[t]; }
            }
        }
        asm volatile("s_waitcnt vmcnt(16)" : "+v"(sm), "+v"(pv), "+v"(xv), "+v"(gt) :: "memory");
        const unsigned sn = (__builtin_amdgcn_readlane(sm, 0) & __builtin_amdgcn_readlane(sm, 1)) | __builtin_amdgcn_readlane(sm, 2) << 1;
        if (tid == 0) prog[n & 1] = sn;
        BAR();
        if (!rcv && tid == 0) ST4(rc, fe + prv * 4, 0, 1u);
        v4f acc = rb[w * 512];
#pragma unroll
        for (int q = 1; q < 8; ++q) acc += rb[w * 512 + q * 64];
        st = __builtin_amdgcn_readfirstlane(prog[n & 1]);
        if (rcv) {
            int slot = -1;
#pragma unroll
            for (int q = 0; q < 8; ++q) slot = sel[q] == row ? q : slot;
            if (!got) { wait_flags(rc, fe + seg * 4, 1, err); pv = __builtin_bit_cast(v4f, LD16(re, eo, 0)); asm volatile("" : "+v"(pv)); }
            v4f b = {lo16(xv[0]), hi16(xv[0]), lo16(xv[1]), hi16(xv[1])};
            if (slot >= 0) { b += upd[slot * 16 + (col >> 2)]; b = v4f{rbf(b[0]), rbf(b[1]), rbf(b[2]), rbf(b[3])}; }
            acc += pv;
            gt = hi16(__float_as_uint(gt));
            const float omg = rbf(1.0f - gt);
#pragma unroll
            for (int e = 0; e < 4; ++e) acc[e] = rbf(gt * rbf(acc[e])) + rbf(omg * b[e]);
            __builtin_amdgcn_raw_buffer_store_b64(v2u{pk2(acc[0], acc[1]), pk2(acc[2], acc[3])}, rout, oo, 0, 0);
        } else {
            ST16(re, eo, 0, __builtin_bit_cast(v4u, acc));
            if (!pre) { WAITV(0); RBAR(); if (tid == 0) ST4(rc, fe + seg * 4, 0, 1u); }
        }
        WSTAMP(9 + (seg >> 2));
    }
    asm volatile("s_waitcnt vmcnt(0)" :: RING_ALL("v") : "memory");
    WSTAMP(25);
}

__global__ void __launch_bounds__(512, 2) k_main(Args) {
    __shared__ __attribute__((aligned(16))) char lds[L_TOT];
    const int b = blockIdx.x, c = (b & 3) * 32 + (b >> 3);
    if ((b & 7) < 4) chain((LAS char*)lds, c); else worker((LAS char*)lds, c);
}

#define RING_MB 160
#define TUNE_DEFAULT {206, 78, 80, 156, 8, 80, 352, 100, 700, 700, 1, 0, 0}

extern "C" void kernel_launch(void* const* d_in, const int* in_sizes, int n_in, void* d_out, int out_size, void* d_ws, size_t ws_size, hipStream_t stream) {
    char* ring = (char*)(((size_t)d_ws + SLOT - 1) & ~(size_t)(SLOT - 1));
    char* ws = ring + NSLOT * SLOT; size_t off = 0;
    if (ws_size >= ((size_t)RING_MB + 12 << 20)) ring += (size_t)RING_MB << 20;
    auto alloc = [&](size_t bytes) { void* p = ws + off; off += (bytes + 255) & ~(size_t)255; return p; };
    Args a;
    a.x = (const bf16_t*)d_in[0]; a.mem0 = (const bf16_t*)d_in[1]; a.Wq = (const bf16_t*)d_in[2]; a.Wk = (const bf16_t*)d_in[3]; a.Wv = (const bf16_t*)d_in[4]; a.Wo = (const bf16_t*)d_in[5];
    a.w_r = (const bf16_t*)d_in[8]; a.wg = (const bf16_t*)d_in[9]; a.wu = (const bf16_t*)d_in[10]; a.wd = (const bf16_t*)d_in[11]; a.gw = (const bf16_t*)d_in[12];
    a.qkv = (char*)alloc(3 * (size_t)QSZ); a.mb = (char*)alloc(NSEG * NH * 4096); a.ob = (char*)alloc(QSZ);
    a.idx = (int*)alloc(256); a.part = (float*)alloc(NMOE * TOPC * DM * 4); a.upd = (float*)alloc(TOPC * DM * 4); a.ex = (float*)alloc(S_TOK * DM * 4); a.wt = (char*)alloc(HD * DM * 2); a.out = (bf16_t*)d_out;
    a.ring = ring; a.ctl = (unsigned*)alloc(CTL_SZ); a.dbg = (unsigned*)alloc((65536 + 1024 * 512) * 4);
    const int tn[13] = TUNE_DEFAULT;
    a.per = tn[0] * 368.0f; a.per0 = tn[0] * 368 / tn[6]; a.pf = tn[10]; a.ms = tn[11]; a.ws = tn[12];
    a.d1 = ((u64)tn[1] << 32) / tn[0]; a.dv = ((u64)tn[2] << 32) / tn[0]; a.d2 = ((u64)tn[3] << 32) / tn[0]; a.dp = tn[4]; a.slack = tn[5]; a.thr = tn[6]; a.spare = tn[7]; a.wq = tn[8]; a.wx = tn[9];
    k_main<<<256, 512, 0, stream>>>(a);
}
```

```cpp
#include <hip/hip_runtime.h>
#include <stdint.h>

typedef unsigned short bf16_t;
typedef unsigned v2u __attribute__((ext_vector_type(2)));
typedef unsigned v4u __attribute__((ext_vector_type(4)));
typedef unsigned v8u __attribute__((ext_vector_type(8)));
typedef float v2f __attribute__((ext_vector_type(2)));
typedef float v4f __attribute__((ext_vector_type(4)));
typedef short v4s __attribute__((ext_vector_type(4)));
typedef __bf16 v2b __attribute__((ext_vector_type(2)));
typedef __bf16 v8b __attribute__((ext_vector_type(8)));
typedef __bf16 v16b __attribute__((ext_vector_type(16)));
typedef unsigned long long u64;
typedef __amdgpu_buffer_rsrc_t rsrc_t;
#define LAS __attribute__((address_space(3)))
#define DEV __device__ __forceinline__

static constexpr int S_TOK = 2048, DM = 1024, NH = 32, DH = 128, SEG = 32, NSEG = 64;
static constexpr int HD = NH * DH;
static constexpr int FF = 3584, TOPC = 8;
static constexpr float SCALE = 0.08838834764831845f, LOG2E = 1.4426950408889634f, MASKV = -998244352.0f;

DEV unsigned pk2(float lo, float hi) { v2f f = {lo, hi}; return __builtin_bit_cast(unsigned, __builtin_convertvector(f, v2b)); }
DEV float lo16(unsigned u) { return __uint_as_float(u << 16); }
DEV float hi16(unsigned u) { return __uint_as_float(u & 0xffff0000u); }
DEV float rbf(float f) { return __uint_as_float(pk2(0.0f, f)); }
DEV v4f smf(v4u a, v8u b, v4f c, int idx) { return __builtin_amdgcn_smfmac_f32_16x16x64_bf16(__builtin_bit_cast(v8b, a), __builtin_bit_cast(v16b, b), c, idx, 0, 0); }
DEV v4f mf(v4u a, v4u b, v4f c) { return __builtin_amdgcn_mfma_f32_16x16x32_bf16(__builtin_bit_cast(v8b, a), __builtin_bit_cast(v8b, b), c, 0, 0, 0); }
DEV v2u trd(const LAS char* p) { return __builtin_bit_cast(v2u, __builtin_amdgcn_ds_read_tr16_b64_v4i16((LAS v4s*)p)); }
DEV v4u cat(v2u a, v2u b) { return __builtin_shufflevector(a, b, 0, 1, 2, 3); }
DEV v2u sw16(unsigned x, unsigned y) { return __builtin_amdgcn_permlane16_swap(x, y, false, false); }
DEV v2u sw32(unsigned x, unsigned y) { return __builtin_amdgcn_permlane32_swap(x, y, false, false); }
DEV float half_add(float x, float y) { v2u r = sw32(__float_as_uint(x), __float_as_uint(y)); return __uint_as_float(r[0]) + __uint_as_float(r[1]); }
DEV float ror8(float v) { return __int_as_float(__builtin_amdgcn_update_dpp(0, __float_as_int(v), 0x128, 0xf, 0xf, false)); }
template <bool MAX> DEV float red_g(float v) {
    auto op = [](float a, float b) { return MAX ? fmaxf(a, b) : a + b; };
    v2u r = sw16(__float_as_uint(v), __float_as_uint(v)); v = op(__uint_as_float(r[0]), __uint_as_float(r[1]));
    r = sw32(__float_as_uint(v), __float_as_uint(v)); return op(__uint_as_float(r[0]), __uint_as_float(r[1]));
}
DEV float sigmoidf_(float z) { return 1.0f / (1.0f + __expf(-z)); }
DEV v4u mkrs(const void* p, unsigned bytes) { const u64 a = (u64)p; return v4u{(unsigned)a, (unsigned)(a >> 32) & 0xffffu, bytes, 0x00020000u}; }
DEV rsrc_t mkrsrc(const void* p, unsigned bytes) { return __builtin_amdgcn_make_buffer_rsrc((void*)p, (short)0, (int)bytes, 0x00020000); }

#define ST16(RS, OFF, SOFF, V) __builtin_amdgcn_raw_buffer_store_b128(V, RS, (OFF) + (SOFF), 0, 16)
#define NTS 0
#define ST16X(RS, OFF, V, NT) __builtin_amdgcn_raw_buffer_store_b128(V, RS, OFF, 0, (NT) ? 18 : 16)
#define LD16X(RS, OFF, SOFF, NT) __builtin_amdgcn_raw_buffer_load_b128(RS, OFF, SOFF, (NT) ? 18 : 16)
#define ST4(RS, OFF, SOFF, V) __builtin_amdgcn_raw_buffer_store_b32(V, RS, OFF, SOFF, 16)
#define LD16(RS, OFF, SOFF) __builtin_amdgcn_raw_buffer_load_b128(RS, OFF, SOFF, 16)
#define LD8(RS, OFF, SOFF) __builtin_amdgcn_raw_buffer_load_b64(RS, OFF, SOFF, 16)
#define LD4(RS, OFF, SOFF) __builtin_amdgcn_raw_buffer_load_b32(RS, OFF, SOFF, 16)
#define DMA16(RS, VOFF, SOFF, LOFF) asm volatile("s_mov_b32 m0, %0\n\ts_nop 0\n\tbuffer_load_dwordx4 %1, %2, %3 offen lds" :: "s"(LOFF), "v"(VOFF), "s"(RS), "s"(SOFF) : "memory", "m0")
#define DMA16N(RS, VOFF, SOFF, LOFF) asm volatile("s_mov_b32 m0, %0\n\ts_nop 0\n\tbuffer_load_dwordx4 %1, %2, %3 offen nt lds" :: "s"(LOFF), "v"(VOFF), "s"(RS), "s"(SOFF) : "memory", "m0")
#define WSTAMP(k) do {} while (0)
#define EXP 0
#define XI 3
#define BK 9
#define FENCE() asm volatile("" ::: "memory")
#define WAITV(n) asm volatile("s_waitcnt vmcnt(" #n ")" ::: "memory")
#define WAITL(n) asm volatile("s_waitcnt lgkmcnt(" #n ")" ::: "memory")
#define RBAR() asm volatile("s_barrier" ::: "memory")
#define BAR() asm volatile("s_waitcnt lgkmcnt(0)\n\ts_barrier" ::: "memory")
#define SCHED() __builtin_amdgcn_sched_barrier(0)

#define KVS(m, h) ((((m) & 2 ? (h) : (m)) & 1u) << 13)
#define FAR(u) ((~(u) & 1u) << 13)
DEV unsigned side(unsigned o) { return o ^ ((o >> 8) & 8192u); }
static constexpr int XP = 1056, XS = 32 * XP, L_RED = 4 * XS;

DEV unsigned wt_src(int tid, unsigned ldw) { return (tid >> 4) * ldw + (((tid & 15) ^ ((((tid >> 4) & 3) << 2) | (((tid >> 7) & 1) << 1))) << 4); }
DEV int wt_rd(int w, int l, int n) { const int fr = l & 15, fq = l >> 4; return (w >> 2) * 16384 + (8 * fq + (fr >> 2)) * 256 + (((4 * (w & 3) + 2 * n + ((fr >> 1) & 1)) ^ (((fr >> 2) << 2) | ((fq & 1) << 1))) << 4) + (fr & 1) * 8; }
DEV v4u wt_frag(const LAS char* p) { return cat(trd(p), trd(p + 1024)); }
#define WT_ISSUE(R0, R1, VB, S0, S1, LDW, LB) do { DMA16N(R0, VB, S0, LB); DMA16N(R0, VB, (S0) + 32 * (LDW), (LB) + 8192); DMA16N(R1, VB, S1, (LB) + 16384); DMA16N(R1, VB, (S1) + 32 * (LDW), (LB) + 24576); } while (0)

static constexpr unsigned F_BC = 512, F_CK = 2048, F_EX = 12288, F_W = 16384, F_PG = 32768, F_GR = 49152, CTL_SZ = 65536;
DEV unsigned fw(int role, int h) { return F_W + ((h >> 3) * 32 + role * 8 + (h & 7)) * 128; }
static constexpr unsigned POI = 0xFFFFFFFFu, OOB = 0x40000000u, SPIN_CAP = 200000u;
DEV bool spin_fail(unsigned tries, unsigned* err) {
    __builtin_amdgcn_s_sleep(1);
    if ((tries & 255u) != 255u) return false;
    unsigned e; asm volatile("global_load_dword %0, %1, off sc1\n\ts_waitcnt vmcnt(0)" : "=&v"(e) : "v"(err) : "memory");
    if (__builtin_amdgcn_readfirstlane(e) == 1u) return true;
    if (tries > SPIN_CAP) { unsigned one = 1u; asm volatile("global_store_dword %0, %1, off sc1" :: "v"(err), "v"(one) : "memory"); return true; }
    return false;
}
DEV bool all_ge(unsigned v, unsigned t) { return __builtin_amdgcn_ballot_w64(v < t) == 0; }
template <int Z = 6> DEV void wait_flags(rsrc_t rc, unsigned off, unsigned t, unsigned* err) {
    for (unsigned tries = 0;; ++tries) {
        FENCE();
        if (all_ge(LD4(rc, off, 0), t)) break;
        __builtin_amdgcn_s_sleep(Z);
        if (spin_fail(tries, err)) break;
    }
    FENCE();
}

#define BC_LD(B, RS) asm volatile("buffer_load_dword %0, off, %1, 0 offset:512 sc1" : "=&v"(B) : "s"(RS) : "memory")
DEV unsigned beacon(v4u rs, unsigned b0, unsigned b1, unsigned need, bool seen = false) {
    asm volatile("" : "+v"(b0), "+v"(b1));
    for (unsigned tries = 0;; ++tries) {
        const unsigned u = __builtin_amdgcn_readfirstlane(b1);
        if (((seen || u != __builtin_amdgcn_readfirstlane(b0)) && (u & need) == need) || tries > SPIN_CAP) return u;
        __builtin_amdgcn_s_sleep(4);
        BC_LD(b1, rs); asm volatile("s_waitcnt vmcnt(0)" : "+v"(b1) :: "memory");
    }
}

template <bool VT> DEV unsigned stream_qkv(LAS char* lds, v4u rx, v4u rw, rsrc_t ro, rsrc_t rc, v4u rsc, bool hs, unsigned fo, int h, unsigned fx, unsigned* dbg) {
    unsigned far = 0;
    const int tid = threadIdx.x, w = __builtin_amdgcn_readfirstlane(tid >> 6), l = tid & 63, kh = w >> 2, cg = w & 3;
    const unsigned lb = (unsigned)(size_t)lds, vb = wt_src(tid, HD * 2), l16 = l * 16;
    const unsigned x0 = (4 * cg + (VT ? 2 : 16) * (1 - kh)) * 2048 + kh * 1024, x1 = (4 * cg + (VT ? 2 : 16) * kh) * 2048 + kh * 1024;
#define STW(s) WT_ISSUE(rw, rw, vb, h * 256 + (s) * (64 * HD * 2), h * 256 + ((s) + 8) * (64 * HD * 2), HD * 2, lb + w * 1024 + ((s) & 3) * XS)
#define STX(u, q, e) DMA16(rx, l16, ((u) >> 1) * 65536 + ((q) & 1 ? x1 : x0) + (VT ? ((e) & 1) + 16 * ((e) >> 1) : (e)) * 2048, lb + (w * 4 + (e)) * XP + (q) * XS)
    const unsigned fl = tid == 0 ? fo : OOB;
    v4u W[16][2];
    unsigned b0, b1;
    STW(0); STW(1); STW(2); STW(3);
    BC_LD(b0, rsc);
#pragma unroll
    for (int s = 0; s < 8; ++s) {
        WAITV(12); RBAR();
#pragma unroll
        for (int n = 0; n < 2; ++n) { const LAS char* p = lds + wt_rd(w, l, n) + (s & 3) * XS; W[2 * s][n] = wt_frag(p); W[2 * s + 1][n] = wt_frag(p + 8192); }
        BAR();
        if (s == 4) BC_LD(b1, rsc);
        if (s == 7) { far ^= FAR(beacon(rsc, b0, b1, 16, !hs)); ST4(rc, OOB, 0, 0u); FENCE(); }
        if (s < 4) STW(s + 4); else { STX(s - 4, s - 4, 0); STX(s - 4, s - 4, 1); if (s < 7) { STX(s - 4, s - 4, 2); STX(s - 4, s - 4, 3); } }
    }
    const LAS char* px = lds + (kh * 16 + (l & 15)) * XP + (l >> 4) * 16;
    LAS v4f* ps = (LAS v4f*)(lds + L_RED + w * 2048 + l16);
    const LAS v4f* pr = (const LAS v4f*)(lds + L_RED + (w ^ 4) * 2048 + l16);
    unsigned oo = OOB, on = (unsigned)h * 16384u + (far ^ fx) + cg * 2048u + kh * 1024u + l16;
    v4f c[2][2] = {}, r0 = {}, r1 = {};
    v4u f[8];
    auto fin = [&]() { r0 += c[1][0]; r1 += c[1][1]; ST16X(ro, oo, (v4u{pk2(r0[0], r0[1]), pk2(r0[2], r0[3]), pk2(r1[0], r1[1]), pk2(r1[2], r1[3])}), NTS & 1); oo = side(on); on += NH * 16384u; };
    WSTAMP(3);
    WAITV(10); RBAR();
    WSTAMP(4);
    const LAS char* px2 = px + 2 * XS; asm volatile("" : "+v"(px2));
#define RDX(q, k) f[(k) & 7] = *(const LAS v4u*)(((q) & 2 ? px2 : px) + ((q) & 1) * XS + (k) * 64)
#pragma unroll
    for (int k = 0; k < 8; ++k) RDX(0, k);
#pragma unroll 1
    for (int u0 = 0; u0 < 2 * NSEG; u0 += 4) {
        if (u0 == 4) WSTAMP(5);
        if (u0 == 64) WSTAMP(6);
#pragma unroll
        for (int q = 0; q < 4; ++q) {
#pragma unroll
            for (int k = 0; k < 16; ++k) {
                const v4u x = f[k & 7];
#pragma unroll
                for (int n = 0; n < 2; ++n) { const v4f z = k ? c[q & 1][n] : v4f{0, 0, 0, 0}; c[q & 1][n] = VT ? mf(x, W[k][n], z) : mf(W[k][n], x, z); }
                if (k < 8) RDX(q, k + 8);
                if (k == BK) { SCHED(); WAITV(10); BAR(); for (int e = 0; e < BK - 8; ++e) RDX(q + 1, e); }
                if (k >= BK) RDX(q + 1, k - 8);
                if (((k + 16 - BK) & 3) == 0) asm volatile("s_setprio %0" :: "n"(3 - (((k + 16 - BK) >> 2) & 3)));
                if (k == 2) { if (q & 1) { ps[0] = c[0][0]; ps[64] = c[0][1]; } else fin(); }
                if (k == 13) { if (q & 1) { r0 = pr[0]; r1 = pr[64]; } else ST4(rc, fl, 0, max((u0 + q - 2) >> 1, 0)); }
                if (!(EXP & 1) && !(k & 1) && kh == (((k >> 1) ^ ((BK + 1) >> 1)) & 1)) { if (k > BK) STX(u0 + q + 4, q, (k - BK) >> 2); else STX(u0 + q + 3, (q + 3) & 3, (k + 16 - BK) >> 2); }
                SCHED();
            }
        }
    }
    fin(); __builtin_amdgcn_s_setprio(0);
    WAITV(0); BAR();
    if (tid == 0) ST4(rc, fo, 0, NSEG);
    return far;
#undef STW
#undef STX
}

#define LDBX(RS, K, BASE, NT) LD16X(RS, l * 16 + ((K) & 3) * 1024, (BASE) + ((K) >> 2) * 4096, NT)
#define LDB(RS, K, BASE) LDBX(RS, K, BASE, 0)
DEV unsigned xattn(rsrc_t rq, rsrc_t rk, rsrc_t rv, rsrc_t rm, rsrc_t rc, unsigned* err, int seg, int h, int l, unsigned fl, unsigned far, unsigned ms, unsigned* dbg) {
#define XST(k) do {} while (0)
    XST(3);
    asm volatile("" : "+v"(l));
    const int fr = l & 15, g = l >> 4;
    const unsigned sb = side((unsigned)(seg * NH + h) * 16384u + far), mb = (unsigned)(seg * NH + h) * 4096u;
    const unsigned f0 = fw(min(l & 3, 2), h) + 12, f1 = F_PG + h * 512 + (l & 3) * 128;
    const unsigned old = __builtin_amdgcn_ballot_w64(fl <= (unsigned)seg);
    if (old & 15) wait_flags(rc, f0, seg + 1, err);
    XST(4);
    v4u q[4][2], k[4][2], km[4], v[8]; v2u vm[8];
    auto ldm = [&]() {
#pragma unroll
        for (int j = 0; j < 4; ++j) km[j] = LD16(rm, (g * 8 + (l & 7)) * 16 + j * 512, mb);
#pragma unroll
        for (int d = 0; d < 8; ++d) vm[d] = LD8(rm, 2048 + fr * 16 + (g & 1) * 8 + d * 256, mb); };
#pragma unroll
    for (int j = 0; j < 8; ++j) q[j >> 1][j & 1] = LDBX(rq, j, sb, 1);
    if (!(old & 240)) {
#pragma unroll
        for (int j = 0; j < 8; ++j) k[j >> 1][j & 1] = LDBX(rk, j, sb ^ KVS(ms >> 2, h), 1);
#pragma unroll
        for (int d = 0; d < 8; ++d) v[d] = LDBX(rv, d, sb ^ KVS(ms >> 4, h), 1);
        ldm();
    } else {
#pragma unroll
        for (int j = 0; j < 8; ++j) k[j >> 1][j & 1] = LDB(rk, j, sb ^ KVS(ms >> 2, h));
#pragma unroll
        for (int d = 0; d < 8; ++d) v[d] = LDB(rv, d, sb ^ KVS(ms >> 4, h));
    }
    v4f sx[3] = {}, o[2][8];
    float mx[2], zx[2];
    constexpr float C = SCALE * LOG2E, MR = MASKV / SCALE;
#pragma unroll
    for (int j = 0; j < 4; ++j) { sx[0] = mf(k[j][0], q[j][0], sx[0]); sx[1] = mf(k[j][0], q[j][1], sx[1]); sx[2] = mf(k[j][1], q[j][1], sx[2]); }
#pragma unroll
    for (int u = 0; u < 2; ++u) {
        float e[8];
#pragma unroll
        for (int r = 0; r < 4; ++r) { const bool vis = 4 * g + r <= fr; e[r] = (u || vis) ? sx[u][r] : MR; e[4 + r] = (u && vis) ? sx[2][r] : MR; }
        float m = e[0];
#pragma unroll
        for (int r = 1; r < 8; ++r) m = fmaxf(m, e[r]);
        mx[u] = m = red_g<true>(m); zx[u] = 0;
#pragma unroll
        for (int r = 0; r < 8; ++r) { e[r] = __builtin_amdgcn_exp2f((e[r] - m) * C); zx[u] += e[r]; }
        const v4u P = {pk2(e[0], e[1]), pk2(e[4], e[5]), pk2(e[2], e[3]), pk2(e[6], e[7])};
#pragma unroll
        for (int d = 0; d < 8; d += 2) {
            o[u][d] = mf(__builtin_shufflevector(v[d], v[d + 1], 0, 1, 4, 5), P, v4f{0, 0, 0, 0});
            o[u][d + 1] = mf(__builtin_shufflevector(v[d], v[d + 1], 2, 3, 6, 7), P, v4f{0, 0, 0, 0});
        }
    }
    asm volatile("" : "+v"(zx[0]), "+v"(zx[1]));
    SCHED(); XST(5);
    if (old & 240) { wait_flags<2>(rc, f1, seg + 1, err); ldm(); }
    XST(6);
    v4f sm[2] = {};
#pragma unroll
    for (int j = 0; j < 4; ++j) { sm[0] = mf(km[j], q[j][0], sm[0]); sm[1] = mf(km[j], q[j][1], sm[1]); }
    XST(28);
#pragma unroll
    for (int u = 0; u < 2; ++u) {
        const float m = red_g<true>(fmaxf(fmaxf(fmaxf(sm[u][0], sm[u][1]), fmaxf(sm[u][2], sm[u][3])), mx[u])), al = __builtin_amdgcn_exp2f((mx[u] - m) * C);
        float e[4];
#pragma unroll
        for (int r = 0; r < 4; ++r) e[r] = __builtin_amdgcn_exp2f((sm[u][r] - m) * C);
        const float inv = __builtin_amdgcn_rcpf(red_g<false>(al * zx[u] + 0.5f * (e[0] + e[1] + e[2] + e[3]))), f = al * inv, im = g < 2 ? inv : 0.0f;
        const unsigned pm0 = pk2(e[0] * im, e[1] * im), pm1 = pk2(e[2] * im, e[3] * im);
        const v4u Pa = {pm0, pm1, 0u, 0u}, Pb = {0u, 0u, pm0, pm1};
#pragma unroll
        for (int d = 0; d < 4; ++d) {
            const v4u vv = cat(vm[2 * d], vm[2 * d + 1]);
            const v4f o0 = mf(vv, Pa, o[u][2 * d] * f), o1 = mf(vv, Pb, o[u][2 * d + 1] * f);
            ST16(rq, l * 16 + d * 1024, sb + u * 4096, (v4u{pk2(o0[0], o0[1]), pk2(o0[2], o0[3]), pk2(o1[0], o1[1]), pk2(o1[2], o1[3])}));
        }
    }
    XST(29);
    FENCE(); const unsigned fn = LD4(rc, fl > seg + 4u ? OOB : (l & 4 ? f1 : f0), 0);
    WAITV(1); XST(30); RBAR(); XST(31);
    if (threadIdx.x == 0) ST4(rc, fw(3, (h & 24) + (seg & 3)) + 64 + (seg & 60), 0, 1u);
    FENCE();
    return max(fl, fn);
}

template <int N> DEV unsigned ror(unsigned v) { return __builtin_amdgcn_update_dpp(0u, v, 0x120 + N, 0xf, 0xf, false); }
DEV void prep_rows(const bf16_t* x, const bf16_t* gw, const bf16_t* wr, rsrc_t rg, int row, int l) {
    v4u xv[16], gv[2], rv[2];
#pragma unroll
    for (int i = 0; i < 2; ++i) { gv[i] = *(const v4u*)(gw + i * 512 + l * 8); rv[i] = *(const v4u*)(wr + i * 512 + l * 8); }
#pragma unroll
    for (int i = 0; i < 16; ++i) xv[i] = *(const v4u*)(x + (size_t)row * DM + i * 512 + l * 8);
    float ga = 0, gb = 0;
#pragma unroll
    for (int k = 0; k < 8; ++k) {
        float a = 0, b = 0;
#pragma unroll
        for (int i = 0; i < 8; ++i) {
            const unsigned u = xv[2 * k + (i >> 2)][i & 3], g = gv[i >> 2][i & 3], r = rv[i >> 2][i & 3];
            a += rbf(lo16(u) * lo16(g)) + rbf(hi16(u) * hi16(g));
            b += lo16(u) * lo16(r) + hi16(u) * hi16(r);
        }
        a = half_add(a, b);
        a += __uint_as_float(ror<8>(__float_as_uint(a))); a += __uint_as_float(ror<4>(__float_as_uint(a)));
        a += __uint_as_float(ror<2>(__float_as_uint(a))); a += __uint_as_float(ror<1>(__float_as_uint(a)));
        const v2u s = sw16(__float_as_uint(a), __float_as_uint(a)); a = __uint_as_float(s[0]) + __uint_as_float(s[1]);
        if ((l & 31) == k) ga = a;
    }
    gb = rbf(ga);
    const unsigned u = __float_as_uint(l < 32 ? rbf(sigmoidf_(gb)) : gb) | 1u;
    const v2u e = sw32(u, u);
    ST16(rg, l < 8 && !(l & 1) ? F_GR + (row + l) * 8 : OOB, 0, (v4u{e[0], e[1], ror<15>(e[0]), ror<15>(e[1])}));
}
template <int N> DEV unsigned top_keys(unsigned* key, int l) {
    unsigned res = 0;
#pragma unroll 1
    for (int c = 0; c < TOPC; ++c) {
        unsigned m = key[0];
#pragma unroll
        for (int i = 1; i < N; ++i) m = max(m, key[i]);
        m = max(m, ror<8>(m)); m = max(m, ror<4>(m)); m = max(m, ror<2>(m)); m = max(m, ror<1>(m));
        v2u s = sw16(m, m); m = max(s[0], s[1]); s = sw32(m, m); m = max(s[0], s[1]);
#pragma unroll
        for (int i = 0; i < N; ++i) key[i] = key[i] == m ? 0u : key[i];
        if (l == c) res = m;
    }
    return res;
}
DEV int top8(LAS unsigned* cand, rsrc_t rc, unsigned* err, int w, int l) {
    v4u t[2];
    for (unsigned tries = 0;; ++tries) {
        FENCE();
        t[0] = LD16(rc, F_GR + w * 2048 + l * 16, 0); t[1] = LD16(rc, F_GR + w * 2048 + 1024 + l * 16, 0);
        const v4u v = t[0] & t[1];
        if (all_ge(v[0] & v[1] & v[2] & v[3] & 1u, 1u)) break;
        __builtin_amdgcn_s_sleep(8);
        if (spin_fail(tries, err)) break;
    }
    unsigned key[4];
#pragma unroll
    for (int i = 0; i < 4; ++i) { const unsigned r = t[i >> 1][(i & 1) * 2 + 1] - 1u; key[i] = ((int)r < 0 ? 0xffff0000u - r : r + 0x80000000u) + (0xffffu - (w * 256 + (i >> 1) * 128 + l * 2 + (i & 1))); }
    key[0] = top_keys<4>(key, l);
    if (l < TOPC) cand[w * TOPC + l] = key[0];
    BAR();
    key[0] = cand[l];
    return 0xffff - (int)(top_keys<1>(key, l) & 0xffffu);
}
static constexpr int NMOE = FF / 128, XR = 2080;
DEV void moe(LAS char* lds, v4u rx, int sel, v4u rg, v4u ru, v4u rd, rsrc_t rp, int cu) {
    const int tid = threadIdx.x, w = __builtin_amdgcn_readfirstlane(tid >> 6), l = tid & 63, kh = w >> 2, cg = w & 3, fr = l & 15, fq = l >> 4;
    const unsigned lb = (unsigned)(size_t)lds, v1 = wt_src(tid, FF * 2), v2 = wt_src(tid, DM * 2);
    const unsigned xo = __builtin_amdgcn_readlane(sel, w) * 2048;
    DMA16(rx, l * 16, xo, lb + L_RED + w * XR); DMA16(rx, l * 16, xo + 1024, lb + L_RED + w * XR + 1024);
    auto issue = [&](int s) {
        const unsigned d = lb + w * 1024 + (s & 3) * XS;
        if (s < 16) { const unsigned o = s * (64 * FF * 2) + cu * 256; WT_ISSUE(rg, ru, v1, o, o, FF * 2, d); }
        else { const unsigned o = s < 24 ? (cu * 128 + (s & 1) * 64) * (DM * 2) + (((s >> 1) + cu) & 3) * 512 : OOB; WT_ISSUE(rd, rd, v2, o, o + 256, DM * 2, d); } };
    issue(0); issue(1); issue(2); issue(3);
    const int p0 = wt_rd(w, l, 0), p1 = wt_rd(w, l, 1);
    v4f c[2] = {};
    auto stage = [&](int s, const LAS char* pa) {
        WAITV(12); RBAR();
        const LAS char* p = lds + (s & 3) * XS;
        const v4u a0 = *(const LAS v4u*)pa, a1 = *(const LAS v4u*)(pa + 64), b0 = wt_frag(p + p0), b1 = wt_frag(p + p1), b2 = wt_frag(p + p0 + 8192), b3 = wt_frag(p + p1 + 8192);
        BAR();
        issue(s + 4);
        c[0] = mf(b2, a1, mf(b0, a0, c[0])); c[1] = mf(b3, a1, mf(b1, a0, c[1])); };
#pragma unroll 1
    for (int s = 0; s < 16; ++s) stage(s, lds + L_RED + (l & 7) * XR + fq * 16 + s * 128);
    LAS v4f* pu = (LAS v4f*)(lds + L_RED + cg * 2048 + l * 16);
    if (kh) { pu[0] = c[0]; pu[64] = c[1]; }
    BAR();
    if (!kh && fr < 8) {
#pragma unroll
        for (int n = 0; n < 2; ++n) {
            const v4f u = pu[64 * n]; float hv[4];
#pragma unroll
            for (int e = 0; e < 4; ++e) { const float g = rbf(c[n][e]); hv[e] = rbf(g * sigmoidf_(g)) * rbf(u[e]); }
            *(LAS v2u*)(lds + L_RED + 8192 + fr * 288 + cg * 64 + n * 32 + fq * 8) = v2u{pk2(hv[0], hv[1]), pk2(hv[2], hv[3])};
        }
    }
    WAITL(0);
#pragma unroll 1
    for (int s = 16; s < 24; s += 2) {
        c[0] = v4f{0, 0, 0, 0}; c[1] = c[0];
        const LAS char* pa = lds + L_RED + 8192 + (l & 7) * 288 + fq * 16;
        stage(s, pa); stage(s + 1, pa + 128);
        ST16(rp, cu * 32768 + (l & 7) * 4096 + ((((s >> 1) + cu) & 3) * 256 + kh * 128 + cg * 32 + (fr >> 3) * 16 + fq * 4) * 4, 0, __builtin_bit_cast(v4u, fr < 8 ? c[0] : c[1]));
    }
    WAITV(0); RBAR();
}

static constexpr unsigned H2_OFF = 0, H1_OFF = 512 * 1024, H3_OFF = 768 * 1024, PR_OFF = 896 * 1024;
static constexpr size_t SLOT = 2u << 20;
static constexpr int NSLOT = 4;
struct Args {
    const bf16_t* x; const bf16_t* mem0; const bf16_t* Wq; const bf16_t* Wk; const bf16_t* Wv; const bf16_t* Wo;
    const bf16_t* w_r; const bf16_t* wg; const bf16_t* wu; const bf16_t* wd; const bf16_t* gw;
    char* qkv; char* mb; int* idx; float* part; float* upd; char* wt; bf16_t* out;
    char* ring; unsigned* ctl; unsigned* dbg;
    float per; unsigned per0, d1, dv, d2, dp, slack, thr, spare, wq, wx, pf, ms, ws;
};
typedef const __attribute__((address_space(4))) Args* KArgs;
DEV KArgs args() { KArgs p = (KArgs)__builtin_amdgcn_kernarg_segment_ptr(); asm volatile("" : "+s"(p)); return p; }
static constexpr unsigned QSZ = S_TOK * HD * 4;
typedef unsigned v16u __attribute__((ext_vector_type(16)));
typedef unsigned v32u __attribute__((ext_vector_type(32)));

DEV unsigned nearo(unsigned off) { return (off >> 13) * 16384u + (off & 8191u); }
DEV unsigned rt32() { u64 t; asm volatile("s_memrealtime %0\n\ts_waitcnt lgkmcnt(0)" : "=s"(t) :: "memory"); return (unsigned)t; }
DEV unsigned cyc32() { u64 t; asm volatile("s_memtime %0\n\ts_waitcnt lgkmcnt(0)" : "=s"(t) :: "memory"); return (unsigned)t; }
template <int R1, int R2, int R3> DEV v8u rd_w(const LAS char* p) {
    return __builtin_shufflevector(cat(trd(p), trd(p + R1)), cat(trd(p + R2), trd(p + R3)), 0, 1, 2, 3, 4, 5, 6, 7);
}
DEV v16u cat8(v8u a, v8u b) { return __builtin_shufflevector(a, b, 0, 1, 2, 3, 4, 5, 6, 7, 8, 9, 10, 11, 12, 13, 14, 15); }
DEV v32u cat16(v16u a, v16u b) { return __builtin_shufflevector(a, b, 0, 1, 2, 3, 4, 5, 6, 7, 8, 9, 10, 11, 12, 13, 14, 15, 16, 17, 18, 19, 20, 21, 22, 23, 24, 25, 26, 27, 28, 29, 30, 31); }
DEV v8u cat4(v4u a, v4u b) { return __builtin_shufflevector(a, b, 0, 1, 2, 3, 4, 5, 6, 7); }

static constexpr int L_PQK = 0, L_PV = 8704, L_P = 13824, L_VT = 15360, L_X = 16384, L_K = L_X + 9216, L_MISC = 28672, L_PF = 29696, L_STG = 32768, L_TOT = 163840;
static constexpr int X_FQ = 272, X_BLK = 1152;
#define ASM_A \
    "s_setprio 1\n" \
    "s_mov_b32 s76, 0\n" \
    "s_mov_b32 s99, 0x20000\n" \
    "s_mov_b32 s84, 0\n" \
    "s_mov_b32 s81, 0\n" \
    "s_mov_b32 s86, 0\n" \
    "s_mov_b32 s87, 0\n" \
    "s_mov_b32 s88, 0\n" \
    "s_mov_b32 s89, 0\n" \
    "s_mov_b32 s90, 0\n" \
    "s_mov_b32 s91, 0\n" \
    "s_mov_b32 s92, 0\n" \
    "s_mov_b32 s93, 0\n" \
    "s_mov_b32 s94, 0\n" \
    "s_mov_b32 s95, 0\n" \
    "s_mov_b32 s96, 0\n" \
    "s_mov_b32 s97, 0\n" \
    "s_mov_b32 s98, 0\n" \
    "ds_read_b32 v244, v237\n" \
    "s_waitcnt lgkmcnt(0)\n" \
    "v_readfirstlane_b32 s94, v244\n" \
    "s_mul_i32 s79, s94, s70\n" \
    "s_add_u32 s79, s79, s81\n" \
    "s_mov_b32 s77, s79\n" \
    "s_mul_i32 s96, s65, s71\n" \
    "s_lshr_b32 s96, s96, 4\n" \
    "v_mov_b64 v[208:209], 0\n" \
    "v_mov_b64 v[210:211], 0\n" \
    "v_mov_b64 v[212:213], 0\n" \
    "v_mov_b64 v[214:215], 0\n" \
    "v_mov_b64 v[216:217], 0\n" \
    "v_mov_b64 v[218:219], 0\n" \
    "v_mov_b64 v[220:221], 0\n" \
    "v_mov_b64 v[222:223], 0\n" \
    "v_mov_b64 v[224:225], 0\n" \
    "v_mov_b64 v[226:227], 0\n" \
    "v_mov_b64 v[228:229], 0\n" \
    "v_mov_b64 v[230:231], 0\n" \
    "v_mov_b32 v241, 1\n" \
    "s_branch L%=_Acomp\n" \
    "L%=_Aloop:\n" \
    "s_and_b32 s95, s94, 1\n" \
    "s_lshl_b32 s95, s95, 17\n" \
    "s_add_u32 s95, s95, s60\n" \
    "s_add_u32 s85, s95, 0x1000\n" \
    "s_branch L%=_Au2b\n" \
    "L%=_Au2a:\n" \
    "s_sleep 4\n" \
    "L%=_Au2b:\n" \
    "s_memrealtime vcc\n" \
    "s_add_u32 s80, s77, s64\n" \
    "s_add_u32 s80, s80, -3\n" \
    "s_waitcnt lgkmcnt(0)\n" \
    "s_sub_i32 s79, s80, vcc_lo\n" \
    "s_sub_u32 s79, s79, 40\n" \
    "s_cmp_lt_u32 s79, 0x400\n" \
    "s_cbranch_scc1 L%=_Au2a\n" \
    "s_memtime s[38:39]\n" \
    "s_memrealtime vcc\n" \
    "s_nop 4\n" \
    "s_memrealtime s[32:33]\n" \
    "s_nop 4\n" \
    "s_memrealtime s[34:35]\n" \
    "s_nop 4\n" \
    "s_memrealtime s[36:37]\n" \
    "s_add_u32 s80, s77, s64\n" \
    "s_lshl_b32 s80, s80, 2\n" \
    "s_waitcnt lgkmcnt(0)\n" \
    "s_sub_u32 s80, s80, vcc_lo\n" \
    "s_sub_u32 s80, s80, s32\n" \
    "s_sub_u32 s80, s80, s34\n" \
    "s_sub_u32 s80, s80, s36\n" \
    "s_mul_i32 s80, s80, s71\n" \
    "s_ashr_i32 s80, s80, 6\n" \
    "s_add_u32 s78, s80, s38\n" \
    "L%=_Au3a:\n" \
    "s_memtime s[38:39]\n" \
    "s_mul_i32 s80, -3, 23\n" \
    "s_add_u32 s80, s80, s78\n" \
    "s_waitcnt lgkmcnt(0)\n" \
    "s_sub_i32 s79, s80, s38\n" \
    "s_max_i32 s79, s79, 0\n" \
    "s_cmp_gt_u32 s79, 320\n" \
    "s_cbranch_scc1 L%=_Au3s\n" \
    ".p2align 4\n" \
    "L%=_Au3d:\n" \
    "s_sub_u32 s79, s79, 28\n" \
    "s_cbranch_scc0 L%=_Au3d\n" \
    "L%=_Au3e:\n" \
    "L%=_Apoll:\n" \
    "ds_read_b32 v244, v237 offset:4\n" \
    "s_waitcnt lgkmcnt(0)\n" \
    "v_readfirstlane_b32 s79, v244\n" \
    "s_cmp_lt_u32 s79, s76\n" \
    "s_cbranch_scc1 L%=_Askip\n" \
    "L%=_Ago:\n" \
    "buffer_load_dwordx4 v[192:195], v233, s[40:43], s95 offen sc1\n" \
    "s_nop 5\n" \
    "buffer_load_dwordx4 v[196:199], v233, s[40:43], s85 offen sc1\n" \
    "s_nop 5\n" \
    "buffer_load_dwordx4 v[200:203], v239, s[40:43], s95 offen sc1\n" \
    "s_nop 5\n" \
    "buffer_load_dwordx4 v[204:207], v239, s[40:43], s85 offen sc1\n" \
    "L%=_Acomp:\n" \
    "s_mov_b32 m0, s72\n" \
    "s_waitcnt vmcnt(3)\n" \
    "v_smfmac_f32_16x16x64_bf16 v[208:211], v[192:195], v[0:7], v232\n" \
    "v_smfmac_f32_16x16x64_bf16 v[212:215], v[192:195], v[8:15], v232\n" \
    "v_smfmac_f32_16x16x64_bf16 v[216:219], v[192:195], v[64:71], v232\n" \
    "v_smfmac_f32_16x16x64_bf16 v[220:223], v[192:195], v[72:79], v232\n" \
    "v_smfmac_f32_16x16x64_bf16 v[224:227], v[192:195], v[128:135], v232\n" \
    "s_waitcnt vmcnt(2)\n" \
    "v_smfmac_f32_16x16x64_bf16 v[208:211], v[196:199], v[16:23], v232\n" \
    "v_smfmac_f32_16x16x64_bf16 v[212:215], v[196:199], v[24:31], v232\n" \
    "v_smfmac_f32_16x16x64_bf16 v[216:219], v[196:199], v[80:87], v232\n" \
    "v_smfmac_f32_16x16x64_bf16 v[220:223], v[196:199], v[88:95], v232\n" \
    "v_smfmac_f32_16x16x64_bf16 v[228:231], v[192:195], v[136:143], v232\n" \
    "s_waitcnt vmcnt(1)\n" \
    "v_smfmac_f32_16x16x64_bf16 v[208:211], v[200:203], v[32:39], v232\n" \
    "v_smfmac_f32_16x16x64_bf16 v[212:215], v[200:203], v[40:47], v232\n" \
    "v_smfmac_f32_16x16x64_bf16 v[216:219], v[200:203], v[96:103], v232\n" \
    "v_smfmac_f32_16x16x64_bf16 v[220:223], v[200:203], v[104:111], v232\n" \
    "v_smfmac_f32_16x16x64_bf16 v[224:227], v[196:199], v[144:151], v232\n" \
    "s_waitcnt vmcnt(0)\n" \
    "v_smfmac_f32_16x16x64_bf16 v[208:211], v[204:207], v[48:55], v232\n" \
    "v_smfmac_f32_16x16x64_bf16 v[212:215], v[204:207], v[56:63], v232\n" \
    "v_smfmac_f32_16x16x64_bf16 v[216:219], v[204:207], v[112:119], v232\n" \
    "v_add_f32 v208, v208, v210\n" \
    "v_smfmac_f32_16x16x64_bf16 v[220:223], v[204:207], v[120:127], v232\n" \
    "v_add_f32 v209, v209, v211\n" \
    "v_cmp_u_f32 vcc, v208, v209\n" \
    "v_add_f32 v212, v212, v214\n" \
    "v_add_f32 v213, v213, v215\n" \
    "v_add_f32 v216, v216, v218\n" \
    "v_add_f32 v217, v217, v219\n" \
    "s_nop 1\n" \
    "v_add_f32 v220, v220, v222\n" \
    "v_add_f32 v221, v221, v223\n" \
    "v_smfmac_f32_16x16x64_bf16 v[224:227], v[200:203], v[160:167], v232\n" \
    "s_cbranch_vccnz L%=_Aslow\n" \
    "L%=_Aok:\n" \
    "ds_write_addtid_b32 v208 offset:0\n" \
    "ds_write_addtid_b32 v209 offset:544\n" \
    "v_smfmac_f32_16x16x64_bf16 v[228:231], v[196:199], v[152:159], v232\n" \
    "ds_write_addtid_b32 v216 offset:256\n" \
    "ds_write_addtid_b32 v217 offset:800\n" \
    "v_smfmac_f32_16x16x64_bf16 v[224:227], v[204:207], v[176:183], v232\n" \
    "ds_write_addtid_b32 v212 offset:1088\n" \
    "ds_write_addtid_b32 v213 offset:1632\n" \
    "v_smfmac_f32_16x16x64_bf16 v[228:231], v[200:203], v[168:175], v232\n" \
    "ds_write_addtid_b32 v220 offset:1344\n" \
    "ds_write_addtid_b32 v221 offset:1888\n" \
    "ds_write_b32 v236, v241 offset:32\n" \
    "v_smfmac_f32_16x16x64_bf16 v[228:231], v[204:207], v[184:191], v232\n" \
    "s_barrier\n" \
    "s_mov_b32 m0, s73\n" \
    "v_add_f32 v224, v224, v226\n" \
    "v_add_f32 v225, v225, v227\n" \
    "ds_write_addtid_b32 v224 offset:8704\n" \
    "ds_write_addtid_b32 v225 offset:8992\n" \
    "s_nop 1\n" \
    "v_add_f32 v228, v228, v230\n" \
    "v_add_f32 v229, v229, v231\n" \
    "ds_write_addtid_b32 v228 offset:9344\n" \
    "ds_write_addtid_b32 v229 offset:9632\n" \
    "ds_write_b32 v236, v241 offset:16\n" \
    "v_mov_b64 v[208:209], 0\n" \
    "v_mov_b64 v[210:211], 0\n" \
    "v_mov_b64 v[212:213], 0\n" \
    "v_mov_b64 v[214:215], 0\n" \
    "v_mov_b64 v[216:217], 0\n" \
    "v_mov_b64 v[218:219], 0\n" \
    "v_mov_b64 v[220:221], 0\n" \
    "v_mov_b64 v[222:223], 0\n" \
    "v_mov_b64 v[224:225], 0\n" \
    "v_mov_b64 v[226:227], 0\n" \
    "v_mov_b64 v[228:229], 0\n" \
    "v_mov_b64 v[230:231], 0\n" \
    "v_add_u32 v241, 1, v241\n" \
    "ds_read_b32 v244, v237\n" \
    "s_waitcnt lgkmcnt(0)\n" \
    "v_readfirstlane_b32 s94, v244\n" \
    "s_mul_i32 s79, s94, s70\n" \
    "s_add_u32 s79, s79, s81\n" \
    "s_mov_b32 s77, s79\n" \
    "s_barrier\n" \
    "ds_read_b64 v[244:245], v237 offset:64\n" \
    "s_mov_b32 s79, s60\n" \
    "s_mov_b32 s60, s61\n" \
    "s_mov_b32 s61, s62\n" \
    "s_mov_b32 s62, s63\n" \
    "s_mov_b32 s63, s79\n" \
    "s_add_u32 s76, s76, 1\n" \
    "s_add_u32 s77, s77, s65\n" \
    "s_add_u32 s84, s84, 0x20000\n" \
    "s_add_u32 s81, s81, s65\n" \
    "s_waitcnt lgkmcnt(0)\n" \
    "v_readfirstlane_b32 s65, v244\n" \
    "v_readfirstlane_b32 s71, v245\n" \
    "s_mul_i32 s96, s65, s71\n" \
    "s_lshr_b32 s96, s96, 4\n" \
    "s_cmp_lt_u32 s76, 64\n" \
    "s_cbranch_scc1 L%=_Aloop\n" \
    "s_branch L%=_end\n" \
    "L%=_Aslow:\n" \
    "s_nop 7\n" \
    "s_nop 0\n" \
    "v_max_u32 v244, v192, v193\n" \
    "v_max3_u32 v244, v244, v194, v195\n" \
    "v_max3_u32 v244, v244, v196, v197\n" \
    "v_max3_u32 v244, v244, v198, v199\n" \
    "v_max3_u32 v244, v244, v200, v201\n" \
    "v_max3_u32 v244, v244, v202, v203\n" \
    "v_max3_u32 v244, v244, v204, v205\n" \
    "v_max3_u32 v244, v244, v206, v207\n" \
    "v_cmp_eq_u32 vcc, -1, v244\n" \
    "s_cbranch_vccz L%=_Aok\n" \
    "s_cmp_eq_u32 s99, 0\n" \
    "s_cbranch_scc1 L%=_Aok\n" \
    "s_sub_u32 s99, s99, 1\n" \
    "s_cmp_lg_u32 s99, 0\n" \
    "s_cbranch_scc1 L%=_Au5\n" \
    "v_mov_b32 v244, 1\n" \
    "buffer_store_dword v244, off, s[44:47], 0 sc1\n" \
    "L%=_Au5:\n" \
    "v_mov_b64 v[208:209], 0\n" \
    "v_mov_b64 v[210:211], 0\n" \
    "v_mov_b64 v[212:213], 0\n" \
    "v_mov_b64 v[214:215], 0\n" \
    "v_mov_b64 v[216:217], 0\n" \
    "v_mov_b64 v[218:219], 0\n" \
    "v_mov_b64 v[220:221], 0\n" \
    "v_mov_b64 v[222:223], 0\n" \
    "v_mov_b64 v[224:225], 0\n" \
    "v_mov_b64 v[226:227], 0\n" \
    "v_mov_b64 v[228:229], 0\n" \
    "v_mov_b64 v[230:231], 0\n" \
    "L%=_Ash:\n" \
    "s_memrealtime vcc\n" \
    "s_waitcnt lgkmcnt(0)\n" \
    "s_sub_u32 s80, vcc_lo, s64\n" \
    "s_sub_u32 s80, s80, 4\n" \
    "s_mul_i32 s32, s70, s71\n" \
    "s_lshr_b32 s32, s32, 4\n" \
    "L%=_Au6g:\n" \
    "s_add_u32 s77, s77, s70\n" \
    "s_add_u32 s78, s78, s32\n" \
    "s_add_u32 s94, s94, 1\n" \
    "s_sub_u32 s79, s80, s77\n" \
    "s_cmp_lt_u32 s79, 0x1000000\n" \
    "s_cbranch_scc1 L%=_Au6g\n" \
    "v_mov_b32 v244, s94\n" \
    "ds_max_u32 v237, v244\n" \
    "s_cmp_gt_i32 s79, -20\n" \
    "s_cbranch_scc1 L%=_Au6c\n" \
    "s_branch L%=_Au7b\n" \
    "L%=_Au7a:\n" \
    "s_sleep 4\n" \
    "L%=_Au7b:\n" \
    "s_memrealtime vcc\n" \
    "s_add_u32 s80, s77, s64\n" \
    "s_add_u32 s80, s80, -3\n" \
    "s_waitcnt lgkmcnt(0)\n" \
    "s_sub_i32 s79, s80, vcc_lo\n" \
    "s_sub_u32 s79, s79, 40\n" \
    "s_cmp_lt_u32 s79, 0x400\n" \
    "s_cbranch_scc1 L%=_Au7a\n" \
    "s_memtime s[38:39]\n" \
    "s_memrealtime vcc\n" \
    "s_nop 4\n" \
    "s_memrealtime s[32:33]\n" \
    "s_nop 4\n" \
    "s_memrealtime s[34:35]\n" \
    "s_nop 4\n" \
    "s_memrealtime s[36:37]\n" \
    "s_add_u32 s80, s77, s64\n" \
    "s_lshl_b32 s80, s80, 2\n" \
    "s_waitcnt lgkmcnt(0)\n" \
    "s_sub_u32 s80, s80, vcc_lo\n" \
    "s_sub_u32 s80, s80, s32\n" \
    "s_sub_u32 s80, s80, s34\n" \
    "s_sub_u32 s80, s80, s36\n" \
    "s_mul_i32 s80, s80, s71\n" \
    "s_ashr_i32 s80, s80, 6\n" \
    "s_add_u32 s78, s80, s38\n" \
    "L%=_Au6c:\n" \
    "s_and_b32 s95, s94, 1\n" \
    "s_lshl_b32 s95, s95, 17\n" \
    "s_add_u32 s95, s95, s60\n" \
    "s_add_u32 s85, s95, 0x1000\n" \
    "L%=_Au8a:\n" \
    "s_memtime s[38:39]\n" \
    "s_mul_i32 s80, -3, 23\n" \
    "s_add_u32 s80, s80, s78\n" \
    "s_waitcnt lgkmcnt(0)\n" \
    "s_sub_i32 s79, s80, s38\n" \
    "s_max_i32 s79, s79, 0\n" \
    "s_cmp_gt_u32 s79, 320\n" \
    "s_cbranch_scc1 L%=_Au8s\n" \
    ".p2align 4\n" \
    "L%=_Au8d:\n" \
    "s_sub_u32 s79, s79, 28\n" \
    "s_cbranch_scc0 L%=_Au8d\n" \
    "L%=_Au8e:\n" \
    "s_branch L%=_Apoll\n" \
    "L%=_Askip:\n" \
    "s_cmp_lt_u32 s99, 2\n" \
    "s_cbranch_scc1 L%=_Ago\n" \
    "s_sub_u32 s99, s99, 1\n" \
    "s_branch L%=_Ash\n" \
    "L%=_Au3s:\n" \
    "s_cmp_gt_u32 s79, 0x8000\n" \
    "s_cbranch_scc1 L%=_Au3e\n" \
    "s_sleep 2\n" \
    "s_branch L%=_Au3a\n" \
    "L%=_Au8s:\n" \
    "s_cmp_gt_u32 s79, 0x8000\n" \
    "s_cbranch_scc1 L%=_Au8e\n" \
    "s_sleep 2\n" \
    "s_branch L%=_Au8a\n" \
    "L%=_end:\n"
#define ASM_B \
    "s_and_b32 s79, s78, 3\n" \
    "s_cmp_eq_u32 s79, 1\n" \
    "s_cbranch_scc1 L%=_S\n" \
    "s_cmp_eq_u32 s79, 2\n" \
    "s_cbranch_scc1 L%=_R\n" \
    "s_cmp_eq_u32 s79, 3\n" \
    "s_cbranch_scc1 L%=_P\n" \
    "L%=_T:\n" \
    "s_mov_b32 s76, 0\n" \
    "s_mov_b32 s99, 0x20000\n" \
    "s_mov_b32 s84, 0\n" \
    "s_mov_b32 s81, 0\n" \
    "s_mov_b32 s86, 0\n" \
    "s_mov_b32 s87, 0\n" \
    "s_mov_b32 s88, 0\n" \
    "s_mov_b32 s89, 0\n" \
    "s_mov_b32 s90, 0\n" \
    "s_mov_b32 s91, 0\n" \
    "s_mov_b32 s92, 0\n" \
    "s_mov_b32 s93, 0\n" \
    "s_mov_b32 s94, 0\n" \
    "s_mov_b32 s95, 0\n" \
    "s_mov_b32 s96, 0\n" \
    "s_mov_b32 s97, 0\n" \
    "s_mov_b32 s98, 0\n" \
    "ds_read_b32 v216, v229\n" \
    "s_waitcnt lgkmcnt(0)\n" \
    "v_readfirstlane_b32 s94, v216\n" \
    "s_mul_i32 s79, s94, s70\n" \
    "s_add_u32 s79, s79, s81\n" \
    "s_mov_b32 s77, s79\n" \
    "s_mul_i32 s96, s65, s71\n" \
    "s_lshr_b32 s96, s96, 4\n" \
    "v_mov_b32 v96, 0x3f803f80\n" \
    "v_mov_b32 v97, 0x3f803f80\n" \
    "v_mov_b32 v98, 0x3f803f80\n" \
    "v_mov_b32 v99, 0x3f803f80\n" \
    "v_mov_b32 v100, 0x3f803f80\n" \
    "v_mov_b32 v101, 0x3f803f80\n" \
    "v_mov_b64 v[102:103], 0\n" \
    "v_mov_b64 v[64:65], 0\n" \
    "v_mov_b64 v[66:67], 0\n" \
    "v_mov_b64 v[68:69], 0\n" \
    "v_mov_b64 v[70:71], 0\n" \
    "v_mov_b64 v[72:73], 0\n" \
    "v_mov_b64 v[74:75], 0\n" \
    "v_mov_b64 v[76:77], 0\n" \
    "v_mov_b64 v[78:79], 0\n" \
    "v_mov_b64 v[80:81], 0\n" \
    "v_mov_b64 v[82:83], 0\n" \
    "v_mov_b64 v[84:85], 0\n" \
    "v_mov_b64 v[86:87], 0\n" \
    "v_mov_b64 v[88:89], 0\n" \
    "v_mov_b64 v[90:91], 0\n" \
    "v_mov_b64 v[92:93], 0\n" \
    "v_mov_b64 v[94:95], 0\n" \
    "v_mov_b64 v[192:193], 0\n" \
    "v_mov_b64 v[194:195], 0\n" \
    "L%=_Tloop:\n" \
    "ds_read_b64_tr_b16 v[104:105], v228 offset:16384\n" \
    "ds_read_b64_tr_b16 v[106:107], v228 offset:16416\n" \
    "ds_read_b64_tr_b16 v[108:109], v228 offset:18688\n" \
    "ds_read_b64_tr_b16 v[110:111], v228 offset:18720\n" \
    "ds_read_b64_tr_b16 v[112:113], v228 offset:20992\n" \
    "ds_read_b64_tr_b16 v[114:115], v228 offset:21024\n" \
    "ds_read_b64_tr_b16 v[116:117], v228 offset:23296\n" \
    "ds_read_b64_tr_b16 v[118:119], v228 offset:23328\n" \
    "ds_read_b64_tr_b16 v[120:121], v228 offset:16656\n" \
    "ds_read_b64_tr_b16 v[122:123], v228 offset:16688\n" \
    "ds_read_b64_tr_b16 v[124:125], v228 offset:18960\n" \
    "ds_read_b64_tr_b16 v[126:127], v228 offset:18992\n" \
    "ds_read_b64_tr_b16 v[128:129], v228 offset:21264\n" \
    "ds_read_b64_tr_b16 v[130:131], v228 offset:21296\n" \
    "ds_read_b64_tr_b16 v[132:133], v228 offset:23568\n" \
    "ds_read_b64_tr_b16 v[134:135], v228 offset:23600\n" \
    "s_waitcnt lgkmcnt(14)\n" \
    "v_mfma_f32_16x16x32_bf16 v[136:139], v[104:107], v[0:3], 0\n" \
    "v_mfma_f32_16x16x32_bf16 v[144:147], v[104:107], v[8:11], 0\n" \
    "v_mfma_f32_16x16x32_bf16 v[152:155], v[104:107], v[16:19], 0\n" \
    "v_mfma_f32_16x16x32_bf16 v[160:163], v[104:107], v[24:27], 0\n" \
    "s_waitcnt lgkmcnt(12)\n" \
    "v_mfma_f32_16x16x32_bf16 v[136:139], v[108:111], v[4:7], v[136:139]\n" \
    "v_mfma_f32_16x16x32_bf16 v[144:147], v[108:111], v[12:15], v[144:147]\n" \
    "v_mfma_f32_16x16x32_bf16 v[152:155], v[108:111], v[20:23], v[152:155]\n" \
    "v_mfma_f32_16x16x32_bf16 v[160:163], v[108:111], v[28:31], v[160:163]\n" \
    "s_waitcnt lgkmcnt(10)\n" \
    "v_mfma_f32_16x16x32_bf16 v[136:139], v[112:115], v[32:35], v[136:139]\n" \
    "v_mfma_f32_16x16x32_bf16 v[144:147], v[112:115], v[40:43], v[144:147]\n" \
    "v_mfma_f32_16x16x32_bf16 v[152:155], v[112:115], v[48:51], v[152:155]\n" \
    "v_mfma_f32_16x16x32_bf16 v[160:163], v[112:115], v[56:59], v[160:163]\n" \
    "s_waitcnt lgkmcnt(8)\n" \
    "v_mfma_f32_16x16x32_bf16 v[136:139], v[116:119], v[36:39], v[136:139]\n" \
    "v_mfma_f32_16x16x32_bf16 v[144:147], v[116:119], v[44:47], v[144:147]\n" \
    "v_mfma_f32_16x16x32_bf16 v[152:155], v[116:119], v[52:55], v[152:155]\n" \
    "v_mfma_f32_16x16x32_bf16 v[160:163], v[116:119], v[60:63], v[160:163]\n" \
    "s_waitcnt lgkmcnt(6)\n" \
    "v_mfma_f32_16x16x32_bf16 v[140:143], v[120:123], v[0:3], 0\n" \
    "v_mfma_f32_16x16x32_bf16 v[148:151], v[120:123], v[8:11], 0\n" \
    "v_mfma_f32_16x16x32_bf16 v[156:159], v[120:123], v[16:19], 0\n" \
    "v_mfma_f32_16x16x32_bf16 v[164:167], v[120:123], v[24:27], 0\n" \
    "s_waitcnt lgkmcnt(4)\n" \
    "v_mfma_f32_16x16x32_bf16 v[140:143], v[124:127], v[4:7], v[140:143]\n" \
    "v_mfma_f32_16x16x32_bf16 v[148:151], v[124:127], v[12:15], v[148:151]\n" \
    "v_mfma_f32_16x16x32_bf16 v[156:159], v[124:127], v[20:23], v[156:159]\n" \
    "v_mfma_f32_16x16x32_bf16 v[164:167], v[124:127], v[28:31], v[164:167]\n" \
    "s_waitcnt lgkmcnt(2)\n" \
    "v_mfma_f32_16x16x32_bf16 v[140:143], v[128:131], v[32:35], v[140:143]\n" \
    "v_mfma_f32_16x16x32_bf16 v[148:151], v[128:131], v[40:43], v[148:151]\n" \
    "v_mfma_f32_16x16x32_bf16 v[156:159], v[128:131], v[48:51], v[156:159]\n" \
    "v_mfma_f32_16x16x32_bf16 v[164:167], v[128:131], v[56:59], v[164:167]\n" \
    "s_waitcnt lgkmcnt(0)\n" \
    "v_mfma_f32_16x16x32_bf16 v[140:143], v[132:135], v[36:39], v[140:143]\n" \
    "v_mfma_f32_16x16x32_bf16 v[148:151], v[132:135], v[44:47], v[148:151]\n" \
    "v_mfma_f32_16x16x32_bf16 v[156:159], v[132:135], v[52:55], v[156:159]\n" \
    "v_mfma_f32_16x16x32_bf16 v[164:167], v[132:135], v[60:63], v[164:167]\n" \
    "v_cvt_pk_bf16_f32 v65, v136, v137\n" \
    "v_cvt_pk_bf16_f32 v68, v138, v139\n" \
    "v_cvt_pk_bf16_f32 v73, v144, v145\n" \
    "v_cvt_pk_bf16_f32 v76, v146, v147\n" \
    "v_cvt_pk_bf16_f32 v81, v152, v153\n" \
    "v_cvt_pk_bf16_f32 v84, v154, v155\n" \
    "v_cvt_pk_bf16_f32 v89, v160, v161\n" \
    "v_cvt_pk_bf16_f32 v92, v162, v163\n" \
    "v_cvt_pk_bf16_f32 v66, v142, v143\n" \
    "v_cvt_pk_bf16_f32 v67, v142, v143\n" \
    "v_cvt_pk_bf16_f32 v69, v140, v141\n" \
    "v_cvt_pk_bf16_f32 v74, v150, v151\n" \
    "v_cvt_pk_bf16_f32 v75, v150, v151\n" \
    "v_cvt_pk_bf16_f32 v77, v148, v149\n" \
    "v_cvt_pk_bf16_f32 v82, v158, v159\n" \
    "v_cvt_pk_bf16_f32 v83, v158, v159\n" \
    "v_cvt_pk_bf16_f32 v85, v156, v157\n" \
    "v_cvt_pk_bf16_f32 v90, v166, v167\n" \
    "v_cvt_pk_bf16_f32 v91, v166, v167\n" \
    "v_cvt_pk_bf16_f32 v93, v164, v165\n" \
    "v_permlane16_swap_b32 v66, v67\n" \
    "v_permlane16_swap_b32 v74, v75\n" \
    "v_permlane16_swap_b32 v82, v83\n" \
    "v_permlane16_swap_b32 v90, v91\n" \
    "v_mov_b64 v[176:177], 0\n" \
    "v_mov_b64 v[178:179], 0\n" \
    "v_mov_b64 v[180:181], 0\n" \
    "v_mov_b64 v[182:183], 0\n" \
    "v_mov_b64 v[184:185], 0\n" \
    "v_mov_b64 v[186:187], 0\n" \
    "v_mov_b64 v[188:189], 0\n" \
    "v_mov_b64 v[190:191], 0\n" \
    "v_mov_b64 v[196:197], 0\n" \
    "v_mov_b64 v[198:199], 0\n" \
    "v_mov_b64 v[200:201], 0\n" \
    "v_mov_b64 v[202:203], 0\n" \
    "v_mov_b64 v[204:205], 0\n" \
    "v_mov_b64 v[206:207], 0\n" \
    "v_mov_b64 v[208:209], 0\n" \
    "v_mov_b64 v[210:211], 0\n" \
    "v_mov_b64 v[212:213], 0\n" \
    "v_mov_b64 v[214:215], 0\n" \
    "s_add_u32 s85, s60, 0x1000\n" \
    "s_branch L%=_Tu2b\n" \
    "L%=_Tu2a:\n" \
    "s_sleep 4\n" \
    "L%=_Tu2b:\n" \
    "s_memrealtime vcc\n" \
    "s_add_u32 s80, s77, s64\n" \
    "s_add_u32 s80, s80, s69\n" \
    "s_waitcnt lgkmcnt(0)\n" \
    "s_sub_i32 s79, s80, vcc_lo\n" \
    "s_sub_u32 s79, s79, 40\n" \
    "s_cmp_lt_u32 s79, 0x400\n" \
    "s_cbranch_scc1 L%=_Tu2a\n" \
    "s_memtime s[38:39]\n" \
    "s_memrealtime vcc\n" \
    "s_nop 4\n" \
    "s_memrealtime s[32:33]\n" \
    "s_nop 4\n" \
    "s_memrealtime s[34:35]\n" \
    "s_nop 4\n" \
    "s_memrealtime s[36:37]\n" \
    "s_add_u32 s80, s77, s64\n" \
    "s_lshl_b32 s80, s80, 2\n" \
    "s_waitcnt lgkmcnt(0)\n" \
    "s_sub_u32 s80, s80, vcc_lo\n" \
    "s_sub_u32 s80, s80, s32\n" \
    "s_sub_u32 s80, s80, s34\n" \
    "s_sub_u32 s80, s80, s36\n" \
    "s_mul_i32 s80, s80, s71\n" \
    "s_ashr_i32 s80, s80, 6\n" \
    "s_add_u32 s78, s80, s38\n" \
    "v_mov_b32 v143, s76\n" \
    "s_barrier\n" \
    "s_cmp_eq_u32 s76, 63\n" \
    "s_cbranch_scc1 L%=_Tfin\n" \
    "ds_read_b32 v216, v229\n" \
    "s_waitcnt lgkmcnt(0)\n" \
    "v_readfirstlane_b32 s94, v216\n" \
    "s_mul_i32 s79, s94, s70\n" \
    "s_add_u32 s79, s79, s81\n" \
    "s_cmp_lg_u32 s79, s77\n" \
    "s_cbranch_scc1 L%=_Tu3s\n" \
    "L%=_Tu3k:\n" \
    "L%=_Tu4a:\n" \
    "s_memtime s[38:39]\n" \
    "s_mul_hi_u32 s80, s66, s96\n" \
    "s_add_u32 s80, s80, s78\n" \
    "s_waitcnt lgkmcnt(0)\n" \
    "s_sub_i32 s79, s80, s38\n" \
    "s_max_i32 s79, s79, 0\n" \
    "s_cmp_gt_u32 s79, 320\n" \
    "s_cbranch_scc1 L%=_Tu4s\n" \
    ".p2align 4\n" \
    "L%=_Tu4d:\n" \
    "s_sub_u32 s79, s79, 28\n" \
    "s_cbranch_scc0 L%=_Tu4d\n" \
    "L%=_Tu4e:\n" \
    "buffer_load_dwordx4 v[104:107], v230, s[40:43], s60 offen offset:0 sc1\n" \
    "buffer_load_dwordx2 v[120:121], v231, s[40:43], s60 offen offset:0 sc1\n" \
    "buffer_load_dwordx4 v[108:111], v230, s[40:43], s60 offen offset:2048 sc1\n" \
    "buffer_load_dwordx2 v[122:123], v231, s[40:43], s60 offen offset:2048 sc1\n" \
    "buffer_load_dwordx4 v[112:115], v230, s[40:43], s85 offen offset:0 sc1\n" \
    "buffer_load_dwordx2 v[124:125], v231, s[40:43], s85 offen offset:0 sc1\n" \
    "buffer_load_dwordx4 v[116:119], v230, s[40:43], s85 offen offset:2048 sc1\n" \
    "buffer_load_dwordx2 v[126:127], v231, s[40:43], s85 offen offset:2048 sc1\n" \
    "s_setprio 3\n" \
    "buffer_load_dwordx4 v[168:171], v225, s[40:43], s60 offen sc1\n" \
    "buffer_load_dwordx4 v[172:175], v225, s[40:43], s85 offen sc1\n" \
    "L%=_Tsc:\n" \
    "s_waitcnt vmcnt(6)\n" \
    "v_pk_add_f32 v[128:129], v[104:105], v[108:109]\n" \
    "v_pk_add_f32 v[130:131], v[106:107], v[110:111]\n" \
    "v_pk_add_f32 v[132:133], v[120:121], v[122:123]\n" \
    "s_waitcnt vmcnt(4)\n" \
    "v_pk_add_f32 v[128:129], v[128:129], v[112:113]\n" \
    "v_pk_add_f32 v[130:131], v[130:131], v[114:115]\n" \
    "v_pk_add_f32 v[132:133], v[132:133], v[124:125]\n" \
    "s_waitcnt vmcnt(2)\n" \
    "v_pk_add_f32 v[128:129], v[128:129], v[116:117]\n" \
    "v_pk_add_f32 v[130:131], v[130:131], v[118:119]\n" \
    "v_pk_add_f32 v[132:133], v[132:133], v[126:127]\n" \
    "v_max3_f32 v136, v128, v129, v130\n" \
    "v_max3_f32 v137, v131, v132, v133\n" \
    "v_max_f32 v136, v136, v137\n" \
    "v_fma_f32 v141, v131, v132, v133\n" \
    "v_fma_f32 v140, v128, v129, v130\n" \
    "v_max_f32_dpp v138, v136, v136 row_ror:8 row_mask:0xf bank_mask:0xf\n" \
    "v_max_f32_dpp v139, v136, v136 row_ror:8 row_mask:0xf bank_mask:0xf\n" \
    "s_waitcnt vmcnt(0)\n" \
    "v_smfmac_f32_16x16x64_bf16 v[176:179], v[168:171], v[0:7], v224\n" \
    "v_cmp_u_f32 vcc, v140, v141\n" \
    "v_permlane16_swap_b32 v138, v139\n" \
    "v_max_f32 v136, v138, v139\n" \
    "v_max_f32 v137, v138, v139\n" \
    "v_smfmac_f32_16x16x64_bf16 v[180:183], v[168:171], v[8:15], v224\n" \
    "s_cbranch_vccnz L%=_Tslow\n" \
    "L%=_Tok:\n" \
    "v_permlane32_swap_b32 v136, v137\n" \
    "v_max_f32 v136, v136, v137\n" \
    "v_mul_f32 v136, s72, v136\n" \
    "v_pk_fma_f32 v[128:129], v[128:129], s[72:73], v[136:137] op_sel_hi:[1,1,0] neg_lo:[0,0,1] neg_hi:[0,0,1]\n" \
    "v_pk_fma_f32 v[132:133], v[132:133], s[72:73], v[136:137] op_sel_hi:[1,1,0] neg_lo:[0,0,1] neg_hi:[0,0,1]\n" \
    "v_pk_fma_f32 v[130:131], v[130:131], s[72:73], v[136:137] op_sel_hi:[1,1,0] neg_lo:[0,0,1] neg_hi:[0,0,1]\n" \
    "v_exp_f32 v128, v128\n" \
    "v_exp_f32 v129, v129\n" \
    "v_exp_f32 v132, v132\n" \
    "v_exp_f32 v133, v133\n" \
    "v_exp_f32 v130, v130\n" \
    "v_exp_f32 v131, v131\n" \
    "v_cvt_pk_bf16_f32 v140, v128, v129\n" \
    "v_cvt_pk_bf16_f32 v141, v132, v133\n" \
    "v_cvt_pk_bf16_f32 v142, v130, v131\n" \
    "ds_write_b128 v232, v[140:143]\n" \
    "s_barrier\n" \
    "ds_read_b128 v[192:195], v227\n" \
    "s_waitcnt vmcnt(0)\n" \
    "v_smfmac_f32_16x16x64_bf16 v[176:179], v[172:175], v[32:39], v224\n" \
    "L%=_Tvc:\n" \
    "v_smfmac_f32_16x16x64_bf16 v[180:183], v[172:175], v[40:47], v224\n" \
    "v_smfmac_f32_16x16x64_bf16 v[184:187], v[168:171], v[16:23], v224\n" \
    "v_add_f32 v176, v176, v178\n" \
    "v_smfmac_f32_16x16x64_bf16 v[188:191], v[168:171], v[24:31], v224\n" \
    "v_add_f32 v177, v177, v179\n" \
    "v_smfmac_f32_16x16x64_bf16 v[184:187], v[172:175], v[48:55], v224\n" \
    "v_cmp_u_f32 vcc, v176, v177\n" \
    "v_smfmac_f32_16x16x64_bf16 v[188:191], v[172:175], v[56:63], v224\n" \
    "s_cbranch_vccnz L%=_Tvslow\n" \
    "L%=_Tvok:\n" \
    "v_cvt_pk_bf16_f32 v64, v176, v177\n" \
    "v_add_f32 v180, v180, v182\n" \
    "s_waitcnt lgkmcnt(0)\n" \
    "v_smfmac_f32_16x16x64_bf16 v[212:215], v[192:195], v[96:103], v224\n" \
    "v_add_f32 v181, v181, v183\n" \
    "v_smfmac_f32_16x16x64_bf16 v[196:199], v[192:195], v[64:71], v224\n" \
    "v_cvt_pk_bf16_f32 v72, v180, v181\n" \
    "v_add_f32 v184, v184, v186\n" \
    "v_add_f32 v185, v185, v187\n" \
    "v_smfmac_f32_16x16x64_bf16 v[200:203], v[192:195], v[72:79], v224\n" \
    "v_cvt_pk_bf16_f32 v80, v184, v185\n" \
    "v_add_f32 v188, v188, v190\n" \
    "v_add_f32 v189, v189, v191\n" \
    "v_smfmac_f32_16x16x64_bf16 v[204:207], v[192:195], v[80:87], v224\n" \
    "v_cvt_pk_bf16_f32 v88, v188, v189\n" \
    "v_add_f32 v212, v212, v214\n" \
    "v_add_f32 v213, v213, v215\n" \
    "v_smfmac_f32_16x16x64_bf16 v[208:211], v[192:195], v[88:95], v224\n" \
    "v_rcp_f32 v220, v212\n" \
    "v_rcp_f32 v221, v213\n" \
    "v_add_f32 v196, v196, v198\n" \
    "v_add_f32 v197, v197, v199\n" \
    "v_pk_add_f32 v[200:201], v[200:201], v[202:203]\n" \
    "v_pk_add_f32 v[204:205], v[204:205], v[206:207]\n" \
    "s_nop 1\n" \
    "v_pk_add_f32 v[208:209], v[208:209], v[210:211]\n" \
    "v_pk_mul_f32 v[196:197], v[196:197], v[220:221]\n" \
    "v_pk_mul_f32 v[200:201], v[200:201], v[220:221]\n" \
    "v_pk_mul_f32 v[204:205], v[204:205], v[220:221]\n" \
    "v_pk_mul_f32 v[208:209], v[208:209], v[220:221]\n" \
    "v_cvt_pk_bf16_f32 v216, v196, v200\n" \
    "v_cvt_pk_bf16_f32 v217, v204, v208\n" \
    "v_cvt_pk_bf16_f32 v218, v197, v201\n" \
    "v_cvt_pk_bf16_f32 v219, v205, v209\n" \
    "buffer_store_dwordx4 v[216:219], v226, s[40:43], s60 offen sc1\n" \
    "s_setprio 0\n" \
    "buffer_store_dword v143, v233, s[44:47], 0 offen sc1\n" \
    "ds_read_b64 v[220:221], v229 offset:64\n" \
    "s_mov_b32 s79, s60\n" \
    "s_mov_b32 s60, s61\n" \
    "s_mov_b32 s61, s62\n" \
    "s_mov_b32 s62, s63\n" \
    "s_mov_b32 s63, s79\n" \
    "s_add_u32 s76, s76, 1\n" \
    "s_add_u32 s77, s77, s65\n" \
    "s_add_u32 s84, s84, 0x20000\n" \
    "s_add_u32 s81, s81, s65\n" \
    "s_waitcnt lgkmcnt(0)\n" \
    "v_readfirstlane_b32 s65, v220\n" \
    "v_readfirstlane_b32 s71, v221\n" \
    "s_mul_i32 s96, s65, s71\n" \
    "s_lshr_b32 s96, s96, 4\n" \
    "s_branch L%=_Tloop\n" \
    "L%=_Tslow:\n" \
    "s_nop 7\n" \
    "s_nop 0\n" \
    "v_max_u32 v138, v104, v105\n" \
    "v_max3_u32 v138, v138, v106, v107\n" \
    "v_max3_u32 v138, v138, v108, v109\n" \
    "v_max3_u32 v138, v138, v110, v111\n" \
    "v_max3_u32 v138, v138, v112, v113\n" \
    "v_max3_u32 v138, v138, v114, v115\n" \
    "v_max3_u32 v138, v138, v116, v117\n" \
    "v_max3_u32 v138, v138, v118, v119\n" \
    "v_max3_u32 v138, v138, v120, v121\n" \
    "v_max3_u32 v138, v138, v122, v123\n" \
    "v_max3_u32 v138, v138, v124, v125\n" \
    "v_max3_u32 v138, v138, v126, v127\n" \
    "v_cmp_eq_u32 vcc, -1, v138\n" \
    "s_cbranch_vccz L%=_Tok\n" \
    "s_cmp_eq_u32 s99, 0\n" \
    "s_cbranch_scc1 L%=_Tok\n" \
    "s_sub_u32 s99, s99, 1\n" \
    "s_cmp_lg_u32 s99, 0\n" \
    "s_cbranch_scc1 L%=_Tu5\n" \
    "v_mov_b32 v138, 1\n" \
    "buffer_store_dword v138, off, s[44:47], 0 sc1\n" \
    "L%=_Tu5:\n" \
    "s_sleep 1\n" \
    "buffer_load_dwordx4 v[104:107], v230, s[40:43], s60 offen offset:0 sc1\n" \
    "buffer_load_dwordx2 v[120:121], v231, s[40:43], s60 offen offset:0 sc1\n" \
    "buffer_load_dwordx4 v[108:111], v230, s[40:43], s60 offen offset:2048 sc1\n" \
    "buffer_load_dwordx2 v[122:123], v231, s[40:43], s60 offen offset:2048 sc1\n" \
    "buffer_load_dwordx4 v[112:115], v230, s[40:43], s85 offen offset:0 sc1\n" \
    "buffer_load_dwordx2 v[124:125], v231, s[40:43], s85 offen offset:0 sc1\n" \
    "buffer_load_dwordx4 v[116:119], v230, s[40:43], s85 offen offset:2048 sc1\n" \
    "buffer_load_dwordx2 v[126:127], v231, s[40:43], s85 offen offset:2048 sc1\n" \
    "buffer_load_dwordx4 v[168:171], v225, s[40:43], s60 offen sc1\n" \
    "buffer_load_dwordx4 v[172:175], v225, s[40:43], s85 offen sc1\n" \
    "v_mov_b64 v[176:177], 0\n" \
    "v_mov_b64 v[178:179], 0\n" \
    "v_mov_b64 v[180:181], 0\n" \
    "v_mov_b64 v[182:183], 0\n" \
    "v_mov_b64 v[184:185], 0\n" \
    "v_mov_b64 v[186:187], 0\n" \
    "v_mov_b64 v[188:189], 0\n" \
    "v_mov_b64 v[190:191], 0\n" \
    "s_branch L%=_Tsc\n" \
    "L%=_Tvslow:\n" \
    "s_nop 7\n" \
    "s_nop 0\n" \
    "v_max_u32 v136, v168, v169\n" \
    "v_max3_u32 v136, v136, v170, v171\n" \
    "v_max3_u32 v136, v136, v172, v173\n" \
    "v_max3_u32 v136, v136, v174, v175\n" \
    "v_cmp_eq_u32 vcc, -1, v136\n" \
    "s_cbranch_vccz L%=_Tvok\n" \
    "s_cmp_eq_u32 s99, 0\n" \
    "s_cbranch_scc1 L%=_Tvok\n" \
    "s_sub_u32 s99, s99, 1\n" \
    "s_cmp_lg_u32 s99, 0\n" \
    "s_cbranch_scc1 L%=_Tu6\n" \
    "v_mov_b32 v136, 1\n" \
    "buffer_store_dword v136, off, s[44:47], 0 sc1\n" \
    "L%=_Tu6:\n" \
    "s_sleep 1\n" \
    "buffer_load_dwordx4 v[168:171], v225, s[40:43], s60 offen sc1\n" \
    "buffer_load_dwordx4 v[172:175], v225, s[40:43], s85 offen sc1\n" \
    "v_mov_b64 v[176:177], 0\n" \
    "v_mov_b64 v[178:179], 0\n" \
    "v_mov_b64 v[180:181], 0\n" \
    "v_mov_b64 v[182:183], 0\n" \
    "v_mov_b64 v[184:185], 0\n" \
    "v_mov_b64 v[186:187], 0\n" \
    "v_mov_b64 v[188:189], 0\n" \
    "v_mov_b64 v[190:191], 0\n" \
    "v_mov_b64 v[196:197], 0\n" \
    "v_mov_b64 v[198:199], 0\n" \
    "v_mov_b64 v[200:201], 0\n" \
    "v_mov_b64 v[202:203], 0\n" \
    "v_mov_b64 v[204:205], 0\n" \
    "v_mov_b64 v[206:207], 0\n" \
    "v_mov_b64 v[208:209], 0\n" \
    "v_mov_b64 v[210:211], 0\n" \
    "v_mov_b64 v[212:213], 0\n" \
    "v_mov_b64 v[214:215], 0\n" \
    "s_waitcnt vmcnt(0)\n" \
    "v_smfmac_f32_16x16x64_bf16 v[176:179], v[168:171], v[0:7], v224\n" \
    "v_smfmac_f32_16x16x64_bf16 v[180:183], v[168:171], v[8:15], v224\n" \
    "v_smfmac_f32_16x16x64_bf16 v[176:179], v[172:175], v[32:39], v224\n" \
    "s_nop 7\n" \
    "s_nop 0\n" \
    "s_branch L%=_Tvc\n" \
    "L%=_Tfin:\n" \
    "v_mov_b32 v143, 64\n" \
    "s_barrier\n" \
    "buffer_store_dword v143, v233, s[44:47], 0 offen sc1\n" \
    "s_branch L%=_end\n" \
    "L%=_Tu3s:\n" \
    "s_mov_b32 s77, s79\n" \
    "s_memtime s[38:39]\n" \
    "s_memrealtime vcc\n" \
    "s_nop 4\n" \
    "s_memrealtime s[32:33]\n" \
    "s_nop 4\n" \
    "s_memrealtime s[34:35]\n" \
    "s_nop 4\n" \
    "s_memrealtime s[36:37]\n" \
    "s_add_u32 s80, s77, s64\n" \
    "s_lshl_b32 s80, s80, 2\n" \
    "s_waitcnt lgkmcnt(0)\n" \
    "s_sub_u32 s80, s80, vcc_lo\n" \
    "s_sub_u32 s80, s80, s32\n" \
    "s_sub_u32 s80, s80, s34\n" \
    "s_sub_u32 s80, s80, s36\n" \
    "s_mul_i32 s80, s80, s71\n" \
    "s_ashr_i32 s80, s80, 6\n" \
    "s_add_u32 s78, s80, s38\n" \
    "s_branch L%=_Tu3k\n" \
    "L%=_Tu4s:\n" \
    "s_cmp_gt_u32 s79, 0x8000\n" \
    "s_cbranch_scc1 L%=_Tu4e\n" \
    "s_sleep 2\n" \
    "s_branch L%=_Tu4a\n" \
    "L%=_S:\n" \
    "s_mov_b32 s76, 0\n" \
    "s_mov_b32 s99, 0x20000\n" \
    "s_mov_b32 s84, 0\n" \
    "s_mov_b32 s81, 0\n" \
    "s_mov_b32 s86, 0\n" \
    "s_mov_b32 s87, 0\n" \
    "s_mov_b32 s88, 0\n" \
    "s_mov_b32 s89, 0\n" \
    "s_mov_b32 s90, 0\n" \
    "s_mov_b32 s91, 0\n" \
    "s_mov_b32 s92, 0\n" \
    "s_mov_b32 s93, 0\n" \
    "s_mov_b32 s94, 0\n" \
    "s_mov_b32 s95, 0\n" \
    "s_mov_b32 s96, 0\n" \
    "s_mov_b32 s97, 0\n" \
    "s_mov_b32 s98, 0\n" \
    "ds_read_b32 v216, v229\n" \
    "s_waitcnt lgkmcnt(0)\n" \
    "v_readfirstlane_b32 s94, v216\n" \
    "s_mul_i32 s79, s94, s70\n" \
    "s_add_u32 s79, s79, s81\n" \
    "s_mov_b32 s77, s79\n" \
    "s_mul_i32 s96, s65, s71\n" \
    "s_lshr_b32 s96, s96, 4\n" \
    "v_mov_b32 v96, 0x3f803f80\n" \
    "v_mov_b32 v97, 0x3f803f80\n" \
    "v_mov_b32 v98, 0x3f803f80\n" \
    "v_mov_b32 v99, 0x3f803f80\n" \
    "v_mov_b32 v100, 0x3f803f80\n" \
    "v_mov_b32 v101, 0x3f803f80\n" \
    "v_mov_b64 v[102:103], 0\n" \
    "v_mov_b64 v[64:65], 0\n" \
    "v_mov_b64 v[66:67], 0\n" \
    "v_mov_b64 v[68:69], 0\n" \
    "v_mov_b64 v[70:71], 0\n" \
    "v_mov_b64 v[72:73], 0\n" \
    "v_mov_b64 v[74:75], 0\n" \
    "v_mov_b64 v[76:77], 0\n" \
    "v_mov_b64 v[78:79], 0\n" \
    "v_mov_b64 v[80:81], 0\n" \
    "v_mov_b64 v[82:83], 0\n" \
    "v_mov_b64 v[84:85], 0\n" \
    "v_mov_b64 v[86:87], 0\n" \
    "v_mov_b64 v[88:89], 0\n" \
    "v_mov_b64 v[90:91], 0\n" \
    "v_mov_b64 v[92:93], 0\n" \
    "v_mov_b64 v[94:95], 0\n" \
    "v_mov_b64 v[192:193], 0\n" \
    "v_mov_b64 v[194:195], 0\n" \
    "L%=_Sloop:\n" \
    "ds_read_b64_tr_b16 v[104:105], v228 offset:16384\n" \
    "ds_read_b64_tr_b16 v[106:107], v228 offset:16416\n" \
    "ds_read_b64_tr_b16 v[108:109], v228 offset:18688\n" \
    "ds_read_b64_tr_b16 v[110:111], v228 offset:18720\n" \
    "ds_read_b64_tr_b16 v[112:113], v228 offset:20992\n" \
    "ds_read_b64_tr_b16 v[114:115], v228 offset:21024\n" \
    "ds_read_b64_tr_b16 v[116:117], v228 offset:23296\n" \
    "ds_read_b64_tr_b16 v[118:119], v228 offset:23328\n" \
    "ds_read_b64_tr_b16 v[120:121], v228 offset:16656\n" \
    "ds_read_b64_tr_b16 v[122:123], v228 offset:16688\n" \
    "ds_read_b64_tr_b16 v[124:125], v228 offset:18960\n" \
    "ds_read_b64_tr_b16 v[126:127], v228 offset:18992\n" \
    "ds_read_b64_tr_b16 v[128:129], v228 offset:21264\n" \
    "ds_read_b64_tr_b16 v[130:131], v228 offset:21296\n" \
    "ds_read_b64_tr_b16 v[132:133], v228 offset:23568\n" \
    "ds_read_b64_tr_b16 v[134:135], v228 offset:23600\n" \
    "s_waitcnt lgkmcnt(14)\n" \
    "v_mfma_f32_16x16x32_bf16 v[136:139], v[104:107], v[0:3], 0\n" \
    "v_mfma_f32_16x16x32_bf16 v[144:147], v[104:107], v[8:11], 0\n" \
    "v_mfma_f32_16x16x32_bf16 v[152:155], v[104:107], v[16:19], 0\n" \
    "v_mfma_f32_16x16x32_bf16 v[160:163], v[104:107], v[24:27], 0\n" \
    "s_waitcnt lgkmcnt(12)\n" \
    "v_mfma_f32_16x16x32_bf16 v[136:139], v[108:111], v[4:7], v[136:139]\n" \
    "v_mfma_f32_16x16x32_bf16 v[144:147], v[108:111], v[12:15], v[144:147]\n" \
    "v_mfma_f32_16x16x32_bf16 v[152:155], v[108:111], v[20:23], v[152:155]\n" \
    "v_mfma_f32_16x16x32_bf16 v[160:163], v[108:111], v[28:31], v[160:163]\n" \
    "s_waitcnt lgkmcnt(10)\n" \
    "v_mfma_f32_16x16x32_bf16 v[136:139], v[112:115], v[32:35], v[136:139]\n" \
    "v_mfma_f32_16x16x32_bf16 v[144:147], v[112:115], v[40:43], v[144:147]\n" \
    "v_mfma_f32_16x16x32_bf16 v[152:155], v[112:115], v[48:51], v[152:155]\n" \
    "v_mfma_f32_16x16x32_bf16 v[160:163], v[112:115], v[56:59], v[160:163]\n" \
    "s_waitcnt lgkmcnt(8)\n" \
    "v_mfma_f32_16x16x32_bf16 v[136:139], v[116:119], v[36:39], v[136:139]\n" \
    "v_mfma_f32_16x16x32_bf16 v[144:147], v[116:119], v[44:47], v[144:147]\n" \
    "v_mfma_f32_16x16x32_bf16 v[152:155], v[116:119], v[52:55], v[152:155]\n" \
    "v_mfma_f32_16x16x32_bf16 v[160:163], v[116:119], v[60:63], v[160:163]\n" \
    "s_waitcnt lgkmcnt(6)\n" \
    "v_mfma_f32_16x16x32_bf16 v[140:143], v[120:123], v[0:3], 0\n" \
    "v_mfma_f32_16x16x32_bf16 v[148:151], v[120:123], v[8:11], 0\n" \
    "v_mfma_f32_16x16x32_bf16 v[156:159], v[120:123], v[16:19], 0\n" \
    "v_mfma_f32_16x16x32_bf16 v[164:167], v[120:123], v[24:27], 0\n" \
    "s_waitcnt lgkmcnt(4)\n" \
    "v_mfma_f32_16x16x32_bf16 v[140:143], v[124:127], v[4:7], v[140:143]\n" \
    "v_mfma_f32_16x16x32_bf16 v[148:151], v[124:127], v[12:15], v[148:151]\n" \
    "v_mfma_f32_16x16x32_bf16 v[156:159], v[124:127], v[20:23], v[156:159]\n" \
    "v_mfma_f32_16x16x32_bf16 v[164:167], v[124:127], v[28:31], v[164:167]\n" \
    "s_waitcnt lgkmcnt(2)\n" \
    "v_mfma_f32_16x16x32_bf16 v[140:143], v[128:131], v[32:35], v[140:143]\n" \
    "v_mfma_f32_16x16x32_bf16 v[148:151], v[128:131], v[40:43], v[148:151]\n" \
    "v_mfma_f32_16x16x32_bf16 v[156:159], v[128:131], v[48:51], v[156:159]\n" \
    "v_mfma_f32_16x16x32_bf16 v[164:167], v[128:131], v[56:59], v[164:167]\n" \
    "s_waitcnt lgkmcnt(0)\n" \
    "v_mfma_f32_16x16x32_bf16 v[140:143], v[132:135], v[36:39], v[140:143]\n" \
    "v_mfma_f32_16x16x32_bf16 v[148:151], v[132:135], v[44:47], v[148:151]\n" \
    "v_mfma_f32_16x16x32_bf16 v[156:159], v[132:135], v[52:55], v[156:159]\n" \
    "v_mfma_f32_16x16x32_bf16 v[164:167], v[132:135], v[60:63], v[164:167]\n" \
    "v_cvt_pk_bf16_f32 v65, v136, v137\n" \
    "v_cvt_pk_bf16_f32 v68, v138, v139\n" \
    "v_cvt_pk_bf16_f32 v73, v144, v145\n" \
    "v_cvt_pk_bf16_f32 v76, v146, v147\n" \
    "v_cvt_pk_bf16_f32 v81, v152, v153\n" \
    "v_cvt_pk_bf16_f32 v84, v154, v155\n" \
    "v_cvt_pk_bf16_f32 v89, v160, v161\n" \
    "v_cvt_pk_bf16_f32 v92, v162, v163\n" \
    "v_cvt_pk_bf16_f32 v66, v142, v143\n" \
    "v_cvt_pk_bf16_f32 v67, v142, v143\n" \
    "v_cvt_pk_bf16_f32 v69, v140, v141\n" \
    "v_cvt_pk_bf16_f32 v74, v150, v151\n" \
    "v_cvt_pk_bf16_f32 v75, v150, v151\n" \
    "v_cvt_pk_bf16_f32 v77, v148, v149\n" \
    "v_cvt_pk_bf16_f32 v82, v158, v159\n" \
    "v_cvt_pk_bf16_f32 v83, v158, v159\n" \
    "v_cvt_pk_bf16_f32 v85, v156, v157\n" \
    "v_cvt_pk_bf16_f32 v90, v166, v167\n" \
    "v_cvt_pk_bf16_f32 v91, v166, v167\n" \
    "v_cvt_pk_bf16_f32 v93, v164, v165\n" \
    "v_permlane16_swap_b32 v66, v67\n" \
    "v_permlane16_swap_b32 v74, v75\n" \
    "v_permlane16_swap_b32 v82, v83\n" \
    "v_permlane16_swap_b32 v90, v91\n" \
    "ds_read_b128 v[240:243], v234 offset:25600\n" \
    "ds_read_b128 v[244:247], v234 offset:26624\n" \
    "v_mov_b64 v[176:177], 0\n" \
    "v_mov_b64 v[178:179], 0\n" \
    "v_mov_b64 v[180:181], 0\n" \
    "v_mov_b64 v[182:183], 0\n" \
    "v_mov_b64 v[184:185], 0\n" \
    "v_mov_b64 v[186:187], 0\n" \
    "v_mov_b64 v[188:189], 0\n" \
    "v_mov_b64 v[190:191], 0\n" \
    "v_mov_b64 v[196:197], 0\n" \
    "v_mov_b64 v[198:199], 0\n" \
    "v_mov_b64 v[200:201], 0\n" \
    "v_mov_b64 v[202:203], 0\n" \
    "v_mov_b64 v[204:205], 0\n" \
    "v_mov_b64 v[206:207], 0\n" \
    "v_mov_b64 v[208:209], 0\n" \
    "v_mov_b64 v[210:211], 0\n" \
    "v_mov_b64 v[212:213], 0\n" \
    "v_mov_b64 v[214:215], 0\n" \
    "s_add_u32 s85, s60, 0x1000\n" \
    "s_branch L%=_Su2b\n" \
    "L%=_Su2a:\n" \
    "s_sleep 4\n" \
    "L%=_Su2b:\n" \
    "s_memrealtime vcc\n" \
    "s_add_u32 s80, s77, s64\n" \
    "s_add_u32 s80, s80, s69\n" \
    "s_waitcnt lgkmcnt(0)\n" \
    "s_sub_i32 s79, s80, vcc_lo\n" \
    "s_sub_u32 s79, s79, 40\n" \
    "s_cmp_lt_u32 s79, 0x400\n" \
    "s_cbranch_scc1 L%=_Su2a\n" \
    "s_memtime s[38:39]\n" \
    "s_memrealtime vcc\n" \
    "s_nop 4\n" \
    "s_memrealtime s[32:33]\n" \
    "s_nop 4\n" \
    "s_memrealtime s[34:35]\n" \
    "s_nop 4\n" \
    "s_memrealtime s[36:37]\n" \
    "s_add_u32 s80, s77, s64\n" \
    "s_lshl_b32 s80, s80, 2\n" \
    "s_waitcnt lgkmcnt(0)\n" \
    "s_sub_u32 s80, s80, vcc_lo\n" \
    "s_sub_u32 s80, s80, s32\n" \
    "s_sub_u32 s80, s80, s34\n" \
    "s_sub_u32 s80, s80, s36\n" \
    "s_mul_i32 s80, s80, s71\n" \
    "s_ashr_i32 s80, s80, 6\n" \
    "s_add_u32 s78, s80, s38\n" \
    "L%=_Su3a:\n" \
    "s_memtime s[38:39]\n" \
    "s_mul_i32 s80, s69, 23\n" \
    "s_add_u32 s80, s80, s78\n" \
    "s_waitcnt lgkmcnt(0)\n" \
    "s_sub_i32 s79, s80, s38\n" \
    "s_max_i32 s79, s79, 0\n" \
    "s_cmp_gt_u32 s79, 320\n" \
    "s_cbranch_scc1 L%=_Su3s\n" \
    ".p2align 4\n" \
    "L%=_Su3d:\n" \
    "s_sub_u32 s79, s79, 28\n" \
    "s_cbranch_scc0 L%=_Su3d\n" \
    "L%=_Su3e:\n" \
    "s_sub_u32 s79, s84, 0x20000\n" \
    "s_cselect_b32 s79, 0x40000000, s79\n" \
    "buffer_store_dwordx4 v[236:239], v233, s[48:51], s79 offen sc1\n" \
    "s_setprio 3\n" \
    "s_barrier\n" \
    "L%=_Srd:\n" \
    "ds_read_b128 v[136:139], v229 offset:32\n" \
    "ds_read_b128 v[104:107], v230 offset:0\n" \
    "ds_read_b128 v[108:111], v230 offset:2176\n" \
    "ds_read_b128 v[112:115], v230 offset:4352\n" \
    "ds_read_b128 v[116:119], v230 offset:6528\n" \
    "ds_read_b128 v[120:123], v230 offset:1088\n" \
    "ds_read_b128 v[124:127], v230 offset:3264\n" \
    "ds_read_b128 v[128:131], v230 offset:5440\n" \
    "ds_read_b128 v[132:135], v230 offset:7616\n" \
    "s_waitcnt lgkmcnt(8)\n" \
    "v_min3_u32 v136, v136, v137, v138\n" \
    "v_min_u32 v136, v136, v139\n" \
    "v_cmp_ge_u32 vcc, s76, v136\n" \
    "s_cbranch_vccnz L%=_Srd\n" \
    "s_waitcnt lgkmcnt(6)\n" \
    "v_pk_add_f32 v[104:105], v[104:105], v[108:109]\n" \
    "v_pk_add_f32 v[106:107], v[106:107], v[110:111]\n" \
    "s_waitcnt lgkmcnt(4)\n" \
    "v_pk_add_f32 v[112:113], v[112:113], v[116:117]\n" \
    "v_pk_add_f32 v[114:115], v[114:115], v[118:119]\n" \
    "v_pk_add_f32 v[104:105], v[104:105], v[112:113]\n" \
    "v_pk_add_f32 v[106:107], v[106:107], v[114:115]\n" \
    "v_cvt_pk_bf16_f32 v236, v104, v105\n" \
    "v_cvt_pk_bf16_f32 v237, v106, v107\n" \
    "s_waitcnt lgkmcnt(2)\n" \
    "v_pk_add_f32 v[120:121], v[120:121], v[124:125]\n" \
    "v_pk_add_f32 v[122:123], v[122:123], v[126:127]\n" \
    "s_waitcnt lgkmcnt(0)\n" \
    "v_pk_add_f32 v[128:129], v[128:129], v[132:133]\n" \
    "v_pk_add_f32 v[130:131], v[130:131], v[134:135]\n" \
    "v_pk_add_f32 v[120:121], v[120:121], v[128:129]\n" \
    "v_pk_add_f32 v[122:123], v[122:123], v[130:131]\n" \
    "v_cvt_pk_bf16_f32 v238, v120, v121\n" \
    "v_cvt_pk_bf16_f32 v239, v122, v123\n" \
    "s_nop 1\n" \
    "v_mfma_f32_16x16x32_bf16 v[140:143], v[240:243], v[236:239], 0\n" \
    "v_mfma_f32_16x16x32_bf16 v[144:147], v[244:247], v[236:239], 0\n" \
    "v_mfma_f32_16x16x32_bf16 v[148:151], v[236:239], v[236:239], 0\n" \
    "buffer_store_dwordx4 v[140:143], v231, s[40:43], s60 offen sc1\n" \
    "s_nop 2\n" \
    "buffer_store_dwordx4 v[144:147], v231, s[40:43], s60 offen offset:512 sc1\n" \
    "s_nop 2\n" \
    "buffer_store_dwordx4 v[148:151], v232, s[40:43], s60 offen sc1\n" \
    "s_cmp_eq_u32 s76, 63\n" \
    "s_cbranch_scc1 L%=_Sfin\n" \
    "ds_read_b32 v216, v229\n" \
    "s_waitcnt lgkmcnt(0)\n" \
    "v_readfirstlane_b32 s94, v216\n" \
    "s_mul_i32 s79, s94, s70\n" \
    "s_add_u32 s79, s79, s81\n" \
    "s_cmp_lg_u32 s79, s77\n" \
    "s_cbranch_scc1 L%=_Su4s\n" \
    "L%=_Su4k:\n" \
    "L%=_Su5a:\n" \
    "s_memtime s[38:39]\n" \
    "s_mul_hi_u32 s80, s67, s96\n" \
    "s_add_u32 s80, s80, s78\n" \
    "s_waitcnt lgkmcnt(0)\n" \
    "s_sub_i32 s79, s80, s38\n" \
    "s_max_i32 s79, s79, 0\n" \
    "s_cmp_gt_u32 s79, 320\n" \
    "s_cbranch_scc1 L%=_Su5s\n" \
    ".p2align 4\n" \
    "L%=_Su5d:\n" \
    "s_sub_u32 s79, s79, 28\n" \
    "s_cbranch_scc0 L%=_Su5d\n" \
    "L%=_Su5e:\n" \
    "buffer_load_dwordx4 v[168:171], v225, s[40:43], s60 offen sc1\n" \
    "buffer_load_dwordx4 v[172:175], v225, s[40:43], s85 offen sc1\n" \
    "L%=_Svc:\n" \
    "s_waitcnt vmcnt(1)\n" \
    "v_smfmac_f32_16x16x64_bf16 v[176:179], v[168:171], v[0:7], v224\n" \
    "v_smfmac_f32_16x16x64_bf16 v[180:183], v[168:171], v[8:15], v224\n" \
    "s_waitcnt vmcnt(0)\n" \
    "v_smfmac_f32_16x16x64_bf16 v[176:179], v[172:175], v[32:39], v224\n" \
    "v_smfmac_f32_16x16x64_bf16 v[180:183], v[172:175], v[40:47], v224\n" \
    "v_smfmac_f32_16x16x64_bf16 v[184:187], v[168:171], v[16:23], v224\n" \
    "v_smfmac_f32_16x16x64_bf16 v[188:191], v[168:171], v[24:31], v224\n" \
    "v_add_f32 v176, v176, v178\n" \
    "v_smfmac_f32_16x16x64_bf16 v[184:187], v[172:175], v[48:55], v224\n" \
    "v_add_f32 v177, v177, v179\n" \
    "v_smfmac_f32_16x16x64_bf16 v[188:191], v[172:175], v[56:63], v224\n" \
    "v_cmp_u_f32 vcc, v176, v177\n" \
    "v_cvt_pk_bf16_f32 v64, v176, v177\n" \
    "v_add_f32 v180, v180, v182\n" \
    "v_add_f32 v181, v181, v183\n" \
    "v_cvt_pk_bf16_f32 v72, v180, v181\n" \
    "s_cbranch_vccnz L%=_Svslow\n" \
    "L%=_Svok:\n" \
    "v_add_f32 v184, v184, v186\n" \
    "v_add_f32 v185, v185, v187\n" \
    "v_cvt_pk_bf16_f32 v80, v184, v185\n" \
    "v_add_f32 v188, v188, v190\n" \
    "v_add_f32 v189, v189, v191\n" \
    "v_cvt_pk_bf16_f32 v88, v188, v189\n" \
    "s_barrier\n" \
    "L%=_Sfr:\n" \
    "ds_read_b128 v[192:195], v227\n" \
    "s_waitcnt lgkmcnt(0)\n" \
    "v_smfmac_f32_16x16x64_bf16 v[212:215], v[192:195], v[96:103], v224\n" \
    "v_cmp_ne_u32 vcc, s76, v195\n" \
    "v_smfmac_f32_16x16x64_bf16 v[196:199], v[192:195], v[64:71], v224\n" \
    "s_cbranch_vccnz L%=_Sfs\n" \
    "v_smfmac_f32_16x16x64_bf16 v[200:203], v[192:195], v[72:79], v224\n" \
    "v_smfmac_f32_16x16x64_bf16 v[204:207], v[192:195], v[80:87], v224\n" \
    "v_add_f32 v212, v212, v214\n" \
    "v_smfmac_f32_16x16x64_bf16 v[208:211], v[192:195], v[88:95], v224\n" \
    "v_add_f32 v213, v213, v215\n" \
    "v_rcp_f32 v220, v212\n" \
    "v_rcp_f32 v221, v213\n" \
    "v_add_f32 v196, v196, v198\n" \
    "v_add_f32 v197, v197, v199\n" \
    "v_pk_add_f32 v[200:201], v[200:201], v[202:203]\n" \
    "v_pk_add_f32 v[204:205], v[204:205], v[206:207]\n" \
    "s_nop 0\n" \
    "v_pk_add_f32 v[208:209], v[208:209], v[210:211]\n" \
    "v_pk_mul_f32 v[196:197], v[196:197], v[220:221]\n" \
    "v_pk_mul_f32 v[200:201], v[200:201], v[220:221]\n" \
    "v_pk_mul_f32 v[204:205], v[204:205], v[220:221]\n" \
    "v_pk_mul_f32 v[208:209], v[208:209], v[220:221]\n" \
    "v_cvt_pk_bf16_f32 v216, v196, v200\n" \
    "v_cvt_pk_bf16_f32 v217, v204, v208\n" \
    "v_cvt_pk_bf16_f32 v218, v197, v201\n" \
    "v_cvt_pk_bf16_f32 v219, v205, v209\n" \
    "buffer_store_dwordx4 v[216:219], v226, s[40:43], s60 offen sc1\n" \
    "s_setprio 0\n" \
    "ds_read_b64 v[220:221], v229 offset:64\n" \
    "s_mov_b32 s79, s60\n" \
    "s_mov_b32 s60, s61\n" \
    "s_mov_b32 s61, s62\n" \
    "s_mov_b32 s62, s63\n" \
    "s_mov_b32 s63, s79\n" \
    "s_add_u32 s76, s76, 1\n" \
    "s_add_u32 s77, s77, s65\n" \
    "s_add_u32 s84, s84, 0x20000\n" \
    "s_add_u32 s81, s81, s65\n" \
    "s_waitcnt lgkmcnt(0)\n" \
    "v_readfirstlane_b32 s65, v220\n" \
    "v_readfirstlane_b32 s71, v221\n" \
    "s_mul_i32 s96, s65, s71\n" \
    "s_lshr_b32 s96, s96, 4\n" \
    "s_branch L%=_Sloop\n" \
    "L%=_Sfin:\n" \
    "buffer_store_dwordx4 v[236:239], v233, s[48:51], s84 offen sc1\n" \
    "s_waitcnt vmcnt(0)\n" \
    "s_barrier\n" \
    "s_branch L%=_end\n" \
    "L%=_Su3s:\n" \
    "s_cmp_gt_u32 s79, 0x8000\n" \
    "s_cbranch_scc1 L%=_Su3e\n" \
    "s_sleep 2\n" \
    "s_branch L%=_Su3a\n" \
    "L%=_Su4s:\n" \
    "s_mov_b32 s77, s79\n" \
    "s_memtime s[38:39]\n" \
    "s_memrealtime vcc\n" \
    "s_nop 4\n" \
    "s_memrealtime s[32:33]\n" \
    "s_nop 4\n" \
    "s_memrealtime s[34:35]\n" \
    "s_nop 4\n" \
    "s_memrealtime s[36:37]\n" \
    "s_add_u32 s80, s77, s64\n" \
    "s_lshl_b32 s80, s80, 2\n" \
    "s_waitcnt lgkmcnt(0)\n" \
    "s_sub_u32 s80, s80, vcc_lo\n" \
    "s_sub_u32 s80, s80, s32\n" \
    "s_sub_u32 s80, s80, s34\n" \
    "s_sub_u32 s80, s80, s36\n" \
    "s_mul_i32 s80, s80, s71\n" \
    "s_ashr_i32 s80, s80, 6\n" \
    "s_add_u32 s78, s80, s38\n" \
    "s_branch L%=_Su4k\n" \
    "L%=_Su5s:\n" \
    "s_cmp_gt_u32 s79, 0x8000\n" \
    "s_cbranch_scc1 L%=_Su5e\n" \
    "s_sleep 2\n" \
    "s_branch L%=_Su5a\n" \
    "L%=_Svslow:\n" \
    "s_nop 7\n" \
    "s_nop 0\n" \
    "v_max_u32 v216, v168, v169\n" \
    "v_max3_u32 v216, v216, v170, v171\n" \
    "v_max3_u32 v216, v216, v172, v173\n" \
    "v_max3_u32 v216, v216, v174, v175\n" \
    "v_cmp_eq_u32 vcc, -1, v216\n" \
    "s_cbranch_vccz L%=_Svok\n" \
    "s_cmp_eq_u32 s99, 0\n" \
    "s_cbranch_scc1 L%=_Svok\n" \
    "s_sub_u32 s99, s99, 1\n" \
    "s_cmp_lg_u32 s99, 0\n" \
    "s_cbranch_scc1 L%=_Su6\n" \
    "v_mov_b32 v216, 1\n" \
    "buffer_store_dword v216, off, s[44:47], 0 sc1\n" \
    "L%=_Su6:\n" \
    "s_sleep 1\n" \
    "buffer_load_dwordx4 v[168:171], v225, s[40:43], s60 offen sc1\n" \
    "buffer_load_dwordx4 v[172:175], v225, s[40:43], s85 offen sc1\n" \
    "v_mov_b64 v[176:177], 0\n" \
    "v_mov_b64 v[178:179], 0\n" \
    "v_mov_b64 v[180:181], 0\n" \
    "v_mov_b64 v[182:183], 0\n" \
    "v_mov_b64 v[184:185], 0\n" \
    "v_mov_b64 v[186:187], 0\n" \
    "v_mov_b64 v[188:189], 0\n" \
    "v_mov_b64 v[190:191], 0\n" \
    "s_waitcnt vmcnt(0)\n" \
    "s_branch L%=_Svc\n" \
    "L%=_Sfs:\n" \
    "s_nop 7\n" \
    "s_nop 0\n" \
    "v_mov_b64 v[196:197], 0\n" \
    "v_mov_b64 v[198:199], 0\n" \
    "v_mov_b64 v[200:201], 0\n" \
    "v_mov_b64 v[202:203], 0\n" \
    "v_mov_b64 v[204:205], 0\n" \
    "v_mov_b64 v[206:207], 0\n" \
    "v_mov_b64 v[208:209], 0\n" \
    "v_mov_b64 v[210:211], 0\n" \
    "v_mov_b64 v[212:213], 0\n" \
    "v_mov_b64 v[214:215], 0\n" \
    "s_branch L%=_Sfr\n" \
    "L%=_R:\n" \
    "s_mov_b32 s76, 0\n" \
    "s_mov_b32 s99, 0x20000\n" \
    "s_mov_b32 s84, 0\n" \
    "s_mov_b32 s81, 0\n" \
    "s_mov_b32 s86, 0\n" \
    "s_mov_b32 s87, 0\n" \
    "s_mov_b32 s88, 0\n" \
    "s_mov_b32 s89, 0\n" \
    "s_mov_b32 s90, 0\n" \
    "s_mov_b32 s91, 0\n" \
    "s_mov_b32 s92, 0\n" \
    "s_mov_b32 s93, 0\n" \
    "s_mov_b32 s94, 0\n" \
    "s_mov_b32 s95, 0\n" \
    "s_mov_b32 s96, 0\n" \
    "s_mov_b32 s97, 0\n" \
    "s_mov_b32 s98, 0\n" \
    "ds_read_b32 v216, v229\n" \
    "s_waitcnt lgkmcnt(0)\n" \
    "v_readfirstlane_b32 s94, v216\n" \
    "s_mul_i32 s79, s94, s70\n" \
    "s_add_u32 s79, s79, s81\n" \
    "s_mov_b32 s77, s79\n" \
    "s_mul_i32 s96, s65, s71\n" \
    "s_lshr_b32 s96, s96, 4\n" \
    "v_mov_b32 v96, 0x3f803f80\n" \
    "v_mov_b32 v97, 0x3f803f80\n" \
    "v_mov_b32 v98, 0x3f803f80\n" \
    "v_mov_b32 v99, 0x3f803f80\n" \
    "v_mov_b32 v100, 0x3f803f80\n" \
    "v_mov_b32 v101, 0x3f803f80\n" \
    "v_mov_b64 v[102:103], 0\n" \
    "v_mov_b64 v[64:65], 0\n" \
    "v_mov_b64 v[66:67], 0\n" \
    "v_mov_b64 v[68:69], 0\n" \
    "v_mov_b64 v[70:71], 0\n" \
    "v_mov_b64 v[72:73], 0\n" \
    "v_mov_b64 v[74:75], 0\n" \
    "v_mov_b64 v[76:77], 0\n" \
    "v_mov_b64 v[78:79], 0\n" \
    "v_mov_b64 v[80:81], 0\n" \
    "v_mov_b64 v[82:83], 0\n" \
    "v_mov_b64 v[84:85], 0\n" \
    "v_mov_b64 v[86:87], 0\n" \
    "v_mov_b64 v[88:89], 0\n" \
    "v_mov_b64 v[90:91], 0\n" \
    "v_mov_b64 v[92:93], 0\n" \
    "v_mov_b64 v[94:95], 0\n" \
    "v_mov_b64 v[192:193], 0\n" \
    "v_mov_b64 v[194:195], 0\n" \
    "v_mov_b32 v222, v236\n" \
    "v_mov_b32 v223, 0\n" \
    "ds_read_b64_tr_b16 v[104:105], v228 offset:16384\n" \
    "ds_read_b64_tr_b16 v[106:107], v228 offset:16416\n" \
    "ds_read_b64_tr_b16 v[108:109], v228 offset:18688\n" \
    "ds_read_b64_tr_b16 v[110:111], v228 offset:18720\n" \
    "ds_read_b64_tr_b16 v[112:113], v228 offset:20992\n" \
    "ds_read_b64_tr_b16 v[114:115], v228 offset:21024\n" \
    "ds_read_b64_tr_b16 v[116:117], v228 offset:23296\n" \
    "ds_read_b64_tr_b16 v[118:119], v228 offset:23328\n" \
    "ds_read_b64_tr_b16 v[120:121], v228 offset:16656\n" \
    "ds_read_b64_tr_b16 v[122:123], v228 offset:16688\n" \
    "ds_read_b64_tr_b16 v[124:125], v228 offset:18960\n" \
    "ds_read_b64_tr_b16 v[126:127], v228 offset:18992\n" \
    "ds_read_b64_tr_b16 v[128:129], v228 offset:21264\n" \
    "ds_read_b64_tr_b16 v[130:131], v228 offset:21296\n" \
    "ds_read_b64_tr_b16 v[132:133], v228 offset:23568\n" \
    "ds_read_b64_tr_b16 v[134:135], v228 offset:23600\n" \
    "s_waitcnt lgkmcnt(14)\n" \
    "v_mfma_f32_16x16x32_bf16 v[136:139], v[104:107], v[0:3], 0\n" \
    "v_mfma_f32_16x16x32_bf16 v[144:147], v[104:107], v[8:11], 0\n" \
    "v_mfma_f32_16x16x32_bf16 v[152:155], v[104:107], v[16:19], 0\n" \
    "v_mfma_f32_16x16x32_bf16 v[160:163], v[104:107], v[24:27], 0\n" \
    "s_waitcnt lgkmcnt(12)\n" \
    "v_mfma_f32_16x16x32_bf16 v[136:139], v[108:111], v[4:7], v[136:139]\n" \
    "v_mfma_f32_16x16x32_bf16 v[144:147], v[108:111], v[12:15], v[144:147]\n" \
    "v_mfma_f32_16x16x32_bf16 v[152:155], v[108:111], v[20:23], v[152:155]\n" \
    "v_mfma_f32_16x16x32_bf16 v[160:163], v[108:111], v[28:31], v[160:163]\n" \
    "s_waitcnt lgkmcnt(10)\n" \
    "v_mfma_f32_16x16x32_bf16 v[136:139], v[112:115], v[32:35], v[136:139]\n" \
    "v_mfma_f32_16x16x32_bf16 v[144:147], v[112:115], v[40:43], v[144:147]\n" \
    "v_mfma_f32_16x16x32_bf16 v[152:155], v[112:115], v[48:51], v[152:155]\n" \
    "v_mfma_f32_16x16x32_bf16 v[160:163], v[112:115], v[56:59], v[160:163]\n" \
    "s_waitcnt lgkmcnt(8)\n" \
    "v_mfma_f32_16x16x32_bf16 v[136:139], v[116:119], v[36:39], v[136:139]\n" \
    "v_mfma_f32_16x16x32_bf16 v[144:147], v[116:119], v[44:47], v[144:147]\n" \
    "v_mfma_f32_16x16x32_bf16 v[152:155], v[116:119], v[52:55], v[152:155]\n" \
    "v_mfma_f32_16x16x32_bf16 v[160:163], v[116:119], v[60:63], v[160:163]\n" \
    "s_waitcnt lgkmcnt(6)\n" \
    "v_mfma_f32_16x16x32_bf16 v[140:143], v[120:123], v[0:3], 0\n" \
    "v_mfma_f32_16x16x32_bf16 v[148:151], v[120:123], v[8:11], 0\n" \
    "v_mfma_f32_16x16x32_bf16 v[156:159], v[120:123], v[16:19], 0\n" \
    "v_mfma_f32_16x16x32_bf16 v[164:167], v[120:123], v[24:27], 0\n" \
    "s_waitcnt lgkmcnt(4)\n" \
    "v_mfma_f32_16x16x32_bf16 v[140:143], v[124:127], v[4:7], v[140:143]\n" \
    "v_mfma_f32_16x16x32_bf16 v[148:151], v[124:127], v[12:15], v[148:151]\n" \
    "v_mfma_f32_16x16x32_bf16 v[156:159], v[124:127], v[20:23], v[156:159]\n" \
    "v_mfma_f32_16x16x32_bf16 v[164:167], v[124:127], v[28:31], v[164:167]\n" \
    "s_waitcnt lgkmcnt(2)\n" \
    "v_mfma_f32_16x16x32_bf16 v[140:143], v[128:131], v[32:35], v[140:143]\n" \
    "v_mfma_f32_16x16x32_bf16 v[148:151], v[128:131], v[40:43], v[148:151]\n" \
    "v_mfma_f32_16x16x32_bf16 v[156:159], v[128:131], v[48:51], v[156:159]\n" \
    "v_mfma_f32_16x16x32_bf16 v[164:167], v[128:131], v[56:59], v[164:167]\n" \
    "s_waitcnt lgkmcnt(0)\n" \
    "v_mfma_f32_16x16x32_bf16 v[140:143], v[132:135], v[36:39], v[140:143]\n" \
    "v_mfma_f32_16x16x32_bf16 v[148:151], v[132:135], v[44:47], v[148:151]\n" \
    "v_mfma_f32_16x16x32_bf16 v[156:159], v[132:135], v[52:55], v[156:159]\n" \
    "v_mfma_f32_16x16x32_bf16 v[164:167], v[132:135], v[60:63], v[164:167]\n" \
    "v_cvt_pk_bf16_f32 v65, v136, v137\n" \
    "v_cvt_pk_bf16_f32 v68, v138, v139\n" \
    "v_cvt_pk_bf16_f32 v73, v144, v145\n" \
    "v_cvt_pk_bf16_f32 v76, v146, v147\n" \
    "v_cvt_pk_bf16_f32 v81, v152, v153\n" \
    "v_cvt_pk_bf16_f32 v84, v154, v155\n" \
    "v_cvt_pk_bf16_f32 v89, v160, v161\n" \
    "v_cvt_pk_bf16_f32 v92, v162, v163\n" \
    "v_cvt_pk_bf16_f32 v66, v142, v143\n" \
    "v_cvt_pk_bf16_f32 v67, v142, v143\n" \
    "v_cvt_pk_bf16_f32 v69, v140, v141\n" \
    "v_cvt_pk_bf16_f32 v74, v150, v151\n" \
    "v_cvt_pk_bf16_f32 v75, v150, v151\n" \
    "v_cvt_pk_bf16_f32 v77, v148, v149\n" \
    "v_cvt_pk_bf16_f32 v82, v158, v159\n" \
    "v_cvt_pk_bf16_f32 v83, v158, v159\n" \
    "v_cvt_pk_bf16_f32 v85, v156, v157\n" \
    "v_cvt_pk_bf16_f32 v90, v166, v167\n" \
    "v_cvt_pk_bf16_f32 v91, v166, v167\n" \
    "v_cvt_pk_bf16_f32 v93, v164, v165\n" \
    "v_permlane16_swap_b32 v66, v67\n" \
    "v_permlane16_swap_b32 v74, v75\n" \
    "v_permlane16_swap_b32 v82, v83\n" \
    "v_permlane16_swap_b32 v90, v91\n" \
    "L%=_Rloop:\n" \
    "v_mov_b64 v[176:177], 0\n" \
    "v_mov_b64 v[178:179], 0\n" \
    "v_mov_b64 v[180:181], 0\n" \
    "v_mov_b64 v[182:183], 0\n" \
    "v_mov_b64 v[184:185], 0\n" \
    "v_mov_b64 v[186:187], 0\n" \
    "v_mov_b64 v[188:189], 0\n" \
    "v_mov_b64 v[190:191], 0\n" \
    "v_mov_b64 v[196:197], 0\n" \
    "v_mov_b64 v[198:199], 0\n" \
    "v_mov_b64 v[200:201], 0\n" \
    "v_mov_b64 v[202:203], 0\n" \
    "v_mov_b64 v[204:205], 0\n" \
    "v_mov_b64 v[206:207], 0\n" \
    "v_mov_b64 v[208:209], 0\n" \
    "v_mov_b64 v[210:211], 0\n" \
    "v_mov_b64 v[212:213], 0\n" \
    "v_mov_b64 v[214:215], 0\n" \
    "s_add_u32 s85, s60, 0x1000\n" \
    "s_branch L%=_Ru2b\n" \
    "L%=_Ru2a:\n" \
    "s_sleep 4\n" \
    "L%=_Ru2b:\n" \
    "s_memrealtime vcc\n" \
    "s_add_u32 s80, s77, s64\n" \
    "s_add_u32 s80, s80, s69\n" \
    "s_waitcnt lgkmcnt(0)\n" \
    "s_sub_i32 s79, s80, vcc_lo\n" \
    "s_sub_u32 s79, s79, 40\n" \
    "s_cmp_lt_u32 s79, 0x400\n" \
    "s_cbranch_scc1 L%=_Ru2a\n" \
    "s_memtime s[38:39]\n" \
    "s_memrealtime vcc\n" \
    "s_nop 4\n" \
    "s_memrealtime s[32:33]\n" \
    "s_nop 4\n" \
    "s_memrealtime s[34:35]\n" \
    "s_nop 4\n" \
    "s_memrealtime s[36:37]\n" \
    "s_add_u32 s80, s77, s64\n" \
    "s_lshl_b32 s80, s80, 2\n" \
    "s_waitcnt lgkmcnt(0)\n" \
    "s_sub_u32 s80, s80, vcc_lo\n" \
    "s_sub_u32 s80, s80, s32\n" \
    "s_sub_u32 s80, s80, s34\n" \
    "s_sub_u32 s80, s80, s36\n" \
    "s_mul_i32 s80, s80, s71\n" \
    "s_ashr_i32 s80, s80, 6\n" \
    "s_add_u32 s78, s80, s38\n" \
    "L%=_Ru3a:\n" \
    "s_memtime s[38:39]\n" \
    "s_mul_i32 s80, s69, 23\n" \
    "s_add_u32 s80, s80, s78\n" \
    "s_waitcnt lgkmcnt(0)\n" \
    "s_sub_i32 s79, s80, s38\n" \
    "s_max_i32 s79, s79, 0\n" \
    "s_cmp_gt_u32 s79, 320\n" \
    "s_cbranch_scc1 L%=_Ru3s\n" \
    ".p2align 4\n" \
    "L%=_Ru3d:\n" \
    "s_sub_u32 s79, s79, 28\n" \
    "s_cbranch_scc0 L%=_Ru3d\n" \
    "L%=_Ru3e:\n" \
    "s_sub_u32 s79, s84, 0x20000\n" \
    "s_cselect_b32 s79, 0x40000000, s79\n" \
    "buffer_store_dwordx4 v[236:239], v234, s[48:51], s79 offen sc1\n" \
    "s_setprio 3\n" \
    "s_barrier\n" \
    "s_nop 14\n" \
    "L%=_Rvs:\n" \
    "ds_read_b128 v[104:107], v229 offset:16\n" \
    "ds_read_b128 v[108:111], v230 offset:8704\n" \
    "ds_read_b128 v[112:115], v230 offset:9984\n" \
    "ds_read_b128 v[116:119], v230 offset:11264\n" \
    "ds_read_b128 v[120:123], v230 offset:12544\n" \
    "s_waitcnt lgkmcnt(4)\n" \
    "v_min3_u32 v104, v104, v105, v106\n" \
    "v_min_u32 v104, v104, v107\n" \
    "s_nop 0\n" \
    "v_readfirstlane_b32 s79, v104\n" \
    "s_cmp_le_u32 s79, s76\n" \
    "s_cbranch_scc1 L%=_Rvs\n" \
    "s_waitcnt lgkmcnt(2)\n" \
    "v_pk_add_f32 v[108:109], v[108:109], v[112:113]\n" \
    "v_pk_add_f32 v[110:111], v[110:111], v[114:115]\n" \
    "s_waitcnt lgkmcnt(0)\n" \
    "v_pk_add_f32 v[116:117], v[116:117], v[120:121]\n" \
    "v_pk_add_f32 v[118:119], v[118:119], v[122:123]\n" \
    "v_pk_add_f32 v[108:109], v[108:109], v[116:117]\n" \
    "v_pk_add_f32 v[110:111], v[110:111], v[118:119]\n" \
    "v_cvt_pk_bf16_f32 v124, v108, v109\n" \
    "v_cvt_pk_bf16_f32 v125, v110, v111\n" \
    "s_nop 0\n" \
    "v_mov_b32_dpp v126, v124 quad_perm:[1,0,3,2] row_mask:0xf bank_mask:0xf\n" \
    "v_mov_b32_dpp v127, v125 quad_perm:[1,0,3,2] row_mask:0xf bank_mask:0xf\n" \
    "buffer_store_dwordx4 v[124:127], v231, s[40:43], s60 offen sc1\n" \
    "ds_write_b64 v232, v[124:125]\n" \
    "ds_read_b64_tr_b16 v[236:237], v233\n" \
    "ds_read_b64_tr_b16 v[238:239], v233 offset:256\n" \
    "s_cmp_eq_u32 s76, 63\n" \
    "s_cbranch_scc1 L%=_Rfin\n" \
    "ds_read_b32 v216, v229\n" \
    "s_waitcnt lgkmcnt(0)\n" \
    "v_readfirstlane_b32 s94, v216\n" \
    "s_mul_i32 s79, s94, s70\n" \
    "s_add_u32 s79, s79, s81\n" \
    "s_cmp_lg_u32 s79, s77\n" \
    "s_cbranch_scc1 L%=_Ru4s\n" \
    "L%=_Ru4k:\n" \
    "L%=_Ru5a:\n" \
    "s_memtime s[38:39]\n" \
    "s_mul_hi_u32 s80, s67, s96\n" \
    "s_add_u32 s80, s80, s78\n" \
    "s_waitcnt lgkmcnt(0)\n" \
    "s_sub_i32 s79, s80, s38\n" \
    "s_max_i32 s79, s79, 0\n" \
    "s_cmp_gt_u32 s79, 320\n" \
    "s_cbranch_scc1 L%=_Ru5s\n" \
    ".p2align 4\n" \
    "L%=_Ru5d:\n" \
    "s_sub_u32 s79, s79, 28\n" \
    "s_cbranch_scc0 L%=_Ru5d\n" \
    "L%=_Ru5e:\n" \
    "buffer_load_dwordx4 v[168:171], v225, s[40:43], s60 offen sc1\n" \
    "buffer_load_dwordx4 v[172:175], v225, s[40:43], s85 offen sc1\n" \
    "s_memtime s[38:39]\n" \
    "s_memrealtime vcc\n" \
    "s_nop 4\n" \
    "s_memrealtime s[32:33]\n" \
    "s_nop 4\n" \
    "s_memrealtime s[34:35]\n" \
    "s_nop 4\n" \
    "s_memrealtime s[36:37]\n" \
    "s_add_u32 s80, s77, s64\n" \
    "s_lshl_b32 s80, s80, 2\n" \
    "s_waitcnt lgkmcnt(0)\n" \
    "s_sub_u32 s80, s80, vcc_lo\n" \
    "s_sub_u32 s80, s80, s32\n" \
    "s_sub_u32 s80, s80, s34\n" \
    "s_sub_u32 s80, s80, s36\n" \
    "s_mul_i32 s80, s80, s71\n" \
    "s_ashr_i32 s80, s80, 6\n" \
    "s_add_u32 s78, s80, s38\n" \
    "L%=_Rvc:\n" \
    "s_waitcnt vmcnt(1)\n" \
    "v_smfmac_f32_16x16x64_bf16 v[176:179], v[168:171], v[0:7], v224\n" \
    "v_smfmac_f32_16x16x64_bf16 v[180:183], v[168:171], v[8:15], v224\n" \
    "s_waitcnt vmcnt(0)\n" \
    "v_smfmac_f32_16x16x64_bf16 v[176:179], v[172:175], v[32:39], v224\n" \
    "v_smfmac_f32_16x16x64_bf16 v[180:183], v[172:175], v[40:47], v224\n" \
    "v_smfmac_f32_16x16x64_bf16 v[184:187], v[168:171], v[16:23], v224\n" \
    "v_smfmac_f32_16x16x64_bf16 v[188:191], v[168:171], v[24:31], v224\n" \
    "v_add_f32 v176, v176, v178\n" \
    "v_smfmac_f32_16x16x64_bf16 v[184:187], v[172:175], v[48:55], v224\n" \
    "v_add_f32 v177, v177, v179\n" \
    "v_smfmac_f32_16x16x64_bf16 v[188:191], v[172:175], v[56:63], v224\n" \
    "v_cmp_u_f32 vcc, v176, v177\n" \
    "v_cvt_pk_bf16_f32 v64, v176, v177\n" \
    "v_add_f32 v180, v180, v182\n" \
    "v_add_f32 v181, v181, v183\n" \
    "v_cvt_pk_bf16_f32 v72, v180, v181\n" \
    "s_cbranch_vccnz L%=_Rvslow\n" \
    "L%=_Rvok:\n" \
    "v_add_f32 v184, v184, v186\n" \
    "v_add_f32 v185, v185, v187\n" \
    "v_cvt_pk_bf16_f32 v80, v184, v185\n" \
    "v_add_f32 v188, v188, v190\n" \
    "v_add_f32 v189, v189, v191\n" \
    "v_cvt_pk_bf16_f32 v88, v188, v189\n" \
    "s_barrier\n" \
    "L%=_Rfr:\n" \
    "ds_read_b128 v[192:195], v227\n" \
    "s_waitcnt lgkmcnt(0)\n" \
    "v_smfmac_f32_16x16x64_bf16 v[212:215], v[192:195], v[96:103], v224\n" \
    "v_cmp_ne_u32 vcc, s76, v195\n" \
    "v_smfmac_f32_16x16x64_bf16 v[196:199], v[192:195], v[64:71], v224\n" \
    "s_cbranch_vccnz L%=_Rfs\n" \
    "v_smfmac_f32_16x16x64_bf16 v[200:203], v[192:195], v[72:79], v224\n" \
    "v_smfmac_f32_16x16x64_bf16 v[204:207], v[192:195], v[80:87], v224\n" \
    "v_add_f32 v212, v212, v214\n" \
    "v_smfmac_f32_16x16x64_bf16 v[208:211], v[192:195], v[88:95], v224\n" \
    "v_add_f32 v213, v213, v215\n" \
    "v_rcp_f32 v220, v212\n" \
    "v_rcp_f32 v221, v213\n" \
    "v_add_f32 v196, v196, v198\n" \
    "v_add_f32 v197, v197, v199\n" \
    "v_pk_add_f32 v[200:201], v[200:201], v[202:203]\n" \
    "v_pk_add_f32 v[204:205], v[204:205], v[206:207]\n" \
    "s_nop 0\n" \
    "v_pk_add_f32 v[208:209], v[208:209], v[210:211]\n" \
    "v_pk_mul_f32 v[196:197], v[196:197], v[220:221]\n" \
    "v_pk_mul_f32 v[200:201], v[200:201], v[220:221]\n" \
    "v_pk_mul_f32 v[204:205], v[204:205], v[220:221]\n" \
    "v_pk_mul_f32 v[208:209], v[208:209], v[220:221]\n" \
    "v_cvt_pk_bf16_f32 v216, v196, v200\n" \
    "v_cvt_pk_bf16_f32 v217, v204, v208\n" \
    "v_cvt_pk_bf16_f32 v218, v197, v201\n" \
    "v_cvt_pk_bf16_f32 v219, v205, v209\n" \
    "buffer_store_dwordx4 v[216:219], v226, s[40:43], s60 offen sc1\n" \
    "ds_read_b64_tr_b16 v[104:105], v228 offset:16384\n" \
    "ds_read_b64_tr_b16 v[106:107], v228 offset:16416\n" \
    "ds_read_b64_tr_b16 v[108:109], v228 offset:18688\n" \
    "ds_read_b64_tr_b16 v[110:111], v228 offset:18720\n" \
    "ds_read_b64_tr_b16 v[112:113], v228 offset:20992\n" \
    "ds_read_b64_tr_b16 v[114:115], v228 offset:21024\n" \
    "ds_read_b64_tr_b16 v[116:117], v228 offset:23296\n" \
    "ds_read_b64_tr_b16 v[118:119], v228 offset:23328\n" \
    "ds_read_b64_tr_b16 v[120:121], v228 offset:16656\n" \
    "ds_read_b64_tr_b16 v[122:123], v228 offset:16688\n" \
    "ds_read_b64_tr_b16 v[124:125], v228 offset:18960\n" \
    "ds_read_b64_tr_b16 v[126:127], v228 offset:18992\n" \
    "ds_read_b64_tr_b16 v[128:129], v228 offset:21264\n" \
    "ds_read_b64_tr_b16 v[130:131], v228 offset:21296\n" \
    "ds_read_b64_tr_b16 v[132:133], v228 offset:23568\n" \
    "ds_read_b64_tr_b16 v[134:135], v228 offset:23600\n" \
    "s_waitcnt lgkmcnt(14)\n" \
    "v_mfma_f32_16x16x32_bf16 v[136:139], v[104:107], v[0:3], 0\n" \
    "v_mfma_f32_16x16x32_bf16 v[144:147], v[104:107], v[8:11], 0\n" \
    "v_mfma_f32_16x16x32_bf16 v[152:155], v[104:107], v[16:19], 0\n" \
    "v_mfma_f32_16x16x32_bf16 v[160:163], v[104:107], v[24:27], 0\n" \
    "s_waitcnt lgkmcnt(12)\n" \
    "v_mfma_f32_16x16x32_bf16 v[136:139], v[108:111], v[4:7], v[136:139]\n" \
    "v_mfma_f32_16x16x32_bf16 v[144:147], v[108:111], v[12:15], v[144:147]\n" \
    "v_mfma_f32_16x16x32_bf16 v[152:155], v[108:111], v[20:23], v[152:155]\n" \
    "v_mfma_f32_16x16x32_bf16 v[160:163], v[108:111], v[28:31], v[160:163]\n" \
    "s_waitcnt lgkmcnt(10)\n" \
    "v_mfma_f32_16x16x32_bf16 v[136:139], v[112:115], v[32:35], v[136:139]\n" \
    "v_mfma_f32_16x16x32_bf16 v[144:147], v[112:115], v[40:43], v[144:147]\n" \
    "v_mfma_f32_16x16x32_bf16 v[152:155], v[112:115], v[48:51], v[152:155]\n" \
    "v_mfma_f32_16x16x32_bf16 v[160:163], v[112:115], v[56:59], v[160:163]\n" \
    "s_waitcnt lgkmcnt(8)\n" \
    "v_mfma_f32_16x16x32_bf16 v[136:139], v[116:119], v[36:39], v[136:139]\n" \
    "v_mfma_f32_16x16x32_bf16 v[144:147], v[116:119], v[44:47], v[144:147]\n" \
    "v_mfma_f32_16x16x32_bf16 v[152:155], v[116:119], v[52:55], v[152:155]\n" \
    "v_mfma_f32_16x16x32_bf16 v[160:163], v[116:119], v[60:63], v[160:163]\n" \
    "v_add_u32 v223, 1, v223\n" \
    "s_and_b32 s95, s94, 1\n" \
    "s_lshl_b32 s95, s95, 17\n" \
    "s_add_u32 s95, s95, s61\n" \
    "s_add_u32 s85, s95, 0x1000\n" \
    "s_xor_b32 s85, s95, 0x20000\n" \
    "L%=_Ru6a:\n" \
    "s_memtime s[38:39]\n" \
    "s_mul_hi_u32 s80, s68, s96\n" \
    "s_add_u32 s80, s80, s78\n" \
    "s_waitcnt lgkmcnt(0)\n" \
    "s_sub_i32 s79, s80, s38\n" \
    "s_max_i32 s79, s79, 0\n" \
    "s_cmp_gt_u32 s79, 320\n" \
    "s_cbranch_scc1 L%=_Ru6s\n" \
    ".p2align 4\n" \
    "L%=_Ru6d:\n" \
    "s_sub_u32 s79, s79, 28\n" \
    "s_cbranch_scc0 L%=_Ru6d\n" \
    "L%=_Ru6e:\n" \
    "buffer_load_dwordx4 v[176:179], v235, s[40:43], s60 offen offset:0 sc1\n" \
    "buffer_load_dwordx4 v[180:183], v235, s[40:43], s60 offen offset:1024 sc1\n" \
    "buffer_load_dwordx4 v[184:187], v235, s[40:43], s60 offen offset:2048 sc1\n" \
    "buffer_load_dwordx4 v[188:191], v235, s[40:43], s60 offen offset:3072 sc1\n" \
    "v_mfma_f32_16x16x32_bf16 v[140:143], v[120:123], v[0:3], 0\n" \
    "v_mfma_f32_16x16x32_bf16 v[148:151], v[120:123], v[8:11], 0\n" \
    "v_mfma_f32_16x16x32_bf16 v[156:159], v[120:123], v[16:19], 0\n" \
    "v_mfma_f32_16x16x32_bf16 v[164:167], v[120:123], v[24:27], 0\n" \
    "v_mfma_f32_16x16x32_bf16 v[140:143], v[124:127], v[4:7], v[140:143]\n" \
    "v_mfma_f32_16x16x32_bf16 v[148:151], v[124:127], v[12:15], v[148:151]\n" \
    "v_mfma_f32_16x16x32_bf16 v[156:159], v[124:127], v[20:23], v[156:159]\n" \
    "v_mfma_f32_16x16x32_bf16 v[164:167], v[124:127], v[28:31], v[164:167]\n" \
    "v_mfma_f32_16x16x32_bf16 v[140:143], v[128:131], v[32:35], v[140:143]\n" \
    "v_mfma_f32_16x16x32_bf16 v[148:151], v[128:131], v[40:43], v[148:151]\n" \
    "v_mfma_f32_16x16x32_bf16 v[156:159], v[128:131], v[48:51], v[156:159]\n" \
    "v_mfma_f32_16x16x32_bf16 v[164:167], v[128:131], v[56:59], v[164:167]\n" \
    "v_mfma_f32_16x16x32_bf16 v[140:143], v[132:135], v[36:39], v[140:143]\n" \
    "v_mfma_f32_16x16x32_bf16 v[148:151], v[132:135], v[44:47], v[148:151]\n" \
    "v_mfma_f32_16x16x32_bf16 v[156:159], v[132:135], v[52:55], v[156:159]\n" \
    "v_mfma_f32_16x16x32_bf16 v[164:167], v[132:135], v[60:63], v[164:167]\n" \
    "v_cvt_pk_bf16_f32 v65, v136, v137\n" \
    "v_cvt_pk_bf16_f32 v68, v138, v139\n" \
    "v_cvt_pk_bf16_f32 v73, v144, v145\n" \
    "v_cvt_pk_bf16_f32 v76, v146, v147\n" \
    "v_cvt_pk_bf16_f32 v81, v152, v153\n" \
    "v_cvt_pk_bf16_f32 v84, v154, v155\n" \
    "v_cvt_pk_bf16_f32 v89, v160, v161\n" \
    "v_cvt_pk_bf16_f32 v92, v162, v163\n" \
    "L%=_Rred:\n" \
    "s_waitcnt vmcnt(3)\n" \
    "v_mfma_f32_16x16x32_bf16 v[240:243], v[244:247], v[176:179], 0\n" \
    "s_waitcnt vmcnt(2)\n" \
    "v_mfma_f32_16x16x32_bf16 v[240:243], v[244:247], v[180:183], v[240:243]\n" \
    "s_waitcnt vmcnt(1)\n" \
    "v_mfma_f32_16x16x32_bf16 v[240:243], v[244:247], v[184:187], v[240:243]\n" \
    "s_waitcnt vmcnt(0)\n" \
    "v_mfma_f32_16x16x32_bf16 v[240:243], v[244:247], v[188:191], v[240:243]\n" \
    "s_nop 7\n" \
    "v_cmp_u_f32 vcc, v240, v240\n" \
    "v_add_f32_dpp v240, v240, v240 row_ror:8 row_mask:0xf bank_mask:0xf\n" \
    "v_add_f32_dpp v241, v241, v241 row_ror:8 row_mask:0xf bank_mask:0xf\n" \
    "v_add_f32_dpp v242, v242, v242 row_ror:8 row_mask:0xf bank_mask:0xf\n" \
    "v_add_f32_dpp v243, v243, v243 row_ror:8 row_mask:0xf bank_mask:0xf\n" \
    "v_cvt_pk_bf16_f32 v216, v240, v241\n" \
    "v_cvt_pk_bf16_f32 v217, v242, v243\n" \
    "s_cbranch_vccnz L%=_Rslow\n" \
    "L%=_Rok:\n" \
    "v_mov_b32_dpp v218, v216 quad_perm:[1,0,3,2] row_mask:0xf bank_mask:0xf\n" \
    "v_mov_b32_dpp v219, v217 quad_perm:[1,0,3,2] row_mask:0xf bank_mask:0xf\n" \
    "buffer_store_dwordx4 v[216:219], v222, s[40:43], s95 offen sc1\n" \
    "ds_write_b32 v229, v223 offset:4\n" \
    "buffer_store_dwordx4 v[216:219], v222, s[40:43], s85 offen sc1\n" \
    "s_setprio 0\n" \
    "v_cvt_pk_bf16_f32 v66, v142, v143\n" \
    "v_cvt_pk_bf16_f32 v67, v142, v143\n" \
    "v_cvt_pk_bf16_f32 v69, v140, v141\n" \
    "v_cvt_pk_bf16_f32 v74, v150, v151\n" \
    "v_cvt_pk_bf16_f32 v75, v150, v151\n" \
    "v_cvt_pk_bf16_f32 v77, v148, v149\n" \
    "v_cvt_pk_bf16_f32 v82, v158, v159\n" \
    "v_cvt_pk_bf16_f32 v83, v158, v159\n" \
    "v_cvt_pk_bf16_f32 v85, v156, v157\n" \
    "v_cvt_pk_bf16_f32 v90, v166, v167\n" \
    "v_cvt_pk_bf16_f32 v91, v166, v167\n" \
    "v_cvt_pk_bf16_f32 v93, v164, v165\n" \
    "v_permlane16_swap_b32 v66, v67\n" \
    "v_permlane16_swap_b32 v74, v75\n" \
    "v_permlane16_swap_b32 v82, v83\n" \
    "v_permlane16_swap_b32 v90, v91\n" \
    "ds_read_b64 v[220:221], v229 offset:64\n" \
    "s_mov_b32 s79, s60\n" \
    "s_mov_b32 s60, s61\n" \
    "s_mov_b32 s61, s62\n" \
    "s_mov_b32 s62, s63\n" \
    "s_mov_b32 s63, s79\n" \
    "s_add_u32 s76, s76, 1\n" \
    "s_add_u32 s77, s77, s65\n" \
    "s_add_u32 s84, s84, 0x20000\n" \
    "s_add_u32 s81, s81, s65\n" \
    "s_waitcnt lgkmcnt(0)\n" \
    "v_readfirstlane_b32 s65, v220\n" \
    "v_readfirstlane_b32 s71, v221\n" \
    "s_mul_i32 s96, s65, s71\n" \
    "s_lshr_b32 s96, s96, 4\n" \
    "s_branch L%=_Rloop\n" \
    "L%=_Rslow:\n" \
    "s_nop 7\n" \
    "s_nop 0\n" \
    "v_max_u32 v220, v176, v177\n" \
    "v_max3_u32 v220, v220, v178, v179\n" \
    "v_max3_u32 v220, v220, v180, v181\n" \
    "v_max3_u32 v220, v220, v182, v183\n" \
    "v_max3_u32 v220, v220, v184, v185\n" \
    "v_max3_u32 v220, v220, v186, v187\n" \
    "v_max3_u32 v220, v220, v188, v189\n" \
    "v_max3_u32 v220, v220, v190, v191\n" \
    "v_cmp_eq_u32 vcc, -1, v220\n" \
    "s_cbranch_vccz L%=_Rok\n" \
    "s_cmp_eq_u32 s99, 0\n" \
    "s_cbranch_scc1 L%=_Rok\n" \
    "s_sub_u32 s99, s99, 1\n" \
    "s_cmp_lg_u32 s99, 0\n" \
    "s_cbranch_scc1 L%=_Ru7\n" \
    "v_mov_b32 v220, 1\n" \
    "buffer_store_dword v220, off, s[44:47], 0 sc1\n" \
    "L%=_Ru7:\n" \
    "s_sleep 1\n" \
    "buffer_load_dwordx4 v[176:179], v235, s[40:43], s60 offen offset:0 sc1\n" \
    "buffer_load_dwordx4 v[180:183], v235, s[40:43], s60 offen offset:1024 sc1\n" \
    "buffer_load_dwordx4 v[184:187], v235, s[40:43], s60 offen offset:2048 sc1\n" \
    "buffer_load_dwordx4 v[188:191], v235, s[40:43], s60 offen offset:3072 sc1\n" \
    "s_branch L%=_Rred\n" \
    "L%=_Rfin:\n" \
    "s_waitcnt lgkmcnt(0)\n" \
    "buffer_store_dwordx4 v[236:239], v234, s[48:51], s84 offen sc1\n" \
    "s_waitcnt vmcnt(0)\n" \
    "s_barrier\n" \
    "s_branch L%=_end\n" \
    "L%=_Ru3s:\n" \
    "s_cmp_gt_u32 s79, 0x8000\n" \
    "s_cbranch_scc1 L%=_Ru3e\n" \
    "s_sleep 2\n" \
    "s_branch L%=_Ru3a\n" \
    "L%=_Ru4s:\n" \
    "s_mov_b32 s77, s79\n" \
    "s_memtime s[38:39]\n" \
    "s_memrealtime vcc\n" \
    "s_nop 4\n" \
    "s_memrealtime s[32:33]\n" \
    "s_nop 4\n" \
    "s_memrealtime s[34:35]\n" \
    "s_nop 4\n" \
    "s_memrealtime s[36:37]\n" \
    "s_add_u32 s80, s77, s64\n" \
    "s_lshl_b32 s80, s80, 2\n" \
    "s_waitcnt lgkmcnt(0)\n" \
    "s_sub_u32 s80, s80, vcc_lo\n" \
    "s_sub_u32 s80, s80, s32\n" \
    "s_sub_u32 s80, s80, s34\n" \
    "s_sub_u32 s80, s80, s36\n" \
    "s_mul_i32 s80, s80, s71\n" \
    "s_ashr_i32 s80, s80, 6\n" \
    "s_add_u32 s78, s80, s38\n" \
    "s_branch L%=_Ru4k\n" \
    "L%=_Ru5s:\n" \
    "s_cmp_gt_u32 s79, 0x8000\n" \
    "s_cbranch_scc1 L%=_Ru5e\n" \
    "s_sleep 2\n" \
    "s_branch L%=_Ru5a\n" \
    "L%=_Rvslow:\n" \
    "s_nop 7\n" \
    "s_nop 0\n" \
    "v_max_u32 v216, v168, v169\n" \
    "v_max3_u32 v216, v216, v170, v171\n" \
    "v_max3_u32 v216, v216, v172, v173\n" \
    "v_max3_u32 v216, v216, v174, v175\n" \
    "v_cmp_eq_u32 vcc, -1, v216\n" \
    "s_cbranch_vccz L%=_Rvok\n" \
    "s_cmp_eq_u32 s99, 0\n" \
    "s_cbranch_scc1 L%=_Rvok\n" \
    "s_sub_u32 s99, s99, 1\n" \
    "s_cmp_lg_u32 s99, 0\n" \
    "s_cbranch_scc1 L%=_Ru8\n" \
    "v_mov_b32 v216, 1\n" \
    "buffer_store_dword v216, off, s[44:47], 0 sc1\n" \
    "L%=_Ru8:\n" \
    "s_sleep 1\n" \
    "buffer_load_dwordx4 v[168:171], v225, s[40:43], s60 offen sc1\n" \
    "buffer_load_dwordx4 v[172:175], v225, s[40:43], s85 offen sc1\n" \
    "v_mov_b64 v[176:177], 0\n" \
    "v_mov_b64 v[178:179], 0\n" \
    "v_mov_b64 v[180:181], 0\n" \
    "v_mov_b64 v[182:183], 0\n" \
    "v_mov_b64 v[184:185], 0\n" \
    "v_mov_b64 v[186:187], 0\n" \
    "v_mov_b64 v[188:189], 0\n" \
    "v_mov_b64 v[190:191], 0\n" \
    "s_waitcnt vmcnt(0)\n" \
    "s_branch L%=_Rvc\n" \
    "L%=_Rfs:\n" \
    "s_nop 7\n" \
    "s_nop 0\n" \
    "v_mov_b64 v[196:197], 0\n" \
    "v_mov_b64 v[198:199], 0\n" \
    "v_mov_b64 v[200:201], 0\n" \
    "v_mov_b64 v[202:203], 0\n" \
    "v_mov_b64 v[204:205], 0\n" \
    "v_mov_b64 v[206:207], 0\n" \
    "v_mov_b64 v[208:209], 0\n" \
    "v_mov_b64 v[210:211], 0\n" \
    "v_mov_b64 v[212:213], 0\n" \
    "v_mov_b64 v[214:215], 0\n" \
    "s_branch L%=_Rfr\n" \
    "L%=_Ru6s:\n" \
    "s_cmp_gt_u32 s79, 0x8000\n" \
    "s_cbranch_scc1 L%=_Ru6e\n" \
    "s_sleep 2\n" \
    "s_branch L%=_Ru6a\n" \
    "L%=_P:\n" \
    "s_mov_b32 s76, 0\n" \
    "s_mov_b32 s99, 0x20000\n" \
    "s_mov_b32 s84, 0\n" \
    "s_mov_b32 s81, 0\n" \
    "s_mov_b32 s86, 0\n" \
    "s_mov_b32 s87, 0\n" \
    "s_mov_b32 s88, 0\n" \
    "s_mov_b32 s89, 0\n" \
    "s_mov_b32 s90, 0\n" \
    "s_mov_b32 s91, 0\n" \
    "s_mov_b32 s92, 0\n" \
    "s_mov_b32 s93, 0\n" \
    "s_mov_b32 s94, 0\n" \
    "s_mov_b32 s95, 0\n" \
    "s_mov_b32 s96, 0\n" \
    "s_mov_b32 s97, 0\n" \
    "s_mov_b32 s98, 0\n" \
    "ds_read_b32 v216, v229\n" \
    "s_waitcnt lgkmcnt(0)\n" \
    "v_readfirstlane_b32 s94, v216\n" \
    "s_mul_i32 s79, s94, s70\n" \
    "s_add_u32 s79, s79, s81\n" \
    "s_mov_b32 s77, s79\n" \
    "s_mul_i32 s96, s65, s71\n" \
    "s_lshr_b32 s96, s96, 4\n" \
    "v_mov_b32 v96, 0x3f803f80\n" \
    "v_mov_b32 v97, 0x3f803f80\n" \
    "v_mov_b32 v98, 0x3f803f80\n" \
    "v_mov_b32 v99, 0x3f803f80\n" \
    "v_mov_b32 v100, 0x3f803f80\n" \
    "v_mov_b32 v101, 0x3f803f80\n" \
    "v_mov_b64 v[102:103], 0\n" \
    "v_mov_b64 v[64:65], 0\n" \
    "v_mov_b64 v[66:67], 0\n" \
    "v_mov_b64 v[68:69], 0\n" \
    "v_mov_b64 v[70:71], 0\n" \
    "v_mov_b64 v[72:73], 0\n" \
    "v_mov_b64 v[74:75], 0\n" \
    "v_mov_b64 v[76:77], 0\n" \
    "v_mov_b64 v[78:79], 0\n" \
    "v_mov_b64 v[80:81], 0\n" \
    "v_mov_b64 v[82:83], 0\n" \
    "v_mov_b64 v[84:85], 0\n" \
    "v_mov_b64 v[86:87], 0\n" \
    "v_mov_b64 v[88:89], 0\n" \
    "v_mov_b64 v[90:91], 0\n" \
    "v_mov_b64 v[92:93], 0\n" \
    "v_mov_b64 v[94:95], 0\n" \
    "v_mov_b64 v[192:193], 0\n" \
    "v_mov_b64 v[194:195], 0\n" \
    "v_cvt_f32_u32 v240, s71\n" \
    "v_mov_b32 v241, 1.0\n" \
    "v_mov_b32 v242, 0\n" \
    "v_mov_b32 v243, s65\n" \
    "s_mov_b32 s74, s94\n" \
    "v_mov_b32 v244, -1\n" \
    "v_mov_b32 v245, -1\n" \
    "v_mov_b32 v246, -1\n" \
    "v_mov_b32 v247, -1\n" \
    "L%=_Ploop:\n" \
    "ds_read_b64_tr_b16 v[104:105], v228 offset:16384\n" \
    "ds_read_b64_tr_b16 v[106:107], v228 offset:16416\n" \
    "ds_read_b64_tr_b16 v[108:109], v228 offset:18688\n" \
    "ds_read_b64_tr_b16 v[110:111], v228 offset:18720\n" \
    "ds_read_b64_tr_b16 v[112:113], v228 offset:20992\n" \
    "ds_read_b64_tr_b16 v[114:115], v228 offset:21024\n" \
    "ds_read_b64_tr_b16 v[116:117], v228 offset:23296\n" \
    "ds_read_b64_tr_b16 v[118:119], v228 offset:23328\n" \
    "ds_read_b64_tr_b16 v[120:121], v228 offset:16656\n" \
    "ds_read_b64_tr_b16 v[122:123], v228 offset:16688\n" \
    "ds_read_b64_tr_b16 v[124:125], v228 offset:18960\n" \
    "ds_read_b64_tr_b16 v[126:127], v228 offset:18992\n" \
    "ds_read_b64_tr_b16 v[128:129], v228 offset:21264\n" \
    "ds_read_b64_tr_b16 v[130:131], v228 offset:21296\n" \
    "ds_read_b64_tr_b16 v[132:133], v228 offset:23568\n" \
    "ds_read_b64_tr_b16 v[134:135], v228 offset:23600\n" \
    "s_waitcnt lgkmcnt(14)\n" \
    "v_mfma_f32_16x16x32_bf16 v[136:139], v[104:107], v[0:3], 0\n" \
    "v_mfma_f32_16x16x32_bf16 v[144:147], v[104:107], v[8:11], 0\n" \
    "v_mfma_f32_16x16x32_bf16 v[152:155], v[104:107], v[16:19], 0\n" \
    "v_mfma_f32_16x16x32_bf16 v[160:163], v[104:107], v[24:27], 0\n" \
    "s_waitcnt lgkmcnt(12)\n" \
    "v_mfma_f32_16x16x32_bf16 v[136:139], v[108:111], v[4:7], v[136:139]\n" \
    "v_mfma_f32_16x16x32_bf16 v[144:147], v[108:111], v[12:15], v[144:147]\n" \
    "v_mfma_f32_16x16x32_bf16 v[152:155], v[108:111], v[20:23], v[152:155]\n" \
    "v_mfma_f32_16x16x32_bf16 v[160:163], v[108:111], v[28:31], v[160:163]\n" \
    "s_waitcnt lgkmcnt(10)\n" \
    "v_mfma_f32_16x16x32_bf16 v[136:139], v[112:115], v[32:35], v[136:139]\n" \
    "v_mfma_f32_16x16x32_bf16 v[144:147], v[112:115], v[40:43], v[144:147]\n" \
    "v_mfma_f32_16x16x32_bf16 v[152:155], v[112:115], v[48:51], v[152:155]\n" \
    "v_mfma_f32_16x16x32_bf16 v[160:163], v[112:115], v[56:59], v[160:163]\n" \
    "s_waitcnt lgkmcnt(8)\n" \
    "v_mfma_f32_16x16x32_bf16 v[136:139], v[116:119], v[36:39], v[136:139]\n" \
    "v_mfma_f32_16x16x32_bf16 v[144:147], v[116:119], v[44:47], v[144:147]\n" \
    "v_mfma_f32_16x16x32_bf16 v[152:155], v[116:119], v[52:55], v[152:155]\n" \
    "v_mfma_f32_16x16x32_bf16 v[160:163], v[116:119], v[60:63], v[160:163]\n" \
    "s_waitcnt lgkmcnt(6)\n" \
    "v_mfma_f32_16x16x32_bf16 v[140:143], v[120:123], v[0:3], 0\n" \
    "v_mfma_f32_16x16x32_bf16 v[148:151], v[120:123], v[8:11], 0\n" \
    "v_mfma_f32_16x16x32_bf16 v[156:159], v[120:123], v[16:19], 0\n" \
    "v_mfma_f32_16x16x32_bf16 v[164:167], v[120:123], v[24:27], 0\n" \
    "s_waitcnt lgkmcnt(4)\n" \
    "v_mfma_f32_16x16x32_bf16 v[140:143], v[124:127], v[4:7], v[140:143]\n" \
    "v_mfma_f32_16x16x32_bf16 v[148:151], v[124:127], v[12:15], v[148:151]\n" \
    "v_mfma_f32_16x16x32_bf16 v[156:159], v[124:127], v[20:23], v[156:159]\n" \
    "v_mfma_f32_16x16x32_bf16 v[164:167], v[124:127], v[28:31], v[164:167]\n" \
    "s_waitcnt lgkmcnt(2)\n" \
    "v_mfma_f32_16x16x32_bf16 v[140:143], v[128:131], v[32:35], v[140:143]\n" \
    "v_mfma_f32_16x16x32_bf16 v[148:151], v[128:131], v[40:43], v[148:151]\n" \
    "v_mfma_f32_16x16x32_bf16 v[156:159], v[128:131], v[48:51], v[156:159]\n" \
    "v_mfma_f32_16x16x32_bf16 v[164:167], v[128:131], v[56:59], v[164:167]\n" \
    "s_waitcnt lgkmcnt(0)\n" \
    "v_mfma_f32_16x16x32_bf16 v[140:143], v[132:135], v[36:39], v[140:143]\n" \
    "v_mfma_f32_16x16x32_bf16 v[148:151], v[132:135], v[44:47], v[148:151]\n" \
    "v_mfma_f32_16x16x32_bf16 v[156:159], v[132:135], v[52:55], v[156:159]\n" \
    "v_mfma_f32_16x16x32_bf16 v[164:167], v[132:135], v[60:63], v[164:167]\n" \
    "v_cvt_pk_bf16_f32 v65, v136, v137\n" \
    "v_cvt_pk_bf16_f32 v68, v138, v139\n" \
    "v_cvt_pk_bf16_f32 v73, v144, v145\n" \
    "v_cvt_pk_bf16_f32 v76, v146, v147\n" \
    "v_cvt_pk_bf16_f32 v81, v152, v153\n" \
    "v_cvt_pk_bf16_f32 v84, v154, v155\n" \
    "v_cvt_pk_bf16_f32 v89, v160, v161\n" \
    "v_cvt_pk_bf16_f32 v92, v162, v163\n" \
    "v_cvt_pk_bf16_f32 v66, v142, v143\n" \
    "v_cvt_pk_bf16_f32 v67, v142, v143\n" \
    "v_cvt_pk_bf16_f32 v69, v140, v141\n" \
    "v_cvt_pk_bf16_f32 v74, v150, v151\n" \
    "v_cvt_pk_bf16_f32 v75, v150, v151\n" \
    "v_cvt_pk_bf16_f32 v77, v148, v149\n" \
    "v_cvt_pk_bf16_f32 v82, v158, v159\n" \
    "v_cvt_pk_bf16_f32 v83, v158, v159\n" \
    "v_cvt_pk_bf16_f32 v85, v156, v157\n" \
    "v_cvt_pk_bf16_f32 v90, v166, v167\n" \
    "v_cvt_pk_bf16_f32 v91, v166, v167\n" \
    "v_cvt_pk_bf16_f32 v93, v164, v165\n" \
    "v_permlane16_swap_b32 v66, v67\n" \
    "v_permlane16_swap_b32 v74, v75\n" \
    "v_permlane16_swap_b32 v82, v83\n" \
    "v_permlane16_swap_b32 v90, v91\n" \
    "v_mov_b64 v[176:177], 0\n" \
    "v_mov_b64 v[178:179], 0\n" \
    "v_mov_b64 v[180:181], 0\n" \
    "v_mov_b64 v[182:183], 0\n" \
    "v_mov_b64 v[184:185], 0\n" \
    "v_mov_b64 v[186:187], 0\n" \
    "v_mov_b64 v[188:189], 0\n" \
    "v_mov_b64 v[190:191], 0\n" \
    "v_mov_b64 v[196:197], 0\n" \
    "v_mov_b64 v[198:199], 0\n" \
    "v_mov_b64 v[200:201], 0\n" \
    "v_mov_b64 v[202:203], 0\n" \
    "v_mov_b64 v[204:205], 0\n" \
    "v_mov_b64 v[206:207], 0\n" \
    "v_mov_b64 v[208:209], 0\n" \
    "v_mov_b64 v[210:211], 0\n" \
    "v_mov_b64 v[212:213], 0\n" \
    "v_mov_b64 v[214:215], 0\n" \
    "s_add_u32 s85, s60, 0x1000\n" \
    "s_add_u32 s82, s76, 1\n" \
    "s_min_u32 s82, s82, 63\n" \
    "s_lshl_b32 s83, s82, 19\n" \
    "s_add_u32 s83, s83, s72\n" \
    "s_lshl_b32 s79, s82, 11\n" \
    "s_and_b32 s79, s79, 0x2000\n" \
    "s_xor_b32 s83, s83, s79\n" \
    "v_cmp_ge_u32 vcc, s82, v237\n" \
    "s_cbranch_vccnz L%=_Pfslow\n" \
    "L%=_Pfok:\n" \
    "s_add_u32 s79, s82, 3\n" \
    "v_mov_b32 v150, 0x40000000\n" \
    "v_cmp_lt_u32 vcc, s79, v237\n" \
    "s_nop 1\n" \
    "v_cndmask_b32 v150, v235, v150, vcc\n" \
    "s_branch L%=_Pu2b\n" \
    "L%=_Pu2a:\n" \
    "s_sleep 4\n" \
    "L%=_Pu2b:\n" \
    "s_memrealtime vcc\n" \
    "s_add_u32 s80, s77, s64\n" \
    "s_add_u32 s80, s80, s69\n" \
    "s_waitcnt lgkmcnt(0)\n" \
    "s_sub_i32 s79, s80, vcc_lo\n" \
    "s_sub_u32 s79, s79, 40\n" \
    "s_cmp_lt_u32 s79, 0x400\n" \
    "s_cbranch_scc1 L%=_Pu2a\n" \
    "s_memtime s[38:39]\n" \
    "s_memrealtime vcc\n" \
    "s_nop 4\n" \
    "s_memrealtime s[32:33]\n" \
    "s_nop 4\n" \
    "s_memrealtime s[34:35]\n" \
    "s_nop 4\n" \
    "s_memrealtime s[36:37]\n" \
    "s_add_u32 s80, s77, s64\n" \
    "s_lshl_b32 s80, s80, 2\n" \
    "s_waitcnt lgkmcnt(0)\n" \
    "s_sub_u32 s80, s80, vcc_lo\n" \
    "s_sub_u32 s80, s80, s32\n" \
    "s_sub_u32 s80, s80, s34\n" \
    "s_sub_u32 s80, s80, s36\n" \
    "s_mul_i32 s80, s80, s71\n" \
    "s_ashr_i32 s80, s80, 6\n" \
    "s_add_u32 s78, s80, s38\n" \
    "L%=_Pu3a:\n" \
    "s_memtime s[38:39]\n" \
    "s_mul_i32 s80, s69, 23\n" \
    "s_add_u32 s80, s80, s78\n" \
    "s_waitcnt lgkmcnt(0)\n" \
    "s_sub_i32 s79, s80, s38\n" \
    "s_max_i32 s79, s79, 0\n" \
    "s_cmp_gt_u32 s79, 320\n" \
    "s_cbranch_scc1 L%=_Pu3s\n" \
    ".p2align 4\n" \
    "L%=_Pu3d:\n" \
    "s_sub_u32 s79, s79, 28\n" \
    "s_cbranch_scc0 L%=_Pu3d\n" \
    "L%=_Pu3e:\n" \
    "buffer_load_dwordx4 v[104:107], v230, s[56:59], s83 offen offset:0 sc1\n" \
    "buffer_load_dwordx4 v[108:111], v230, s[56:59], s83 offen offset:1024 sc1\n" \
    "buffer_load_dwordx4 v[112:115], v230, s[56:59], s83 offen offset:2048 sc1\n" \
    "buffer_load_dwordx4 v[116:119], v230, s[56:59], s83 offen offset:3072 sc1\n" \
    "s_add_u32 s79, s83, 0x1000\n" \
    "buffer_load_dwordx4 v[120:123], v230, s[56:59], s79 offen offset:0 sc1\n" \
    "buffer_load_dwordx4 v[124:127], v230, s[56:59], s79 offen offset:1024 sc1\n" \
    "buffer_load_dwordx4 v[128:131], v230, s[56:59], s79 offen offset:2048 sc1\n" \
    "buffer_load_dwordx4 v[132:135], v230, s[56:59], s79 offen offset:3072 sc1\n" \
    "s_xor_b32 s79, s83, s75\n" \
    "buffer_load_dwordx4 v[136:139], v230, s[52:55], s79 offen offset:0 sc1\n" \
    "buffer_load_dwordx4 v[140:143], v230, s[52:55], s79 offen offset:1024 sc1\n" \
    "buffer_load_dword v151, v150, s[44:47], 0 offen sc1\n" \
    "s_lshl_b32 s79, s76, 7\n" \
    "buffer_load_dwordx4 v[144:147], off, s[44:47], s79 offset:2176 sc1\n" \
    "buffer_store_dword v243, v239, s[44:47], s79 offen sc1\n" \
    "buffer_store_dwordx4 v[244:247], v232, s[40:43], s62 offen offset:0 sc1\n" \
    "buffer_store_dwordx4 v[244:247], v232, s[40:43], s62 offen offset:1024 sc1\n" \
    "buffer_store_dwordx4 v[244:247], v232, s[40:43], s62 offen offset:2048 sc1\n" \
    "buffer_store_dwordx4 v[244:247], v232, s[40:43], s62 offen offset:3072 sc1\n" \
    "buffer_store_dwordx4 v[244:247], v233, s[40:43], s62 offen offset:0 sc1\n" \
    "buffer_store_dwordx4 v[244:247], v233, s[40:43], s62 offen offset:1024 sc1\n" \
    "buffer_store_dwordx4 v[244:247], v234, s[40:43], s62 offen sc1\n" \
    "s_barrier\n" \
    "s_cmp_eq_u32 s76, 63\n" \
    "s_cbranch_scc1 L%=_Pfin\n" \
    "ds_read_b32 v216, v229\n" \
    "s_waitcnt lgkmcnt(0)\n" \
    "v_readfirstlane_b32 s94, v216\n" \
    "s_mul_i32 s79, s94, s70\n" \
    "s_add_u32 s79, s79, s81\n" \
    "s_cmp_lg_u32 s79, s77\n" \
    "s_cbranch_scc1 L%=_Pu4s\n" \
    "L%=_Pu4k:\n" \
    "s_sub_u32 s32, s78, s97\n" \
    "s_sub_u32 s33, s77, s98\n" \
    "s_mov_b32 s97, s78\n" \
    "s_mov_b32 s98, s77\n" \
    "s_lshl_b32 s32, s32, 4\n" \
    "s_cmp_eq_u32 s76, 0\n" \
    "s_cbranch_scc1 L%=_Pck\n" \
    "v_cvt_f32_u32 v148, s33\n" \
    "v_cvt_f32_u32 v149, s32\n" \
    "v_rcp_f32 v148, v148\n" \
    "s_cmp_lt_u32 s76, 4\n" \
    "s_cselect_b32 s34, 0, 0x3c449ba6\n" \
    "s_cmp_eq_u32 s94, s74\n" \
    "s_mov_b32 s74, s94\n" \
    "s_cselect_b32 s32, 0x7f000000, 0\n" \
    "s_cselect_b32 s33, 0.5, 1.0\n" \
    "s_cselect_b32 s34, 0, s34\n" \
    "v_fma_f32 v148, v148, v149, -v240\n" \
    "v_max_f32 v149, 0.5, v241\n" \
    "v_min_f32 v148, s32, v148\n" \
    "v_cmp_gt_f32 vcc, 0, v148\n" \
    "v_mul_f32 v242, 0x3f7851ec, v242\n" \
    "v_add_f32 v242, s34, v242\n" \
    "v_cndmask_b32 v149, v241, v149, vcc\n" \
    "v_fma_f32 v240, v148, v149, v240\n" \
    "v_mul_f32 v241, s33, v241\n" \
    "v_max_f32 v240, 0x43400000, v240\n" \
    "v_max_f32 v241, 0x3e000000, v241\n" \
    "v_min_f32 v240, 0x43e00000, v240\n" \
    "v_fma_f32 v148, v242, s73, s73\n" \
    "v_rcp_f32 v149, v240\n" \
    "s_nop 0\n" \
    "v_fma_f32 v148, v148, v149, 0.5\n" \
    "v_cvt_u32_f32 v243, v148\n" \
    "L%=_Pck:\n" \
    "v_add_f32 v149, 0.5, v240\n" \
    "v_mov_b32 v148, s65\n" \
    "v_cvt_u32_f32 v149, v149\n" \
    "s_waitcnt vmcnt(8)\n" \
    "ds_write_b128 v231, v[104:107] offset:16384\n" \
    "ds_write_b128 v231, v[108:111] offset:17536\n" \
    "ds_write_b128 v231, v[112:115] offset:18688\n" \
    "ds_write_b128 v231, v[116:119] offset:19840\n" \
    "ds_write_b128 v231, v[120:123] offset:20992\n" \
    "ds_write_b128 v231, v[124:127] offset:22144\n" \
    "ds_write_b128 v231, v[128:131] offset:23296\n" \
    "ds_write_b128 v231, v[132:135] offset:24448\n" \
    "ds_write_b128 v230, v[136:139] offset:25600\n" \
    "ds_write_b128 v230, v[140:143] offset:26624\n" \
    "v_max3_u32 v144, v144, v145, v146\n" \
    "v_max_u32 v144, v144, v147\n" \
    "v_cmp_eq_u32 vcc, 0, v144\n" \
    "s_nop 1\n" \
    "v_cndmask_b32 v148, v144, v148, vcc\n" \
    "ds_write_b64 v229, v[148:149] offset:64\n" \
    "v_max_u32 v237, v237, v151\n" \
    "s_setprio 3\n" \
    "L%=_Pu5a:\n" \
    "s_memtime s[38:39]\n" \
    "s_mul_hi_u32 s80, s67, s96\n" \
    "s_add_u32 s80, s80, s78\n" \
    "s_waitcnt lgkmcnt(0)\n" \
    "s_sub_i32 s79, s80, s38\n" \
    "s_max_i32 s79, s79, 0\n" \
    "s_cmp_gt_u32 s79, 320\n" \
    "s_cbranch_scc1 L%=_Pu5s\n" \
    ".p2align 4\n" \
    "L%=_Pu5d:\n" \
    "s_sub_u32 s79, s79, 28\n" \
    "s_cbranch_scc0 L%=_Pu5d\n" \
    "L%=_Pu5e:\n" \
    "buffer_load_dwordx4 v[168:171], v225, s[40:43], s60 offen sc1\n" \
    "buffer_load_dwordx4 v[172:175], v225, s[40:43], s85 offen sc1\n" \
    "L%=_Pvc:\n" \
    "s_waitcnt vmcnt(1)\n" \
    "v_smfmac_f32_16x16x64_bf16 v[176:179], v[168:171], v[0:7], v224\n" \
    "v_smfmac_f32_16x16x64_bf16 v[180:183], v[168:171], v[8:15], v224\n" \
    "s_waitcnt vmcnt(0)\n" \
    "v_smfmac_f32_16x16x64_bf16 v[176:179], v[172:175], v[32:39], v224\n" \
    "v_smfmac_f32_16x16x64_bf16 v[180:183], v[172:175], v[40:47], v224\n" \
    "v_smfmac_f32_16x16x64_bf16 v[184:187], v[168:171], v[16:23], v224\n" \
    "v_smfmac_f32_16x16x64_bf16 v[188:191], v[168:171], v[24:31], v224\n" \
    "v_add_f32 v176, v176, v178\n" \
    "v_smfmac_f32_16x16x64_bf16 v[184:187], v[172:175], v[48:55], v224\n" \
    "v_add_f32 v177, v177, v179\n" \
    "v_smfmac_f32_16x16x64_bf16 v[188:191], v[172:175], v[56:63], v224\n" \
    "v_cmp_u_f32 vcc, v176, v177\n" \
    "v_cvt_pk_bf16_f32 v64, v176, v177\n" \
    "v_add_f32 v180, v180, v182\n" \
    "v_add_f32 v181, v181, v183\n" \
    "v_cvt_pk_bf16_f32 v72, v180, v181\n" \
    "s_cbranch_vccnz L%=_Pvslow\n" \
    "L%=_Pvok:\n" \
    "v_add_f32 v184, v184, v186\n" \
    "v_add_f32 v185, v185, v187\n" \
    "v_cvt_pk_bf16_f32 v80, v184, v185\n" \
    "v_add_f32 v188, v188, v190\n" \
    "v_add_f32 v189, v189, v191\n" \
    "v_cvt_pk_bf16_f32 v88, v188, v189\n" \
    "s_barrier\n" \
    "L%=_Pfr:\n" \
    "ds_read_b128 v[192:195], v227\n" \
    "s_waitcnt lgkmcnt(0)\n" \
    "v_smfmac_f32_16x16x64_bf16 v[212:215], v[192:195], v[96:103], v224\n" \
    "v_cmp_ne_u32 vcc, s76, v195\n" \
    "v_smfmac_f32_16x16x64_bf16 v[196:199], v[192:195], v[64:71], v224\n" \
    "s_cbranch_vccnz L%=_Pfs\n" \
    "v_smfmac_f32_16x16x64_bf16 v[200:203], v[192:195], v[72:79], v224\n" \
    "v_smfmac_f32_16x16x64_bf16 v[204:207], v[192:195], v[80:87], v224\n" \
    "v_add_f32 v212, v212, v214\n" \
    "v_smfmac_f32_16x16x64_bf16 v[208:211], v[192:195], v[88:95], v224\n" \
    "v_add_f32 v213, v213, v215\n" \
    "v_rcp_f32 v220, v212\n" \
    "v_rcp_f32 v221, v213\n" \
    "v_add_f32 v196, v196, v198\n" \
    "v_add_f32 v197, v197, v199\n" \
    "v_pk_add_f32 v[200:201], v[200:201], v[202:203]\n" \
    "v_pk_add_f32 v[204:205], v[204:205], v[206:207]\n" \
    "s_nop 0\n" \
    "v_pk_add_f32 v[208:209], v[208:209], v[210:211]\n" \
    "v_pk_mul_f32 v[196:197], v[196:197], v[220:221]\n" \
    "v_pk_mul_f32 v[200:201], v[200:201], v[220:221]\n" \
    "v_pk_mul_f32 v[204:205], v[204:205], v[220:221]\n" \
    "v_pk_mul_f32 v[208:209], v[208:209], v[220:221]\n" \
    "v_cvt_pk_bf16_f32 v216, v196, v200\n" \
    "v_cvt_pk_bf16_f32 v217, v204, v208\n" \
    "v_cvt_pk_bf16_f32 v218, v197, v201\n" \
    "v_cvt_pk_bf16_f32 v219, v205, v209\n" \
    "buffer_store_dwordx4 v[216:219], v226, s[40:43], s60 offen sc1\n" \
    "s_setprio 0\n" \
    "ds_read_b64 v[220:221], v229 offset:64\n" \
    "s_mov_b32 s79, s60\n" \
    "s_mov_b32 s60, s61\n" \
    "s_mov_b32 s61, s62\n" \
    "s_mov_b32 s62, s63\n" \
    "s_mov_b32 s63, s79\n" \
    "s_add_u32 s76, s76, 1\n" \
    "s_add_u32 s77, s77, s65\n" \
    "s_add_u32 s84, s84, 0x20000\n" \
    "s_add_u32 s81, s81, s65\n" \
    "s_waitcnt lgkmcnt(0)\n" \
    "v_readfirstlane_b32 s65, v220\n" \
    "v_readfirstlane_b32 s71, v221\n" \
    "s_mul_i32 s96, s65, s71\n" \
    "s_lshr_b32 s96, s96, 4\n" \
    "s_branch L%=_Ploop\n" \
    "L%=_Pfslow:\n" \
    "s_sleep 4\n" \
    "buffer_load_dword v237, v235, s[44:47], 0 offen sc1\n" \
    "s_waitcnt vmcnt(0)\n" \
    "v_cmp_ge_u32 vcc, s82, v237\n" \
    "s_cbranch_vccz L%=_Pfok\n" \
    "s_cmp_eq_u32 s99, 0\n" \
    "s_cbranch_scc1 L%=_Pfok\n" \
    "s_sub_u32 s99, s99, 1\n" \
    "s_branch L%=_Pfslow\n" \
    "L%=_Pfin:\n" \
    "s_waitcnt vmcnt(0)\n" \
    "s_barrier\n" \
    "s_branch L%=_end\n" \
    "L%=_Pu3s:\n" \
    "s_cmp_gt_u32 s79, 0x8000\n" \
    "s_cbranch_scc1 L%=_Pu3e\n" \
    "s_sleep 2\n" \
    "s_branch L%=_Pu3a\n" \
    "L%=_Pu4s:\n" \
    "s_mov_b32 s77, s79\n" \
    "s_memtime s[38:39]\n" \
    "s_memrealtime vcc\n" \
    "s_nop 4\n" \
    "s_memrealtime s[32:33]\n" \
    "s_nop 4\n" \
    "s_memrealtime s[34:35]\n" \
    "s_nop 4\n" \
    "s_memrealtime s[36:37]\n" \
    "s_add_u32 s80, s77, s64\n" \
    "s_lshl_b32 s80, s80, 2\n" \
    "s_waitcnt lgkmcnt(0)\n" \
    "s_sub_u32 s80, s80, vcc_lo\n" \
    "s_sub_u32 s80, s80, s32\n" \
    "s_sub_u32 s80, s80, s34\n" \
    "s_sub_u32 s80, s80, s36\n" \
    "s_mul_i32 s80, s80, s71\n" \
    "s_ashr_i32 s80, s80, 6\n" \
    "s_add_u32 s78, s80, s38\n" \
    "s_branch L%=_Pu4k\n" \
    "L%=_Pu5s:\n" \
    "s_cmp_gt_u32 s79, 0x8000\n" \
    "s_cbranch_scc1 L%=_Pu5e\n" \
    "s_sleep 2\n" \
    "s_branch L%=_Pu5a\n" \
    "L%=_Pvslow:\n" \
    "s_nop 7\n" \
    "s_nop 0\n" \
    "v_max_u32 v216, v168, v169\n" \
    "v_max3_u32 v216, v216, v170, v171\n" \
    "v_max3_u32 v216, v216, v172, v173\n" \
    "v_max3_u32 v216, v216, v174, v175\n" \
    "v_cmp_eq_u32 vcc, -1, v216\n" \
    "s_cbranch_vccz L%=_Pvok\n" \
    "s_cmp_eq_u32 s99, 0\n" \
    "s_cbranch_scc1 L%=_Pvok\n" \
    "s_sub_u32 s99, s99, 1\n" \
    "s_cmp_lg_u32 s99, 0\n" \
    "s_cbranch_scc1 L%=_Pu6\n" \
    "v_mov_b32 v216, 1\n" \
    "buffer_store_dword v216, off, s[44:47], 0 sc1\n" \
    "L%=_Pu6:\n" \
    "s_sleep 1\n" \
    "buffer_load_dwordx4 v[168:171], v225, s[40:43], s60 offen sc1\n" \
    "buffer_load_dwordx4 v[172:175], v225, s[40:43], s85 offen sc1\n" \
    "v_mov_b64 v[176:177], 0\n" \
    "v_mov_b64 v[178:179], 0\n" \
    "v_mov_b64 v[180:181], 0\n" \
    "v_mov_b64 v[182:183], 0\n" \
    "v_mov_b64 v[184:185], 0\n" \
    "v_mov_b64 v[186:187], 0\n" \
    "v_mov_b64 v[188:189], 0\n" \
    "v_mov_b64 v[190:191], 0\n" \
    "s_waitcnt vmcnt(0)\n" \
    "s_branch L%=_Pvc\n" \
    "L%=_Pfs:\n" \
    "s_nop 7\n" \
    "s_nop 0\n" \
    "v_mov_b64 v[196:197], 0\n" \
    "v_mov_b64 v[198:199], 0\n" \
    "v_mov_b64 v[200:201], 0\n" \
    "v_mov_b64 v[202:203], 0\n" \
    "v_mov_b64 v[204:205], 0\n" \
    "v_mov_b64 v[206:207], 0\n" \
    "v_mov_b64 v[208:209], 0\n" \
    "v_mov_b64 v[210:211], 0\n" \
    "v_mov_b64 v[212:213], 0\n" \
    "v_mov_b64 v[214:215], 0\n" \
    "s_branch L%=_Pfr\n" \
    "L%=_end:\n"
#define V8(b) "v" #b "0", "v" #b "1", "v" #b "2", "v" #b "3", "v" #b "4", "v" #b "5", "v" #b "6", "v" #b "7", "v" #b "8", "v" #b "9"
#define S_IO "+{s[40:55]}"(sA), "+{s[56:71]}"(sB), "+{s[72:75]}"(sC), "+{s78}"(lwo), "={s[92:99]}"(so)
#define FIN2() do {} while (0)
#define S_CLOB "memory", "vcc", "scc", "m0", "s32", "s33", "s34", "s35", "s36", "s37", "s38", "s39", "s76", "s77", "s79", "s80", "s81", "s82", "s83", "s84", "s85", "s86", "s87", "s88", "s89", "s90", "s91"
#define B_CLOB S_CLOB, "v64", "v65", "v66", "v67", "v68", "v69", V8(7), V8(8), V8(9), V8(10), V8(11), V8(12), V8(13), V8(14), V8(15), V8(16), V8(17), V8(18), V8(19), V8(20), V8(21), \
    "v220", "v221", "v222", "v223", "v240", "v241", "v242", "v243"
#define FIN() do {} while (0)

#define MK_S() \
    a = args(); \
    v16u sA = cat8(cat4(mkrs(a->ring, NSLOT * SLOT), mkrs(a->ctl, CTL_SZ)), cat4(mkrs(a->mb, NSEG * NH * 4096), mkrs(a->qkv + QSZ, QSZ))); \
    v16u sB = cat8(cat4(mkrs(a->qkv + 2 * (size_t)QSZ, QSZ), v4u{0, 0, 0, 0}), \
                   v8u{0, (unsigned)a->per0, (unsigned)a->d1, (unsigned)a->dv, (unsigned)a->d2, (unsigned)a->dp, (unsigned)a->slack, (unsigned)a->thr}); \
    const v4u rsc = __builtin_shufflevector(sA, sA, 4, 5, 6, 7); \
    unsigned b0, b1
#define HALVES() do { const unsigned u_ = beacon(rsc, b0, b1, 16); \
_Pragma("unroll") \
    for (int k = 0; k < NSLOT; ++k) sB[4 + k] = k * (unsigned)SLOT + ((u_ >> k) & 1) * 8192u; } while (0)

#define START() __hip_atomic_fetch_max((LAS unsigned*)(lds + L_MISC), (rt32() - spare) / sB[14] + 1, __ATOMIC_RELAXED, __HIP_MEMORY_SCOPE_WORKGROUP)

DEV void chain(LAS char* lds, const int c) {
    KArgs a = args();
    const int w = __builtin_amdgcn_readfirstlane(threadIdx.x >> 6), h = c >> 2, j = c & 3;
    const unsigned lb = (unsigned)(size_t)lds, lbw = lb + L_STG + w * 16384;
#define LANE() int l = threadIdx.x & 63; asm volatile("" : "+v"(l)); const int l15 = l & 15, g = l >> 4, hb = (l >> 1) & 1, q4 = l15 >> 2; const unsigned idx = hb ? 0xEEEE : 0x4444
    v4u sC, stv; v2u stw; unsigned stx; v8u so;
    unsigned lwo = w & 3;
    if (w < 4) {
        LANE();
        v16u wt[12], m;
            const v4u rw[3] = {mkrs(a->Wk, DM * HD * 2), mkrs(a->Wv, DM * HD * 2), mkrs(a->Wq, DM * HD * 2)};
            const unsigned vo = (l >> 2) * (HD * 2) + (l & 3) * 16, sw = (256 * w * HD + h * DH + 32 * j) * 2;
            auto issue = [&](int p) {
#pragma unroll
                for (int i = 0; i < 8; ++i) DMA16N(rw[p >> 1], vo, sw + ((p & 1) * 128 + i * 16) * (HD * 2), lbw + (p & 1) * 8192 + i * 1024); };
            {
                const v4u rp = w & 1 ? rw[2] : rw[1];
                const unsigned ps = ((rp[0] >> 7) + (c >> 5) * 16) & 63, po = w < 2 ? ((3 - ps) & 7) * 128 + (l & 1) * 1024 : (63 - ps < 16 && !(l & 1) ? (63 - ps) * 128 : OOB);
                if (a->pf) asm volatile("s_mov_b32 m0, %0\n\ts_nop 0\n\tbuffer_load_dword %1, %2, 0 offen lds" :: "s"(lb + L_PF + w * 256), "v"(((c & 31) * 32 + (l >> 1)) * (HD * 2) + (c >> 5) * 2048 + po), "s"(rp) : "memory", "m0");
            }
            unsigned mr[16];
#pragma unroll
            for (int k = 0; k < 16; ++k) mr[k] = *(const unsigned*)(a->mem0 + (2 * q4 + (l & 1)) * DM + 256 * w + 16 * k + 4 * g + 2 * hb);
            SCHED();
            issue(0); issue(1);
            RBAR();
            MK_S();
            unsigned spare = a->spare; asm volatile("" : "+s"(sA), "+s"(sB), "+s"(spare));
            BC_LD(b0, rsc);
            const LAS char* pw = lds + L_STG + w * 16384 + (4 * (g >> 1) + (g & 1) + 2 * (q4 >> 1) + 16 * (q4 & 1)) * 64 + (l & 3) * 8;
#pragma unroll
            for (int p = 0; p < 6; ++p) {
                if (p < 5) WAITV(8); else WAITV(0);
#pragma unroll
                for (int k = 0; k < 2; ++k) wt[(2 * p + k + 4) % 12] = cat8(rd_w<2048, 512, 2560>(pw + (p & 1) * 8192 + k * 4096), rd_w<2048, 512, 2560>(pw + (p & 1) * 8192 + k * 4096 + 32));
                WAITL(0);
                if (p < 4) issue(p + 2);
                if (p == 3) BC_LD(b1, rsc);
            }
#pragma unroll
            for (int k = 0; k < 16; k += 4) {
                m[k] = __builtin_amdgcn_perm(mr[k + 1], mr[k], 0x05040100u); m[k + 1] = __builtin_amdgcn_perm(mr[k + 3], mr[k + 2], 0x05040100u);
                m[k + 2] = __builtin_amdgcn_perm(mr[k + 1], mr[k], 0x07060302u); m[k + 3] = __builtin_amdgcn_perm(mr[k + 3], mr[k + 2], 0x07060302u);
            }
        const unsigned pq = nearo(H3_OFF + (c >> 5) * 16384) + w * 1024 + l * 16;
        const v8u va = {idx, pq, 0, 0, lb + L_MISC + w * 4, lb + L_MISC, 0, pq + 16384};
        sC = v4u{lb + w * 2176u, lb + w * 1280u, 0, 0};
        HALVES();
        START();
        asm volatile(ASM_A : S_IO, "+{v[192:207]}"(m)
                     : "{v[0:31]}"(cat16(wt[0], wt[1])), "{v[32:63]}"(cat16(wt[2], wt[3])), "{v[64:95]}"(cat16(wt[4], wt[5])), "{v[96:127]}"(cat16(wt[6], wt[7])),
                       "{v[128:159]}"(cat16(wt[8], wt[9])), "{v[160:191]}"(cat16(wt[10], wt[11])), "{v[232:239]}"(va)
                     : S_CLOB, "v208", "v209", V8(21), V8(22), "v230", "v231", "v240", "v241", "v244", "v245", "v246", "v247");
        FIN();
    } else {
        LANE();
        const int bw = w - 4, l7 = l & 7;
        MK_S();
        if (w == 6 && l < 16) ((LAS unsigned*)(lds + L_MISC))[l] = l ? 0 : rt32() / sB[14];
        BAR();
        BC_LD(b0, rsc);
        {
            const v4u rw = mkrs(a->Wo, HD * DM * 2);
            const unsigned vo = (l >> 3) * (DM * 2) + l7 * 16, sw = (h * DH * DM + 256 * j + 64 * bw) * 2;
#pragma unroll
            for (int i = 0; i < 16; ++i) DMA16N(rw, vo, sw + i * 8 * (DM * 2), lbw + i * 1024);
        }
        BC_LD(b1, rsc);
        WAITV(0);
        HALVES();
        const LAS char* pw = lds + L_STG + w * 16384 + (16 * (g & 1) + 8 * (g >> 1) + (q4 & 1) + 4 * (q4 >> 1)) * 128 + (l & 3) * 8;
#define RDO(o) rd_w<256, 4096, 4352>(pw + (o))
        const v32u wo0 = cat16(cat8(RDO(0), RDO(32)), cat8(RDO(64), RDO(96))), wo1 = cat16(cat8(RDO(8192), RDO(8224)), cat8(RDO(8256), RDO(8288)));
        const unsigned c3 = ((c >> 3) & 3) * 4096 + (c >> 5) * 1024 + (c & 7) * 128, h1 = nearo(H1_OFF + h * 8192), h1j = h1 + j * 2048;
        v16u va = {};
        va[0] = idx; va[1] = h1 + (g >> 1) * 2048 + 1280 + (l & 31) * 16;
        va[2] = nearo(H2_OFF + (32 * j + 8 * bw + q4 * 2 + (g >> 1)) * 4096) + h * 128 + ((g & 1) * 4 + (l & 3)) * 16;
        va[3] = lb + L_P + g * 320 + l15 * 16;
        va[4] = lb + (l & 1) * X_BLK + (l & 2) * X_FQ + (8 * (g >> 1) + (q4 & 1) + 4 * (q4 >> 1)) * 16 + (g & 1) * 8;
        va[5] = lb + L_MISC;
        v4u sel = {};
        if (w == 4) {
            const int tok = l & 7, u = l >> 5, z = (l >> 3) & 3;
            va[6] = h1 + (z == 0 ? 8 + u : (z == 1 ? 2 * u : (z == 2 ? 4 + 2 * u : 2 * u + 1))) * 128 + tok * 16;
            va[7] = h1 + (5 + 2 * u) * 128 + tok * 16 + (z & 1) * 8;
            va[8] = lb + L_P + ((z & 2) + u) * 320 + (4 * (tok >> 1) + 2 * (z & 1) + (tok & 1)) * 16;
            va[9] = l == 0 ? F_PG + c * 128 : OOB;
            *(LAS v4u*)(lds + va[8]) = v4u{POI, POI, POI, POI};
            sC = v4u{__float_as_uint(SCALE * LOG2E), __float_as_uint(SCALE * LOG2E), 0, 0};
        } else if (w == 5) {
            va[6] = lb + L_PQK + (l15 >> 3) * 256 + (l & 1) * 544 + (l7 >> 1) * 64 + g * 16;
            va[7] = (l & 8) ? OOB : h1j + g * 128 + l7 * 16;
            va[8] = ((l & 8) || g < 2) ? OOB : h1j + 1024 + ((g & 1) * 8 + l7) * 16;
            va[9] = (l & 8) ? h * 4096 + j * 512 + (g * 8 + l7) * 16 : OOB;
            va[10] = lb + l * 16;
            sC = v4u{0, 0, 0, 0};
        } else if (w == 6) {
            const int rho = (l >> 1) & 15, tok = 2 * (rho >> 2) + (rho & 1), c4 = 2 * (l >> 5) + ((rho >> 1) & 1);
            va[6] = lb + ((l & 1) * 160 + (tok & 1) * 72 + (tok >> 1) * 16 + c4 * 4) * 4;
            va[7] = (l & 1) ? OOB : h1j + 1280 + (l >> 1) * 16;
            va[8] = lb + L_VT + tok * 64 + (l & 1) * 32 + c4 * 8;
            va[9] = lb + L_VT + q4 * 64 + (g & 1) * 32 + (l & 3) * 8;
            va[10] = l < 32 ? h * 4096 + 2048 + (32 * j + l) * 16 : OOB;
            va[11] = nearo(H2_OFF + c * 4096) + l * 16;
            va[12] = (l & 1) ? OOB : nearo(H3_OFF + (2 * (g >> 1) + (l15 >> 3)) * 16384 + c3) + ((l7 & 6) + (g & 1)) * 16;
#pragma unroll
            for (int i = 0; i < 4; ++i) sel[i] = (l7 == 2 * i ? 0x3f80u : 0u) | (l7 == 2 * i + 1 ? 0x3f800000u : 0u);
            sC = v4u{0, 0, 0, 0};
        } else {
            const rsrc_t rk = mkrsrc(a->qkv + QSZ, QSZ), rv = mkrsrc(a->qkv + 2 * (size_t)QSZ, QSZ);
            wait_flags(mkrsrc(a->ctl, CTL_SZ), fw(l & 1, h) + 12, 1, a->ctl);
            v4u pf[10];
            const unsigned hb0 = h * 16384 + (KVS(a->ms >> 4, h) ^ sB[4] ^ 8192), kx = j * 2048 + (KVS(a->ms >> 4, h) ^ KVS(a->ms >> 2, h));
#pragma unroll
            for (int i = 0; i < 8; ++i) pf[i] = LDB(rv, i, hb0);
            pf[8] = LDB(rk, 0, hb0 ^ kx); pf[9] = LDB(rk, 1, hb0 ^ kx);
            va[13] = LD4(mkrsrc(a->ctl, CTL_SZ), fw(l & 1, h) + 12, 0);
#pragma unroll
            for (int i = 0; i < 8; ++i) *(LAS v4u*)(lds + L_X + i * X_BLK + g * X_FQ + l15 * 16) = pf[i];
            *(LAS v4u*)(lds + L_K + l * 16) = pf[8]; *(LAS v4u*)(lds + L_K + 1024 + l * 16) = pf[9];
            FENCE(); ((volatile LAS unsigned*)(lds + L_MISC))[2] = 1;
            va[6] = l * 16; va[7] = lb + g * X_FQ + l15 * 16;
            va[8] = nearo(H2_OFF + c * 4096) + l * 16; va[9] = h1j + l * 16;
            va[10] = nearo(H3_OFF + (l >> 3) * 16384 + c3) + l7 * 16;
            va[11] = fw(l & 1, h) + 12;
            va[15] = l == 0 && (c & 31) == 0 ? F_CK + 384 + (c >> 5) * 4 : OOB;
            sC = v4u{hb0, __float_as_uint(a->per), 0, kx};
        }
        while (((volatile LAS unsigned*)(lds + L_MISC))[2] == 0) __builtin_amdgcn_s_sleep(1);
        asm volatile(ASM_B : S_IO, "+{v[224:239]}"(va), "+{v[244:247]}"(sel) : "{v[0:31]}"(wo0), "{v[32:63]}"(wo1) : B_CLOB);
        FIN();
    }
}

#define RING_LD(K, B) asm volatile("buffer_load_dwordx4 %0, %1, %2, %3 offen offset:%4 sc1" : "+v"(ring[K]) : "v"(l16), "s"(rso), "s"((B) + ((K) >> 3) * 4096 + (((K) >> 2) & 1) * 16384), "n"(((K) & 3) * 1024) : "memory")
#define RING_MM(K, M, C0, C1, C2, C3) asm volatile("s_waitcnt vmcnt(19)\n\tv_mfma_f32_16x16x32_bf16 %0, %5, %4, " C0 "\n\tv_mfma_f32_16x16x32_bf16 %1, %6, %4, " C1 "\n\tv_mfma_f32_16x16x32_bf16 %2, %7, %4, " C2 "\n\tv_mfma_f32_16x16x32_bf16 %3, %8, %4, " C3 \
    : M(c[0]), M(c[1]), M(c[2]), M(c[3]), "+v"(ring[K]) : "v"(wo[(K) & 7][0]), "v"(wo[(K) & 7][1]), "v"(wo[(K) & 7][2]), "v"(wo[(K) & 7][3]) : "memory")
#define RING_1(K, B, O) "buffer_load_dwordx4 %" #K ", %16, %17, %" #B " offen offset:" #O " sc1\n\t"
#define RING_4(K0, K1, K2, K3, B) RING_1(K0, B, 0) RING_1(K1, B, 1024) RING_1(K2, B, 2048) RING_1(K3, B, 3072)
#define RING_ALL(P) P(ring[0]), P(ring[1]), P(ring[2]), P(ring[3]), P(ring[4]), P(ring[5]), P(ring[6]), P(ring[7]), P(ring[8]), P(ring[9]), P(ring[10]), P(ring[11]), P(ring[12]), P(ring[13]), P(ring[14]), P(ring[15])
#define NPROBE 3
#define TSEG 34
static constexpr int RED_B = 65536;

DEV void worker(LAS char* lds, const int wk) {
    KArgs a = args();
    const int tid = threadIdx.x, w = __builtin_amdgcn_readfirstlane(tid >> 6), l = tid & 63, fr = l & 15, fq = l >> 4;
    const int g = wk >> 5, i = wk & 31;
    const rsrc_t rc = mkrsrc(a->ctl, CTL_SZ);
    unsigned* err = a->ctl;
    unsigned* dbg = a->dbg + 32768 + wk * 32;
    WSTAMP(0);
    const v4u rx = mkrs(a->x, S_TOK * DM * 2);
    const int role = i >> 3;
    const int h = 8 * g + (i & 7);
    const unsigned me = F_W + wk * 128;
    unsigned far = 0;
    v4u rsc = mkrs(a->ctl, CTL_SZ);
    if (role >= 2) {
        const unsigned t0 = rt32();
        const rsrc_t rr = mkrsrc(a->ring, NSLOT * SLOT);
        unsigned b0, ak = 1;
        BC_LD(b0, rsc);
        if (role == 2) {
#pragma unroll
            for (int k = 0; k < 14; ++k) ST16(rr, (h >> 4) * (unsigned)SLOT + ((h & 15) * 14 + k) * 8192 + tid * 16, 0, (v4u{POI, POI, POI, POI}));
        } else if (wk == 127) {
#pragma unroll
            for (int k = 0; k < 8; ++k) ST16(rc, k == 0 && (tid >> 3) == F_BC / 128 ? OOB : k * 8192 + tid * 16, 0, (v4u{0, 0, 0, 0}));
        } else if (i < 28 && w == 0) {
            unsigned t[2] = {~0u, ~0u};
            for (int it = 0; it < NPROBE; ++it) {
#pragma unroll
                for (int p = 0; p < 2; ++p) { const unsigned c0 = cyc32(); ST4(rr, (i & 3) * (unsigned)SLOT + p * 8192 + nearo(PR_OFF) + g * 256 + (l & 31) * 4, 0, c0); WAITV(0); t[p] = min(t[p], cyc32() - c0); }
            }
            ak = 256 + min(max((int)(t[1] - t[0]), -100), 100);
        }
        asm volatile("s_waitcnt vmcnt(0)\n\ts_barrier" : "+v"(b0) :: "memory");
        WSTAMP(1);
        if (w == 0) {
            if (wk != 127) beacon(rsc, b0, b0, 0);
            ST4(rc, l ? OOB : (wk == 127 ? F_BC : me), 0, ak);
        }
        RBAR();
        const unsigned d = role == 2 ? a->wq : (wk == 127 ? 0 : a->wx);
        while (rt32() - t0 < d) __builtin_amdgcn_s_sleep(8);
        if (role == 3) prep_rows(a->x, a->gw, a->w_r, rc, h * 64 + w * 8, l);
        if (w == 0 && wk == 127) {
            unsigned bc = 0;
            for (unsigned tries = 0; tries < SPIN_CAP; ++tries) {
                bc += 32; ST4(rc, l ? OOB : F_BC, 0, bc);
                FENCE();
                if (!(bc & 16)) {
                    unsigned v = LD4(rc, F_W + ((l >> 4) * 32 + 16 + fr) * 128, 0);
                    if (__builtin_amdgcn_ballot_w64(v == 0) << 1 == 0) {
                        v += __shfl_xor((int)v, 16); v += __shfl_xor((int)v, 32);
                        bc |= 16 | ((unsigned)(__builtin_amdgcn_ballot_w64(v > 1024) >> 8) & 15);
                        continue;
                    }
                } else if (rt32() - t0 > 2400 && all_ge(LD4(rc, F_PG, 0), 1)) break;
                __builtin_amdgcn_s_sleep(16);
            }
            WSTAMP(5);
        }
    }
    if (role == 1) far = stream_qkv<true>(lds, rx, mkrs(a->Wv, DM * HD * 2), mkrsrc(a->qkv + 2 * (size_t)QSZ, QSZ), rc, rsc, true, me + 12, h, KVS(a->ms >> 4, h), dbg);
    else if (role < 3) far = stream_qkv<false>(lds, rx, mkrs(role ? a->Wq : a->Wk, DM * HD * 2), mkrsrc(a->qkv + (role ? 0 : QSZ), QSZ), rc, rsc, role == 0, me + 12, h, role ? 0 : KVS(a->ms >> 2, h), dbg);
    else if (h < NMOE) {
        const int sel = top8((LAS unsigned*)(lds + L_TOT - 256), rc, err, w, l);
        WSTAMP(26);
        if (h == 0 && tid < TOPC) ST4(mkrsrc(a->idx, 32), tid * 4, 0, sel);
        moe(lds, rx, sel, mkrs(a->wg, DM * FF * 2), mkrs(a->wu, DM * FF * 2), mkrs(a->wd, FF * DM * 2), mkrsrc(a->part, NMOE * TOPC * DM * 4), h);
        if (tid == 0) ST4(rc, me + 4, 0, 1u);
    }
    if (role == 3) far ^= FAR(beacon(rsc, 0, 0, 16));
    WSTAMP(2);
    a = args();
    const rsrc_t ri = mkrsrc(a->idx, 32), ru = mkrsrc(a->upd, TOPC * DM * 4), rt = mkrsrc(a->wt, HD * DM * 4);
    const bool rcv = role == 3 ? i < 28 : role == 2 || g >= 2;
    const int cg = role == 3 ? 4 * g + (i & 3) : (role == 2 ? h & 15 : i), k0 = (rcv ? 0 : 64) + 8 * w;
    const int seg0 = role == 3 ? 0 : (role == 2 ? 2 + (g >> 1) : (rcv ? g - 2 : TSEG + g)), t1 = role == 3 ? TSEG - 1 : 0, sst = rcv ? 4 : 3;
    auto next = [&](int s) { return s + (s < t1 ? 1 : sst); };
    auto fx = [&](int s, int l) { return fw(3, (rcv ? 0 : 16) + (l & 1) * 8 + (s & 3)) + 64 + (s & 60); };
    const unsigned fe = F_EX + cg * 256, wb = side((cg * 128 + k0) * 8192 + far);
    volatile LAS unsigned* prog = (volatile LAS unsigned*)(lds + 2 * RED_B);
    LAS v4f* upd = (LAS v4f*)(lds + 2 * RED_B + 64);
    v4u wo[8][4];
    int sel[8];
    if (role == 3) {
        {
            const v4u rw = mkrs(a->Wo, HD * DM * 2);
            const unsigned vo = (l >> 3) * (DM * 2) + (((l & 7) ^ ((l >> 3) & 6)) << 4), lb = (unsigned)(size_t)lds + w * 16384;
            auto issue = [&](int p) {
#pragma unroll
                for (int k = 0; k < 8; ++k) DMA16N(rw, vo, ((32 * k0 + 64 * p + 8 * k) * DM + 64 * cg) * 2, lb + (p & 1) * 8192 + k * 1024); };
            issue(0); issue(1);
            const int row = 4 * fq + (fr >> 2);
#pragma unroll
            for (int p = 0; p < 4; ++p) {
                if (p < 3) WAITV(8); else WAITV(0);
#pragma unroll
                for (int t = 0; t < 4; ++t) {
                    const LAS char* q = lds + w * 16384 + (p & 1) * 8192 + row * 128 + (((2 * t + ((l >> 1) & 1)) ^ (row & 6)) << 4) + (l & 1) * 8;
                    wo[2 * p][t] = cat(trd(q), trd(q + 2048)); wo[2 * p + 1][t] = cat(trd(q + 4096), trd(q + 6144));
                }
                WAITL(0);
                if (p < 2) issue(p + 2);
            }
#pragma unroll
            for (int k = 0; k < 32; ++k) ST16(rt, wb + l * 16 + (k + (k & 24)) * 1024, 0, wo[k >> 2][k & 3]);
        }
        WSTAMP(7);
        {
            wait_flags<16>(rc, fw(3, min(l, NMOE - 1)) + 4, 1, err);
            const rsrc_t rp = mkrsrc(a->part, NMOE * TOPC * DM * 4);
            const unsigned t = LD4(ri, (l >> 3) * 4, 0), o = (l >> 3) * 4096 + h * 128 + (l & 7) * 16;
            v4f p = {0, 0, 0, 0};
#pragma unroll
            for (int q = 0; q < 4; ++q) p += __builtin_bit_cast(v4f, LD16(rp, (w * 4 + q) * 32768 + o, 0));
            const float r = rbf(sigmoidf_(hi16(LD4(rc, F_GR + t * 8 + 4, 0))));
            upd[tid] = p;
            WAITV(0); BAR();
            if (w == 0) {
#pragma unroll
                for (int q = 1; q < 7; ++q) p += upd[q * 64 + l];
                ST16(ru, o, 0, __builtin_bit_cast(v4u, (v4f{rbf(rbf(p[0]) * r), rbf(rbf(p[1]) * r), rbf(rbf(p[2]) * r), rbf(rbf(p[3]) * r)})));
                WAITV(0);
                if (l == 0) ST4(rc, me + 8, 0, 1u);
                prog[1] = 0;
            }
        }
        if (rcv) {
            WSTAMP(8);
            const rsrc_t rq = mkrsrc(a->qkv, QSZ), rk = mkrsrc(a->qkv + QSZ, QSZ), rv = mkrsrc(a->qkv + 2 * (size_t)QSZ, QSZ), rm = mkrsrc(a->mb, NSEG * NH * 4096);
            unsigned fl = 0;
#pragma unroll 1
            for (int xs = i & 3; xs < NSEG; xs += 4) { fl = xattn(rq, rk, rv, rm, rc, err, xs, 8 * g + w, l, fl, far, a->ms, dbg); WSTAMP(9 + (xs >> 2)); }
            WSTAMP(25);
            return;
        }
#pragma unroll
        for (int q = 0; q < 8; ++q) sel[q] = 0;
    } else {
        wait_flags(rc, fw(3, l < 2 ? 2 * cg + l : 2 * (cg & 12) + (cg & 3) + (rcv ? 0 : 4)) + 8, 1, err);
        const unsigned t = LD4(ri, (l & 7) * 4, 0), n1 = LD4(rc, l < 2 ? fx(next(seg0), l) : fe + seg0 * 4, 0);
        const v4u u = LD16(ru, w < 2 ? (tid >> 4) * 4096 + cg * 256 + fr * 16 : OOB, 0);
#pragma unroll
        for (int k = 0; k < 32; ++k) wo[k >> 2][k & 3] = LDBX(rt, k + (k & 24), wb, NTS & 8);
        if (w < 2) upd[tid] = __builtin_bit_cast(v4f, u);
#pragma unroll
        for (int q = 0; q < 8; ++q) sel[q] = __builtin_amdgcn_readlane(t, q);
        if (w == 0) prog[1] = (__builtin_amdgcn_readlane(n1, 0) & __builtin_amdgcn_readlane(n1, 1)) | __builtin_amdgcn_readlane(n1, 2) << 1;
        WAITV(0);
        WSTAMP(7);
    }
    unsigned pre = 0;
    v4u ring[16];
    asm volatile("" : RING_ALL("=v"));
    const v4u rso = mkrs(a->qkv, QSZ), rse = mkrs(a->qkv + QSZ, QSZ);
    rsc = mkrs(a->ctl, CTL_SZ);
    const rsrc_t re = mkrsrc(a->qkv + QSZ, QSZ), rout = mkrsrc(a->out, S_TOK * DM * 2);
    BAR();
    unsigned st = __builtin_amdgcn_readfirstlane(prog[1]);
    WSTAMP(8);
#pragma unroll 1
    for (int n = 0, seg = seg0, prv = OOB / 4; seg < NSEG; ++n, prv = seg, seg = next(seg)) {
        const int s1 = next(seg), s2 = next(s1);
        const unsigned ob = side((unsigned)(seg * 128 + k0) * 4096u + far);
        int l = tid & 63; asm volatile("" : "+v"(l));
        const int fr = l & 15, fq = l >> 4, l16 = l * 16, row = seg * 32 + (w & 1) * 16 + fr, col = (w >> 1) * 16 + 4 * fq;
        if (!pre) wait_flags(rc, fx(seg, l), 1, err);
        asm volatile("s_cmp_lg_u32 %18, 0\n\ts_cbranch_scc1 L%=\n\t" RING_4(0, 1, 2, 3, 19) RING_4(4, 5, 6, 7, 20) RING_4(8, 9, 10, 11, 21) RING_4(12, 13, 14, 15, 22) "L%=:"
                     : RING_ALL("+v") : "v"(l16), "s"(rso), "s"(pre), "s"(ob), "s"(ob + 16384), "s"(ob + 4096), "s"(ob + 20480) : "memory", "scc");
        pre = st & 1;
        const bool got = rcv && (st & 2);
        const unsigned nb = pre ? side((unsigned)(s1 * 128 + k0) * 4096u + far) : OOB, eo = side((unsigned)(seg * NH + 16 + cg) * 16384u + far) + tid * 16, oo = rcv ? (row * DM + 64 * cg + col) * 2 : OOB;
        unsigned sm; v2u xv; float gt; v4f pv;
        v4f c[4];
        asm volatile("buffer_load_dword %[sm], %[so], %[rc], 0 offen sc1\n\tbuffer_load_dwordx4 %[pv], %[eo], %[re], 0 offen sc1\n\tbuffer_load_dwordx2 %[xv], %[xo], %[rx], 0 offen\n\t"
                     "buffer_load_dword %[gt], %[go], %[rc], 0 offen sc1"
                     : [sm] "=&v"(sm), [pv] "=&v"(pv), [xv] "=&v"(xv), [gt] "=&v"(gt)
                     : [rc] "s"(rsc), [re] "s"(rse), [rx] "s"(rx), [so] "v"(w ? OOB : (l < 2 ? (s2 < NSEG ? fx(s2, l) : OOB) : (rcv ? fe + s1 * 4 : OOB))),
                       [eo] "v"(got ? eo : OOB), [xo] "v"(oo), [go] "v"(F_GR + row * 8) : "memory");
        LAS v4f* rb = (LAS v4f*)(lds + (n & 1) * RED_B) + l;
#pragma unroll
        for (int k = 0; k < 16; ++k) {
            if (k & 7) RING_MM(k, "+v", "%0", "%1", "%2", "%3"); else RING_MM(k, "=&v", "0", "0", "0", "0");
            RING_LD(k, nb);
            if ((k & 7) == 7) {
#pragma unroll
                for (int t = 0; t < 4; ++t) { asm volatile("s_nop 3" : "+v"(c[t]) :: "memory"); rb[((2 * t + (k >> 3)) * 8 + w) * 64] = c[t]; }
            }
        }
        asm volatile("s_waitcnt vmcnt(16)" : "+v"(sm), "+v"(pv), "+v"(xv), "+v"(gt) :: "memory");
        const unsigned sn = (__builtin_amdgcn_readlane(sm, 0) & __builtin_amdgcn_readlane(sm, 1)) | __builtin_amdgcn_readlane(sm, 2) << 1;
        if (tid == 0) prog[n & 1] = sn;
        BAR();
        if (!rcv && tid == 0) ST4(rc, fe + prv * 4, 0, 1u);
        v4f acc = rb[w * 512];
#pragma unroll
        for (int q = 1; q < 8; ++q) acc += rb[w * 512 + q * 64];
        st = __builtin_amdgcn_readfirstlane(prog[n & 1]);
        if (rcv) {
            int slot = -1;
#pragma unroll
            for (int q = 0; q < 8; ++q) slot = sel[q] == row ? q : slot;
            if (!got) { wait_flags(rc, fe + seg * 4, 1, err); pv = __builtin_bit_cast(v4f, LD16(re, eo, 0)); asm volatile("" : "+v"(pv)); }
            v4f b = {lo16(xv[0]), hi16(xv[0]), lo16(xv[1]), hi16(xv[1])};
            if (slot >= 0) { b += upd[slot * 16 + (col >> 2)]; b = v4f{rbf(b[0]), rbf(b[1]), rbf(b[2]), rbf(b[3])}; }
            acc += pv;
            gt = hi16(__float_as_uint(gt));
            const float omg = rbf(1.0f - gt);
#pragma unroll
            for (int e = 0; e < 4; ++e) acc[e] = rbf(gt * rbf(acc[e])) + rbf(omg * b[e]);
            __builtin_amdgcn_raw_buffer_store_b64(v2u{pk2(acc[0], acc[1]), pk2(acc[2], acc[3])}, rout, oo, 0, 0);
        } else {
            ST16(re, eo, 0, __builtin_bit_cast(v4u, acc));
            if (!pre) { WAITV(0); RBAR(); if (tid == 0) ST4(rc, fe + seg * 4, 0, 1u); }
        }
        WSTAMP(9 + (seg >> 2));
    }
    asm volatile("s_waitcnt vmcnt(0)" :: RING_ALL("v") : "memory");
    WSTAMP(25);
}

__global__ void __launch_bounds__(512, 2) k_main(Args) {
    __shared__ __attribute__((aligned(16))) char lds[L_TOT];
    const int b = blockIdx.x, c = (b & 3) * 32 + (b >> 3);
    if ((b & 7) < 4) chain((LAS char*)lds, c); else worker((LAS char*)lds, c);
}

#define RING_MB 160
#define TUNE_DEFAULT {184, 73, 77, 138, 8, 80, 352, 100, 700, 700, 1, 20, 0}

extern "C" void kernel_launch(void* const* d_in, const int* in_sizes, int n_in, void* d_out, int out_size, void* d_ws, size_t ws_size, hipStream_t stream) {
    char* ring = (char*)(((size_t)d_ws + SLOT - 1) & ~(size_t)(SLOT - 1));
    char* ws = ring + NSLOT * SLOT; size_t off = 0;
    if (ws_size >= ((size_t)RING_MB + 12 << 20)) ring += (size_t)RING_MB << 20;
    auto alloc = [&](size_t bytes) { void* p = ws + off; off += (bytes + 255) & ~(size_t)255; return p; };
    Args a;
    a.x = (const bf16_t*)d_in[0]; a.mem0 = (const bf16_t*)d_in[1]; a.Wq = (const bf16_t*)d_in[2]; a.Wk = (const bf16_t*)d_in[3]; a.Wv = (const bf16_t*)d_in[4]; a.Wo = (const bf16_t*)d_in[5];
    a.w_r = (const bf16_t*)d_in[8]; a.wg = (const bf16_t*)d_in[9]; a.wu = (const bf16_t*)d_in[10]; a.wd = (const bf16_t*)d_in[11]; a.gw = (const bf16_t*)d_in[12];
    a.qkv = (char*)alloc(3 * (size_t)QSZ); a.wt = (char*)alloc(HD * DM * 4); a.mb = (char*)alloc(NSEG * NH * 4096);
    a.idx = (int*)alloc(256); a.part = (float*)alloc(NMOE * TOPC * DM * 4); a.upd = (float*)alloc(TOPC * DM * 4); a.out = (bf16_t*)d_out;
    a.ring = ring; a.ctl = (unsigned*)alloc(CTL_SZ); a.dbg = (unsigned*)alloc((65536 + 1024 * 512) * 4);
    const int tn[13] = TUNE_DEFAULT;
    a.per = tn[0] * 368.0f; a.per0 = tn[0] * 368 / tn[6]; a.pf = tn[10]; a.ms = tn[11]; a.ws = tn[12];
    a.d1 = ((u64)tn[1] << 32) / tn[0]; a.dv = ((u64)tn[2] << 32) / tn[0]; a.d2 = ((u64)tn[3] << 32) / tn[0]; a.dp = tn[4]; a.slack = tn[5]; a.thr = tn[6]; a.spare = tn[7]; a.wq = tn[8]; a.wx = tn[9];
    k_main<<<256, 512, 0, stream>>>(a);
}
```

```cpp
#include <hip/hip_runtime.h>
#include <stdint.h>

typedef unsigned short bf16_t;
typedef unsigned v2u __attribute__((ext_vector_type(2)));
typedef unsigned v4u __attribute__((ext_vector_type(4)));
typedef unsigned v8u __attribute__((ext_vector_type(8)));
typedef float v2f __attribute__((ext_vector_type(2)));
typedef float v4f __attribute__((ext_vector_type(4)));
typedef short v4s __attribute__((ext_vector_type(4)));
typedef __bf16 v2b __attribute__((ext_vector_type(2)));
typedef __bf16 v8b __attribute__((ext_vector_type(8)));
typedef __bf16 v16b __attribute__((ext_vector_type(16)));
typedef unsigned long long u64;
typedef __amdgpu_buffer_rsrc_t rsrc_t;
#define LAS __attribute__((address_space(3)))
#define DEV __device__ __forceinline__

static constexpr int S_TOK = 2048, DM = 1024, NH = 32, DH = 128, SEG = 32, NSEG = 64;
static constexpr int HD = NH * DH;
static constexpr int FF = 3584, TOPC = 8;
static constexpr float SCALE = 0.08838834764831845f, LOG2E = 1.4426950408889634f, MASKV = -998244352.0f;

DEV unsigned pk2(float lo, float hi) { v2f f = {lo, hi}; return __builtin_bit_cast(unsigned, __builtin_convertvector(f, v2b)); }
DEV float lo16(unsigned u) { return __uint_as_float(u << 16); }
DEV float hi16(unsigned u) { return __uint_as_float(u & 0xffff0000u); }
DEV float rbf(float f) { return __uint_as_float(pk2(0.0f, f)); }
DEV v4f smf(v4u a, v8u b, v4f c, int idx) { return __builtin_amdgcn_smfmac_f32_16x16x64_bf16(__builtin_bit_cast(v8b, a), __builtin_bit_cast(v16b, b), c, idx, 0, 0); }
DEV v4f mf(v4u a, v4u b, v4f c) { return __builtin_amdgcn_mfma_f32_16x16x32_bf16(__builtin_bit_cast(v8b, a), __builtin_bit_cast(v8b, b), c, 0, 0, 0); }
DEV v2u trd(const LAS char* p) { return __builtin_bit_cast(v2u, __builtin_amdgcn_ds_read_tr16_b64_v4i16((LAS v4s*)p)); }
DEV v4u cat(v2u a, v2u b) { return __builtin_shufflevector(a, b, 0, 1, 2, 3); }
DEV v2u sw16(unsigned x, unsigned y) { return __builtin_amdgcn_permlane16_swap(x, y, false, false); }
DEV v2u sw32(unsigned x, unsigned y) { return __builtin_amdgcn_permlane32_swap(x, y, false, false); }
DEV float half_add(float x, float y) { v2u r = sw32(__float_as_uint(x), __float_as_uint(y)); return __uint_as_float(r[0]) + __uint_as_float(r[1]); }
DEV float ror8(float v) { return __int_as_float(__builtin_amdgcn_update_dpp(0, __float_as_int(v), 0x128, 0xf, 0xf, false)); }
template <bool MAX> DEV float red_g(float v) {
    auto op = [](float a, float b) { return MAX ? fmaxf(a, b) : a + b; };
    v2u r = sw16(__float_as_uint(v), __float_as_uint(v)); v = op(__uint_as_float(r[0]), __uint_as_float(r[1]));
    r = sw32(__float_as_uint(v), __float_as_uint(v)); return op(__uint_as_float(r[0]), __uint_as_float(r[1]));
}
DEV float add1(float a, float b) { float r; asm("v_add_f32 %0, %1, %2" : "=v"(r) : "v"(a), "v"(b)); return r; }
DEV v4f add4(v4f a, v4f b) { return v4f{add1(a[0], b[0]), add1(a[1], b[1]), add1(a[2], b[2]), add1(a[3], b[3])}; }
DEV float sigmoidf_(float z) { return 1.0f / (1.0f + __expf(-z)); }
DEV v4u mkrs(const void* p, unsigned bytes) { const u64 a = (u64)p; return v4u{(unsigned)a, (unsigned)(a >> 32) & 0xffffu, bytes, 0x00020000u}; }
DEV rsrc_t mkrsrc(const void* p, unsigned bytes) { return __builtin_amdgcn_make_buffer_rsrc((void*)p, (short)0, (int)bytes, 0x00020000); }

#define ST16(RS, OFF, SOFF, V) __builtin_amdgcn_raw_buffer_store_b128(V, RS, (OFF) + (SOFF), 0, 16)
#define NTS 0
#define ST16X(RS, OFF, V, NT) __builtin_amdgcn_raw_buffer_store_b128(V, RS, OFF, 0, (NT) ? 18 : 16)
#define LD16X(RS, OFF, SOFF, NT) __builtin_amdgcn_raw_buffer_load_b128(RS, OFF, SOFF, (NT) ? 18 : 16)
#define ST4(RS, OFF, SOFF, V) __builtin_amdgcn_raw_buffer_store_b32(V, RS, OFF, SOFF, 16)
#define LD16(RS, OFF, SOFF) __builtin_amdgcn_raw_buffer_load_b128(RS, OFF, SOFF, 16)
#define LD8(RS, OFF, SOFF) __builtin_amdgcn_raw_buffer_load_b64(RS, OFF, SOFF, 16)
#define LD4(RS, OFF, SOFF) __builtin_amdgcn_raw_buffer_load_b32(RS, OFF, SOFF, 16)
#define DMA16(RS, VOFF, SOFF, LOFF) asm volatile("s_mov_b32 m0, %0\n\ts_nop 0\n\tbuffer_load_dwordx4 %1, %2, %3 offen lds" :: "s"(LOFF), "v"(VOFF), "s"(RS), "s"(SOFF) : "memory", "m0")
#define DMA16N(RS, VOFF, SOFF, LOFF) asm volatile("s_mov_b32 m0, %0\n\ts_nop 0\n\tbuffer_load_dwordx4 %1, %2, %3 offen nt lds" :: "s"(LOFF), "v"(VOFF), "s"(RS), "s"(SOFF) : "memory", "m0")
#define WSTAMP(k) do {} while (0)
#define EXP 0
#define XI 3
#define BK 9
#define FENCE() asm volatile("" ::: "memory")
#define WAITV(n) asm volatile("s_waitcnt vmcnt(" #n ")" ::: "memory")
#define WAITL(n) asm volatile("s_waitcnt lgkmcnt(" #n ")" ::: "memory")
#define RBAR() asm volatile("s_barrier" ::: "memory")
#define BAR() asm volatile("s_waitcnt lgkmcnt(0)\n\ts_barrier" ::: "memory")
#define SCHED() __builtin_amdgcn_sched_barrier(0)

#define KVS(m, h) ((((m) & 2 ? (h) : (m)) & 1u) << 13)
#define FAR(u) ((~(u) & 1u) << 13)
DEV unsigned side(unsigned o) { return o ^ ((o >> 8) & 8192u); }
static constexpr int XP = 1056, XS = 32 * XP, L_RED = 4 * XS;

DEV unsigned wt_src(int tid, unsigned ldw) { return (tid >> 4) * ldw + (((tid & 15) ^ ((((tid >> 4) & 3) << 2) | (((tid >> 7) & 1) << 1))) << 4); }
DEV int wt_rd(int w, int l, int n) { const int fr = l & 15, fq = l >> 4; return (w >> 2) * 16384 + (8 * fq + (fr >> 2)) * 256 + (((4 * (w & 3) + 2 * n + ((fr >> 1) & 1)) ^ (((fr >> 2) << 2) | ((fq & 1) << 1))) << 4) + (fr & 1) * 8; }
DEV v4u wt_frag(const LAS char* p) { return cat(trd(p), trd(p + 1024)); }
#define WT_ISSUE(R0, R1, VB, S0, S1, LDW, LB) do { DMA16N(R0, VB, S0, LB); DMA16N(R0, VB, (S0) + 32 * (LDW), (LB) + 8192); DMA16N(R1, VB, S1, (LB) + 16384); DMA16N(R1, VB, (S1) + 32 * (LDW), (LB) + 24576); } while (0)

static constexpr unsigned F_BC = 512, F_CK = 2048, F_EX = 12288, F_W = 16384, F_PG = 32768, F_GR = 49152, CTL_SZ = 65536;
DEV unsigned fw(int role, int h) { return F_W + ((h >> 3) * 32 + role * 8 + (h & 7)) * 128; }
static constexpr unsigned POI = 0xFFFFFFFFu, OOB = 0x40000000u, SPIN_CAP = 200000u;
DEV bool spin_fail(unsigned tries, unsigned* err) {
    __builtin_amdgcn_s_sleep(1);
    if ((tries & 255u) != 255u) return false;
    unsigned e; asm volatile("global_load_dword %0, %1, off sc1\n\ts_waitcnt vmcnt(0)" : "=&v"(e) : "v"(err) : "memory");
    if (__builtin_amdgcn_readfirstlane(e) == 1u) return true;
    if (tries > SPIN_CAP) { unsigned one = 1u; asm volatile("global_store_dword %0, %1, off sc1" :: "v"(err), "v"(one) : "memory"); return true; }
    return false;
}
DEV bool all_ge(unsigned v, unsigned t) { return __builtin_amdgcn_ballot_w64(v < t) == 0; }
template <int Z = 6> DEV void wait_flags(rsrc_t rc, unsigned off, unsigned t, unsigned* err) {
    for (unsigned tries = 0;; ++tries) {
        FENCE();
        if (all_ge(LD4(rc, off, 0), t)) break;
        __builtin_amdgcn_s_sleep(Z);
        if (spin_fail(tries, err)) break;
    }
    FENCE();
}

#define BC_LD(B, RS) asm volatile("buffer_load_dword %0, off, %1, 0 offset:512 sc1" : "=&v"(B) : "s"(RS) : "memory")
DEV unsigned beacon(v4u rs, unsigned b0, unsigned b1, unsigned need, bool seen = false) {
    asm volatile("" : "+v"(b0), "+v"(b1));
    for (unsigned tries = 0;; ++tries) {
        const unsigned u = __builtin_amdgcn_readfirstlane(b1);
        if (((seen || u != __builtin_amdgcn_readfirstlane(b0)) && (u & need) == need) || tries > SPIN_CAP) return u;
        __builtin_amdgcn_s_sleep(4);
        BC_LD(b1, rs); asm volatile("s_waitcnt vmcnt(0)" : "+v"(b1) :: "memory");
    }
}

template <bool VT> DEV unsigned stream_qkv(LAS char* lds, v4u rx, v4u rw, rsrc_t ro, rsrc_t rc, v4u rsc, bool hs, unsigned fo, int h, unsigned fx, unsigned* dbg) {
    unsigned far = 0;
    const int tid = threadIdx.x, w = __builtin_amdgcn_readfirstlane(tid >> 6), l = tid & 63, kh = w >> 2, cg = w & 3;
    const unsigned lb = (unsigned)(size_t)lds, vb = wt_src(tid, HD * 2), l16 = l * 16;
    const unsigned x0 = (4 * cg + (VT ? 2 : 16) * (1 - kh)) * 2048 + kh * 1024, x1 = (4 * cg + (VT ? 2 : 16) * kh) * 2048 + kh * 1024;
#define STW(s) WT_ISSUE(rw, rw, vb, h * 256 + (s) * (64 * HD * 2), h * 256 + ((s) + 8) * (64 * HD * 2), HD * 2, lb + w * 1024 + ((s) & 3) * XS)
#define STX(u, q, e) DMA16(rx, l16, ((u) >> 1) * 65536 + ((q) & 1 ? x1 : x0) + (VT ? ((e) & 1) + 16 * ((e) >> 1) : (e)) * 2048, lb + (w * 4 + (e)) * XP + (q) * XS)
    const unsigned fl = tid == 0 ? fo : OOB;
    v4u W[16][2];
    unsigned b0, b1;
    STW(0); STW(1); STW(2); STW(3);
    BC_LD(b0, rsc);
#pragma unroll
    for (int s = 0; s < 8; ++s) {
        WAITV(12); RBAR();
#pragma unroll
        for (int n = 0; n < 2; ++n) { const LAS char* p = lds + wt_rd(w, l, n) + (s & 3) * XS; W[2 * s][n] = wt_frag(p); W[2 * s + 1][n] = wt_frag(p + 8192); }
        BAR();
        if (s == 4) BC_LD(b1, rsc);
        if (s == 7) { far ^= FAR(beacon(rsc, b0, b1, 16, !hs)); ST4(rc, OOB, 0, 0u); FENCE(); }
        if (s < 4) STW(s + 4); else { STX(s - 4, s - 4, 0); STX(s - 4, s - 4, 1); if (s < 7) { STX(s - 4, s - 4, 2); STX(s - 4, s - 4, 3); } }
    }
    const LAS char* px = lds + (kh * 16 + (l & 15)) * XP + (l >> 4) * 16;
    LAS v4f* ps = (LAS v4f*)(lds + L_RED + w * 2048 + l16);
    const LAS v4f* pr = (const LAS v4f*)(lds + L_RED + (w ^ 4) * 2048 + l16);
    unsigned oo = OOB, on = (unsigned)h * 16384u + (far ^ fx) + cg * 2048u + kh * 1024u + l16;
    v4f c[2][2] = {}, r0 = {}, r1 = {};
    v4u f[8];
    auto fin = [&]() { r0 = add4(r0, c[1][0]); r1 = add4(r1, c[1][1]); ST16X(ro, oo, (v4u{pk2(r0[0], r0[1]), pk2(r0[2], r0[3]), pk2(r1[0], r1[1]), pk2(r1[2], r1[3])}), NTS & 1); oo = side(on); on += NH * 16384u; };
    WSTAMP(3);
    WAITV(10); RBAR();
    WSTAMP(4);
    const LAS char* px2 = px + 2 * XS; asm volatile("" : "+v"(px2));
#define RDX(q, k) f[(k) & 7] = *(const LAS v4u*)(((q) & 2 ? px2 : px) + ((q) & 1) * XS + (k) * 64)
#pragma unroll
    for (int k = 0; k < 8; ++k) RDX(0, k);
#pragma unroll 1
    for (int u0 = 0; u0 < 2 * NSEG; u0 += 4) {
        if (u0 == 4) WSTAMP(5);
        if (u0 == 64) WSTAMP(6);
#pragma unroll
        for (int q = 0; q < 4; ++q) {
#pragma unroll
            for (int k = 0; k < 16; ++k) {
                const v4u x = f[k & 7];
#pragma unroll
                for (int n = 0; n < 2; ++n) { const v4f z = k ? c[q & 1][n] : v4f{0, 0, 0, 0}; c[q & 1][n] = VT ? mf(x, W[k][n], z) : mf(W[k][n], x, z); }
                if (k < 8) RDX(q, k + 8);
                if (k == BK) { SCHED(); WAITV(10); BAR(); for (int e = 0; e < BK - 8; ++e) RDX(q + 1, e); }
                if (k >= BK) RDX(q + 1, k - 8);
                if (((k + 16 - BK) & 3) == 0) asm volatile("s_setprio %0" :: "n"(3 - (((k + 16 - BK) >> 2) & 3)));
                if (k == 2) { if (q & 1) { ps[0] = c[0][0]; ps[64] = c[0][1]; } else fin(); }
                if (k == 13) { if (q & 1) { r0 = pr[0]; r1 = pr[64]; } else ST4(rc, fl, 0, max((u0 + q - 2) >> 1, 0)); }
                if (!(EXP & 1) && !(k & 1) && kh == (((k >> 1) ^ ((BK + 1) >> 1)) & 1)) { if (k > BK) STX(u0 + q + 4, q, (k - BK) >> 2); else STX(u0 + q + 3, (q + 3) & 3, (k + 16 - BK) >> 2); }
                SCHED();
            }
        }
    }
    asm volatile("s_nop 7" : "+v"(c[1][0]), "+v"(c[1][1]));
    fin(); __builtin_amdgcn_s_setprio(0);
    WAITV(0); BAR();
    if (tid == 0) ST4(rc, fo, 0, NSEG);
    return far;
#undef STW
#undef STX
}

#define LDBX(RS, K, BASE, NT) LD16X(RS, l * 16 + ((K) & 3) * 1024, (BASE) + ((K) >> 2) * 4096, NT)
#define LDB(RS, K, BASE) LDBX(RS, K, BASE, 0)
DEV unsigned xattn(rsrc_t rq, rsrc_t rk, rsrc_t rv, rsrc_t rm, rsrc_t rc, unsigned* err, int seg, int h, int l, unsigned fl, unsigned far, unsigned ms, unsigned* dbg) {
#define XST(k) do {} while (0)
    XST(3);
    asm volatile("" : "+v"(l));
    const int fr = l & 15, g = l >> 4;
    const unsigned sb = side((unsigned)(seg * NH + h) * 16384u + far), mb = (unsigned)(seg * NH + h) * 4096u;
    const unsigned f0 = fw(min(l & 3, 2), h) + 12, f1 = F_PG + h * 512 + (l & 3) * 128;
    const unsigned old = __builtin_amdgcn_ballot_w64(fl <= (unsigned)seg);
    if (old & 15) wait_flags(rc, f0, seg + 1, err);
    XST(4);
    v4u q[4][2], k[4][2], km[4], v[8]; v2u vm[8];
    auto ldm = [&]() {
#pragma unroll
        for (int j = 0; j < 4; ++j) km[j] = LD16(rm, (g * 8 + (l & 7)) * 16 + j * 512, mb);
#pragma unroll
        for (int d = 0; d < 8; ++d) vm[d] = LD8(rm, 2048 + fr * 16 + (g & 1) * 8 + d * 256, mb); };
#pragma unroll
    for (int j = 0; j < 8; ++j) q[j >> 1][j & 1] = LDBX(rq, j, sb, 1);
    if (!(old & 240)) {
#pragma unroll
        for (int j = 0; j < 8; ++j) k[j >> 1][j & 1] = LDBX(rk, j, sb ^ KVS(ms >> 2, h), 1);
#pragma unroll
        for (int d = 0; d < 8; ++d) v[d] = LDBX(rv, d, sb ^ KVS(ms >> 4, h), 1);
        ldm();
    } else {
#pragma unroll
        for (int j = 0; j < 8; ++j) k[j >> 1][j & 1] = LDB(rk, j, sb ^ KVS(ms >> 2, h));
#pragma unroll
        for (int d = 0; d < 8; ++d) v[d] = LDB(rv, d, sb ^ KVS(ms >> 4, h));
    }
    v4f sx[3] = {}, o[2][8];
    float mx[2], zx[2];
    constexpr float C = SCALE * LOG2E, MR = MASKV / SCALE;
#pragma unroll
    for (int j = 0; j < 4; ++j) { sx[0] = mf(k[j][0], q[j][0], sx[0]); sx[1] = mf(k[j][0], q[j][1], sx[1]); sx[2] = mf(k[j][1], q[j][1], sx[2]); }
#pragma unroll
    for (int u = 0; u < 2; ++u) {
        float e[8];
#pragma unroll
        for (int r = 0; r < 4; ++r) { const bool vis = 4 * g + r <= fr; e[r] = (u || vis) ? sx[u][r] : MR; e[4 + r] = (u && vis) ? sx[2][r] : MR; }
        float m = e[0];
#pragma unroll
        for (int r = 1; r < 8; ++r) m = fmaxf(m, e[r]);
        mx[u] = m = red_g<true>(m); zx[u] = 0;
#pragma unroll
        for (int r = 0; r < 8; ++r) { e[r] = __builtin_amdgcn_exp2f((e[r] - m) * C); zx[u] += e[r]; }
        const v4u P = {pk2(e[0], e[1]), pk2(e[4], e[5]), pk2(e[2], e[3]), pk2(e[6], e[7])};
#pragma unroll
        for (int d = 0; d < 8; d += 2) {
            o[u][d] = mf(__builtin_shufflevector(v[d], v[d + 1], 0, 1, 4, 5), P, v4f{0, 0, 0, 0});
            o[u][d + 1] = mf(__builtin_shufflevector(v[d], v[d + 1], 2, 3, 6, 7), P, v4f{0, 0, 0, 0});
        }
    }
    asm volatile("" : "+v"(zx[0]), "+v"(zx[1]));
    SCHED(); XST(5);
    if (old & 240) { wait_flags<2>(rc, f1, seg + 1, err); ldm(); }
    XST(6);
    v4f sm[2] = {};
#pragma unroll
    for (int j = 0; j < 4; ++j) { sm[0] = mf(km[j], q[j][0], sm[0]); sm[1] = mf(km[j], q[j][1], sm[1]); }
    XST(28);
#pragma unroll
    for (int u = 0; u < 2; ++u) {
        const float m = red_g<true>(fmaxf(fmaxf(fmaxf(sm[u][0], sm[u][1]), fmaxf(sm[u][2], sm[u][3])), mx[u])), al = __builtin_amdgcn_exp2f((mx[u] - m) * C);
        float e[4];
#pragma unroll
        for (int r = 0; r < 4; ++r) e[r] = __builtin_amdgcn_exp2f((sm[u][r] - m) * C);
        const float inv = __builtin_amdgcn_rcpf(red_g<false>(al * zx[u] + 0.5f * (e[0] + e[1] + e[2] + e[3]))), f = al * inv, im = g < 2 ? inv : 0.0f;
        const unsigned pm0 = pk2(e[0] * im, e[1] * im), pm1 = pk2(e[2] * im, e[3] * im);
        const v4u Pa = {pm0, pm1, 0u, 0u}, Pb = {0u, 0u, pm0, pm1};
#pragma unroll
        for (int d = 0; d < 4; ++d) {
            const v4u vv = cat(vm[2 * d], vm[2 * d + 1]);
            const v4f o0 = mf(vv, Pa, o[u][2 * d] * f), o1 = mf(vv, Pb, o[u][2 * d + 1] * f);
            ST16(rq, l * 16 + d * 1024, sb + u * 4096, (v4u{pk2(o0[0], o0[1]), pk2(o0[2], o0[3]), pk2(o1[0], o1[1]), pk2(o1[2], o1[3])}));
        }
    }
    XST(29);
    FENCE(); const unsigned fn = LD4(rc, fl > seg + 4u ? OOB : (l & 4 ? f1 : f0), 0);
    WAITV(1); XST(30); RBAR(); XST(31);
    if (threadIdx.x == 0) ST4(rc, fw(3, (h & 24) + (seg & 3)) + 64 + (seg & 60), 0, 1u);
    FENCE();
    return max(fl, fn);
}

template <int N> DEV unsigned ror(unsigned v) { return __builtin_amdgcn_update_dpp(0u, v, 0x120 + N, 0xf, 0xf, false); }
DEV void prep_rows(const bf16_t* x, const bf16_t* gw, const bf16_t* wr, rsrc_t rg, int row, int l) {
    v4u xv[16], gv[2], rv[2];
#pragma unroll
    for (int i = 0; i < 2; ++i) { gv[i] = *(const v4u*)(gw + i * 512 + l * 8); rv[i] = *(const v4u*)(wr + i * 512 + l * 8); }
#pragma unroll
    for (int i = 0; i < 16; ++i) xv[i] = *(const v4u*)(x + (size_t)row * DM + i * 512 + l * 8);
    float ga = 0, gb = 0;
#pragma unroll
    for (int k = 0; k < 8; ++k) {
        float a = 0, b = 0;
#pragma unroll
        for (int i = 0; i < 8; ++i) {
            const unsigned u = xv[2 * k + (i >> 2)][i & 3], g = gv[i >> 2][i & 3], r = rv[i >> 2][i & 3];
            a += rbf(lo16(u) * lo16(g)) + rbf(hi16(u) * hi16(g));
            b += lo16(u) * lo16(r) + hi16(u) * hi16(r);
        }
        a = half_add(a, b);
        a += __uint_as_float(ror<8>(__float_as_uint(a))); a += __uint_as_float(ror<4>(__float_as_uint(a)));
        a += __uint_as_float(ror<2>(__float_as_uint(a))); a += __uint_as_float(ror<1>(__float_as_uint(a)));
        const v2u s = sw16(__float_as_uint(a), __float_as_uint(a)); a = __uint_as_float(s[0]) + __uint_as_float(s[1]);
        if ((l & 31) == k) ga = a;
    }
    gb = rbf(ga);
    const unsigned u = __float_as_uint(l < 32 ? rbf(sigmoidf_(gb)) : gb) | 1u;
    const v2u e = sw32(u, u);
    ST16(rg, l < 8 && !(l & 1) ? F_GR + (row + l) * 8 : OOB, 0, (v4u{e[0], e[1], ror<15>(e[0]), ror<15>(e[1])}));
}
template <int N> DEV unsigned top_keys(unsigned* key, int l) {
    unsigned res = 0;
#pragma unroll 1
    for (int c = 0; c < TOPC; ++c) {
        unsigned m = key[0];
#pragma unroll
        for (int i = 1; i < N; ++i) m = max(m, key[i]);
        m = max(m, ror<8>(m)); m = max(m, ror<4>(m)); m = max(m, ror<2>(m)); m = max(m, ror<1>(m));
        v2u s = sw16(m, m); m = max(s[0], s[1]); s = sw32(m, m); m = max(s[0], s[1]);
#pragma unroll
        for (int i = 0; i < N; ++i) key[i] = key[i] == m ? 0u : key[i];
        if (l == c) res = m;
    }
    return res;
}
DEV int top8(LAS unsigned* cand, rsrc_t rc, unsigned* err, int w, int l) {
    v4u t[2];
    for (unsigned tries = 0;; ++tries) {
        FENCE();
        t[0] = LD16(rc, F_GR + w * 2048 + l * 16, 0); t[1] = LD16(rc, F_GR + w * 2048 + 1024 + l * 16, 0);
        const v4u v = t[0] & t[1];
        if (all_ge(v[0] & v[1] & v[2] & v[3] & 1u, 1u)) break;
        __builtin_amdgcn_s_sleep(8);
        if (spin_fail(tries, err)) break;
    }
    unsigned key[4];
#pragma unroll
    for (int i = 0; i < 4; ++i) { const unsigned r = t[i >> 1][(i & 1) * 2 + 1] - 1u; key[i] = ((int)r < 0 ? 0xffff0000u - r : r + 0x80000000u) + (0xffffu - (w * 256 + (i >> 1) * 128 + l * 2 + (i & 1))); }
    key[0] = top_keys<4>(key, l);
    if (l < TOPC) cand[w * TOPC + l] = key[0];
    BAR();
    key[0] = cand[l];
    return 0xffff - (int)(top_keys<1>(key, l) & 0xffffu);
}
static constexpr int NMOE = FF / 128, XR = 2080;
DEV void moe(LAS char* lds, v4u rx, int sel, v4u rg, v4u ru, v4u rd, rsrc_t rp, int cu) {
    const int tid = threadIdx.x, w = __builtin_amdgcn_readfirstlane(tid >> 6), l = tid & 63, kh = w >> 2, cg = w & 3, fr = l & 15, fq = l >> 4;
    const unsigned lb = (unsigned)(size_t)lds, v1 = wt_src(tid, FF * 2), v2 = wt_src(tid, DM * 2);
    const unsigned xo = __builtin_amdgcn_readlane(sel, w) * 2048;
    DMA16(rx, l * 16, xo, lb + L_RED + w * XR); DMA16(rx, l * 16, xo + 1024, lb + L_RED + w * XR + 1024);
    auto issue = [&](int s) {
        const unsigned d = lb + w * 1024 + (s & 3) * XS;
        if (s < 16) { const unsigned o = s * (64 * FF * 2) + cu * 256; WT_ISSUE(rg, ru, v1, o, o, FF * 2, d); }
        else { const unsigned o = s < 24 ? (cu * 128 + (s & 1) * 64) * (DM * 2) + (((s >> 1) + cu) & 3) * 512 : OOB; WT_ISSUE(rd, rd, v2, o, o + 256, DM * 2, d); } };
    issue(0); issue(1); issue(2); issue(3);
    const int p0 = wt_rd(w, l, 0), p1 = wt_rd(w, l, 1);
    v4f c[2] = {};
    auto stage = [&](int s, const LAS char* pa) {
        WAITV(12); RBAR();
        const LAS char* p = lds + (s & 3) * XS;
        const v4u a0 = *(const LAS v4u*)pa, a1 = *(const LAS v4u*)(pa + 64), b0 = wt_frag(p + p0), b1 = wt_frag(p + p1), b2 = wt_frag(p + p0 + 8192), b3 = wt_frag(p + p1 + 8192);
        BAR();
        issue(s + 4);
        c[0] = mf(b2, a1, mf(b0, a0, c[0])); c[1] = mf(b3, a1, mf(b1, a0, c[1])); };
#pragma unroll 1
    for (int s = 0; s < 16; ++s) stage(s, lds + L_RED + (l & 7) * XR + fq * 16 + s * 128);
    LAS v4f* pu = (LAS v4f*)(lds + L_RED + cg * 2048 + l * 16);
    if (kh) { pu[0] = c[0]; pu[64] = c[1]; }
    BAR();
    if (!kh && fr < 8) {
#pragma unroll
        for (int n = 0; n < 2; ++n) {
            const v4f u = pu[64 * n]; float hv[4];
#pragma unroll
            for (int e = 0; e < 4; ++e) { const float g = rbf(c[n][e]); hv[e] = rbf(g * sigmoidf_(g)) * rbf(u[e]); }
            *(LAS v2u*)(lds + L_RED + 8192 + fr * 288 + cg * 64 + n * 32 + fq * 8) = v2u{pk2(hv[0], hv[1]), pk2(hv[2], hv[3])};
        }
    }
    WAITL(0);
#pragma unroll 1
    for (int s = 16; s < 24; s += 2) {
        c[0] = v4f{0, 0, 0, 0}; c[1] = c[0];
        const LAS char* pa = lds + L_RED + 8192 + (l & 7) * 288 + fq * 16;
        stage(s, pa); stage(s + 1, pa + 128);
        ST16(rp, cu * 32768 + (l & 7) * 4096 + ((((s >> 1) + cu) & 3) * 256 + kh * 128 + cg * 32 + (fr >> 3) * 16 + fq * 4) * 4, 0, __builtin_bit_cast(v4u, fr < 8 ? c[0] : c[1]));
    }
    WAITV(0); RBAR();
}

static constexpr unsigned H2_OFF = 0, H1_OFF = 512 * 1024, H3_OFF = 768 * 1024, PR_OFF = 896 * 1024;
static constexpr size_t SLOT = 2u << 20;
static constexpr int NSLOT = 4;
struct Args {
    const bf16_t* x; const bf16_t* mem0; const bf16_t* Wq; const bf16_t* Wk; const bf16_t* Wv; const bf16_t* Wo;
    const bf16_t* w_r; const bf16_t* wg; const bf16_t* wu; const bf16_t* wd; const bf16_t* gw;
    char* qkv; char* mb; int* idx; float* part; float* upd; char* wt; bf16_t* out;
    char* ring; unsigned* ctl; unsigned* dbg;
    u64 segs[7];
    float per; unsigned per0, d1, dv, d2, dp, slack, thr, spare, wq, wx, pf, ms, ws, wi, gap;
};
typedef const __attribute__((address_space(4))) Args* KArgs;
DEV KArgs args() { KArgs p = (KArgs)__builtin_amdgcn_kernarg_segment_ptr(); asm volatile("" : "+s"(p)); return p; }
static constexpr unsigned QSZ = S_TOK * HD * 4;
typedef unsigned v16u __attribute__((ext_vector_type(16)));
typedef unsigned v32u __attribute__((ext_vector_type(32)));

DEV unsigned nearo(unsigned off) { return (off >> 13) * 16384u + (off & 8191u); }
DEV unsigned rt32() { u64 t; asm volatile("s_memrealtime %0\n\ts_waitcnt lgkmcnt(0)" : "=s"(t) :: "memory"); return (unsigned)t; }
DEV unsigned cyc32() { u64 t; asm volatile("s_memtime %0\n\ts_waitcnt lgkmcnt(0)" : "=s"(t) :: "memory"); return (unsigned)t; }
template <int R1, int R2, int R3> DEV v8u rd_w(const LAS char* p) {
    return __builtin_shufflevector(cat(trd(p), trd(p + R1)), cat(trd(p + R2), trd(p + R3)), 0, 1, 2, 3, 4, 5, 6, 7);
}
DEV v16u cat8(v8u a, v8u b) { return __builtin_shufflevector(a, b, 0, 1, 2, 3, 4, 5, 6, 7, 8, 9, 10, 11, 12, 13, 14, 15); }
DEV v32u cat16(v16u a, v16u b) { return __builtin_shufflevector(a, b, 0, 1, 2, 3, 4, 5, 6, 7, 8, 9, 10, 11, 12, 13, 14, 15, 16, 17, 18, 19, 20, 21, 22, 23, 24, 25, 26, 27, 28, 29, 30, 31); }
DEV v8u cat4(v4u a, v4u b) { return __builtin_shufflevector(a, b, 0, 1, 2, 3, 4, 5, 6, 7); }

static constexpr int L_PQK = 0, L_PV = 8704, L_P = 13824, L_VT = 15360, L_X = 16384, L_K = L_X + 9216, L_MISC = 28672, L_PF = 29696, L_STG = 32768, L_TOT = 163840;
static constexpr int X_FQ = 272, X_BLK = 1152;
#define ASM_A \
    "s_setprio 1\n" \
    "s_mov_b32 s76, 0\n" \
    "s_mov_b32 s99, 0x20000\n" \
    "s_mov_b32 s84, 0\n" \
    "s_mov_b32 s81, 0\n" \
    "s_mov_b32 s86, 0\n" \
    "s_mov_b32 s87, 0\n" \
    "s_mov_b32 s88, 0\n" \
    "s_mov_b32 s89, 0\n" \
    "s_mov_b32 s90, 0\n" \
    "s_mov_b32 s91, 0\n" \
    "s_mov_b32 s92, 0\n" \
    "s_mov_b32 s93, 0\n" \
    "s_mov_b32 s94, 0\n" \
    "s_mov_b32 s95, 0\n" \
    "s_mov_b32 s96, 0\n" \
    "s_mov_b32 s97, 0\n" \
    "s_mov_b32 s98, 0\n" \
    "ds_read_b32 v244, v237\n" \
    "s_waitcnt lgkmcnt(0)\n" \
    "v_readfirstlane_b32 s94, v244\n" \
    "s_mul_i32 s79, s94, s70\n" \
    "s_add_u32 s79, s79, s81\n" \
    "s_mov_b32 s77, s79\n" \
    "s_mul_i32 s96, s65, s71\n" \
    "s_lshr_b32 s96, s96, 4\n" \
    "v_mov_b64 v[208:209], 0\n" \
    "v_mov_b64 v[210:211], 0\n" \
    "v_mov_b64 v[212:213], 0\n" \
    "v_mov_b64 v[214:215], 0\n" \
    "v_mov_b64 v[216:217], 0\n" \
    "v_mov_b64 v[218:219], 0\n" \
    "v_mov_b64 v[220:221], 0\n" \
    "v_mov_b64 v[222:223], 0\n" \
    "v_mov_b64 v[224:225], 0\n" \
    "v_mov_b64 v[226:227], 0\n" \
    "v_mov_b64 v[228:229], 0\n" \
    "v_mov_b64 v[230:231], 0\n" \
    "v_mov_b32 v241, 1\n" \
    "s_branch L%=_Acomp\n" \
    "L%=_Aloop:\n" \
    "s_and_b32 s95, s94, 1\n" \
    "s_lshl_b32 s95, s95, 17\n" \
    "s_add_u32 s95, s95, s60\n" \
    "s_add_u32 s85, s95, 0x1000\n" \
    "s_branch L%=_Au2b\n" \
    "L%=_Au2a:\n" \
    "s_sleep 4\n" \
    "L%=_Au2b:\n" \
    "s_memrealtime vcc\n" \
    "s_add_u32 s80, s77, s64\n" \
    "s_add_u32 s80, s80, -3\n" \
    "s_waitcnt lgkmcnt(0)\n" \
    "s_sub_i32 s79, s80, vcc_lo\n" \
    "s_sub_u32 s79, s79, 40\n" \
    "s_cmp_lt_u32 s79, 0x400\n" \
    "s_cbranch_scc1 L%=_Au2a\n" \
    "s_memtime s[38:39]\n" \
    "s_memrealtime vcc\n" \
    "s_nop 4\n" \
    "s_memrealtime s[32:33]\n" \
    "s_nop 4\n" \
    "s_memrealtime s[34:35]\n" \
    "s_nop 4\n" \
    "s_memrealtime s[36:37]\n" \
    "s_add_u32 s80, s77, s64\n" \
    "s_lshl_b32 s80, s80, 2\n" \
    "s_waitcnt lgkmcnt(0)\n" \
    "s_sub_u32 s80, s80, vcc_lo\n" \
    "s_sub_u32 s80, s80, s32\n" \
    "s_sub_u32 s80, s80, s34\n" \
    "s_sub_u32 s80, s80, s36\n" \
    "s_mul_i32 s80, s80, s71\n" \
    "s_ashr_i32 s80, s80, 6\n" \
    "s_add_u32 s78, s80, s38\n" \
    "L%=_Au3a:\n" \
    "s_memtime s[38:39]\n" \
    "s_mul_i32 s80, -3, 23\n" \
    "s_add_u32 s80, s80, s78\n" \
    "s_waitcnt lgkmcnt(0)\n" \
    "s_sub_i32 s79, s80, s38\n" \
    "s_max_i32 s79, s79, 0\n" \
    "s_cmp_gt_u32 s79, 320\n" \
    "s_cbranch_scc1 L%=_Au3s\n" \
    ".p2align 4\n" \
    "L%=_Au3d:\n" \
    "s_sub_u32 s79, s79, 28\n" \
    "s_cbranch_scc0 L%=_Au3d\n" \
    "L%=_Au3e:\n" \
    "L%=_Apoll:\n" \
    "ds_read_b32 v244, v237 offset:4\n" \
    "s_waitcnt lgkmcnt(0)\n" \
    "v_readfirstlane_b32 s79, v244\n" \
    "s_cmp_lt_u32 s79, s76\n" \
    "s_cbranch_scc1 L%=_Askip\n" \
    "L%=_Ago:\n" \
    "buffer_load_dwordx4 v[192:195], v233, s[40:43], s95 offen sc1\n" \
    "s_nop 5\n" \
    "buffer_load_dwordx4 v[196:199], v233, s[40:43], s85 offen sc1\n" \
    "s_nop 5\n" \
    "buffer_load_dwordx4 v[200:203], v239, s[40:43], s95 offen sc1\n" \
    "s_nop 5\n" \
    "buffer_load_dwordx4 v[204:207], v239, s[40:43], s85 offen sc1\n" \
    "L%=_Acomp:\n" \
    "s_mov_b32 m0, s72\n" \
    "s_waitcnt vmcnt(3)\n" \
    "v_smfmac_f32_16x16x64_bf16 v[208:211], v[192:195], v[0:7], v232\n" \
    "v_smfmac_f32_16x16x64_bf16 v[212:215], v[192:195], v[8:15], v232\n" \
    "v_smfmac_f32_16x16x64_bf16 v[216:219], v[192:195], v[64:71], v232\n" \
    "v_smfmac_f32_16x16x64_bf16 v[220:223], v[192:195], v[72:79], v232\n" \
    "v_smfmac_f32_16x16x64_bf16 v[224:227], v[192:195], v[128:135], v232\n" \
    "s_waitcnt vmcnt(2)\n" \
    "v_smfmac_f32_16x16x64_bf16 v[208:211], v[196:199], v[16:23], v232\n" \
    "v_smfmac_f32_16x16x64_bf16 v[212:215], v[196:199], v[24:31], v232\n" \
    "v_smfmac_f32_16x16x64_bf16 v[216:219], v[196:199], v[80:87], v232\n" \
    "v_smfmac_f32_16x16x64_bf16 v[220:223], v[196:199], v[88:95], v232\n" \
    "v_smfmac_f32_16x16x64_bf16 v[228:231], v[192:195], v[136:143], v232\n" \
    "s_waitcnt vmcnt(1)\n" \
    "v_smfmac_f32_16x16x64_bf16 v[208:211], v[200:203], v[32:39], v232\n" \
    "v_smfmac_f32_16x16x64_bf16 v[212:215], v[200:203], v[40:47], v232\n" \
    "v_smfmac_f32_16x16x64_bf16 v[216:219], v[200:203], v[96:103], v232\n" \
    "v_smfmac_f32_16x16x64_bf16 v[220:223], v[200:203], v[104:111], v232\n" \
    "v_smfmac_f32_16x16x64_bf16 v[224:227], v[196:199], v[144:151], v232\n" \
    "s_waitcnt vmcnt(0)\n" \
    "v_smfmac_f32_16x16x64_bf16 v[208:211], v[204:207], v[48:55], v232\n" \
    "v_smfmac_f32_16x16x64_bf16 v[212:215], v[204:207], v[56:63], v232\n" \
    "v_smfmac_f32_16x16x64_bf16 v[216:219], v[204:207], v[112:119], v232\n" \
    "v_add_f32 v208, v208, v210\n" \
    "v_smfmac_f32_16x16x64_bf16 v[220:223], v[204:207], v[120:127], v232\n" \
    "v_add_f32 v209, v209, v211\n" \
    "v_cmp_u_f32 vcc, v208, v209\n" \
    "v_add_f32 v212, v212, v214\n" \
    "v_add_f32 v213, v213, v215\n" \
    "v_add_f32 v216, v216, v218\n" \
    "v_add_f32 v217, v217, v219\n" \
    "s_nop 1\n" \
    "v_add_f32 v220, v220, v222\n" \
    "v_add_f32 v221, v221, v223\n" \
    "v_smfmac_f32_16x16x64_bf16 v[224:227], v[200:203], v[160:167], v232\n" \
    "s_cbranch_vccnz L%=_Aslow\n" \
    "L%=_Aok:\n" \
    "ds_write_addtid_b32 v208 offset:0\n" \
    "ds_write_addtid_b32 v209 offset:544\n" \
    "v_smfmac_f32_16x16x64_bf16 v[228:231], v[196:199], v[152:159], v232\n" \
    "ds_write_addtid_b32 v216 offset:256\n" \
    "ds_write_addtid_b32 v217 offset:800\n" \
    "v_smfmac_f32_16x16x64_bf16 v[224:227], v[204:207], v[176:183], v232\n" \
    "ds_write_addtid_b32 v212 offset:1088\n" \
    "ds_write_addtid_b32 v213 offset:1632\n" \
    "v_smfmac_f32_16x16x64_bf16 v[228:231], v[200:203], v[168:175], v232\n" \
    "ds_write_addtid_b32 v220 offset:1344\n" \
    "ds_write_addtid_b32 v221 offset:1888\n" \
    "ds_write_b32 v236, v241 offset:32\n" \
    "v_smfmac_f32_16x16x64_bf16 v[228:231], v[204:207], v[184:191], v232\n" \
    "s_barrier\n" \
    "s_mov_b32 m0, s73\n" \
    "v_add_f32 v224, v224, v226\n" \
    "v_add_f32 v225, v225, v227\n" \
    "ds_write_addtid_b32 v224 offset:8704\n" \
    "ds_write_addtid_b32 v225 offset:8992\n" \
    "s_nop 1\n" \
    "v_add_f32 v228, v228, v230\n" \
    "v_add_f32 v229, v229, v231\n" \
    "ds_write_addtid_b32 v228 offset:9344\n" \
    "ds_write_addtid_b32 v229 offset:9632\n" \
    "ds_write_b32 v236, v241 offset:16\n" \
    "v_mov_b64 v[208:209], 0\n" \
    "v_mov_b64 v[210:211], 0\n" \
    "v_mov_b64 v[212:213], 0\n" \
    "v_mov_b64 v[214:215], 0\n" \
    "v_mov_b64 v[216:217], 0\n" \
    "v_mov_b64 v[218:219], 0\n" \
    "v_mov_b64 v[220:221], 0\n" \
    "v_mov_b64 v[222:223], 0\n" \
    "v_mov_b64 v[224:225], 0\n" \
    "v_mov_b64 v[226:227], 0\n" \
    "v_mov_b64 v[228:229], 0\n" \
    "v_mov_b64 v[230:231], 0\n" \
    "v_add_u32 v241, 1, v241\n" \
    "ds_read_b32 v244, v237\n" \
    "s_waitcnt lgkmcnt(0)\n" \
    "v_readfirstlane_b32 s94, v244\n" \
    "s_mul_i32 s79, s94, s70\n" \
    "s_add_u32 s79, s79, s81\n" \
    "s_mov_b32 s77, s79\n" \
    "s_barrier\n" \
    "ds_read_b64 v[244:245], v237 offset:64\n" \
    "s_mov_b32 s79, s60\n" \
    "s_mov_b32 s60, s61\n" \
    "s_mov_b32 s61, s62\n" \
    "s_mov_b32 s62, s63\n" \
    "s_mov_b32 s63, s79\n" \
    "s_add_u32 s76, s76, 1\n" \
    "s_add_u32 s77, s77, s65\n" \
    "s_add_u32 s84, s84, 0x20000\n" \
    "s_add_u32 s81, s81, s65\n" \
    "s_waitcnt lgkmcnt(0)\n" \
    "v_readfirstlane_b32 s65, v244\n" \
    "v_readfirstlane_b32 s71, v245\n" \
    "s_mul_i32 s96, s65, s71\n" \
    "s_lshr_b32 s96, s96, 4\n" \
    "s_cmp_lt_u32 s76, 64\n" \
    "s_cbranch_scc1 L%=_Aloop\n" \
    "s_branch L%=_end\n" \
    "L%=_Aslow:\n" \
    "s_nop 7\n" \
    "s_nop 0\n" \
    "v_max_u32 v244, v192, v193\n" \
    "v_max3_u32 v244, v244, v194, v195\n" \
    "v_max3_u32 v244, v244, v196, v197\n" \
    "v_max3_u32 v244, v244, v198, v199\n" \
    "v_max3_u32 v244, v244, v200, v201\n" \
    "v_max3_u32 v244, v244, v202, v203\n" \
    "v_max3_u32 v244, v244, v204, v205\n" \
    "v_max3_u32 v244, v244, v206, v207\n" \
    "v_cmp_eq_u32 vcc, -1, v244\n" \
    "s_cbranch_vccz L%=_Aok\n" \
    "s_cmp_eq_u32 s99, 0\n" \
    "s_cbranch_scc1 L%=_Aok\n" \
    "s_sub_u32 s99, s99, 1\n" \
    "s_cmp_lg_u32 s99, 0\n" \
    "s_cbranch_scc1 L%=_Au5\n" \
    "v_mov_b32 v244, 1\n" \
    "buffer_store_dword v244, off, s[44:47], 0 sc1\n" \
    "L%=_Au5:\n" \
    "v_mov_b64 v[208:209], 0\n" \
    "v_mov_b64 v[210:211], 0\n" \
    "v_mov_b64 v[212:213], 0\n" \
    "v_mov_b64 v[214:215], 0\n" \
    "v_mov_b64 v[216:217], 0\n" \
    "v_mov_b64 v[218:219], 0\n" \
    "v_mov_b64 v[220:221], 0\n" \
    "v_mov_b64 v[222:223], 0\n" \
    "v_mov_b64 v[224:225], 0\n" \
    "v_mov_b64 v[226:227], 0\n" \
    "v_mov_b64 v[228:229], 0\n" \
    "v_mov_b64 v[230:231], 0\n" \
    "L%=_Ash:\n" \
    "s_memrealtime vcc\n" \
    "s_waitcnt lgkmcnt(0)\n" \
    "s_sub_u32 s80, vcc_lo, s64\n" \
    "s_sub_u32 s80, s80, 4\n" \
    "s_mul_i32 s32, s70, s71\n" \
    "s_lshr_b32 s32, s32, 4\n" \
    "L%=_Au6g:\n" \
    "s_add_u32 s77, s77, s70\n" \
    "s_add_u32 s78, s78, s32\n" \
    "s_add_u32 s94, s94, 1\n" \
    "s_sub_u32 s79, s80, s77\n" \
    "s_cmp_lt_u32 s79, 0x1000000\n" \
    "s_cbranch_scc1 L%=_Au6g\n" \
    "v_mov_b32 v244, s94\n" \
    "ds_max_u32 v237, v244\n" \
    "s_cmp_gt_i32 s79, -20\n" \
    "s_cbranch_scc1 L%=_Au6c\n" \
    "s_branch L%=_Au7b\n" \
    "L%=_Au7a:\n" \
    "s_sleep 4\n" \
    "L%=_Au7b:\n" \
    "s_memrealtime vcc\n" \
    "s_add_u32 s80, s77, s64\n" \
    "s_add_u32 s80, s80, -3\n" \
    "s_waitcnt lgkmcnt(0)\n" \
    "s_sub_i32 s79, s80, vcc_lo\n" \
    "s_sub_u32 s79, s79, 40\n" \
    "s_cmp_lt_u32 s79, 0x400\n" \
    "s_cbranch_scc1 L%=_Au7a\n" \
    "s_memtime s[38:39]\n" \
    "s_memrealtime vcc\n" \
    "s_nop 4\n" \
    "s_memrealtime s[32:33]\n" \
    "s_nop 4\n" \
    "s_memrealtime s[34:35]\n" \
    "s_nop 4\n" \
    "s_memrealtime s[36:37]\n" \
    "s_add_u32 s80, s77, s64\n" \
    "s_lshl_b32 s80, s80, 2\n" \
    "s_waitcnt lgkmcnt(0)\n" \
    "s_sub_u32 s80, s80, vcc_lo\n" \
    "s_sub_u32 s80, s80, s32\n" \
    "s_sub_u32 s80, s80, s34\n" \
    "s_sub_u32 s80, s80, s36\n" \
    "s_mul_i32 s80, s80, s71\n" \
    "s_ashr_i32 s80, s80, 6\n" \
    "s_add_u32 s78, s80, s38\n" \
    "L%=_Au6c:\n" \
    "s_and_b32 s95, s94, 1\n" \
    "s_lshl_b32 s95, s95, 17\n" \
    "s_add_u32 s95, s95, s60\n" \
    "s_add_u32 s85, s95, 0x1000\n" \
    "L%=_Au8a:\n" \
    "s_memtime s[38:39]\n" \
    "s_mul_i32 s80, -3, 23\n" \
    "s_add_u32 s80, s80, s78\n" \
    "s_waitcnt lgkmcnt(0)\n" \
    "s_sub_i32 s79, s80, s38\n" \
    "s_max_i32 s79, s79, 0\n" \
    "s_cmp_gt_u32 s79, 320\n" \
    "s_cbranch_scc1 L%=_Au8s\n" \
    ".p2align 4\n" \
    "L%=_Au8d:\n" \
    "s_sub_u32 s79, s79, 28\n" \
    "s_cbranch_scc0 L%=_Au8d\n" \
    "L%=_Au8e:\n" \
    "s_branch L%=_Apoll\n" \
    "L%=_Askip:\n" \
    "s_cmp_lt_u32 s99, 2\n" \
    "s_cbranch_scc1 L%=_Ago\n" \
    "s_sub_u32 s99, s99, 1\n" \
    "s_branch L%=_Ash\n" \
    "L%=_Au3s:\n" \
    "s_cmp_gt_u32 s79, 0x8000\n" \
    "s_cbranch_scc1 L%=_Au3e\n" \
    "s_sleep 2\n" \
    "s_branch L%=_Au3a\n" \
    "L%=_Au8s:\n" \
    "s_cmp_gt_u32 s79, 0x8000\n" \
    "s_cbranch_scc1 L%=_Au8e\n" \
    "s_sleep 2\n" \
    "s_branch L%=_Au8a\n" \
    "L%=_end:\n"
#define ASM_B \
    "s_and_b32 s79, s78, 3\n" \
    "s_cmp_eq_u32 s79, 1\n" \
    "s_cbranch_scc1 L%=_S\n" \
    "s_cmp_eq_u32 s79, 2\n" \
    "s_cbranch_scc1 L%=_R\n" \
    "s_cmp_eq_u32 s79, 3\n" \
    "s_cbranch_scc1 L%=_P\n" \
    "L%=_T:\n" \
    "s_mov_b32 s76, 0\n" \
    "s_mov_b32 s99, 0x20000\n" \
    "s_mov_b32 s84, 0\n" \
    "s_mov_b32 s81, 0\n" \
    "s_mov_b32 s86, 0\n" \
    "s_mov_b32 s87, 0\n" \
    "s_mov_b32 s88, 0\n" \
    "s_mov_b32 s89, 0\n" \
    "s_mov_b32 s90, 0\n" \
    "s_mov_b32 s91, 0\n" \
    "s_mov_b32 s92, 0\n" \
    "s_mov_b32 s93, 0\n" \
    "s_mov_b32 s94, 0\n" \
    "s_mov_b32 s95, 0\n" \
    "s_mov_b32 s96, 0\n" \
    "s_mov_b32 s97, 0\n" \
    "s_mov_b32 s98, 0\n" \
    "ds_read_b32 v216, v229\n" \
    "s_waitcnt lgkmcnt(0)\n" \
    "v_readfirstlane_b32 s94, v216\n" \
    "s_mul_i32 s79, s94, s70\n" \
    "s_add_u32 s79, s79, s81\n" \
    "s_mov_b32 s77, s79\n" \
    "s_mul_i32 s96, s65, s71\n" \
    "s_lshr_b32 s96, s96, 4\n" \
    "v_mov_b32 v96, 0x3f803f80\n" \
    "v_mov_b32 v97, 0x3f803f80\n" \
    "v_mov_b32 v98, 0x3f803f80\n" \
    "v_mov_b32 v99, 0x3f803f80\n" \
    "v_mov_b32 v100, 0x3f803f80\n" \
    "v_mov_b32 v101, 0x3f803f80\n" \
    "v_mov_b64 v[102:103], 0\n" \
    "v_mov_b64 v[64:65], 0\n" \
    "v_mov_b64 v[66:67], 0\n" \
    "v_mov_b64 v[68:69], 0\n" \
    "v_mov_b64 v[70:71], 0\n" \
    "v_mov_b64 v[72:73], 0\n" \
    "v_mov_b64 v[74:75], 0\n" \
    "v_mov_b64 v[76:77], 0\n" \
    "v_mov_b64 v[78:79], 0\n" \
    "v_mov_b64 v[80:81], 0\n" \
    "v_mov_b64 v[82:83], 0\n" \
    "v_mov_b64 v[84:85], 0\n" \
    "v_mov_b64 v[86:87], 0\n" \
    "v_mov_b64 v[88:89], 0\n" \
    "v_mov_b64 v[90:91], 0\n" \
    "v_mov_b64 v[92:93], 0\n" \
    "v_mov_b64 v[94:95], 0\n" \
    "v_mov_b64 v[192:193], 0\n" \
    "v_mov_b64 v[194:195], 0\n" \
    "L%=_Tloop:\n" \
    "ds_read_b64_tr_b16 v[104:105], v228 offset:16384\n" \
    "ds_read_b64_tr_b16 v[106:107], v228 offset:16416\n" \
    "ds_read_b64_tr_b16 v[108:109], v228 offset:18688\n" \
    "ds_read_b64_tr_b16 v[110:111], v228 offset:18720\n" \
    "ds_read_b64_tr_b16 v[112:113], v228 offset:20992\n" \
    "ds_read_b64_tr_b16 v[114:115], v228 offset:21024\n" \
    "ds_read_b64_tr_b16 v[116:117], v228 offset:23296\n" \
    "ds_read_b64_tr_b16 v[118:119], v228 offset:23328\n" \
    "ds_read_b64_tr_b16 v[120:121], v228 offset:16656\n" \
    "ds_read_b64_tr_b16 v[122:123], v228 offset:16688\n" \
    "ds_read_b64_tr_b16 v[124:125], v228 offset:18960\n" \
    "ds_read_b64_tr_b16 v[126:127], v228 offset:18992\n" \
    "ds_read_b64_tr_b16 v[128:129], v228 offset:21264\n" \
    "ds_read_b64_tr_b16 v[130:131], v228 offset:21296\n" \
    "ds_read_b64_tr_b16 v[132:133], v228 offset:23568\n" \
    "ds_read_b64_tr_b16 v[134:135], v228 offset:23600\n" \
    "s_waitcnt lgkmcnt(14)\n" \
    "v_mfma_f32_16x16x32_bf16 v[136:139], v[104:107], v[0:3], 0\n" \
    "v_mfma_f32_16x16x32_bf16 v[144:147], v[104:107], v[8:11], 0\n" \
    "v_mfma_f32_16x16x32_bf16 v[152:155], v[104:107], v[16:19], 0\n" \
    "v_mfma_f32_16x16x32_bf16 v[160:163], v[104:107], v[24:27], 0\n" \
    "s_waitcnt lgkmcnt(12)\n" \
    "v_mfma_f32_16x16x32_bf16 v[136:139], v[108:111], v[4:7], v[136:139]\n" \
    "v_mfma_f32_16x16x32_bf16 v[144:147], v[108:111], v[12:15], v[144:147]\n" \
    "v_mfma_f32_16x16x32_bf16 v[152:155], v[108:111], v[20:23], v[152:155]\n" \
    "v_mfma_f32_16x16x32_bf16 v[160:163], v[108:111], v[28:31], v[160:163]\n" \
    "s_waitcnt lgkmcnt(10)\n" \
    "v_mfma_f32_16x16x32_bf16 v[136:139], v[112:115], v[32:35], v[136:139]\n" \
    "v_mfma_f32_16x16x32_bf16 v[144:147], v[112:115], v[40:43], v[144:147]\n" \
    "v_mfma_f32_16x16x32_bf16 v[152:155], v[112:115], v[48:51], v[152:155]\n" \
    "v_mfma_f32_16x16x32_bf16 v[160:163], v[112:115], v[56:59], v[160:163]\n" \
    "s_waitcnt lgkmcnt(8)\n" \
    "v_mfma_f32_16x16x32_bf16 v[136:139], v[116:119], v[36:39], v[136:139]\n" \
    "v_mfma_f32_16x16x32_bf16 v[144:147], v[116:119], v[44:47], v[144:147]\n" \
    "v_mfma_f32_16x16x32_bf16 v[152:155], v[116:119], v[52:55], v[152:155]\n" \
    "v_mfma_f32_16x16x32_bf16 v[160:163], v[116:119], v[60:63], v[160:163]\n" \
    "s_waitcnt lgkmcnt(6)\n" \
    "v_mfma_f32_16x16x32_bf16 v[140:143], v[120:123], v[0:3], 0\n" \
    "v_mfma_f32_16x16x32_bf16 v[148:151], v[120:123], v[8:11], 0\n" \
    "v_mfma_f32_16x16x32_bf16 v[156:159], v[120:123], v[16:19], 0\n" \
    "v_mfma_f32_16x16x32_bf16 v[164:167], v[120:123], v[24:27], 0\n" \
    "s_waitcnt lgkmcnt(4)\n" \
    "v_mfma_f32_16x16x32_bf16 v[140:143], v[124:127], v[4:7], v[140:143]\n" \
    "v_mfma_f32_16x16x32_bf16 v[148:151], v[124:127], v[12:15], v[148:151]\n" \
    "v_mfma_f32_16x16x32_bf16 v[156:159], v[124:127], v[20:23], v[156:159]\n" \
    "v_mfma_f32_16x16x32_bf16 v[164:167], v[124:127], v[28:31], v[164:167]\n" \
    "s_waitcnt lgkmcnt(2)\n" \
    "v_mfma_f32_16x16x32_bf16 v[140:143], v[128:131], v[32:35], v[140:143]\n" \
    "v_mfma_f32_16x16x32_bf16 v[148:151], v[128:131], v[40:43], v[148:151]\n" \
    "v_mfma_f32_16x16x32_bf16 v[156:159], v[128:131], v[48:51], v[156:159]\n" \
    "v_mfma_f32_16x16x32_bf16 v[164:167], v[128:131], v[56:59], v[164:167]\n" \
    "s_waitcnt lgkmcnt(0)\n" \
    "v_mfma_f32_16x16x32_bf16 v[140:143], v[132:135], v[36:39], v[140:143]\n" \
    "v_mfma_f32_16x16x32_bf16 v[148:151], v[132:135], v[44:47], v[148:151]\n" \
    "v_mfma_f32_16x16x32_bf16 v[156:159], v[132:135], v[52:55], v[156:159]\n" \
    "v_mfma_f32_16x16x32_bf16 v[164:167], v[132:135], v[60:63], v[164:167]\n" \
    "v_cvt_pk_bf16_f32 v65, v136, v137\n" \
    "v_cvt_pk_bf16_f32 v68, v138, v139\n" \
    "v_cvt_pk_bf16_f32 v73, v144, v145\n" \
    "v_cvt_pk_bf16_f32 v76, v146, v147\n" \
    "v_cvt_pk_bf16_f32 v81, v152, v153\n" \
    "v_cvt_pk_bf16_f32 v84, v154, v155\n" \
    "v_cvt_pk_bf16_f32 v89, v160, v161\n" \
    "v_cvt_pk_bf16_f32 v92, v162, v163\n" \
    "v_cvt_pk_bf16_f32 v66, v142, v143\n" \
    "v_cvt_pk_bf16_f32 v67, v142, v143\n" \
    "v_cvt_pk_bf16_f32 v69, v140, v141\n" \
    "v_cvt_pk_bf16_f32 v74, v150, v151\n" \
    "v_cvt_pk_bf16_f32 v75, v150, v151\n" \
    "v_cvt_pk_bf16_f32 v77, v148, v149\n" \
    "v_cvt_pk_bf16_f32 v82, v158, v159\n" \
    "v_cvt_pk_bf16_f32 v83, v158, v159\n" \
    "v_cvt_pk_bf16_f32 v85, v156, v157\n" \
    "v_cvt_pk_bf16_f32 v90, v166, v167\n" \
    "v_cvt_pk_bf16_f32 v91, v166, v167\n" \
    "v_cvt_pk_bf16_f32 v93, v164, v165\n" \
    "v_permlane16_swap_b32 v66, v67\n" \
    "v_permlane16_swap_b32 v74, v75\n" \
    "v_permlane16_swap_b32 v82, v83\n" \
    "v_permlane16_swap_b32 v90, v91\n" \
    "v_mov_b64 v[176:177], 0\n" \
    "v_mov_b64 v[178:179], 0\n" \
    "v_mov_b64 v[180:181], 0\n" \
    "v_mov_b64 v[182:183], 0\n" \
    "v_mov_b64 v[184:185], 0\n" \
    "v_mov_b64 v[186:187], 0\n" \
    "v_mov_b64 v[188:189], 0\n" \
    "v_mov_b64 v[190:191], 0\n" \
    "v_mov_b64 v[196:197], 0\n" \
    "v_mov_b64 v[198:199], 0\n" \
    "v_mov_b64 v[200:201], 0\n" \
    "v_mov_b64 v[202:203], 0\n" \
    "v_mov_b64 v[204:205], 0\n" \
    "v_mov_b64 v[206:207], 0\n" \
    "v_mov_b64 v[208:209], 0\n" \
    "v_mov_b64 v[210:211], 0\n" \
    "v_mov_b64 v[212:213], 0\n" \
    "v_mov_b64 v[214:215], 0\n" \
    "s_add_u32 s85, s60, 0x1000\n" \
    "s_branch L%=_Tu2b\n" \
    "L%=_Tu2a:\n" \
    "s_sleep 4\n" \
    "L%=_Tu2b:\n" \
    "s_memrealtime vcc\n" \
    "s_add_u32 s80, s77, s64\n" \
    "s_add_u32 s80, s80, s69\n" \
    "s_waitcnt lgkmcnt(0)\n" \
    "s_sub_i32 s79, s80, vcc_lo\n" \
    "s_sub_u32 s79, s79, 40\n" \
    "s_cmp_lt_u32 s79, 0x400\n" \
    "s_cbranch_scc1 L%=_Tu2a\n" \
    "s_memtime s[38:39]\n" \
    "s_memrealtime vcc\n" \
    "s_nop 4\n" \
    "s_memrealtime s[32:33]\n" \
    "s_nop 4\n" \
    "s_memrealtime s[34:35]\n" \
    "s_nop 4\n" \
    "s_memrealtime s[36:37]\n" \
    "s_add_u32 s80, s77, s64\n" \
    "s_lshl_b32 s80, s80, 2\n" \
    "s_waitcnt lgkmcnt(0)\n" \
    "s_sub_u32 s80, s80, vcc_lo\n" \
    "s_sub_u32 s80, s80, s32\n" \
    "s_sub_u32 s80, s80, s34\n" \
    "s_sub_u32 s80, s80, s36\n" \
    "s_mul_i32 s80, s80, s71\n" \
    "s_ashr_i32 s80, s80, 6\n" \
    "s_add_u32 s78, s80, s38\n" \
    "v_mov_b32 v143, s76\n" \
    "s_barrier\n" \
    "s_cmp_eq_u32 s76, 63\n" \
    "s_cbranch_scc1 L%=_Tfin\n" \
    "ds_read_b32 v216, v229\n" \
    "s_waitcnt lgkmcnt(0)\n" \
    "v_readfirstlane_b32 s94, v216\n" \
    "s_mul_i32 s79, s94, s70\n" \
    "s_add_u32 s79, s79, s81\n" \
    "s_cmp_lg_u32 s79, s77\n" \
    "s_cbranch_scc1 L%=_Tu3s\n" \
    "L%=_Tu3k:\n" \
    "L%=_Tu4a:\n" \
    "s_memtime s[38:39]\n" \
    "s_mul_hi_u32 s80, s66, s96\n" \
    "s_add_u32 s80, s80, s78\n" \
    "s_waitcnt lgkmcnt(0)\n" \
    "s_sub_i32 s79, s80, s38\n" \
    "s_max_i32 s79, s79, 0\n" \
    "s_cmp_gt_u32 s79, 320\n" \
    "s_cbranch_scc1 L%=_Tu4s\n" \
    ".p2align 4\n" \
    "L%=_Tu4d:\n" \
    "s_sub_u32 s79, s79, 28\n" \
    "s_cbranch_scc0 L%=_Tu4d\n" \
    "L%=_Tu4e:\n" \
    "buffer_load_dwordx4 v[104:107], v230, s[40:43], s60 offen offset:0 sc1\n" \
    "buffer_load_dwordx2 v[120:121], v231, s[40:43], s60 offen offset:0 sc1\n" \
    "buffer_load_dwordx4 v[108:111], v230, s[40:43], s60 offen offset:2048 sc1\n" \
    "buffer_load_dwordx2 v[122:123], v231, s[40:43], s60 offen offset:2048 sc1\n" \
    "buffer_load_dwordx4 v[112:115], v230, s[40:43], s85 offen offset:0 sc1\n" \
    "buffer_load_dwordx2 v[124:125], v231, s[40:43], s85 offen offset:0 sc1\n" \
    "buffer_load_dwordx4 v[116:119], v230, s[40:43], s85 offen offset:2048 sc1\n" \
    "buffer_load_dwordx2 v[126:127], v231, s[40:43], s85 offen offset:2048 sc1\n" \
    "s_setprio 3\n" \
    "buffer_load_dwordx4 v[168:171], v225, s[40:43], s60 offen sc1\n" \
    "buffer_load_dwordx4 v[172:175], v225, s[40:43], s85 offen sc1\n" \
    "L%=_Tsc:\n" \
    "s_waitcnt vmcnt(6)\n" \
    "v_pk_add_f32 v[128:129], v[104:105], v[108:109]\n" \
    "v_pk_add_f32 v[130:131], v[106:107], v[110:111]\n" \
    "v_pk_add_f32 v[132:133], v[120:121], v[122:123]\n" \
    "s_waitcnt vmcnt(4)\n" \
    "v_pk_add_f32 v[128:129], v[128:129], v[112:113]\n" \
    "v_pk_add_f32 v[130:131], v[130:131], v[114:115]\n" \
    "v_pk_add_f32 v[132:133], v[132:133], v[124:125]\n" \
    "s_waitcnt vmcnt(2)\n" \
    "v_pk_add_f32 v[128:129], v[128:129], v[116:117]\n" \
    "v_pk_add_f32 v[130:131], v[130:131], v[118:119]\n" \
    "v_pk_add_f32 v[132:133], v[132:133], v[126:127]\n" \
    "v_max3_f32 v136, v128, v129, v130\n" \
    "v_max3_f32 v137, v131, v132, v133\n" \
    "v_max_f32 v136, v136, v137\n" \
    "v_fma_f32 v141, v131, v132, v133\n" \
    "v_fma_f32 v140, v128, v129, v130\n" \
    "v_max_f32_dpp v138, v136, v136 row_ror:8 row_mask:0xf bank_mask:0xf\n" \
    "v_max_f32_dpp v139, v136, v136 row_ror:8 row_mask:0xf bank_mask:0xf\n" \
    "s_waitcnt vmcnt(0)\n" \
    "v_smfmac_f32_16x16x64_bf16 v[176:179], v[168:171], v[0:7], v224\n" \
    "v_cmp_u_f32 vcc, v140, v141\n" \
    "v_permlane16_swap_b32 v138, v139\n" \
    "v_max_f32 v136, v138, v139\n" \
    "v_max_f32 v137, v138, v139\n" \
    "v_smfmac_f32_16x16x64_bf16 v[180:183], v[168:171], v[8:15], v224\n" \
    "s_cbranch_vccnz L%=_Tslow\n" \
    "L%=_Tok:\n" \
    "v_permlane32_swap_b32 v136, v137\n" \
    "v_max_f32 v136, v136, v137\n" \
    "v_mul_f32 v136, s72, v136\n" \
    "v_pk_fma_f32 v[128:129], v[128:129], s[72:73], v[136:137] op_sel_hi:[1,1,0] neg_lo:[0,0,1] neg_hi:[0,0,1]\n" \
    "v_pk_fma_f32 v[132:133], v[132:133], s[72:73], v[136:137] op_sel_hi:[1,1,0] neg_lo:[0,0,1] neg_hi:[0,0,1]\n" \
    "v_pk_fma_f32 v[130:131], v[130:131], s[72:73], v[136:137] op_sel_hi:[1,1,0] neg_lo:[0,0,1] neg_hi:[0,0,1]\n" \
    "v_exp_f32 v128, v128\n" \
    "v_exp_f32 v129, v129\n" \
    "v_exp_f32 v132, v132\n" \
    "v_exp_f32 v133, v133\n" \
    "v_exp_f32 v130, v130\n" \
    "v_exp_f32 v131, v131\n" \
    "v_cvt_pk_bf16_f32 v140, v128, v129\n" \
    "v_cvt_pk_bf16_f32 v141, v132, v133\n" \
    "v_cvt_pk_bf16_f32 v142, v130, v131\n" \
    "ds_write_b128 v232, v[140:143]\n" \
    "s_barrier\n" \
    "ds_read_b128 v[192:195], v227\n" \
    "s_waitcnt vmcnt(0)\n" \
    "v_smfmac_f32_16x16x64_bf16 v[176:179], v[172:175], v[32:39], v224\n" \
    "L%=_Tvc:\n" \
    "v_smfmac_f32_16x16x64_bf16 v[180:183], v[172:175], v[40:47], v224\n" \
    "v_smfmac_f32_16x16x64_bf16 v[184:187], v[168:171], v[16:23], v224\n" \
    "v_add_f32 v176, v176, v178\n" \
    "v_smfmac_f32_16x16x64_bf16 v[188:191], v[168:171], v[24:31], v224\n" \
    "v_add_f32 v177, v177, v179\n" \
    "v_smfmac_f32_16x16x64_bf16 v[184:187], v[172:175], v[48:55], v224\n" \
    "v_cmp_u_f32 vcc, v176, v177\n" \
    "v_smfmac_f32_16x16x64_bf16 v[188:191], v[172:175], v[56:63], v224\n" \
    "s_cbranch_vccnz L%=_Tvslow\n" \
    "L%=_Tvok:\n" \
    "v_cvt_pk_bf16_f32 v64, v176, v177\n" \
    "v_add_f32 v180, v180, v182\n" \
    "s_waitcnt lgkmcnt(0)\n" \
    "v_smfmac_f32_16x16x64_bf16 v[212:215], v[192:195], v[96:103], v224\n" \
    "v_add_f32 v181, v181, v183\n" \
    "v_smfmac_f32_16x16x64_bf16 v[196:199], v[192:195], v[64:71], v224\n" \
    "v_cvt_pk_bf16_f32 v72, v180, v181\n" \
    "v_add_f32 v184, v184, v186\n" \
    "v_add_f32 v185, v185, v187\n" \
    "v_smfmac_f32_16x16x64_bf16 v[200:203], v[192:195], v[72:79], v224\n" \
    "v_cvt_pk_bf16_f32 v80, v184, v185\n" \
    "v_add_f32 v188, v188, v190\n" \
    "v_add_f32 v189, v189, v191\n" \
    "v_smfmac_f32_16x16x64_bf16 v[204:207], v[192:195], v[80:87], v224\n" \
    "v_cvt_pk_bf16_f32 v88, v188, v189\n" \
    "v_add_f32 v212, v212, v214\n" \
    "v_add_f32 v213, v213, v215\n" \
    "v_smfmac_f32_16x16x64_bf16 v[208:211], v[192:195], v[88:95], v224\n" \
    "v_rcp_f32 v220, v212\n" \
    "v_rcp_f32 v221, v213\n" \
    "v_add_f32 v196, v196, v198\n" \
    "v_add_f32 v197, v197, v199\n" \
    "v_pk_add_f32 v[200:201], v[200:201], v[202:203]\n" \
    "v_pk_add_f32 v[204:205], v[204:205], v[206:207]\n" \
    "s_nop 1\n" \
    "v_pk_add_f32 v[208:209], v[208:209], v[210:211]\n" \
    "v_pk_mul_f32 v[196:197], v[196:197], v[220:221]\n" \
    "v_pk_mul_f32 v[200:201], v[200:201], v[220:221]\n" \
    "v_pk_mul_f32 v[204:205], v[204:205], v[220:221]\n" \
    "v_pk_mul_f32 v[208:209], v[208:209], v[220:221]\n" \
    "v_cvt_pk_bf16_f32 v216, v196, v200\n" \
    "v_cvt_pk_bf16_f32 v217, v204, v208\n" \
    "v_cvt_pk_bf16_f32 v218, v197, v201\n" \
    "v_cvt_pk_bf16_f32 v219, v205, v209\n" \
    "buffer_store_dwordx4 v[216:219], v226, s[40:43], s60 offen sc1\n" \
    "s_setprio 0\n" \
    "buffer_store_dword v143, v233, s[44:47], 0 offen sc1\n" \
    "ds_read_b64 v[220:221], v229 offset:64\n" \
    "s_mov_b32 s79, s60\n" \
    "s_mov_b32 s60, s61\n" \
    "s_mov_b32 s61, s62\n" \
    "s_mov_b32 s62, s63\n" \
    "s_mov_b32 s63, s79\n" \
    "s_add_u32 s76, s76, 1\n" \
    "s_add_u32 s77, s77, s65\n" \
    "s_add_u32 s84, s84, 0x20000\n" \
    "s_add_u32 s81, s81, s65\n" \
    "s_waitcnt lgkmcnt(0)\n" \
    "v_readfirstlane_b32 s65, v220\n" \
    "v_readfirstlane_b32 s71, v221\n" \
    "s_mul_i32 s96, s65, s71\n" \
    "s_lshr_b32 s96, s96, 4\n" \
    "s_branch L%=_Tloop\n" \
    "L%=_Tslow:\n" \
    "s_nop 7\n" \
    "s_nop 0\n" \
    "v_max_u32 v138, v104, v105\n" \
    "v_max3_u32 v138, v138, v106, v107\n" \
    "v_max3_u32 v138, v138, v108, v109\n" \
    "v_max3_u32 v138, v138, v110, v111\n" \
    "v_max3_u32 v138, v138, v112, v113\n" \
    "v_max3_u32 v138, v138, v114, v115\n" \
    "v_max3_u32 v138, v138, v116, v117\n" \
    "v_max3_u32 v138, v138, v118, v119\n" \
    "v_max3_u32 v138, v138, v120, v121\n" \
    "v_max3_u32 v138, v138, v122, v123\n" \
    "v_max3_u32 v138, v138, v124, v125\n" \
    "v_max3_u32 v138, v138, v126, v127\n" \
    "v_cmp_eq_u32 vcc, -1, v138\n" \
    "s_cbranch_vccz L%=_Tok\n" \
    "s_cmp_eq_u32 s99, 0\n" \
    "s_cbranch_scc1 L%=_Tok\n" \
    "s_sub_u32 s99, s99, 1\n" \
    "s_cmp_lg_u32 s99, 0\n" \
    "s_cbranch_scc1 L%=_Tu5\n" \
    "v_mov_b32 v138, 1\n" \
    "buffer_store_dword v138, off, s[44:47], 0 sc1\n" \
    "L%=_Tu5:\n" \
    "s_sleep 1\n" \
    "buffer_load_dwordx4 v[104:107], v230, s[40:43], s60 offen offset:0 sc1\n" \
    "buffer_load_dwordx2 v[120:121], v231, s[40:43], s60 offen offset:0 sc1\n" \
    "buffer_load_dwordx4 v[108:111], v230, s[40:43], s60 offen offset:2048 sc1\n" \
    "buffer_load_dwordx2 v[122:123], v231, s[40:43], s60 offen offset:2048 sc1\n" \
    "buffer_load_dwordx4 v[112:115], v230, s[40:43], s85 offen offset:0 sc1\n" \
    "buffer_load_dwordx2 v[124:125], v231, s[40:43], s85 offen offset:0 sc1\n" \
    "buffer_load_dwordx4 v[116:119], v230, s[40:43], s85 offen offset:2048 sc1\n" \
    "buffer_load_dwordx2 v[126:127], v231, s[40:43], s85 offen offset:2048 sc1\n" \
    "buffer_load_dwordx4 v[168:171], v225, s[40:43], s60 offen sc1\n" \
    "buffer_load_dwordx4 v[172:175], v225, s[40:43], s85 offen sc1\n" \
    "v_mov_b64 v[176:177], 0\n" \
    "v_mov_b64 v[178:179], 0\n" \
    "v_mov_b64 v[180:181], 0\n" \
    "v_mov_b64 v[182:183], 0\n" \
    "v_mov_b64 v[184:185], 0\n" \
    "v_mov_b64 v[186:187], 0\n" \
    "v_mov_b64 v[188:189], 0\n" \
    "v_mov_b64 v[190:191], 0\n" \
    "s_branch L%=_Tsc\n" \
    "L%=_Tvslow:\n" \
    "s_nop 7\n" \
    "s_nop 0\n" \
    "v_max_u32 v136, v168, v169\n" \
    "v_max3_u32 v136, v136, v170, v171\n" \
    "v_max3_u32 v136, v136, v172, v173\n" \
    "v_max3_u32 v136, v136, v174, v175\n" \
    "v_cmp_eq_u32 vcc, -1, v136\n" \
    "s_cbranch_vccz L%=_Tvok\n" \
    "s_cmp_eq_u32 s99, 0\n" \
    "s_cbranch_scc1 L%=_Tvok\n" \
    "s_sub_u32 s99, s99, 1\n" \
    "s_cmp_lg_u32 s99, 0\n" \
    "s_cbranch_scc1 L%=_Tu6\n" \
    "v_mov_b32 v136, 1\n" \
    "buffer_store_dword v136, off, s[44:47], 0 sc1\n" \
    "L%=_Tu6:\n" \
    "s_sleep 1\n" \
    "buffer_load_dwordx4 v[168:171], v225, s[40:43], s60 offen sc1\n" \
    "buffer_load_dwordx4 v[172:175], v225, s[40:43], s85 offen sc1\n" \
    "v_mov_b64 v[176:177], 0\n" \
    "v_mov_b64 v[178:179], 0\n" \
    "v_mov_b64 v[180:181], 0\n" \
    "v_mov_b64 v[182:183], 0\n" \
    "v_mov_b64 v[184:185], 0\n" \
    "v_mov_b64 v[186:187], 0\n" \
    "v_mov_b64 v[188:189], 0\n" \
    "v_mov_b64 v[190:191], 0\n" \
    "v_mov_b64 v[196:197], 0\n" \
    "v_mov_b64 v[198:199], 0\n" \
    "v_mov_b64 v[200:201], 0\n" \
    "v_mov_b64 v[202:203], 0\n" \
    "v_mov_b64 v[204:205], 0\n" \
    "v_mov_b64 v[206:207], 0\n" \
    "v_mov_b64 v[208:209], 0\n" \
    "v_mov_b64 v[210:211], 0\n" \
    "v_mov_b64 v[212:213], 0\n" \
    "v_mov_b64 v[214:215], 0\n" \
    "s_waitcnt vmcnt(0)\n" \
    "v_smfmac_f32_16x16x64_bf16 v[176:179], v[168:171], v[0:7], v224\n" \
    "v_smfmac_f32_16x16x64_bf16 v[180:183], v[168:171], v[8:15], v224\n" \
    "v_smfmac_f32_16x16x64_bf16 v[176:179], v[172:175], v[32:39], v224\n" \
    "s_nop 7\n" \
    "s_nop 0\n" \
    "s_branch L%=_Tvc\n" \
    "L%=_Tfin:\n" \
    "v_mov_b32 v143, 64\n" \
    "s_barrier\n" \
    "buffer_store_dword v143, v233, s[44:47], 0 offen sc1\n" \
    "s_branch L%=_end\n" \
    "L%=_Tu3s:\n" \
    "s_mov_b32 s77, s79\n" \
    "s_memtime s[38:39]\n" \
    "s_memrealtime vcc\n" \
    "s_nop 4\n" \
    "s_memrealtime s[32:33]\n" \
    "s_nop 4\n" \
    "s_memrealtime s[34:35]\n" \
    "s_nop 4\n" \
    "s_memrealtime s[36:37]\n" \
    "s_add_u32 s80, s77, s64\n" \
    "s_lshl_b32 s80, s80, 2\n" \
    "s_waitcnt lgkmcnt(0)\n" \
    "s_sub_u32 s80, s80, vcc_lo\n" \
    "s_sub_u32 s80, s80, s32\n" \
    "s_sub_u32 s80, s80, s34\n" \
    "s_sub_u32 s80, s80, s36\n" \
    "s_mul_i32 s80, s80, s71\n" \
    "s_ashr_i32 s80, s80, 6\n" \
    "s_add_u32 s78, s80, s38\n" \
    "s_branch L%=_Tu3k\n" \
    "L%=_Tu4s:\n" \
    "s_cmp_gt_u32 s79, 0x8000\n" \
    "s_cbranch_scc1 L%=_Tu4e\n" \
    "s_sleep 2\n" \
    "s_branch L%=_Tu4a\n" \
    "L%=_S:\n" \
    "s_mov_b32 s76, 0\n" \
    "s_mov_b32 s99, 0x20000\n" \
    "s_mov_b32 s84, 0\n" \
    "s_mov_b32 s81, 0\n" \
    "s_mov_b32 s86, 0\n" \
    "s_mov_b32 s87, 0\n" \
    "s_mov_b32 s88, 0\n" \
    "s_mov_b32 s89, 0\n" \
    "s_mov_b32 s90, 0\n" \
    "s_mov_b32 s91, 0\n" \
    "s_mov_b32 s92, 0\n" \
    "s_mov_b32 s93, 0\n" \
    "s_mov_b32 s94, 0\n" \
    "s_mov_b32 s95, 0\n" \
    "s_mov_b32 s96, 0\n" \
    "s_mov_b32 s97, 0\n" \
    "s_mov_b32 s98, 0\n" \
    "ds_read_b32 v216, v229\n" \
    "s_waitcnt lgkmcnt(0)\n" \
    "v_readfirstlane_b32 s94, v216\n" \
    "s_mul_i32 s79, s94, s70\n" \
    "s_add_u32 s79, s79, s81\n" \
    "s_mov_b32 s77, s79\n" \
    "s_mul_i32 s96, s65, s71\n" \
    "s_lshr_b32 s96, s96, 4\n" \
    "v_mov_b32 v96, 0x3f803f80\n" \
    "v_mov_b32 v97, 0x3f803f80\n" \
    "v_mov_b32 v98, 0x3f803f80\n" \
    "v_mov_b32 v99, 0x3f803f80\n" \
    "v_mov_b32 v100, 0x3f803f80\n" \
    "v_mov_b32 v101, 0x3f803f80\n" \
    "v_mov_b64 v[102:103], 0\n" \
    "v_mov_b64 v[64:65], 0\n" \
    "v_mov_b64 v[66:67], 0\n" \
    "v_mov_b64 v[68:69], 0\n" \
    "v_mov_b64 v[70:71], 0\n" \
    "v_mov_b64 v[72:73], 0\n" \
    "v_mov_b64 v[74:75], 0\n" \
    "v_mov_b64 v[76:77], 0\n" \
    "v_mov_b64 v[78:79], 0\n" \
    "v_mov_b64 v[80:81], 0\n" \
    "v_mov_b64 v[82:83], 0\n" \
    "v_mov_b64 v[84:85], 0\n" \
    "v_mov_b64 v[86:87], 0\n" \
    "v_mov_b64 v[88:89], 0\n" \
    "v_mov_b64 v[90:91], 0\n" \
    "v_mov_b64 v[92:93], 0\n" \
    "v_mov_b64 v[94:95], 0\n" \
    "v_mov_b64 v[192:193], 0\n" \
    "v_mov_b64 v[194:195], 0\n" \
    "L%=_Sloop:\n" \
    "ds_read_b64_tr_b16 v[104:105], v228 offset:16384\n" \
    "ds_read_b64_tr_b16 v[106:107], v228 offset:16416\n" \
    "ds_read_b64_tr_b16 v[108:109], v228 offset:18688\n" \
    "ds_read_b64_tr_b16 v[110:111], v228 offset:18720\n" \
    "ds_read_b64_tr_b16 v[112:113], v228 offset:20992\n" \
    "ds_read_b64_tr_b16 v[114:115], v228 offset:21024\n" \
    "ds_read_b64_tr_b16 v[116:117], v228 offset:23296\n" \
    "ds_read_b64_tr_b16 v[118:119], v228 offset:23328\n" \
    "ds_read_b64_tr_b16 v[120:121], v228 offset:16656\n" \
    "ds_read_b64_tr_b16 v[122:123], v228 offset:16688\n" \
    "ds_read_b64_tr_b16 v[124:125], v228 offset:18960\n" \
    "ds_read_b64_tr_b16 v[126:127], v228 offset:18992\n" \
    "ds_read_b64_tr_b16 v[128:129], v228 offset:21264\n" \
    "ds_read_b64_tr_b16 v[130:131], v228 offset:21296\n" \
    "ds_read_b64_tr_b16 v[132:133], v228 offset:23568\n" \
    "ds_read_b64_tr_b16 v[134:135], v228 offset:23600\n" \
    "s_waitcnt lgkmcnt(14)\n" \
    "v_mfma_f32_16x16x32_bf16 v[136:139], v[104:107], v[0:3], 0\n" \
    "v_mfma_f32_16x16x32_bf16 v[144:147], v[104:107], v[8:11], 0\n" \
    "v_mfma_f32_16x16x32_bf16 v[152:155], v[104:107], v[16:19], 0\n" \
    "v_mfma_f32_16x16x32_bf16 v[160:163], v[104:107], v[24:27], 0\n" \
    "s_waitcnt lgkmcnt(12)\n" \
    "v_mfma_f32_16x16x32_bf16 v[136:139], v[108:111], v[4:7], v[136:139]\n" \
    "v_mfma_f32_16x16x32_bf16 v[144:147], v[108:111], v[12:15], v[144:147]\n" \
    "v_mfma_f32_16x16x32_bf16 v[152:155], v[108:111], v[20:23], v[152:155]\n" \
    "v_mfma_f32_16x16x32_bf16 v[160:163], v[108:111], v[28:31], v[160:163]\n" \
    "s_waitcnt lgkmcnt(10)\n" \
    "v_mfma_f32_16x16x32_bf16 v[136:139], v[112:115], v[32:35], v[136:139]\n" \
    "v_mfma_f32_16x16x32_bf16 v[144:147], v[112:115], v[40:43], v[144:147]\n" \
    "v_mfma_f32_16x16x32_bf16 v[152:155], v[112:115], v[48:51], v[152:155]\n" \
    "v_mfma_f32_16x16x32_bf16 v[160:163], v[112:115], v[56:59], v[160:163]\n" \
    "s_waitcnt lgkmcnt(8)\n" \
    "v_mfma_f32_16x16x32_bf16 v[136:139], v[116:119], v[36:39], v[136:139]\n" \
    "v_mfma_f32_16x16x32_bf16 v[144:147], v[116:119], v[44:47], v[144:147]\n" \
    "v_mfma_f32_16x16x32_bf16 v[152:155], v[116:119], v[52:55], v[152:155]\n" \
    "v_mfma_f32_16x16x32_bf16 v[160:163], v[116:119], v[60:63], v[160:163]\n" \
    "s_waitcnt lgkmcnt(6)\n" \
    "v_mfma_f32_16x16x32_bf16 v[140:143], v[120:123], v[0:3], 0\n" \
    "v_mfma_f32_16x16x32_bf16 v[148:151], v[120:123], v[8:11], 0\n" \
    "v_mfma_f32_16x16x32_bf16 v[156:159], v[120:123], v[16:19], 0\n" \
    "v_mfma_f32_16x16x32_bf16 v[164:167], v[120:123], v[24:27], 0\n" \
    "s_waitcnt lgkmcnt(4)\n" \
    "v_mfma_f32_16x16x32_bf16 v[140:143], v[124:127], v[4:7], v[140:143]\n" \
    "v_mfma_f32_16x16x32_bf16 v[148:151], v[124:127], v[12:15], v[148:151]\n" \
    "v_mfma_f32_16x16x32_bf16 v[156:159], v[124:127], v[20:23], v[156:159]\n" \
    "v_mfma_f32_16x16x32_bf16 v[164:167], v[124:127], v[28:31], v[164:167]\n" \
    "s_waitcnt lgkmcnt(2)\n" \
    "v_mfma_f32_16x16x32_bf16 v[140:143], v[128:131], v[32:35], v[140:143]\n" \
    "v_mfma_f32_16x16x32_bf16 v[148:151], v[128:131], v[40:43], v[148:151]\n" \
    "v_mfma_f32_16x16x32_bf16 v[156:159], v[128:131], v[48:51], v[156:159]\n" \
    "v_mfma_f32_16x16x32_bf16 v[164:167], v[128:131], v[56:59], v[164:167]\n" \
    "s_waitcnt lgkmcnt(0)\n" \
    "v_mfma_f32_16x16x32_bf16 v[140:143], v[132:135], v[36:39], v[140:143]\n" \
    "v_mfma_f32_16x16x32_bf16 v[148:151], v[132:135], v[44:47], v[148:151]\n" \
    "v_mfma_f32_16x16x32_bf16 v[156:159], v[132:135], v[52:55], v[156:159]\n" \
    "v_mfma_f32_16x16x32_bf16 v[164:167], v[132:135], v[60:63], v[164:167]\n" \
    "v_cvt_pk_bf16_f32 v65, v136, v137\n" \
    "v_cvt_pk_bf16_f32 v68, v138, v139\n" \
    "v_cvt_pk_bf16_f32 v73, v144, v145\n" \
    "v_cvt_pk_bf16_f32 v76, v146, v147\n" \
    "v_cvt_pk_bf16_f32 v81, v152, v153\n" \
    "v_cvt_pk_bf16_f32 v84, v154, v155\n" \
    "v_cvt_pk_bf16_f32 v89, v160, v161\n" \
    "v_cvt_pk_bf16_f32 v92, v162, v163\n" \
    "v_cvt_pk_bf16_f32 v66, v142, v143\n" \
    "v_cvt_pk_bf16_f32 v67, v142, v143\n" \
    "v_cvt_pk_bf16_f32 v69, v140, v141\n" \
    "v_cvt_pk_bf16_f32 v74, v150, v151\n" \
    "v_cvt_pk_bf16_f32 v75, v150, v151\n" \
    "v_cvt_pk_bf16_f32 v77, v148, v149\n" \
    "v_cvt_pk_bf16_f32 v82, v158, v159\n" \
    "v_cvt_pk_bf16_f32 v83, v158, v159\n" \
    "v_cvt_pk_bf16_f32 v85, v156, v157\n" \
    "v_cvt_pk_bf16_f32 v90, v166, v167\n" \
    "v_cvt_pk_bf16_f32 v91, v166, v167\n" \
    "v_cvt_pk_bf16_f32 v93, v164, v165\n" \
    "v_permlane16_swap_b32 v66, v67\n" \
    "v_permlane16_swap_b32 v74, v75\n" \
    "v_permlane16_swap_b32 v82, v83\n" \
    "v_permlane16_swap_b32 v90, v91\n" \
    "ds_read_b128 v[240:243], v234 offset:25600\n" \
    "ds_read_b128 v[244:247], v234 offset:26624\n" \
    "v_mov_b64 v[176:177], 0\n" \
    "v_mov_b64 v[178:179], 0\n" \
    "v_mov_b64 v[180:181], 0\n" \
    "v_mov_b64 v[182:183], 0\n" \
    "v_mov_b64 v[184:185], 0\n" \
    "v_mov_b64 v[186:187], 0\n" \
    "v_mov_b64 v[188:189], 0\n" \
    "v_mov_b64 v[190:191], 0\n" \
    "v_mov_b64 v[196:197], 0\n" \
    "v_mov_b64 v[198:199], 0\n" \
    "v_mov_b64 v[200:201], 0\n" \
    "v_mov_b64 v[202:203], 0\n" \
    "v_mov_b64 v[204:205], 0\n" \
    "v_mov_b64 v[206:207], 0\n" \
    "v_mov_b64 v[208:209], 0\n" \
    "v_mov_b64 v[210:211], 0\n" \
    "v_mov_b64 v[212:213], 0\n" \
    "v_mov_b64 v[214:215], 0\n" \
    "s_add_u32 s85, s60, 0x1000\n" \
    "s_branch L%=_Su2b\n" \
    "L%=_Su2a:\n" \
    "s_sleep 4\n" \
    "L%=_Su2b:\n" \
    "s_memrealtime vcc\n" \
    "s_add_u32 s80, s77, s64\n" \
    "s_add_u32 s80, s80, s69\n" \
    "s_waitcnt lgkmcnt(0)\n" \
    "s_sub_i32 s79, s80, vcc_lo\n" \
    "s_sub_u32 s79, s79, 40\n" \
    "s_cmp_lt_u32 s79, 0x400\n" \
    "s_cbranch_scc1 L%=_Su2a\n" \
    "s_memtime s[38:39]\n" \
    "s_memrealtime vcc\n" \
    "s_nop 4\n" \
    "s_memrealtime s[32:33]\n" \
    "s_nop 4\n" \
    "s_memrealtime s[34:35]\n" \
    "s_nop 4\n" \
    "s_memrealtime s[36:37]\n" \
    "s_add_u32 s80, s77, s64\n" \
    "s_lshl_b32 s80, s80, 2\n" \
    "s_waitcnt lgkmcnt(0)\n" \
    "s_sub_u32 s80, s80, vcc_lo\n" \
    "s_sub_u32 s80, s80, s32\n" \
    "s_sub_u32 s80, s80, s34\n" \
    "s_sub_u32 s80, s80, s36\n" \
    "s_mul_i32 s80, s80, s71\n" \
    "s_ashr_i32 s80, s80, 6\n" \
    "s_add_u32 s78, s80, s38\n" \
    "L%=_Su3a:\n" \
    "s_memtime s[38:39]\n" \
    "s_mul_i32 s80, s69, 23\n" \
    "s_add_u32 s80, s80, s78\n" \
    "s_waitcnt lgkmcnt(0)\n" \
    "s_sub_i32 s79, s80, s38\n" \
    "s_max_i32 s79, s79, 0\n" \
    "s_cmp_gt_u32 s79, 320\n" \
    "s_cbranch_scc1 L%=_Su3s\n" \
    ".p2align 4\n" \
    "L%=_Su3d:\n" \
    "s_sub_u32 s79, s79, 28\n" \
    "s_cbranch_scc0 L%=_Su3d\n" \
    "L%=_Su3e:\n" \
    "s_sub_u32 s79, s84, 0x20000\n" \
    "s_cselect_b32 s79, 0x40000000, s79\n" \
    "buffer_store_dwordx4 v[236:239], v233, s[48:51], s79 offen sc1\n" \
    "s_setprio 3\n" \
    "s_barrier\n" \
    "L%=_Srd:\n" \
    "ds_read_b128 v[136:139], v229 offset:32\n" \
    "ds_read_b128 v[104:107], v230 offset:0\n" \
    "ds_read_b128 v[108:111], v230 offset:2176\n" \
    "ds_read_b128 v[112:115], v230 offset:4352\n" \
    "ds_read_b128 v[116:119], v230 offset:6528\n" \
    "ds_read_b128 v[120:123], v230 offset:1088\n" \
    "ds_read_b128 v[124:127], v230 offset:3264\n" \
    "ds_read_b128 v[128:131], v230 offset:5440\n" \
    "ds_read_b128 v[132:135], v230 offset:7616\n" \
    "s_waitcnt lgkmcnt(8)\n" \
    "v_min3_u32 v136, v136, v137, v138\n" \
    "v_min_u32 v136, v136, v139\n" \
    "v_cmp_ge_u32 vcc, s76, v136\n" \
    "s_cbranch_vccnz L%=_Srd\n" \
    "s_waitcnt lgkmcnt(6)\n" \
    "v_pk_add_f32 v[104:105], v[104:105], v[108:109]\n" \
    "v_pk_add_f32 v[106:107], v[106:107], v[110:111]\n" \
    "s_waitcnt lgkmcnt(4)\n" \
    "v_pk_add_f32 v[112:113], v[112:113], v[116:117]\n" \
    "v_pk_add_f32 v[114:115], v[114:115], v[118:119]\n" \
    "v_pk_add_f32 v[104:105], v[104:105], v[112:113]\n" \
    "v_pk_add_f32 v[106:107], v[106:107], v[114:115]\n" \
    "v_cvt_pk_bf16_f32 v236, v104, v105\n" \
    "v_cvt_pk_bf16_f32 v237, v106, v107\n" \
    "s_waitcnt lgkmcnt(2)\n" \
    "v_pk_add_f32 v[120:121], v[120:121], v[124:125]\n" \
    "v_pk_add_f32 v[122:123], v[122:123], v[126:127]\n" \
    "s_waitcnt lgkmcnt(0)\n" \
    "v_pk_add_f32 v[128:129], v[128:129], v[132:133]\n" \
    "v_pk_add_f32 v[130:131], v[130:131], v[134:135]\n" \
    "v_pk_add_f32 v[120:121], v[120:121], v[128:129]\n" \
    "v_pk_add_f32 v[122:123], v[122:123], v[130:131]\n" \
    "v_cvt_pk_bf16_f32 v238, v120, v121\n" \
    "v_cvt_pk_bf16_f32 v239, v122, v123\n" \
    "s_nop 1\n" \
    "v_mfma_f32_16x16x32_bf16 v[140:143], v[240:243], v[236:239], 0\n" \
    "v_mfma_f32_16x16x32_bf16 v[144:147], v[244:247], v[236:239], 0\n" \
    "v_mfma_f32_16x16x32_bf16 v[148:151], v[236:239], v[236:239], 0\n" \
    "buffer_store_dwordx4 v[140:143], v231, s[40:43], s60 offen sc1\n" \
    "s_nop 2\n" \
    "buffer_store_dwordx4 v[144:147], v231, s[40:43], s60 offen offset:512 sc1\n" \
    "s_nop 2\n" \
    "buffer_store_dwordx4 v[148:151], v232, s[40:43], s60 offen sc1\n" \
    "s_cmp_eq_u32 s76, 63\n" \
    "s_cbranch_scc1 L%=_Sfin\n" \
    "ds_read_b32 v216, v229\n" \
    "s_waitcnt lgkmcnt(0)\n" \
    "v_readfirstlane_b32 s94, v216\n" \
    "s_mul_i32 s79, s94, s70\n" \
    "s_add_u32 s79, s79, s81\n" \
    "s_cmp_lg_u32 s79, s77\n" \
    "s_cbranch_scc1 L%=_Su4s\n" \
    "L%=_Su4k:\n" \
    "L%=_Su5a:\n" \
    "s_memtime s[38:39]\n" \
    "s_mul_hi_u32 s80, s67, s96\n" \
    "s_add_u32 s80, s80, s78\n" \
    "s_waitcnt lgkmcnt(0)\n" \
    "s_sub_i32 s79, s80, s38\n" \
    "s_max_i32 s79, s79, 0\n" \
    "s_cmp_gt_u32 s79, 320\n" \
    "s_cbranch_scc1 L%=_Su5s\n" \
    ".p2align 4\n" \
    "L%=_Su5d:\n" \
    "s_sub_u32 s79, s79, 28\n" \
    "s_cbranch_scc0 L%=_Su5d\n" \
    "L%=_Su5e:\n" \
    "buffer_load_dwordx4 v[168:171], v225, s[40:43], s60 offen sc1\n" \
    "buffer_load_dwordx4 v[172:175], v225, s[40:43], s85 offen sc1\n" \
    "L%=_Svc:\n" \
    "s_waitcnt vmcnt(1)\n" \
    "v_smfmac_f32_16x16x64_bf16 v[176:179], v[168:171], v[0:7], v224\n" \
    "v_smfmac_f32_16x16x64_bf16 v[180:183], v[168:171], v[8:15], v224\n" \
    "s_waitcnt vmcnt(0)\n" \
    "v_smfmac_f32_16x16x64_bf16 v[176:179], v[172:175], v[32:39], v224\n" \
    "v_smfmac_f32_16x16x64_bf16 v[180:183], v[172:175], v[40:47], v224\n" \
    "v_smfmac_f32_16x16x64_bf16 v[184:187], v[168:171], v[16:23], v224\n" \
    "v_smfmac_f32_16x16x64_bf16 v[188:191], v[168:171], v[24:31], v224\n" \
    "v_add_f32 v176, v176, v178\n" \
    "v_smfmac_f32_16x16x64_bf16 v[184:187], v[172:175], v[48:55], v224\n" \
    "v_add_f32 v177, v177, v179\n" \
    "v_smfmac_f32_16x16x64_bf16 v[188:191], v[172:175], v[56:63], v224\n" \
    "v_cmp_u_f32 vcc, v176, v177\n" \
    "v_cvt_pk_bf16_f32 v64, v176, v177\n" \
    "v_add_f32 v180, v180, v182\n" \
    "v_add_f32 v181, v181, v183\n" \
    "v_cvt_pk_bf16_f32 v72, v180, v181\n" \
    "s_cbranch_vccnz L%=_Svslow\n" \
    "L%=_Svok:\n" \
    "v_add_f32 v184, v184, v186\n" \
    "v_add_f32 v185, v185, v187\n" \
    "v_cvt_pk_bf16_f32 v80, v184, v185\n" \
    "v_add_f32 v188, v188, v190\n" \
    "v_add_f32 v189, v189, v191\n" \
    "v_cvt_pk_bf16_f32 v88, v188, v189\n" \
    "s_barrier\n" \
    "L%=_Sfr:\n" \
    "ds_read_b128 v[192:195], v227\n" \
    "s_waitcnt lgkmcnt(0)\n" \
    "v_smfmac_f32_16x16x64_bf16 v[212:215], v[192:195], v[96:103], v224\n" \
    "v_cmp_ne_u32 vcc, s76, v195\n" \
    "v_smfmac_f32_16x16x64_bf16 v[196:199], v[192:195], v[64:71], v224\n" \
    "s_cbranch_vccnz L%=_Sfs\n" \
    "v_smfmac_f32_16x16x64_bf16 v[200:203], v[192:195], v[72:79], v224\n" \
    "v_smfmac_f32_16x16x64_bf16 v[204:207], v[192:195], v[80:87], v224\n" \
    "v_add_f32 v212, v212, v214\n" \
    "v_smfmac_f32_16x16x64_bf16 v[208:211], v[192:195], v[88:95], v224\n" \
    "v_add_f32 v213, v213, v215\n" \
    "v_rcp_f32 v220, v212\n" \
    "v_rcp_f32 v221, v213\n" \
    "v_add_f32 v196, v196, v198\n" \
    "v_add_f32 v197, v197, v199\n" \
    "v_pk_add_f32 v[200:201], v[200:201], v[202:203]\n" \
    "v_pk_add_f32 v[204:205], v[204:205], v[206:207]\n" \
    "s_nop 0\n" \
    "v_pk_add_f32 v[208:209], v[208:209], v[210:211]\n" \
    "v_pk_mul_f32 v[196:197], v[196:197], v[220:221]\n" \
    "v_pk_mul_f32 v[200:201], v[200:201], v[220:221]\n" \
    "v_pk_mul_f32 v[204:205], v[204:205], v[220:221]\n" \
    "v_pk_mul_f32 v[208:209], v[208:209], v[220:221]\n" \
    "v_cvt_pk_bf16_f32 v216, v196, v200\n" \
    "v_cvt_pk_bf16_f32 v217, v204, v208\n" \
    "v_cvt_pk_bf16_f32 v218, v197, v201\n" \
    "v_cvt_pk_bf16_f32 v219, v205, v209\n" \
    "buffer_store_dwordx4 v[216:219], v226, s[40:43], s60 offen sc1\n" \
    "s_setprio 0\n" \
    "ds_read_b64 v[220:221], v229 offset:64\n" \
    "s_mov_b32 s79, s60\n" \
    "s_mov_b32 s60, s61\n" \
    "s_mov_b32 s61, s62\n" \
    "s_mov_b32 s62, s63\n" \
    "s_mov_b32 s63, s79\n" \
    "s_add_u32 s76, s76, 1\n" \
    "s_add_u32 s77, s77, s65\n" \
    "s_add_u32 s84, s84, 0x20000\n" \
    "s_add_u32 s81, s81, s65\n" \
    "s_waitcnt lgkmcnt(0)\n" \
    "v_readfirstlane_b32 s65, v220\n" \
    "v_readfirstlane_b32 s71, v221\n" \
    "s_mul_i32 s96, s65, s71\n" \
    "s_lshr_b32 s96, s96, 4\n" \
    "s_branch L%=_Sloop\n" \
    "L%=_Sfin:\n" \
    "buffer_store_dwordx4 v[236:239], v233, s[48:51], s84 offen sc1\n" \
    "s_waitcnt vmcnt(0)\n" \
    "s_barrier\n" \
    "s_branch L%=_end\n" \
    "L%=_Su3s:\n" \
    "s_cmp_gt_u32 s79, 0x8000\n" \
    "s_cbranch_scc1 L%=_Su3e\n" \
    "s_sleep 2\n" \
    "s_branch L%=_Su3a\n" \
    "L%=_Su4s:\n" \
    "s_mov_b32 s77, s79\n" \
    "s_memtime s[38:39]\n" \
    "s_memrealtime vcc\n" \
    "s_nop 4\n" \
    "s_memrealtime s[32:33]\n" \
    "s_nop 4\n" \
    "s_memrealtime s[34:35]\n" \
    "s_nop 4\n" \
    "s_memrealtime s[36:37]\n" \
    "s_add_u32 s80, s77, s64\n" \
    "s_lshl_b32 s80, s80, 2\n" \
    "s_waitcnt lgkmcnt(0)\n" \
    "s_sub_u32 s80, s80, vcc_lo\n" \
    "s_sub_u32 s80, s80, s32\n" \
    "s_sub_u32 s80, s80, s34\n" \
    "s_sub_u32 s80, s80, s36\n" \
    "s_mul_i32 s80, s80, s71\n" \
    "s_ashr_i32 s80, s80, 6\n" \
    "s_add_u32 s78, s80, s38\n" \
    "s_branch L%=_Su4k\n" \
    "L%=_Su5s:\n" \
    "s_cmp_gt_u32 s79, 0x8000\n" \
    "s_cbranch_scc1 L%=_Su5e\n" \
    "s_sleep 2\n" \
    "s_branch L%=_Su5a\n" \
    "L%=_Svslow:\n" \
    "s_nop 7\n" \
    "s_nop 0\n" \
    "v_max_u32 v216, v168, v169\n" \
    "v_max3_u32 v216, v216, v170, v171\n" \
    "v_max3_u32 v216, v216, v172, v173\n" \
    "v_max3_u32 v216, v216, v174, v175\n" \
    "v_cmp_eq_u32 vcc, -1, v216\n" \
    "s_cbranch_vccz L%=_Svok\n" \
    "s_cmp_eq_u32 s99, 0\n" \
    "s_cbranch_scc1 L%=_Svok\n" \
    "s_sub_u32 s99, s99, 1\n" \
    "s_cmp_lg_u32 s99, 0\n" \
    "s_cbranch_scc1 L%=_Su6\n" \
    "v_mov_b32 v216, 1\n" \
    "buffer_store_dword v216, off, s[44:47], 0 sc1\n" \
    "L%=_Su6:\n" \
    "s_sleep 1\n" \
    "buffer_load_dwordx4 v[168:171], v225, s[40:43], s60 offen sc1\n" \
    "buffer_load_dwordx4 v[172:175], v225, s[40:43], s85 offen sc1\n" \
    "v_mov_b64 v[176:177], 0\n" \
    "v_mov_b64 v[178:179], 0\n" \
    "v_mov_b64 v[180:181], 0\n" \
    "v_mov_b64 v[182:183], 0\n" \
    "v_mov_b64 v[184:185], 0\n" \
    "v_mov_b64 v[186:187], 0\n" \
    "v_mov_b64 v[188:189], 0\n" \
    "v_mov_b64 v[190:191], 0\n" \
    "s_waitcnt vmcnt(0)\n" \
    "s_branch L%=_Svc\n" \
    "L%=_Sfs:\n" \
    "s_nop 7\n" \
    "s_nop 0\n" \
    "v_mov_b64 v[196:197], 0\n" \
    "v_mov_b64 v[198:199], 0\n" \
    "v_mov_b64 v[200:201], 0\n" \
    "v_mov_b64 v[202:203], 0\n" \
    "v_mov_b64 v[204:205], 0\n" \
    "v_mov_b64 v[206:207], 0\n" \
    "v_mov_b64 v[208:209], 0\n" \
    "v_mov_b64 v[210:211], 0\n" \
    "v_mov_b64 v[212:213], 0\n" \
    "v_mov_b64 v[214:215], 0\n" \
    "s_branch L%=_Sfr\n" \
    "L%=_R:\n" \
    "s_mov_b32 s76, 0\n" \
    "s_mov_b32 s99, 0x20000\n" \
    "s_mov_b32 s84, 0\n" \
    "s_mov_b32 s81, 0\n" \
    "s_mov_b32 s86, 0\n" \
    "s_mov_b32 s87, 0\n" \
    "s_mov_b32 s88, 0\n" \
    "s_mov_b32 s89, 0\n" \
    "s_mov_b32 s90, 0\n" \
    "s_mov_b32 s91, 0\n" \
    "s_mov_b32 s92, 0\n" \
    "s_mov_b32 s93, 0\n" \
    "s_mov_b32 s94, 0\n" \
    "s_mov_b32 s95, 0\n" \
    "s_mov_b32 s96, 0\n" \
    "s_mov_b32 s97, 0\n" \
    "s_mov_b32 s98, 0\n" \
    "ds_read_b32 v216, v229\n" \
    "s_waitcnt lgkmcnt(0)\n" \
    "v_readfirstlane_b32 s94, v216\n" \
    "s_mul_i32 s79, s94, s70\n" \
    "s_add_u32 s79, s79, s81\n" \
    "s_mov_b32 s77, s79\n" \
    "s_mul_i32 s96, s65, s71\n" \
    "s_lshr_b32 s96, s96, 4\n" \
    "v_mov_b32 v96, 0x3f803f80\n" \
    "v_mov_b32 v97, 0x3f803f80\n" \
    "v_mov_b32 v98, 0x3f803f80\n" \
    "v_mov_b32 v99, 0x3f803f80\n" \
    "v_mov_b32 v100, 0x3f803f80\n" \
    "v_mov_b32 v101, 0x3f803f80\n" \
    "v_mov_b64 v[102:103], 0\n" \
    "v_mov_b64 v[64:65], 0\n" \
    "v_mov_b64 v[66:67], 0\n" \
    "v_mov_b64 v[68:69], 0\n" \
    "v_mov_b64 v[70:71], 0\n" \
    "v_mov_b64 v[72:73], 0\n" \
    "v_mov_b64 v[74:75], 0\n" \
    "v_mov_b64 v[76:77], 0\n" \
    "v_mov_b64 v[78:79], 0\n" \
    "v_mov_b64 v[80:81], 0\n" \
    "v_mov_b64 v[82:83], 0\n" \
    "v_mov_b64 v[84:85], 0\n" \
    "v_mov_b64 v[86:87], 0\n" \
    "v_mov_b64 v[88:89], 0\n" \
    "v_mov_b64 v[90:91], 0\n" \
    "v_mov_b64 v[92:93], 0\n" \
    "v_mov_b64 v[94:95], 0\n" \
    "v_mov_b64 v[192:193], 0\n" \
    "v_mov_b64 v[194:195], 0\n" \
    "v_mov_b32 v222, v236\n" \
    "v_mov_b32 v223, 0\n" \
    "ds_read_b64_tr_b16 v[104:105], v228 offset:16384\n" \
    "ds_read_b64_tr_b16 v[106:107], v228 offset:16416\n" \
    "ds_read_b64_tr_b16 v[108:109], v228 offset:18688\n" \
    "ds_read_b64_tr_b16 v[110:111], v228 offset:18720\n" \
    "ds_read_b64_tr_b16 v[112:113], v228 offset:20992\n" \
    "ds_read_b64_tr_b16 v[114:115], v228 offset:21024\n" \
    "ds_read_b64_tr_b16 v[116:117], v228 offset:23296\n" \
    "ds_read_b64_tr_b16 v[118:119], v228 offset:23328\n" \
    "ds_read_b64_tr_b16 v[120:121], v228 offset:16656\n" \
    "ds_read_b64_tr_b16 v[122:123], v228 offset:16688\n" \
    "ds_read_b64_tr_b16 v[124:125], v228 offset:18960\n" \
    "ds_read_b64_tr_b16 v[126:127], v228 offset:18992\n" \
    "ds_read_b64_tr_b16 v[128:129], v228 offset:21264\n" \
    "ds_read_b64_tr_b16 v[130:131], v228 offset:21296\n" \
    "ds_read_b64_tr_b16 v[132:133], v228 offset:23568\n" \
    "ds_read_b64_tr_b16 v[134:135], v228 offset:23600\n" \
    "s_waitcnt lgkmcnt(14)\n" \
    "v_mfma_f32_16x16x32_bf16 v[136:139], v[104:107], v[0:3], 0\n" \
    "v_mfma_f32_16x16x32_bf16 v[144:147], v[104:107], v[8:11], 0\n" \
    "v_mfma_f32_16x16x32_bf16 v[152:155], v[104:107], v[16:19], 0\n" \
    "v_mfma_f32_16x16x32_bf16 v[160:163], v[104:107], v[24:27], 0\n" \
    "s_waitcnt lgkmcnt(12)\n" \
    "v_mfma_f32_16x16x32_bf16 v[136:139], v[108:111], v[4:7], v[136:139]\n" \
    "v_mfma_f32_16x16x32_bf16 v[144:147], v[108:111], v[12:15], v[144:147]\n" \
    "v_mfma_f32_16x16x32_bf16 v[152:155], v[108:111], v[20:23], v[152:155]\n" \
    "v_mfma_f32_16x16x32_bf16 v[160:163], v[108:111], v[28:31], v[160:163]\n" \
    "s_waitcnt lgkmcnt(10)\n" \
    "v_mfma_f32_16x16x32_bf16 v[136:139], v[112:115], v[32:35], v[136:139]\n" \
    "v_mfma_f32_16x16x32_bf16 v[144:147], v[112:115], v[40:43], v[144:147]\n" \
    "v_mfma_f32_16x16x32_bf16 v[152:155], v[112:115], v[48:51], v[152:155]\n" \
    "v_mfma_f32_16x16x32_bf16 v[160:163], v[112:115], v[56:59], v[160:163]\n" \
    "s_waitcnt lgkmcnt(8)\n" \
    "v_mfma_f32_16x16x32_bf16 v[136:139], v[116:119], v[36:39], v[136:139]\n" \
    "v_mfma_f32_16x16x32_bf16 v[144:147], v[116:119], v[44:47], v[144:147]\n" \
    "v_mfma_f32_16x16x32_bf16 v[152:155], v[116:119], v[52:55], v[152:155]\n" \
    "v_mfma_f32_16x16x32_bf16 v[160:163], v[116:119], v[60:63], v[160:163]\n" \
    "s_waitcnt lgkmcnt(6)\n" \
    "v_mfma_f32_16x16x32_bf16 v[140:143], v[120:123], v[0:3], 0\n" \
    "v_mfma_f32_16x16x32_bf16 v[148:151], v[120:123], v[8:11], 0\n" \
    "v_mfma_f32_16x16x32_bf16 v[156:159], v[120:123], v[16:19], 0\n" \
    "v_mfma_f32_16x16x32_bf16 v[164:167], v[120:123], v[24:27], 0\n" \
    "s_waitcnt lgkmcnt(4)\n" \
    "v_mfma_f32_16x16x32_bf16 v[140:143], v[124:127], v[4:7], v[140:143]\n" \
    "v_mfma_f32_16x16x32_bf16 v[148:151], v[124:127], v[12:15], v[148:151]\n" \
    "v_mfma_f32_16x16x32_bf16 v[156:159], v[124:127], v[20:23], v[156:159]\n" \
    "v_mfma_f32_16x16x32_bf16 v[164:167], v[124:127], v[28:31], v[164:167]\n" \
    "s_waitcnt lgkmcnt(2)\n" \
    "v_mfma_f32_16x16x32_bf16 v[140:143], v[128:131], v[32:35], v[140:143]\n" \
    "v_mfma_f32_16x16x32_bf16 v[148:151], v[128:131], v[40:43], v[148:151]\n" \
    "v_mfma_f32_16x16x32_bf16 v[156:159], v[128:131], v[48:51], v[156:159]\n" \
    "v_mfma_f32_16x16x32_bf16 v[164:167], v[128:131], v[56:59], v[164:167]\n" \
    "s_waitcnt lgkmcnt(0)\n" \
    "v_mfma_f32_16x16x32_bf16 v[140:143], v[132:135], v[36:39], v[140:143]\n" \
    "v_mfma_f32_16x16x32_bf16 v[148:151], v[132:135], v[44:47], v[148:151]\n" \
    "v_mfma_f32_16x16x32_bf16 v[156:159], v[132:135], v[52:55], v[156:159]\n" \
    "v_mfma_f32_16x16x32_bf16 v[164:167], v[132:135], v[60:63], v[164:167]\n" \
    "v_cvt_pk_bf16_f32 v65, v136, v137\n" \
    "v_cvt_pk_bf16_f32 v68, v138, v139\n" \
    "v_cvt_pk_bf16_f32 v73, v144, v145\n" \
    "v_cvt_pk_bf16_f32 v76, v146, v147\n" \
    "v_cvt_pk_bf16_f32 v81, v152, v153\n" \
    "v_cvt_pk_bf16_f32 v84, v154, v155\n" \
    "v_cvt_pk_bf16_f32 v89, v160, v161\n" \
    "v_cvt_pk_bf16_f32 v92, v162, v163\n" \
    "v_cvt_pk_bf16_f32 v66, v142, v143\n" \
    "v_cvt_pk_bf16_f32 v67, v142, v143\n" \
    "v_cvt_pk_bf16_f32 v69, v140, v141\n" \
    "v_cvt_pk_bf16_f32 v74, v150, v151\n" \
    "v_cvt_pk_bf16_f32 v75, v150, v151\n" \
    "v_cvt_pk_bf16_f32 v77, v148, v149\n" \
    "v_cvt_pk_bf16_f32 v82, v158, v159\n" \
    "v_cvt_pk_bf16_f32 v83, v158, v159\n" \
    "v_cvt_pk_bf16_f32 v85, v156, v157\n" \
    "v_cvt_pk_bf16_f32 v90, v166, v167\n" \
    "v_cvt_pk_bf16_f32 v91, v166, v167\n" \
    "v_cvt_pk_bf16_f32 v93, v164, v165\n" \
    "v_permlane16_swap_b32 v66, v67\n" \
    "v_permlane16_swap_b32 v74, v75\n" \
    "v_permlane16_swap_b32 v82, v83\n" \
    "v_permlane16_swap_b32 v90, v91\n" \
    "L%=_Rloop:\n" \
    "v_mov_b64 v[176:177], 0\n" \
    "v_mov_b64 v[178:179], 0\n" \
    "v_mov_b64 v[180:181], 0\n" \
    "v_mov_b64 v[182:183], 0\n" \
    "v_mov_b64 v[184:185], 0\n" \
    "v_mov_b64 v[186:187], 0\n" \
    "v_mov_b64 v[188:189], 0\n" \
    "v_mov_b64 v[190:191], 0\n" \
    "v_mov_b64 v[196:197], 0\n" \
    "v_mov_b64 v[198:199], 0\n" \
    "v_mov_b64 v[200:201], 0\n" \
    "v_mov_b64 v[202:203], 0\n" \
    "v_mov_b64 v[204:205], 0\n" \
    "v_mov_b64 v[206:207], 0\n" \
    "v_mov_b64 v[208:209], 0\n" \
    "v_mov_b64 v[210:211], 0\n" \
    "v_mov_b64 v[212:213], 0\n" \
    "v_mov_b64 v[214:215], 0\n" \
    "s_add_u32 s85, s60, 0x1000\n" \
    "s_branch L%=_Ru2b\n" \
    "L%=_Ru2a:\n" \
    "s_sleep 4\n" \
    "L%=_Ru2b:\n" \
    "s_memrealtime vcc\n" \
    "s_add_u32 s80, s77, s64\n" \
    "s_add_u32 s80, s80, s69\n" \
    "s_waitcnt lgkmcnt(0)\n" \
    "s_sub_i32 s79, s80, vcc_lo\n" \
    "s_sub_u32 s79, s79, 40\n" \
    "s_cmp_lt_u32 s79, 0x400\n" \
    "s_cbranch_scc1 L%=_Ru2a\n" \
    "s_memtime s[38:39]\n" \
    "s_memrealtime vcc\n" \
    "s_nop 4\n" \
    "s_memrealtime s[32:33]\n" \
    "s_nop 4\n" \
    "s_memrealtime s[34:35]\n" \
    "s_nop 4\n" \
    "s_memrealtime s[36:37]\n" \
    "s_add_u32 s80, s77, s64\n" \
    "s_lshl_b32 s80, s80, 2\n" \
    "s_waitcnt lgkmcnt(0)\n" \
    "s_sub_u32 s80, s80, vcc_lo\n" \
    "s_sub_u32 s80, s80, s32\n" \
    "s_sub_u32 s80, s80, s34\n" \
    "s_sub_u32 s80, s80, s36\n" \
    "s_mul_i32 s80, s80, s71\n" \
    "s_ashr_i32 s80, s80, 6\n" \
    "s_add_u32 s78, s80, s38\n" \
    "L%=_Ru3a:\n" \
    "s_memtime s[38:39]\n" \
    "s_mul_i32 s80, s69, 23\n" \
    "s_add_u32 s80, s80, s78\n" \
    "s_waitcnt lgkmcnt(0)\n" \
    "s_sub_i32 s79, s80, s38\n" \
    "s_max_i32 s79, s79, 0\n" \
    "s_cmp_gt_u32 s79, 320\n" \
    "s_cbranch_scc1 L%=_Ru3s\n" \
    ".p2align 4\n" \
    "L%=_Ru3d:\n" \
    "s_sub_u32 s79, s79, 28\n" \
    "s_cbranch_scc0 L%=_Ru3d\n" \
    "L%=_Ru3e:\n" \
    "s_sub_u32 s79, s84, 0x20000\n" \
    "s_cselect_b32 s79, 0x40000000, s79\n" \
    "buffer_store_dwordx4 v[236:239], v234, s[48:51], s79 offen sc1\n" \
    "s_setprio 3\n" \
    "s_barrier\n" \
    "s_nop 14\n" \
    "L%=_Rvs:\n" \
    "ds_read_b128 v[104:107], v229 offset:16\n" \
    "ds_read_b128 v[108:111], v230 offset:8704\n" \
    "ds_read_b128 v[112:115], v230 offset:9984\n" \
    "ds_read_b128 v[116:119], v230 offset:11264\n" \
    "ds_read_b128 v[120:123], v230 offset:12544\n" \
    "s_waitcnt lgkmcnt(4)\n" \
    "v_min3_u32 v104, v104, v105, v106\n" \
    "v_min_u32 v104, v104, v107\n" \
    "s_nop 0\n" \
    "v_readfirstlane_b32 s79, v104\n" \
    "s_cmp_le_u32 s79, s76\n" \
    "s_cbranch_scc1 L%=_Rvs\n" \
    "s_waitcnt lgkmcnt(2)\n" \
    "v_pk_add_f32 v[108:109], v[108:109], v[112:113]\n" \
    "v_pk_add_f32 v[110:111], v[110:111], v[114:115]\n" \
    "s_waitcnt lgkmcnt(0)\n" \
    "v_pk_add_f32 v[116:117], v[116:117], v[120:121]\n" \
    "v_pk_add_f32 v[118:119], v[118:119], v[122:123]\n" \
    "v_pk_add_f32 v[108:109], v[108:109], v[116:117]\n" \
    "v_pk_add_f32 v[110:111], v[110:111], v[118:119]\n" \
    "v_cvt_pk_bf16_f32 v124, v108, v109\n" \
    "v_cvt_pk_bf16_f32 v125, v110, v111\n" \
    "s_nop 0\n" \
    "v_mov_b32_dpp v126, v124 quad_perm:[1,0,3,2] row_mask:0xf bank_mask:0xf\n" \
    "v_mov_b32_dpp v127, v125 quad_perm:[1,0,3,2] row_mask:0xf bank_mask:0xf\n" \
    "buffer_store_dwordx4 v[124:127], v231, s[40:43], s60 offen sc1\n" \
    "ds_write_b64 v232, v[124:125]\n" \
    "ds_read_b64_tr_b16 v[236:237], v233\n" \
    "ds_read_b64_tr_b16 v[238:239], v233 offset:256\n" \
    "s_cmp_eq_u32 s76, 63\n" \
    "s_cbranch_scc1 L%=_Rfin\n" \
    "ds_read_b32 v216, v229\n" \
    "s_waitcnt lgkmcnt(0)\n" \
    "v_readfirstlane_b32 s94, v216\n" \
    "s_mul_i32 s79, s94, s70\n" \
    "s_add_u32 s79, s79, s81\n" \
    "s_cmp_lg_u32 s79, s77\n" \
    "s_cbranch_scc1 L%=_Ru4s\n" \
    "L%=_Ru4k:\n" \
    "L%=_Ru5a:\n" \
    "s_memtime s[38:39]\n" \
    "s_mul_hi_u32 s80, s67, s96\n" \
    "s_add_u32 s80, s80, s78\n" \
    "s_waitcnt lgkmcnt(0)\n" \
    "s_sub_i32 s79, s80, s38\n" \
    "s_max_i32 s79, s79, 0\n" \
    "s_cmp_gt_u32 s79, 320\n" \
    "s_cbranch_scc1 L%=_Ru5s\n" \
    ".p2align 4\n" \
    "L%=_Ru5d:\n" \
    "s_sub_u32 s79, s79, 28\n" \
    "s_cbranch_scc0 L%=_Ru5d\n" \
    "L%=_Ru5e:\n" \
    "buffer_load_dwordx4 v[168:171], v225, s[40:43], s60 offen sc1\n" \
    "buffer_load_dwordx4 v[172:175], v225, s[40:43], s85 offen sc1\n" \
    "s_memtime s[38:39]\n" \
    "s_memrealtime vcc\n" \
    "s_nop 4\n" \
    "s_memrealtime s[32:33]\n" \
    "s_nop 4\n" \
    "s_memrealtime s[34:35]\n" \
    "s_nop 4\n" \
    "s_memrealtime s[36:37]\n" \
    "s_add_u32 s80, s77, s64\n" \
    "s_lshl_b32 s80, s80, 2\n" \
    "s_waitcnt lgkmcnt(0)\n" \
    "s_sub_u32 s80, s80, vcc_lo\n" \
    "s_sub_u32 s80, s80, s32\n" \
    "s_sub_u32 s80, s80, s34\n" \
    "s_sub_u32 s80, s80, s36\n" \
    "s_mul_i32 s80, s80, s71\n" \
    "s_ashr_i32 s80, s80, 6\n" \
    "s_add_u32 s78, s80, s38\n" \
    "L%=_Rvc:\n" \
    "s_waitcnt vmcnt(1)\n" \
    "v_smfmac_f32_16x16x64_bf16 v[176:179], v[168:171], v[0:7], v224\n" \
    "v_smfmac_f32_16x16x64_bf16 v[180:183], v[168:171], v[8:15], v224\n" \
    "s_waitcnt vmcnt(0)\n" \
    "v_smfmac_f32_16x16x64_bf16 v[176:179], v[172:175], v[32:39], v224\n" \
    "v_smfmac_f32_16x16x64_bf16 v[180:183], v[172:175], v[40:47], v224\n" \
    "v_smfmac_f32_16x16x64_bf16 v[184:187], v[168:171], v[16:23], v224\n" \
    "v_smfmac_f32_16x16x64_bf16 v[188:191], v[168:171], v[24:31], v224\n" \
    "v_add_f32 v176, v176, v178\n" \
    "v_smfmac_f32_16x16x64_bf16 v[184:187], v[172:175], v[48:55], v224\n" \
    "v_add_f32 v177, v177, v179\n" \
    "v_smfmac_f32_16x16x64_bf16 v[188:191], v[172:175], v[56:63], v224\n" \
    "v_cmp_u_f32 vcc, v176, v177\n" \
    "v_cvt_pk_bf16_f32 v64, v176, v177\n" \
    "v_add_f32 v180, v180, v182\n" \
    "v_add_f32 v181, v181, v183\n" \
    "v_cvt_pk_bf16_f32 v72, v180, v181\n" \
    "s_cbranch_vccnz L%=_Rvslow\n" \
    "L%=_Rvok:\n" \
    "v_add_f32 v184, v184, v186\n" \
    "v_add_f32 v185, v185, v187\n" \
    "v_cvt_pk_bf16_f32 v80, v184, v185\n" \
    "v_add_f32 v188, v188, v190\n" \
    "v_add_f32 v189, v189, v191\n" \
    "v_cvt_pk_bf16_f32 v88, v188, v189\n" \
    "s_barrier\n" \
    "L%=_Rfr:\n" \
    "ds_read_b128 v[192:195], v227\n" \
    "s_waitcnt lgkmcnt(0)\n" \
    "v_smfmac_f32_16x16x64_bf16 v[212:215], v[192:195], v[96:103], v224\n" \
    "v_cmp_ne_u32 vcc, s76, v195\n" \
    "v_smfmac_f32_16x16x64_bf16 v[196:199], v[192:195], v[64:71], v224\n" \
    "s_cbranch_vccnz L%=_Rfs\n" \
    "v_smfmac_f32_16x16x64_bf16 v[200:203], v[192:195], v[72:79], v224\n" \
    "v_smfmac_f32_16x16x64_bf16 v[204:207], v[192:195], v[80:87], v224\n" \
    "v_add_f32 v212, v212, v214\n" \
    "v_smfmac_f32_16x16x64_bf16 v[208:211], v[192:195], v[88:95], v224\n" \
    "v_add_f32 v213, v213, v215\n" \
    "v_rcp_f32 v220, v212\n" \
    "v_rcp_f32 v221, v213\n" \
    "v_add_f32 v196, v196, v198\n" \
    "v_add_f32 v197, v197, v199\n" \
    "v_pk_add_f32 v[200:201], v[200:201], v[202:203]\n" \
    "v_pk_add_f32 v[204:205], v[204:205], v[206:207]\n" \
    "s_nop 0\n" \
    "v_pk_add_f32 v[208:209], v[208:209], v[210:211]\n" \
    "v_pk_mul_f32 v[196:197], v[196:197], v[220:221]\n" \
    "v_pk_mul_f32 v[200:201], v[200:201], v[220:221]\n" \
    "v_pk_mul_f32 v[204:205], v[204:205], v[220:221]\n" \
    "v_pk_mul_f32 v[208:209], v[208:209], v[220:221]\n" \
    "v_cvt_pk_bf16_f32 v216, v196, v200\n" \
    "v_cvt_pk_bf16_f32 v217, v204, v208\n" \
    "v_cvt_pk_bf16_f32 v218, v197, v201\n" \
    "v_cvt_pk_bf16_f32 v219, v205, v209\n" \
    "buffer_store_dwordx4 v[216:219], v226, s[40:43], s60 offen sc1\n" \
    "ds_read_b64_tr_b16 v[104:105], v228 offset:16384\n" \
    "ds_read_b64_tr_b16 v[106:107], v228 offset:16416\n" \
    "ds_read_b64_tr_b16 v[108:109], v228 offset:18688\n" \
    "ds_read_b64_tr_b16 v[110:111], v228 offset:18720\n" \
    "ds_read_b64_tr_b16 v[112:113], v228 offset:20992\n" \
    "ds_read_b64_tr_b16 v[114:115], v228 offset:21024\n" \
    "ds_read_b64_tr_b16 v[116:117], v228 offset:23296\n" \
    "ds_read_b64_tr_b16 v[118:119], v228 offset:23328\n" \
    "ds_read_b64_tr_b16 v[120:121], v228 offset:16656\n" \
    "ds_read_b64_tr_b16 v[122:123], v228 offset:16688\n" \
    "ds_read_b64_tr_b16 v[124:125], v228 offset:18960\n" \
    "ds_read_b64_tr_b16 v[126:127], v228 offset:18992\n" \
    "ds_read_b64_tr_b16 v[128:129], v228 offset:21264\n" \
    "ds_read_b64_tr_b16 v[130:131], v228 offset:21296\n" \
    "ds_read_b64_tr_b16 v[132:133], v228 offset:23568\n" \
    "ds_read_b64_tr_b16 v[134:135], v228 offset:23600\n" \
    "s_waitcnt lgkmcnt(14)\n" \
    "v_mfma_f32_16x16x32_bf16 v[136:139], v[104:107], v[0:3], 0\n" \
    "v_mfma_f32_16x16x32_bf16 v[144:147], v[104:107], v[8:11], 0\n" \
    "v_mfma_f32_16x16x32_bf16 v[152:155], v[104:107], v[16:19], 0\n" \
    "v_mfma_f32_16x16x32_bf16 v[160:163], v[104:107], v[24:27], 0\n" \
    "s_waitcnt lgkmcnt(12)\n" \
    "v_mfma_f32_16x16x32_bf16 v[136:139], v[108:111], v[4:7], v[136:139]\n" \
    "v_mfma_f32_16x16x32_bf16 v[144:147], v[108:111], v[12:15], v[144:147]\n" \
    "v_mfma_f32_16x16x32_bf16 v[152:155], v[108:111], v[20:23], v[152:155]\n" \
    "v_mfma_f32_16x16x32_bf16 v[160:163], v[108:111], v[28:31], v[160:163]\n" \
    "s_waitcnt lgkmcnt(10)\n" \
    "v_mfma_f32_16x16x32_bf16 v[136:139], v[112:115], v[32:35], v[136:139]\n" \
    "v_mfma_f32_16x16x32_bf16 v[144:147], v[112:115], v[40:43], v[144:147]\n" \
    "v_mfma_f32_16x16x32_bf16 v[152:155], v[112:115], v[48:51], v[152:155]\n" \
    "v_mfma_f32_16x16x32_bf16 v[160:163], v[112:115], v[56:59], v[160:163]\n" \
    "s_waitcnt lgkmcnt(8)\n" \
    "v_mfma_f32_16x16x32_bf16 v[136:139], v[116:119], v[36:39], v[136:139]\n" \
    "v_mfma_f32_16x16x32_bf16 v[144:147], v[116:119], v[44:47], v[144:147]\n" \
    "v_mfma_f32_16x16x32_bf16 v[152:155], v[116:119], v[52:55], v[152:155]\n" \
    "v_mfma_f32_16x16x32_bf16 v[160:163], v[116:119], v[60:63], v[160:163]\n" \
    "v_add_u32 v223, 1, v223\n" \
    "s_and_b32 s95, s94, 1\n" \
    "s_lshl_b32 s95, s95, 17\n" \
    "s_add_u32 s95, s95, s61\n" \
    "s_add_u32 s85, s95, 0x1000\n" \
    "s_xor_b32 s85, s95, 0x20000\n" \
    "L%=_Ru6a:\n" \
    "s_memtime s[38:39]\n" \
    "s_mul_hi_u32 s80, s68, s96\n" \
    "s_add_u32 s80, s80, s78\n" \
    "s_waitcnt lgkmcnt(0)\n" \
    "s_sub_i32 s79, s80, s38\n" \
    "s_max_i32 s79, s79, 0\n" \
    "s_cmp_gt_u32 s79, 320\n" \
    "s_cbranch_scc1 L%=_Ru6s\n" \
    ".p2align 4\n" \
    "L%=_Ru6d:\n" \
    "s_sub_u32 s79, s79, 28\n" \
    "s_cbranch_scc0 L%=_Ru6d\n" \
    "L%=_Ru6e:\n" \
    "buffer_load_dwordx4 v[176:179], v235, s[40:43], s60 offen offset:0 sc1\n" \
    "buffer_load_dwordx4 v[180:183], v235, s[40:43], s60 offen offset:1024 sc1\n" \
    "buffer_load_dwordx4 v[184:187], v235, s[40:43], s60 offen offset:2048 sc1\n" \
    "buffer_load_dwordx4 v[188:191], v235, s[40:43], s60 offen offset:3072 sc1\n" \
    "v_mfma_f32_16x16x32_bf16 v[140:143], v[120:123], v[0:3], 0\n" \
    "v_mfma_f32_16x16x32_bf16 v[148:151], v[120:123], v[8:11], 0\n" \
    "v_mfma_f32_16x16x32_bf16 v[156:159], v[120:123], v[16:19], 0\n" \
    "v_mfma_f32_16x16x32_bf16 v[164:167], v[120:123], v[24:27], 0\n" \
    "v_mfma_f32_16x16x32_bf16 v[140:143], v[124:127], v[4:7], v[140:143]\n" \
    "v_mfma_f32_16x16x32_bf16 v[148:151], v[124:127], v[12:15], v[148:151]\n" \
    "v_mfma_f32_16x16x32_bf16 v[156:159], v[124:127], v[20:23], v[156:159]\n" \
    "v_mfma_f32_16x16x32_bf16 v[164:167], v[124:127], v[28:31], v[164:167]\n" \
    "v_mfma_f32_16x16x32_bf16 v[140:143], v[128:131], v[32:35], v[140:143]\n" \
    "v_mfma_f32_16x16x32_bf16 v[148:151], v[128:131], v[40:43], v[148:151]\n" \
    "v_mfma_f32_16x16x32_bf16 v[156:159], v[128:131], v[48:51], v[156:159]\n" \
    "v_mfma_f32_16x16x32_bf16 v[164:167], v[128:131], v[56:59], v[164:167]\n" \
    "v_mfma_f32_16x16x32_bf16 v[140:143], v[132:135], v[36:39], v[140:143]\n" \
    "v_mfma_f32_16x16x32_bf16 v[148:151], v[132:135], v[44:47], v[148:151]\n" \
    "v_mfma_f32_16x16x32_bf16 v[156:159], v[132:135], v[52:55], v[156:159]\n" \
    "v_mfma_f32_16x16x32_bf16 v[164:167], v[132:135], v[60:63], v[164:167]\n" \
    "v_cvt_pk_bf16_f32 v65, v136, v137\n" \
    "v_cvt_pk_bf16_f32 v68, v138, v139\n" \
    "v_cvt_pk_bf16_f32 v73, v144, v145\n" \
    "v_cvt_pk_bf16_f32 v76, v146, v147\n" \
    "v_cvt_pk_bf16_f32 v81, v152, v153\n" \
    "v_cvt_pk_bf16_f32 v84, v154, v155\n" \
    "v_cvt_pk_bf16_f32 v89, v160, v161\n" \
    "v_cvt_pk_bf16_f32 v92, v162, v163\n" \
    "L%=_Rred:\n" \
    "s_waitcnt vmcnt(3)\n" \
    "v_mfma_f32_16x16x32_bf16 v[240:243], v[244:247], v[176:179], 0\n" \
    "s_waitcnt vmcnt(2)\n" \
    "v_mfma_f32_16x16x32_bf16 v[240:243], v[244:247], v[180:183], v[240:243]\n" \
    "s_waitcnt vmcnt(1)\n" \
    "v_mfma_f32_16x16x32_bf16 v[240:243], v[244:247], v[184:187], v[240:243]\n" \
    "s_waitcnt vmcnt(0)\n" \
    "v_mfma_f32_16x16x32_bf16 v[240:243], v[244:247], v[188:191], v[240:243]\n" \
    "s_nop 7\n" \
    "v_cmp_u_f32 vcc, v240, v240\n" \
    "v_add_f32_dpp v240, v240, v240 row_ror:8 row_mask:0xf bank_mask:0xf\n" \
    "v_add_f32_dpp v241, v241, v241 row_ror:8 row_mask:0xf bank_mask:0xf\n" \
    "v_add_f32_dpp v242, v242, v242 row_ror:8 row_mask:0xf bank_mask:0xf\n" \
    "v_add_f32_dpp v243, v243, v243 row_ror:8 row_mask:0xf bank_mask:0xf\n" \
    "v_cvt_pk_bf16_f32 v216, v240, v241\n" \
    "v_cvt_pk_bf16_f32 v217, v242, v243\n" \
    "s_cbranch_vccnz L%=_Rslow\n" \
    "L%=_Rok:\n" \
    "v_mov_b32_dpp v218, v216 quad_perm:[1,0,3,2] row_mask:0xf bank_mask:0xf\n" \
    "v_mov_b32_dpp v219, v217 quad_perm:[1,0,3,2] row_mask:0xf bank_mask:0xf\n" \
    "buffer_store_dwordx4 v[216:219], v222, s[40:43], s95 offen sc1\n" \
    "ds_write_b32 v229, v223 offset:4\n" \
    "buffer_store_dwordx4 v[216:219], v222, s[40:43], s85 offen sc1\n" \
    "s_setprio 0\n" \
    "v_cvt_pk_bf16_f32 v66, v142, v143\n" \
    "v_cvt_pk_bf16_f32 v67, v142, v143\n" \
    "v_cvt_pk_bf16_f32 v69, v140, v141\n" \
    "v_cvt_pk_bf16_f32 v74, v150, v151\n" \
    "v_cvt_pk_bf16_f32 v75, v150, v151\n" \
    "v_cvt_pk_bf16_f32 v77, v148, v149\n" \
    "v_cvt_pk_bf16_f32 v82, v158, v159\n" \
    "v_cvt_pk_bf16_f32 v83, v158, v159\n" \
    "v_cvt_pk_bf16_f32 v85, v156, v157\n" \
    "v_cvt_pk_bf16_f32 v90, v166, v167\n" \
    "v_cvt_pk_bf16_f32 v91, v166, v167\n" \
    "v_cvt_pk_bf16_f32 v93, v164, v165\n" \
    "v_permlane16_swap_b32 v66, v67\n" \
    "v_permlane16_swap_b32 v74, v75\n" \
    "v_permlane16_swap_b32 v82, v83\n" \
    "v_permlane16_swap_b32 v90, v91\n" \
    "ds_read_b64 v[220:221], v229 offset:64\n" \
    "s_mov_b32 s79, s60\n" \
    "s_mov_b32 s60, s61\n" \
    "s_mov_b32 s61, s62\n" \
    "s_mov_b32 s62, s63\n" \
    "s_mov_b32 s63, s79\n" \
    "s_add_u32 s76, s76, 1\n" \
    "s_add_u32 s77, s77, s65\n" \
    "s_add_u32 s84, s84, 0x20000\n" \
    "s_add_u32 s81, s81, s65\n" \
    "s_waitcnt lgkmcnt(0)\n" \
    "v_readfirstlane_b32 s65, v220\n" \
    "v_readfirstlane_b32 s71, v221\n" \
    "s_mul_i32 s96, s65, s71\n" \
    "s_lshr_b32 s96, s96, 4\n" \
    "s_branch L%=_Rloop\n" \
    "L%=_Rslow:\n" \
    "s_nop 7\n" \
    "s_nop 0\n" \
    "v_max_u32 v220, v176, v177\n" \
    "v_max3_u32 v220, v220, v178, v179\n" \
    "v_max3_u32 v220, v220, v180, v181\n" \
    "v_max3_u32 v220, v220, v182, v183\n" \
    "v_max3_u32 v220, v220, v184, v185\n" \
    "v_max3_u32 v220, v220, v186, v187\n" \
    "v_max3_u32 v220, v220, v188, v189\n" \
    "v_max3_u32 v220, v220, v190, v191\n" \
    "v_cmp_eq_u32 vcc, -1, v220\n" \
    "s_cbranch_vccz L%=_Rok\n" \
    "s_cmp_eq_u32 s99, 0\n" \
    "s_cbranch_scc1 L%=_Rok\n" \
    "s_sub_u32 s99, s99, 1\n" \
    "s_cmp_lg_u32 s99, 0\n" \
    "s_cbranch_scc1 L%=_Ru7\n" \
    "v_mov_b32 v220, 1\n" \
    "buffer_store_dword v220, off, s[44:47], 0 sc1\n" \
    "L%=_Ru7:\n" \
    "s_sleep 1\n" \
    "buffer_load_dwordx4 v[176:179], v235, s[40:43], s60 offen offset:0 sc1\n" \
    "buffer_load_dwordx4 v[180:183], v235, s[40:43], s60 offen offset:1024 sc1\n" \
    "buffer_load_dwordx4 v[184:187], v235, s[40:43], s60 offen offset:2048 sc1\n" \
    "buffer_load_dwordx4 v[188:191], v235, s[40:43], s60 offen offset:3072 sc1\n" \
    "s_branch L%=_Rred\n" \
    "L%=_Rfin:\n" \
    "s_waitcnt lgkmcnt(0)\n" \
    "buffer_store_dwordx4 v[236:239], v234, s[48:51], s84 offen sc1\n" \
    "s_waitcnt vmcnt(0)\n" \
    "s_barrier\n" \
    "s_branch L%=_end\n" \
    "L%=_Ru3s:\n" \
    "s_cmp_gt_u32 s79, 0x8000\n" \
    "s_cbranch_scc1 L%=_Ru3e\n" \
    "s_sleep 2\n" \
    "s_branch L%=_Ru3a\n" \
    "L%=_Ru4s:\n" \
    "s_mov_b32 s77, s79\n" \
    "s_memtime s[38:39]\n" \
    "s_memrealtime vcc\n" \
    "s_nop 4\n" \
    "s_memrealtime s[32:33]\n" \
    "s_nop 4\n" \
    "s_memrealtime s[34:35]\n" \
    "s_nop 4\n" \
    "s_memrealtime s[36:37]\n" \
    "s_add_u32 s80, s77, s64\n" \
    "s_lshl_b32 s80, s80, 2\n" \
    "s_waitcnt lgkmcnt(0)\n" \
    "s_sub_u32 s80, s80, vcc_lo\n" \
    "s_sub_u32 s80, s80, s32\n" \
    "s_sub_u32 s80, s80, s34\n" \
    "s_sub_u32 s80, s80, s36\n" \
    "s_mul_i32 s80, s80, s71\n" \
    "s_ashr_i32 s80, s80, 6\n" \
    "s_add_u32 s78, s80, s38\n" \
    "s_branch L%=_Ru4k\n" \
    "L%=_Ru5s:\n" \
    "s_cmp_gt_u32 s79, 0x8000\n" \
    "s_cbranch_scc1 L%=_Ru5e\n" \
    "s_sleep 2\n" \
    "s_branch L%=_Ru5a\n" \
    "L%=_Rvslow:\n" \
    "s_nop 7\n" \
    "s_nop 0\n" \
    "v_max_u32 v216, v168, v169\n" \
    "v_max3_u32 v216, v216, v170, v171\n" \
    "v_max3_u32 v216, v216, v172, v173\n" \
    "v_max3_u32 v216, v216, v174, v175\n" \
    "v_cmp_eq_u32 vcc, -1, v216\n" \
    "s_cbranch_vccz L%=_Rvok\n" \
    "s_cmp_eq_u32 s99, 0\n" \
    "s_cbranch_scc1 L%=_Rvok\n" \
    "s_sub_u32 s99, s99, 1\n" \
    "s_cmp_lg_u32 s99, 0\n" \
    "s_cbranch_scc1 L%=_Ru8\n" \
    "v_mov_b32 v216, 1\n" \
    "buffer_store_dword v216, off, s[44:47], 0 sc1\n" \
    "L%=_Ru8:\n" \
    "s_sleep 1\n" \
    "buffer_load_dwordx4 v[168:171], v225, s[40:43], s60 offen sc1\n" \
    "buffer_load_dwordx4 v[172:175], v225, s[40:43], s85 offen sc1\n" \
    "v_mov_b64 v[176:177], 0\n" \
    "v_mov_b64 v[178:179], 0\n" \
    "v_mov_b64 v[180:181], 0\n" \
    "v_mov_b64 v[182:183], 0\n" \
    "v_mov_b64 v[184:185], 0\n" \
    "v_mov_b64 v[186:187], 0\n" \
    "v_mov_b64 v[188:189], 0\n" \
    "v_mov_b64 v[190:191], 0\n" \
    "s_waitcnt vmcnt(0)\n" \
    "s_branch L%=_Rvc\n" \
    "L%=_Rfs:\n" \
    "s_nop 7\n" \
    "s_nop 0\n" \
    "v_mov_b64 v[196:197], 0\n" \
    "v_mov_b64 v[198:199], 0\n" \
    "v_mov_b64 v[200:201], 0\n" \
    "v_mov_b64 v[202:203], 0\n" \
    "v_mov_b64 v[204:205], 0\n" \
    "v_mov_b64 v[206:207], 0\n" \
    "v_mov_b64 v[208:209], 0\n" \
    "v_mov_b64 v[210:211], 0\n" \
    "v_mov_b64 v[212:213], 0\n" \
    "v_mov_b64 v[214:215], 0\n" \
    "s_branch L%=_Rfr\n" \
    "L%=_Ru6s:\n" \
    "s_cmp_gt_u32 s79, 0x8000\n" \
    "s_cbranch_scc1 L%=_Ru6e\n" \
    "s_sleep 2\n" \
    "s_branch L%=_Ru6a\n" \
    "L%=_P:\n" \
    "s_mov_b32 s76, 0\n" \
    "s_mov_b32 s99, 0x20000\n" \
    "s_mov_b32 s84, 0\n" \
    "s_mov_b32 s81, 0\n" \
    "s_mov_b32 s86, 0\n" \
    "s_mov_b32 s87, 0\n" \
    "s_mov_b32 s88, 0\n" \
    "s_mov_b32 s89, 0\n" \
    "s_mov_b32 s90, 0\n" \
    "s_mov_b32 s91, 0\n" \
    "s_mov_b32 s92, 0\n" \
    "s_mov_b32 s93, 0\n" \
    "s_mov_b32 s94, 0\n" \
    "s_mov_b32 s95, 0\n" \
    "s_mov_b32 s96, 0\n" \
    "s_mov_b32 s97, 0\n" \
    "s_mov_b32 s98, 0\n" \
    "ds_read_b32 v216, v229\n" \
    "s_waitcnt lgkmcnt(0)\n" \
    "v_readfirstlane_b32 s94, v216\n" \
    "s_mul_i32 s79, s94, s70\n" \
    "s_add_u32 s79, s79, s81\n" \
    "s_mov_b32 s77, s79\n" \
    "s_mul_i32 s96, s65, s71\n" \
    "s_lshr_b32 s96, s96, 4\n" \
    "v_mov_b32 v96, 0x3f803f80\n" \
    "v_mov_b32 v97, 0x3f803f80\n" \
    "v_mov_b32 v98, 0x3f803f80\n" \
    "v_mov_b32 v99, 0x3f803f80\n" \
    "v_mov_b32 v100, 0x3f803f80\n" \
    "v_mov_b32 v101, 0x3f803f80\n" \
    "v_mov_b64 v[102:103], 0\n" \
    "v_mov_b64 v[64:65], 0\n" \
    "v_mov_b64 v[66:67], 0\n" \
    "v_mov_b64 v[68:69], 0\n" \
    "v_mov_b64 v[70:71], 0\n" \
    "v_mov_b64 v[72:73], 0\n" \
    "v_mov_b64 v[74:75], 0\n" \
    "v_mov_b64 v[76:77], 0\n" \
    "v_mov_b64 v[78:79], 0\n" \
    "v_mov_b64 v[80:81], 0\n" \
    "v_mov_b64 v[82:83], 0\n" \
    "v_mov_b64 v[84:85], 0\n" \
    "v_mov_b64 v[86:87], 0\n" \
    "v_mov_b64 v[88:89], 0\n" \
    "v_mov_b64 v[90:91], 0\n" \
    "v_mov_b64 v[92:93], 0\n" \
    "v_mov_b64 v[94:95], 0\n" \
    "v_mov_b64 v[192:193], 0\n" \
    "v_mov_b64 v[194:195], 0\n" \
    "v_cvt_f32_u32 v240, s71\n" \
    "v_mov_b32 v241, 1.0\n" \
    "v_mov_b32 v242, 0\n" \
    "v_mov_b32 v243, s65\n" \
    "s_mov_b32 s74, s94\n" \
    "v_mov_b32 v244, -1\n" \
    "v_mov_b32 v245, -1\n" \
    "v_mov_b32 v246, -1\n" \
    "v_mov_b32 v247, -1\n" \
    "L%=_Ploop:\n" \
    "ds_read_b64_tr_b16 v[104:105], v228 offset:16384\n" \
    "ds_read_b64_tr_b16 v[106:107], v228 offset:16416\n" \
    "ds_read_b64_tr_b16 v[108:109], v228 offset:18688\n" \
    "ds_read_b64_tr_b16 v[110:111], v228 offset:18720\n" \
    "ds_read_b64_tr_b16 v[112:113], v228 offset:20992\n" \
    "ds_read_b64_tr_b16 v[114:115], v228 offset:21024\n" \
    "ds_read_b64_tr_b16 v[116:117], v228 offset:23296\n" \
    "ds_read_b64_tr_b16 v[118:119], v228 offset:23328\n" \
    "ds_read_b64_tr_b16 v[120:121], v228 offset:16656\n" \
    "ds_read_b64_tr_b16 v[122:123], v228 offset:16688\n" \
    "ds_read_b64_tr_b16 v[124:125], v228 offset:18960\n" \
    "ds_read_b64_tr_b16 v[126:127], v228 offset:18992\n" \
    "ds_read_b64_tr_b16 v[128:129], v228 offset:21264\n" \
    "ds_read_b64_tr_b16 v[130:131], v228 offset:21296\n" \
    "ds_read_b64_tr_b16 v[132:133], v228 offset:23568\n" \
    "ds_read_b64_tr_b16 v[134:135], v228 offset:23600\n" \
    "s_waitcnt lgkmcnt(14)\n" \
    "v_mfma_f32_16x16x32_bf16 v[136:139], v[104:107], v[0:3], 0\n" \
    "v_mfma_f32_16x16x32_bf16 v[144:147], v[104:107], v[8:11], 0\n" \
    "v_mfma_f32_16x16x32_bf16 v[152:155], v[104:107], v[16:19], 0\n" \
    "v_mfma_f32_16x16x32_bf16 v[160:163], v[104:107], v[24:27], 0\n" \
    "s_waitcnt lgkmcnt(12)\n" \
    "v_mfma_f32_16x16x32_bf16 v[136:139], v[108:111], v[4:7], v[136:139]\n" \
    "v_mfma_f32_16x16x32_bf16 v[144:147], v[108:111], v[12:15], v[144:147]\n" \
    "v_mfma_f32_16x16x32_bf16 v[152:155], v[108:111], v[20:23], v[152:155]\n" \
    "v_mfma_f32_16x16x32_bf16 v[160:163], v[108:111], v[28:31], v[160:163]\n" \
    "s_waitcnt lgkmcnt(10)\n" \
    "v_mfma_f32_16x16x32_bf16 v[136:139], v[112:115], v[32:35], v[136:139]\n" \
    "v_mfma_f32_16x16x32_bf16 v[144:147], v[112:115], v[40:43], v[144:147]\n" \
    "v_mfma_f32_16x16x32_bf16 v[152:155], v[112:115], v[48:51], v[152:155]\n" \
    "v_mfma_f32_16x16x32_bf16 v[160:163], v[112:115], v[56:59], v[160:163]\n" \
    "s_waitcnt lgkmcnt(8)\n" \
    "v_mfma_f32_16x16x32_bf16 v[136:139], v[116:119], v[36:39], v[136:139]\n" \
    "v_mfma_f32_16x16x32_bf16 v[144:147], v[116:119], v[44:47], v[144:147]\n" \
    "v_mfma_f32_16x16x32_bf16 v[152:155], v[116:119], v[52:55], v[152:155]\n" \
    "v_mfma_f32_16x16x32_bf16 v[160:163], v[116:119], v[60:63], v[160:163]\n" \
    "s_waitcnt lgkmcnt(6)\n" \
    "v_mfma_f32_16x16x32_bf16 v[140:143], v[120:123], v[0:3], 0\n" \
    "v_mfma_f32_16x16x32_bf16 v[148:151], v[120:123], v[8:11], 0\n" \
    "v_mfma_f32_16x16x32_bf16 v[156:159], v[120:123], v[16:19], 0\n" \
    "v_mfma_f32_16x16x32_bf16 v[164:167], v[120:123], v[24:27], 0\n" \
    "s_waitcnt lgkmcnt(4)\n" \
    "v_mfma_f32_16x16x32_bf16 v[140:143], v[124:127], v[4:7], v[140:143]\n" \
    "v_mfma_f32_16x16x32_bf16 v[148:151], v[124:127], v[12:15], v[148:151]\n" \
    "v_mfma_f32_16x16x32_bf16 v[156:159], v[124:127], v[20:23], v[156:159]\n" \
    "v_mfma_f32_16x16x32_bf16 v[164:167], v[124:127], v[28:31], v[164:167]\n" \
    "s_waitcnt lgkmcnt(2)\n" \
    "v_mfma_f32_16x16x32_bf16 v[140:143], v[128:131], v[32:35], v[140:143]\n" \
    "v_mfma_f32_16x16x32_bf16 v[148:151], v[128:131], v[40:43], v[148:151]\n" \
    "v_mfma_f32_16x16x32_bf16 v[156:159], v[128:131], v[48:51], v[156:159]\n" \
    "v_mfma_f32_16x16x32_bf16 v[164:167], v[128:131], v[56:59], v[164:167]\n" \
    "s_waitcnt lgkmcnt(0)\n" \
    "v_mfma_f32_16x16x32_bf16 v[140:143], v[132:135], v[36:39], v[140:143]\n" \
    "v_mfma_f32_16x16x32_bf16 v[148:151], v[132:135], v[44:47], v[148:151]\n" \
    "v_mfma_f32_16x16x32_bf16 v[156:159], v[132:135], v[52:55], v[156:159]\n" \
    "v_mfma_f32_16x16x32_bf16 v[164:167], v[132:135], v[60:63], v[164:167]\n" \
    "v_cvt_pk_bf16_f32 v65, v136, v137\n" \
    "v_cvt_pk_bf16_f32 v68, v138, v139\n" \
    "v_cvt_pk_bf16_f32 v73, v144, v145\n" \
    "v_cvt_pk_bf16_f32 v76, v146, v147\n" \
    "v_cvt_pk_bf16_f32 v81, v152, v153\n" \
    "v_cvt_pk_bf16_f32 v84, v154, v155\n" \
    "v_cvt_pk_bf16_f32 v89, v160, v161\n" \
    "v_cvt_pk_bf16_f32 v92, v162, v163\n" \
    "v_cvt_pk_bf16_f32 v66, v142, v143\n" \
    "v_cvt_pk_bf16_f32 v67, v142, v143\n" \
    "v_cvt_pk_bf16_f32 v69, v140, v141\n" \
    "v_cvt_pk_bf16_f32 v74, v150, v151\n" \
    "v_cvt_pk_bf16_f32 v75, v150, v151\n" \
    "v_cvt_pk_bf16_f32 v77, v148, v149\n" \
    "v_cvt_pk_bf16_f32 v82, v158, v159\n" \
    "v_cvt_pk_bf16_f32 v83, v158, v159\n" \
    "v_cvt_pk_bf16_f32 v85, v156, v157\n" \
    "v_cvt_pk_bf16_f32 v90, v166, v167\n" \
    "v_cvt_pk_bf16_f32 v91, v166, v167\n" \
    "v_cvt_pk_bf16_f32 v93, v164, v165\n" \
    "v_permlane16_swap_b32 v66, v67\n" \
    "v_permlane16_swap_b32 v74, v75\n" \
    "v_permlane16_swap_b32 v82, v83\n" \
    "v_permlane16_swap_b32 v90, v91\n" \
    "v_mov_b64 v[176:177], 0\n" \
    "v_mov_b64 v[178:179], 0\n" \
    "v_mov_b64 v[180:181], 0\n" \
    "v_mov_b64 v[182:183], 0\n" \
    "v_mov_b64 v[184:185], 0\n" \
    "v_mov_b64 v[186:187], 0\n" \
    "v_mov_b64 v[188:189], 0\n" \
    "v_mov_b64 v[190:191], 0\n" \
    "v_mov_b64 v[196:197], 0\n" \
    "v_mov_b64 v[198:199], 0\n" \
    "v_mov_b64 v[200:201], 0\n" \
    "v_mov_b64 v[202:203], 0\n" \
    "v_mov_b64 v[204:205], 0\n" \
    "v_mov_b64 v[206:207], 0\n" \
    "v_mov_b64 v[208:209], 0\n" \
    "v_mov_b64 v[210:211], 0\n" \
    "v_mov_b64 v[212:213], 0\n" \
    "v_mov_b64 v[214:215], 0\n" \
    "s_add_u32 s85, s60, 0x1000\n" \
    "s_add_u32 s82, s76, 1\n" \
    "s_min_u32 s82, s82, 63\n" \
    "s_lshl_b32 s83, s82, 19\n" \
    "s_add_u32 s83, s83, s72\n" \
    "s_lshl_b32 s79, s82, 11\n" \
    "s_and_b32 s79, s79, 0x2000\n" \
    "s_xor_b32 s83, s83, s79\n" \
    "v_cmp_ge_u32 vcc, s82, v237\n" \
    "s_cbranch_vccnz L%=_Pfslow\n" \
    "L%=_Pfok:\n" \
    "s_add_u32 s79, s82, 3\n" \
    "v_mov_b32 v150, 0x40000000\n" \
    "v_cmp_lt_u32 vcc, s79, v237\n" \
    "s_nop 1\n" \
    "v_cndmask_b32 v150, v235, v150, vcc\n" \
    "s_branch L%=_Pu2b\n" \
    "L%=_Pu2a:\n" \
    "s_sleep 4\n" \
    "L%=_Pu2b:\n" \
    "s_memrealtime vcc\n" \
    "s_add_u32 s80, s77, s64\n" \
    "s_add_u32 s80, s80, s69\n" \
    "s_waitcnt lgkmcnt(0)\n" \
    "s_sub_i32 s79, s80, vcc_lo\n" \
    "s_sub_u32 s79, s79, 40\n" \
    "s_cmp_lt_u32 s79, 0x400\n" \
    "s_cbranch_scc1 L%=_Pu2a\n" \
    "s_memtime s[38:39]\n" \
    "s_memrealtime vcc\n" \
    "s_nop 4\n" \
    "s_memrealtime s[32:33]\n" \
    "s_nop 4\n" \
    "s_memrealtime s[34:35]\n" \
    "s_nop 4\n" \
    "s_memrealtime s[36:37]\n" \
    "s_add_u32 s80, s77, s64\n" \
    "s_lshl_b32 s80, s80, 2\n" \
    "s_waitcnt lgkmcnt(0)\n" \
    "s_sub_u32 s80, s80, vcc_lo\n" \
    "s_sub_u32 s80, s80, s32\n" \
    "s_sub_u32 s80, s80, s34\n" \
    "s_sub_u32 s80, s80, s36\n" \
    "s_mul_i32 s80, s80, s71\n" \
    "s_ashr_i32 s80, s80, 6\n" \
    "s_add_u32 s78, s80, s38\n" \
    "L%=_Pu3a:\n" \
    "s_memtime s[38:39]\n" \
    "s_mul_i32 s80, s69, 23\n" \
    "s_add_u32 s80, s80, s78\n" \
    "s_waitcnt lgkmcnt(0)\n" \
    "s_sub_i32 s79, s80, s38\n" \
    "s_max_i32 s79, s79, 0\n" \
    "s_cmp_gt_u32 s79, 320\n" \
    "s_cbranch_scc1 L%=_Pu3s\n" \
    ".p2align 4\n" \
    "L%=_Pu3d:\n" \
    "s_sub_u32 s79, s79, 28\n" \
    "s_cbranch_scc0 L%=_Pu3d\n" \
    "L%=_Pu3e:\n" \
    "buffer_load_dwordx4 v[104:107], v230, s[56:59], s83 offen offset:0 sc1\n" \
    "buffer_load_dwordx4 v[108:111], v230, s[56:59], s83 offen offset:1024 sc1\n" \
    "buffer_load_dwordx4 v[112:115], v230, s[56:59], s83 offen offset:2048 sc1\n" \
    "buffer_load_dwordx4 v[116:119], v230, s[56:59], s83 offen offset:3072 sc1\n" \
    "s_add_u32 s79, s83, 0x1000\n" \
    "buffer_load_dwordx4 v[120:123], v230, s[56:59], s79 offen offset:0 sc1\n" \
    "buffer_load_dwordx4 v[124:127], v230, s[56:59], s79 offen offset:1024 sc1\n" \
    "buffer_load_dwordx4 v[128:131], v230, s[56:59], s79 offen offset:2048 sc1\n" \
    "buffer_load_dwordx4 v[132:135], v230, s[56:59], s79 offen offset:3072 sc1\n" \
    "s_xor_b32 s79, s83, s75\n" \
    "buffer_load_dwordx4 v[136:139], v230, s[52:55], s79 offen offset:0 sc1\n" \
    "buffer_load_dwordx4 v[140:143], v230, s[52:55], s79 offen offset:1024 sc1\n" \
    "buffer_load_dword v151, v150, s[44:47], 0 offen sc1\n" \
    "s_lshl_b32 s79, s76, 7\n" \
    "buffer_load_dwordx4 v[144:147], off, s[44:47], s79 offset:2176 sc1\n" \
    "buffer_store_dword v243, v239, s[44:47], s79 offen sc1\n" \
    "buffer_store_dwordx4 v[244:247], v232, s[40:43], s62 offen offset:0 sc1\n" \
    "buffer_store_dwordx4 v[244:247], v232, s[40:43], s62 offen offset:1024 sc1\n" \
    "buffer_store_dwordx4 v[244:247], v232, s[40:43], s62 offen offset:2048 sc1\n" \
    "buffer_store_dwordx4 v[244:247], v232, s[40:43], s62 offen offset:3072 sc1\n" \
    "buffer_store_dwordx4 v[244:247], v233, s[40:43], s62 offen offset:0 sc1\n" \
    "buffer_store_dwordx4 v[244:247], v233, s[40:43], s62 offen offset:1024 sc1\n" \
    "buffer_store_dwordx4 v[244:247], v234, s[40:43], s62 offen sc1\n" \
    "s_barrier\n" \
    "s_cmp_eq_u32 s76, 63\n" \
    "s_cbranch_scc1 L%=_Pfin\n" \
    "ds_read_b32 v216, v229\n" \
    "s_waitcnt lgkmcnt(0)\n" \
    "v_readfirstlane_b32 s94, v216\n" \
    "s_mul_i32 s79, s94, s70\n" \
    "s_add_u32 s79, s79, s81\n" \
    "s_cmp_lg_u32 s79, s77\n" \
    "s_cbranch_scc1 L%=_Pu4s\n" \
    "L%=_Pu4k:\n" \
    "s_sub_u32 s32, s78, s97\n" \
    "s_sub_u32 s33, s77, s98\n" \
    "s_mov_b32 s97, s78\n" \
    "s_mov_b32 s98, s77\n" \
    "s_lshl_b32 s32, s32, 4\n" \
    "s_cmp_eq_u32 s76, 0\n" \
    "s_cbranch_scc1 L%=_Pck\n" \
    "v_cvt_f32_u32 v148, s33\n" \
    "v_cvt_f32_u32 v149, s32\n" \
    "v_rcp_f32 v148, v148\n" \
    "s_cmp_lt_u32 s76, 4\n" \
    "s_cselect_b32 s34, 0, 0x3c449ba6\n" \
    "s_cmp_eq_u32 s94, s74\n" \
    "s_mov_b32 s74, s94\n" \
    "s_cselect_b32 s32, 0x7f000000, 0\n" \
    "s_cselect_b32 s33, 0.5, 1.0\n" \
    "s_cselect_b32 s34, 0, s34\n" \
    "v_fma_f32 v148, v148, v149, -v240\n" \
    "v_max_f32 v149, 0.5, v241\n" \
    "v_min_f32 v148, s32, v148\n" \
    "v_cmp_gt_f32 vcc, 0, v148\n" \
    "v_mul_f32 v242, 0x3f7851ec, v242\n" \
    "v_add_f32 v242, s34, v242\n" \
    "v_cndmask_b32 v149, v241, v149, vcc\n" \
    "v_fma_f32 v240, v148, v149, v240\n" \
    "v_mul_f32 v241, s33, v241\n" \
    "v_max_f32 v240, 0x43400000, v240\n" \
    "v_max_f32 v241, 0x3e000000, v241\n" \
    "v_min_f32 v240, 0x43e00000, v240\n" \
    "v_fma_f32 v148, v242, s73, s73\n" \
    "v_rcp_f32 v149, v240\n" \
    "s_nop 0\n" \
    "v_fma_f32 v148, v148, v149, 0.5\n" \
    "v_cvt_u32_f32 v243, v148\n" \
    "L%=_Pck:\n" \
    "v_add_f32 v149, 0.5, v240\n" \
    "v_mov_b32 v148, s65\n" \
    "v_cvt_u32_f32 v149, v149\n" \
    "s_waitcnt vmcnt(8)\n" \
    "ds_write_b128 v231, v[104:107] offset:16384\n" \
    "ds_write_b128 v231, v[108:111] offset:17536\n" \
    "ds_write_b128 v231, v[112:115] offset:18688\n" \
    "ds_write_b128 v231, v[116:119] offset:19840\n" \
    "ds_write_b128 v231, v[120:123] offset:20992\n" \
    "ds_write_b128 v231, v[124:127] offset:22144\n" \
    "ds_write_b128 v231, v[128:131] offset:23296\n" \
    "ds_write_b128 v231, v[132:135] offset:24448\n" \
    "ds_write_b128 v230, v[136:139] offset:25600\n" \
    "ds_write_b128 v230, v[140:143] offset:26624\n" \
    "v_max3_u32 v144, v144, v145, v146\n" \
    "v_max_u32 v144, v144, v147\n" \
    "v_cmp_eq_u32 vcc, 0, v144\n" \
    "s_nop 1\n" \
    "v_cndmask_b32 v148, v144, v148, vcc\n" \
    "ds_write_b64 v229, v[148:149] offset:64\n" \
    "v_max_u32 v237, v237, v151\n" \
    "s_setprio 3\n" \
    "L%=_Pu5a:\n" \
    "s_memtime s[38:39]\n" \
    "s_mul_hi_u32 s80, s67, s96\n" \
    "s_add_u32 s80, s80, s78\n" \
    "s_waitcnt lgkmcnt(0)\n" \
    "s_sub_i32 s79, s80, s38\n" \
    "s_max_i32 s79, s79, 0\n" \
    "s_cmp_gt_u32 s79, 320\n" \
    "s_cbranch_scc1 L%=_Pu5s\n" \
    ".p2align 4\n" \
    "L%=_Pu5d:\n" \
    "s_sub_u32 s79, s79, 28\n" \
    "s_cbranch_scc0 L%=_Pu5d\n" \
    "L%=_Pu5e:\n" \
    "buffer_load_dwordx4 v[168:171], v225, s[40:43], s60 offen sc1\n" \
    "buffer_load_dwordx4 v[172:175], v225, s[40:43], s85 offen sc1\n" \
    "L%=_Pvc:\n" \
    "s_waitcnt vmcnt(1)\n" \
    "v_smfmac_f32_16x16x64_bf16 v[176:179], v[168:171], v[0:7], v224\n" \
    "v_smfmac_f32_16x16x64_bf16 v[180:183], v[168:171], v[8:15], v224\n" \
    "s_waitcnt vmcnt(0)\n" \
    "v_smfmac_f32_16x16x64_bf16 v[176:179], v[172:175], v[32:39], v224\n" \
    "v_smfmac_f32_16x16x64_bf16 v[180:183], v[172:175], v[40:47], v224\n" \
    "v_smfmac_f32_16x16x64_bf16 v[184:187], v[168:171], v[16:23], v224\n" \
    "v_smfmac_f32_16x16x64_bf16 v[188:191], v[168:171], v[24:31], v224\n" \
    "v_add_f32 v176, v176, v178\n" \
    "v_smfmac_f32_16x16x64_bf16 v[184:187], v[172:175], v[48:55], v224\n" \
    "v_add_f32 v177, v177, v179\n" \
    "v_smfmac_f32_16x16x64_bf16 v[188:191], v[172:175], v[56:63], v224\n" \
    "v_cmp_u_f32 vcc, v176, v177\n" \
    "v_cvt_pk_bf16_f32 v64, v176, v177\n" \
    "v_add_f32 v180, v180, v182\n" \
    "v_add_f32 v181, v181, v183\n" \
    "v_cvt_pk_bf16_f32 v72, v180, v181\n" \
    "s_cbranch_vccnz L%=_Pvslow\n" \
    "L%=_Pvok:\n" \
    "v_add_f32 v184, v184, v186\n" \
    "v_add_f32 v185, v185, v187\n" \
    "v_cvt_pk_bf16_f32 v80, v184, v185\n" \
    "v_add_f32 v188, v188, v190\n" \
    "v_add_f32 v189, v189, v191\n" \
    "v_cvt_pk_bf16_f32 v88, v188, v189\n" \
    "s_barrier\n" \
    "L%=_Pfr:\n" \
    "ds_read_b128 v[192:195], v227\n" \
    "s_waitcnt lgkmcnt(0)\n" \
    "v_smfmac_f32_16x16x64_bf16 v[212:215], v[192:195], v[96:103], v224\n" \
    "v_cmp_ne_u32 vcc, s76, v195\n" \
    "v_smfmac_f32_16x16x64_bf16 v[196:199], v[192:195], v[64:71], v224\n" \
    "s_cbranch_vccnz L%=_Pfs\n" \
    "v_smfmac_f32_16x16x64_bf16 v[200:203], v[192:195], v[72:79], v224\n" \
    "v_smfmac_f32_16x16x64_bf16 v[204:207], v[192:195], v[80:87], v224\n" \
    "v_add_f32 v212, v212, v214\n" \
    "v_smfmac_f32_16x16x64_bf16 v[208:211], v[192:195], v[88:95], v224\n" \
    "v_add_f32 v213, v213, v215\n" \
    "v_rcp_f32 v220, v212\n" \
    "v_rcp_f32 v221, v213\n" \
    "v_add_f32 v196, v196, v198\n" \
    "v_add_f32 v197, v197, v199\n" \
    "v_pk_add_f32 v[200:201], v[200:201], v[202:203]\n" \
    "v_pk_add_f32 v[204:205], v[204:205], v[206:207]\n" \
    "s_nop 0\n" \
    "v_pk_add_f32 v[208:209], v[208:209], v[210:211]\n" \
    "v_pk_mul_f32 v[196:197], v[196:197], v[220:221]\n" \
    "v_pk_mul_f32 v[200:201], v[200:201], v[220:221]\n" \
    "v_pk_mul_f32 v[204:205], v[204:205], v[220:221]\n" \
    "v_pk_mul_f32 v[208:209], v[208:209], v[220:221]\n" \
    "v_cvt_pk_bf16_f32 v216, v196, v200\n" \
    "v_cvt_pk_bf16_f32 v217, v204, v208\n" \
    "v_cvt_pk_bf16_f32 v218, v197, v201\n" \
    "v_cvt_pk_bf16_f32 v219, v205, v209\n" \
    "buffer_store_dwordx4 v[216:219], v226, s[40:43], s60 offen sc1\n" \
    "s_setprio 0\n" \
    "ds_read_b64 v[220:221], v229 offset:64\n" \
    "s_mov_b32 s79, s60\n" \
    "s_mov_b32 s60, s61\n" \
    "s_mov_b32 s61, s62\n" \
    "s_mov_b32 s62, s63\n" \
    "s_mov_b32 s63, s79\n" \
    "s_add_u32 s76, s76, 1\n" \
    "s_add_u32 s77, s77, s65\n" \
    "s_add_u32 s84, s84, 0x20000\n" \
    "s_add_u32 s81, s81, s65\n" \
    "s_waitcnt lgkmcnt(0)\n" \
    "v_readfirstlane_b32 s65, v220\n" \
    "v_readfirstlane_b32 s71, v221\n" \
    "s_mul_i32 s96, s65, s71\n" \
    "s_lshr_b32 s96, s96, 4\n" \
    "s_branch L%=_Ploop\n" \
    "L%=_Pfslow:\n" \
    "s_sleep 4\n" \
    "buffer_load_dword v237, v235, s[44:47], 0 offen sc1\n" \
    "s_waitcnt vmcnt(0)\n" \
    "v_cmp_ge_u32 vcc, s82, v237\n" \
    "s_cbranch_vccz L%=_Pfok\n" \
    "s_cmp_eq_u32 s99, 0\n" \
    "s_cbranch_scc1 L%=_Pfok\n" \
    "s_sub_u32 s99, s99, 1\n" \
    "s_branch L%=_Pfslow\n" \
    "L%=_Pfin:\n" \
    "s_waitcnt vmcnt(0)\n" \
    "s_barrier\n" \
    "s_branch L%=_end\n" \
    "L%=_Pu3s:\n" \
    "s_cmp_gt_u32 s79, 0x8000\n" \
    "s_cbranch_scc1 L%=_Pu3e\n" \
    "s_sleep 2\n" \
    "s_branch L%=_Pu3a\n" \
    "L%=_Pu4s:\n" \
    "s_mov_b32 s77, s79\n" \
    "s_memtime s[38:39]\n" \
    "s_memrealtime vcc\n" \
    "s_nop 4\n" \
    "s_memrealtime s[32:33]\n" \
    "s_nop 4\n" \
    "s_memrealtime s[34:35]\n" \
    "s_nop 4\n" \
    "s_memrealtime s[36:37]\n" \
    "s_add_u32 s80, s77, s64\n" \
    "s_lshl_b32 s80, s80, 2\n" \
    "s_waitcnt lgkmcnt(0)\n" \
    "s_sub_u32 s80, s80, vcc_lo\n" \
    "s_sub_u32 s80, s80, s32\n" \
    "s_sub_u32 s80, s80, s34\n" \
    "s_sub_u32 s80, s80, s36\n" \
    "s_mul_i32 s80, s80, s71\n" \
    "s_ashr_i32 s80, s80, 6\n" \
    "s_add_u32 s78, s80, s38\n" \
    "s_branch L%=_Pu4k\n" \
    "L%=_Pu5s:\n" \
    "s_cmp_gt_u32 s79, 0x8000\n" \
    "s_cbranch_scc1 L%=_Pu5e\n" \
    "s_sleep 2\n" \
    "s_branch L%=_Pu5a\n" \
    "L%=_Pvslow:\n" \
    "s_nop 7\n" \
    "s_nop 0\n" \
    "v_max_u32 v216, v168, v169\n" \
    "v_max3_u32 v216, v216, v170, v171\n" \
    "v_max3_u32 v216, v216, v172, v173\n" \
    "v_max3_u32 v216, v216, v174, v175\n" \
    "v_cmp_eq_u32 vcc, -1, v216\n" \
    "s_cbranch_vccz L%=_Pvok\n" \
    "s_cmp_eq_u32 s99, 0\n" \
    "s_cbranch_scc1 L%=_Pvok\n" \
    "s_sub_u32 s99, s99, 1\n" \
    "s_cmp_lg_u32 s99, 0\n" \
    "s_cbranch_scc1 L%=_Pu6\n" \
    "v_mov_b32 v216, 1\n" \
    "buffer_store_dword v216, off, s[44:47], 0 sc1\n" \
    "L%=_Pu6:\n" \
    "s_sleep 1\n" \
    "buffer_load_dwordx4 v[168:171], v225, s[40:43], s60 offen sc1\n" \
    "buffer_load_dwordx4 v[172:175], v225, s[40:43], s85 offen sc1\n" \
    "v_mov_b64 v[176:177], 0\n" \
    "v_mov_b64 v[178:179], 0\n" \
    "v_mov_b64 v[180:181], 0\n" \
    "v_mov_b64 v[182:183], 0\n" \
    "v_mov_b64 v[184:185], 0\n" \
    "v_mov_b64 v[186:187], 0\n" \
    "v_mov_b64 v[188:189], 0\n" \
    "v_mov_b64 v[190:191], 0\n" \
    "s_waitcnt vmcnt(0)\n" \
    "s_branch L%=_Pvc\n" \
    "L%=_Pfs:\n" \
    "s_nop 7\n" \
    "s_nop 0\n" \
    "v_mov_b64 v[196:197], 0\n" \
    "v_mov_b64 v[198:199], 0\n" \
    "v_mov_b64 v[200:201], 0\n" \
    "v_mov_b64 v[202:203], 0\n" \
    "v_mov_b64 v[204:205], 0\n" \
    "v_mov_b64 v[206:207], 0\n" \
    "v_mov_b64 v[208:209], 0\n" \
    "v_mov_b64 v[210:211], 0\n" \
    "v_mov_b64 v[212:213], 0\n" \
    "v_mov_b64 v[214:215], 0\n" \
    "s_branch L%=_Pfr\n" \
    "L%=_end:\n"
#define V8(b) "v" #b "0", "v" #b "1", "v" #b "2", "v" #b "3", "v" #b "4", "v" #b "5", "v" #b "6", "v" #b "7", "v" #b "8", "v" #b "9"
#define S_IO "+{s[40:55]}"(sA), "+{s[56:71]}"(sB), "+{s[72:75]}"(sC), "+{s78}"(lwo), "={s[92:99]}"(so)
#define FIN2() do {} while (0)
#define S_CLOB "memory", "vcc", "scc", "m0", "s32", "s33", "s34", "s35", "s36", "s37", "s38", "s39", "s76", "s77", "s79", "s80", "s81", "s82", "s83", "s84", "s85", "s86", "s87", "s88", "s89", "s90", "s91"
#define B_CLOB S_CLOB, "v64", "v65", "v66", "v67", "v68", "v69", V8(7), V8(8), V8(9), V8(10), V8(11), V8(12), V8(13), V8(14), V8(15), V8(16), V8(17), V8(18), V8(19), V8(20), V8(21), \
    "v220", "v221", "v222", "v223", "v240", "v241", "v242", "v243"
#define FIN() do {} while (0)

#define MK_S() \
    a = args(); \
    v16u sA = cat8(cat4(mkrs(a->ring, NSLOT * SLOT), mkrs(a->ctl, CTL_SZ)), cat4(mkrs(a->mb, NSEG * NH * 4096), mkrs(a->qkv + QSZ, QSZ))); \
    v16u sB = cat8(cat4(mkrs(a->qkv + 2 * (size_t)QSZ, QSZ), v4u{0, 0, 0, 0}), \
                   v8u{0, (unsigned)a->per0, (unsigned)a->d1, (unsigned)a->dv, (unsigned)a->d2, (unsigned)a->dp, (unsigned)a->slack, (unsigned)a->thr}); \
    const v4u rsc = __builtin_shufflevector(sA, sA, 4, 5, 6, 7); \
    unsigned b0, b1
#define HALVES() do { const unsigned u_ = beacon(rsc, b0, b1, 16); \
_Pragma("unroll") \
    for (int k = 0; k < NSLOT; ++k) sB[4 + k] = k * (unsigned)SLOT + ((u_ >> k) & 1) * 8192u; } while (0)

#define START() __hip_atomic_fetch_max((LAS unsigned*)(lds + L_MISC), (rt32() - spare) / sB[14] + 1, __ATOMIC_RELAXED, __HIP_MEMORY_SCOPE_WORKGROUP)

DEV void chain(LAS char* lds, const int c) {
    KArgs a = args();
    const int w = __builtin_amdgcn_readfirstlane(threadIdx.x >> 6), h = c >> 2, j = c & 3;
    const unsigned lb = (unsigned)(size_t)lds, lbw = lb + L_STG + w * 16384;
#define LANE() int l = threadIdx.x & 63; asm volatile("" : "+v"(l)); const int l15 = l & 15, g = l >> 4, hb = (l >> 1) & 1, q4 = l15 >> 2; const unsigned idx = hb ? 0xEEEE : 0x4444
    v4u sC, stv; v2u stw; unsigned stx; v8u so;
    unsigned lwo = w & 3;
    if (w < 4) {
        LANE();
        v16u wt[12], m;
            const v4u rw[3] = {mkrs(a->Wk, DM * HD * 2), mkrs(a->Wv, DM * HD * 2), mkrs(a->Wq, DM * HD * 2)};
            const unsigned vo = (l >> 2) * (HD * 2) + (l & 3) * 16, sw = (256 * w * HD + h * DH + 32 * j) * 2;
            auto issue = [&](int p) {
#pragma unroll
                for (int i = 0; i < 8; ++i) DMA16N(rw[p >> 1], vo, sw + ((p & 1) * 128 + i * 16) * (HD * 2), lbw + (p & 1) * 8192 + i * 1024); };
            {
                const v4u rp = w & 1 ? rw[2] : rw[1];
                const unsigned ps = ((rp[0] >> 7) + (c >> 5) * 16) & 63, po = w < 2 ? ((3 - ps) & 7) * 128 + (l & 1) * 1024 : (63 - ps < 16 && !(l & 1) ? (63 - ps) * 128 : OOB);
                if (a->pf) asm volatile("s_mov_b32 m0, %0\n\ts_nop 0\n\tbuffer_load_dword %1, %2, 0 offen lds" :: "s"(lb + L_PF + w * 256), "v"(((c & 31) * 32 + (l >> 1)) * (HD * 2) + (c >> 5) * 2048 + po), "s"(rp) : "memory", "m0");
            }
            unsigned mr[16];
#pragma unroll
            for (int k = 0; k < 16; ++k) mr[k] = *(const unsigned*)(a->mem0 + (2 * q4 + (l & 1)) * DM + 256 * w + 16 * k + 4 * g + 2 * hb);
            SCHED();
            issue(0); issue(1);
            RBAR();
            MK_S();
            unsigned spare = a->spare; asm volatile("" : "+s"(sA), "+s"(sB), "+s"(spare));
            BC_LD(b0, rsc);
            const LAS char* pw = lds + L_STG + w * 16384 + (4 * (g >> 1) + (g & 1) + 2 * (q4 >> 1) + 16 * (q4 & 1)) * 64 + (l & 3) * 8;
#pragma unroll
            for (int p = 0; p < 6; ++p) {
                if (p < 5) WAITV(8); else WAITV(0);
#pragma unroll
                for (int k = 0; k < 2; ++k) wt[(2 * p + k + 4) % 12] = cat8(rd_w<2048, 512, 2560>(pw + (p & 1) * 8192 + k * 4096), rd_w<2048, 512, 2560>(pw + (p & 1) * 8192 + k * 4096 + 32));
                WAITL(0);
                if (p < 4) issue(p + 2);
                if (p == 3) BC_LD(b1, rsc);
            }
#pragma unroll
            for (int k = 0; k < 16; k += 4) {
                m[k] = __builtin_amdgcn_perm(mr[k + 1], mr[k], 0x05040100u); m[k + 1] = __builtin_amdgcn_perm(mr[k + 3], mr[k + 2], 0x05040100u);
                m[k + 2] = __builtin_amdgcn_perm(mr[k + 1], mr[k], 0x07060302u); m[k + 3] = __builtin_amdgcn_perm(mr[k + 3], mr[k + 2], 0x07060302u);
            }
        const unsigned pq = nearo(H3_OFF + (c >> 5) * 16384) + w * 1024 + l * 16;
        const v8u va = {idx, pq, 0, 0, lb + L_MISC + w * 4, lb + L_MISC, 0, pq + 16384};
        sC = v4u{lb + w * 2176u, lb + w * 1280u, 0, 0};
        HALVES();
        START();
        asm volatile(ASM_A : S_IO, "+{v[192:207]}"(m)
                     : "{v[0:31]}"(cat16(wt[0], wt[1])), "{v[32:63]}"(cat16(wt[2], wt[3])), "{v[64:95]}"(cat16(wt[4], wt[5])), "{v[96:127]}"(cat16(wt[6], wt[7])),
                       "{v[128:159]}"(cat16(wt[8], wt[9])), "{v[160:191]}"(cat16(wt[10], wt[11])), "{v[232:239]}"(va)
                     : S_CLOB, "v208", "v209", V8(21), V8(22), "v230", "v231", "v240", "v241", "v244", "v245", "v246", "v247");
        FIN();
    } else {
        LANE();
        const int bw = w - 4, l7 = l & 7;
        MK_S();
        if (w == 6 && l < 16) ((LAS unsigned*)(lds + L_MISC))[l] = l ? 0 : rt32() / sB[14];
        BAR();
        BC_LD(b0, rsc);
        {
            const v4u rw = mkrs(a->Wo, HD * DM * 2);
            const unsigned vo = (l >> 3) * (DM * 2) + l7 * 16, sw = (h * DH * DM + 256 * j + 64 * bw) * 2;
#pragma unroll
            for (int i = 0; i < 16; ++i) DMA16N(rw, vo, sw + i * 8 * (DM * 2), lbw + i * 1024);
        }
        BC_LD(b1, rsc);
        WAITV(0);
        HALVES();
        const LAS char* pw = lds + L_STG + w * 16384 + (16 * (g & 1) + 8 * (g >> 1) + (q4 & 1) + 4 * (q4 >> 1)) * 128 + (l & 3) * 8;
#define RDO(o) rd_w<256, 4096, 4352>(pw + (o))
        const v32u wo0 = cat16(cat8(RDO(0), RDO(32)), cat8(RDO(64), RDO(96))), wo1 = cat16(cat8(RDO(8192), RDO(8224)), cat8(RDO(8256), RDO(8288)));
        const unsigned c3 = ((c >> 3) & 3) * 4096 + (c >> 5) * 1024 + (c & 7) * 128, h1 = nearo(H1_OFF + h * 8192), h1j = h1 + j * 2048;
        v16u va = {};
        va[0] = idx; va[1] = h1 + (g >> 1) * 2048 + 1280 + (l & 31) * 16;
        va[2] = nearo(H2_OFF + (32 * j + 8 * bw + q4 * 2 + (g >> 1)) * 4096) + h * 128 + ((g & 1) * 4 + (l & 3)) * 16;
        va[3] = lb + L_P + g * 320 + l15 * 16;
        va[4] = lb + (l & 1) * X_BLK + (l & 2) * X_FQ + (8 * (g >> 1) + (q4 & 1) + 4 * (q4 >> 1)) * 16 + (g & 1) * 8;
        va[5] = lb + L_MISC;
        v4u sel = {};
        if (w == 4) {
            const int tok = l & 7, u = l >> 5, z = (l >> 3) & 3;
            va[6] = h1 + (z == 0 ? 8 + u : (z == 1 ? 2 * u : (z == 2 ? 4 + 2 * u : 2 * u + 1))) * 128 + tok * 16;
            va[7] = h1 + (5 + 2 * u) * 128 + tok * 16 + (z & 1) * 8;
            va[8] = lb + L_P + ((z & 2) + u) * 320 + (4 * (tok >> 1) + 2 * (z & 1) + (tok & 1)) * 16;
            va[9] = l == 0 ? F_PG + c * 128 : OOB;
            *(LAS v4u*)(lds + va[8]) = v4u{POI, POI, POI, POI};
            sC = v4u{__float_as_uint(SCALE * LOG2E), __float_as_uint(SCALE * LOG2E), 0, 0};
        } else if (w == 5) {
            va[6] = lb + L_PQK + (l15 >> 3) * 256 + (l & 1) * 544 + (l7 >> 1) * 64 + g * 16;
            va[7] = (l & 8) ? OOB : h1j + g * 128 + l7 * 16;
            va[8] = ((l & 8) || g < 2) ? OOB : h1j + 1024 + ((g & 1) * 8 + l7) * 16;
            va[9] = (l & 8) ? h * 4096 + j * 512 + (g * 8 + l7) * 16 : OOB;
            va[10] = lb + l * 16;
            sC = v4u{0, 0, 0, 0};
        } else if (w == 6) {
            const int rho = (l >> 1) & 15, tok = 2 * (rho >> 2) + (rho & 1), c4 = 2 * (l >> 5) + ((rho >> 1) & 1);
            va[6] = lb + ((l & 1) * 160 + (tok & 1) * 72 + (tok >> 1) * 16 + c4 * 4) * 4;
            va[7] = (l & 1) ? OOB : h1j + 1280 + (l >> 1) * 16;
            va[8] = lb + L_VT + tok * 64 + (l & 1) * 32 + c4 * 8;
            va[9] = lb + L_VT + q4 * 64 + (g & 1) * 32 + (l & 3) * 8;
            va[10] = l < 32 ? h * 4096 + 2048 + (32 * j + l) * 16 : OOB;
            va[11] = nearo(H2_OFF + c * 4096) + l * 16;
            va[12] = (l & 1) ? OOB : nearo(H3_OFF + (2 * (g >> 1) + (l15 >> 3)) * 16384 + c3) + ((l7 & 6) + (g & 1)) * 16;
#pragma unroll
            for (int i = 0; i < 4; ++i) sel[i] = (l7 == 2 * i ? 0x3f80u : 0u) | (l7 == 2 * i + 1 ? 0x3f800000u : 0u);
            sC = v4u{0, 0, 0, 0};
        } else {
            const rsrc_t rk = mkrsrc(a->qkv + QSZ, QSZ), rv = mkrsrc(a->qkv + 2 * (size_t)QSZ, QSZ);
            wait_flags(mkrsrc(a->ctl, CTL_SZ), fw(l & 1, h) + 12, 1, a->ctl);
            v4u pf[10];
            const unsigned hb0 = h * 16384 + (KVS(a->ms >> 4, h) ^ sB[4] ^ 8192), kx = j * 2048 + (KVS(a->ms >> 4, h) ^ KVS(a->ms >> 2, h));
#pragma unroll
            for (int i = 0; i < 8; ++i) pf[i] = LDB(rv, i, hb0);
            pf[8] = LDB(rk, 0, hb0 ^ kx); pf[9] = LDB(rk, 1, hb0 ^ kx);
            va[13] = LD4(mkrsrc(a->ctl, CTL_SZ), fw(l & 1, h) + 12, 0);
#pragma unroll
            for (int i = 0; i < 8; ++i) *(LAS v4u*)(lds + L_X + i * X_BLK + g * X_FQ + l15 * 16) = pf[i];
            *(LAS v4u*)(lds + L_K + l * 16) = pf[8]; *(LAS v4u*)(lds + L_K + 1024 + l * 16) = pf[9];
            FENCE(); ((volatile LAS unsigned*)(lds + L_MISC))[2] = 1;
            va[6] = l * 16; va[7] = lb + g * X_FQ + l15 * 16;
            va[8] = nearo(H2_OFF + c * 4096) + l * 16; va[9] = h1j + l * 16;
            va[10] = nearo(H3_OFF + (l >> 3) * 16384 + c3) + l7 * 16;
            va[11] = fw(l & 1, h) + 12;
            va[15] = l == 0 && (c & 31) == 0 ? F_CK + 384 + (c >> 5) * 4 : OOB;
            sC = v4u{hb0, __float_as_uint(a->per), 0, kx};
        }
        while (((volatile LAS unsigned*)(lds + L_MISC))[2] == 0) __builtin_amdgcn_s_sleep(1);
        asm volatile(ASM_B : S_IO, "+{v[224:239]}"(va), "+{v[244:247]}"(sel) : "{v[0:31]}"(wo0), "{v[32:63]}"(wo1) : B_CLOB);
        FIN();
    }
}

#define RING_LD "buffer_load_dwordx4 %5, %10, %11, %12 offen offset:%13 sc1\n\t"
#define RING_M(C, W, Z) "v_mfma_f32_16x16x32_bf16 %" #C ", %" #W ", %4, " Z "\n\t"
#define RING_W(C, O) "ds_write_b128 %14, %" #C " offset:" #O "\n\t"
#define RING_OP(K, T) asm volatile(T : "+v"(c[0]), "+v"(c[1]), "+v"(c[2]), "+v"(c[3]), "+v"(ring[(K) & 15]), "+v"(ring[((K) + 15) & 15]) \
    : "v"(wo[(K) & 7][0]), "v"(wo[(K) & 7][1]), "v"(wo[(K) & 7][2]), "v"(wo[(K) & 7][3]), "v"(l16), "s"(rso), "s"(nb[(((K) + 15) >> 2) & 3]), "n"((((K) + 15) & 3) * 1024), "v"(rb) : "memory")
#define RING_1(K, B, O) "buffer_load_dwordx4 %" #K ", %16, %17, %" #B " offen offset:" #O " sc1\n\t"
#define RING_4(K0, K1, K2, K3, B) RING_1(K0, B, 0) RING_1(K1, B, 1024) RING_1(K2, B, 2048) RING_1(K3, B, 3072)
#define RING_ALL(P) P(ring[0]), P(ring[1]), P(ring[2]), P(ring[3]), P(ring[4]), P(ring[5]), P(ring[6]), P(ring[7]), P(ring[8]), P(ring[9]), P(ring[10]), P(ring[11]), P(ring[12]), P(ring[13]), P(ring[14]), P(ring[15])
#define NPROBE 3
static constexpr int RED_B = 65536;
#define OS(k) do {} while (0)

DEV void load_wo(LAS char* lds, v4u rw, unsigned o, int w, int l, v4u (&wo)[8][4]) {
    const unsigned vo = (l >> 3) * (DM * 2) + (((l & 7) ^ ((l >> 3) & 6)) << 4), lb = (unsigned)(size_t)lds + w * 16384;
    auto issue = [&](int p) {
#pragma unroll
        for (int k = 0; k < 8; ++k) DMA16N(rw, vo, o + (64 * p + 8 * k) * (DM * 2), lb + (p & 1) * 8192 + k * 1024); };
    issue(0); issue(1);
    const int row = l >> 2;
#pragma unroll
    for (int p = 0; p < 4; ++p) {
        if (p < 3) WAITV(8); else WAITV(0);
#pragma unroll
        for (int t = 0; t < 4; ++t) {
            const LAS char* q = lds + w * 16384 + (p & 1) * 8192 + row * 128 + (((2 * t + ((l >> 1) & 1)) ^ (row & 6)) << 4) + (l & 1) * 8;
            wo[2 * p][t] = cat(trd(q), trd(q + 2048)); wo[2 * p + 1][t] = cat(trd(q + 4096), trd(q + 6144));
        }
        WAITL(0);
        if (p < 2) issue(p + 2);
    }
}

DEV void worker(LAS char* lds, const int wk) {
    KArgs a = args();
    const int tid = threadIdx.x, w = __builtin_amdgcn_readfirstlane(tid >> 6), l = tid & 63, fr = l & 15, fq = l >> 4;
    const int g = wk >> 5, i = wk & 31;
    const rsrc_t rc = mkrsrc(a->ctl, CTL_SZ);
    unsigned* err = a->ctl;
    unsigned* dbg = a->dbg + 32768 + wk * 32;
    WSTAMP(0);
    const v4u rx = mkrs(a->x, S_TOK * DM * 2);
    const int role = i >> 3;
    const int h = 8 * g + (i & 7);
    const unsigned me = F_W + wk * 128;
    unsigned far = 0;
    v4u rsc = mkrs(a->ctl, CTL_SZ);
    if (role >= 2) {
        const unsigned t0 = rt32();
        const rsrc_t rr = mkrsrc(a->ring, NSLOT * SLOT);
        unsigned b0, ak = 1;
        BC_LD(b0, rsc);
        if (role == 2) {
#pragma unroll
            for (int k = 0; k < 14; ++k) ST16(rr, (h >> 4) * (unsigned)SLOT + ((h & 15) * 14 + k) * 8192 + tid * 16, 0, (v4u{POI, POI, POI, POI}));
        } else if (wk == 127) {
#pragma unroll
            for (int k = 0; k < 8; ++k) ST16(rc, k == 0 && (tid >> 3) == F_BC / 128 ? OOB : k * 8192 + tid * 16, 0, (v4u{0, 0, 0, 0}));
        } else if (i < 28 && w == 0) {
            unsigned t[2] = {~0u, ~0u};
            for (int it = 0; it < NPROBE; ++it) {
#pragma unroll
                for (int p = 0; p < 2; ++p) { const unsigned c0 = cyc32(); ST4(rr, (i & 3) * (unsigned)SLOT + p * 8192 + nearo(PR_OFF) + g * 256 + (l & 31) * 4, 0, c0); WAITV(0); t[p] = min(t[p], cyc32() - c0); }
            }
            ak = 256 + min(max((int)(t[1] - t[0]), -100), 100);
        }
        asm volatile("s_waitcnt vmcnt(0)\n\ts_barrier" : "+v"(b0) :: "memory");
        WSTAMP(1);
        if (w == 0) {
            if (wk != 127) beacon(rsc, b0, b0, 0);
            ST4(rc, l ? OOB : (wk == 127 ? F_BC : me), 0, ak);
            if (wk == 127) WAITV(0);
        }
        RBAR();
        const unsigned d = role == 2 ? a->wq : (wk == 127 ? 0 : a->wx);
        while (rt32() - t0 < d) __builtin_amdgcn_s_sleep(8);
        if (role == 3) prep_rows(a->x, a->gw, a->w_r, rc, h * 64 + w * 8, l);
        if (w == 0 && wk == 127) {
            unsigned bc = 0;
            for (unsigned tries = 0; tries < SPIN_CAP; ++tries) {
                bc += 32; ST4(rc, l ? OOB : F_BC, 0, bc);
                FENCE();
                if (!(bc & 16)) {
                    unsigned v = LD4(rc, F_W + ((l >> 4) * 32 + 16 + fr) * 128, 0);
                    if (__builtin_amdgcn_ballot_w64(v == 0) << 1 == 0) {
                        v += __shfl_xor((int)v, 16); v += __shfl_xor((int)v, 32);
                        bc |= 16 | ((unsigned)(__builtin_amdgcn_ballot_w64(v > 1024) >> 8) & 15);
                        continue;
                    }
                } else if (rt32() - t0 > 2400 && all_ge(LD4(rc, F_PG, 0), 1)) break;
                __builtin_amdgcn_s_sleep(16);
            }
            WSTAMP(5);
        }
        if (role == 3 && h >= NMOE) while (rt32() - t0 < 2600) __builtin_amdgcn_s_sleep(64);
    }
    if (role == 1) far = stream_qkv<true>(lds, rx, mkrs(a->Wv, DM * HD * 2), mkrsrc(a->qkv + 2 * (size_t)QSZ, QSZ), rc, rsc, true, me + 12, h, KVS(a->ms >> 4, h), dbg);
    else if (role < 3) far = stream_qkv<false>(lds, rx, mkrs(role ? a->Wq : a->Wk, DM * HD * 2), mkrsrc(a->qkv + (role ? 0 : QSZ), QSZ), rc, rsc, role == 0, me + 12, h, role ? 0 : KVS(a->ms >> 2, h), dbg);
    else if (h < NMOE) {
        const int sel = top8((LAS unsigned*)(lds + L_TOT - 256), rc, err, w, l);
        WSTAMP(26);
        if (h == 0 && tid < TOPC) ST4(mkrsrc(a->idx, 32), tid * 4, 0, sel);
        moe(lds, rx, sel, mkrs(a->wg, DM * FF * 2), mkrs(a->wu, DM * FF * 2), mkrs(a->wd, FF * DM * 2), mkrsrc(a->part, NMOE * TOPC * DM * 4), h);
        if (tid == 0) ST4(rc, me + 4, 0, 1u);
    }
    if (role == 3) far ^= FAR(beacon(rsc, 0, 0, 16));
    WSTAMP(2);
    a = args();
    const rsrc_t ri = mkrsrc(a->idx, 32), ru = mkrsrc(a->upd, TOPC * DM * 4), rt = mkrsrc(a->wt, HD * DM * 2);
    const int cg = role == 3 ? 4 * g + (i & 3) : (role == 2 ? h & 15 : i);
    const unsigned wb = side((cg * 64 + 8 * w) * 8192 + far);
    v4u wo[8][4];
    if (role == 3 && i < 28) {
        const rsrc_t rq = mkrsrc(a->qkv, QSZ), rk = mkrsrc(a->qkv + QSZ, QSZ), rv = mkrsrc(a->qkv + 2 * (size_t)QSZ, QSZ), rm = mkrsrc(a->mb, NSEG * NH * 4096);
        unsigned fl = 0;
#pragma unroll 1
        for (int xs = i & 3; xs < NSEG; xs += 4) {
            if ((xs & 60) == a->wi) {
                int l = tid & 63; asm volatile("" : "+v"(l));
                load_wo(lds, mkrs(a->Wo, HD * DM * 2), (256 * w * DM + 64 * cg) * 2, w, l, wo);
#pragma unroll
                for (int k = 0; k < 32; ++k) ST16(rt, wb + l * 16 + (k >> 3) * 16384 + (k & 7) * 1024, 0, wo[k >> 2][k & 3]);
            }
            fl = xattn(rq, rk, rv, rm, rc, err, xs, 8 * g + w, l, fl, far, a->ms, dbg); WSTAMP(9 + (xs >> 2));
        }
        WSTAMP(25);
        return;
    }
    const bool rcv = role == 2 || (role < 2 && g);
    const int k0 = (rcv ? 0 : 64) + 8 * w;
    const u64 mine = a->segs[role < 2 ? g : (role == 2 ? 4 + (g >> 1) : 6)];
    auto next = [&](int s) { return s < NSEG - 1 && mine >> (s + 1) ? s + 1 + __builtin_ctzll(mine >> (s + 1)) : NSEG; };
    const int seg0 = next(-1);
    auto fx = [&](int s, int l) { return fw(3, (rcv ? 0 : 16) + (l & 1) * 8 + (s & 3)) + 64 + (s & 60); };
    const unsigned fe = F_EX + cg * 256;
    volatile LAS unsigned* prog = (volatile LAS unsigned*)(lds + 2 * RED_B);
    LAS v4f* upd = (LAS v4f*)(lds + 2 * RED_B + 16384);
    u64 hit = 0;
    int sel = 0;
    if (rcv) {
        wait_flags(rc, fw(3, 2 * (cg & 12) + (cg & 3)) + (l & 1 ? 64 + a->wi : 512 + 8), 1, err);
        sel = LD4(ri, (l & 7) * 4, 0);
        const v4u u = LD16(ru, w < 2 ? (tid >> 4) * 4096 + cg * 256 + fr * 16 : OOB, 0);
#pragma unroll
        for (int k = 0; k < 32; ++k) wo[k >> 2][k & 3] = LDB(rt, k & 7, wb + (k >> 3) * 16384);
        if (w < 2) upd[tid] = __builtin_bit_cast(v4f, u);
#pragma unroll
        for (int q = 0; q < 8; ++q) hit |= 1ull << (__builtin_amdgcn_readlane(sel, q) >> 5);
    } else load_wo(lds, mkrs(a->Wo, HD * DM * 2), (32 * k0 * DM + 64 * cg) * 2, w, l, wo);
    WSTAMP(7);
    if (role == 3) {
        wait_flags<16>(rc, fw(3, min(l, NMOE - 1)) + 4, 1, err);
        const rsrc_t rp = mkrsrc(a->part, NMOE * TOPC * DM * 4);
        const unsigned t = LD4(ri, (tid & 112) >> 2, 0), o = (tid & 112) * 256 + cg * 256 + fr * 16;
        v4f p = {0, 0, 0, 0};
#pragma unroll
        for (int q = 0; q < 7; ++q) p += __builtin_bit_cast(v4f, LD16(rp, o, ((w >> 1) * 7 + q) * 32768));
        const float r = rbf(sigmoidf_(hi16(LD4(rc, F_GR + t * 8 + 4, 0))));
        upd[tid] = p;
        BAR();
        p += upd[tid ^ 128]; p += upd[tid ^ 256] + upd[tid ^ 384];
        ST16(ru, w < 2 ? o : OOB, 0, __builtin_bit_cast(v4u, (v4f{rbf(rbf(p[0]) * r), rbf(rbf(p[1]) * r), rbf(rbf(p[2]) * r), rbf(rbf(p[3]) * r)})));
        WAITV(0); RBAR();
        if (tid == 0) ST4(rc, me + 8, 0, 1u);
    }
    prog[2048 + tid * 4] = LD4(rc, l < 2 ? fx(next(seg0), l) : (rcv ? fe + seg0 * 4 : OOB), 0);
    WAITV(0);
    bool pend = false;
    const unsigned ws = a->ws;
    v4u ring[16];
    asm volatile("" : RING_ALL("=v"));
    const v4u rso = mkrs(a->qkv, QSZ), rse = mkrs(a->qkv + QSZ, QSZ);
    rsc = mkrs(a->ctl, CTL_SZ);
    const rsrc_t re = mkrsrc(a->qkv + QSZ, QSZ), rout = mkrsrc(a->out, S_TOK * DM * 2);
    BAR();
    const unsigned l16 = l * 16, lw = (unsigned)(size_t)lds + w * 1024, r0 = (w >> 2) * 16 + fr;
    const unsigned xo = rcv ? (r0 * DM + 64 * cg + (w & 3) * 16 + 4 * fq) * 2 : OOB, go = F_GR + r0 * 8, so = w ? OOB : (l < 2 ? fx(0, l) : (rcv ? fe : OOB));
    auto state = [&](int n) { return (unsigned)__builtin_amdgcn_ballot_w64(prog[(n & 1) * 2048 + l * 4] != 0); };
    auto exo = [&](int s) { return side((unsigned)(s * NH + 16 + cg) * 16384u + far) + w * 1024; };
    unsigned st = state(1);
    WSTAMP(8);
    auto fin = [&](int n, int seg, bool got, v4f pv, v2u xv, float gt) {
        const LAS v4f* rb = (const LAS v4f*)(lds + ((n & 1) * RED_B + w * 8192 + l16));
        v4f acc = add4(add4(add4(rb[0], rb[64]), add4(rb[128], rb[192])), add4(add4(rb[256], rb[320]), add4(rb[384], rb[448])));
        if (rcv) {
            if (!got) { wait_flags(rc, fe + seg * 4, 1, err); pv = __builtin_bit_cast(v4f, LD16(re, l16, exo(seg))); asm volatile("" : "+v"(pv)); }
            v4f b = {lo16(xv[0]), hi16(xv[0]), lo16(xv[1]), hi16(xv[1])};
            if ((hit >> seg) & 1) {
                for (int q = 0; q < TOPC; ++q) if (__builtin_amdgcn_readlane(sel, q) == seg * 32 + (int)(go - F_GR) / 8) {
                    b += upd[q * 16 + (w & 3) * 4 + (l16 >> 8)]; b = v4f{rbf(b[0]), rbf(b[1]), rbf(b[2]), rbf(b[3])}; }
            }
            acc = add4(acc, pv);
            gt = hi16(__float_as_uint(gt));
            const float omg = rbf(1.0f - gt);
#pragma unroll
            for (int e = 0; e < 4; ++e) acc[e] = add1(rbf(gt * rbf(acc[e])), rbf(omg * b[e]));
            __builtin_amdgcn_raw_buffer_store_b64(v2u{pk2(acc[0], acc[1]), pk2(acc[2], acc[3])}, rout, xo, seg * (32 * DM * 2), 0);
        } else ST16(re, l16, exo(seg), __builtin_bit_cast(v4u, acc));
        FENCE(); };
    unsigned sm; v2u xv; float gt; v4f pv;
    asm volatile("" : "=v"(sm), "=v"(pv), "=v"(xv), "=v"(gt));
    bool got = false;
#define SM_LD "buffer_load_dword %[sm], %[so], %[rc], 0 offen sc1\n\tbuffer_load_dwordx4 %[pv], %[l16], %[re], %[eo] offen sc1\n\tbuffer_load_dwordx2 %[xv], %[xo], %[rx], %[xs] offen\n\tbuffer_load_dword %[gt], %[go], %[rc], %[gs] offen sc1\n\t"
#define SM_O [sm] "+v"(sm), [pv] "+v"(pv), [xv] "+v"(xv), [gt] "+v"(gt)
#define SM_I(QO, QE) [rc] "s"(rsc), [re] "s"(rse), [rx] "s"(rx), [so] "v"(so + (l16 < 32 ? ((QO) < NSEG ? ((QO) & 3) * 128 + ((QO) & 60) : OOB) : (QE) * 4)), \
    [l16] "v"(l16), [eo] "s"(got ? exo(seg) : OOB), [xo] "v"(xo), [xs] "s"(seg * (32 * DM * 2)), [go] "v"(go), [gs] "s"(seg * 256)
#pragma unroll 1
    for (int n = 0, seg = seg0, p1 = OOB / 4, p2 = p1, p3 = p1; seg < NSEG; ++n, p3 = p2, p2 = p1, p1 = seg, seg = next(seg)) {
        const int s1 = next(seg), s2 = next(s1);
        const bool pre = (st & 3) == 3;
        if (!pend) {
            const unsigned ob = side((unsigned)(seg * 128 + k0) * 4096u + far);
            got = st & 4;
            wait_flags(rc, fx(seg, l16 >> 4), 1, err);
            asm volatile(RING_4(0, 1, 2, 3, 18) RING_4(4, 5, 6, 7, 19) RING_4(8, 9, 10, 11, 20) RING_4(12, 13, 14, 15, 21)
                         : RING_ALL("+v") : "v"(l16), "s"(rso), "s"(ob), "s"(ob ^ 16384), "s"(ob ^ 4096), "s"(ob ^ 20480) : "memory");
            asm volatile(SM_LD : SM_O : SM_I(s2, pre ? seg : s1) : "memory");
            ST4(rc, OOB, 0, 0u);
        }
        unsigned nb[4], rb = lw + (n & 1) * RED_B + l16, pa = lw + 2 * RED_B + (n & 1) * 8192 + l16;
        nb[0] = pre && !(ws & 4) ? side((unsigned)(s1 * 128 + k0) * 4096u + far) : OOB; nb[1] = nb[0] ^ 16384; nb[2] = nb[0] ^ 4096; nb[3] = nb[0] ^ 20480;
        v4f c[4];
        asm volatile("" : "+s"(nb[0]), "+s"(nb[1]), "+s"(nb[2]), "+s"(nb[3])); asm volatile("" : "+v"(rb), "+v"(pa), "=v"(c[0]), "=v"(c[1]), "=v"(c[2]), "=v"(c[3]));
        OS(0);
        if (pend || (pre && w >= 4)) BAR();
        OS(1);
        if (ws & 1) __builtin_amdgcn_s_setprio(1); else __builtin_amdgcn_s_setprio(0);
        RING_OP(0, "s_waitcnt vmcnt(20)\n\t" RING_M(0, 6, "0") RING_M(1, 7, "0") "s_waitcnt vmcnt(19)\n\t" RING_M(2, 8, "0") RING_M(3, 9, "0"));
#pragma unroll
        for (int k = 1; k < 16; ++k) {
            if (k == 8) RING_OP(k, RING_LD "s_nop 3\n\t" RING_W(0, 0) RING_M(0, 6, "0") RING_W(1, 8192) RING_M(1, 7, "0") RING_W(2, 16384) RING_M(2, 8, "0") RING_W(3, 24576) RING_M(3, 9, "0") "s_waitcnt vmcnt(19)");
            else if (k == 15) RING_OP(k, RING_M(0, 6, "%0") RING_LD RING_M(1, 7, "%1") RING_M(2, 8, "%2") RING_M(3, 9, "%3"));
            else RING_OP(k, RING_M(0, 6, "%0") RING_LD RING_M(1, 7, "%1") "s_waitcnt vmcnt(19)\n\t" RING_M(2, 8, "%2") RING_M(3, 9, "%3"));
            if (k == 13) asm volatile("s_waitcnt vmcnt(14)\n\tds_write_b32 %4, %0" : SM_O : "v"(pa) : "memory");
        }
        RING_OP(16, RING_LD);
        if (ws & 2) __builtin_amdgcn_s_setprio(1); else __builtin_amdgcn_s_setprio(0);
        OS(2);
        v4f pv1; v2u xv1; float gt1;
        const bool got1 = got;
        if (pre || pend) {
            BAR();
            OS(3);
            st = state(n);
            if (!rcv && tid == 256) ST4(rc, fe + p3 * 4, 0, 1u);
            got = st & 4;
            pv1 = pv; xv1 = xv; gt1 = gt;
            asm volatile(SM_LD : SM_O, "+v"(pv1), "+v"(xv1), "+v"(gt1) : SM_I(next(s2), s1) : "memory");
        }
        OS(4);
        asm volatile("s_nop 5\n\tds_write_b128 %4, %0 offset:32768\n\tds_write_b128 %4, %1 offset:40960\n\tds_write_b128 %4, %2 offset:49152\n\tds_write_b128 %4, %3 offset:57344"
                     :: "v"(c[0]), "v"(c[1]), "v"(c[2]), "v"(c[3]), "v"(rb) : "memory");
        OS(5);
        if (pend) fin(n - 1, p1, got1, pv1, xv1, gt1); else ST4(rc, OOB, 0, 0u);
        OS(6);
        if (!pre) {
            BAR();
            if (pend && w < 4) RBAR();
            asm volatile("s_waitcnt vmcnt(1)" : SM_O :: "memory");
            fin(n, seg, got, pv, xv, gt);
            if (!rcv) { WAITV(0); RBAR(); if (tid < 3) ST4(rc, fe + (tid ? (tid == 1 ? p1 : p2) : seg) * 4, 0, 1u); }
            st = pend ? st & 3 : state(n);
        }
        pend = pre;
        WSTAMP(9 + (seg >> 2));
    }
    asm volatile("s_waitcnt vmcnt(0)" :: RING_ALL("v") : "memory");
    WSTAMP(25);
}

__global__ void __launch_bounds__(512, 2) k_main(Args) {
    __shared__ __attribute__((aligned(16))) char lds[L_TOT];
    const int b = blockIdx.x, c = (b & 3) * 32 + (b >> 3);
    if ((b & 7) < 4) chain((LAS char*)lds, c); else worker((LAS char*)lds, c);
}

#define RING_MB 160
#define TUNE_DEFAULT {184, 73, 77, 138, 8, 80, 336, 100, 700, 700, 1, 20, 2, 9, 38, 20}

extern "C" void kernel_launch(void* const* d_in, const int* in_sizes, int n_in, void* d_out, int out_size, void* d_ws, size_t ws_size, hipStream_t stream) {
    char* ring = (char*)(((size_t)d_ws + SLOT - 1) & ~(size_t)(SLOT - 1));
    char* ws = ring + NSLOT * SLOT; size_t off = 0;
    if (ws_size >= ((size_t)RING_MB + 12 << 20)) ring += (size_t)RING_MB << 20;
    auto alloc = [&](size_t bytes) { void* p = ws + off; off += (bytes + 255) & ~(size_t)255; return p; };
    Args a;
    a.x = (const bf16_t*)d_in[0]; a.mem0 = (const bf16_t*)d_in[1]; a.Wq = (const bf16_t*)d_in[2]; a.Wk = (const bf16_t*)d_in[3]; a.Wv = (const bf16_t*)d_in[4]; a.Wo = (const bf16_t*)d_in[5];
    a.w_r = (const bf16_t*)d_in[8]; a.wg = (const bf16_t*)d_in[9]; a.wu = (const bf16_t*)d_in[10]; a.wd = (const bf16_t*)d_in[11]; a.gw = (const bf16_t*)d_in[12];
    a.qkv = (char*)alloc(3 * (size_t)QSZ); a.wt = (char*)alloc(HD * DM * 2); a.mb = (char*)alloc(NSEG * NH * 4096);
    a.idx = (int*)alloc(256); a.part = (float*)alloc(NMOE * TOPC * DM * 4); a.upd = (float*)alloc(TOPC * DM * 4); a.out = (bf16_t*)d_out;
    a.ring = ring; a.ctl = (unsigned*)alloc(CTL_SZ); a.dbg = (unsigned*)alloc((65536 + 1024 * 512) * 4);
    const int tn[16] = TUNE_DEFAULT;
    a.per = tn[0] * 368.0f; a.per0 = tn[0] * 368 / tn[6]; a.pf = tn[10]; a.ms = tn[11]; a.ws = tn[12]; a.wi = tn[15]; a.gap = 0;
    a.d1 = ((u64)tn[1] << 32) / tn[0]; a.dv = ((u64)tn[2] << 32) / tn[0]; a.d2 = ((u64)tn[3] << 32) / tn[0]; a.dp = tn[4]; a.slack = tn[5]; a.thr = tn[6]; a.spare = tn[7]; a.wq = tn[8]; a.wx = tn[9];
    for (int c = 0; c < 7; ++c) a.segs[c] = 0;
    for (int s = 0; s < NSEG; ++s) {
        a.segs[1 + (s < tn[13] ? s % 3 : (s - tn[13]) % 5)] |= 1ull << s;
        a.segs[s >= tn[14] && !((s - tn[14]) & 1) ? 0 : 6] |= 1ull << s;
    }
    k_main<<<256, 512, 0, stream>>>(a);
}
```

```cpp
#include <hip/hip_runtime.h>
#include <stdint.h>

typedef unsigned short bf16_t;
typedef unsigned v2u __attribute__((ext_vector_type(2)));
typedef unsigned v4u __attribute__((ext_vector_type(4)));
typedef unsigned v8u __attribute__((ext_vector_type(8)));
typedef float v2f __attribute__((ext_vector_type(2)));
typedef float v4f __attribute__((ext_vector_type(4)));
typedef short v4s __attribute__((ext_vector_type(4)));
typedef __bf16 v2b __attribute__((ext_vector_type(2)));
typedef __bf16 v8b __attribute__((ext_vector_type(8)));
typedef __bf16 v16b __attribute__((ext_vector_type(16)));
typedef unsigned long long u64;
typedef __amdgpu_buffer_rsrc_t rsrc_t;
#define LAS __attribute__((address_space(3)))
#define DEV __device__ __forceinline__

static constexpr int S_TOK = 2048, DM = 1024, NH = 32, DH = 128, SEG = 32, NSEG = 64;
static constexpr int HD = NH * DH;
static constexpr int FF = 3584, TOPC = 8;
static constexpr float SCALE = 0.08838834764831845f, LOG2E = 1.4426950408889634f, MASKV = -998244352.0f;

DEV unsigned pk2(float lo, float hi) { v2f f = {lo, hi}; return __builtin_bit_cast(unsigned, __builtin_convertvector(f, v2b)); }
DEV float lo16(unsigned u) { return __uint_as_float(u << 16); }
DEV float hi16(unsigned u) { return __uint_as_float(u & 0xffff0000u); }
DEV float rbf(float f) { return __uint_as_float(pk2(0.0f, f)); }
DEV v4f smf(v4u a, v8u b, v4f c, int idx) { return __builtin_amdgcn_smfmac_f32_16x16x64_bf16(__builtin_bit_cast(v8b, a), __builtin_bit_cast(v16b, b), c, idx, 0, 0); }
DEV v4f mf(v4u a, v4u b, v4f c) { return __builtin_amdgcn_mfma_f32_16x16x32_bf16(__builtin_bit_cast(v8b, a), __builtin_bit_cast(v8b, b), c, 0, 0, 0); }
DEV v2u trd(const LAS char* p) { return __builtin_bit_cast(v2u, __builtin_amdgcn_ds_read_tr16_b64_v4i16((LAS v4s*)p)); }
DEV v4u cat(v2u a, v2u b) { return __builtin_shufflevector(a, b, 0, 1, 2, 3); }
DEV v2u sw16(unsigned x, unsigned y) { return __builtin_amdgcn_permlane16_swap(x, y, false, false); }
DEV v2u sw32(unsigned x, unsigned y) { return __builtin_amdgcn_permlane32_swap(x, y, false, false); }
DEV float half_add(float x, float y) { v2u r = sw32(__float_as_uint(x), __float_as_uint(y)); return __uint_as_float(r[0]) + __uint_as_float(r[1]); }
DEV float ror8(float v) { return __int_as_float(__builtin_amdgcn_update_dpp(0, __float_as_int(v), 0x128, 0xf, 0xf, false)); }
template <bool MAX> DEV float red_g(float v) {
    auto op = [](float a, float b) { return MAX ? fmaxf(a, b) : a + b; };
    v2u r = sw16(__float_as_uint(v), __float_as_uint(v)); v = op(__uint_as_float(r[0]), __uint_as_float(r[1]));
    r = sw32(__float_as_uint(v), __float_as_uint(v)); return op(__uint_as_float(r[0]), __uint_as_float(r[1]));
}
DEV float add1(float a, float b) { float r; asm("v_add_f32 %0, %1, %2" : "=v"(r) : "v"(a), "v"(b)); return r; }
DEV v4f add4(v4f a, v4f b) { return v4f{add1(a[0], b[0]), add1(a[1], b[1]), add1(a[2], b[2]), add1(a[3], b[3])}; }
DEV float sigmoidf_(float z) { return 1.0f / (1.0f + __expf(-z)); }
DEV v4u mkrs(const void* p, unsigned bytes) { const u64 a = (u64)p; return v4u{(unsigned)a, (unsigned)(a >> 32) & 0xffffu, bytes, 0x00020000u}; }
DEV rsrc_t mkrsrc(const void* p, unsigned bytes) { return __builtin_amdgcn_make_buffer_rsrc((void*)p, (short)0, (int)bytes, 0x00020000); }

#define ST16(RS, OFF, SOFF, V) __builtin_amdgcn_raw_buffer_store_b128(V, RS, (OFF) + (SOFF), 0, 16)
#define NTS 0
#define ST16X(RS, OFF, V, NT) __builtin_amdgcn_raw_buffer_store_b128(V, RS, OFF, 0, (NT) ? 18 : 16)
#define LD16X(RS, OFF, SOFF, NT) __builtin_amdgcn_raw_buffer_load_b128(RS, OFF, SOFF, (NT) ? 18 : 16)
#define ST4(RS, OFF, SOFF, V) __builtin_amdgcn_raw_buffer_store_b32(V, RS, OFF, SOFF, 16)
#define LD16(RS, OFF, SOFF) __builtin_amdgcn_raw_buffer_load_b128(RS, OFF, SOFF, 16)
#define LD8(RS, OFF, SOFF) __builtin_amdgcn_raw_buffer_load_b64(RS, OFF, SOFF, 16)
#define LD4(RS, OFF, SOFF) __builtin_amdgcn_raw_buffer_load_b32(RS, OFF, SOFF, 16)
#define DMA16(RS, VOFF, SOFF, LOFF) asm volatile("s_mov_b32 m0, %0\n\ts_nop 0\n\tbuffer_load_dwordx4 %1, %2, %3 offen lds" :: "s"(LOFF), "v"(VOFF), "s"(RS), "s"(SOFF) : "memory", "m0")
#define DMA16N(RS, VOFF, SOFF, LOFF) asm volatile("s_mov_b32 m0, %0\n\ts_nop 0\n\tbuffer_load_dwordx4 %1, %2, %3 offen nt lds" :: "s"(LOFF), "v"(VOFF), "s"(RS), "s"(SOFF) : "memory", "m0")
#define WSTAMP(k) do {} while (0)
#define EXP 0
#define XI 3
#define BK 9
#define FENCE() asm volatile("" ::: "memory")
#define WAITV(n) asm volatile("s_waitcnt vmcnt(" #n ")" ::: "memory")
#define WAITL(n) asm volatile("s_waitcnt lgkmcnt(" #n ")" ::: "memory")
#define RBAR() asm volatile("s_barrier" ::: "memory")
#define BAR() asm volatile("s_waitcnt lgkmcnt(0)\n\ts_barrier" ::: "memory")
#define SCHED() __builtin_amdgcn_sched_barrier(0)

#define KVS(m, h) ((((m) & 2 ? (h) : (m)) & 1u) << 13)
#define FAR(u) ((~(u) & 1u) << 13)
DEV unsigned side(unsigned o) { return o ^ ((o >> 8) & 8192u); }
static constexpr int XP = 1056, XS = 32 * XP, L_RED = 4 * XS;

DEV unsigned wt_src(int tid, unsigned ldw) { return (tid >> 4) * ldw + (((tid & 15) ^ ((((tid >> 4) & 3) << 2) | (((tid >> 7) & 1) << 1))) << 4); }
DEV int wt_rd(int w, int l, int n) { const int fr = l & 15, fq = l >> 4; return (w >> 2) * 16384 + (8 * fq + (fr >> 2)) * 256 + (((4 * (w & 3) + 2 * n + ((fr >> 1) & 1)) ^ (((fr >> 2) << 2) | ((fq & 1) << 1))) << 4) + (fr & 1) * 8; }
DEV v4u wt_frag(const LAS char* p) { return cat(trd(p), trd(p + 1024)); }
#define WT_ISSUE(R0, R1, VB, S0, S1, LDW, LB) do { DMA16N(R0, VB, S0, LB); DMA16N(R0, VB, (S0) + 32 * (LDW), (LB) + 8192); DMA16N(R1, VB, S1, (LB) + 16384); DMA16N(R1, VB, (S1) + 32 * (LDW), (LB) + 24576); } while (0)

static constexpr unsigned F_BC = 512, F_CK = 2048, F_EX = 12288, F_W = 16384, F_PG = 32768, F_GR = 49152, CTL_SZ = 65536;
DEV unsigned fw(int role, int h) { return F_W + ((h >> 3) * 32 + role * 8 + (h & 7)) * 128; }
static constexpr unsigned POI = 0xFFFFFFFFu, OOB = 0x40000000u, SPIN_CAP = 200000u;
DEV bool spin_fail(unsigned tries, unsigned* err) {
    __builtin_amdgcn_s_sleep(1);
    if ((tries & 255u) != 255u) return false;
    unsigned e; asm volatile("global_load_dword %0, %1, off sc1\n\ts_waitcnt vmcnt(0)" : "=&v"(e) : "v"(err) : "memory");
    if (__builtin_amdgcn_readfirstlane(e) == 1u) return true;
    if (tries > SPIN_CAP) { unsigned one = 1u; asm volatile("global_store_dword %0, %1, off sc1" :: "v"(err), "v"(one) : "memory"); return true; }
    return false;
}
DEV bool all_ge(unsigned v, unsigned t) { return __builtin_amdgcn_ballot_w64(v < t) == 0; }
template <int Z = 6> DEV void wait_flags(rsrc_t rc, unsigned off, unsigned t, unsigned* err) {
    for (unsigned tries = 0;; ++tries) {
        FENCE();
        if (all_ge(LD4(rc, off, 0), t)) break;
        __builtin_amdgcn_s_sleep(Z);
        if (spin_fail(tries, err)) break;
    }
    FENCE();
}

#define BC_LD(B, RS) asm volatile("buffer_load_dword %0, off, %1, 0 offset:512 sc1" : "=&v"(B) : "s"(RS) : "memory")
DEV unsigned beacon(v4u rs, unsigned b0, unsigned b1, unsigned need, bool seen = false) {
    asm volatile("" : "+v"(b0), "+v"(b1));
    for (unsigned tries = 0;; ++tries) {
        const unsigned u = __builtin_amdgcn_readfirstlane(b1);
        if (((seen || u != __builtin_amdgcn_readfirstlane(b0)) && (u & need) == need) || tries > SPIN_CAP) return u;
        __builtin_amdgcn_s_sleep(4);
        BC_LD(b1, rs); asm volatile("s_waitcnt vmcnt(0)" : "+v"(b1) :: "memory");
    }
}

template <bool VT> DEV unsigned stream_qkv(LAS char* lds, v4u rx, v4u rw, rsrc_t ro, rsrc_t rc, v4u rsc, bool hs, unsigned fo, int h, unsigned fx, unsigned* dbg) {
    unsigned far = 0;
    const int tid = threadIdx.x, w = __builtin_amdgcn_readfirstlane(tid >> 6), l = tid & 63, kh = w >> 2, cg = w & 3;
    const unsigned lb = (unsigned)(size_t)lds, vb = wt_src(tid, HD * 2), l16 = l * 16;
    const unsigned x0 = (4 * cg + (VT ? 2 : 16) * (1 - kh)) * 2048 + kh * 1024, x1 = (4 * cg + (VT ? 2 : 16) * kh) * 2048 + kh * 1024;
#define STW(s) WT_ISSUE(rw, rw, vb, h * 256 + (s) * (64 * HD * 2), h * 256 + ((s) + 8) * (64 * HD * 2), HD * 2, lb + w * 1024 + ((s) & 3) * XS)
#define STX(u, q, e) DMA16(rx, l16, ((u) >> 1) * 65536 + ((q) & 1 ? x1 : x0) + (VT ? ((e) & 1) + 16 * ((e) >> 1) : (e)) * 2048, lb + (w * 4 + (e)) * XP + (q) * XS)
    const unsigned fl = tid == 0 ? fo : OOB;
    v4u W[16][2];
    unsigned b0, b1;
    STW(0); STW(1); STW(2); STW(3);
    BC_LD(b0, rsc);
#pragma unroll
    for (int s = 0; s < 8; ++s) {
        WAITV(12); RBAR();
#pragma unroll
        for (int n = 0; n < 2; ++n) { const LAS char* p = lds + wt_rd(w, l, n) + (s & 3) * XS; W[2 * s][n] = wt_frag(p); W[2 * s + 1][n] = wt_frag(p + 8192); }
        BAR();
        if (s == 4) BC_LD(b1, rsc);
        if (s == 7) { far ^= FAR(beacon(rsc, b0, b1, 16, !hs)); ST4(rc, OOB, 0, 0u); FENCE(); }
        if (s < 4) STW(s + 4); else { STX(s - 4, s - 4, 0); STX(s - 4, s - 4, 1); if (s < 7) { STX(s - 4, s - 4, 2); STX(s - 4, s - 4, 3); } }
    }
    const LAS char* px = lds + (kh * 16 + (l & 15)) * XP + (l >> 4) * 16;
    LAS v4f* ps = (LAS v4f*)(lds + L_RED + w * 2048 + l16);
    const LAS v4f* pr = (const LAS v4f*)(lds + L_RED + (w ^ 4) * 2048 + l16);
    unsigned oo = OOB, on = (unsigned)h * 16384u + (far ^ fx) + cg * 2048u + kh * 1024u + l16;
    v4f c[2][2] = {}, r0 = {}, r1 = {};
    v4u f[8];
    auto fin = [&]() { r0 = add4(r0, c[1][0]); r1 = add4(r1, c[1][1]); ST16X(ro, oo, (v4u{pk2(r0[0], r0[1]), pk2(r0[2], r0[3]), pk2(r1[0], r1[1]), pk2(r1[2], r1[3])}), NTS & 1); oo = side(on); on += NH * 16384u; };
    WSTAMP(3);
    WAITV(10); RBAR();
    WSTAMP(4);
    const LAS char* px2 = px + 2 * XS; asm volatile("" : "+v"(px2));
#define RDX(q, k) f[(k) & 7] = *(const LAS v4u*)(((q) & 2 ? px2 : px) + ((q) & 1) * XS + (k) * 64)
#pragma unroll
    for (int k = 0; k < 8; ++k) RDX(0, k);
#pragma unroll 1
    for (int u0 = 0; u0 < 2 * NSEG; u0 += 4) {
        if (u0 == 4) WSTAMP(5);
        if (u0 == 64) WSTAMP(6);
#pragma unroll
        for (int q = 0; q < 4; ++q) {
#pragma unroll
            for (int k = 0; k < 16; ++k) {
                const v4u x = f[k & 7];
#pragma unroll
                for (int n = 0; n < 2; ++n) { const v4f z = k ? c[q & 1][n] : v4f{0, 0, 0, 0}; c[q & 1][n] = VT ? mf(x, W[k][n], z) : mf(W[k][n], x, z); }
                if (k < 8) RDX(q, k + 8);
                if (k == BK) { SCHED(); WAITV(10); BAR(); for (int e = 0; e < BK - 8; ++e) RDX(q + 1, e); }
                if (k >= BK) RDX(q + 1, k - 8);
                if (((k + 16 - BK) & 3) == 0) asm volatile("s_setprio %0" :: "n"(3 - (((k + 16 - BK) >> 2) & 3)));
                if (k == 2) { if (q & 1) { ps[0] = c[0][0]; ps[64] = c[0][1]; } else fin(); }
                if (k == 13) { if (q & 1) { r0 = pr[0]; r1 = pr[64]; } else ST4(rc, fl, 0, max((u0 + q - 2) >> 1, 0)); }
                if (!(EXP & 1) && !(k & 1) && kh == (((k >> 1) ^ ((BK + 1) >> 1)) & 1)) { if (k > BK) STX(u0 + q + 4, q, (k - BK) >> 2); else STX(u0 + q + 3, (q + 3) & 3, (k + 16 - BK) >> 2); }
                SCHED();
            }
        }
    }
    asm volatile("s_nop 7" : "+v"(c[1][0]), "+v"(c[1][1]));
    fin(); __builtin_amdgcn_s_setprio(0);
    WAITV(0); BAR();
    if (tid == 0) ST4(rc, fo, 0, NSEG);
    return far;
#undef STW
#undef STX
}

#define LDBX(RS, K, BASE, NT) LD16X(RS, l * 16 + ((K) & 3) * 1024, (BASE) + ((K) >> 2) * 4096, NT)
#define LDB(RS, K, BASE) LDBX(RS, K, BASE, 0)
DEV unsigned xattn(rsrc_t rq, rsrc_t rk, rsrc_t rv, rsrc_t rm, rsrc_t rc, unsigned* err, int seg, int h, int l, unsigned fl, unsigned far, unsigned ms, unsigned* dbg) {
#define XST(k) do {} while (0)
    XST(3);
    asm volatile("" : "+v"(l));
    const int fr = l & 15, g = l >> 4;
    const unsigned sb = side((unsigned)(seg * NH + h) * 16384u + far), mb = (unsigned)(seg * NH + h) * 4096u;
    const unsigned f0 = fw(min(l & 3, 2), h) + 12, f1 = F_PG + h * 512 + (l & 3) * 128;
    const unsigned old = __builtin_amdgcn_ballot_w64(fl <= (unsigned)seg);
    if (old & 15) wait_flags(rc, f0, seg + 1, err);
    XST(4);
    v4u q[4][2], k[4][2], km[4], v[8]; v2u vm[8];
    auto ldm = [&]() {
#pragma unroll
        for (int j = 0; j < 4; ++j) km[j] = LD16(rm, (g * 8 + (l & 7)) * 16 + j * 512, mb);
#pragma unroll
        for (int d = 0; d < 8; ++d) vm[d] = LD8(rm, 2048 + fr * 16 + (g & 1) * 8 + d * 256, mb); };
#pragma unroll
    for (int j = 0; j < 8; ++j) q[j >> 1][j & 1] = LDBX(rq, j, sb, 1);
    if (!(old & 240)) {
#pragma unroll
        for (int j = 0; j < 8; ++j) k[j >> 1][j & 1] = LDBX(rk, j, sb ^ KVS(ms >> 2, h), 1);
#pragma unroll
        for (int d = 0; d < 8; ++d) v[d] = LDBX(rv, d, sb ^ KVS(ms >> 4, h), 1);
        ldm();
    } else {
#pragma unroll
        for (int j = 0; j < 8; ++j) k[j >> 1][j & 1] = LDB(rk, j, sb ^ KVS(ms >> 2, h));
#pragma unroll
        for (int d = 0; d < 8; ++d) v[d] = LDB(rv, d, sb ^ KVS(ms >> 4, h));
    }
    v4f sx[3] = {}, o[2][8];
    float mx[2], zx[2];
    constexpr float C = SCALE * LOG2E, MR = MASKV / SCALE;
#pragma unroll
    for (int j = 0; j < 4; ++j) { sx[0] = mf(k[j][0], q[j][0], sx[0]); sx[1] = mf(k[j][0], q[j][1], sx[1]); sx[2] = mf(k[j][1], q[j][1], sx[2]); }
#pragma unroll
    for (int u = 0; u < 2; ++u) {
        float e[8];
#pragma unroll
        for (int r = 0; r < 4; ++r) { const bool vis = 4 * g + r <= fr; e[r] = (u || vis) ? sx[u][r] : MR; e[4 + r] = (u && vis) ? sx[2][r] : MR; }
        float m = e[0];
#pragma unroll
        for (int r = 1; r < 8; ++r) m = fmaxf(m, e[r]);
        mx[u] = m = red_g<true>(m); zx[u] = 0;
#pragma unroll
        for (int r = 0; r < 8; ++r) { e[r] = __builtin_amdgcn_exp2f((e[r] - m) * C); zx[u] += e[r]; }
        const v4u P = {pk2(e[0], e[1]), pk2(e[4], e[5]), pk2(e[2], e[3]), pk2(e[6], e[7])};
#pragma unroll
        for (int d = 0; d < 8; d += 2) {
            o[u][d] = mf(__builtin_shufflevector(v[d], v[d + 1], 0, 1, 4, 5), P, v4f{0, 0, 0, 0});
            o[u][d + 1] = mf(__builtin_shufflevector(v[d], v[d + 1], 2, 3, 6, 7), P, v4f{0, 0, 0, 0});
        }
    }
    asm volatile("" : "+v"(zx[0]), "+v"(zx[1]));
    SCHED(); XST(5);
    if (old & 240) { wait_flags<2>(rc, f1, seg + 1, err); ldm(); }
    XST(6);
    v4f sm[2] = {};
#pragma unroll
    for (int j = 0; j < 4; ++j) { sm[0] = mf(km[j], q[j][0], sm[0]); sm[1] = mf(km[j], q[j][1], sm[1]); }
    XST(28);
#pragma unroll
    for (int u = 0; u < 2; ++u) {
        const float m = red_g<true>(fmaxf(fmaxf(fmaxf(sm[u][0], sm[u][1]), fmaxf(sm[u][2], sm[u][3])), mx[u])), al = __builtin_amdgcn_exp2f((mx[u] - m) * C);
        float e[4];
#pragma unroll
        for (int r = 0; r < 4; ++r) e[r] = __builtin_amdgcn_exp2f((sm[u][r] - m) * C);
        const float inv = __builtin_amdgcn_rcpf(red_g<false>(al * zx[u] + 0.5f * (e[0] + e[1] + e[2] + e[3]))), f = al * inv, im = g < 2 ? inv : 0.0f;
        const unsigned pm0 = pk2(e[0] * im, e[1] * im), pm1 = pk2(e[2] * im, e[3] * im);
        const v4u Pa = {pm0, pm1, 0u, 0u}, Pb = {0u, 0u, pm0, pm1};
#pragma unroll
        for (int d = 0; d < 4; ++d) {
            const v4u vv = cat(vm[2 * d], vm[2 * d + 1]);
            const v4f o0 = mf(vv, Pa, o[u][2 * d] * f), o1 = mf(vv, Pb, o[u][2 * d + 1] * f);
            ST16(rq, l * 16 + d * 1024, sb + u * 4096, (v4u{pk2(o0[0], o0[1]), pk2(o0[2], o0[3]), pk2(o1[0], o1[1]), pk2(o1[2], o1[3])}));
        }
    }
    XST(29);
    FENCE(); const unsigned fn = LD4(rc, fl > seg + 4u ? OOB : (l & 4 ? f1 : f0), 0);
    WAITV(1); XST(30); RBAR(); XST(31);
    if (threadIdx.x == 0) ST4(rc, fw(3, (h & 24) + (seg & 3)) + 64 + (seg & 60), 0, 1u);
    FENCE();
    return max(fl, fn);
}

template <int N> DEV unsigned ror(unsigned v) { return __builtin_amdgcn_update_dpp(0u, v, 0x120 + N, 0xf, 0xf, false); }
DEV void prep_rows(const bf16_t* x, const bf16_t* gw, const bf16_t* wr, rsrc_t rg, int row, int l) {
    v4u xv[16], gv[2], rv[2];
#pragma unroll
    for (int i = 0; i < 2; ++i) { gv[i] = *(const v4u*)(gw + i * 512 + l * 8); rv[i] = *(const v4u*)(wr + i * 512 + l * 8); }
#pragma unroll
    for (int i = 0; i < 16; ++i) xv[i] = *(const v4u*)(x + (size_t)row * DM + i * 512 + l * 8);
    float ga = 0, gb = 0;
#pragma unroll
    for (int k = 0; k < 8; ++k) {
        float a = 0, b = 0;
#pragma unroll
        for (int i = 0; i < 8; ++i) {
            const unsigned u = xv[2 * k + (i >> 2)][i & 3], g = gv[i >> 2][i & 3], r = rv[i >> 2][i & 3];
            a += rbf(lo16(u) * lo16(g)) + rbf(hi16(u) * hi16(g));
            b += lo16(u) * lo16(r) + hi16(u) * hi16(r);
        }
        a = half_add(a, b);
        a += __uint_as_float(ror<8>(__float_as_uint(a))); a += __uint_as_float(ror<4>(__float_as_uint(a)));
        a += __uint_as_float(ror<2>(__float_as_uint(a))); a += __uint_as_float(ror<1>(__float_as_uint(a)));
        const v2u s = sw16(__float_as_uint(a), __float_as_uint(a)); a = __uint_as_float(s[0]) + __uint_as_float(s[1]);
        if ((l & 31) == k) ga = a;
    }
    gb = rbf(ga);
    const unsigned u = __float_as_uint(l < 32 ? rbf(sigmoidf_(gb)) : gb) | 1u;
    const v2u e = sw32(u, u);
    ST16(rg, l < 8 && !(l & 1) ? F_GR + (row + l) * 8 : OOB, 0, (v4u{e[0], e[1], ror<15>(e[0]), ror<15>(e[1])}));
}
template <int N> DEV unsigned top_keys(unsigned* key, int l) {
    unsigned res = 0;
#pragma unroll 1
    for (int c = 0; c < TOPC; ++c) {
        unsigned m = key[0];
#pragma unroll
        for (int i = 1; i < N; ++i) m = max(m, key[i]);
        m = max(m, ror<8>(m)); m = max(m, ror<4>(m)); m = max(m, ror<2>(m)); m = max(m, ror<1>(m));
        v2u s = sw16(m, m); m = max(s[0], s[1]); s = sw32(m, m); m = max(s[0], s[1]);
#pragma unroll
        for (int i = 0; i < N; ++i) key[i] = key[i] == m ? 0u : key[i];
        if (l == c) res = m;
    }
    return res;
}
DEV int top8(LAS unsigned* cand, rsrc_t rc, unsigned* err, int w, int l) {
    v4u t[2];
    for (unsigned tries = 0;; ++tries) {
        FENCE();
        t[0] = LD16(rc, F_GR + w * 2048 + l * 16, 0); t[1] = LD16(rc, F_GR + w * 2048 + 1024 + l * 16, 0);
        const v4u v = t[0] & t[1];
        if (all_ge(v[0] & v[1] & v[2] & v[3] & 1u, 1u)) break;
        __builtin_amdgcn_s_sleep(8);
        if (spin_fail(tries, err)) break;
    }
    unsigned key[4];
#pragma unroll
    for (int i = 0; i < 4; ++i) { const unsigned r = t[i >> 1][(i & 1) * 2 + 1] - 1u; key[i] = ((int)r < 0 ? 0xffff0000u - r : r + 0x80000000u) + (0xffffu - (w * 256 + (i >> 1) * 128 + l * 2 + (i & 1))); }
    key[0] = top_keys<4>(key, l);
    if (l < TOPC) cand[w * TOPC + l] = key[0];
    BAR();
    key[0] = cand[l];
    return 0xffff - (int)(top_keys<1>(key, l) & 0xffffu);
}
static constexpr int NMOE = FF / 128, XR = 2080;
DEV void moe(LAS char* lds, v4u rx, int sel, v4u rg, v4u ru, v4u rd, rsrc_t rp, int cu) {
    const int tid = threadIdx.x, w = __builtin_amdgcn_readfirstlane(tid >> 6), l = tid & 63, kh = w >> 2, cg = w & 3, fr = l & 15, fq = l >> 4;
    const unsigned lb = (unsigned)(size_t)lds, v1 = wt_src(tid, FF * 2), v2 = wt_src(tid, DM * 2);
    const unsigned xo = __builtin_amdgcn_readlane(sel, w) * 2048;
    DMA16(rx, l * 16, xo, lb + L_RED + w * XR); DMA16(rx, l * 16, xo + 1024, lb + L_RED + w * XR + 1024);
    auto issue = [&](int s) {
        const unsigned d = lb + w * 1024 + (s & 3) * XS;
        if (s < 16) { const unsigned o = s * (64 * FF * 2) + cu * 256; WT_ISSUE(rg, ru, v1, o, o, FF * 2, d); }
        else { const unsigned o = s < 24 ? (cu * 128 + (s & 1) * 64) * (DM * 2) + (((s >> 1) + cu) & 3) * 512 : OOB; WT_ISSUE(rd, rd, v2, o, o + 256, DM * 2, d); } };
    issue(0); issue(1); issue(2); issue(3);
    const int p0 = wt_rd(w, l, 0), p1 = wt_rd(w, l, 1);
    v4f c[2] = {};
    auto stage = [&](int s, const LAS char* pa) {
        WAITV(12); RBAR();
        const LAS char* p = lds + (s & 3) * XS;
        const v4u a0 = *(const LAS v4u*)pa, a1 = *(const LAS v4u*)(pa + 64), b0 = wt_frag(p + p0), b1 = wt_frag(p + p1), b2 = wt_frag(p + p0 + 8192), b3 = wt_frag(p + p1 + 8192);
        BAR();
        issue(s + 4);
        c[0] = mf(b2, a1, mf(b0, a0, c[0])); c[1] = mf(b3, a1, mf(b1, a0, c[1])); };
#pragma unroll 1
    for (int s = 0; s < 16; ++s) stage(s, lds + L_RED + (l & 7) * XR + fq * 16 + s * 128);
    LAS v4f* pu = (LAS v4f*)(lds + L_RED + cg * 2048 + l * 16);
    if (kh) { pu[0] = c[0]; pu[64] = c[1]; }
    BAR();
    if (!kh && fr < 8) {
#pragma unroll
        for (int n = 0; n < 2; ++n) {
            const v4f u = pu[64 * n]; float hv[4];
#pragma unroll
            for (int e = 0; e < 4; ++e) { const float g = rbf(c[n][e]); hv[e] = rbf(g * sigmoidf_(g)) * rbf(u[e]); }
            *(LAS v2u*)(lds + L_RED + 8192 + fr * 288 + cg * 64 + n * 32 + fq * 8) = v2u{pk2(hv[0], hv[1]), pk2(hv[2], hv[3])};
        }
    }
    WAITL(0);
#pragma unroll 1
    for (int s = 16; s < 24; s += 2) {
        c[0] = v4f{0, 0, 0, 0}; c[1] = c[0];
        const LAS char* pa = lds + L_RED + 8192 + (l & 7) * 288 + fq * 16;
        stage(s, pa); stage(s + 1, pa + 128);
        ST16(rp, cu * 32768 + (l & 7) * 4096 + ((((s >> 1) + cu) & 3) * 256 + kh * 128 + cg * 32 + (fr >> 3) * 16 + fq * 4) * 4, 0, __builtin_bit_cast(v4u, fr < 8 ? c[0] : c[1]));
    }
    WAITV(0); RBAR();
}

static constexpr unsigned H2_OFF = 0, H1_OFF = 512 * 1024, H3_OFF = 768 * 1024, PR_OFF = 896 * 1024;
static constexpr size_t SLOT = 2u << 20;
static constexpr int NSLOT = 4;
struct Args {
    const bf16_t* x; const bf16_t* mem0; const bf16_t* Wq; const bf16_t* Wk; const bf16_t* Wv; const bf16_t* Wo;
    const bf16_t* w_r; const bf16_t* wg; const bf16_t* wu; const bf16_t* wd; const bf16_t* gw;
    char* qkv; char* mb; int* idx; float* part; float* upd; char* wt; bf16_t* out;
    char* ring; unsigned* ctl; unsigned* dbg;
    u64 segs[7];
    float per; unsigned per0, d1, dv, d2, dp, slack, thr, spare, wq, wx, pf, ms, ws, wi, gap;
};
typedef const __attribute__((address_space(4))) Args* KArgs;
DEV KArgs args() { KArgs p = (KArgs)__builtin_amdgcn_kernarg_segment_ptr(); asm volatile("" : "+s"(p)); return p; }
static constexpr unsigned QSZ = S_TOK * HD * 4;
typedef unsigned v16u __attribute__((ext_vector_type(16)));
typedef unsigned v32u __attribute__((ext_vector_type(32)));

DEV unsigned nearo(unsigned off) { return (off >> 13) * 16384u + (off & 8191u); }
DEV unsigned rt32() { u64 t; asm volatile("s_memrealtime %0\n\ts_waitcnt lgkmcnt(0)" : "=s"(t) :: "memory"); return (unsigned)t; }
DEV unsigned cyc32() { u64 t; asm volatile("s_memtime %0\n\ts_waitcnt lgkmcnt(0)" : "=s"(t) :: "memory"); return (unsigned)t; }
template <int R1, int R2, int R3> DEV v8u rd_w(const LAS char* p) {
    return __builtin_shufflevector(cat(trd(p), trd(p + R1)), cat(trd(p + R2), trd(p + R3)), 0, 1, 2, 3, 4, 5, 6, 7);
}
DEV v16u cat8(v8u a, v8u b) { return __builtin_shufflevector(a, b, 0, 1, 2, 3, 4, 5, 6, 7, 8, 9, 10, 11, 12, 13, 14, 15); }
DEV v32u cat16(v16u a, v16u b) { return __builtin_shufflevector(a, b, 0, 1, 2, 3, 4, 5, 6, 7, 8, 9, 10, 11, 12, 13, 14, 15, 16, 17, 18, 19, 20, 21, 22, 23, 24, 25, 26, 27, 28, 29, 30, 31); }
DEV v8u cat4(v4u a, v4u b) { return __builtin_shufflevector(a, b, 0, 1, 2, 3, 4, 5, 6, 7); }

static constexpr int L_PQK = 0, L_PV = 8704, L_P = 13824, L_VT = 15360, L_X = 16384, L_K = L_X + 9216, L_MISC = 28672, L_PF = 29696, L_STG = 32768, L_TOT = 163840;
static constexpr int X_FQ = 272, X_BLK = 1152;
#define ASM_A \
    "s_setprio 1\n" \
    "s_mov_b32 s76, 0\n" \
    "s_mov_b32 s99, 0x20000\n" \
    "s_mov_b32 s84, 0\n" \
    "s_mov_b32 s81, 0\n" \
    "s_mov_b32 s86, 0\n" \
    "s_mov_b32 s87, 0\n" \
    "s_mov_b32 s88, 0\n" \
    "s_mov_b32 s89, 0\n" \
    "s_mov_b32 s90, 0\n" \
    "s_mov_b32 s91, 0\n" \
    "s_mov_b32 s92, 0\n" \
    "s_mov_b32 s93, 0\n" \
    "s_mov_b32 s94, 0\n" \
    "s_mov_b32 s95, 0\n" \
    "s_mov_b32 s96, 0\n" \
    "s_mov_b32 s97, 0\n" \
    "s_mov_b32 s98, 0\n" \
    "ds_read_b32 v244, v237\n" \
    "s_waitcnt lgkmcnt(0)\n" \
    "v_readfirstlane_b32 s94, v244\n" \
    "s_mul_i32 s79, s94, s70\n" \
    "s_add_u32 s79, s79, s81\n" \
    "s_mov_b32 s77, s79\n" \
    "s_mul_i32 s96, s65, s71\n" \
    "s_lshr_b32 s96, s96, 4\n" \
    "v_mov_b64 v[208:209], 0\n" \
    "v_mov_b64 v[210:211], 0\n" \
    "v_mov_b64 v[212:213], 0\n" \
    "v_mov_b64 v[214:215], 0\n" \
    "v_mov_b64 v[216:217], 0\n" \
    "v_mov_b64 v[218:219], 0\n" \
    "v_mov_b64 v[220:221], 0\n" \
    "v_mov_b64 v[222:223], 0\n" \
    "v_mov_b64 v[224:225], 0\n" \
    "v_mov_b64 v[226:227], 0\n" \
    "v_mov_b64 v[228:229], 0\n" \
    "v_mov_b64 v[230:231], 0\n" \
    "v_mov_b32 v241, 1\n" \
    "s_branch L%=_Acomp\n" \
    "L%=_Aloop:\n" \
    "s_and_b32 s95, s94, 1\n" \
    "s_lshl_b32 s95, s95, 17\n" \
    "s_add_u32 s95, s95, s60\n" \
    "s_add_u32 s85, s95, 0x1000\n" \
    "s_branch L%=_Au2b\n" \
    "L%=_Au2a:\n" \
    "s_sleep 4\n" \
    "L%=_Au2b:\n" \
    "s_memrealtime vcc\n" \
    "s_add_u32 s80, s77, s64\n" \
    "s_add_u32 s80, s80, -3\n" \
    "s_waitcnt lgkmcnt(0)\n" \
    "s_sub_i32 s79, s80, vcc_lo\n" \
    "s_sub_u32 s79, s79, 40\n" \
    "s_cmp_lt_u32 s79, 0x400\n" \
    "s_cbranch_scc1 L%=_Au2a\n" \
    "s_memtime s[38:39]\n" \
    "s_memrealtime vcc\n" \
    "s_nop 4\n" \
    "s_memrealtime s[32:33]\n" \
    "s_nop 4\n" \
    "s_memrealtime s[34:35]\n" \
    "s_nop 4\n" \
    "s_memrealtime s[36:37]\n" \
    "s_add_u32 s80, s77, s64\n" \
    "s_lshl_b32 s80, s80, 2\n" \
    "s_waitcnt lgkmcnt(0)\n" \
    "s_sub_u32 s80, s80, vcc_lo\n" \
    "s_sub_u32 s80, s80, s32\n" \
    "s_sub_u32 s80, s80, s34\n" \
    "s_sub_u32 s80, s80, s36\n" \
    "s_mul_i32 s80, s80, s71\n" \
    "s_ashr_i32 s80, s80, 6\n" \
    "s_add_u32 s78, s80, s38\n" \
    "L%=_Au3a:\n" \
    "s_memtime s[38:39]\n" \
    "s_mul_i32 s80, -3, 23\n" \
    "s_add_u32 s80, s80, s78\n" \
    "s_waitcnt lgkmcnt(0)\n" \
    "s_sub_i32 s79, s80, s38\n" \
    "s_max_i32 s79, s79, 0\n" \
    "s_cmp_gt_u32 s79, 320\n" \
    "s_cbranch_scc1 L%=_Au3s\n" \
    ".p2align 4\n" \
    "L%=_Au3d:\n" \
    "s_sub_u32 s79, s79, 28\n" \
    "s_cbranch_scc0 L%=_Au3d\n" \
    "L%=_Au3e:\n" \
    "L%=_Apoll:\n" \
    "ds_read_b32 v244, v237 offset:4\n" \
    "s_waitcnt lgkmcnt(0)\n" \
    "v_readfirstlane_b32 s79, v244\n" \
    "s_cmp_lt_u32 s79, s76\n" \
    "s_cbranch_scc1 L%=_Askip\n" \
    "L%=_Ago:\n" \
    "buffer_load_dwordx4 v[192:195], v233, s[40:43], s95 offen sc1\n" \
    "s_nop 5\n" \
    "buffer_load_dwordx4 v[196:199], v233, s[40:43], s85 offen sc1\n" \
    "s_nop 5\n" \
    "buffer_load_dwordx4 v[200:203], v239, s[40:43], s95 offen sc1\n" \
    "s_nop 5\n" \
    "buffer_load_dwordx4 v[204:207], v239, s[40:43], s85 offen sc1\n" \
    "L%=_Acomp:\n" \
    "s_mov_b32 m0, s72\n" \
    "s_waitcnt vmcnt(3)\n" \
    "v_smfmac_f32_16x16x64_bf16 v[208:211], v[192:195], v[0:7], v232\n" \
    "v_smfmac_f32_16x16x64_bf16 v[212:215], v[192:195], v[8:15], v232\n" \
    "v_smfmac_f32_16x16x64_bf16 v[216:219], v[192:195], v[64:71], v232\n" \
    "v_smfmac_f32_16x16x64_bf16 v[220:223], v[192:195], v[72:79], v232\n" \
    "v_smfmac_f32_16x16x64_bf16 v[224:227], v[192:195], v[128:135], v232\n" \
    "s_waitcnt vmcnt(2)\n" \
    "v_smfmac_f32_16x16x64_bf16 v[208:211], v[196:199], v[16:23], v232\n" \
    "v_smfmac_f32_16x16x64_bf16 v[212:215], v[196:199], v[24:31], v232\n" \
    "v_smfmac_f32_16x16x64_bf16 v[216:219], v[196:199], v[80:87], v232\n" \
    "v_smfmac_f32_16x16x64_bf16 v[220:223], v[196:199], v[88:95], v232\n" \
    "v_smfmac_f32_16x16x64_bf16 v[228:231], v[192:195], v[136:143], v232\n" \
    "s_waitcnt vmcnt(1)\n" \
    "v_smfmac_f32_16x16x64_bf16 v[208:211], v[200:203], v[32:39], v232\n" \
    "v_smfmac_f32_16x16x64_bf16 v[212:215], v[200:203], v[40:47], v232\n" \
    "v_smfmac_f32_16x16x64_bf16 v[216:219], v[200:203], v[96:103], v232\n" \
    "v_smfmac_f32_16x16x64_bf16 v[220:223], v[200:203], v[104:111], v232\n" \
    "v_smfmac_f32_16x16x64_bf16 v[224:227], v[196:199], v[144:151], v232\n" \
    "s_waitcnt vmcnt(0)\n" \
    "v_smfmac_f32_16x16x64_bf16 v[208:211], v[204:207], v[48:55], v232\n" \
    "v_smfmac_f32_16x16x64_bf16 v[212:215], v[204:207], v[56:63], v232\n" \
    "v_smfmac_f32_16x16x64_bf16 v[216:219], v[204:207], v[112:119], v232\n" \
    "v_add_f32 v208, v208, v210\n" \
    "v_smfmac_f32_16x16x64_bf16 v[220:223], v[204:207], v[120:127], v232\n" \
    "v_add_f32 v209, v209, v211\n" \
    "v_cmp_u_f32 vcc, v208, v209\n" \
    "v_add_f32 v212, v212, v214\n" \
    "v_add_f32 v213, v213, v215\n" \
    "v_add_f32 v216, v216, v218\n" \
    "v_add_f32 v217, v217, v219\n" \
    "s_nop 1\n" \
    "v_add_f32 v220, v220, v222\n" \
    "v_add_f32 v221, v221, v223\n" \
    "v_smfmac_f32_16x16x64_bf16 v[224:227], v[200:203], v[160:167], v232\n" \
    "s_cbranch_vccnz L%=_Aslow\n" \
    "L%=_Aok:\n" \
    "ds_write_addtid_b32 v208 offset:0\n" \
    "ds_write_addtid_b32 v209 offset:544\n" \
    "v_smfmac_f32_16x16x64_bf16 v[228:231], v[196:199], v[152:159], v232\n" \
    "ds_write_addtid_b32 v216 offset:256\n" \
    "ds_write_addtid_b32 v217 offset:800\n" \
    "v_smfmac_f32_16x16x64_bf16 v[224:227], v[204:207], v[176:183], v232\n" \
    "ds_write_addtid_b32 v212 offset:1088\n" \
    "ds_write_addtid_b32 v213 offset:1632\n" \
    "v_smfmac_f32_16x16x64_bf16 v[228:231], v[200:203], v[168:175], v232\n" \
    "ds_write_addtid_b32 v220 offset:1344\n" \
    "ds_write_addtid_b32 v221 offset:1888\n" \
    "ds_write_b32 v236, v241 offset:32\n" \
    "v_smfmac_f32_16x16x64_bf16 v[228:231], v[204:207], v[184:191], v232\n" \
    "s_barrier\n" \
    "s_mov_b32 m0, s73\n" \
    "v_add_f32 v224, v224, v226\n" \
    "v_add_f32 v225, v225, v227\n" \
    "ds_write_addtid_b32 v224 offset:8704\n" \
    "ds_write_addtid_b32 v225 offset:8992\n" \
    "s_nop 1\n" \
    "v_add_f32 v228, v228, v230\n" \
    "v_add_f32 v229, v229, v231\n" \
    "ds_write_addtid_b32 v228 offset:9344\n" \
    "ds_write_addtid_b32 v229 offset:9632\n" \
    "ds_write_b32 v236, v241 offset:16\n" \
    "v_mov_b64 v[208:209], 0\n" \
    "v_mov_b64 v[210:211], 0\n" \
    "v_mov_b64 v[212:213], 0\n" \
    "v_mov_b64 v[214:215], 0\n" \
    "v_mov_b64 v[216:217], 0\n" \
    "v_mov_b64 v[218:219], 0\n" \
    "v_mov_b64 v[220:221], 0\n" \
    "v_mov_b64 v[222:223], 0\n" \
    "v_mov_b64 v[224:225], 0\n" \
    "v_mov_b64 v[226:227], 0\n" \
    "v_mov_b64 v[228:229], 0\n" \
    "v_mov_b64 v[230:231], 0\n" \
    "v_add_u32 v241, 1, v241\n" \
    "ds_read_b32 v244, v237\n" \
    "s_waitcnt lgkmcnt(0)\n" \
    "v_readfirstlane_b32 s94, v244\n" \
    "s_mul_i32 s79, s94, s70\n" \
    "s_add_u32 s79, s79, s81\n" \
    "s_mov_b32 s77, s79\n" \
    "s_barrier\n" \
    "ds_read_b64 v[244:245], v237 offset:64\n" \
    "s_mov_b32 s79, s60\n" \
    "s_mov_b32 s60, s61\n" \
    "s_mov_b32 s61, s62\n" \
    "s_mov_b32 s62, s63\n" \
    "s_mov_b32 s63, s79\n" \
    "s_add_u32 s76, s76, 1\n" \
    "s_add_u32 s77, s77, s65\n" \
    "s_add_u32 s84, s84, 0x20000\n" \
    "s_add_u32 s81, s81, s65\n" \
    "s_waitcnt lgkmcnt(0)\n" \
    "v_readfirstlane_b32 s65, v244\n" \
    "v_readfirstlane_b32 s71, v245\n" \
    "s_mul_i32 s96, s65, s71\n" \
    "s_lshr_b32 s96, s96, 4\n" \
    "s_cmp_lt_u32 s76, 64\n" \
    "s_cbranch_scc1 L%=_Aloop\n" \
    "s_branch L%=_end\n" \
    "L%=_Aslow:\n" \
    "s_nop 7\n" \
    "s_nop 0\n" \
    "v_max_u32 v244, v192, v193\n" \
    "v_max3_u32 v244, v244, v194, v195\n" \
    "v_max3_u32 v244, v244, v196, v197\n" \
    "v_max3_u32 v244, v244, v198, v199\n" \
    "v_max3_u32 v244, v244, v200, v201\n" \
    "v_max3_u32 v244, v244, v202, v203\n" \
    "v_max3_u32 v244, v244, v204, v205\n" \
    "v_max3_u32 v244, v244, v206, v207\n" \
    "v_cmp_eq_u32 vcc, -1, v244\n" \
    "s_cbranch_vccz L%=_Aok\n" \
    "s_cmp_eq_u32 s99, 0\n" \
    "s_cbranch_scc1 L%=_Aok\n" \
    "s_sub_u32 s99, s99, 1\n" \
    "s_cmp_lg_u32 s99, 0\n" \
    "s_cbranch_scc1 L%=_Au5\n" \
    "v_mov_b32 v244, 1\n" \
    "buffer_store_dword v244, off, s[44:47], 0 sc1\n" \
    "L%=_Au5:\n" \
    "v_mov_b64 v[208:209], 0\n" \
    "v_mov_b64 v[210:211], 0\n" \
    "v_mov_b64 v[212:213], 0\n" \
    "v_mov_b64 v[214:215], 0\n" \
    "v_mov_b64 v[216:217], 0\n" \
    "v_mov_b64 v[218:219], 0\n" \
    "v_mov_b64 v[220:221], 0\n" \
    "v_mov_b64 v[222:223], 0\n" \
    "v_mov_b64 v[224:225], 0\n" \
    "v_mov_b64 v[226:227], 0\n" \
    "v_mov_b64 v[228:229], 0\n" \
    "v_mov_b64 v[230:231], 0\n" \
    "L%=_Ash:\n" \
    "s_memrealtime vcc\n" \
    "s_waitcnt lgkmcnt(0)\n" \
    "s_sub_u32 s80, vcc_lo, s64\n" \
    "s_sub_u32 s80, s80, 4\n" \
    "s_mul_i32 s32, s70, s71\n" \
    "s_lshr_b32 s32, s32, 4\n" \
    "L%=_Au6g:\n" \
    "s_add_u32 s77, s77, s70\n" \
    "s_add_u32 s78, s78, s32\n" \
    "s_add_u32 s94, s94, 1\n" \
    "s_sub_u32 s79, s80, s77\n" \
    "s_cmp_lt_u32 s79, 0x1000000\n" \
    "s_cbranch_scc1 L%=_Au6g\n" \
    "v_mov_b32 v244, s94\n" \
    "ds_max_u32 v237, v244\n" \
    "s_cmp_gt_i32 s79, -20\n" \
    "s_cbranch_scc1 L%=_Au6c\n" \
    "s_branch L%=_Au7b\n" \
    "L%=_Au7a:\n" \
    "s_sleep 4\n" \
    "L%=_Au7b:\n" \
    "s_memrealtime vcc\n" \
    "s_add_u32 s80, s77, s64\n" \
    "s_add_u32 s80, s80, -3\n" \
    "s_waitcnt lgkmcnt(0)\n" \
    "s_sub_i32 s79, s80, vcc_lo\n" \
    "s_sub_u32 s79, s79, 40\n" \
    "s_cmp_lt_u32 s79, 0x400\n" \
    "s_cbranch_scc1 L%=_Au7a\n" \
    "s_memtime s[38:39]\n" \
    "s_memrealtime vcc\n" \
    "s_nop 4\n" \
    "s_memrealtime s[32:33]\n" \
    "s_nop 4\n" \
    "s_memrealtime s[34:35]\n" \
    "s_nop 4\n" \
    "s_memrealtime s[36:37]\n" \
    "s_add_u32 s80, s77, s64\n" \
    "s_lshl_b32 s80, s80, 2\n" \
    "s_waitcnt lgkmcnt(0)\n" \
    "s_sub_u32 s80, s80, vcc_lo\n" \
    "s_sub_u32 s80, s80, s32\n" \
    "s_sub_u32 s80, s80, s34\n" \
    "s_sub_u32 s80, s80, s36\n" \
    "s_mul_i32 s80, s80, s71\n" \
    "s_ashr_i32 s80, s80, 6\n" \
    "s_add_u32 s78, s80, s38\n" \
    "L%=_Au6c:\n" \
    "s_and_b32 s95, s94, 1\n" \
    "s_lshl_b32 s95, s95, 17\n" \
    "s_add_u32 s95, s95, s60\n" \
    "s_add_u32 s85, s95, 0x1000\n" \
    "L%=_Au8a:\n" \
    "s_memtime s[38:39]\n" \
    "s_mul_i32 s80, -3, 23\n" \
    "s_add_u32 s80, s80, s78\n" \
    "s_waitcnt lgkmcnt(0)\n" \
    "s_sub_i32 s79, s80, s38\n" \
    "s_max_i32 s79, s79, 0\n" \
    "s_cmp_gt_u32 s79, 320\n" \
    "s_cbranch_scc1 L%=_Au8s\n" \
    ".p2align 4\n" \
    "L%=_Au8d:\n" \
    "s_sub_u32 s79, s79, 28\n" \
    "s_cbranch_scc0 L%=_Au8d\n" \
    "L%=_Au8e:\n" \
    "s_branch L%=_Apoll\n" \
    "L%=_Askip:\n" \
    "s_cmp_lt_u32 s99, 2\n" \
    "s_cbranch_scc1 L%=_Ago\n" \
    "s_sub_u32 s99, s99, 1\n" \
    "s_branch L%=_Ash\n" \
    "L%=_Au3s:\n" \
    "s_cmp_gt_u32 s79, 0x8000\n" \
    "s_cbranch_scc1 L%=_Au3e\n" \
    "s_sleep 2\n" \
    "s_branch L%=_Au3a\n" \
    "L%=_Au8s:\n" \
    "s_cmp_gt_u32 s79, 0x8000\n" \
    "s_cbranch_scc1 L%=_Au8e\n" \
    "s_sleep 2\n" \
    "s_branch L%=_Au8a\n" \
    "L%=_end:\n"
#define ASM_B \
    "s_and_b32 s79, s78, 3\n" \
    "s_cmp_eq_u32 s79, 1\n" \
    "s_cbranch_scc1 L%=_S\n" \
    "s_cmp_eq_u32 s79, 2\n" \
    "s_cbranch_scc1 L%=_R\n" \
    "s_cmp_eq_u32 s79, 3\n" \
    "s_cbranch_scc1 L%=_P\n" \
    "L%=_T:\n" \
    "s_mov_b32 s76, 0\n" \
    "s_mov_b32 s99, 0x20000\n" \
    "s_mov_b32 s84, 0\n" \
    "s_mov_b32 s81, 0\n" \
    "s_mov_b32 s86, 0\n" \
    "s_mov_b32 s87, 0\n" \
    "s_mov_b32 s88, 0\n" \
    "s_mov_b32 s89, 0\n" \
    "s_mov_b32 s90, 0\n" \
    "s_mov_b32 s91, 0\n" \
    "s_mov_b32 s92, 0\n" \
    "s_mov_b32 s93, 0\n" \
    "s_mov_b32 s94, 0\n" \
    "s_mov_b32 s95, 0\n" \
    "s_mov_b32 s96, 0\n" \
    "s_mov_b32 s97, 0\n" \
    "s_mov_b32 s98, 0\n" \
    "ds_read_b32 v216, v229\n" \
    "s_waitcnt lgkmcnt(0)\n" \
    "v_readfirstlane_b32 s94, v216\n" \
    "s_mul_i32 s79, s94, s70\n" \
    "s_add_u32 s79, s79, s81\n" \
    "s_mov_b32 s77, s79\n" \
    "s_mul_i32 s96, s65, s71\n" \
    "s_lshr_b32 s96, s96, 4\n" \
    "v_mov_b32 v96, 0x3f803f80\n" \
    "v_mov_b32 v97, 0x3f803f80\n" \
    "v_mov_b32 v98, 0x3f803f80\n" \
    "v_mov_b32 v99, 0x3f803f80\n" \
    "v_mov_b32 v100, 0x3f803f80\n" \
    "v_mov_b32 v101, 0x3f803f80\n" \
    "v_mov_b64 v[102:103], 0\n" \
    "v_mov_b64 v[64:65], 0\n" \
    "v_mov_b64 v[66:67], 0\n" \
    "v_mov_b64 v[68:69], 0\n" \
    "v_mov_b64 v[70:71], 0\n" \
    "v_mov_b64 v[72:73], 0\n" \
    "v_mov_b64 v[74:75], 0\n" \
    "v_mov_b64 v[76:77], 0\n" \
    "v_mov_b64 v[78:79], 0\n" \
    "v_mov_b64 v[80:81], 0\n" \
    "v_mov_b64 v[82:83], 0\n" \
    "v_mov_b64 v[84:85], 0\n" \
    "v_mov_b64 v[86:87], 0\n" \
    "v_mov_b64 v[88:89], 0\n" \
    "v_mov_b64 v[90:91], 0\n" \
    "v_mov_b64 v[92:93], 0\n" \
    "v_mov_b64 v[94:95], 0\n" \
    "v_mov_b64 v[192:193], 0\n" \
    "v_mov_b64 v[194:195], 0\n" \
    "L%=_Tloop:\n" \
    "ds_read_b64_tr_b16 v[104:105], v228 offset:16384\n" \
    "ds_read_b64_tr_b16 v[106:107], v228 offset:16416\n" \
    "ds_read_b64_tr_b16 v[108:109], v228 offset:18688\n" \
    "ds_read_b64_tr_b16 v[110:111], v228 offset:18720\n" \
    "ds_read_b64_tr_b16 v[112:113], v228 offset:20992\n" \
    "ds_read_b64_tr_b16 v[114:115], v228 offset:21024\n" \
    "ds_read_b64_tr_b16 v[116:117], v228 offset:23296\n" \
    "ds_read_b64_tr_b16 v[118:119], v228 offset:23328\n" \
    "ds_read_b64_tr_b16 v[120:121], v228 offset:16656\n" \
    "ds_read_b64_tr_b16 v[122:123], v228 offset:16688\n" \
    "ds_read_b64_tr_b16 v[124:125], v228 offset:18960\n" \
    "ds_read_b64_tr_b16 v[126:127], v228 offset:18992\n" \
    "ds_read_b64_tr_b16 v[128:129], v228 offset:21264\n" \
    "ds_read_b64_tr_b16 v[130:131], v228 offset:21296\n" \
    "ds_read_b64_tr_b16 v[132:133], v228 offset:23568\n" \
    "ds_read_b64_tr_b16 v[134:135], v228 offset:23600\n" \
    "s_waitcnt lgkmcnt(14)\n" \
    "v_mfma_f32_16x16x32_bf16 v[136:139], v[104:107], v[0:3], 0\n" \
    "v_mfma_f32_16x16x32_bf16 v[144:147], v[104:107], v[8:11], 0\n" \
    "v_mfma_f32_16x16x32_bf16 v[152:155], v[104:107], v[16:19], 0\n" \
    "v_mfma_f32_16x16x32_bf16 v[160:163], v[104:107], v[24:27], 0\n" \
    "s_waitcnt lgkmcnt(12)\n" \
    "v_mfma_f32_16x16x32_bf16 v[136:139], v[108:111], v[4:7], v[136:139]\n" \
    "v_mfma_f32_16x16x32_bf16 v[144:147], v[108:111], v[12:15], v[144:147]\n" \
    "v_mfma_f32_16x16x32_bf16 v[152:155], v[108:111], v[20:23], v[152:155]\n" \
    "v_mfma_f32_16x16x32_bf16 v[160:163], v[108:111], v[28:31], v[160:163]\n" \
    "s_waitcnt lgkmcnt(10)\n" \
    "v_mfma_f32_16x16x32_bf16 v[136:139], v[112:115], v[32:35], v[136:139]\n" \
    "v_mfma_f32_16x16x32_bf16 v[144:147], v[112:115], v[40:43], v[144:147]\n" \
    "v_mfma_f32_16x16x32_bf16 v[152:155], v[112:115], v[48:51], v[152:155]\n" \
    "v_mfma_f32_16x16x32_bf16 v[160:163], v[112:115], v[56:59], v[160:163]\n" \
    "s_waitcnt lgkmcnt(8)\n" \
    "v_mfma_f32_16x16x32_bf16 v[136:139], v[116:119], v[36:39], v[136:139]\n" \
    "v_mfma_f32_16x16x32_bf16 v[144:147], v[116:119], v[44:47], v[144:147]\n" \
    "v_mfma_f32_16x16x32_bf16 v[152:155], v[116:119], v[52:55], v[152:155]\n" \
    "v_mfma_f32_16x16x32_bf16 v[160:163], v[116:119], v[60:63], v[160:163]\n" \
    "s_waitcnt lgkmcnt(6)\n" \
    "v_mfma_f32_16x16x32_bf16 v[140:143], v[120:123], v[0:3], 0\n" \
    "v_mfma_f32_16x16x32_bf16 v[148:151], v[120:123], v[8:11], 0\n" \
    "v_mfma_f32_16x16x32_bf16 v[156:159], v[120:123], v[16:19], 0\n" \
    "v_mfma_f32_16x16x32_bf16 v[164:167], v[120:123], v[24:27], 0\n" \
    "s_waitcnt lgkmcnt(4)\n" \
    "v_mfma_f32_16x16x32_bf16 v[140:143], v[124:127], v[4:7], v[140:143]\n" \
    "v_mfma_f32_16x16x32_bf16 v[148:151], v[124:127], v[12:15], v[148:151]\n" \
    "v_mfma_f32_16x16x32_bf16 v[156:159], v[124:127], v[20:23], v[156:159]\n" \
    "v_mfma_f32_16x16x32_bf16 v[164:167], v[124:127], v[28:31], v[164:167]\n" \
    "s_waitcnt lgkmcnt(2)\n" \
    "v_mfma_f32_16x16x32_bf16 v[140:143], v[128:131], v[32:35], v[140:143]\n" \
    "v_mfma_f32_16x16x32_bf16 v[148:151], v[128:131], v[40:43], v[148:151]\n" \
    "v_mfma_f32_16x16x32_bf16 v[156:159], v[128:131], v[48:51], v[156:159]\n" \
    "v_mfma_f32_16x16x32_bf16 v[164:167], v[128:131], v[56:59], v[164:167]\n" \
    "s_waitcnt lgkmcnt(0)\n" \
    "v_mfma_f32_16x16x32_bf16 v[140:143], v[132:135], v[36:39], v[140:143]\n" \
    "v_mfma_f32_16x16x32_bf16 v[148:151], v[132:135], v[44:47], v[148:151]\n" \
    "v_mfma_f32_16x16x32_bf16 v[156:159], v[132:135], v[52:55], v[156:159]\n" \
    "v_mfma_f32_16x16x32_bf16 v[164:167], v[132:135], v[60:63], v[164:167]\n" \
    "v_cvt_pk_bf16_f32 v65, v136, v137\n" \
    "v_cvt_pk_bf16_f32 v68, v138, v139\n" \
    "v_cvt_pk_bf16_f32 v73, v144, v145\n" \
    "v_cvt_pk_bf16_f32 v76, v146, v147\n" \
    "v_cvt_pk_bf16_f32 v81, v152, v153\n" \
    "v_cvt_pk_bf16_f32 v84, v154, v155\n" \
    "v_cvt_pk_bf16_f32 v89, v160, v161\n" \
    "v_cvt_pk_bf16_f32 v92, v162, v163\n" \
    "v_cvt_pk_bf16_f32 v66, v142, v143\n" \
    "v_cvt_pk_bf16_f32 v67, v142, v143\n" \
    "v_cvt_pk_bf16_f32 v69, v140, v141\n" \
    "v_cvt_pk_bf16_f32 v74, v150, v151\n" \
    "v_cvt_pk_bf16_f32 v75, v150, v151\n" \
    "v_cvt_pk_bf16_f32 v77, v148, v149\n" \
    "v_cvt_pk_bf16_f32 v82, v158, v159\n" \
    "v_cvt_pk_bf16_f32 v83, v158, v159\n" \
    "v_cvt_pk_bf16_f32 v85, v156, v157\n" \
    "v_cvt_pk_bf16_f32 v90, v166, v167\n" \
    "v_cvt_pk_bf16_f32 v91, v166, v167\n" \
    "v_cvt_pk_bf16_f32 v93, v164, v165\n" \
    "v_permlane16_swap_b32 v66, v67\n" \
    "v_permlane16_swap_b32 v74, v75\n" \
    "v_permlane16_swap_b32 v82, v83\n" \
    "v_permlane16_swap_b32 v90, v91\n" \
    "v_mov_b64 v[176:177], 0\n" \
    "v_mov_b64 v[178:179], 0\n" \
    "v_mov_b64 v[180:181], 0\n" \
    "v_mov_b64 v[182:183], 0\n" \
    "v_mov_b64 v[184:185], 0\n" \
    "v_mov_b64 v[186:187], 0\n" \
    "v_mov_b64 v[188:189], 0\n" \
    "v_mov_b64 v[190:191], 0\n" \
    "v_mov_b64 v[196:197], 0\n" \
    "v_mov_b64 v[198:199], 0\n" \
    "v_mov_b64 v[200:201], 0\n" \
    "v_mov_b64 v[202:203], 0\n" \
    "v_mov_b64 v[204:205], 0\n" \
    "v_mov_b64 v[206:207], 0\n" \
    "v_mov_b64 v[208:209], 0\n" \
    "v_mov_b64 v[210:211], 0\n" \
    "v_mov_b64 v[212:213], 0\n" \
    "v_mov_b64 v[214:215], 0\n" \
    "s_add_u32 s85, s60, 0x1000\n" \
    "s_branch L%=_Tu2b\n" \
    "L%=_Tu2a:\n" \
    "s_sleep 4\n" \
    "L%=_Tu2b:\n" \
    "s_memrealtime vcc\n" \
    "s_add_u32 s80, s77, s64\n" \
    "s_add_u32 s80, s80, s69\n" \
    "s_waitcnt lgkmcnt(0)\n" \
    "s_sub_i32 s79, s80, vcc_lo\n" \
    "s_sub_u32 s79, s79, 40\n" \
    "s_cmp_lt_u32 s79, 0x400\n" \
    "s_cbranch_scc1 L%=_Tu2a\n" \
    "s_memtime s[38:39]\n" \
    "s_memrealtime vcc\n" \
    "s_nop 4\n" \
    "s_memrealtime s[32:33]\n" \
    "s_nop 4\n" \
    "s_memrealtime s[34:35]\n" \
    "s_nop 4\n" \
    "s_memrealtime s[36:37]\n" \
    "s_add_u32 s80, s77, s64\n" \
    "s_lshl_b32 s80, s80, 2\n" \
    "s_waitcnt lgkmcnt(0)\n" \
    "s_sub_u32 s80, s80, vcc_lo\n" \
    "s_sub_u32 s80, s80, s32\n" \
    "s_sub_u32 s80, s80, s34\n" \
    "s_sub_u32 s80, s80, s36\n" \
    "s_mul_i32 s80, s80, s71\n" \
    "s_ashr_i32 s80, s80, 6\n" \
    "s_add_u32 s78, s80, s38\n" \
    "v_mov_b32 v143, s76\n" \
    "s_barrier\n" \
    "s_cmp_eq_u32 s76, 63\n" \
    "s_cbranch_scc1 L%=_Tfin\n" \
    "ds_read_b32 v216, v229\n" \
    "s_waitcnt lgkmcnt(0)\n" \
    "v_readfirstlane_b32 s94, v216\n" \
    "s_mul_i32 s79, s94, s70\n" \
    "s_add_u32 s79, s79, s81\n" \
    "s_cmp_lg_u32 s79, s77\n" \
    "s_cbranch_scc1 L%=_Tu3s\n" \
    "L%=_Tu3k:\n" \
    "L%=_Tu4a:\n" \
    "s_memtime s[38:39]\n" \
    "s_mul_hi_u32 s80, s66, s96\n" \
    "s_add_u32 s80, s80, s78\n" \
    "s_waitcnt lgkmcnt(0)\n" \
    "s_sub_i32 s79, s80, s38\n" \
    "s_max_i32 s79, s79, 0\n" \
    "s_cmp_gt_u32 s79, 320\n" \
    "s_cbranch_scc1 L%=_Tu4s\n" \
    ".p2align 4\n" \
    "L%=_Tu4d:\n" \
    "s_sub_u32 s79, s79, 28\n" \
    "s_cbranch_scc0 L%=_Tu4d\n" \
    "L%=_Tu4e:\n" \
    "buffer_load_dwordx4 v[104:107], v230, s[40:43], s60 offen offset:0 sc1\n" \
    "buffer_load_dwordx2 v[120:121], v231, s[40:43], s60 offen offset:0 sc1\n" \
    "buffer_load_dwordx4 v[108:111], v230, s[40:43], s60 offen offset:2048 sc1\n" \
    "buffer_load_dwordx2 v[122:123], v231, s[40:43], s60 offen offset:2048 sc1\n" \
    "buffer_load_dwordx4 v[112:115], v230, s[40:43], s85 offen offset:0 sc1\n" \
    "buffer_load_dwordx2 v[124:125], v231, s[40:43], s85 offen offset:0 sc1\n" \
    "buffer_load_dwordx4 v[116:119], v230, s[40:43], s85 offen offset:2048 sc1\n" \
    "buffer_load_dwordx2 v[126:127], v231, s[40:43], s85 offen offset:2048 sc1\n" \
    "s_setprio 3\n" \
    "buffer_load_dwordx4 v[168:171], v225, s[40:43], s60 offen sc1\n" \
    "buffer_load_dwordx4 v[172:175], v225, s[40:43], s85 offen sc1\n" \
    "L%=_Tsc:\n" \
    "s_waitcnt vmcnt(6)\n" \
    "v_pk_add_f32 v[128:129], v[104:105], v[108:109]\n" \
    "v_pk_add_f32 v[130:131], v[106:107], v[110:111]\n" \
    "v_pk_add_f32 v[132:133], v[120:121], v[122:123]\n" \
    "s_waitcnt vmcnt(4)\n" \
    "v_pk_add_f32 v[128:129], v[128:129], v[112:113]\n" \
    "v_pk_add_f32 v[130:131], v[130:131], v[114:115]\n" \
    "v_pk_add_f32 v[132:133], v[132:133], v[124:125]\n" \
    "s_waitcnt vmcnt(2)\n" \
    "v_pk_add_f32 v[128:129], v[128:129], v[116:117]\n" \
    "v_pk_add_f32 v[130:131], v[130:131], v[118:119]\n" \
    "v_pk_add_f32 v[132:133], v[132:133], v[126:127]\n" \
    "v_max3_f32 v136, v128, v129, v130\n" \
    "v_max3_f32 v137, v131, v132, v133\n" \
    "v_max_f32 v136, v136, v137\n" \
    "v_fma_f32 v141, v131, v132, v133\n" \
    "v_fma_f32 v140, v128, v129, v130\n" \
    "v_max_f32_dpp v138, v136, v136 row_ror:8 row_mask:0xf bank_mask:0xf\n" \
    "v_max_f32_dpp v139, v136, v136 row_ror:8 row_mask:0xf bank_mask:0xf\n" \
    "s_waitcnt vmcnt(0)\n" \
    "v_smfmac_f32_16x16x64_bf16 v[176:179], v[168:171], v[0:7], v224\n" \
    "v_cmp_u_f32 vcc, v140, v141\n" \
    "v_permlane16_swap_b32 v138, v139\n" \
    "v_max_f32 v136, v138, v139\n" \
    "v_max_f32 v137, v138, v139\n" \
    "v_smfmac_f32_16x16x64_bf16 v[180:183], v[168:171], v[8:15], v224\n" \
    "s_cbranch_vccnz L%=_Tslow\n" \
    "L%=_Tok:\n" \
    "v_permlane32_swap_b32 v136, v137\n" \
    "v_max_f32 v136, v136, v137\n" \
    "v_mul_f32 v136, s72, v136\n" \
    "v_pk_fma_f32 v[128:129], v[128:129], s[72:73], v[136:137] op_sel_hi:[1,1,0] neg_lo:[0,0,1] neg_hi:[0,0,1]\n" \
    "v_pk_fma_f32 v[132:133], v[132:133], s[72:73], v[136:137] op_sel_hi:[1,1,0] neg_lo:[0,0,1] neg_hi:[0,0,1]\n" \
    "v_pk_fma_f32 v[130:131], v[130:131], s[72:73], v[136:137] op_sel_hi:[1,1,0] neg_lo:[0,0,1] neg_hi:[0,0,1]\n" \
    "v_exp_f32 v128, v128\n" \
    "v_exp_f32 v129, v129\n" \
    "v_exp_f32 v132, v132\n" \
    "v_exp_f32 v133, v133\n" \
    "v_exp_f32 v130, v130\n" \
    "v_exp_f32 v131, v131\n" \
    "v_cvt_pk_bf16_f32 v140, v128, v129\n" \
    "v_cvt_pk_bf16_f32 v141, v132, v133\n" \
    "v_cvt_pk_bf16_f32 v142, v130, v131\n" \
    "ds_write_b128 v232, v[140:143]\n" \
    "s_barrier\n" \
    "ds_read_b128 v[192:195], v227\n" \
    "s_waitcnt vmcnt(0)\n" \
    "v_smfmac_f32_16x16x64_bf16 v[176:179], v[172:175], v[32:39], v224\n" \
    "L%=_Tvc:\n" \
    "v_smfmac_f32_16x16x64_bf16 v[180:183], v[172:175], v[40:47], v224\n" \
    "v_smfmac_f32_16x16x64_bf16 v[184:187], v[168:171], v[16:23], v224\n" \
    "v_add_f32 v176, v176, v178\n" \
    "v_smfmac_f32_16x16x64_bf16 v[188:191], v[168:171], v[24:31], v224\n" \
    "v_add_f32 v177, v177, v179\n" \
    "v_smfmac_f32_16x16x64_bf16 v[184:187], v[172:175], v[48:55], v224\n" \
    "v_cmp_u_f32 vcc, v176, v177\n" \
    "v_smfmac_f32_16x16x64_bf16 v[188:191], v[172:175], v[56:63], v224\n" \
    "s_cbranch_vccnz L%=_Tvslow\n" \
    "L%=_Tvok:\n" \
    "v_cvt_pk_bf16_f32 v64, v176, v177\n" \
    "v_add_f32 v180, v180, v182\n" \
    "s_waitcnt lgkmcnt(0)\n" \
    "v_smfmac_f32_16x16x64_bf16 v[212:215], v[192:195], v[96:103], v224\n" \
    "v_add_f32 v181, v181, v183\n" \
    "v_smfmac_f32_16x16x64_bf16 v[196:199], v[192:195], v[64:71], v224\n" \
    "v_cvt_pk_bf16_f32 v72, v180, v181\n" \
    "v_add_f32 v184, v184, v186\n" \
    "v_add_f32 v185, v185, v187\n" \
    "v_smfmac_f32_16x16x64_bf16 v[200:203], v[192:195], v[72:79], v224\n" \
    "v_cvt_pk_bf16_f32 v80, v184, v185\n" \
    "v_add_f32 v188, v188, v190\n" \
    "v_add_f32 v189, v189, v191\n" \
    "v_smfmac_f32_16x16x64_bf16 v[204:207], v[192:195], v[80:87], v224\n" \
    "v_cvt_pk_bf16_f32 v88, v188, v189\n" \
    "v_add_f32 v212, v212, v214\n" \
    "v_add_f32 v213, v213, v215\n" \
    "v_smfmac_f32_16x16x64_bf16 v[208:211], v[192:195], v[88:95], v224\n" \
    "v_rcp_f32 v220, v212\n" \
    "v_rcp_f32 v221, v213\n" \
    "v_add_f32 v196, v196, v198\n" \
    "v_add_f32 v197, v197, v199\n" \
    "v_pk_add_f32 v[200:201], v[200:201], v[202:203]\n" \
    "v_pk_add_f32 v[204:205], v[204:205], v[206:207]\n" \
    "s_nop 1\n" \
    "v_pk_add_f32 v[208:209], v[208:209], v[210:211]\n" \
    "v_pk_mul_f32 v[196:197], v[196:197], v[220:221]\n" \
    "v_pk_mul_f32 v[200:201], v[200:201], v[220:221]\n" \
    "v_pk_mul_f32 v[204:205], v[204:205], v[220:221]\n" \
    "v_pk_mul_f32 v[208:209], v[208:209], v[220:221]\n" \
    "v_cvt_pk_bf16_f32 v216, v196, v200\n" \
    "v_cvt_pk_bf16_f32 v217, v204, v208\n" \
    "v_cvt_pk_bf16_f32 v218, v197, v201\n" \
    "v_cvt_pk_bf16_f32 v219, v205, v209\n" \
    "buffer_store_dwordx4 v[216:219], v226, s[40:43], s60 offen sc1\n" \
    "s_setprio 0\n" \
    "buffer_store_dword v143, v233, s[44:47], 0 offen sc1\n" \
    "ds_read_b64 v[220:221], v229 offset:64\n" \
    "s_mov_b32 s79, s60\n" \
    "s_mov_b32 s60, s61\n" \
    "s_mov_b32 s61, s62\n" \
    "s_mov_b32 s62, s63\n" \
    "s_mov_b32 s63, s79\n" \
    "s_add_u32 s76, s76, 1\n" \
    "s_add_u32 s77, s77, s65\n" \
    "s_add_u32 s84, s84, 0x20000\n" \
    "s_add_u32 s81, s81, s65\n" \
    "s_waitcnt lgkmcnt(0)\n" \
    "v_readfirstlane_b32 s65, v220\n" \
    "v_readfirstlane_b32 s71, v221\n" \
    "s_mul_i32 s96, s65, s71\n" \
    "s_lshr_b32 s96, s96, 4\n" \
    "s_branch L%=_Tloop\n" \
    "L%=_Tslow:\n" \
    "s_nop 7\n" \
    "s_nop 0\n" \
    "v_max_u32 v138, v104, v105\n" \
    "v_max3_u32 v138, v138, v106, v107\n" \
    "v_max3_u32 v138, v138, v108, v109\n" \
    "v_max3_u32 v138, v138, v110, v111\n" \
    "v_max3_u32 v138, v138, v112, v113\n" \
    "v_max3_u32 v138, v138, v114, v115\n" \
    "v_max3_u32 v138, v138, v116, v117\n" \
    "v_max3_u32 v138, v138, v118, v119\n" \
    "v_max3_u32 v138, v138, v120, v121\n" \
    "v_max3_u32 v138, v138, v122, v123\n" \
    "v_max3_u32 v138, v138, v124, v125\n" \
    "v_max3_u32 v138, v138, v126, v127\n" \
    "v_cmp_eq_u32 vcc, -1, v138\n" \
    "s_cbranch_vccz L%=_Tok\n" \
    "s_cmp_eq_u32 s99, 0\n" \
    "s_cbranch_scc1 L%=_Tok\n" \
    "s_sub_u32 s99, s99, 1\n" \
    "s_cmp_lg_u32 s99, 0\n" \
    "s_cbranch_scc1 L%=_Tu5\n" \
    "v_mov_b32 v138, 1\n" \
    "buffer_store_dword v138, off, s[44:47], 0 sc1\n" \
    "L%=_Tu5:\n" \
    "s_sleep 1\n" \
    "buffer_load_dwordx4 v[104:107], v230, s[40:43], s60 offen offset:0 sc1\n" \
    "buffer_load_dwordx2 v[120:121], v231, s[40:43], s60 offen offset:0 sc1\n" \
    "buffer_load_dwordx4 v[108:111], v230, s[40:43], s60 offen offset:2048 sc1\n" \
    "buffer_load_dwordx2 v[122:123], v231, s[40:43], s60 offen offset:2048 sc1\n" \
    "buffer_load_dwordx4 v[112:115], v230, s[40:43], s85 offen offset:0 sc1\n" \
    "buffer_load_dwordx2 v[124:125], v231, s[40:43], s85 offen offset:0 sc1\n" \
    "buffer_load_dwordx4 v[116:119], v230, s[40:43], s85 offen offset:2048 sc1\n" \
    "buffer_load_dwordx2 v[126:127], v231, s[40:43], s85 offen offset:2048 sc1\n" \
    "buffer_load_dwordx4 v[168:171], v225, s[40:43], s60 offen sc1\n" \
    "buffer_load_dwordx4 v[172:175], v225, s[40:43], s85 offen sc1\n" \
    "v_mov_b64 v[176:177], 0\n" \
    "v_mov_b64 v[178:179], 0\n" \
    "v_mov_b64 v[180:181], 0\n" \
    "v_mov_b64 v[182:183], 0\n" \
    "v_mov_b64 v[184:185], 0\n" \
    "v_mov_b64 v[186:187], 0\n" \
    "v_mov_b64 v[188:189], 0\n" \
    "v_mov_b64 v[190:191], 0\n" \
    "s_branch L%=_Tsc\n" \
    "L%=_Tvslow:\n" \
    "s_nop 7\n" \
    "s_nop 0\n" \
    "v_max_u32 v136, v168, v169\n" \
    "v_max3_u32 v136, v136, v170, v171\n" \
    "v_max3_u32 v136, v136, v172, v173\n" \
    "v_max3_u32 v136, v136, v174, v175\n" \
    "v_cmp_eq_u32 vcc, -1, v136\n" \
    "s_cbranch_vccz L%=_Tvok\n" \
    "s_cmp_eq_u32 s99, 0\n" \
    "s_cbranch_scc1 L%=_Tvok\n" \
    "s_sub_u32 s99, s99, 1\n" \
    "s_cmp_lg_u32 s99, 0\n" \
    "s_cbranch_scc1 L%=_Tu6\n" \
    "v_mov_b32 v136, 1\n" \
    "buffer_store_dword v136, off, s[44:47], 0 sc1\n" \
    "L%=_Tu6:\n" \
    "s_sleep 1\n" \
    "buffer_load_dwordx4 v[168:171], v225, s[40:43], s60 offen sc1\n" \
    "buffer_load_dwordx4 v[172:175], v225, s[40:43], s85 offen sc1\n" \
    "v_mov_b64 v[176:177], 0\n" \
    "v_mov_b64 v[178:179], 0\n" \
    "v_mov_b64 v[180:181], 0\n" \
    "v_mov_b64 v[182:183], 0\n" \
    "v_mov_b64 v[184:185], 0\n" \
    "v_mov_b64 v[186:187], 0\n" \
    "v_mov_b64 v[188:189], 0\n" \
    "v_mov_b64 v[190:191], 0\n" \
    "v_mov_b64 v[196:197], 0\n" \
    "v_mov_b64 v[198:199], 0\n" \
    "v_mov_b64 v[200:201], 0\n" \
    "v_mov_b64 v[202:203], 0\n" \
    "v_mov_b64 v[204:205], 0\n" \
    "v_mov_b64 v[206:207], 0\n" \
    "v_mov_b64 v[208:209], 0\n" \
    "v_mov_b64 v[210:211], 0\n" \
    "v_mov_b64 v[212:213], 0\n" \
    "v_mov_b64 v[214:215], 0\n" \
    "s_waitcnt vmcnt(0)\n" \
    "v_smfmac_f32_16x16x64_bf16 v[176:179], v[168:171], v[0:7], v224\n" \
    "v_smfmac_f32_16x16x64_bf16 v[180:183], v[168:171], v[8:15], v224\n" \
    "v_smfmac_f32_16x16x64_bf16 v[176:179], v[172:175], v[32:39], v224\n" \
    "s_nop 7\n" \
    "s_nop 0\n" \
    "s_branch L%=_Tvc\n" \
    "L%=_Tfin:\n" \
    "v_mov_b32 v143, 64\n" \
    "s_barrier\n" \
    "buffer_store_dword v143, v233, s[44:47], 0 offen sc1\n" \
    "s_branch L%=_end\n" \
    "L%=_Tu3s:\n" \
    "s_mov_b32 s77, s79\n" \
    "s_memtime s[38:39]\n" \
    "s_memrealtime vcc\n" \
    "s_nop 4\n" \
    "s_memrealtime s[32:33]\n" \
    "s_nop 4\n" \
    "s_memrealtime s[34:35]\n" \
    "s_nop 4\n" \
    "s_memrealtime s[36:37]\n" \
    "s_add_u32 s80, s77, s64\n" \
    "s_lshl_b32 s80, s80, 2\n" \
    "s_waitcnt lgkmcnt(0)\n" \
    "s_sub_u32 s80, s80, vcc_lo\n" \
    "s_sub_u32 s80, s80, s32\n" \
    "s_sub_u32 s80, s80, s34\n" \
    "s_sub_u32 s80, s80, s36\n" \
    "s_mul_i32 s80, s80, s71\n" \
    "s_ashr_i32 s80, s80, 6\n" \
    "s_add_u32 s78, s80, s38\n" \
    "s_branch L%=_Tu3k\n" \
    "L%=_Tu4s:\n" \
    "s_cmp_gt_u32 s79, 0x8000\n" \
    "s_cbranch_scc1 L%=_Tu4e\n" \
    "s_sleep 2\n" \
    "s_branch L%=_Tu4a\n" \
    "L%=_S:\n" \
    "s_mov_b32 s76, 0\n" \
    "s_mov_b32 s99, 0x20000\n" \
    "s_mov_b32 s84, 0\n" \
    "s_mov_b32 s81, 0\n" \
    "s_mov_b32 s86, 0\n" \
    "s_mov_b32 s87, 0\n" \
    "s_mov_b32 s88, 0\n" \
    "s_mov_b32 s89, 0\n" \
    "s_mov_b32 s90, 0\n" \
    "s_mov_b32 s91, 0\n" \
    "s_mov_b32 s92, 0\n" \
    "s_mov_b32 s93, 0\n" \
    "s_mov_b32 s94, 0\n" \
    "s_mov_b32 s95, 0\n" \
    "s_mov_b32 s96, 0\n" \
    "s_mov_b32 s97, 0\n" \
    "s_mov_b32 s98, 0\n" \
    "ds_read_b32 v216, v229\n" \
    "s_waitcnt lgkmcnt(0)\n" \
    "v_readfirstlane_b32 s94, v216\n" \
    "s_mul_i32 s79, s94, s70\n" \
    "s_add_u32 s79, s79, s81\n" \
    "s_mov_b32 s77, s79\n" \
    "s_mul_i32 s96, s65, s71\n" \
    "s_lshr_b32 s96, s96, 4\n" \
    "v_mov_b32 v96, 0x3f803f80\n" \
    "v_mov_b32 v97, 0x3f803f80\n" \
    "v_mov_b32 v98, 0x3f803f80\n" \
    "v_mov_b32 v99, 0x3f803f80\n" \
    "v_mov_b32 v100, 0x3f803f80\n" \
    "v_mov_b32 v101, 0x3f803f80\n" \
    "v_mov_b64 v[102:103], 0\n" \
    "v_mov_b64 v[64:65], 0\n" \
    "v_mov_b64 v[66:67], 0\n" \
    "v_mov_b64 v[68:69], 0\n" \
    "v_mov_b64 v[70:71], 0\n" \
    "v_mov_b64 v[72:73], 0\n" \
    "v_mov_b64 v[74:75], 0\n" \
    "v_mov_b64 v[76:77], 0\n" \
    "v_mov_b64 v[78:79], 0\n" \
    "v_mov_b64 v[80:81], 0\n" \
    "v_mov_b64 v[82:83], 0\n" \
    "v_mov_b64 v[84:85], 0\n" \
    "v_mov_b64 v[86:87], 0\n" \
    "v_mov_b64 v[88:89], 0\n" \
    "v_mov_b64 v[90:91], 0\n" \
    "v_mov_b64 v[92:93], 0\n" \
    "v_mov_b64 v[94:95], 0\n" \
    "v_mov_b64 v[192:193], 0\n" \
    "v_mov_b64 v[194:195], 0\n" \
    "L%=_Sloop:\n" \
    "ds_read_b64_tr_b16 v[104:105], v228 offset:16384\n" \
    "ds_read_b64_tr_b16 v[106:107], v228 offset:16416\n" \
    "ds_read_b64_tr_b16 v[108:109], v228 offset:18688\n" \
    "ds_read_b64_tr_b16 v[110:111], v228 offset:18720\n" \
    "ds_read_b64_tr_b16 v[112:113], v228 offset:20992\n" \
    "ds_read_b64_tr_b16 v[114:115], v228 offset:21024\n" \
    "ds_read_b64_tr_b16 v[116:117], v228 offset:23296\n" \
    "ds_read_b64_tr_b16 v[118:119], v228 offset:23328\n" \
    "ds_read_b64_tr_b16 v[120:121], v228 offset:16656\n" \
    "ds_read_b64_tr_b16 v[122:123], v228 offset:16688\n" \
    "ds_read_b64_tr_b16 v[124:125], v228 offset:18960\n" \
    "ds_read_b64_tr_b16 v[126:127], v228 offset:18992\n" \
    "ds_read_b64_tr_b16 v[128:129], v228 offset:21264\n" \
    "ds_read_b64_tr_b16 v[130:131], v228 offset:21296\n" \
    "ds_read_b64_tr_b16 v[132:133], v228 offset:23568\n" \
    "ds_read_b64_tr_b16 v[134:135], v228 offset:23600\n" \
    "s_waitcnt lgkmcnt(14)\n" \
    "v_mfma_f32_16x16x32_bf16 v[136:139], v[104:107], v[0:3], 0\n" \
    "v_mfma_f32_16x16x32_bf16 v[144:147], v[104:107], v[8:11], 0\n" \
    "v_mfma_f32_16x16x32_bf16 v[152:155], v[104:107], v[16:19], 0\n" \
    "v_mfma_f32_16x16x32_bf16 v[160:163], v[104:107], v[24:27], 0\n" \
    "s_waitcnt lgkmcnt(12)\n" \
    "v_mfma_f32_16x16x32_bf16 v[136:139], v[108:111], v[4:7], v[136:139]\n" \
    "v_mfma_f32_16x16x32_bf16 v[144:147], v[108:111], v[12:15], v[144:147]\n" \
    "v_mfma_f32_16x16x32_bf16 v[152:155], v[108:111], v[20:23], v[152:155]\n" \
    "v_mfma_f32_16x16x32_bf16 v[160:163], v[108:111], v[28:31], v[160:163]\n" \
    "s_waitcnt lgkmcnt(10)\n" \
    "v_mfma_f32_16x16x32_bf16 v[136:139], v[112:115], v[32:35], v[136:139]\n" \
    "v_mfma_f32_16x16x32_bf16 v[144:147], v[112:115], v[40:43], v[144:147]\n" \
    "v_mfma_f32_16x16x32_bf16 v[152:155], v[112:115], v[48:51], v[152:155]\n" \
    "v_mfma_f32_16x16x32_bf16 v[160:163], v[112:115], v[56:59], v[160:163]\n" \
    "s_waitcnt lgkmcnt(8)\n" \
    "v_mfma_f32_16x16x32_bf16 v[136:139], v[116:119], v[36:39], v[136:139]\n" \
    "v_mfma_f32_16x16x32_bf16 v[144:147], v[116:119], v[44:47], v[144:147]\n" \
    "v_mfma_f32_16x16x32_bf16 v[152:155], v[116:119], v[52:55], v[152:155]\n" \
    "v_mfma_f32_16x16x32_bf16 v[160:163], v[116:119], v[60:63], v[160:163]\n" \
    "s_waitcnt lgkmcnt(6)\n" \
    "v_mfma_f32_16x16x32_bf16 v[140:143], v[120:123], v[0:3], 0\n" \
    "v_mfma_f32_16x16x32_bf16 v[148:151], v[120:123], v[8:11], 0\n" \
    "v_mfma_f32_16x16x32_bf16 v[156:159], v[120:123], v[16:19], 0\n" \
    "v_mfma_f32_16x16x32_bf16 v[164:167], v[120:123], v[24:27], 0\n" \
    "s_waitcnt lgkmcnt(4)\n" \
    "v_mfma_f32_16x16x32_bf16 v[140:143], v[124:127], v[4:7], v[140:143]\n" \
    "v_mfma_f32_16x16x32_bf16 v[148:151], v[124:127], v[12:15], v[148:151]\n" \
    "v_mfma_f32_16x16x32_bf16 v[156:159], v[124:127], v[20:23], v[156:159]\n" \
    "v_mfma_f32_16x16x32_bf16 v[164:167], v[124:127], v[28:31], v[164:167]\n" \
    "s_waitcnt lgkmcnt(2)\n" \
    "v_mfma_f32_16x16x32_bf16 v[140:143], v[128:131], v[32:35], v[140:143]\n" \
    "v_mfma_f32_16x16x32_bf16 v[148:151], v[128:131], v[40:43], v[148:151]\n" \
    "v_mfma_f32_16x16x32_bf16 v[156:159], v[128:131], v[48:51], v[156:159]\n" \
    "v_mfma_f32_16x16x32_bf16 v[164:167], v[128:131], v[56:59], v[164:167]\n" \
    "s_waitcnt lgkmcnt(0)\n" \
    "v_mfma_f32_16x16x32_bf16 v[140:143], v[132:135], v[36:39], v[140:143]\n" \
    "v_mfma_f32_16x16x32_bf16 v[148:151], v[132:135], v[44:47], v[148:151]\n" \
    "v_mfma_f32_16x16x32_bf16 v[156:159], v[132:135], v[52:55], v[156:159]\n" \
    "v_mfma_f32_16x16x32_bf16 v[164:167], v[132:135], v[60:63], v[164:167]\n" \
    "v_cvt_pk_bf16_f32 v65, v136, v137\n" \
    "v_cvt_pk_bf16_f32 v68, v138, v139\n" \
    "v_cvt_pk_bf16_f32 v73, v144, v145\n" \
    "v_cvt_pk_bf16_f32 v76, v146, v147\n" \
    "v_cvt_pk_bf16_f32 v81, v152, v153\n" \
    "v_cvt_pk_bf16_f32 v84, v154, v155\n" \
    "v_cvt_pk_bf16_f32 v89, v160, v161\n" \
    "v_cvt_pk_bf16_f32 v92, v162, v163\n" \
    "v_cvt_pk_bf16_f32 v66, v142, v143\n" \
    "v_cvt_pk_bf16_f32 v67, v142, v143\n" \
    "v_cvt_pk_bf16_f32 v69, v140, v141\n" \
    "v_cvt_pk_bf16_f32 v74, v150, v151\n" \
    "v_cvt_pk_bf16_f32 v75, v150, v151\n" \
    "v_cvt_pk_bf16_f32 v77, v148, v149\n" \
    "v_cvt_pk_bf16_f32 v82, v158, v159\n" \
    "v_cvt_pk_bf16_f32 v83, v158, v159\n" \
    "v_cvt_pk_bf16_f32 v85, v156, v157\n" \
    "v_cvt_pk_bf16_f32 v90, v166, v167\n" \
    "v_cvt_pk_bf16_f32 v91, v166, v167\n" \
    "v_cvt_pk_bf16_f32 v93, v164, v165\n" \
    "v_permlane16_swap_b32 v66, v67\n" \
    "v_permlane16_swap_b32 v74, v75\n" \
    "v_permlane16_swap_b32 v82, v83\n" \
    "v_permlane16_swap_b32 v90, v91\n" \
    "ds_read_b128 v[240:243], v234 offset:25600\n" \
    "ds_read_b128 v[244:247], v234 offset:26624\n" \
    "v_mov_b64 v[176:177], 0\n" \
    "v_mov_b64 v[178:179], 0\n" \
    "v_mov_b64 v[180:181], 0\n" \
    "v_mov_b64 v[182:183], 0\n" \
    "v_mov_b64 v[184:185], 0\n" \
    "v_mov_b64 v[186:187], 0\n" \
    "v_mov_b64 v[188:189], 0\n" \
    "v_mov_b64 v[190:191], 0\n" \
    "v_mov_b64 v[196:197], 0\n" \
    "v_mov_b64 v[198:199], 0\n" \
    "v_mov_b64 v[200:201], 0\n" \
    "v_mov_b64 v[202:203], 0\n" \
    "v_mov_b64 v[204:205], 0\n" \
    "v_mov_b64 v[206:207], 0\n" \
    "v_mov_b64 v[208:209], 0\n" \
    "v_mov_b64 v[210:211], 0\n" \
    "v_mov_b64 v[212:213], 0\n" \
    "v_mov_b64 v[214:215], 0\n" \
    "s_add_u32 s85, s60, 0x1000\n" \
    "s_branch L%=_Su2b\n" \
    "L%=_Su2a:\n" \
    "s_sleep 4\n" \
    "L%=_Su2b:\n" \
    "s_memrealtime vcc\n" \
    "s_add_u32 s80, s77, s64\n" \
    "s_add_u32 s80, s80, s69\n" \
    "s_waitcnt lgkmcnt(0)\n" \
    "s_sub_i32 s79, s80, vcc_lo\n" \
    "s_sub_u32 s79, s79, 40\n" \
    "s_cmp_lt_u32 s79, 0x400\n" \
    "s_cbranch_scc1 L%=_Su2a\n" \
    "s_memtime s[38:39]\n" \
    "s_memrealtime vcc\n" \
    "s_nop 4\n" \
    "s_memrealtime s[32:33]\n" \
    "s_nop 4\n" \
    "s_memrealtime s[34:35]\n" \
    "s_nop 4\n" \
    "s_memrealtime s[36:37]\n" \
    "s_add_u32 s80, s77, s64\n" \
    "s_lshl_b32 s80, s80, 2\n" \
    "s_waitcnt lgkmcnt(0)\n" \
    "s_sub_u32 s80, s80, vcc_lo\n" \
    "s_sub_u32 s80, s80, s32\n" \
    "s_sub_u32 s80, s80, s34\n" \
    "s_sub_u32 s80, s80, s36\n" \
    "s_mul_i32 s80, s80, s71\n" \
    "s_ashr_i32 s80, s80, 6\n" \
    "s_add_u32 s78, s80, s38\n" \
    "L%=_Su3a:\n" \
    "s_memtime s[38:39]\n" \
    "s_mul_i32 s80, s69, 23\n" \
    "s_add_u32 s80, s80, s78\n" \
    "s_waitcnt lgkmcnt(0)\n" \
    "s_sub_i32 s79, s80, s38\n" \
    "s_max_i32 s79, s79, 0\n" \
    "s_cmp_gt_u32 s79, 320\n" \
    "s_cbranch_scc1 L%=_Su3s\n" \
    ".p2align 4\n" \
    "L%=_Su3d:\n" \
    "s_sub_u32 s79, s79, 28\n" \
    "s_cbranch_scc0 L%=_Su3d\n" \
    "L%=_Su3e:\n" \
    "s_sub_u32 s79, s84, 0x20000\n" \
    "s_cselect_b32 s79, 0x40000000, s79\n" \
    "buffer_store_dwordx4 v[236:239], v233, s[48:51], s79 offen sc1\n" \
    "s_setprio 3\n" \
    "s_barrier\n" \
    "L%=_Srd:\n" \
    "ds_read_b128 v[136:139], v229 offset:32\n" \
    "ds_read_b128 v[104:107], v230 offset:0\n" \
    "ds_read_b128 v[108:111], v230 offset:2176\n" \
    "ds_read_b128 v[112:115], v230 offset:4352\n" \
    "ds_read_b128 v[116:119], v230 offset:6528\n" \
    "ds_read_b128 v[120:123], v230 offset:1088\n" \
    "ds_read_b128 v[124:127], v230 offset:3264\n" \
    "ds_read_b128 v[128:131], v230 offset:5440\n" \
    "ds_read_b128 v[132:135], v230 offset:7616\n" \
    "s_waitcnt lgkmcnt(8)\n" \
    "v_min3_u32 v136, v136, v137, v138\n" \
    "v_min_u32 v136, v136, v139\n" \
    "v_cmp_ge_u32 vcc, s76, v136\n" \
    "s_cbranch_vccnz L%=_Srd\n" \
    "s_waitcnt lgkmcnt(6)\n" \
    "v_pk_add_f32 v[104:105], v[104:105], v[108:109]\n" \
    "v_pk_add_f32 v[106:107], v[106:107], v[110:111]\n" \
    "s_waitcnt lgkmcnt(4)\n" \
    "v_pk_add_f32 v[112:113], v[112:113], v[116:117]\n" \
    "v_pk_add_f32 v[114:115], v[114:115], v[118:119]\n" \
    "v_pk_add_f32 v[104:105], v[104:105], v[112:113]\n" \
    "v_pk_add_f32 v[106:107], v[106:107], v[114:115]\n" \
    "v_cvt_pk_bf16_f32 v236, v104, v105\n" \
    "v_cvt_pk_bf16_f32 v237, v106, v107\n" \
    "s_waitcnt lgkmcnt(2)\n" \
    "v_pk_add_f32 v[120:121], v[120:121], v[124:125]\n" \
    "v_pk_add_f32 v[122:123], v[122:123], v[126:127]\n" \
    "s_waitcnt lgkmcnt(0)\n" \
    "v_pk_add_f32 v[128:129], v[128:129], v[132:133]\n" \
    "v_pk_add_f32 v[130:131], v[130:131], v[134:135]\n" \
    "v_pk_add_f32 v[120:121], v[120:121], v[128:129]\n" \
    "v_pk_add_f32 v[122:123], v[122:123], v[130:131]\n" \
    "v_cvt_pk_bf16_f32 v238, v120, v121\n" \
    "v_cvt_pk_bf16_f32 v239, v122, v123\n" \
    "s_nop 1\n" \
    "v_mfma_f32_16x16x32_bf16 v[140:143], v[240:243], v[236:239], 0\n" \
    "v_mfma_f32_16x16x32_bf16 v[144:147], v[244:247], v[236:239], 0\n" \
    "v_mfma_f32_16x16x32_bf16 v[148:151], v[236:239], v[236:239], 0\n" \
    "buffer_store_dwordx4 v[140:143], v231, s[40:43], s60 offen sc1\n" \
    "s_nop 2\n" \
    "buffer_store_dwordx4 v[144:147], v231, s[40:43], s60 offen offset:512 sc1\n" \
    "s_nop 2\n" \
    "buffer_store_dwordx4 v[148:151], v232, s[40:43], s60 offen sc1\n" \
    "s_cmp_eq_u32 s76, 63\n" \
    "s_cbranch_scc1 L%=_Sfin\n" \
    "ds_read_b32 v216, v229\n" \
    "s_waitcnt lgkmcnt(0)\n" \
    "v_readfirstlane_b32 s94, v216\n" \
    "s_mul_i32 s79, s94, s70\n" \
    "s_add_u32 s79, s79, s81\n" \
    "s_cmp_lg_u32 s79, s77\n" \
    "s_cbranch_scc1 L%=_Su4s\n" \
    "L%=_Su4k:\n" \
    "L%=_Su5a:\n" \
    "s_memtime s[38:39]\n" \
    "s_mul_hi_u32 s80, s67, s96\n" \
    "s_add_u32 s80, s80, s78\n" \
    "s_waitcnt lgkmcnt(0)\n" \
    "s_sub_i32 s79, s80, s38\n" \
    "s_max_i32 s79, s79, 0\n" \
    "s_cmp_gt_u32 s79, 320\n" \
    "s_cbranch_scc1 L%=_Su5s\n" \
    ".p2align 4\n" \
    "L%=_Su5d:\n" \
    "s_sub_u32 s79, s79, 28\n" \
    "s_cbranch_scc0 L%=_Su5d\n" \
    "L%=_Su5e:\n" \
    "buffer_load_dwordx4 v[168:171], v225, s[40:43], s60 offen sc1\n" \
    "buffer_load_dwordx4 v[172:175], v225, s[40:43], s85 offen sc1\n" \
    "L%=_Svc:\n" \
    "s_waitcnt vmcnt(1)\n" \
    "v_smfmac_f32_16x16x64_bf16 v[176:179], v[168:171], v[0:7], v224\n" \
    "v_smfmac_f32_16x16x64_bf16 v[180:183], v[168:171], v[8:15], v224\n" \
    "s_waitcnt vmcnt(0)\n" \
    "v_smfmac_f32_16x16x64_bf16 v[176:179], v[172:175], v[32:39], v224\n" \
    "v_smfmac_f32_16x16x64_bf16 v[180:183], v[172:175], v[40:47], v224\n" \
    "v_smfmac_f32_16x16x64_bf16 v[184:187], v[168:171], v[16:23], v224\n" \
    "v_smfmac_f32_16x16x64_bf16 v[188:191], v[168:171], v[24:31], v224\n" \
    "v_add_f32 v176, v176, v178\n" \
    "v_smfmac_f32_16x16x64_bf16 v[184:187], v[172:175], v[48:55], v224\n" \
    "v_add_f32 v177, v177, v179\n" \
    "v_smfmac_f32_16x16x64_bf16 v[188:191], v[172:175], v[56:63], v224\n" \
    "v_cmp_u_f32 vcc, v176, v177\n" \
    "v_cvt_pk_bf16_f32 v64, v176, v177\n" \
    "v_add_f32 v180, v180, v182\n" \
    "v_add_f32 v181, v181, v183\n" \
    "v_cvt_pk_bf16_f32 v72, v180, v181\n" \
    "s_cbranch_vccnz L%=_Svslow\n" \
    "L%=_Svok:\n" \
    "v_add_f32 v184, v184, v186\n" \
    "v_add_f32 v185, v185, v187\n" \
    "v_cvt_pk_bf16_f32 v80, v184, v185\n" \
    "v_add_f32 v188, v188, v190\n" \
    "v_add_f32 v189, v189, v191\n" \
    "v_cvt_pk_bf16_f32 v88, v188, v189\n" \
    "s_barrier\n" \
    "L%=_Sfr:\n" \
    "ds_read_b128 v[192:195], v227\n" \
    "s_waitcnt lgkmcnt(0)\n" \
    "v_smfmac_f32_16x16x64_bf16 v[212:215], v[192:195], v[96:103], v224\n" \
    "v_cmp_ne_u32 vcc, s76, v195\n" \
    "v_smfmac_f32_16x16x64_bf16 v[196:199], v[192:195], v[64:71], v224\n" \
    "s_cbranch_vccnz L%=_Sfs\n" \
    "v_smfmac_f32_16x16x64_bf16 v[200:203], v[192:195], v[72:79], v224\n" \
    "v_smfmac_f32_16x16x64_bf16 v[204:207], v[192:195], v[80:87], v224\n" \
    "v_add_f32 v212, v212, v214\n" \
    "v_smfmac_f32_16x16x64_bf16 v[208:211], v[192:195], v[88:95], v224\n" \
    "v_add_f32 v213, v213, v215\n" \
    "v_rcp_f32 v220, v212\n" \
    "v_rcp_f32 v221, v213\n" \
    "v_add_f32 v196, v196, v198\n" \
    "v_add_f32 v197, v197, v199\n" \
    "v_pk_add_f32 v[200:201], v[200:201], v[202:203]\n" \
    "v_pk_add_f32 v[204:205], v[204:205], v[206:207]\n" \
    "s_nop 0\n" \
    "v_pk_add_f32 v[208:209], v[208:209], v[210:211]\n" \
    "v_pk_mul_f32 v[196:197], v[196:197], v[220:221]\n" \
    "v_pk_mul_f32 v[200:201], v[200:201], v[220:221]\n" \
    "v_pk_mul_f32 v[204:205], v[204:205], v[220:221]\n" \
    "v_pk_mul_f32 v[208:209], v[208:209], v[220:221]\n" \
    "v_cvt_pk_bf16_f32 v216, v196, v200\n" \
    "v_cvt_pk_bf16_f32 v217, v204, v208\n" \
    "v_cvt_pk_bf16_f32 v218, v197, v201\n" \
    "v_cvt_pk_bf16_f32 v219, v205, v209\n" \
    "buffer_store_dwordx4 v[216:219], v226, s[40:43], s60 offen sc1\n" \
    "s_setprio 0\n" \
    "ds_read_b64 v[220:221], v229 offset:64\n" \
    "s_mov_b32 s79, s60\n" \
    "s_mov_b32 s60, s61\n" \
    "s_mov_b32 s61, s62\n" \
    "s_mov_b32 s62, s63\n" \
    "s_mov_b32 s63, s79\n" \
    "s_add_u32 s76, s76, 1\n" \
    "s_add_u32 s77, s77, s65\n" \
    "s_add_u32 s84, s84, 0x20000\n" \
    "s_add_u32 s81, s81, s65\n" \
    "s_waitcnt lgkmcnt(0)\n" \
    "v_readfirstlane_b32 s65, v220\n" \
    "v_readfirstlane_b32 s71, v221\n" \
    "s_mul_i32 s96, s65, s71\n" \
    "s_lshr_b32 s96, s96, 4\n" \
    "s_branch L%=_Sloop\n" \
    "L%=_Sfin:\n" \
    "buffer_store_dwordx4 v[236:239], v233, s[48:51], s84 offen sc1\n" \
    "s_waitcnt vmcnt(0)\n" \
    "s_barrier\n" \
    "s_branch L%=_end\n" \
    "L%=_Su3s:\n" \
    "s_cmp_gt_u32 s79, 0x8000\n" \
    "s_cbranch_scc1 L%=_Su3e\n" \
    "s_sleep 2\n" \
    "s_branch L%=_Su3a\n" \
    "L%=_Su4s:\n" \
    "s_mov_b32 s77, s79\n" \
    "s_memtime s[38:39]\n" \
    "s_memrealtime vcc\n" \
    "s_nop 4\n" \
    "s_memrealtime s[32:33]\n" \
    "s_nop 4\n" \
    "s_memrealtime s[34:35]\n" \
    "s_nop 4\n" \
    "s_memrealtime s[36:37]\n" \
    "s_add_u32 s80, s77, s64\n" \
    "s_lshl_b32 s80, s80, 2\n" \
    "s_waitcnt lgkmcnt(0)\n" \
    "s_sub_u32 s80, s80, vcc_lo\n" \
    "s_sub_u32 s80, s80, s32\n" \
    "s_sub_u32 s80, s80, s34\n" \
    "s_sub_u32 s80, s80, s36\n" \
    "s_mul_i32 s80, s80, s71\n" \
    "s_ashr_i32 s80, s80, 6\n" \
    "s_add_u32 s78, s80, s38\n" \
    "s_branch L%=_Su4k\n" \
    "L%=_Su5s:\n" \
    "s_cmp_gt_u32 s79, 0x8000\n" \
    "s_cbranch_scc1 L%=_Su5e\n" \
    "s_sleep 2\n" \
    "s_branch L%=_Su5a\n" \
    "L%=_Svslow:\n" \
    "s_nop 7\n" \
    "s_nop 0\n" \
    "v_max_u32 v216, v168, v169\n" \
    "v_max3_u32 v216, v216, v170, v171\n" \
    "v_max3_u32 v216, v216, v172, v173\n" \
    "v_max3_u32 v216, v216, v174, v175\n" \
    "v_cmp_eq_u32 vcc, -1, v216\n" \
    "s_cbranch_vccz L%=_Svok\n" \
    "s_cmp_eq_u32 s99, 0\n" \
    "s_cbranch_scc1 L%=_Svok\n" \
    "s_sub_u32 s99, s99, 1\n" \
    "s_cmp_lg_u32 s99, 0\n" \
    "s_cbranch_scc1 L%=_Su6\n" \
    "v_mov_b32 v216, 1\n" \
    "buffer_store_dword v216, off, s[44:47], 0 sc1\n" \
    "L%=_Su6:\n" \
    "s_sleep 1\n" \
    "buffer_load_dwordx4 v[168:171], v225, s[40:43], s60 offen sc1\n" \
    "buffer_load_dwordx4 v[172:175], v225, s[40:43], s85 offen sc1\n" \
    "v_mov_b64 v[176:177], 0\n" \
    "v_mov_b64 v[178:179], 0\n" \
    "v_mov_b64 v[180:181], 0\n" \
    "v_mov_b64 v[182:183], 0\n" \
    "v_mov_b64 v[184:185], 0\n" \
    "v_mov_b64 v[186:187], 0\n" \
    "v_mov_b64 v[188:189], 0\n" \
    "v_mov_b64 v[190:191], 0\n" \
    "s_waitcnt vmcnt(0)\n" \
    "s_branch L%=_Svc\n" \
    "L%=_Sfs:\n" \
    "s_nop 7\n" \
    "s_nop 0\n" \
    "v_mov_b64 v[196:197], 0\n" \
    "v_mov_b64 v[198:199], 0\n" \
    "v_mov_b64 v[200:201], 0\n" \
    "v_mov_b64 v[202:203], 0\n" \
    "v_mov_b64 v[204:205], 0\n" \
    "v_mov_b64 v[206:207], 0\n" \
    "v_mov_b64 v[208:209], 0\n" \
    "v_mov_b64 v[210:211], 0\n" \
    "v_mov_b64 v[212:213], 0\n" \
    "v_mov_b64 v[214:215], 0\n" \
    "s_branch L%=_Sfr\n" \
    "L%=_R:\n" \
    "s_mov_b32 s76, 0\n" \
    "s_mov_b32 s99, 0x20000\n" \
    "s_mov_b32 s84, 0\n" \
    "s_mov_b32 s81, 0\n" \
    "s_mov_b32 s86, 0\n" \
    "s_mov_b32 s87, 0\n" \
    "s_mov_b32 s88, 0\n" \
    "s_mov_b32 s89, 0\n" \
    "s_mov_b32 s90, 0\n" \
    "s_mov_b32 s91, 0\n" \
    "s_mov_b32 s92, 0\n" \
    "s_mov_b32 s93, 0\n" \
    "s_mov_b32 s94, 0\n" \
    "s_mov_b32 s95, 0\n" \
    "s_mov_b32 s96, 0\n" \
    "s_mov_b32 s97, 0\n" \
    "s_mov_b32 s98, 0\n" \
    "ds_read_b32 v216, v229\n" \
    "s_waitcnt lgkmcnt(0)\n" \
    "v_readfirstlane_b32 s94, v216\n" \
    "s_mul_i32 s79, s94, s70\n" \
    "s_add_u32 s79, s79, s81\n" \
    "s_mov_b32 s77, s79\n" \
    "s_mul_i32 s96, s65, s71\n" \
    "s_lshr_b32 s96, s96, 4\n" \
    "v_mov_b32 v96, 0x3f803f80\n" \
    "v_mov_b32 v97, 0x3f803f80\n" \
    "v_mov_b32 v98, 0x3f803f80\n" \
    "v_mov_b32 v99, 0x3f803f80\n" \
    "v_mov_b32 v100, 0x3f803f80\n" \
    "v_mov_b32 v101, 0x3f803f80\n" \
    "v_mov_b64 v[102:103], 0\n" \
    "v_mov_b64 v[64:65], 0\n" \
    "v_mov_b64 v[66:67], 0\n" \
    "v_mov_b64 v[68:69], 0\n" \
    "v_mov_b64 v[70:71], 0\n" \
    "v_mov_b64 v[72:73], 0\n" \
    "v_mov_b64 v[74:75], 0\n" \
    "v_mov_b64 v[76:77], 0\n" \
    "v_mov_b64 v[78:79], 0\n" \
    "v_mov_b64 v[80:81], 0\n" \
    "v_mov_b64 v[82:83], 0\n" \
    "v_mov_b64 v[84:85], 0\n" \
    "v_mov_b64 v[86:87], 0\n" \
    "v_mov_b64 v[88:89], 0\n" \
    "v_mov_b64 v[90:91], 0\n" \
    "v_mov_b64 v[92:93], 0\n" \
    "v_mov_b64 v[94:95], 0\n" \
    "v_mov_b64 v[192:193], 0\n" \
    "v_mov_b64 v[194:195], 0\n" \
    "v_mov_b32 v222, v236\n" \
    "v_mov_b32 v223, 0\n" \
    "ds_read_b64_tr_b16 v[104:105], v228 offset:16384\n" \
    "ds_read_b64_tr_b16 v[106:107], v228 offset:16416\n" \
    "ds_read_b64_tr_b16 v[108:109], v228 offset:18688\n" \
    "ds_read_b64_tr_b16 v[110:111], v228 offset:18720\n" \
    "ds_read_b64_tr_b16 v[112:113], v228 offset:20992\n" \
    "ds_read_b64_tr_b16 v[114:115], v228 offset:21024\n" \
    "ds_read_b64_tr_b16 v[116:117], v228 offset:23296\n" \
    "ds_read_b64_tr_b16 v[118:119], v228 offset:23328\n" \
    "ds_read_b64_tr_b16 v[120:121], v228 offset:16656\n" \
    "ds_read_b64_tr_b16 v[122:123], v228 offset:16688\n" \
    "ds_read_b64_tr_b16 v[124:125], v228 offset:18960\n" \
    "ds_read_b64_tr_b16 v[126:127], v228 offset:18992\n" \
    "ds_read_b64_tr_b16 v[128:129], v228 offset:21264\n" \
    "ds_read_b64_tr_b16 v[130:131], v228 offset:21296\n" \
    "ds_read_b64_tr_b16 v[132:133], v228 offset:23568\n" \
    "ds_read_b64_tr_b16 v[134:135], v228 offset:23600\n" \
    "s_waitcnt lgkmcnt(14)\n" \
    "v_mfma_f32_16x16x32_bf16 v[136:139], v[104:107], v[0:3], 0\n" \
    "v_mfma_f32_16x16x32_bf16 v[144:147], v[104:107], v[8:11], 0\n" \
    "v_mfma_f32_16x16x32_bf16 v[152:155], v[104:107], v[16:19], 0\n" \
    "v_mfma_f32_16x16x32_bf16 v[160:163], v[104:107], v[24:27], 0\n" \
    "s_waitcnt lgkmcnt(12)\n" \
    "v_mfma_f32_16x16x32_bf16 v[136:139], v[108:111], v[4:7], v[136:139]\n" \
    "v_mfma_f32_16x16x32_bf16 v[144:147], v[108:111], v[12:15], v[144:147]\n" \
    "v_mfma_f32_16x16x32_bf16 v[152:155], v[108:111], v[20:23], v[152:155]\n" \
    "v_mfma_f32_16x16x32_bf16 v[160:163], v[108:111], v[28:31], v[160:163]\n" \
    "s_waitcnt lgkmcnt(10)\n" \
    "v_mfma_f32_16x16x32_bf16 v[136:139], v[112:115], v[32:35], v[136:139]\n" \
    "v_mfma_f32_16x16x32_bf16 v[144:147], v[112:115], v[40:43], v[144:147]\n" \
    "v_mfma_f32_16x16x32_bf16 v[152:155], v[112:115], v[48:51], v[152:155]\n" \
    "v_mfma_f32_16x16x32_bf16 v[160:163], v[112:115], v[56:59], v[160:163]\n" \
    "s_waitcnt lgkmcnt(8)\n" \
    "v_mfma_f32_16x16x32_bf16 v[136:139], v[116:119], v[36:39], v[136:139]\n" \
    "v_mfma_f32_16x16x32_bf16 v[144:147], v[116:119], v[44:47], v[144:147]\n" \
    "v_mfma_f32_16x16x32_bf16 v[152:155], v[116:119], v[52:55], v[152:155]\n" \
    "v_mfma_f32_16x16x32_bf16 v[160:163], v[116:119], v[60:63], v[160:163]\n" \
    "s_waitcnt lgkmcnt(6)\n" \
    "v_mfma_f32_16x16x32_bf16 v[140:143], v[120:123], v[0:3], 0\n" \
    "v_mfma_f32_16x16x32_bf16 v[148:151], v[120:123], v[8:11], 0\n" \
    "v_mfma_f32_16x16x32_bf16 v[156:159], v[120:123], v[16:19], 0\n" \
    "v_mfma_f32_16x16x32_bf16 v[164:167], v[120:123], v[24:27], 0\n" \
    "s_waitcnt lgkmcnt(4)\n" \
    "v_mfma_f32_16x16x32_bf16 v[140:143], v[124:127], v[4:7], v[140:143]\n" \
    "v_mfma_f32_16x16x32_bf16 v[148:151], v[124:127], v[12:15], v[148:151]\n" \
    "v_mfma_f32_16x16x32_bf16 v[156:159], v[124:127], v[20:23], v[156:159]\n" \
    "v_mfma_f32_16x16x32_bf16 v[164:167], v[124:127], v[28:31], v[164:167]\n" \
    "s_waitcnt lgkmcnt(2)\n" \
    "v_mfma_f32_16x16x32_bf16 v[140:143], v[128:131], v[32:35], v[140:143]\n" \
    "v_mfma_f32_16x16x32_bf16 v[148:151], v[128:131], v[40:43], v[148:151]\n" \
    "v_mfma_f32_16x16x32_bf16 v[156:159], v[128:131], v[48:51], v[156:159]\n" \
    "v_mfma_f32_16x16x32_bf16 v[164:167], v[128:131], v[56:59], v[164:167]\n" \
    "s_waitcnt lgkmcnt(0)\n" \
    "v_mfma_f32_16x16x32_bf16 v[140:143], v[132:135], v[36:39], v[140:143]\n" \
    "v_mfma_f32_16x16x32_bf16 v[148:151], v[132:135], v[44:47], v[148:151]\n" \
    "v_mfma_f32_16x16x32_bf16 v[156:159], v[132:135], v[52:55], v[156:159]\n" \
    "v_mfma_f32_16x16x32_bf16 v[164:167], v[132:135], v[60:63], v[164:167]\n" \
    "v_cvt_pk_bf16_f32 v65, v136, v137\n" \
    "v_cvt_pk_bf16_f32 v68, v138, v139\n" \
    "v_cvt_pk_bf16_f32 v73, v144, v145\n" \
    "v_cvt_pk_bf16_f32 v76, v146, v147\n" \
    "v_cvt_pk_bf16_f32 v81, v152, v153\n" \
    "v_cvt_pk_bf16_f32 v84, v154, v155\n" \
    "v_cvt_pk_bf16_f32 v89, v160, v161\n" \
    "v_cvt_pk_bf16_f32 v92, v162, v163\n" \
    "v_cvt_pk_bf16_f32 v66, v142, v143\n" \
    "v_cvt_pk_bf16_f32 v67, v142, v143\n" \
    "v_cvt_pk_bf16_f32 v69, v140, v141\n" \
    "v_cvt_pk_bf16_f32 v74, v150, v151\n" \
    "v_cvt_pk_bf16_f32 v75, v150, v151\n" \
    "v_cvt_pk_bf16_f32 v77, v148, v149\n" \
    "v_cvt_pk_bf16_f32 v82, v158, v159\n" \
    "v_cvt_pk_bf16_f32 v83, v158, v159\n" \
    "v_cvt_pk_bf16_f32 v85, v156, v157\n" \
    "v_cvt_pk_bf16_f32 v90, v166, v167\n" \
    "v_cvt_pk_bf16_f32 v91, v166, v167\n" \
    "v_cvt_pk_bf16_f32 v93, v164, v165\n" \
    "v_permlane16_swap_b32 v66, v67\n" \
    "v_permlane16_swap_b32 v74, v75\n" \
    "v_permlane16_swap_b32 v82, v83\n" \
    "v_permlane16_swap_b32 v90, v91\n" \
    "L%=_Rloop:\n" \
    "v_mov_b64 v[176:177], 0\n" \
    "v_mov_b64 v[178:179], 0\n" \
    "v_mov_b64 v[180:181], 0\n" \
    "v_mov_b64 v[182:183], 0\n" \
    "v_mov_b64 v[184:185], 0\n" \
    "v_mov_b64 v[186:187], 0\n" \
    "v_mov_b64 v[188:189], 0\n" \
    "v_mov_b64 v[190:191], 0\n" \
    "v_mov_b64 v[196:197], 0\n" \
    "v_mov_b64 v[198:199], 0\n" \
    "v_mov_b64 v[200:201], 0\n" \
    "v_mov_b64 v[202:203], 0\n" \
    "v_mov_b64 v[204:205], 0\n" \
    "v_mov_b64 v[206:207], 0\n" \
    "v_mov_b64 v[208:209], 0\n" \
    "v_mov_b64 v[210:211], 0\n" \
    "v_mov_b64 v[212:213], 0\n" \
    "v_mov_b64 v[214:215], 0\n" \
    "s_add_u32 s85, s60, 0x1000\n" \
    "s_branch L%=_Ru2b\n" \
    "L%=_Ru2a:\n" \
    "s_sleep 4\n" \
    "L%=_Ru2b:\n" \
    "s_memrealtime vcc\n" \
    "s_add_u32 s80, s77, s64\n" \
    "s_add_u32 s80, s80, s69\n" \
    "s_waitcnt lgkmcnt(0)\n" \
    "s_sub_i32 s79, s80, vcc_lo\n" \
    "s_sub_u32 s79, s79, 40\n" \
    "s_cmp_lt_u32 s79, 0x400\n" \
    "s_cbranch_scc1 L%=_Ru2a\n" \
    "s_memtime s[38:39]\n" \
    "s_memrealtime vcc\n" \
    "s_nop 4\n" \
    "s_memrealtime s[32:33]\n" \
    "s_nop 4\n" \
    "s_memrealtime s[34:35]\n" \
    "s_nop 4\n" \
    "s_memrealtime s[36:37]\n" \
    "s_add_u32 s80, s77, s64\n" \
    "s_lshl_b32 s80, s80, 2\n" \
    "s_waitcnt lgkmcnt(0)\n" \
    "s_sub_u32 s80, s80, vcc_lo\n" \
    "s_sub_u32 s80, s80, s32\n" \
    "s_sub_u32 s80, s80, s34\n" \
    "s_sub_u32 s80, s80, s36\n" \
    "s_mul_i32 s80, s80, s71\n" \
    "s_ashr_i32 s80, s80, 6\n" \
    "s_add_u32 s78, s80, s38\n" \
    "L%=_Ru3a:\n" \
    "s_memtime s[38:39]\n" \
    "s_mul_i32 s80, s69, 23\n" \
    "s_add_u32 s80, s80, s78\n" \
    "s_waitcnt lgkmcnt(0)\n" \
    "s_sub_i32 s79, s80, s38\n" \
    "s_max_i32 s79, s79, 0\n" \
    "s_cmp_gt_u32 s79, 320\n" \
    "s_cbranch_scc1 L%=_Ru3s\n" \
    ".p2align 4\n" \
    "L%=_Ru3d:\n" \
    "s_sub_u32 s79, s79, 28\n" \
    "s_cbranch_scc0 L%=_Ru3d\n" \
    "L%=_Ru3e:\n" \
    "s_sub_u32 s79, s84, 0x20000\n" \
    "s_cselect_b32 s79, 0x40000000, s79\n" \
    "buffer_store_dwordx4 v[236:239], v234, s[48:51], s79 offen sc1\n" \
    "s_setprio 3\n" \
    "s_barrier\n" \
    "s_nop 14\n" \
    "L%=_Rvs:\n" \
    "ds_read_b128 v[104:107], v229 offset:16\n" \
    "ds_read_b128 v[108:111], v230 offset:8704\n" \
    "ds_read_b128 v[112:115], v230 offset:9984\n" \
    "ds_read_b128 v[116:119], v230 offset:11264\n" \
    "ds_read_b128 v[120:123], v230 offset:12544\n" \
    "s_waitcnt lgkmcnt(4)\n" \
    "v_min3_u32 v104, v104, v105, v106\n" \
    "v_min_u32 v104, v104, v107\n" \
    "s_nop 0\n" \
    "v_readfirstlane_b32 s79, v104\n" \
    "s_cmp_le_u32 s79, s76\n" \
    "s_cbranch_scc1 L%=_Rvs\n" \
    "s_waitcnt lgkmcnt(2)\n" \
    "v_pk_add_f32 v[108:109], v[108:109], v[112:113]\n" \
    "v_pk_add_f32 v[110:111], v[110:111], v[114:115]\n" \
    "s_waitcnt lgkmcnt(0)\n" \
    "v_pk_add_f32 v[116:117], v[116:117], v[120:121]\n" \
    "v_pk_add_f32 v[118:119], v[118:119], v[122:123]\n" \
    "v_pk_add_f32 v[108:109], v[108:109], v[116:117]\n" \
    "v_pk_add_f32 v[110:111], v[110:111], v[118:119]\n" \
    "v_cvt_pk_bf16_f32 v124, v108, v109\n" \
    "v_cvt_pk_bf16_f32 v125, v110, v111\n" \
    "s_nop 0\n" \
    "v_mov_b32_dpp v126, v124 quad_perm:[1,0,3,2] row_mask:0xf bank_mask:0xf\n" \
    "v_mov_b32_dpp v127, v125 quad_perm:[1,0,3,2] row_mask:0xf bank_mask:0xf\n" \
    "buffer_store_dwordx4 v[124:127], v231, s[40:43], s60 offen sc1\n" \
    "ds_write_b64 v232, v[124:125]\n" \
    "ds_read_b64_tr_b16 v[236:237], v233\n" \
    "ds_read_b64_tr_b16 v[238:239], v233 offset:256\n" \
    "s_cmp_eq_u32 s76, 63\n" \
    "s_cbranch_scc1 L%=_Rfin\n" \
    "ds_read_b32 v216, v229\n" \
    "s_waitcnt lgkmcnt(0)\n" \
    "v_readfirstlane_b32 s94, v216\n" \
    "s_mul_i32 s79, s94, s70\n" \
    "s_add_u32 s79, s79, s81\n" \
    "s_cmp_lg_u32 s79, s77\n" \
    "s_cbranch_scc1 L%=_Ru4s\n" \
    "L%=_Ru4k:\n" \
    "L%=_Ru5a:\n" \
    "s_memtime s[38:39]\n" \
    "s_mul_hi_u32 s80, s67, s96\n" \
    "s_add_u32 s80, s80, s78\n" \
    "s_waitcnt lgkmcnt(0)\n" \
    "s_sub_i32 s79, s80, s38\n" \
    "s_max_i32 s79, s79, 0\n" \
    "s_cmp_gt_u32 s79, 320\n" \
    "s_cbranch_scc1 L%=_Ru5s\n" \
    ".p2align 4\n" \
    "L%=_Ru5d:\n" \
    "s_sub_u32 s79, s79, 28\n" \
    "s_cbranch_scc0 L%=_Ru5d\n" \
    "L%=_Ru5e:\n" \
    "buffer_load_dwordx4 v[168:171], v225, s[40:43], s60 offen sc1\n" \
    "buffer_load_dwordx4 v[172:175], v225, s[40:43], s85 offen sc1\n" \
    "s_memtime s[38:39]\n" \
    "s_memrealtime vcc\n" \
    "s_nop 4\n" \
    "s_memrealtime s[32:33]\n" \
    "s_nop 4\n" \
    "s_memrealtime s[34:35]\n" \
    "s_nop 4\n" \
    "s_memrealtime s[36:37]\n" \
    "s_add_u32 s80, s77, s64\n" \
    "s_lshl_b32 s80, s80, 2\n" \
    "s_waitcnt lgkmcnt(0)\n" \
    "s_sub_u32 s80, s80, vcc_lo\n" \
    "s_sub_u32 s80, s80, s32\n" \
    "s_sub_u32 s80, s80, s34\n" \
    "s_sub_u32 s80, s80, s36\n" \
    "s_mul_i32 s80, s80, s71\n" \
    "s_ashr_i32 s80, s80, 6\n" \
    "s_add_u32 s78, s80, s38\n" \
    "L%=_Rvc:\n" \
    "s_waitcnt vmcnt(1)\n" \
    "v_smfmac_f32_16x16x64_bf16 v[176:179], v[168:171], v[0:7], v224\n" \
    "v_smfmac_f32_16x16x64_bf16 v[180:183], v[168:171], v[8:15], v224\n" \
    "s_waitcnt vmcnt(0)\n" \
    "v_smfmac_f32_16x16x64_bf16 v[176:179], v[172:175], v[32:39], v224\n" \
    "v_smfmac_f32_16x16x64_bf16 v[180:183], v[172:175], v[40:47], v224\n" \
    "v_smfmac_f32_16x16x64_bf16 v[184:187], v[168:171], v[16:23], v224\n" \
    "v_smfmac_f32_16x16x64_bf16 v[188:191], v[168:171], v[24:31], v224\n" \
    "v_add_f32 v176, v176, v178\n" \
    "v_smfmac_f32_16x16x64_bf16 v[184:187], v[172:175], v[48:55], v224\n" \
    "v_add_f32 v177, v177, v179\n" \
    "v_smfmac_f32_16x16x64_bf16 v[188:191], v[172:175], v[56:63], v224\n" \
    "v_cmp_u_f32 vcc, v176, v177\n" \
    "v_cvt_pk_bf16_f32 v64, v176, v177\n" \
    "v_add_f32 v180, v180, v182\n" \
    "v_add_f32 v181, v181, v183\n" \
    "v_cvt_pk_bf16_f32 v72, v180, v181\n" \
    "s_cbranch_vccnz L%=_Rvslow\n" \
    "L%=_Rvok:\n" \
    "v_add_f32 v184, v184, v186\n" \
    "v_add_f32 v185, v185, v187\n" \
    "v_cvt_pk_bf16_f32 v80, v184, v185\n" \
    "v_add_f32 v188, v188, v190\n" \
    "v_add_f32 v189, v189, v191\n" \
    "v_cvt_pk_bf16_f32 v88, v188, v189\n" \
    "s_barrier\n" \
    "L%=_Rfr:\n" \
    "ds_read_b128 v[192:195], v227\n" \
    "s_waitcnt lgkmcnt(0)\n" \
    "v_smfmac_f32_16x16x64_bf16 v[212:215], v[192:195], v[96:103], v224\n" \
    "v_cmp_ne_u32 vcc, s76, v195\n" \
    "v_smfmac_f32_16x16x64_bf16 v[196:199], v[192:195], v[64:71], v224\n" \
    "s_cbranch_vccnz L%=_Rfs\n" \
    "v_smfmac_f32_16x16x64_bf16 v[200:203], v[192:195], v[72:79], v224\n" \
    "v_smfmac_f32_16x16x64_bf16 v[204:207], v[192:195], v[80:87], v224\n" \
    "v_add_f32 v212, v212, v214\n" \
    "v_smfmac_f32_16x16x64_bf16 v[208:211], v[192:195], v[88:95], v224\n" \
    "v_add_f32 v213, v213, v215\n" \
    "v_rcp_f32 v220, v212\n" \
    "v_rcp_f32 v221, v213\n" \
    "v_add_f32 v196, v196, v198\n" \
    "v_add_f32 v197, v197, v199\n" \
    "v_pk_add_f32 v[200:201], v[200:201], v[202:203]\n" \
    "v_pk_add_f32 v[204:205], v[204:205], v[206:207]\n" \
    "s_nop 0\n" \
    "v_pk_add_f32 v[208:209], v[208:209], v[210:211]\n" \
    "v_pk_mul_f32 v[196:197], v[196:197], v[220:221]\n" \
    "v_pk_mul_f32 v[200:201], v[200:201], v[220:221]\n" \
    "v_pk_mul_f32 v[204:205], v[204:205], v[220:221]\n" \
    "v_pk_mul_f32 v[208:209], v[208:209], v[220:221]\n" \
    "v_cvt_pk_bf16_f32 v216, v196, v200\n" \
    "v_cvt_pk_bf16_f32 v217, v204, v208\n" \
    "v_cvt_pk_bf16_f32 v218, v197, v201\n" \
    "v_cvt_pk_bf16_f32 v219, v205, v209\n" \
    "buffer_store_dwordx4 v[216:219], v226, s[40:43], s60 offen sc1\n" \
    "ds_read_b64_tr_b16 v[104:105], v228 offset:16384\n" \
    "ds_read_b64_tr_b16 v[106:107], v228 offset:16416\n" \
    "ds_read_b64_tr_b16 v[108:109], v228 offset:18688\n" \
    "ds_read_b64_tr_b16 v[110:111], v228 offset:18720\n" \
    "ds_read_b64_tr_b16 v[112:113], v228 offset:20992\n" \
    "ds_read_b64_tr_b16 v[114:115], v228 offset:21024\n" \
    "ds_read_b64_tr_b16 v[116:117], v228 offset:23296\n" \
    "ds_read_b64_tr_b16 v[118:119], v228 offset:23328\n" \
    "ds_read_b64_tr_b16 v[120:121], v228 offset:16656\n" \
    "ds_read_b64_tr_b16 v[122:123], v228 offset:16688\n" \
    "ds_read_b64_tr_b16 v[124:125], v228 offset:18960\n" \
    "ds_read_b64_tr_b16 v[126:127], v228 offset:18992\n" \
    "ds_read_b64_tr_b16 v[128:129], v228 offset:21264\n" \
    "ds_read_b64_tr_b16 v[130:131], v228 offset:21296\n" \
    "ds_read_b64_tr_b16 v[132:133], v228 offset:23568\n" \
    "ds_read_b64_tr_b16 v[134:135], v228 offset:23600\n" \
    "s_waitcnt lgkmcnt(14)\n" \
    "v_mfma_f32_16x16x32_bf16 v[136:139], v[104:107], v[0:3], 0\n" \
    "v_mfma_f32_16x16x32_bf16 v[144:147], v[104:107], v[8:11], 0\n" \
    "v_mfma_f32_16x16x32_bf16 v[152:155], v[104:107], v[16:19], 0\n" \
    "v_mfma_f32_16x16x32_bf16 v[160:163], v[104:107], v[24:27], 0\n" \
    "s_waitcnt lgkmcnt(12)\n" \
    "v_mfma_f32_16x16x32_bf16 v[136:139], v[108:111], v[4:7], v[136:139]\n" \
    "v_mfma_f32_16x16x32_bf16 v[144:147], v[108:111], v[12:15], v[144:147]\n" \
    "v_mfma_f32_16x16x32_bf16 v[152:155], v[108:111], v[20:23], v[152:155]\n" \
    "v_mfma_f32_16x16x32_bf16 v[160:163], v[108:111], v[28:31], v[160:163]\n" \
    "s_waitcnt lgkmcnt(10)\n" \
    "v_mfma_f32_16x16x32_bf16 v[136:139], v[112:115], v[32:35], v[136:139]\n" \
    "v_mfma_f32_16x16x32_bf16 v[144:147], v[112:115], v[40:43], v[144:147]\n" \
    "v_mfma_f32_16x16x32_bf16 v[152:155], v[112:115], v[48:51], v[152:155]\n" \
    "v_mfma_f32_16x16x32_bf16 v[160:163], v[112:115], v[56:59], v[160:163]\n" \
    "s_waitcnt lgkmcnt(8)\n" \
    "v_mfma_f32_16x16x32_bf16 v[136:139], v[116:119], v[36:39], v[136:139]\n" \
    "v_mfma_f32_16x16x32_bf16 v[144:147], v[116:119], v[44:47], v[144:147]\n" \
    "v_mfma_f32_16x16x32_bf16 v[152:155], v[116:119], v[52:55], v[152:155]\n" \
    "v_mfma_f32_16x16x32_bf16 v[160:163], v[116:119], v[60:63], v[160:163]\n" \
    "v_add_u32 v223, 1, v223\n" \
    "s_and_b32 s95, s94, 1\n" \
    "s_lshl_b32 s95, s95, 17\n" \
    "s_add_u32 s95, s95, s61\n" \
    "s_add_u32 s85, s95, 0x1000\n" \
    "s_xor_b32 s85, s95, 0x20000\n" \
    "L%=_Ru6a:\n" \
    "s_memtime s[38:39]\n" \
    "s_mul_hi_u32 s80, s68, s96\n" \
    "s_add_u32 s80, s80, s78\n" \
    "s_waitcnt lgkmcnt(0)\n" \
    "s_sub_i32 s79, s80, s38\n" \
    "s_max_i32 s79, s79, 0\n" \
    "s_cmp_gt_u32 s79, 320\n" \
    "s_cbranch_scc1 L%=_Ru6s\n" \
    ".p2align 4\n" \
    "L%=_Ru6d:\n" \
    "s_sub_u32 s79, s79, 28\n" \
    "s_cbranch_scc0 L%=_Ru6d\n" \
    "L%=_Ru6e:\n" \
    "buffer_load_dwordx4 v[176:179], v235, s[40:43], s60 offen offset:0 sc1\n" \
    "buffer_load_dwordx4 v[180:183], v235, s[40:43], s60 offen offset:1024 sc1\n" \
    "buffer_load_dwordx4 v[184:187], v235, s[40:43], s60 offen offset:2048 sc1\n" \
    "buffer_load_dwordx4 v[188:191], v235, s[40:43], s60 offen offset:3072 sc1\n" \
    "v_mfma_f32_16x16x32_bf16 v[140:143], v[120:123], v[0:3], 0\n" \
    "v_mfma_f32_16x16x32_bf16 v[148:151], v[120:123], v[8:11], 0\n" \
    "v_mfma_f32_16x16x32_bf16 v[156:159], v[120:123], v[16:19], 0\n" \
    "v_mfma_f32_16x16x32_bf16 v[164:167], v[120:123], v[24:27], 0\n" \
    "v_mfma_f32_16x16x32_bf16 v[140:143], v[124:127], v[4:7], v[140:143]\n" \
    "v_mfma_f32_16x16x32_bf16 v[148:151], v[124:127], v[12:15], v[148:151]\n" \
    "v_mfma_f32_16x16x32_bf16 v[156:159], v[124:127], v[20:23], v[156:159]\n" \
    "v_mfma_f32_16x16x32_bf16 v[164:167], v[124:127], v[28:31], v[164:167]\n" \
    "v_mfma_f32_16x16x32_bf16 v[140:143], v[128:131], v[32:35], v[140:143]\n" \
    "v_mfma_f32_16x16x32_bf16 v[148:151], v[128:131], v[40:43], v[148:151]\n" \
    "v_mfma_f32_16x16x32_bf16 v[156:159], v[128:131], v[48:51], v[156:159]\n" \
    "v_mfma_f32_16x16x32_bf16 v[164:167], v[128:131], v[56:59], v[164:167]\n" \
    "v_mfma_f32_16x16x32_bf16 v[140:143], v[132:135], v[36:39], v[140:143]\n" \
    "v_mfma_f32_16x16x32_bf16 v[148:151], v[132:135], v[44:47], v[148:151]\n" \
    "v_mfma_f32_16x16x32_bf16 v[156:159], v[132:135], v[52:55], v[156:159]\n" \
    "v_mfma_f32_16x16x32_bf16 v[164:167], v[132:135], v[60:63], v[164:167]\n" \
    "v_cvt_pk_bf16_f32 v65, v136, v137\n" \
    "v_cvt_pk_bf16_f32 v68, v138, v139\n" \
    "v_cvt_pk_bf16_f32 v73, v144, v145\n" \
    "v_cvt_pk_bf16_f32 v76, v146, v147\n" \
    "v_cvt_pk_bf16_f32 v81, v152, v153\n" \
    "v_cvt_pk_bf16_f32 v84, v154, v155\n" \
    "v_cvt_pk_bf16_f32 v89, v160, v161\n" \
    "v_cvt_pk_bf16_f32 v92, v162, v163\n" \
    "L%=_Rred:\n" \
    "s_waitcnt vmcnt(3)\n" \
    "v_mfma_f32_16x16x32_bf16 v[240:243], v[244:247], v[176:179], 0\n" \
    "s_waitcnt vmcnt(2)\n" \
    "v_mfma_f32_16x16x32_bf16 v[240:243], v[244:247], v[180:183], v[240:243]\n" \
    "s_waitcnt vmcnt(1)\n" \
    "v_mfma_f32_16x16x32_bf16 v[240:243], v[244:247], v[184:187], v[240:243]\n" \
    "s_waitcnt vmcnt(0)\n" \
    "v_mfma_f32_16x16x32_bf16 v[240:243], v[244:247], v[188:191], v[240:243]\n" \
    "s_nop 7\n" \
    "v_cmp_u_f32 vcc, v240, v240\n" \
    "v_add_f32_dpp v240, v240, v240 row_ror:8 row_mask:0xf bank_mask:0xf\n" \
    "v_add_f32_dpp v241, v241, v241 row_ror:8 row_mask:0xf bank_mask:0xf\n" \
    "v_add_f32_dpp v242, v242, v242 row_ror:8 row_mask:0xf bank_mask:0xf\n" \
    "v_add_f32_dpp v243, v243, v243 row_ror:8 row_mask:0xf bank_mask:0xf\n" \
    "v_cvt_pk_bf16_f32 v216, v240, v241\n" \
    "v_cvt_pk_bf16_f32 v217, v242, v243\n" \
    "s_cbranch_vccnz L%=_Rslow\n" \
    "L%=_Rok:\n" \
    "v_mov_b32_dpp v218, v216 quad_perm:[1,0,3,2] row_mask:0xf bank_mask:0xf\n" \
    "v_mov_b32_dpp v219, v217 quad_perm:[1,0,3,2] row_mask:0xf bank_mask:0xf\n" \
    "buffer_store_dwordx4 v[216:219], v222, s[40:43], s95 offen sc1\n" \
    "ds_write_b32 v229, v223 offset:4\n" \
    "buffer_store_dwordx4 v[216:219], v222, s[40:43], s85 offen sc1\n" \
    "s_setprio 0\n" \
    "v_cvt_pk_bf16_f32 v66, v142, v143\n" \
    "v_cvt_pk_bf16_f32 v67, v142, v143\n" \
    "v_cvt_pk_bf16_f32 v69, v140, v141\n" \
    "v_cvt_pk_bf16_f32 v74, v150, v151\n" \
    "v_cvt_pk_bf16_f32 v75, v150, v151\n" \
    "v_cvt_pk_bf16_f32 v77, v148, v149\n" \
    "v_cvt_pk_bf16_f32 v82, v158, v159\n" \
    "v_cvt_pk_bf16_f32 v83, v158, v159\n" \
    "v_cvt_pk_bf16_f32 v85, v156, v157\n" \
    "v_cvt_pk_bf16_f32 v90, v166, v167\n" \
    "v_cvt_pk_bf16_f32 v91, v166, v167\n" \
    "v_cvt_pk_bf16_f32 v93, v164, v165\n" \
    "v_permlane16_swap_b32 v66, v67\n" \
    "v_permlane16_swap_b32 v74, v75\n" \
    "v_permlane16_swap_b32 v82, v83\n" \
    "v_permlane16_swap_b32 v90, v91\n" \
    "ds_read_b64 v[220:221], v229 offset:64\n" \
    "s_mov_b32 s79, s60\n" \
    "s_mov_b32 s60, s61\n" \
    "s_mov_b32 s61, s62\n" \
    "s_mov_b32 s62, s63\n" \
    "s_mov_b32 s63, s79\n" \
    "s_add_u32 s76, s76, 1\n" \
    "s_add_u32 s77, s77, s65\n" \
    "s_add_u32 s84, s84, 0x20000\n" \
    "s_add_u32 s81, s81, s65\n" \
    "s_waitcnt lgkmcnt(0)\n" \
    "v_readfirstlane_b32 s65, v220\n" \
    "v_readfirstlane_b32 s71, v221\n" \
    "s_mul_i32 s96, s65, s71\n" \
    "s_lshr_b32 s96, s96, 4\n" \
    "s_branch L%=_Rloop\n" \
    "L%=_Rslow:\n" \
    "s_nop 7\n" \
    "s_nop 0\n" \
    "v_max_u32 v220, v176, v177\n" \
    "v_max3_u32 v220, v220, v178, v179\n" \
    "v_max3_u32 v220, v220, v180, v181\n" \
    "v_max3_u32 v220, v220, v182, v183\n" \
    "v_max3_u32 v220, v220, v184, v185\n" \
    "v_max3_u32 v220, v220, v186, v187\n" \
    "v_max3_u32 v220, v220, v188, v189\n" \
    "v_max3_u32 v220, v220, v190, v191\n" \
    "v_cmp_eq_u32 vcc, -1, v220\n" \
    "s_cbranch_vccz L%=_Rok\n" \
    "s_cmp_eq_u32 s99, 0\n" \
    "s_cbranch_scc1 L%=_Rok\n" \
    "s_sub_u32 s99, s99, 1\n" \
    "s_cmp_lg_u32 s99, 0\n" \
    "s_cbranch_scc1 L%=_Ru7\n" \
    "v_mov_b32 v220, 1\n" \
    "buffer_store_dword v220, off, s[44:47], 0 sc1\n" \
    "L%=_Ru7:\n" \
    "s_sleep 1\n" \
    "buffer_load_dwordx4 v[176:179], v235, s[40:43], s60 offen offset:0 sc1\n" \
    "buffer_load_dwordx4 v[180:183], v235, s[40:43], s60 offen offset:1024 sc1\n" \
    "buffer_load_dwordx4 v[184:187], v235, s[40:43], s60 offen offset:2048 sc1\n" \
    "buffer_load_dwordx4 v[188:191], v235, s[40:43], s60 offen offset:3072 sc1\n" \
    "s_branch L%=_Rred\n" \
    "L%=_Rfin:\n" \
    "s_waitcnt lgkmcnt(0)\n" \
    "buffer_store_dwordx4 v[236:239], v234, s[48:51], s84 offen sc1\n" \
    "s_waitcnt vmcnt(0)\n" \
    "s_barrier\n" \
    "s_branch L%=_end\n" \
    "L%=_Ru3s:\n" \
    "s_cmp_gt_u32 s79, 0x8000\n" \
    "s_cbranch_scc1 L%=_Ru3e\n" \
    "s_sleep 2\n" \
    "s_branch L%=_Ru3a\n" \
    "L%=_Ru4s:\n" \
    "s_mov_b32 s77, s79\n" \
    "s_memtime s[38:39]\n" \
    "s_memrealtime vcc\n" \
    "s_nop 4\n" \
    "s_memrealtime s[32:33]\n" \
    "s_nop 4\n" \
    "s_memrealtime s[34:35]\n" \
    "s_nop 4\n" \
    "s_memrealtime s[36:37]\n" \
    "s_add_u32 s80, s77, s64\n" \
    "s_lshl_b32 s80, s80, 2\n" \
    "s_waitcnt lgkmcnt(0)\n" \
    "s_sub_u32 s80, s80, vcc_lo\n" \
    "s_sub_u32 s80, s80, s32\n" \
    "s_sub_u32 s80, s80, s34\n" \
    "s_sub_u32 s80, s80, s36\n" \
    "s_mul_i32 s80, s80, s71\n" \
    "s_ashr_i32 s80, s80, 6\n" \
    "s_add_u32 s78, s80, s38\n" \
    "s_branch L%=_Ru4k\n" \
    "L%=_Ru5s:\n" \
    "s_cmp_gt_u32 s79, 0x8000\n" \
    "s_cbranch_scc1 L%=_Ru5e\n" \
    "s_sleep 2\n" \
    "s_branch L%=_Ru5a\n" \
    "L%=_Rvslow:\n" \
    "s_nop 7\n" \
    "s_nop 0\n" \
    "v_max_u32 v216, v168, v169\n" \
    "v_max3_u32 v216, v216, v170, v171\n" \
    "v_max3_u32 v216, v216, v172, v173\n" \
    "v_max3_u32 v216, v216, v174, v175\n" \
    "v_cmp_eq_u32 vcc, -1, v216\n" \
    "s_cbranch_vccz L%=_Rvok\n" \
    "s_cmp_eq_u32 s99, 0\n" \
    "s_cbranch_scc1 L%=_Rvok\n" \
    "s_sub_u32 s99, s99, 1\n" \
    "s_cmp_lg_u32 s99, 0\n" \
    "s_cbranch_scc1 L%=_Ru8\n" \
    "v_mov_b32 v216, 1\n" \
    "buffer_store_dword v216, off, s[44:47], 0 sc1\n" \
    "L%=_Ru8:\n" \
    "s_sleep 1\n" \
    "buffer_load_dwordx4 v[168:171], v225, s[40:43], s60 offen sc1\n" \
    "buffer_load_dwordx4 v[172:175], v225, s[40:43], s85 offen sc1\n" \
    "v_mov_b64 v[176:177], 0\n" \
    "v_mov_b64 v[178:179], 0\n" \
    "v_mov_b64 v[180:181], 0\n" \
    "v_mov_b64 v[182:183], 0\n" \
    "v_mov_b64 v[184:185], 0\n" \
    "v_mov_b64 v[186:187], 0\n" \
    "v_mov_b64 v[188:189], 0\n" \
    "v_mov_b64 v[190:191], 0\n" \
    "s_waitcnt vmcnt(0)\n" \
    "s_branch L%=_Rvc\n" \
    "L%=_Rfs:\n" \
    "s_nop 7\n" \
    "s_nop 0\n" \
    "v_mov_b64 v[196:197], 0\n" \
    "v_mov_b64 v[198:199], 0\n" \
    "v_mov_b64 v[200:201], 0\n" \
    "v_mov_b64 v[202:203], 0\n" \
    "v_mov_b64 v[204:205], 0\n" \
    "v_mov_b64 v[206:207], 0\n" \
    "v_mov_b64 v[208:209], 0\n" \
    "v_mov_b64 v[210:211], 0\n" \
    "v_mov_b64 v[212:213], 0\n" \
    "v_mov_b64 v[214:215], 0\n" \
    "s_branch L%=_Rfr\n" \
    "L%=_Ru6s:\n" \
    "s_cmp_gt_u32 s79, 0x8000\n" \
    "s_cbranch_scc1 L%=_Ru6e\n" \
    "s_sleep 2\n" \
    "s_branch L%=_Ru6a\n" \
    "L%=_P:\n" \
    "s_mov_b32 s76, 0\n" \
    "s_mov_b32 s99, 0x20000\n" \
    "s_mov_b32 s84, 0\n" \
    "s_mov_b32 s81, 0\n" \
    "s_mov_b32 s86, 0\n" \
    "s_mov_b32 s87, 0\n" \
    "s_mov_b32 s88, 0\n" \
    "s_mov_b32 s89, 0\n" \
    "s_mov_b32 s90, 0\n" \
    "s_mov_b32 s91, 0\n" \
    "s_mov_b32 s92, 0\n" \
    "s_mov_b32 s93, 0\n" \
    "s_mov_b32 s94, 0\n" \
    "s_mov_b32 s95, 0\n" \
    "s_mov_b32 s96, 0\n" \
    "s_mov_b32 s97, 0\n" \
    "s_mov_b32 s98, 0\n" \
    "ds_read_b32 v216, v229\n" \
    "s_waitcnt lgkmcnt(0)\n" \
    "v_readfirstlane_b32 s94, v216\n" \
    "s_mul_i32 s79, s94, s70\n" \
    "s_add_u32 s79, s79, s81\n" \
    "s_mov_b32 s77, s79\n" \
    "s_mul_i32 s96, s65, s71\n" \
    "s_lshr_b32 s96, s96, 4\n" \
    "v_mov_b32 v96, 0x3f803f80\n" \
    "v_mov_b32 v97, 0x3f803f80\n" \
    "v_mov_b32 v98, 0x3f803f80\n" \
    "v_mov_b32 v99, 0x3f803f80\n" \
    "v_mov_b32 v100, 0x3f803f80\n" \
    "v_mov_b32 v101, 0x3f803f80\n" \
    "v_mov_b64 v[102:103], 0\n" \
    "v_mov_b64 v[64:65], 0\n" \
    "v_mov_b64 v[66:67], 0\n" \
    "v_mov_b64 v[68:69], 0\n" \
    "v_mov_b64 v[70:71], 0\n" \
    "v_mov_b64 v[72:73], 0\n" \
    "v_mov_b64 v[74:75], 0\n" \
    "v_mov_b64 v[76:77], 0\n" \
    "v_mov_b64 v[78:79], 0\n" \
    "v_mov_b64 v[80:81], 0\n" \
    "v_mov_b64 v[82:83], 0\n" \
    "v_mov_b64 v[84:85], 0\n" \
    "v_mov_b64 v[86:87], 0\n" \
    "v_mov_b64 v[88:89], 0\n" \
    "v_mov_b64 v[90:91], 0\n" \
    "v_mov_b64 v[92:93], 0\n" \
    "v_mov_b64 v[94:95], 0\n" \
    "v_mov_b64 v[192:193], 0\n" \
    "v_mov_b64 v[194:195], 0\n" \
    "v_cvt_f32_u32 v240, s71\n" \
    "v_mov_b32 v241, 1.0\n" \
    "v_mov_b32 v242, 0\n" \
    "v_mov_b32 v243, s65\n" \
    "s_mov_b32 s74, s94\n" \
    "v_mov_b32 v244, -1\n" \
    "v_mov_b32 v245, -1\n" \
    "v_mov_b32 v246, -1\n" \
    "v_mov_b32 v247, -1\n" \
    "L%=_Ploop:\n" \
    "ds_read_b64_tr_b16 v[104:105], v228 offset:16384\n" \
    "ds_read_b64_tr_b16 v[106:107], v228 offset:16416\n" \
    "ds_read_b64_tr_b16 v[108:109], v228 offset:18688\n" \
    "ds_read_b64_tr_b16 v[110:111], v228 offset:18720\n" \
    "ds_read_b64_tr_b16 v[112:113], v228 offset:20992\n" \
    "ds_read_b64_tr_b16 v[114:115], v228 offset:21024\n" \
    "ds_read_b64_tr_b16 v[116:117], v228 offset:23296\n" \
    "ds_read_b64_tr_b16 v[118:119], v228 offset:23328\n" \
    "ds_read_b64_tr_b16 v[120:121], v228 offset:16656\n" \
    "ds_read_b64_tr_b16 v[122:123], v228 offset:16688\n" \
    "ds_read_b64_tr_b16 v[124:125], v228 offset:18960\n" \
    "ds_read_b64_tr_b16 v[126:127], v228 offset:18992\n" \
    "ds_read_b64_tr_b16 v[128:129], v228 offset:21264\n" \
    "ds_read_b64_tr_b16 v[130:131], v228 offset:21296\n" \
    "ds_read_b64_tr_b16 v[132:133], v228 offset:23568\n" \
    "ds_read_b64_tr_b16 v[134:135], v228 offset:23600\n" \
    "s_waitcnt lgkmcnt(14)\n" \
    "v_mfma_f32_16x16x32_bf16 v[136:139], v[104:107], v[0:3], 0\n" \
    "v_mfma_f32_16x16x32_bf16 v[144:147], v[104:107], v[8:11], 0\n" \
    "v_mfma_f32_16x16x32_bf16 v[152:155], v[104:107], v[16:19], 0\n" \
    "v_mfma_f32_16x16x32_bf16 v[160:163], v[104:107], v[24:27], 0\n" \
    "s_waitcnt lgkmcnt(12)\n" \
    "v_mfma_f32_16x16x32_bf16 v[136:139], v[108:111], v[4:7], v[136:139]\n" \
    "v_mfma_f32_16x16x32_bf16 v[144:147], v[108:111], v[12:15], v[144:147]\n" \
    "v_mfma_f32_16x16x32_bf16 v[152:155], v[108:111], v[20:23], v[152:155]\n" \
    "v_mfma_f32_16x16x32_bf16 v[160:163], v[108:111], v[28:31], v[160:163]\n" \
    "s_waitcnt lgkmcnt(10)\n" \
    "v_mfma_f32_16x16x32_bf16 v[136:139], v[112:115], v[32:35], v[136:139]\n" \
    "v_mfma_f32_16x16x32_bf16 v[144:147], v[112:115], v[40:43], v[144:147]\n" \
    "v_mfma_f32_16x16x32_bf16 v[152:155], v[112:115], v[48:51], v[152:155]\n" \
    "v_mfma_f32_16x16x32_bf16 v[160:163], v[112:115], v[56:59], v[160:163]\n" \
    "s_waitcnt lgkmcnt(8)\n" \
    "v_mfma_f32_16x16x32_bf16 v[136:139], v[116:119], v[36:39], v[136:139]\n" \
    "v_mfma_f32_16x16x32_bf16 v[144:147], v[116:119], v[44:47], v[144:147]\n" \
    "v_mfma_f32_16x16x32_bf16 v[152:155], v[116:119], v[52:55], v[152:155]\n" \
    "v_mfma_f32_16x16x32_bf16 v[160:163], v[116:119], v[60:63], v[160:163]\n" \
    "s_waitcnt lgkmcnt(6)\n" \
    "v_mfma_f32_16x16x32_bf16 v[140:143], v[120:123], v[0:3], 0\n" \
    "v_mfma_f32_16x16x32_bf16 v[148:151], v[120:123], v[8:11], 0\n" \
    "v_mfma_f32_16x16x32_bf16 v[156:159], v[120:123], v[16:19], 0\n" \
    "v_mfma_f32_16x16x32_bf16 v[164:167], v[120:123], v[24:27], 0\n" \
    "s_waitcnt lgkmcnt(4)\n" \
    "v_mfma_f32_16x16x32_bf16 v[140:143], v[124:127], v[4:7], v[140:143]\n" \
    "v_mfma_f32_16x16x32_bf16 v[148:151], v[124:127], v[12:15], v[148:151]\n" \
    "v_mfma_f32_16x16x32_bf16 v[156:159], v[124:127], v[20:23], v[156:159]\n" \
    "v_mfma_f32_16x16x32_bf16 v[164:167], v[124:127], v[28:31], v[164:167]\n" \
    "s_waitcnt lgkmcnt(2)\n" \
    "v_mfma_f32_16x16x32_bf16 v[140:143], v[128:131], v[32:35], v[140:143]\n" \
    "v_mfma_f32_16x16x32_bf16 v[148:151], v[128:131], v[40:43], v[148:151]\n" \
    "v_mfma_f32_16x16x32_bf16 v[156:159], v[128:131], v[48:51], v[156:159]\n" \
    "v_mfma_f32_16x16x32_bf16 v[164:167], v[128:131], v[56:59], v[164:167]\n" \
    "s_waitcnt lgkmcnt(0)\n" \
    "v_mfma_f32_16x16x32_bf16 v[140:143], v[132:135], v[36:39], v[140:143]\n" \
    "v_mfma_f32_16x16x32_bf16 v[148:151], v[132:135], v[44:47], v[148:151]\n" \
    "v_mfma_f32_16x16x32_bf16 v[156:159], v[132:135], v[52:55], v[156:159]\n" \
    "v_mfma_f32_16x16x32_bf16 v[164:167], v[132:135], v[60:63], v[164:167]\n" \
    "v_cvt_pk_bf16_f32 v65, v136, v137\n" \
    "v_cvt_pk_bf16_f32 v68, v138, v139\n" \
    "v_cvt_pk_bf16_f32 v73, v144, v145\n" \
    "v_cvt_pk_bf16_f32 v76, v146, v147\n" \
    "v_cvt_pk_bf16_f32 v81, v152, v153\n" \
    "v_cvt_pk_bf16_f32 v84, v154, v155\n" \
    "v_cvt_pk_bf16_f32 v89, v160, v161\n" \
    "v_cvt_pk_bf16_f32 v92, v162, v163\n" \
    "v_cvt_pk_bf16_f32 v66, v142, v143\n" \
    "v_cvt_pk_bf16_f32 v67, v142, v143\n" \
    "v_cvt_pk_bf16_f32 v69, v140, v141\n" \
    "v_cvt_pk_bf16_f32 v74, v150, v151\n" \
    "v_cvt_pk_bf16_f32 v75, v150, v151\n" \
    "v_cvt_pk_bf16_f32 v77, v148, v149\n" \
    "v_cvt_pk_bf16_f32 v82, v158, v159\n" \
    "v_cvt_pk_bf16_f32 v83, v158, v159\n" \
    "v_cvt_pk_bf16_f32 v85, v156, v157\n" \
    "v_cvt_pk_bf16_f32 v90, v166, v167\n" \
    "v_cvt_pk_bf16_f32 v91, v166, v167\n" \
    "v_cvt_pk_bf16_f32 v93, v164, v165\n" \
    "v_permlane16_swap_b32 v66, v67\n" \
    "v_permlane16_swap_b32 v74, v75\n" \
    "v_permlane16_swap_b32 v82, v83\n" \
    "v_permlane16_swap_b32 v90, v91\n" \
    "v_mov_b64 v[176:177], 0\n" \
    "v_mov_b64 v[178:179], 0\n" \
    "v_mov_b64 v[180:181], 0\n" \
    "v_mov_b64 v[182:183], 0\n" \
    "v_mov_b64 v[184:185], 0\n" \
    "v_mov_b64 v[186:187], 0\n" \
    "v_mov_b64 v[188:189], 0\n" \
    "v_mov_b64 v[190:191], 0\n" \
    "v_mov_b64 v[196:197], 0\n" \
    "v_mov_b64 v[198:199], 0\n" \
    "v_mov_b64 v[200:201], 0\n" \
    "v_mov_b64 v[202:203], 0\n" \
    "v_mov_b64 v[204:205], 0\n" \
    "v_mov_b64 v[206:207], 0\n" \
    "v_mov_b64 v[208:209], 0\n" \
    "v_mov_b64 v[210:211], 0\n" \
    "v_mov_b64 v[212:213], 0\n" \
    "v_mov_b64 v[214:215], 0\n" \
    "s_add_u32 s85, s60, 0x1000\n" \
    "s_add_u32 s82, s76, 1\n" \
    "s_min_u32 s82, s82, 63\n" \
    "s_lshl_b32 s83, s82, 19\n" \
    "s_add_u32 s83, s83, s72\n" \
    "s_lshl_b32 s79, s82, 11\n" \
    "s_and_b32 s79, s79, 0x2000\n" \
    "s_xor_b32 s83, s83, s79\n" \
    "v_cmp_ge_u32 vcc, s82, v237\n" \
    "s_cbranch_vccnz L%=_Pfslow\n" \
    "L%=_Pfok:\n" \
    "s_add_u32 s79, s82, 3\n" \
    "v_mov_b32 v150, 0x40000000\n" \
    "v_cmp_lt_u32 vcc, s79, v237\n" \
    "s_nop 1\n" \
    "v_cndmask_b32 v150, v235, v150, vcc\n" \
    "s_branch L%=_Pu2b\n" \
    "L%=_Pu2a:\n" \
    "s_sleep 4\n" \
    "L%=_Pu2b:\n" \
    "s_memrealtime vcc\n" \
    "s_add_u32 s80, s77, s64\n" \
    "s_add_u32 s80, s80, s69\n" \
    "s_waitcnt lgkmcnt(0)\n" \
    "s_sub_i32 s79, s80, vcc_lo\n" \
    "s_sub_u32 s79, s79, 40\n" \
    "s_cmp_lt_u32 s79, 0x400\n" \
    "s_cbranch_scc1 L%=_Pu2a\n" \
    "s_memtime s[38:39]\n" \
    "s_memrealtime vcc\n" \
    "s_nop 4\n" \
    "s_memrealtime s[32:33]\n" \
    "s_nop 4\n" \
    "s_memrealtime s[34:35]\n" \
    "s_nop 4\n" \
    "s_memrealtime s[36:37]\n" \
    "s_add_u32 s80, s77, s64\n" \
    "s_lshl_b32 s80, s80, 2\n" \
    "s_waitcnt lgkmcnt(0)\n" \
    "s_sub_u32 s80, s80, vcc_lo\n" \
    "s_sub_u32 s80, s80, s32\n" \
    "s_sub_u32 s80, s80, s34\n" \
    "s_sub_u32 s80, s80, s36\n" \
    "s_mul_i32 s80, s80, s71\n" \
    "s_ashr_i32 s80, s80, 6\n" \
    "s_add_u32 s78, s80, s38\n" \
    "L%=_Pu3a:\n" \
    "s_memtime s[38:39]\n" \
    "s_mul_i32 s80, s69, 23\n" \
    "s_add_u32 s80, s80, s78\n" \
    "s_waitcnt lgkmcnt(0)\n" \
    "s_sub_i32 s79, s80, s38\n" \
    "s_max_i32 s79, s79, 0\n" \
    "s_cmp_gt_u32 s79, 320\n" \
    "s_cbranch_scc1 L%=_Pu3s\n" \
    ".p2align 4\n" \
    "L%=_Pu3d:\n" \
    "s_sub_u32 s79, s79, 28\n" \
    "s_cbranch_scc0 L%=_Pu3d\n" \
    "L%=_Pu3e:\n" \
    "buffer_load_dwordx4 v[104:107], v230, s[56:59], s83 offen offset:0 sc1\n" \
    "buffer_load_dwordx4 v[108:111], v230, s[56:59], s83 offen offset:1024 sc1\n" \
    "buffer_load_dwordx4 v[112:115], v230, s[56:59], s83 offen offset:2048 sc1\n" \
    "buffer_load_dwordx4 v[116:119], v230, s[56:59], s83 offen offset:3072 sc1\n" \
    "s_add_u32 s79, s83, 0x1000\n" \
    "buffer_load_dwordx4 v[120:123], v230, s[56:59], s79 offen offset:0 sc1\n" \
    "buffer_load_dwordx4 v[124:127], v230, s[56:59], s79 offen offset:1024 sc1\n" \
    "buffer_load_dwordx4 v[128:131], v230, s[56:59], s79 offen offset:2048 sc1\n" \
    "buffer_load_dwordx4 v[132:135], v230, s[56:59], s79 offen offset:3072 sc1\n" \
    "s_xor_b32 s79, s83, s75\n" \
    "buffer_load_dwordx4 v[136:139], v230, s[52:55], s79 offen offset:0 sc1\n" \
    "buffer_load_dwordx4 v[140:143], v230, s[52:55], s79 offen offset:1024 sc1\n" \
    "buffer_load_dword v151, v150, s[44:47], 0 offen sc1\n" \
    "s_lshl_b32 s79, s76, 7\n" \
    "buffer_load_dwordx4 v[144:147], off, s[44:47], s79 offset:2176 sc1\n" \
    "buffer_store_dword v243, v239, s[44:47], s79 offen sc1\n" \
    "buffer_store_dwordx4 v[244:247], v232, s[40:43], s62 offen offset:0 sc1\n" \
    "buffer_store_dwordx4 v[244:247], v232, s[40:43], s62 offen offset:1024 sc1\n" \
    "buffer_store_dwordx4 v[244:247], v232, s[40:43], s62 offen offset:2048 sc1\n" \
    "buffer_store_dwordx4 v[244:247], v232, s[40:43], s62 offen offset:3072 sc1\n" \
    "buffer_store_dwordx4 v[244:247], v233, s[40:43], s62 offen offset:0 sc1\n" \
    "buffer_store_dwordx4 v[244:247], v233, s[40:43], s62 offen offset:1024 sc1\n" \
    "buffer_store_dwordx4 v[244:247], v234, s[40:43], s62 offen sc1\n" \
    "s_barrier\n" \
    "s_cmp_eq_u32 s76, 63\n" \
    "s_cbranch_scc1 L%=_Pfin\n" \
    "ds_read_b32 v216, v229\n" \
    "s_waitcnt lgkmcnt(0)\n" \
    "v_readfirstlane_b32 s94, v216\n" \
    "s_mul_i32 s79, s94, s70\n" \
    "s_add_u32 s79, s79, s81\n" \
    "s_cmp_lg_u32 s79, s77\n" \
    "s_cbranch_scc1 L%=_Pu4s\n" \
    "L%=_Pu4k:\n" \
    "s_sub_u32 s32, s78, s97\n" \
    "s_sub_u32 s33, s77, s98\n" \
    "s_mov_b32 s97, s78\n" \
    "s_mov_b32 s98, s77\n" \
    "s_lshl_b32 s32, s32, 4\n" \
    "s_cmp_eq_u32 s76, 0\n" \
    "s_cbranch_scc1 L%=_Pck\n" \
    "v_cvt_f32_u32 v148, s33\n" \
    "v_cvt_f32_u32 v149, s32\n" \
    "v_rcp_f32 v148, v148\n" \
    "s_cmp_lt_u32 s76, 4\n" \
    "s_cselect_b32 s34, 0, 0x3c449ba6\n" \
    "s_cmp_eq_u32 s94, s74\n" \
    "s_mov_b32 s74, s94\n" \
    "s_cselect_b32 s32, 0x7f000000, 0\n" \
    "s_cselect_b32 s33, 0.5, 1.0\n" \
    "s_cselect_b32 s34, 0, s34\n" \
    "v_fma_f32 v148, v148, v149, -v240\n" \
    "v_max_f32 v149, 0.5, v241\n" \
    "v_min_f32 v148, s32, v148\n" \
    "v_cmp_gt_f32 vcc, 0, v148\n" \
    "v_mul_f32 v242, 0x3f7851ec, v242\n" \
    "v_add_f32 v242, s34, v242\n" \
    "v_cndmask_b32 v149, v241, v149, vcc\n" \
    "v_fma_f32 v240, v148, v149, v240\n" \
    "v_mul_f32 v241, s33, v241\n" \
    "v_max_f32 v240, 0x43400000, v240\n" \
    "v_max_f32 v241, 0x3e000000, v241\n" \
    "v_min_f32 v240, 0x43e00000, v240\n" \
    "v_fma_f32 v148, v242, s73, s73\n" \
    "v_rcp_f32 v149, v240\n" \
    "s_nop 0\n" \
    "v_fma_f32 v148, v148, v149, 0.5\n" \
    "v_cvt_u32_f32 v243, v148\n" \
    "L%=_Pck:\n" \
    "v_add_f32 v149, 0.5, v240\n" \
    "v_mov_b32 v148, s65\n" \
    "v_cvt_u32_f32 v149, v149\n" \
    "s_waitcnt vmcnt(8)\n" \
    "ds_write_b128 v231, v[104:107] offset:16384\n" \
    "ds_write_b128 v231, v[108:111] offset:17536\n" \
    "ds_write_b128 v231, v[112:115] offset:18688\n" \
    "ds_write_b128 v231, v[116:119] offset:19840\n" \
    "ds_write_b128 v231, v[120:123] offset:20992\n" \
    "ds_write_b128 v231, v[124:127] offset:22144\n" \
    "ds_write_b128 v231, v[128:131] offset:23296\n" \
    "ds_write_b128 v231, v[132:135] offset:24448\n" \
    "ds_write_b128 v230, v[136:139] offset:25600\n" \
    "ds_write_b128 v230, v[140:143] offset:26624\n" \
    "v_max3_u32 v144, v144, v145, v146\n" \
    "v_max_u32 v144, v144, v147\n" \
    "v_cmp_eq_u32 vcc, 0, v144\n" \
    "s_nop 1\n" \
    "v_cndmask_b32 v148, v144, v148, vcc\n" \
    "ds_write_b64 v229, v[148:149] offset:64\n" \
    "v_max_u32 v237, v237, v151\n" \
    "s_setprio 3\n" \
    "L%=_Pu5a:\n" \
    "s_memtime s[38:39]\n" \
    "s_mul_hi_u32 s80, s67, s96\n" \
    "s_add_u32 s80, s80, s78\n" \
    "s_waitcnt lgkmcnt(0)\n" \
    "s_sub_i32 s79, s80, s38\n" \
    "s_max_i32 s79, s79, 0\n" \
    "s_cmp_gt_u32 s79, 320\n" \
    "s_cbranch_scc1 L%=_Pu5s\n" \
    ".p2align 4\n" \
    "L%=_Pu5d:\n" \
    "s_sub_u32 s79, s79, 28\n" \
    "s_cbranch_scc0 L%=_Pu5d\n" \
    "L%=_Pu5e:\n" \
    "buffer_load_dwordx4 v[168:171], v225, s[40:43], s60 offen sc1\n" \
    "buffer_load_dwordx4 v[172:175], v225, s[40:43], s85 offen sc1\n" \
    "L%=_Pvc:\n" \
    "s_waitcnt vmcnt(1)\n" \
    "v_smfmac_f32_16x16x64_bf16 v[176:179], v[168:171], v[0:7], v224\n" \
    "v_smfmac_f32_16x16x64_bf16 v[180:183], v[168:171], v[8:15], v224\n" \
    "s_waitcnt vmcnt(0)\n" \
    "v_smfmac_f32_16x16x64_bf16 v[176:179], v[172:175], v[32:39], v224\n" \
    "v_smfmac_f32_16x16x64_bf16 v[180:183], v[172:175], v[40:47], v224\n" \
    "v_smfmac_f32_16x16x64_bf16 v[184:187], v[168:171], v[16:23], v224\n" \
    "v_smfmac_f32_16x16x64_bf16 v[188:191], v[168:171], v[24:31], v224\n" \
    "v_add_f32 v176, v176, v178\n" \
    "v_smfmac_f32_16x16x64_bf16 v[184:187], v[172:175], v[48:55], v224\n" \
    "v_add_f32 v177, v177, v179\n" \
    "v_smfmac_f32_16x16x64_bf16 v[188:191], v[172:175], v[56:63], v224\n" \
    "v_cmp_u_f32 vcc, v176, v177\n" \
    "v_cvt_pk_bf16_f32 v64, v176, v177\n" \
    "v_add_f32 v180, v180, v182\n" \
    "v_add_f32 v181, v181, v183\n" \
    "v_cvt_pk_bf16_f32 v72, v180, v181\n" \
    "s_cbranch_vccnz L%=_Pvslow\n" \
    "L%=_Pvok:\n" \
    "v_add_f32 v184, v184, v186\n" \
    "v_add_f32 v185, v185, v187\n" \
    "v_cvt_pk_bf16_f32 v80, v184, v185\n" \
    "v_add_f32 v188, v188, v190\n" \
    "v_add_f32 v189, v189, v191\n" \
    "v_cvt_pk_bf16_f32 v88, v188, v189\n" \
    "s_barrier\n" \
    "L%=_Pfr:\n" \
    "ds_read_b128 v[192:195], v227\n" \
    "s_waitcnt lgkmcnt(0)\n" \
    "v_smfmac_f32_16x16x64_bf16 v[212:215], v[192:195], v[96:103], v224\n" \
    "v_cmp_ne_u32 vcc, s76, v195\n" \
    "v_smfmac_f32_16x16x64_bf16 v[196:199], v[192:195], v[64:71], v224\n" \
    "s_cbranch_vccnz L%=_Pfs\n" \
    "v_smfmac_f32_16x16x64_bf16 v[200:203], v[192:195], v[72:79], v224\n" \
    "v_smfmac_f32_16x16x64_bf16 v[204:207], v[192:195], v[80:87], v224\n" \
    "v_add_f32 v212, v212, v214\n" \
    "v_smfmac_f32_16x16x64_bf16 v[208:211], v[192:195], v[88:95], v224\n" \
    "v_add_f32 v213, v213, v215\n" \
    "v_rcp_f32 v220, v212\n" \
    "v_rcp_f32 v221, v213\n" \
    "v_add_f32 v196, v196, v198\n" \
    "v_add_f32 v197, v197, v199\n" \
    "v_pk_add_f32 v[200:201], v[200:201], v[202:203]\n" \
    "v_pk_add_f32 v[204:205], v[204:205], v[206:207]\n" \
    "s_nop 0\n" \
    "v_pk_add_f32 v[208:209], v[208:209], v[210:211]\n" \
    "v_pk_mul_f32 v[196:197], v[196:197], v[220:221]\n" \
    "v_pk_mul_f32 v[200:201], v[200:201], v[220:221]\n" \
    "v_pk_mul_f32 v[204:205], v[204:205], v[220:221]\n" \
    "v_pk_mul_f32 v[208:209], v[208:209], v[220:221]\n" \
    "v_cvt_pk_bf16_f32 v216, v196, v200\n" \
    "v_cvt_pk_bf16_f32 v217, v204, v208\n" \
    "v_cvt_pk_bf16_f32 v218, v197, v201\n" \
    "v_cvt_pk_bf16_f32 v219, v205, v209\n" \
    "buffer_store_dwordx4 v[216:219], v226, s[40:43], s60 offen sc1\n" \
    "s_setprio 0\n" \
    "ds_read_b64 v[220:221], v229 offset:64\n" \
    "s_mov_b32 s79, s60\n" \
    "s_mov_b32 s60, s61\n" \
    "s_mov_b32 s61, s62\n" \
    "s_mov_b32 s62, s63\n" \
    "s_mov_b32 s63, s79\n" \
    "s_add_u32 s76, s76, 1\n" \
    "s_add_u32 s77, s77, s65\n" \
    "s_add_u32 s84, s84, 0x20000\n" \
    "s_add_u32 s81, s81, s65\n" \
    "s_waitcnt lgkmcnt(0)\n" \
    "v_readfirstlane_b32 s65, v220\n" \
    "v_readfirstlane_b32 s71, v221\n" \
    "s_mul_i32 s96, s65, s71\n" \
    "s_lshr_b32 s96, s96, 4\n" \
    "s_branch L%=_Ploop\n" \
    "L%=_Pfslow:\n" \
    "s_sleep 4\n" \
    "buffer_load_dword v237, v235, s[44:47], 0 offen sc1\n" \
    "s_waitcnt vmcnt(0)\n" \
    "v_cmp_ge_u32 vcc, s82, v237\n" \
    "s_cbranch_vccz L%=_Pfok\n" \
    "s_cmp_eq_u32 s99, 0\n" \
    "s_cbranch_scc1 L%=_Pfok\n" \
    "s_sub_u32 s99, s99, 1\n" \
    "s_branch L%=_Pfslow\n" \
    "L%=_Pfin:\n" \
    "s_waitcnt vmcnt(0)\n" \
    "s_barrier\n" \
    "s_branch L%=_end\n" \
    "L%=_Pu3s:\n" \
    "s_cmp_gt_u32 s79, 0x8000\n" \
    "s_cbranch_scc1 L%=_Pu3e\n" \
    "s_sleep 2\n" \
    "s_branch L%=_Pu3a\n" \
    "L%=_Pu4s:\n" \
    "s_mov_b32 s77, s79\n" \
    "s_memtime s[38:39]\n" \
    "s_memrealtime vcc\n" \
    "s_nop 4\n" \
    "s_memrealtime s[32:33]\n" \
    "s_nop 4\n" \
    "s_memrealtime s[34:35]\n" \
    "s_nop 4\n" \
    "s_memrealtime s[36:37]\n" \
    "s_add_u32 s80, s77, s64\n" \
    "s_lshl_b32 s80, s80, 2\n" \
    "s_waitcnt lgkmcnt(0)\n" \
    "s_sub_u32 s80, s80, vcc_lo\n" \
    "s_sub_u32 s80, s80, s32\n" \
    "s_sub_u32 s80, s80, s34\n" \
    "s_sub_u32 s80, s80, s36\n" \
    "s_mul_i32 s80, s80, s71\n" \
    "s_ashr_i32 s80, s80, 6\n" \
    "s_add_u32 s78, s80, s38\n" \
    "s_branch L%=_Pu4k\n" \
    "L%=_Pu5s:\n" \
    "s_cmp_gt_u32 s79, 0x8000\n" \
    "s_cbranch_scc1 L%=_Pu5e\n" \
    "s_sleep 2\n" \
    "s_branch L%=_Pu5a\n" \
    "L%=_Pvslow:\n" \
    "s_nop 7\n" \
    "s_nop 0\n" \
    "v_max_u32 v216, v168, v169\n" \
    "v_max3_u32 v216, v216, v170, v171\n" \
    "v_max3_u32 v216, v216, v172, v173\n" \
    "v_max3_u32 v216, v216, v174, v175\n" \
    "v_cmp_eq_u32 vcc, -1, v216\n" \
    "s_cbranch_vccz L%=_Pvok\n" \
    "s_cmp_eq_u32 s99, 0\n" \
    "s_cbranch_scc1 L%=_Pvok\n" \
    "s_sub_u32 s99, s99, 1\n" \
    "s_cmp_lg_u32 s99, 0\n" \
    "s_cbranch_scc1 L%=_Pu6\n" \
    "v_mov_b32 v216, 1\n" \
    "buffer_store_dword v216, off, s[44:47], 0 sc1\n" \
    "L%=_Pu6:\n" \
    "s_sleep 1\n" \
    "buffer_load_dwordx4 v[168:171], v225, s[40:43], s60 offen sc1\n" \
    "buffer_load_dwordx4 v[172:175], v225, s[40:43], s85 offen sc1\n" \
    "v_mov_b64 v[176:177], 0\n" \
    "v_mov_b64 v[178:179], 0\n" \
    "v_mov_b64 v[180:181], 0\n" \
    "v_mov_b64 v[182:183], 0\n" \
    "v_mov_b64 v[184:185], 0\n" \
    "v_mov_b64 v[186:187], 0\n" \
    "v_mov_b64 v[188:189], 0\n" \
    "v_mov_b64 v[190:191], 0\n" \
    "s_waitcnt vmcnt(0)\n" \
    "s_branch L%=_Pvc\n" \
    "L%=_Pfs:\n" \
    "s_nop 7\n" \
    "s_nop 0\n" \
    "v_mov_b64 v[196:197], 0\n" \
    "v_mov_b64 v[198:199], 0\n" \
    "v_mov_b64 v[200:201], 0\n" \
    "v_mov_b64 v[202:203], 0\n" \
    "v_mov_b64 v[204:205], 0\n" \
    "v_mov_b64 v[206:207], 0\n" \
    "v_mov_b64 v[208:209], 0\n" \
    "v_mov_b64 v[210:211], 0\n" \
    "v_mov_b64 v[212:213], 0\n" \
    "v_mov_b64 v[214:215], 0\n" \
    "s_branch L%=_Pfr\n" \
    "L%=_end:\n"
#define V8(b) "v" #b "0", "v" #b "1", "v" #b "2", "v" #b "3", "v" #b "4", "v" #b "5", "v" #b "6", "v" #b "7", "v" #b "8", "v" #b "9"
#define S_IO "+{s[40:55]}"(sA), "+{s[56:71]}"(sB), "+{s[72:75]}"(sC), "+{s78}"(lwo), "={s[92:99]}"(so)
#define FIN2() do {} while (0)
#define S_CLOB "memory", "vcc", "scc", "m0", "s32", "s33", "s34", "s35", "s36", "s37", "s38", "s39", "s76", "s77", "s79", "s80", "s81", "s82", "s83", "s84", "s85", "s86", "s87", "s88", "s89", "s90", "s91"
#define B_CLOB S_CLOB, "v64", "v65", "v66", "v67", "v68", "v69", V8(7), V8(8), V8(9), V8(10), V8(11), V8(12), V8(13), V8(14), V8(15), V8(16), V8(17), V8(18), V8(19), V8(20), V8(21), \
    "v220", "v221", "v222", "v223", "v240", "v241", "v242", "v243"
#define FIN() do {} while (0)

#define MK_S() \
    a = args(); \
    v16u sA = cat8(cat4(mkrs(a->ring, NSLOT * SLOT), mkrs(a->ctl, CTL_SZ)), cat4(mkrs(a->mb, NSEG * NH * 4096), mkrs(a->qkv + QSZ, QSZ))); \
    v16u sB = cat8(cat4(mkrs(a->qkv + 2 * (size_t)QSZ, QSZ), v4u{0, 0, 0, 0}), \
                   v8u{0, (unsigned)a->per0, (unsigned)a->d1, (unsigned)a->dv, (unsigned)a->d2, (unsigned)a->dp, (unsigned)a->slack, (unsigned)a->thr}); \
    const v4u rsc = __builtin_shufflevector(sA, sA, 4, 5, 6, 7); \
    unsigned b0, b1
#define HALVES() do { const unsigned u_ = beacon(rsc, b0, b1, 16); \
_Pragma("unroll") \
    for (int k = 0; k < NSLOT; ++k) sB[4 + k] = k * (unsigned)SLOT + ((u_ >> k) & 1) * 8192u; } while (0)

#define START() __hip_atomic_fetch_max((LAS unsigned*)(lds + L_MISC), (rt32() - spare) / sB[14] + 1, __ATOMIC_RELAXED, __HIP_MEMORY_SCOPE_WORKGROUP)

DEV void chain(LAS char* lds, const int c) {
    KArgs a = args();
    const int w = __builtin_amdgcn_readfirstlane(threadIdx.x >> 6), h = c >> 2, j = c & 3;
    const unsigned lb = (unsigned)(size_t)lds, lbw = lb + L_STG + w * 16384;
#define LANE() int l = threadIdx.x & 63; asm volatile("" : "+v"(l)); const int l15 = l & 15, g = l >> 4, hb = (l >> 1) & 1, q4 = l15 >> 2; const unsigned idx = hb ? 0xEEEE : 0x4444
    v4u sC, stv; v2u stw; unsigned stx; v8u so;
    unsigned lwo = w & 3;
    if (w < 4) {
        LANE();
        v16u wt[12], m;
            const v4u rw[3] = {mkrs(a->Wk, DM * HD * 2), mkrs(a->Wv, DM * HD * 2), mkrs(a->Wq, DM * HD * 2)};
            const unsigned vo = (l >> 2) * (HD * 2) + (l & 3) * 16, sw = (256 * w * HD + h * DH + 32 * j) * 2;
            auto issue = [&](int p) {
#pragma unroll
                for (int i = 0; i < 8; ++i) DMA16N(rw[p >> 1], vo, sw + ((p & 1) * 128 + i * 16) * (HD * 2), lbw + (p & 1) * 8192 + i * 1024); };
            {
                const v4u rp = w & 1 ? rw[2] : rw[1];
                const unsigned ps = ((rp[0] >> 7) + (c >> 5) * 16) & 63, po = w < 2 ? ((3 - ps) & 7) * 128 + (l & 1) * 1024 : (63 - ps < 16 && !(l & 1) ? (63 - ps) * 128 : OOB);
                if (a->pf) asm volatile("s_mov_b32 m0, %0\n\ts_nop 0\n\tbuffer_load_dword %1, %2, 0 offen lds" :: "s"(lb + L_PF + w * 256), "v"(((c & 31) * 32 + (l >> 1)) * (HD * 2) + (c >> 5) * 2048 + po), "s"(rp) : "memory", "m0");
            }
            unsigned mr[16];
#pragma unroll
            for (int k = 0; k < 16; ++k) mr[k] = *(const unsigned*)(a->mem0 + (2 * q4 + (l & 1)) * DM + 256 * w + 16 * k + 4 * g + 2 * hb);
            SCHED();
            issue(0); issue(1);
            RBAR();
            MK_S();
            unsigned spare = a->spare; asm volatile("" : "+s"(sA), "+s"(sB), "+s"(spare));
            BC_LD(b0, rsc);
            const LAS char* pw = lds + L_STG + w * 16384 + (4 * (g >> 1) + (g & 1) + 2 * (q4 >> 1) + 16 * (q4 & 1)) * 64 + (l & 3) * 8;
#pragma unroll
            for (int p = 0; p < 6; ++p) {
                if (p < 5) WAITV(8); else WAITV(0);
#pragma unroll
                for (int k = 0; k < 2; ++k) wt[(2 * p + k + 4) % 12] = cat8(rd_w<2048, 512, 2560>(pw + (p & 1) * 8192 + k * 4096), rd_w<2048, 512, 2560>(pw + (p & 1) * 8192 + k * 4096 + 32));
                WAITL(0);
                if (p < 4) issue(p + 2);
                if (p == 3) BC_LD(b1, rsc);
            }
#pragma unroll
            for (int k = 0; k < 16; k += 4) {
                m[k] = __builtin_amdgcn_perm(mr[k + 1], mr[k], 0x05040100u); m[k + 1] = __builtin_amdgcn_perm(mr[k + 3], mr[k + 2], 0x05040100u);
                m[k + 2] = __builtin_amdgcn_perm(mr[k + 1], mr[k], 0x07060302u); m[k + 3] = __builtin_amdgcn_perm(mr[k + 3], mr[k + 2], 0x07060302u);
            }
        const unsigned pq = nearo(H3_OFF + (c >> 5) * 16384) + w * 1024 + l * 16;
        const v8u va = {idx, pq, 0, 0, lb + L_MISC + w * 4, lb + L_MISC, 0, pq + 16384};
        sC = v4u{lb + w * 2176u, lb + w * 1280u, 0, 0};
        HALVES();
        START();
        asm volatile(ASM_A : S_IO, "+{v[192:207]}"(m)
                     : "{v[0:31]}"(cat16(wt[0], wt[1])), "{v[32:63]}"(cat16(wt[2], wt[3])), "{v[64:95]}"(cat16(wt[4], wt[5])), "{v[96:127]}"(cat16(wt[6], wt[7])),
                       "{v[128:159]}"(cat16(wt[8], wt[9])), "{v[160:191]}"(cat16(wt[10], wt[11])), "{v[232:239]}"(va)
                     : S_CLOB, "v208", "v209", V8(21), V8(22), "v230", "v231", "v240", "v241", "v244", "v245", "v246", "v247");
        FIN();
    } else {
        LANE();
        const int bw = w - 4, l7 = l & 7;
        MK_S();
        if (w == 6 && l < 16) ((LAS unsigned*)(lds + L_MISC))[l] = l ? 0 : rt32() / sB[14];
        BAR();
        BC_LD(b0, rsc);
        {
            const v4u rw = mkrs(a->Wo, HD * DM * 2);
            const unsigned vo = (l >> 3) * (DM * 2) + l7 * 16, sw = (h * DH * DM + 256 * j + 64 * bw) * 2;
#pragma unroll
            for (int i = 0; i < 16; ++i) DMA16N(rw, vo, sw + i * 8 * (DM * 2), lbw + i * 1024);
        }
        BC_LD(b1, rsc);
        WAITV(0);
        HALVES();
        const LAS char* pw = lds + L_STG + w * 16384 + (16 * (g & 1) + 8 * (g >> 1) + (q4 & 1) + 4 * (q4 >> 1)) * 128 + (l & 3) * 8;
#define RDO(o) rd_w<256, 4096, 4352>(pw + (o))
        const v32u wo0 = cat16(cat8(RDO(0), RDO(32)), cat8(RDO(64), RDO(96))), wo1 = cat16(cat8(RDO(8192), RDO(8224)), cat8(RDO(8256), RDO(8288)));
        const unsigned c3 = ((c >> 3) & 3) * 4096 + (c >> 5) * 1024 + (c & 7) * 128, h1 = nearo(H1_OFF + h * 8192), h1j = h1 + j * 2048;
        v16u va = {};
        va[0] = idx; va[1] = h1 + (g >> 1) * 2048 + 1280 + (l & 31) * 16;
        va[2] = nearo(H2_OFF + (32 * j + 8 * bw + q4 * 2 + (g >> 1)) * 4096) + h * 128 + ((g & 1) * 4 + (l & 3)) * 16;
        va[3] = lb + L_P + g * 320 + l15 * 16;
        va[4] = lb + (l & 1) * X_BLK + (l & 2) * X_FQ + (8 * (g >> 1) + (q4 & 1) + 4 * (q4 >> 1)) * 16 + (g & 1) * 8;
        va[5] = lb + L_MISC;
        v4u sel = {};
        if (w == 4) {
            const int tok = l & 7, u = l >> 5, z = (l >> 3) & 3;
            va[6] = h1 + (z == 0 ? 8 + u : (z == 1 ? 2 * u : (z == 2 ? 4 + 2 * u : 2 * u + 1))) * 128 + tok * 16;
            va[7] = h1 + (5 + 2 * u) * 128 + tok * 16 + (z & 1) * 8;
            va[8] = lb + L_P + ((z & 2) + u) * 320 + (4 * (tok >> 1) + 2 * (z & 1) + (tok & 1)) * 16;
            va[9] = l == 0 ? F_PG + c * 128 : OOB;
            *(LAS v4u*)(lds + va[8]) = v4u{POI, POI, POI, POI};
            sC = v4u{__float_as_uint(SCALE * LOG2E), __float_as_uint(SCALE * LOG2E), 0, 0};
        } else if (w == 5) {
            va[6] = lb + L_PQK + (l15 >> 3) * 256 + (l & 1) * 544 + (l7 >> 1) * 64 + g * 16;
            va[7] = (l & 8) ? OOB : h1j + g * 128 + l7 * 16;
            va[8] = ((l & 8) || g < 2) ? OOB : h1j + 1024 + ((g & 1) * 8 + l7) * 16;
            va[9] = (l & 8) ? h * 4096 + j * 512 + (g * 8 + l7) * 16 : OOB;
            va[10] = lb + l * 16;
            sC = v4u{0, 0, 0, 0};
        } else if (w == 6) {
            const int rho = (l >> 1) & 15, tok = 2 * (rho >> 2) + (rho & 1), c4 = 2 * (l >> 5) + ((rho >> 1) & 1);
            va[6] = lb + ((l & 1) * 160 + (tok & 1) * 72 + (tok >> 1) * 16 + c4 * 4) * 4;
            va[7] = (l & 1) ? OOB : h1j + 1280 + (l >> 1) * 16;
            va[8] = lb + L_VT + tok * 64 + (l & 1) * 32 + c4 * 8;
            va[9] = lb + L_VT + q4 * 64 + (g & 1) * 32 + (l & 3) * 8;
            va[10] = l < 32 ? h * 4096 + 2048 + (32 * j + l) * 16 : OOB;
            va[11] = nearo(H2_OFF + c * 4096) + l * 16;
            va[12] = (l & 1) ? OOB : nearo(H3_OFF + (2 * (g >> 1) + (l15 >> 3)) * 16384 + c3) + ((l7 & 6) + (g & 1)) * 16;
#pragma unroll
            for (int i = 0; i < 4; ++i) sel[i] = (l7 == 2 * i ? 0x3f80u : 0u) | (l7 == 2 * i + 1 ? 0x3f800000u : 0u);
            sC = v4u{0, 0, 0, 0};
        } else {
            const rsrc_t rk = mkrsrc(a->qkv + QSZ, QSZ), rv = mkrsrc(a->qkv + 2 * (size_t)QSZ, QSZ);
            wait_flags(mkrsrc(a->ctl, CTL_SZ), fw(l & 1, h) + 12, 1, a->ctl);
            v4u pf[10];
            const unsigned hb0 = h * 16384 + (KVS(a->ms >> 4, h) ^ sB[4] ^ 8192), kx = j * 2048 + (KVS(a->ms >> 4, h) ^ KVS(a->ms >> 2, h));
#pragma unroll
            for (int i = 0; i < 8; ++i) pf[i] = LDB(rv, i, hb0);
            pf[8] = LDB(rk, 0, hb0 ^ kx); pf[9] = LDB(rk, 1, hb0 ^ kx);
            va[13] = LD4(mkrsrc(a->ctl, CTL_SZ), fw(l & 1, h) + 12, 0);
#pragma unroll
            for (int i = 0; i < 8; ++i) *(LAS v4u*)(lds + L_X + i * X_BLK + g * X_FQ + l15 * 16) = pf[i];
            *(LAS v4u*)(lds + L_K + l * 16) = pf[8]; *(LAS v4u*)(lds + L_K + 1024 + l * 16) = pf[9];
            FENCE(); ((volatile LAS unsigned*)(lds + L_MISC))[2] = 1;
            va[6] = l * 16; va[7] = lb + g * X_FQ + l15 * 16;
            va[8] = nearo(H2_OFF + c * 4096) + l * 16; va[9] = h1j + l * 16;
            va[10] = nearo(H3_OFF + (l >> 3) * 16384 + c3) + l7 * 16;
            va[11] = fw(l & 1, h) + 12;
            va[15] = l == 0 && (c & 31) == 0 ? F_CK + 384 + (c >> 5) * 4 : OOB;
            sC = v4u{hb0, __float_as_uint(a->per), 0, kx};
        }
        while (((volatile LAS unsigned*)(lds + L_MISC))[2] == 0) __builtin_amdgcn_s_sleep(1);
        asm volatile(ASM_B : S_IO, "+{v[224:239]}"(va), "+{v[244:247]}"(sel) : "{v[0:31]}"(wo0), "{v[32:63]}"(wo1) : B_CLOB);
        FIN();
    }
}

#define RING_LD "buffer_load_dwordx4 %5, %10, %11, %12 offen offset:%13 sc1\n\t"
#define RING_M(C, W, Z) "v_mfma_f32_16x16x32_bf16 %" #C ", %" #W ", %4, " Z "\n\t"
#define RING_W(C, O) "ds_write_b128 %14, %" #C " offset:" #O "\n\t"
#define RING_OP(K, T) asm volatile(T : "+v"(c[0]), "+v"(c[1]), "+v"(c[2]), "+v"(c[3]), "+v"(ring[(K) & 15]), "+v"(ring[((K) + 15) & 15]) \
    : "v"(wo[(K) & 7][0]), "v"(wo[(K) & 7][1]), "v"(wo[(K) & 7][2]), "v"(wo[(K) & 7][3]), "v"(l16), "s"(rso), "s"(nb[(((K) + 15) >> 2) & 3]), "n"((((K) + 15) & 3) * 1024), "v"(rb) : "memory")
#define RING_1(K, B, O) "buffer_load_dwordx4 %" #K ", %16, %17, %" #B " offen offset:" #O " sc1\n\t"
#define RING_4(K0, K1, K2, K3, B) RING_1(K0, B, 0) RING_1(K1, B, 1024) RING_1(K2, B, 2048) RING_1(K3, B, 3072)
#define RING_ALL(P) P(ring[0]), P(ring[1]), P(ring[2]), P(ring[3]), P(ring[4]), P(ring[5]), P(ring[6]), P(ring[7]), P(ring[8]), P(ring[9]), P(ring[10]), P(ring[11]), P(ring[12]), P(ring[13]), P(ring[14]), P(ring[15])
#define NPROBE 3
static constexpr int RED_B = 65536;
#define OS(k) do {} while (0)

DEV void load_wo(LAS char* lds, v4u rw, unsigned o, int w, int l, v4u (&wo)[8][4]) {
    const unsigned vo = (l >> 3) * (DM * 2) + (((l & 7) ^ ((l >> 3) & 6)) << 4), lb = (unsigned)(size_t)lds + w * 16384;
    auto issue = [&](int p) {
#pragma unroll
        for (int k = 0; k < 8; ++k) DMA16N(rw, vo, o + (64 * p + 8 * k) * (DM * 2), lb + (p & 1) * 8192 + k * 1024); };
    issue(0); issue(1);
    const int row = l >> 2;
#pragma unroll
    for (int p = 0; p < 4; ++p) {
        if (p < 3) WAITV(8); else WAITV(0);
#pragma unroll
        for (int t = 0; t < 4; ++t) {
            const LAS char* q = lds + w * 16384 + (p & 1) * 8192 + row * 128 + (((2 * t + ((l >> 1) & 1)) ^ (row & 6)) << 4) + (l & 1) * 8;
            wo[2 * p][t] = cat(trd(q), trd(q + 2048)); wo[2 * p + 1][t] = cat(trd(q + 4096), trd(q + 6144));
        }
        WAITL(0);
        if (p < 2) issue(p + 2);
    }
}

DEV void worker(LAS char* lds, const int wk) {
    KArgs a = args();
    const int tid = threadIdx.x, w = __builtin_amdgcn_readfirstlane(tid >> 6), l = tid & 63, fr = l & 15, fq = l >> 4;
    const int g = wk >> 5, i = wk & 31;
    const rsrc_t rc = mkrsrc(a->ctl, CTL_SZ);
    unsigned* err = a->ctl;
    unsigned* dbg = a->dbg + 32768 + wk * 32;
    WSTAMP(0);
    const v4u rx = mkrs(a->x, S_TOK * DM * 2);
    const int role = i >> 3;
    const int h = 8 * g + (i & 7);
    const unsigned me = F_W + wk * 128;
    unsigned far = 0;
    v4u rsc = mkrs(a->ctl, CTL_SZ);
    if (role >= 2) {
        const unsigned t0 = rt32();
        const rsrc_t rr = mkrsrc(a->ring, NSLOT * SLOT);
        unsigned b0, ak = 1;
        BC_LD(b0, rsc);
        if (role == 2) {
#pragma unroll
            for (int k = 0; k < 14; ++k) ST16(rr, (h >> 4) * (unsigned)SLOT + ((h & 15) * 14 + k) * 8192 + tid * 16, 0, (v4u{POI, POI, POI, POI}));
        } else if (wk == 127) {
#pragma unroll
            for (int k = 0; k < 8; ++k) ST16(rc, k == 0 && (tid >> 3) == F_BC / 128 ? OOB : k * 8192 + tid * 16, 0, (v4u{0, 0, 0, 0}));
        } else if (i < 28 && w == 0) {
            unsigned t[2] = {~0u, ~0u};
            for (int it = 0; it < NPROBE; ++it) {
#pragma unroll
                for (int p = 0; p < 2; ++p) { const unsigned c0 = cyc32(); ST4(rr, (i & 3) * (unsigned)SLOT + p * 8192 + nearo(PR_OFF) + g * 256 + (l & 31) * 4, 0, c0); WAITV(0); t[p] = min(t[p], cyc32() - c0); }
            }
            ak = 256 + min(max((int)(t[1] - t[0]), -100), 100);
        }
        asm volatile("s_waitcnt vmcnt(0)\n\ts_barrier" : "+v"(b0) :: "memory");
        WSTAMP(1);
        if (w == 0) {
            if (wk != 127) beacon(rsc, b0, b0, 0);
            ST4(rc, l ? OOB : (wk == 127 ? F_BC : me), 0, ak);
            if (wk == 127) WAITV(0);
        }
        RBAR();
        const unsigned d = role == 2 ? a->wq : (wk == 127 ? 0 : a->wx);
        while (rt32() - t0 < d) __builtin_amdgcn_s_sleep(8);
        if (role == 3) prep_rows(a->x, a->gw, a->w_r, rc, h * 64 + w * 8, l);
        if (w == 0 && wk == 127) {
            unsigned bc = 0;
            for (unsigned tries = 0; tries < SPIN_CAP; ++tries) {
                bc += 32; ST4(rc, l ? OOB : F_BC, 0, bc);
                FENCE();
                if (!(bc & 16)) {
                    unsigned v = LD4(rc, F_W + ((l >> 4) * 32 + 16 + fr) * 128, 0);
                    if (__builtin_amdgcn_ballot_w64(v == 0) << 1 == 0) {
                        v += __shfl_xor((int)v, 16); v += __shfl_xor((int)v, 32);
                        bc |= 16 | ((unsigned)(__builtin_amdgcn_ballot_w64(v > 1024) >> 8) & 15);
                        continue;
                    }
                } else if (rt32() - t0 > 2400 && all_ge(LD4(rc, F_PG, 0), 1)) break;
                __builtin_amdgcn_s_sleep(16);
            }
            WSTAMP(5);
        }
        if (role == 3 && h >= NMOE) while (rt32() - t0 < 2600) __builtin_amdgcn_s_sleep(64);
    }
    if (role == 1) far = stream_qkv<true>(lds, rx, mkrs(a->Wv, DM * HD * 2), mkrsrc(a->qkv + 2 * (size_t)QSZ, QSZ), rc, rsc, true, me + 12, h, KVS(a->ms >> 4, h), dbg);
    else if (role < 3) far = stream_qkv<false>(lds, rx, mkrs(role ? a->Wq : a->Wk, DM * HD * 2), mkrsrc(a->qkv + (role ? 0 : QSZ), QSZ), rc, rsc, role == 0, me + 12, h, role ? 0 : KVS(a->ms >> 2, h), dbg);
    else if (h < NMOE) {
        const int sel = top8((LAS unsigned*)(lds + L_TOT - 256), rc, err, w, l);
        WSTAMP(26);
        if (h == 0 && tid < TOPC) ST4(mkrsrc(a->idx, 32), tid * 4, 0, sel);
        moe(lds, rx, sel, mkrs(a->wg, DM * FF * 2), mkrs(a->wu, DM * FF * 2), mkrs(a->wd, FF * DM * 2), mkrsrc(a->part, NMOE * TOPC * DM * 4), h);
        if (tid == 0) ST4(rc, me + 4, 0, 1u);
    }
    if (role == 3) far ^= FAR(beacon(rsc, 0, 0, 16));
    WSTAMP(2);
    a = args();
    const rsrc_t ri = mkrsrc(a->idx, 32), ru = mkrsrc(a->upd, TOPC * DM * 4), rt = mkrsrc(a->wt, HD * DM * 2);
    const int cg = role == 3 ? 4 * g + (i & 3) : (role == 2 ? h & 15 : i);
    const unsigned wb = side((cg * 64 + 8 * w) * 8192 + far);
    v4u wo[8][4];
    if (role == 3 && i < 28) {
        const rsrc_t rq = mkrsrc(a->qkv, QSZ), rk = mkrsrc(a->qkv + QSZ, QSZ), rv = mkrsrc(a->qkv + 2 * (size_t)QSZ, QSZ), rm = mkrsrc(a->mb, NSEG * NH * 4096);
        unsigned fl = 0;
#pragma unroll 1
        for (int xs = i & 3; xs < NSEG; xs += 4) {
            if ((xs & 60) == a->wi) {
                int l = tid & 63; asm volatile("" : "+v"(l));
                load_wo(lds, mkrs(a->Wo, HD * DM * 2), (256 * w * DM + 64 * cg) * 2, w, l, wo);
#pragma unroll
                for (int k = 0; k < 32; ++k) ST16(rt, wb + l * 16 + (k >> 3) * 16384 + (k & 7) * 1024, 0, wo[k >> 2][k & 3]);
            }
            fl = xattn(rq, rk, rv, rm, rc, err, xs, 8 * g + w, l, fl, far, a->ms, dbg); WSTAMP(9 + (xs >> 2));
        }
        WSTAMP(25);
        return;
    }
    const bool rcv = role == 2 || (role < 2 && g);
    const int k0 = (rcv ? 0 : 64) + 8 * w;
    const u64 mine = a->segs[role < 2 ? g : (role == 2 ? 4 + (g >> 1) : 6)];
    auto next = [&](int s) { return s < NSEG - 1 && mine >> (s + 1) ? s + 1 + __builtin_ctzll(mine >> (s + 1)) : NSEG; };
    auto fx = [&](int s, int l) { return fw(3, (rcv ? 0 : 16) + (l & 1) * 8 + (s & 3)) + 64 + (s & 60); };
    const unsigned fe = F_EX + cg * 256;
    volatile LAS unsigned* prog = (volatile LAS unsigned*)(lds + 2 * RED_B);
    LAS v4f* upd = (LAS v4f*)(lds + 2 * RED_B + 16384);
    u64 hit = 0;
    int sel = 0;
    if (rcv) {
        wait_flags(rc, fw(3, 2 * (cg & 12) + (cg & 3)) + (l & 1 ? 64 + a->wi : 512 + 8), 1, err);
        sel = LD4(ri, (l & 7) * 4, 0);
        const v4u u = LD16(ru, w < 2 ? (tid >> 4) * 4096 + cg * 256 + fr * 16 : OOB, 0);
#pragma unroll
        for (int k = 0; k < 32; ++k) wo[k >> 2][k & 3] = LDB(rt, k & 7, wb + (k >> 3) * 16384);
        if (w < 2) upd[tid] = __builtin_bit_cast(v4f, u);
#pragma unroll
        for (int q = 0; q < 8; ++q) hit |= 1ull << (__builtin_amdgcn_readlane(sel, q) >> 5);
    } else load_wo(lds, mkrs(a->Wo, HD * DM * 2), (32 * k0 * DM + 64 * cg) * 2, w, l, wo);
    WSTAMP(7);
    if (role == 3) {
        wait_flags<16>(rc, fw(3, min(l, NMOE - 1)) + 4, 1, err);
        const rsrc_t rp = mkrsrc(a->part, NMOE * TOPC * DM * 4);
        const unsigned t = LD4(ri, (tid & 112) >> 2, 0), o = (tid & 112) * 256 + cg * 256 + fr * 16;
        v4f p = {0, 0, 0, 0};
#pragma unroll
        for (int q = 0; q < 7; ++q) p += __builtin_bit_cast(v4f, LD16(rp, o, ((w >> 1) * 7 + q) * 32768));
        const float r = rbf(sigmoidf_(hi16(LD4(rc, F_GR + t * 8 + 4, 0))));
        upd[tid] = p;
        BAR();
        p += upd[tid ^ 128]; p += upd[tid ^ 256] + upd[tid ^ 384];
        ST16(ru, w < 2 ? o : OOB, 0, __builtin_bit_cast(v4u, (v4f{rbf(rbf(p[0]) * r), rbf(rbf(p[1]) * r), rbf(rbf(p[2]) * r), rbf(rbf(p[3]) * r)})));
        WAITV(0); RBAR();
        if (tid == 0) ST4(rc, me + 8, 0, 1u);
    }
    __builtin_amdgcn_s_waitcnt(0x0f70);
    bool pend = false;
    v4u ring[16];
    asm volatile("" : RING_ALL("=v"));
    const v4u rso = mkrs(a->qkv, QSZ), rse = mkrs(a->qkv + QSZ, QSZ);
    rsc = mkrs(a->ctl, CTL_SZ);
    const rsrc_t re = mkrsrc(a->qkv + QSZ, QSZ), rout = mkrsrc(a->out, S_TOK * DM * 2);
    BAR();
    const unsigned l16 = l * 16, lw = (unsigned)(size_t)lds + w * 1024, r0 = (w >> 2) * 16 + fr;
    const unsigned xo = rcv ? (r0 * DM + 64 * cg + (w & 3) * 16 + 4 * fq) * 2 : OOB, go = F_GR + r0 * 8, so = w ? OOB : (l < 2 ? fx(0, l) : (rcv ? fe : OOB));
    auto state = [&](int n) { return (unsigned)__builtin_amdgcn_ballot_w64(prog[(n & 1) * 2048 + l * 4] != 0); };
    auto exo = [&](int s) { return side((unsigned)(s * NH + 16 + cg) * 16384u + far) + w * 1024; };
    auto ask = [&](int o, int e) { unsigned u = o < NSEG ? (o & 3) * 128 + (o & 60) : OOB, v = e * 4; asm("" : "+s"(u), "+s"(v)); return so + (l16 < 32 ? u : v); };
    unsigned st;
    WSTAMP(8);
    v4f r[6];
    const LAS char* rp;
    auto get = [&](int n) {
        rp = lds + ((n & 1) * RED_B + w * 8192 + l16);
#pragma unroll
        for (int q = 0; q < 6; ++q) r[q] = *(const LAS v4f*)(rp + q * 1024); };
    auto fin = [&](int seg, bool got, v4f pv, v2u xv, float gt) {
        v4f acc = add4(r[0], r[1]); SCHED();
        r[0] = *(const LAS v4f*)(rp + 6144); r[1] = *(const LAS v4f*)(rp + 7168);
        acc = add4(add4(acc, add4(r[2], r[3])), add4(add4(r[4], r[5]), add4(r[0], r[1])));
        if (rcv) {
            if (!got) { wait_flags(rc, fe + seg * 4, 1, err); pv = __builtin_bit_cast(v4f, LD16(re, l16, exo(seg))); asm volatile("" : "+v"(pv)); }
            v4f b = {lo16(xv[0]), hi16(xv[0]), lo16(xv[1]), hi16(xv[1])};
            if ((hit >> seg) & 1) {
#pragma unroll 1
                for (int q = 0; q < TOPC; ++q) if (__builtin_amdgcn_readlane(sel, q) == seg * 32 + (int)(go - F_GR) / 8) {
                    b += upd[q * 16 + (w & 3) * 4 + (l16 >> 8)]; b = v4f{rbf(b[0]), rbf(b[1]), rbf(b[2]), rbf(b[3])}; }
            }
            acc = add4(acc, pv);
            gt = hi16(__float_as_uint(gt));
            const float omg = rbf(1.0f - gt);
#pragma unroll
            for (int e = 0; e < 4; ++e) acc[e] = add1(rbf(gt * rbf(acc[e])), rbf(omg * b[e]));
            __builtin_amdgcn_raw_buffer_store_b64(v2u{pk2(acc[0], acc[1]), pk2(acc[2], acc[3])}, rout, xo, seg * (32 * DM * 2), 0);
        } else ST16(re, l16, exo(seg), __builtin_bit_cast(v4u, acc));
        FENCE(); };
    unsigned sm; v2u xv; float gt; v4f pv;
    asm volatile("" : "=v"(sm), "=v"(pv), "=v"(xv), "=v"(gt));
    bool got = false;
#define SM_LD "buffer_load_dword %[sm], %[so], %[rc], 0 offen sc1\n\tbuffer_load_dwordx4 %[pv], %[l16], %[re], %[eo] offen sc1\n\tbuffer_load_dwordx2 %[xv], %[xo], %[rx], %[xs] offen\n\tbuffer_load_dword %[gt], %[go], %[rc], %[gs] offen sc1\n\t"
#define SM_O [sm] "+v"(sm), [pv] "+v"(pv), [xv] "+v"(xv), [gt] "+v"(gt)
#define SM_I(QO, QE) [rc] "s"(rsc), [re] "s"(rse), [rx] "s"(rx), [so] "v"(ask(QO, QE)), \
    [l16] "v"(l16), [eo] "s"(got ? exo(seg) : OOB), [xo] "v"(xo), [xs] "s"(seg * (32 * DM * 2)), [go] "v"(go), [gs] "s"(seg * 256)
#pragma unroll 1
    for (int n = 0, seg = next(-1), s1 = next(seg), s2 = next(s1), s3, p1 = OOB / 4, p2 = p1, p3 = p1; seg < NSEG; ++n, p3 = p2, p2 = p1, p1 = seg, seg = s1, s1 = s2, s2 = s3) {
        s3 = next(s2);
        if (!pend) {
            const unsigned ob = side((unsigned)(seg * 128 + k0) * 4096u + far), fo = l16 < 64 ? ask(s1, seg) : fx(seg, l16 >> 4);
            unsigned v;
            for (unsigned tries = 0;; ++tries) {
                FENCE(); v = LD4(rc, fo, 0);
                if ((__builtin_amdgcn_ballot_w64(v != 0) & 48) == 48) break;
                __builtin_amdgcn_s_sleep(6);
                if (spin_fail(tries, err)) break;
            }
            asm volatile(RING_4(0, 1, 2, 3, 18) RING_4(4, 5, 6, 7, 19) RING_4(8, 9, 10, 11, 20) RING_4(12, 13, 14, 15, 21)
                         : RING_ALL("+v") : "v"(l16), "s"(rso), "s"(ob), "s"(ob ^ 16384), "s"(ob ^ 4096), "s"(ob ^ 20480) : "memory");
            prog[(~n & 1) * 2048 + tid * 4] = v;
            BAR();
            st = state(n + 1); got = st & 4;
            asm volatile(SM_LD : SM_O : SM_I(s2, seg) : "memory");
            ST4(rc, OOB, 0, 0u);
        }
        const bool pre = (st & 3) == 3;
        unsigned nb[4], rb = lw + (n & 1) * RED_B + l16, pa = lw + 2 * RED_B + (n & 1) * 8192 + l16;
        nb[0] = pre ? side((unsigned)(s1 * 128 + k0) * 4096u + far) : OOB; nb[1] = nb[0] ^ 16384; nb[2] = nb[0] ^ 4096; nb[3] = nb[0] ^ 20480;
        v4f c[4];
        asm volatile("" : "+s"(nb[0]), "+s"(nb[1]), "+s"(nb[2]), "+s"(nb[3])); asm volatile("" : "+v"(rb), "+v"(pa), "=v"(c[0]), "=v"(c[1]), "=v"(c[2]), "=v"(c[3]));
        OS(0);
        if (pend || (pre && w >= 4)) BAR();
        OS(1);
        RING_OP(0, "s_setprio 0\n\ts_waitcnt vmcnt(20)\n\t" RING_M(0, 6, "0") RING_M(1, 7, "0") "s_waitcnt vmcnt(19)\n\t" RING_M(2, 8, "0") RING_M(3, 9, "0"));
#pragma unroll
        for (int k = 1; k < 16; ++k) {
            if (k == 8) RING_OP(k, RING_LD "s_nop 3\n\t" RING_W(0, 0) RING_M(0, 6, "0") RING_W(1, 8192) RING_M(1, 7, "0") RING_W(2, 16384) RING_M(2, 8, "0") RING_W(3, 24576) RING_M(3, 9, "0") "s_waitcnt vmcnt(19)");
            else if (k == 15) RING_OP(k, RING_M(0, 6, "%0") RING_LD RING_M(1, 7, "%1") RING_M(2, 8, "%2") RING_M(3, 9, "%3"));
            else RING_OP(k, RING_M(0, 6, "%0") RING_LD RING_M(1, 7, "%1") "s_waitcnt vmcnt(19)\n\t" RING_M(2, 8, "%2") RING_M(3, 9, "%3"));
            if (k == 13) asm volatile("s_waitcnt vmcnt(14)\n\tds_write_b32 %4, %0" : SM_O : "v"(pa) : "memory");
        }
        RING_OP(16, RING_LD "s_setprio 1");
        OS(2);
        if (pre || pend) BAR();
        OS(3);
        pa -= lw;
        asm volatile("ds_read_b32 %0, %0\n\ts_nop 3\n\tds_write_b128 %5, %1 offset:32768\n\tds_write_b128 %5, %2 offset:40960\n\tds_write_b128 %5, %3 offset:49152\n\tds_write_b128 %5, %4 offset:57344\n\ts_waitcnt lgkmcnt(4)"
                     : "+v"(pa) : "v"(c[0]), "v"(c[1]), "v"(c[2]), "v"(c[3]), "v"(rb) : "memory");
        OS(4);
        if (pre || pend) {
            get(n - 1);
            const bool got1 = got;
            st = __builtin_amdgcn_ballot_w64(pa != 0); got = st & 4;
            v4f pv1 = pv; v2u xv1 = xv; float gt1 = gt;
            asm volatile(SM_LD : SM_O, "+v"(pv1), "+v"(xv1), "+v"(gt1) : SM_I(s3, s1) : "memory");
            OS(5);
            if (pend) fin(p1, got1, pv1, xv1, gt1); else ST4(rc, OOB, 0, 0u);
            if (!rcv && tid == 256) ST4(rc, fe + p3 * 4, 0, 1u);
        }
        OS(6);
        if (!pre) {
            BAR();
            if (pend && w < 4) RBAR();
            get(n);
            asm volatile("s_waitcnt vmcnt(1)" : SM_O :: "memory");
            fin(seg, got, pv, xv, gt);
            if (!rcv) { WAITV(0); RBAR(); if (tid < 3) ST4(rc, fe + (tid ? (tid == 1 ? p1 : p2) : seg) * 4, 0, 1u); }
        }
        pend = pre;
        WSTAMP(9 + (seg >> 2));
    }
    asm volatile("s_waitcnt vmcnt(0)" :: RING_ALL("v") : "memory");
    WSTAMP(25);
}

__global__ void __launch_bounds__(512, 2) k_main(Args) {
    __shared__ __attribute__((aligned(16))) char lds[L_TOT];
    const int b = blockIdx.x, c = (b & 3) * 32 + (b >> 3);
    if ((b & 7) < 4) chain((LAS char*)lds, c); else worker((LAS char*)lds, c);
}

#define RING_MB 160
#define TUNE_DEFAULT {184, 73, 77, 138, 8, 80, 336, 100, 700, 700, 1, 20, 2, 9, 38, 20}

extern "C" void kernel_launch(void* const* d_in, const int* in_sizes, int n_in, void* d_out, int out_size, void* d_ws, size_t ws_size, hipStream_t stream) {
    char* ring = (char*)(((size_t)d_ws + SLOT - 1) & ~(size_t)(SLOT - 1));
    char* ws = ring + NSLOT * SLOT; size_t off = 0;
    if (ws_size >= ((size_t)RING_MB + 12 << 20)) ring += (size_t)RING_MB << 20;
    auto alloc = [&](size_t bytes) { void* p = ws + off; off += (bytes + 255) & ~(size_t)255; return p; };
    Args a;
    a.x = (const bf16_t*)d_in[0]; a.mem0 = (const bf16_t*)d_in[1]; a.Wq = (const bf16_t*)d_in[2]; a.Wk = (const bf16_t*)d_in[3]; a.Wv = (const bf16_t*)d_in[4]; a.Wo = (const bf16_t*)d_in[5];
    a.w_r = (const bf16_t*)d_in[8]; a.wg = (const bf16_t*)d_in[9]; a.wu = (const bf16_t*)d_in[10]; a.wd = (const bf16_t*)d_in[11]; a.gw = (const bf16_t*)d_in[12];
    a.qkv = (char*)alloc(3 * (size_t)QSZ); a.wt = (char*)alloc(HD * DM * 2); a.mb = (char*)alloc(NSEG * NH * 4096);
    a.idx = (int*)alloc(256); a.part = (float*)alloc(NMOE * TOPC * DM * 4); a.upd = (float*)alloc(TOPC * DM * 4); a.out = (bf16_t*)d_out;
    a.ring = ring; a.ctl = (unsigned*)alloc(CTL_SZ); a.dbg = (unsigned*)alloc((65536 + 1024 * 512) * 4);
    const int tn[16] = TUNE_DEFAULT;
    a.per = tn[0] * 368.0f; a.per0 = tn[0] * 368 / tn[6]; a.pf = tn[10]; a.ms = tn[11]; a.ws = tn[12]; a.wi = tn[15]; a.gap = 0;
    a.d1 = ((u64)tn[1] << 32) / tn[0]; a.dv = ((u64)tn[2] << 32) / tn[0]; a.d2 = ((u64)tn[3] << 32) / tn[0]; a.dp = tn[4]; a.slack = tn[5]; a.thr = tn[6]; a.spare = tn[7]; a.wq = tn[8]; a.wx = tn[9];
    for (int c = 0; c < 7; ++c) a.segs[c] = 0;
    for (int s = 0; s < NSEG; ++s) {
        a.segs[1 + (s < tn[13] ? s % 3 : (s - tn[13]) % 5)] |= 1ull << s;
        a.segs[s >= tn[14] && !((s - tn[14]) & 1) ? 0 : 6] |= 1ull << s;
    }
    k_main<<<256, 512, 0, stream>>>(a);
}
```

```cpp
#include <hip/hip_runtime.h>
#include <stdint.h>

typedef unsigned short bf16_t;
typedef unsigned v2u __attribute__((ext_vector_type(2)));
typedef unsigned v4u __attribute__((ext_vector_type(4)));
typedef unsigned v8u __attribute__((ext_vector_type(8)));
typedef float v2f __attribute__((ext_vector_type(2)));
typedef float v4f __attribute__((ext_vector_type(4)));
typedef short v4s __attribute__((ext_vector_type(4)));
typedef __bf16 v2b __attribute__((ext_vector_type(2)));
typedef __bf16 v8b __attribute__((ext_vector_type(8)));
typedef __bf16 v16b __attribute__((ext_vector_type(16)));
typedef unsigned long long u64;
typedef __amdgpu_buffer_rsrc_t rsrc_t;
#define LAS __attribute__((address_space(3)))
#define DEV __device__ __forceinline__

static constexpr int S_TOK = 2048, DM = 1024, NH = 32, DH = 128, SEG = 32, NSEG = 64;
static constexpr int HD = NH * DH;
static constexpr int FF = 3584, TOPC = 8;
static constexpr float SCALE = 0.08838834764831845f, LOG2E = 1.4426950408889634f, MASKV = -998244352.0f;

DEV unsigned pk2(float lo, float hi) { v2f f = {lo, hi}; return __builtin_bit_cast(unsigned, __builtin_convertvector(f, v2b)); }
DEV float lo16(unsigned u) { return __uint_as_float(u << 16); }
DEV float hi16(unsigned u) { return __uint_as_float(u & 0xffff0000u); }
DEV float rbf(float f) { return __uint_as_float(pk2(0.0f, f)); }
DEV v4f smf(v4u a, v8u b, v4f c, int idx) { return __builtin_amdgcn_smfmac_f32_16x16x64_bf16(__builtin_bit_cast(v8b, a), __builtin_bit_cast(v16b, b), c, idx, 0, 0); }
DEV v4f mf(v4u a, v4u b, v4f c) { return __builtin_amdgcn_mfma_f32_16x16x32_bf16(__builtin_bit_cast(v8b, a), __builtin_bit_cast(v8b, b), c, 0, 0, 0); }
DEV v2u trd(const LAS char* p) { return __builtin_bit_cast(v2u, __builtin_amdgcn_ds_read_tr16_b64_v4i16((LAS v4s*)p)); }
DEV v4u cat(v2u a, v2u b) { return __builtin_shufflevector(a, b, 0, 1, 2, 3); }
DEV v2u sw16(unsigned x, unsigned y) { return __builtin_amdgcn_permlane16_swap(x, y, false, false); }
DEV v2u sw32(unsigned x, unsigned y) { return __builtin_amdgcn_permlane32_swap(x, y, false, false); }
DEV float half_add(float x, float y) { v2u r = sw32(__float_as_uint(x), __float_as_uint(y)); return __uint_as_float(r[0]) + __uint_as_float(r[1]); }
DEV float ror8(float v) { return __int_as_float(__builtin_amdgcn_update_dpp(0, __float_as_int(v), 0x128, 0xf, 0xf, false)); }
template <bool MAX> DEV float red_g(float v) {
    auto op = [](float a, float b) { return MAX ? fmaxf(a, b) : a + b; };
    v2u r = sw16(__float_as_uint(v), __float_as_uint(v)); v = op(__uint_as_float(r[0]), __uint_as_float(r[1]));
    r = sw32(__float_as_uint(v), __float_as_uint(v)); return op(__uint_as_float(r[0]), __uint_as_float(r[1]));
}
DEV float add1(float a, float b) { float r; asm("v_add_f32 %0, %1, %2" : "=v"(r) : "v"(a), "v"(b)); return r; }
DEV v4f add4(v4f a, v4f b) { return v4f{add1(a[0], b[0]), add1(a[1], b[1]), add1(a[2], b[2]), add1(a[3], b[3])}; }
DEV float sigmoidf_(float z) { return 1.0f / (1.0f + __expf(-z)); }
DEV v4u mkrs(const void* p, unsigned bytes) { const u64 a = (u64)p; return v4u{(unsigned)a, (unsigned)(a >> 32) & 0xffffu, bytes, 0x00020000u}; }
DEV rsrc_t mkrsrc(const void* p, unsigned bytes) { return __builtin_amdgcn_make_buffer_rsrc((void*)p, (short)0, (int)bytes, 0x00020000); }

#define ST16(RS, OFF, SOFF, V) __builtin_amdgcn_raw_buffer_store_b128(V, RS, (OFF) + (SOFF), 0, 16)
#define NTS 0
#define ST16X(RS, OFF, V, NT) __builtin_amdgcn_raw_buffer_store_b128(V, RS, OFF, 0, (NT) ? 18 : 16)
#define LD16X(RS, OFF, SOFF, NT) __builtin_amdgcn_raw_buffer_load_b128(RS, OFF, SOFF, (NT) ? 18 : 16)
#define ST4(RS, OFF, SOFF, V) __builtin_amdgcn_raw_buffer_store_b32(V, RS, OFF, SOFF, 16)
#define LD16(RS, OFF, SOFF) __builtin_amdgcn_raw_buffer_load_b128(RS, OFF, SOFF, 16)
#define LD8(RS, OFF, SOFF) __builtin_amdgcn_raw_buffer_load_b64(RS, OFF, SOFF, 16)
#define LD4(RS, OFF, SOFF) __builtin_amdgcn_raw_buffer_load_b32(RS, OFF, SOFF, 16)
#define DMA16(RS, VOFF, SOFF, LOFF) asm volatile("s_mov_b32 m0, %0\n\ts_nop 0\n\tbuffer_load_dwordx4 %1, %2, %3 offen lds" :: "s"(LOFF), "v"(VOFF), "s"(RS), "s"(SOFF) : "memory", "m0")
#define DMA16N(RS, VOFF, SOFF, LOFF) asm volatile("s_mov_b32 m0, %0\n\ts_nop 0\n\tbuffer_load_dwordx4 %1, %2, %3 offen nt lds" :: "s"(LOFF), "v"(VOFF), "s"(RS), "s"(SOFF) : "memory", "m0")
#define WSTAMP(k) do {} while (0)
#define EXP 0
#define XI 3
#define BK 9
#define FENCE() asm volatile("" ::: "memory")
#define WAITV(n) asm volatile("s_waitcnt vmcnt(" #n ")" ::: "memory")
#define WAITL(n) asm volatile("s_waitcnt lgkmcnt(" #n ")" ::: "memory")
#define RBAR() asm volatile("s_barrier" ::: "memory")
#define BAR() asm volatile("s_waitcnt lgkmcnt(0)\n\ts_barrier" ::: "memory")
#define SCHED() __builtin_amdgcn_sched_barrier(0)

#define KVS(m, h) ((((m) & 2 ? (h) : (m)) & 1u) << 13)
#define FAR(u) ((~(u) & 1u) << 13)
DEV unsigned side(unsigned o) { return o ^ ((o >> 8) & 8192u); }
static constexpr int XP = 1056, XS = 32 * XP, L_RED = 4 * XS;

DEV unsigned wt_src(int tid, unsigned ldw) { return (tid >> 4) * ldw + (((tid & 15) ^ ((((tid >> 4) & 3) << 2) | (((tid >> 7) & 1) << 1))) << 4); }
DEV int wt_rd(int w, int l, int n) { const int fr = l & 15, fq = l >> 4; return (w >> 2) * 16384 + (8 * fq + (fr >> 2)) * 256 + (((4 * (w & 3) + 2 * n + ((fr >> 1) & 1)) ^ (((fr >> 2) << 2) | ((fq & 1) << 1))) << 4) + (fr & 1) * 8; }
DEV v4u wt_frag(const LAS char* p) { return cat(trd(p), trd(p + 1024)); }
#define WT_ISSUE(R0, R1, VB, S0, S1, LDW, LB) do { DMA16N(R0, VB, S0, LB); DMA16N(R0, VB, (S0) + 32 * (LDW), (LB) + 8192); DMA16N(R1, VB, S1, (LB) + 16384); DMA16N(R1, VB, (S1) + 32 * (LDW), (LB) + 24576); } while (0)

static constexpr unsigned F_BC = 512, F_CK = 2048, F_EX = 12288, F_W = 16384, F_PG = 32768, F_GR = 49152, CTL_SZ = 65536;
DEV unsigned fw(int role, int h) { return F_W + ((h >> 3) * 32 + role * 8 + (h & 7)) * 128; }
static constexpr unsigned POI = 0xFFFFFFFFu, OOB = 0x40000000u, SPIN_CAP = 200000u;
DEV bool spin_fail(unsigned tries, unsigned* err) {
    __builtin_amdgcn_s_sleep(1);
    if ((tries & 255u) != 255u) return false;
    unsigned e; asm volatile("global_load_dword %0, %1, off sc1\n\ts_waitcnt vmcnt(0)" : "=&v"(e) : "v"(err) : "memory");
    if (__builtin_amdgcn_readfirstlane(e) == 1u) return true;
    if (tries > SPIN_CAP) { unsigned one = 1u; asm volatile("global_store_dword %0, %1, off sc1" :: "v"(err), "v"(one) : "memory"); return true; }
    return false;
}
DEV bool all_ge(unsigned v, unsigned t) { return __builtin_amdgcn_ballot_w64(v < t) == 0; }
template <int Z = 6> DEV void wait_flags(rsrc_t rc, unsigned off, unsigned t, unsigned* err) {
    for (unsigned tries = 0;; ++tries) {
        FENCE();
        if (all_ge(LD4(rc, off, 0), t)) break;
        __builtin_amdgcn_s_sleep(Z);
        if (spin_fail(tries, err)) break;
    }
    FENCE();
}

#define BC_LD(B, RS) asm volatile("buffer_load_dword %0, off, %1, 0 offset:512 sc1" : "=&v"(B) : "s"(RS) : "memory")
DEV unsigned beacon(v4u rs, unsigned b0, unsigned b1, unsigned need, bool seen = false) {
    asm volatile("" : "+v"(b0), "+v"(b1));
    for (unsigned tries = 0;; ++tries) {
        const unsigned u = __builtin_amdgcn_readfirstlane(b1);
        if (((seen || u != __builtin_amdgcn_readfirstlane(b0)) && (u & need) == need) || tries > SPIN_CAP) return u;
        __builtin_amdgcn_s_sleep(4);
        BC_LD(b1, rs); asm volatile("s_waitcnt vmcnt(0)" : "+v"(b1) :: "memory");
    }
}

template <bool VT> DEV unsigned stream_qkv(LAS char* lds, v4u rx, v4u rw, rsrc_t ro, rsrc_t rc, v4u rsc, bool hs, unsigned fo, int h, unsigned fx, unsigned pf, unsigned* dbg) {
    unsigned far = 0;
    const int tid = threadIdx.x, w = __builtin_amdgcn_readfirstlane(tid >> 6), l = tid & 63, kh = w >> 2, cg = w & 3;
    const unsigned lb = (unsigned)(size_t)lds, vb = wt_src(tid, HD * 2), l16 = l * 16;
    const unsigned x0 = (4 * cg + (VT ? 2 : 16) * (1 - kh)) * 2048 + kh * 1024, x1 = (4 * cg + (VT ? 2 : 16) * kh) * 2048 + kh * 1024;
#define STW(s) WT_ISSUE(rw, rw, vb, h * 256 + (s) * (64 * HD * 2), h * 256 + ((s) + 8) * (64 * HD * 2), HD * 2, lb + w * 1024 + ((s) & 3) * XS)
#define STX(u, q, e) DMA16(rx, l16, ((u) >> 1) * 65536 + ((q) & 1 ? x1 : x0) + (VT ? ((e) & 1) + 16 * ((e) >> 1) : (e)) * 2048, lb + (w * 4 + (e)) * XP + (q) * XS)
    const unsigned fl = tid == 0 ? fo : OOB;
    v4u W[16][2];
    unsigned b0, b1;
    BC_LD(b0, rsc);
    if (pf & 4) asm volatile("s_mov_b32 m0, %0\n\ts_nop 0\n\tbuffer_load_dword %1, %2, %3 offen lds"
                             :: "s"(lb + L_RED), "v"(hs && l < 16 ? l * 128 : OOB), "s"(rx), "s"((((h & 7) * 2 + VT) * 8 + w) * 2048) : "memory", "m0");
    STW(0); STW(1); STW(2); STW(3);
#pragma unroll
    for (int s = 0; s < 8; ++s) {
        WAITV(12); RBAR();
#pragma unroll
        for (int n = 0; n < 2; ++n) { const LAS char* p = lds + wt_rd(w, l, n) + (s & 3) * XS; W[2 * s][n] = wt_frag(p); W[2 * s + 1][n] = wt_frag(p + 8192); }
        BAR();
        if (s == 4) BC_LD(b1, rsc);
        if (s == 7) { far ^= FAR(beacon(rsc, b0, b1, 16, !hs)); ST4(rc, OOB, 0, 0u); FENCE(); }
        if (s < 4) STW(s + 4); else { STX(s - 4, s - 4, 0); STX(s - 4, s - 4, 1); if (s < 7) { STX(s - 4, s - 4, 2); STX(s - 4, s - 4, 3); } }
    }
    const LAS char* px = lds + (kh * 16 + (l & 15)) * XP + (l >> 4) * 16;
    LAS v4f* ps = (LAS v4f*)(lds + L_RED + w * 2048 + l16);
    const LAS v4f* pr = (const LAS v4f*)(lds + L_RED + (w ^ 4) * 2048 + l16);
    unsigned oo = OOB, on = (unsigned)h * 16384u + (far ^ fx) + cg * 2048u + kh * 1024u + l16;
    v4f c[2][2] = {}, r0 = {}, r1 = {};
    v4u f[8];
    auto fin = [&]() { r0 = add4(r0, c[1][0]); r1 = add4(r1, c[1][1]); ST16X(ro, oo, (v4u{pk2(r0[0], r0[1]), pk2(r0[2], r0[3]), pk2(r1[0], r1[1]), pk2(r1[2], r1[3])}), NTS & 1); oo = side(on); on += NH * 16384u; };
    WSTAMP(3);
    WAITV(10); RBAR();
    WSTAMP(4);
    const LAS char* px2 = px + 2 * XS; asm volatile("" : "+v"(px2));
#define RDX(q, k) f[(k) & 7] = *(const LAS v4u*)(((q) & 2 ? px2 : px) + ((q) & 1) * XS + (k) * 64)
#pragma unroll
    for (int k = 0; k < 8; ++k) RDX(0, k);
#pragma unroll 1
    for (int u0 = 0; u0 < 2 * NSEG; u0 += 4) {
        if (u0 == 4) WSTAMP(5);
        if (u0 == 64) WSTAMP(6);
#pragma unroll
        for (int q = 0; q < 4; ++q) {
#pragma unroll
            for (int k = 0; k < 16; ++k) {
                const v4u x = f[k & 7];
#pragma unroll
                for (int n = 0; n < 2; ++n) { const v4f z = k ? c[q & 1][n] : v4f{0, 0, 0, 0}; c[q & 1][n] = VT ? mf(x, W[k][n], z) : mf(W[k][n], x, z); }
                if (k < 8) RDX(q, k + 8);
                if (k == BK) { SCHED(); WAITV(10); BAR(); for (int e = 0; e < BK - 8; ++e) RDX(q + 1, e); }
                if (k >= BK) RDX(q + 1, k - 8);
                if (((k + 16 - BK) & 3) == 0) asm volatile("s_setprio %0" :: "n"(3 - (((k + 16 - BK) >> 2) & 3)));
                if (k == 2) { if (q & 1) { ps[0] = c[0][0]; ps[64] = c[0][1]; } else fin(); }
                if (k == 13) { if (q & 1) { r0 = pr[0]; r1 = pr[64]; } else ST4(rc, fl, 0, max((u0 + q - 2) >> 1, 0)); }
                if (!(EXP & 1) && !(k & 1) && kh == (((k >> 1) ^ ((BK + 1) >> 1)) & 1)) { if (k > BK) STX(u0 + q + 4, q, (k - BK) >> 2); else STX(u0 + q + 3, (q + 3) & 3, (k + 16 - BK) >> 2); }
                SCHED();
            }
        }
    }
    asm volatile("s_nop 7" : "+v"(c[1][0]), "+v"(c[1][1]));
    fin(); __builtin_amdgcn_s_setprio(0);
    WAITV(0); BAR();
    if (tid == 0) ST4(rc, fo, 0, NSEG);
    return far;
#undef STW
#undef STX
}

#define LDBX(RS, K, BASE, NT) LD16X(RS, l * 16 + ((K) & 3) * 1024, (BASE) + ((K) >> 2) * 4096, NT)
#define LDB(RS, K, BASE) LDBX(RS, K, BASE, 0)
DEV unsigned xattn(rsrc_t rq, rsrc_t rk, rsrc_t rv, rsrc_t rm, rsrc_t rc, unsigned* err, int seg, int h, int l, unsigned fl, unsigned far, unsigned ms, unsigned* dbg) {
#define XST(k) do {} while (0)
    XST(3);
    asm volatile("" : "+v"(l));
    const int fr = l & 15, g = l >> 4;
    const unsigned sb = side((unsigned)(seg * NH + h) * 16384u + far), mb = (unsigned)(seg * NH + h) * 4096u;
    const unsigned f0 = fw(min(l & 3, 2), h) + 12, f1 = F_PG + h * 512 + (l & 3) * 128;
    const unsigned old = __builtin_amdgcn_ballot_w64(fl <= (unsigned)seg);
    if (old & 15) wait_flags(rc, f0, seg + 1, err);
    XST(4);
    v4u q[4][2], k[4][2], km[4], v[8]; v2u vm[8];
    auto ldm = [&]() {
#pragma unroll
        for (int j = 0; j < 4; ++j) km[j] = LD16(rm, (g * 8 + (l & 7)) * 16 + j * 512, mb);
#pragma unroll
        for (int d = 0; d < 8; ++d) vm[d] = LD8(rm, 2048 + fr * 16 + (g & 1) * 8 + d * 256, mb); };
#pragma unroll
    for (int j = 0; j < 8; ++j) q[j >> 1][j & 1] = LDBX(rq, j, sb, 1);
    if (!(old & 240)) {
#pragma unroll
        for (int j = 0; j < 8; ++j) k[j >> 1][j & 1] = LDBX(rk, j, sb ^ KVS(ms >> 2, h), 1);
#pragma unroll
        for (int d = 0; d < 8; ++d) v[d] = LDBX(rv, d, sb ^ KVS(ms >> 4, h), 1);
        ldm();
    } else {
#pragma unroll
        for (int j = 0; j < 8; ++j) k[j >> 1][j & 1] = LDB(rk, j, sb ^ KVS(ms >> 2, h));
#pragma unroll
        for (int d = 0; d < 8; ++d) v[d] = LDB(rv, d, sb ^ KVS(ms >> 4, h));
    }
    v4f sx[3] = {}, o[2][8];
    float mx[2], zx[2];
    constexpr float C = SCALE * LOG2E, MR = MASKV / SCALE;
#pragma unroll
    for (int j = 0; j < 4; ++j) { sx[0] = mf(k[j][0], q[j][0], sx[0]); sx[1] = mf(k[j][0], q[j][1], sx[1]); sx[2] = mf(k[j][1], q[j][1], sx[2]); }
#pragma unroll
    for (int u = 0; u < 2; ++u) {
        float e[8];
#pragma unroll
        for (int r = 0; r < 4; ++r) { const bool vis = 4 * g + r <= fr; e[r] = (u || vis) ? sx[u][r] : MR; e[4 + r] = (u && vis) ? sx[2][r] : MR; }
        float m = e[0];
#pragma unroll
        for (int r = 1; r < 8; ++r) m = fmaxf(m, e[r]);
        mx[u] = m = red_g<true>(m); zx[u] = 0;
#pragma unroll
        for (int r = 0; r < 8; ++r) { e[r] = __builtin_amdgcn_exp2f((e[r] - m) * C); zx[u] += e[r]; }
        const v4u P = {pk2(e[0], e[1]), pk2(e[4], e[5]), pk2(e[2], e[3]), pk2(e[6], e[7])};
#pragma unroll
        for (int d = 0; d < 8; d += 2) {
            o[u][d] = mf(__builtin_shufflevector(v[d], v[d + 1], 0, 1, 4, 5), P, v4f{0, 0, 0, 0});
            o[u][d + 1] = mf(__builtin_shufflevector(v[d], v[d + 1], 2, 3, 6, 7), P, v4f{0, 0, 0, 0});
        }
    }
    asm volatile("" : "+v"(zx[0]), "+v"(zx[1]));
    SCHED(); XST(5);
    if (old & 240) { wait_flags<2>(rc, f1, seg + 1, err); ldm(); }
    XST(6);
    v4f sm[2] = {};
#pragma unroll
    for (int j = 0; j < 4; ++j) { sm[0] = mf(km[j], q[j][0], sm[0]); sm[1] = mf(km[j], q[j][1], sm[1]); }
    XST(28);
#pragma unroll
    for (int u = 0; u < 2; ++u) {
        const float m = red_g<true>(fmaxf(fmaxf(fmaxf(sm[u][0], sm[u][1]), fmaxf(sm[u][2], sm[u][3])), mx[u])), al = __builtin_amdgcn_exp2f((mx[u] - m) * C);
        float e[4];
#pragma unroll
        for (int r = 0; r < 4; ++r) e[r] = __builtin_amdgcn_exp2f((sm[u][r] - m) * C);
        const float inv = __builtin_amdgcn_rcpf(red_g<false>(al * zx[u] + 0.5f * (e[0] + e[1] + e[2] + e[3]))), f = al * inv, im = g < 2 ? inv : 0.0f;
        const unsigned pm0 = pk2(e[0] * im, e[1] * im), pm1 = pk2(e[2] * im, e[3] * im);
        const v4u Pa = {pm0, pm1, 0u, 0u}, Pb = {0u, 0u, pm0, pm1};
#pragma unroll
        for (int d = 0; d < 4; ++d) {
            const v4u vv = cat(vm[2 * d], vm[2 * d + 1]);
            const v4f o0 = mf(vv, Pa, o[u][2 * d] * f), o1 = mf(vv, Pb, o[u][2 * d + 1] * f);
            ST16(rq, l * 16 + d * 1024, sb + u * 4096, (v4u{pk2(o0[0], o0[1]), pk2(o0[2], o0[3]), pk2(o1[0], o1[1]), pk2(o1[2], o1[3])}));
        }
    }
    XST(29);
    FENCE(); const unsigned fn = LD4(rc, fl > seg + 4u ? OOB : (l & 4 ? f1 : f0), 0);
    WAITV(1); XST(30); RBAR(); XST(31);
    if (threadIdx.x == 0) ST4(rc, fw(3, (h & 24) + (seg & 3)) + 64 + (seg & 60), 0, 1u);
    FENCE();
    return max(fl, fn);
}

template <int N> DEV unsigned ror(unsigned v) { return __builtin_amdgcn_update_dpp(0u, v, 0x120 + N, 0xf, 0xf, false); }
DEV void prep_rows(const bf16_t* x, const bf16_t* gw, const bf16_t* wr, rsrc_t rg, int row, int l) {
    v4u xv[16], gv[2], rv[2];
#pragma unroll
    for (int i = 0; i < 2; ++i) { gv[i] = *(const v4u*)(gw + i * 512 + l * 8); rv[i] = *(const v4u*)(wr + i * 512 + l * 8); }
#pragma unroll
    for (int i = 0; i < 16; ++i) xv[i] = *(const v4u*)(x + (size_t)row * DM + i * 512 + l * 8);
    float ga = 0, gb = 0;
#pragma unroll
    for (int k = 0; k < 8; ++k) {
        float a = 0, b = 0;
#pragma unroll
        for (int i = 0; i < 8; ++i) {
            const unsigned u = xv[2 * k + (i >> 2)][i & 3], g = gv[i >> 2][i & 3], r = rv[i >> 2][i & 3];
            a += rbf(lo16(u) * lo16(g)) + rbf(hi16(u) * hi16(g));
            b += lo16(u) * lo16(r) + hi16(u) * hi16(r);
        }
        a = half_add(a, b);
        a += __uint_as_float(ror<8>(__float_as_uint(a))); a += __uint_as_float(ror<4>(__float_as_uint(a)));
        a += __uint_as_float(ror<2>(__float_as_uint(a))); a += __uint_as_float(ror<1>(__float_as_uint(a)));
        const v2u s = sw16(__float_as_uint(a), __float_as_uint(a)); a = __uint_as_float(s[0]) + __uint_as_float(s[1]);
        if ((l & 31) == k) ga = a;
    }
    gb = rbf(ga);
    const unsigned u = __float_as_uint(l < 32 ? rbf(sigmoidf_(gb)) : gb) | 1u;
    const v2u e = sw32(u, u);
    ST16(rg, l < 8 && !(l & 1) ? F_GR + (row + l) * 8 : OOB, 0, (v4u{e[0], e[1], ror<15>(e[0]), ror<15>(e[1])}));
}
template <int N> DEV unsigned top_keys(unsigned* key, int l) {
    unsigned res = 0;
#pragma unroll 1
    for (int c = 0; c < TOPC; ++c) {
        unsigned m = key[0];
#pragma unroll
        for (int i = 1; i < N; ++i) m = max(m, key[i]);
        m = max(m, ror<8>(m)); m = max(m, ror<4>(m)); m = max(m, ror<2>(m)); m = max(m, ror<1>(m));
        v2u s = sw16(m, m); m = max(s[0], s[1]); s = sw32(m, m); m = max(s[0], s[1]);
#pragma unroll
        for (int i = 0; i < N; ++i) key[i] = key[i] == m ? 0u : key[i];
        if (l == c) res = m;
    }
    return res;
}
DEV int top8(LAS unsigned* cand, rsrc_t rc, unsigned* err, int w, int l) {
    v4u t[2];
    for (unsigned tries = 0;; ++tries) {
        FENCE();
        t[0] = LD16(rc, F_GR + w * 2048 + l * 16, 0); t[1] = LD16(rc, F_GR + w * 2048 + 1024 + l * 16, 0);
        const v4u v = t[0] & t[1];
        if (all_ge(v[0] & v[1] & v[2] & v[3] & 1u, 1u)) break;
        __builtin_amdgcn_s_sleep(8);
        if (spin_fail(tries, err)) break;
    }
    unsigned key[4];
#pragma unroll
    for (int i = 0; i < 4; ++i) { const unsigned r = t[i >> 1][(i & 1) * 2 + 1] - 1u; key[i] = ((int)r < 0 ? 0xffff0000u - r : r + 0x80000000u) + (0xffffu - (w * 256 + (i >> 1) * 128 + l * 2 + (i & 1))); }
    key[0] = top_keys<4>(key, l);
    if (l < TOPC) cand[w * TOPC + l] = key[0];
    BAR();
    key[0] = cand[l];
    return 0xffff - (int)(top_keys<1>(key, l) & 0xffffu);
}
static constexpr int NMOE = FF / 128, XR = 2080;
DEV void moe(LAS char* lds, v4u rx, int sel, v4u rg, v4u ru, v4u rd, rsrc_t rp, int cu) {
    const int tid = threadIdx.x, w = __builtin_amdgcn_readfirstlane(tid >> 6), l = tid & 63, kh = w >> 2, cg = w & 3, fr = l & 15, fq = l >> 4;
    const unsigned lb = (unsigned)(size_t)lds, v1 = wt_src(tid, FF * 2), v2 = wt_src(tid, DM * 2);
    const unsigned xo = __builtin_amdgcn_readlane(sel, w) * 2048;
    DMA16(rx, l * 16, xo, lb + L_RED + w * XR); DMA16(rx, l * 16, xo + 1024, lb + L_RED + w * XR + 1024);
    auto issue = [&](int s) {
        const unsigned d = lb + w * 1024 + (s & 3) * XS;
        if (s < 16) { const unsigned o = s * (64 * FF * 2) + cu * 256; WT_ISSUE(rg, ru, v1, o, o, FF * 2, d); }
        else { const unsigned o = s < 24 ? (cu * 128 + (s & 1) * 64) * (DM * 2) + (((s >> 1) + cu) & 3) * 512 : OOB; WT_ISSUE(rd, rd, v2, o, o + 256, DM * 2, d); } };
    issue(0); issue(1); issue(2); issue(3);
    const int p0 = wt_rd(w, l, 0), p1 = wt_rd(w, l, 1);
    v4f c[2] = {};
    auto stage = [&](int s, const LAS char* pa) {
        WAITV(12); RBAR();
        const LAS char* p = lds + (s & 3) * XS;
        const v4u a0 = *(const LAS v4u*)pa, a1 = *(const LAS v4u*)(pa + 64), b0 = wt_frag(p + p0), b1 = wt_frag(p + p1), b2 = wt_frag(p + p0 + 8192), b3 = wt_frag(p + p1 + 8192);
        BAR();
        issue(s + 4);
        c[0] = mf(b2, a1, mf(b0, a0, c[0])); c[1] = mf(b3, a1, mf(b1, a0, c[1])); };
#pragma unroll 1
    for (int s = 0; s < 16; ++s) stage(s, lds + L_RED + (l & 7) * XR + fq * 16 + s * 128);
    LAS v4f* pu = (LAS v4f*)(lds + L_RED + cg * 2048 + l * 16);
    if (kh) { pu[0] = c[0]; pu[64] = c[1]; }
    BAR();
    if (!kh && fr < 8) {
#pragma unroll
        for (int n = 0; n < 2; ++n) {
            const v4f u = pu[64 * n]; float hv[4];
#pragma unroll
            for (int e = 0; e < 4; ++e) { const float g = rbf(c[n][e]); hv[e] = rbf(g * sigmoidf_(g)) * rbf(u[e]); }
            *(LAS v2u*)(lds + L_RED + 8192 + fr * 288 + cg * 64 + n * 32 + fq * 8) = v2u{pk2(hv[0], hv[1]), pk2(hv[2], hv[3])};
        }
    }
    WAITL(0);
#pragma unroll 1
    for (int s = 16; s < 24; s += 2) {
        c[0] = v4f{0, 0, 0, 0}; c[1] = c[0];
        const LAS char* pa = lds + L_RED + 8192 + (l & 7) * 288 + fq * 16;
        stage(s, pa); stage(s + 1, pa + 128);
        ST16(rp, cu * 32768 + (l & 7) * 4096 + ((((s >> 1) + cu) & 3) * 256 + kh * 128 + cg * 32 + (fr >> 3) * 16 + fq * 4) * 4, 0, __builtin_bit_cast(v4u, fr < 8 ? c[0] : c[1]));
    }
    WAITV(0); RBAR();
}

static constexpr unsigned H2_OFF = 0, H1_OFF = 512 * 1024, H3_OFF = 768 * 1024, PR_OFF = 896 * 1024;
static constexpr size_t SLOT = 2u << 20;
static constexpr int NSLOT = 4;
struct Args {
    const bf16_t* x; const bf16_t* mem0; const bf16_t* Wq; const bf16_t* Wk; const bf16_t* Wv; const bf16_t* Wo;
    const bf16_t* w_r; const bf16_t* wg; const bf16_t* wu; const bf16_t* wd; const bf16_t* gw;
    char* qkv; char* mb; int* idx; float* part; float* upd; char* wt; bf16_t* out;
    char* ring; unsigned* ctl; unsigned* dbg;
    u64 segs[7];
    float per; unsigned per0, d1, dv, d2, dp, slack, thr, spare, wq, wx, pf, ms, ws, wi, gap;
};
typedef const __attribute__((address_space(4))) Args* KArgs;
DEV KArgs args() { KArgs p = (KArgs)__builtin_amdgcn_kernarg_segment_ptr(); asm volatile("" : "+s"(p)); return p; }
static constexpr unsigned QSZ = S_TOK * HD * 4;
typedef unsigned v16u __attribute__((ext_vector_type(16)));
typedef unsigned v32u __attribute__((ext_vector_type(32)));

DEV unsigned nearo(unsigned off) { return (off >> 13) * 16384u + (off & 8191u); }
DEV unsigned rt32() { u64 t; asm volatile("s_memrealtime %0\n\ts_waitcnt lgkmcnt(0)" : "=s"(t) :: "memory"); return (unsigned)t; }
DEV unsigned cyc32() { u64 t; asm volatile("s_memtime %0\n\ts_waitcnt lgkmcnt(0)" : "=s"(t) :: "memory"); return (unsigned)t; }
template <int R1, int R2, int R3> DEV v8u rd_w(const LAS char* p) {
    return __builtin_shufflevector(cat(trd(p), trd(p + R1)), cat(trd(p + R2), trd(p + R3)), 0, 1, 2, 3, 4, 5, 6, 7);
}
DEV v16u cat8(v8u a, v8u b) { return __builtin_shufflevector(a, b, 0, 1, 2, 3, 4, 5, 6, 7, 8, 9, 10, 11, 12, 13, 14, 15); }
DEV v32u cat16(v16u a, v16u b) { return __builtin_shufflevector(a, b, 0, 1, 2, 3, 4, 5, 6, 7, 8, 9, 10, 11, 12, 13, 14, 15, 16, 17, 18, 19, 20, 21, 22, 23, 24, 25, 26, 27, 28, 29, 30, 31); }
DEV v8u cat4(v4u a, v4u b) { return __builtin_shufflevector(a, b, 0, 1, 2, 3, 4, 5, 6, 7); }

static constexpr int L_PQK = 0, L_PV = 8704, L_P = 13824, L_VT = 15360, L_X = 16384, L_K = L_X + 9216, L_MISC = 28672, L_PF = 29696, L_STG = 32768, L_TOT = 163840;
static constexpr int X_FQ = 272, X_BLK = 1152;
#define ASM_A \
    "s_setprio 1\n" \
    "s_mov_b32 s76, 0\n" \
    "s_mov_b32 s99, 0x20000\n" \
    "s_mov_b32 s84, 0\n" \
    "s_mov_b32 s81, 0\n" \
    "s_mov_b32 s86, 0\n" \
    "s_mov_b32 s87, 0\n" \
    "s_mov_b32 s88, 0\n" \
    "s_mov_b32 s89, 0\n" \
    "s_mov_b32 s90, 0\n" \
    "s_mov_b32 s91, 0\n" \
    "s_mov_b32 s92, 0\n" \
    "s_mov_b32 s93, 0\n" \
    "s_mov_b32 s94, 0\n" \
    "s_mov_b32 s95, 0\n" \
    "s_mov_b32 s96, 0\n" \
    "s_mov_b32 s97, 0\n" \
    "s_mov_b32 s98, 0\n" \
    "ds_read_b32 v244, v237\n" \
    "s_waitcnt lgkmcnt(0)\n" \
    "v_readfirstlane_b32 s94, v244\n" \
    "s_mul_i32 s79, s94, s70\n" \
    "s_add_u32 s79, s79, s81\n" \
    "s_mov_b32 s77, s79\n" \
    "s_mul_i32 s96, s65, s71\n" \
    "s_lshr_b32 s96, s96, 4\n" \
    "v_mov_b64 v[208:209], 0\n" \
    "v_mov_b64 v[210:211], 0\n" \
    "v_mov_b64 v[212:213], 0\n" \
    "v_mov_b64 v[214:215], 0\n" \
    "v_mov_b64 v[216:217], 0\n" \
    "v_mov_b64 v[218:219], 0\n" \
    "v_mov_b64 v[220:221], 0\n" \
    "v_mov_b64 v[222:223], 0\n" \
    "v_mov_b64 v[224:225], 0\n" \
    "v_mov_b64 v[226:227], 0\n" \
    "v_mov_b64 v[228:229], 0\n" \
    "v_mov_b64 v[230:231], 0\n" \
    "v_mov_b32 v241, 1\n" \
    "s_branch L%=_Acomp\n" \
    "L%=_Aloop:\n" \
    "s_and_b32 s95, s94, 1\n" \
    "s_lshl_b32 s95, s95, 17\n" \
    "s_add_u32 s95, s95, s60\n" \
    "s_add_u32 s85, s95, 0x1000\n" \
    "s_branch L%=_Au2b\n" \
    "L%=_Au2a:\n" \
    "s_sleep 4\n" \
    "L%=_Au2b:\n" \
    "s_memrealtime vcc\n" \
    "s_add_u32 s80, s77, s64\n" \
    "s_add_u32 s80, s80, -3\n" \
    "s_waitcnt lgkmcnt(0)\n" \
    "s_sub_i32 s79, s80, vcc_lo\n" \
    "s_sub_u32 s79, s79, 40\n" \
    "s_cmp_lt_u32 s79, 0x400\n" \
    "s_cbranch_scc1 L%=_Au2a\n" \
    "s_memtime s[38:39]\n" \
    "s_memrealtime vcc\n" \
    "s_nop 4\n" \
    "s_memrealtime s[32:33]\n" \
    "s_nop 4\n" \
    "s_memrealtime s[34:35]\n" \
    "s_nop 4\n" \
    "s_memrealtime s[36:37]\n" \
    "s_add_u32 s80, s77, s64\n" \
    "s_lshl_b32 s80, s80, 2\n" \
    "s_waitcnt lgkmcnt(0)\n" \
    "s_sub_u32 s80, s80, vcc_lo\n" \
    "s_sub_u32 s80, s80, s32\n" \
    "s_sub_u32 s80, s80, s34\n" \
    "s_sub_u32 s80, s80, s36\n" \
    "s_mul_i32 s80, s80, s71\n" \
    "s_ashr_i32 s80, s80, 6\n" \
    "s_add_u32 s78, s80, s38\n" \
    "L%=_Au3a:\n" \
    "s_memtime s[38:39]\n" \
    "s_mul_i32 s80, -3, 23\n" \
    "s_add_u32 s80, s80, s78\n" \
    "s_waitcnt lgkmcnt(0)\n" \
    "s_sub_i32 s79, s80, s38\n" \
    "s_max_i32 s79, s79, 0\n" \
    "s_cmp_gt_u32 s79, 320\n" \
    "s_cbranch_scc1 L%=_Au3s\n" \
    ".p2align 4\n" \
    "L%=_Au3d:\n" \
    "s_sub_u32 s79, s79, 28\n" \
    "s_cbranch_scc0 L%=_Au3d\n" \
    "L%=_Au3e:\n" \
    "L%=_Apoll:\n" \
    "ds_read_b32 v244, v237 offset:4\n" \
    "s_waitcnt lgkmcnt(0)\n" \
    "v_readfirstlane_b32 s79, v244\n" \
    "s_cmp_lt_u32 s79, s76\n" \
    "s_cbranch_scc1 L%=_Askip\n" \
    "L%=_Ago:\n" \
    "buffer_load_dwordx4 v[192:195], v233, s[40:43], s95 offen sc1\n" \
    "s_nop 5\n" \
    "buffer_load_dwordx4 v[196:199], v233, s[40:43], s85 offen sc1\n" \
    "s_nop 5\n" \
    "buffer_load_dwordx4 v[200:203], v239, s[40:43], s95 offen sc1\n" \
    "s_nop 5\n" \
    "buffer_load_dwordx4 v[204:207], v239, s[40:43], s85 offen sc1\n" \
    "L%=_Acomp:\n" \
    "s_mov_b32 m0, s72\n" \
    "s_waitcnt vmcnt(3)\n" \
    "v_smfmac_f32_16x16x64_bf16 v[208:211], v[192:195], v[0:7], v232\n" \
    "v_smfmac_f32_16x16x64_bf16 v[212:215], v[192:195], v[8:15], v232\n" \
    "v_smfmac_f32_16x16x64_bf16 v[216:219], v[192:195], v[64:71], v232\n" \
    "v_smfmac_f32_16x16x64_bf16 v[220:223], v[192:195], v[72:79], v232\n" \
    "v_smfmac_f32_16x16x64_bf16 v[224:227], v[192:195], v[128:135], v232\n" \
    "s_waitcnt vmcnt(2)\n" \
    "v_smfmac_f32_16x16x64_bf16 v[208:211], v[196:199], v[16:23], v232\n" \
    "v_smfmac_f32_16x16x64_bf16 v[212:215], v[196:199], v[24:31], v232\n" \
    "v_smfmac_f32_16x16x64_bf16 v[216:219], v[196:199], v[80:87], v232\n" \
    "v_smfmac_f32_16x16x64_bf16 v[220:223], v[196:199], v[88:95], v232\n" \
    "v_smfmac_f32_16x16x64_bf16 v[228:231], v[192:195], v[136:143], v232\n" \
    "s_waitcnt vmcnt(1)\n" \
    "v_smfmac_f32_16x16x64_bf16 v[208:211], v[200:203], v[32:39], v232\n" \
    "v_smfmac_f32_16x16x64_bf16 v[212:215], v[200:203], v[40:47], v232\n" \
    "v_smfmac_f32_16x16x64_bf16 v[216:219], v[200:203], v[96:103], v232\n" \
    "v_smfmac_f32_16x16x64_bf16 v[220:223], v[200:203], v[104:111], v232\n" \
    "v_smfmac_f32_16x16x64_bf16 v[224:227], v[196:199], v[144:151], v232\n" \
    "s_waitcnt vmcnt(0)\n" \
    "v_smfmac_f32_16x16x64_bf16 v[208:211], v[204:207], v[48:55], v232\n" \
    "v_smfmac_f32_16x16x64_bf16 v[212:215], v[204:207], v[56:63], v232\n" \
    "v_smfmac_f32_16x16x64_bf16 v[216:219], v[204:207], v[112:119], v232\n" \
    "v_add_f32 v208, v208, v210\n" \
    "v_smfmac_f32_16x16x64_bf16 v[220:223], v[204:207], v[120:127], v232\n" \
    "v_add_f32 v209, v209, v211\n" \
    "v_cmp_u_f32 vcc, v208, v209\n" \
    "v_add_f32 v212, v212, v214\n" \
    "v_add_f32 v213, v213, v215\n" \
    "v_add_f32 v216, v216, v218\n" \
    "v_add_f32 v217, v217, v219\n" \
    "s_nop 1\n" \
    "v_add_f32 v220, v220, v222\n" \
    "v_add_f32 v221, v221, v223\n" \
    "v_smfmac_f32_16x16x64_bf16 v[224:227], v[200:203], v[160:167], v232\n" \
    "s_cbranch_vccnz L%=_Aslow\n" \
    "L%=_Aok:\n" \
    "ds_write_addtid_b32 v208 offset:0\n" \
    "ds_write_addtid_b32 v209 offset:544\n" \
    "v_smfmac_f32_16x16x64_bf16 v[228:231], v[196:199], v[152:159], v232\n" \
    "ds_write_addtid_b32 v216 offset:256\n" \
    "ds_write_addtid_b32 v217 offset:800\n" \
    "v_smfmac_f32_16x16x64_bf16 v[224:227], v[204:207], v[176:183], v232\n" \
    "ds_write_addtid_b32 v212 offset:1088\n" \
    "ds_write_addtid_b32 v213 offset:1632\n" \
    "v_smfmac_f32_16x16x64_bf16 v[228:231], v[200:203], v[168:175], v232\n" \
    "ds_write_addtid_b32 v220 offset:1344\n" \
    "ds_write_addtid_b32 v221 offset:1888\n" \
    "ds_write_b32 v236, v241 offset:32\n" \
    "v_smfmac_f32_16x16x64_bf16 v[228:231], v[204:207], v[184:191], v232\n" \
    "s_barrier\n" \
    "s_mov_b32 m0, s73\n" \
    "v_add_f32 v224, v224, v226\n" \
    "v_add_f32 v225, v225, v227\n" \
    "ds_write_addtid_b32 v224 offset:8704\n" \
    "ds_write_addtid_b32 v225 offset:8992\n" \
    "s_nop 1\n" \
    "v_add_f32 v228, v228, v230\n" \
    "v_add_f32 v229, v229, v231\n" \
    "ds_write_addtid_b32 v228 offset:9344\n" \
    "ds_write_addtid_b32 v229 offset:9632\n" \
    "ds_write_b32 v236, v241 offset:16\n" \
    "v_mov_b64 v[208:209], 0\n" \
    "v_mov_b64 v[210:211], 0\n" \
    "v_mov_b64 v[212:213], 0\n" \
    "v_mov_b64 v[214:215], 0\n" \
    "v_mov_b64 v[216:217], 0\n" \
    "v_mov_b64 v[218:219], 0\n" \
    "v_mov_b64 v[220:221], 0\n" \
    "v_mov_b64 v[222:223], 0\n" \
    "v_mov_b64 v[224:225], 0\n" \
    "v_mov_b64 v[226:227], 0\n" \
    "v_mov_b64 v[228:229], 0\n" \
    "v_mov_b64 v[230:231], 0\n" \
    "v_add_u32 v241, 1, v241\n" \
    "ds_read_b32 v244, v237\n" \
    "s_waitcnt lgkmcnt(0)\n" \
    "v_readfirstlane_b32 s94, v244\n" \
    "s_mul_i32 s79, s94, s70\n" \
    "s_add_u32 s79, s79, s81\n" \
    "s_mov_b32 s77, s79\n" \
    "s_barrier\n" \
    "ds_read_b64 v[244:245], v237 offset:64\n" \
    "s_mov_b32 s79, s60\n" \
    "s_mov_b32 s60, s61\n" \
    "s_mov_b32 s61, s62\n" \
    "s_mov_b32 s62, s63\n" \
    "s_mov_b32 s63, s79\n" \
    "s_add_u32 s76, s76, 1\n" \
    "s_add_u32 s77, s77, s65\n" \
    "s_add_u32 s84, s84, 0x20000\n" \
    "s_add_u32 s81, s81, s65\n" \
    "s_waitcnt lgkmcnt(0)\n" \
    "v_readfirstlane_b32 s65, v244\n" \
    "v_readfirstlane_b32 s71, v245\n" \
    "s_mul_i32 s96, s65, s71\n" \
    "s_lshr_b32 s96, s96, 4\n" \
    "s_cmp_lt_u32 s76, 64\n" \
    "s_cbranch_scc1 L%=_Aloop\n" \
    "s_branch L%=_end\n" \
    "L%=_Aslow:\n" \
    "s_nop 7\n" \
    "s_nop 0\n" \
    "v_max_u32 v244, v192, v193\n" \
    "v_max3_u32 v244, v244, v194, v195\n" \
    "v_max3_u32 v244, v244, v196, v197\n" \
    "v_max3_u32 v244, v244, v198, v199\n" \
    "v_max3_u32 v244, v244, v200, v201\n" \
    "v_max3_u32 v244, v244, v202, v203\n" \
    "v_max3_u32 v244, v244, v204, v205\n" \
    "v_max3_u32 v244, v244, v206, v207\n" \
    "v_cmp_eq_u32 vcc, -1, v244\n" \
    "s_cbranch_vccz L%=_Aok\n" \
    "s_cmp_eq_u32 s99, 0\n" \
    "s_cbranch_scc1 L%=_Aok\n" \
    "s_sub_u32 s99, s99, 1\n" \
    "s_cmp_lg_u32 s99, 0\n" \
    "s_cbranch_scc1 L%=_Au5\n" \
    "v_mov_b32 v244, 1\n" \
    "buffer_store_dword v244, off, s[44:47], 0 sc1\n" \
    "L%=_Au5:\n" \
    "v_mov_b64 v[208:209], 0\n" \
    "v_mov_b64 v[210:211], 0\n" \
    "v_mov_b64 v[212:213], 0\n" \
    "v_mov_b64 v[214:215], 0\n" \
    "v_mov_b64 v[216:217], 0\n" \
    "v_mov_b64 v[218:219], 0\n" \
    "v_mov_b64 v[220:221], 0\n" \
    "v_mov_b64 v[222:223], 0\n" \
    "v_mov_b64 v[224:225], 0\n" \
    "v_mov_b64 v[226:227], 0\n" \
    "v_mov_b64 v[228:229], 0\n" \
    "v_mov_b64 v[230:231], 0\n" \
    "L%=_Ash:\n" \
    "s_memrealtime vcc\n" \
    "s_waitcnt lgkmcnt(0)\n" \
    "s_sub_u32 s80, vcc_lo, s64\n" \
    "s_sub_u32 s80, s80, 4\n" \
    "s_mul_i32 s32, s70, s71\n" \
    "s_lshr_b32 s32, s32, 4\n" \
    "L%=_Au6g:\n" \
    "s_add_u32 s77, s77, s70\n" \
    "s_add_u32 s78, s78, s32\n" \
    "s_add_u32 s94, s94, 1\n" \
    "s_sub_u32 s79, s80, s77\n" \
    "s_cmp_lt_u32 s79, 0x1000000\n" \
    "s_cbranch_scc1 L%=_Au6g\n" \
    "v_mov_b32 v244, s94\n" \
    "ds_max_u32 v237, v244\n" \
    "s_cmp_gt_i32 s79, -20\n" \
    "s_cbranch_scc1 L%=_Au6c\n" \
    "s_branch L%=_Au7b\n" \
    "L%=_Au7a:\n" \
    "s_sleep 4\n" \
    "L%=_Au7b:\n" \
    "s_memrealtime vcc\n" \
    "s_add_u32 s80, s77, s64\n" \
    "s_add_u32 s80, s80, -3\n" \
    "s_waitcnt lgkmcnt(0)\n" \
    "s_sub_i32 s79, s80, vcc_lo\n" \
    "s_sub_u32 s79, s79, 40\n" \
    "s_cmp_lt_u32 s79, 0x400\n" \
    "s_cbranch_scc1 L%=_Au7a\n" \
    "s_memtime s[38:39]\n" \
    "s_memrealtime vcc\n" \
    "s_nop 4\n" \
    "s_memrealtime s[32:33]\n" \
    "s_nop 4\n" \
    "s_memrealtime s[34:35]\n" \
    "s_nop 4\n" \
    "s_memrealtime s[36:37]\n" \
    "s_add_u32 s80, s77, s64\n" \
    "s_lshl_b32 s80, s80, 2\n" \
    "s_waitcnt lgkmcnt(0)\n" \
    "s_sub_u32 s80, s80, vcc_lo\n" \
    "s_sub_u32 s80, s80, s32\n" \
    "s_sub_u32 s80, s80, s34\n" \
    "s_sub_u32 s80, s80, s36\n" \
    "s_mul_i32 s80, s80, s71\n" \
    "s_ashr_i32 s80, s80, 6\n" \
    "s_add_u32 s78, s80, s38\n" \
    "L%=_Au6c:\n" \
    "s_and_b32 s95, s94, 1\n" \
    "s_lshl_b32 s95, s95, 17\n" \
    "s_add_u32 s95, s95, s60\n" \
    "s_add_u32 s85, s95, 0x1000\n" \
    "L%=_Au8a:\n" \
    "s_memtime s[38:39]\n" \
    "s_mul_i32 s80, -3, 23\n" \
    "s_add_u32 s80, s80, s78\n" \
    "s_waitcnt lgkmcnt(0)\n" \
    "s_sub_i32 s79, s80, s38\n" \
    "s_max_i32 s79, s79, 0\n" \
    "s_cmp_gt_u32 s79, 320\n" \
    "s_cbranch_scc1 L%=_Au8s\n" \
    ".p2align 4\n" \
    "L%=_Au8d:\n" \
    "s_sub_u32 s79, s79, 28\n" \
    "s_cbranch_scc0 L%=_Au8d\n" \
    "L%=_Au8e:\n" \
    "s_branch L%=_Apoll\n" \
    "L%=_Askip:\n" \
    "s_cmp_lt_u32 s99, 2\n" \
    "s_cbranch_scc1 L%=_Ago\n" \
    "s_sub_u32 s99, s99, 1\n" \
    "s_branch L%=_Ash\n" \
    "L%=_Au3s:\n" \
    "s_cmp_gt_u32 s79, 0x8000\n" \
    "s_cbranch_scc1 L%=_Au3e\n" \
    "s_sleep 2\n" \
    "s_branch L%=_Au3a\n" \
    "L%=_Au8s:\n" \
    "s_cmp_gt_u32 s79, 0x8000\n" \
    "s_cbranch_scc1 L%=_Au8e\n" \
    "s_sleep 2\n" \
    "s_branch L%=_Au8a\n" \
    "L%=_end:\n"
#define ASM_B \
    "s_and_b32 s79, s78, 3\n" \
    "s_cmp_eq_u32 s79, 1\n" \
    "s_cbranch_scc1 L%=_S\n" \
    "s_cmp_eq_u32 s79, 2\n" \
    "s_cbranch_scc1 L%=_R\n" \
    "s_cmp_eq_u32 s79, 3\n" \
    "s_cbranch_scc1 L%=_P\n" \
    "L%=_T:\n" \
    "s_mov_b32 s76, 0\n" \
    "s_mov_b32 s99, 0x20000\n" \
    "s_mov_b32 s84, 0\n" \
    "s_mov_b32 s81, 0\n" \
    "s_mov_b32 s86, 0\n" \
    "s_mov_b32 s87, 0\n" \
    "s_mov_b32 s88, 0\n" \
    "s_mov_b32 s89, 0\n" \
    "s_mov_b32 s90, 0\n" \
    "s_mov_b32 s91, 0\n" \
    "s_mov_b32 s92, 0\n" \
    "s_mov_b32 s93, 0\n" \
    "s_mov_b32 s94, 0\n" \
    "s_mov_b32 s95, 0\n" \
    "s_mov_b32 s96, 0\n" \
    "s_mov_b32 s97, 0\n" \
    "s_mov_b32 s98, 0\n" \
    "ds_read_b32 v216, v229\n" \
    "s_waitcnt lgkmcnt(0)\n" \
    "v_readfirstlane_b32 s94, v216\n" \
    "s_mul_i32 s79, s94, s70\n" \
    "s_add_u32 s79, s79, s81\n" \
    "s_mov_b32 s77, s79\n" \
    "s_mul_i32 s96, s65, s71\n" \
    "s_lshr_b32 s96, s96, 4\n" \
    "v_mov_b32 v96, 0x3f803f80\n" \
    "v_mov_b32 v97, 0x3f803f80\n" \
    "v_mov_b32 v98, 0x3f803f80\n" \
    "v_mov_b32 v99, 0x3f803f80\n" \
    "v_mov_b32 v100, 0x3f803f80\n" \
    "v_mov_b32 v101, 0x3f803f80\n" \
    "v_mov_b64 v[102:103], 0\n" \
    "v_mov_b64 v[64:65], 0\n" \
    "v_mov_b64 v[66:67], 0\n" \
    "v_mov_b64 v[68:69], 0\n" \
    "v_mov_b64 v[70:71], 0\n" \
    "v_mov_b64 v[72:73], 0\n" \
    "v_mov_b64 v[74:75], 0\n" \
    "v_mov_b64 v[76:77], 0\n" \
    "v_mov_b64 v[78:79], 0\n" \
    "v_mov_b64 v[80:81], 0\n" \
    "v_mov_b64 v[82:83], 0\n" \
    "v_mov_b64 v[84:85], 0\n" \
    "v_mov_b64 v[86:87], 0\n" \
    "v_mov_b64 v[88:89], 0\n" \
    "v_mov_b64 v[90:91], 0\n" \
    "v_mov_b64 v[92:93], 0\n" \
    "v_mov_b64 v[94:95], 0\n" \
    "v_mov_b64 v[192:193], 0\n" \
    "v_mov_b64 v[194:195], 0\n" \
    "L%=_Tloop:\n" \
    "ds_read_b64_tr_b16 v[104:105], v228 offset:16384\n" \
    "ds_read_b64_tr_b16 v[106:107], v228 offset:16416\n" \
    "ds_read_b64_tr_b16 v[108:109], v228 offset:18688\n" \
    "ds_read_b64_tr_b16 v[110:111], v228 offset:18720\n" \
    "ds_read_b64_tr_b16 v[112:113], v228 offset:20992\n" \
    "ds_read_b64_tr_b16 v[114:115], v228 offset:21024\n" \
    "ds_read_b64_tr_b16 v[116:117], v228 offset:23296\n" \
    "ds_read_b64_tr_b16 v[118:119], v228 offset:23328\n" \
    "ds_read_b64_tr_b16 v[120:121], v228 offset:16656\n" \
    "ds_read_b64_tr_b16 v[122:123], v228 offset:16688\n" \
    "ds_read_b64_tr_b16 v[124:125], v228 offset:18960\n" \
    "ds_read_b64_tr_b16 v[126:127], v228 offset:18992\n" \
    "ds_read_b64_tr_b16 v[128:129], v228 offset:21264\n" \
    "ds_read_b64_tr_b16 v[130:131], v228 offset:21296\n" \
    "ds_read_b64_tr_b16 v[132:133], v228 offset:23568\n" \
    "ds_read_b64_tr_b16 v[134:135], v228 offset:23600\n" \
    "s_waitcnt lgkmcnt(14)\n" \
    "v_mfma_f32_16x16x32_bf16 v[136:139], v[104:107], v[0:3], 0\n" \
    "v_mfma_f32_16x16x32_bf16 v[144:147], v[104:107], v[8:11], 0\n" \
    "v_mfma_f32_16x16x32_bf16 v[152:155], v[104:107], v[16:19], 0\n" \
    "v_mfma_f32_16x16x32_bf16 v[160:163], v[104:107], v[24:27], 0\n" \
    "s_waitcnt lgkmcnt(12)\n" \
    "v_mfma_f32_16x16x32_bf16 v[136:139], v[108:111], v[4:7], v[136:139]\n" \
    "v_mfma_f32_16x16x32_bf16 v[144:147], v[108:111], v[12:15], v[144:147]\n" \
    "v_mfma_f32_16x16x32_bf16 v[152:155], v[108:111], v[20:23], v[152:155]\n" \
    "v_mfma_f32_16x16x32_bf16 v[160:163], v[108:111], v[28:31], v[160:163]\n" \
    "s_waitcnt lgkmcnt(10)\n" \
    "v_mfma_f32_16x16x32_bf16 v[136:139], v[112:115], v[32:35], v[136:139]\n" \
    "v_mfma_f32_16x16x32_bf16 v[144:147], v[112:115], v[40:43], v[144:147]\n" \
    "v_mfma_f32_16x16x32_bf16 v[152:155], v[112:115], v[48:51], v[152:155]\n" \
    "v_mfma_f32_16x16x32_bf16 v[160:163], v[112:115], v[56:59], v[160:163]\n" \
    "s_waitcnt lgkmcnt(8)\n" \
    "v_mfma_f32_16x16x32_bf16 v[136:139], v[116:119], v[36:39], v[136:139]\n" \
    "v_mfma_f32_16x16x32_bf16 v[144:147], v[116:119], v[44:47], v[144:147]\n" \
    "v_mfma_f32_16x16x32_bf16 v[152:155], v[116:119], v[52:55], v[152:155]\n" \
    "v_mfma_f32_16x16x32_bf16 v[160:163], v[116:119], v[60:63], v[160:163]\n" \
    "s_waitcnt lgkmcnt(6)\n" \
    "v_mfma_f32_16x16x32_bf16 v[140:143], v[120:123], v[0:3], 0\n" \
    "v_mfma_f32_16x16x32_bf16 v[148:151], v[120:123], v[8:11], 0\n" \
    "v_mfma_f32_16x16x32_bf16 v[156:159], v[120:123], v[16:19], 0\n" \
    "v_mfma_f32_16x16x32_bf16 v[164:167], v[120:123], v[24:27], 0\n" \
    "s_waitcnt lgkmcnt(4)\n" \
    "v_mfma_f32_16x16x32_bf16 v[140:143], v[124:127], v[4:7], v[140:143]\n" \
    "v_mfma_f32_16x16x32_bf16 v[148:151], v[124:127], v[12:15], v[148:151]\n" \
    "v_mfma_f32_16x16x32_bf16 v[156:159], v[124:127], v[20:23], v[156:159]\n" \
    "v_mfma_f32_16x16x32_bf16 v[164:167], v[124:127], v[28:31], v[164:167]\n" \
    "s_waitcnt lgkmcnt(2)\n" \
    "v_mfma_f32_16x16x32_bf16 v[140:143], v[128:131], v[32:35], v[140:143]\n" \
    "v_mfma_f32_16x16x32_bf16 v[148:151], v[128:131], v[40:43], v[148:151]\n" \
    "v_mfma_f32_16x16x32_bf16 v[156:159], v[128:131], v[48:51], v[156:159]\n" \
    "v_mfma_f32_16x16x32_bf16 v[164:167], v[128:131], v[56:59], v[164:167]\n" \
    "s_waitcnt lgkmcnt(0)\n" \
    "v_mfma_f32_16x16x32_bf16 v[140:143], v[132:135], v[36:39], v[140:143]\n" \
    "v_mfma_f32_16x16x32_bf16 v[148:151], v[132:135], v[44:47], v[148:151]\n" \
    "v_mfma_f32_16x16x32_bf16 v[156:159], v[132:135], v[52:55], v[156:159]\n" \
    "v_mfma_f32_16x16x32_bf16 v[164:167], v[132:135], v[60:63], v[164:167]\n" \
    "v_cvt_pk_bf16_f32 v65, v136, v137\n" \
    "v_cvt_pk_bf16_f32 v68, v138, v139\n" \
    "v_cvt_pk_bf16_f32 v73, v144, v145\n" \
    "v_cvt_pk_bf16_f32 v76, v146, v147\n" \
    "v_cvt_pk_bf16_f32 v81, v152, v153\n" \
    "v_cvt_pk_bf16_f32 v84, v154, v155\n" \
    "v_cvt_pk_bf16_f32 v89, v160, v161\n" \
    "v_cvt_pk_bf16_f32 v92, v162, v163\n" \
    "v_cvt_pk_bf16_f32 v66, v142, v143\n" \
    "v_cvt_pk_bf16_f32 v67, v142, v143\n" \
    "v_cvt_pk_bf16_f32 v69, v140, v141\n" \
    "v_cvt_pk_bf16_f32 v74, v150, v151\n" \
    "v_cvt_pk_bf16_f32 v75, v150, v151\n" \
    "v_cvt_pk_bf16_f32 v77, v148, v149\n" \
    "v_cvt_pk_bf16_f32 v82, v158, v159\n" \
    "v_cvt_pk_bf16_f32 v83, v158, v159\n" \
    "v_cvt_pk_bf16_f32 v85, v156, v157\n" \
    "v_cvt_pk_bf16_f32 v90, v166, v167\n" \
    "v_cvt_pk_bf16_f32 v91, v166, v167\n" \
    "v_cvt_pk_bf16_f32 v93, v164, v165\n" \
    "v_permlane16_swap_b32 v66, v67\n" \
    "v_permlane16_swap_b32 v74, v75\n" \
    "v_permlane16_swap_b32 v82, v83\n" \
    "v_permlane16_swap_b32 v90, v91\n" \
    "v_mov_b64 v[176:177], 0\n" \
    "v_mov_b64 v[178:179], 0\n" \
    "v_mov_b64 v[180:181], 0\n" \
    "v_mov_b64 v[182:183], 0\n" \
    "v_mov_b64 v[184:185], 0\n" \
    "v_mov_b64 v[186:187], 0\n" \
    "v_mov_b64 v[188:189], 0\n" \
    "v_mov_b64 v[190:191], 0\n" \
    "v_mov_b64 v[196:197], 0\n" \
    "v_mov_b64 v[198:199], 0\n" \
    "v_mov_b64 v[200:201], 0\n" \
    "v_mov_b64 v[202:203], 0\n" \
    "v_mov_b64 v[204:205], 0\n" \
    "v_mov_b64 v[206:207], 0\n" \
    "v_mov_b64 v[208:209], 0\n" \
    "v_mov_b64 v[210:211], 0\n" \
    "v_mov_b64 v[212:213], 0\n" \
    "v_mov_b64 v[214:215], 0\n" \
    "s_add_u32 s85, s60, 0x1000\n" \
    "s_branch L%=_Tu2b\n" \
    "L%=_Tu2a:\n" \
    "s_sleep 4\n" \
    "L%=_Tu2b:\n" \
    "s_memrealtime vcc\n" \
    "s_add_u32 s80, s77, s64\n" \
    "s_add_u32 s80, s80, s69\n" \
    "s_waitcnt lgkmcnt(0)\n" \
    "s_sub_i32 s79, s80, vcc_lo\n" \
    "s_sub_u32 s79, s79, 40\n" \
    "s_cmp_lt_u32 s79, 0x400\n" \
    "s_cbranch_scc1 L%=_Tu2a\n" \
    "s_memtime s[38:39]\n" \
    "s_memrealtime vcc\n" \
    "s_nop 4\n" \
    "s_memrealtime s[32:33]\n" \
    "s_nop 4\n" \
    "s_memrealtime s[34:35]\n" \
    "s_nop 4\n" \
    "s_memrealtime s[36:37]\n" \
    "s_add_u32 s80, s77, s64\n" \
    "s_lshl_b32 s80, s80, 2\n" \
    "s_waitcnt lgkmcnt(0)\n" \
    "s_sub_u32 s80, s80, vcc_lo\n" \
    "s_sub_u32 s80, s80, s32\n" \
    "s_sub_u32 s80, s80, s34\n" \
    "s_sub_u32 s80, s80, s36\n" \
    "s_mul_i32 s80, s80, s71\n" \
    "s_ashr_i32 s80, s80, 6\n" \
    "s_add_u32 s78, s80, s38\n" \
    "v_mov_b32 v143, s76\n" \
    "s_barrier\n" \
    "s_cmp_eq_u32 s76, 63\n" \
    "s_cbranch_scc1 L%=_Tfin\n" \
    "ds_read_b32 v216, v229\n" \
    "s_waitcnt lgkmcnt(0)\n" \
    "v_readfirstlane_b32 s94, v216\n" \
    "s_mul_i32 s79, s94, s70\n" \
    "s_add_u32 s79, s79, s81\n" \
    "s_cmp_lg_u32 s79, s77\n" \
    "s_cbranch_scc1 L%=_Tu3s\n" \
    "L%=_Tu3k:\n" \
    "L%=_Tu4a:\n" \
    "s_memtime s[38:39]\n" \
    "s_mul_hi_u32 s80, s66, s96\n" \
    "s_add_u32 s80, s80, s78\n" \
    "s_waitcnt lgkmcnt(0)\n" \
    "s_sub_i32 s79, s80, s38\n" \
    "s_max_i32 s79, s79, 0\n" \
    "s_cmp_gt_u32 s79, 320\n" \
    "s_cbranch_scc1 L%=_Tu4s\n" \
    ".p2align 4\n" \
    "L%=_Tu4d:\n" \
    "s_sub_u32 s79, s79, 28\n" \
    "s_cbranch_scc0 L%=_Tu4d\n" \
    "L%=_Tu4e:\n" \
    "buffer_load_dwordx4 v[104:107], v230, s[40:43], s60 offen offset:0 sc1\n" \
    "buffer_load_dwordx2 v[120:121], v231, s[40:43], s60 offen offset:0 sc1\n" \
    "buffer_load_dwordx4 v[108:111], v230, s[40:43], s60 offen offset:2048 sc1\n" \
    "buffer_load_dwordx2 v[122:123], v231, s[40:43], s60 offen offset:2048 sc1\n" \
    "buffer_load_dwordx4 v[112:115], v230, s[40:43], s85 offen offset:0 sc1\n" \
    "buffer_load_dwordx2 v[124:125], v231, s[40:43], s85 offen offset:0 sc1\n" \
    "buffer_load_dwordx4 v[116:119], v230, s[40:43], s85 offen offset:2048 sc1\n" \
    "buffer_load_dwordx2 v[126:127], v231, s[40:43], s85 offen offset:2048 sc1\n" \
    "s_setprio 3\n" \
    "buffer_load_dwordx4 v[168:171], v225, s[40:43], s60 offen sc1\n" \
    "buffer_load_dwordx4 v[172:175], v225, s[40:43], s85 offen sc1\n" \
    "L%=_Tsc:\n" \
    "s_waitcnt vmcnt(6)\n" \
    "v_pk_add_f32 v[128:129], v[104:105], v[108:109]\n" \
    "v_pk_add_f32 v[130:131], v[106:107], v[110:111]\n" \
    "v_pk_add_f32 v[132:133], v[120:121], v[122:123]\n" \
    "s_waitcnt vmcnt(4)\n" \
    "v_pk_add_f32 v[128:129], v[128:129], v[112:113]\n" \
    "v_pk_add_f32 v[130:131], v[130:131], v[114:115]\n" \
    "v_pk_add_f32 v[132:133], v[132:133], v[124:125]\n" \
    "s_waitcnt vmcnt(2)\n" \
    "v_pk_add_f32 v[128:129], v[128:129], v[116:117]\n" \
    "v_pk_add_f32 v[130:131], v[130:131], v[118:119]\n" \
    "v_pk_add_f32 v[132:133], v[132:133], v[126:127]\n" \
    "v_max3_f32 v136, v128, v129, v130\n" \
    "v_max3_f32 v137, v131, v132, v133\n" \
    "v_max_f32 v136, v136, v137\n" \
    "v_fma_f32 v141, v131, v132, v133\n" \
    "v_fma_f32 v140, v128, v129, v130\n" \
    "v_max_f32_dpp v138, v136, v136 row_ror:8 row_mask:0xf bank_mask:0xf\n" \
    "v_max_f32_dpp v139, v136, v136 row_ror:8 row_mask:0xf bank_mask:0xf\n" \
    "s_waitcnt vmcnt(0)\n" \
    "v_smfmac_f32_16x16x64_bf16 v[176:179], v[168:171], v[0:7], v224\n" \
    "v_cmp_u_f32 vcc, v140, v141\n" \
    "v_permlane16_swap_b32 v138, v139\n" \
    "v_max_f32 v136, v138, v139\n" \
    "v_max_f32 v137, v138, v139\n" \
    "v_smfmac_f32_16x16x64_bf16 v[180:183], v[168:171], v[8:15], v224\n" \
    "s_cbranch_vccnz L%=_Tslow\n" \
    "L%=_Tok:\n" \
    "v_permlane32_swap_b32 v136, v137\n" \
    "v_max_f32 v136, v136, v137\n" \
    "v_mul_f32 v136, s72, v136\n" \
    "v_pk_fma_f32 v[128:129], v[128:129], s[72:73], v[136:137] op_sel_hi:[1,1,0] neg_lo:[0,0,1] neg_hi:[0,0,1]\n" \
    "v_pk_fma_f32 v[132:133], v[132:133], s[72:73], v[136:137] op_sel_hi:[1,1,0] neg_lo:[0,0,1] neg_hi:[0,0,1]\n" \
    "v_pk_fma_f32 v[130:131], v[130:131], s[72:73], v[136:137] op_sel_hi:[1,1,0] neg_lo:[0,0,1] neg_hi:[0,0,1]\n" \
    "v_exp_f32 v128, v128\n" \
    "v_exp_f32 v129, v129\n" \
    "v_exp_f32 v132, v132\n" \
    "v_exp_f32 v133, v133\n" \
    "v_exp_f32 v130, v130\n" \
    "v_exp_f32 v131, v131\n" \
    "v_cvt_pk_bf16_f32 v140, v128, v129\n" \
    "v_cvt_pk_bf16_f32 v141, v132, v133\n" \
    "v_cvt_pk_bf16_f32 v142, v130, v131\n" \
    "ds_write_b128 v232, v[140:143]\n" \
    "s_barrier\n" \
    "ds_read_b128 v[192:195], v227\n" \
    "s_waitcnt vmcnt(0)\n" \
    "v_smfmac_f32_16x16x64_bf16 v[176:179], v[172:175], v[32:39], v224\n" \
    "L%=_Tvc:\n" \
    "v_smfmac_f32_16x16x64_bf16 v[180:183], v[172:175], v[40:47], v224\n" \
    "v_smfmac_f32_16x16x64_bf16 v[184:187], v[168:171], v[16:23], v224\n" \
    "v_add_f32 v176, v176, v178\n" \
    "v_smfmac_f32_16x16x64_bf16 v[188:191], v[168:171], v[24:31], v224\n" \
    "v_add_f32 v177, v177, v179\n" \
    "v_smfmac_f32_16x16x64_bf16 v[184:187], v[172:175], v[48:55], v224\n" \
    "v_cmp_u_f32 vcc, v176, v177\n" \
    "v_smfmac_f32_16x16x64_bf16 v[188:191], v[172:175], v[56:63], v224\n" \
    "s_cbranch_vccnz L%=_Tvslow\n" \
    "L%=_Tvok:\n" \
    "v_cvt_pk_bf16_f32 v64, v176, v177\n" \
    "v_add_f32 v180, v180, v182\n" \
    "s_waitcnt lgkmcnt(0)\n" \
    "v_smfmac_f32_16x16x64_bf16 v[212:215], v[192:195], v[96:103], v224\n" \
    "v_add_f32 v181, v181, v183\n" \
    "v_smfmac_f32_16x16x64_bf16 v[196:199], v[192:195], v[64:71], v224\n" \
    "v_cvt_pk_bf16_f32 v72, v180, v181\n" \
    "v_add_f32 v184, v184, v186\n" \
    "v_add_f32 v185, v185, v187\n" \
    "v_smfmac_f32_16x16x64_bf16 v[200:203], v[192:195], v[72:79], v224\n" \
    "v_cvt_pk_bf16_f32 v80, v184, v185\n" \
    "v_add_f32 v188, v188, v190\n" \
    "v_add_f32 v189, v189, v191\n" \
    "v_smfmac_f32_16x16x64_bf16 v[204:207], v[192:195], v[80:87], v224\n" \
    "v_cvt_pk_bf16_f32 v88, v188, v189\n" \
    "v_add_f32 v212, v212, v214\n" \
    "v_add_f32 v213, v213, v215\n" \
    "v_smfmac_f32_16x16x64_bf16 v[208:211], v[192:195], v[88:95], v224\n" \
    "v_rcp_f32 v220, v212\n" \
    "v_rcp_f32 v221, v213\n" \
    "v_add_f32 v196, v196, v198\n" \
    "v_add_f32 v197, v197, v199\n" \
    "v_pk_add_f32 v[200:201], v[200:201], v[202:203]\n" \
    "v_pk_add_f32 v[204:205], v[204:205], v[206:207]\n" \
    "s_nop 1\n" \
    "v_pk_add_f32 v[208:209], v[208:209], v[210:211]\n" \
    "v_pk_mul_f32 v[196:197], v[196:197], v[220:221]\n" \
    "v_pk_mul_f32 v[200:201], v[200:201], v[220:221]\n" \
    "v_pk_mul_f32 v[204:205], v[204:205], v[220:221]\n" \
    "v_pk_mul_f32 v[208:209], v[208:209], v[220:221]\n" \
    "v_cvt_pk_bf16_f32 v216, v196, v200\n" \
    "v_cvt_pk_bf16_f32 v217, v204, v208\n" \
    "v_cvt_pk_bf16_f32 v218, v197, v201\n" \
    "v_cvt_pk_bf16_f32 v219, v205, v209\n" \
    "buffer_store_dwordx4 v[216:219], v226, s[40:43], s60 offen sc1\n" \
    "s_setprio 0\n" \
    "buffer_store_dword v143, v233, s[44:47], 0 offen sc1\n" \
    "ds_read_b64 v[220:221], v229 offset:64\n" \
    "s_mov_b32 s79, s60\n" \
    "s_mov_b32 s60, s61\n" \
    "s_mov_b32 s61, s62\n" \
    "s_mov_b32 s62, s63\n" \
    "s_mov_b32 s63, s79\n" \
    "s_add_u32 s76, s76, 1\n" \
    "s_add_u32 s77, s77, s65\n" \
    "s_add_u32 s84, s84, 0x20000\n" \
    "s_add_u32 s81, s81, s65\n" \
    "s_waitcnt lgkmcnt(0)\n" \
    "v_readfirstlane_b32 s65, v220\n" \
    "v_readfirstlane_b32 s71, v221\n" \
    "s_mul_i32 s96, s65, s71\n" \
    "s_lshr_b32 s96, s96, 4\n" \
    "s_branch L%=_Tloop\n" \
    "L%=_Tslow:\n" \
    "s_nop 7\n" \
    "s_nop 0\n" \
    "v_max_u32 v138, v104, v105\n" \
    "v_max3_u32 v138, v138, v106, v107\n" \
    "v_max3_u32 v138, v138, v108, v109\n" \
    "v_max3_u32 v138, v138, v110, v111\n" \
    "v_max3_u32 v138, v138, v112, v113\n" \
    "v_max3_u32 v138, v138, v114, v115\n" \
    "v_max3_u32 v138, v138, v116, v117\n" \
    "v_max3_u32 v138, v138, v118, v119\n" \
    "v_max3_u32 v138, v138, v120, v121\n" \
    "v_max3_u32 v138, v138, v122, v123\n" \
    "v_max3_u32 v138, v138, v124, v125\n" \
    "v_max3_u32 v138, v138, v126, v127\n" \
    "v_cmp_eq_u32 vcc, -1, v138\n" \
    "s_cbranch_vccz L%=_Tok\n" \
    "s_cmp_eq_u32 s99, 0\n" \
    "s_cbranch_scc1 L%=_Tok\n" \
    "s_sub_u32 s99, s99, 1\n" \
    "s_cmp_lg_u32 s99, 0\n" \
    "s_cbranch_scc1 L%=_Tu5\n" \
    "v_mov_b32 v138, 1\n" \
    "buffer_store_dword v138, off, s[44:47], 0 sc1\n" \
    "L%=_Tu5:\n" \
    "s_sleep 1\n" \
    "buffer_load_dwordx4 v[104:107], v230, s[40:43], s60 offen offset:0 sc1\n" \
    "buffer_load_dwordx2 v[120:121], v231, s[40:43], s60 offen offset:0 sc1\n" \
    "buffer_load_dwordx4 v[108:111], v230, s[40:43], s60 offen offset:2048 sc1\n" \
    "buffer_load_dwordx2 v[122:123], v231, s[40:43], s60 offen offset:2048 sc1\n" \
    "buffer_load_dwordx4 v[112:115], v230, s[40:43], s85 offen offset:0 sc1\n" \
    "buffer_load_dwordx2 v[124:125], v231, s[40:43], s85 offen offset:0 sc1\n" \
    "buffer_load_dwordx4 v[116:119], v230, s[40:43], s85 offen offset:2048 sc1\n" \
    "buffer_load_dwordx2 v[126:127], v231, s[40:43], s85 offen offset:2048 sc1\n" \
    "buffer_load_dwordx4 v[168:171], v225, s[40:43], s60 offen sc1\n" \
    "buffer_load_dwordx4 v[172:175], v225, s[40:43], s85 offen sc1\n" \
    "v_mov_b64 v[176:177], 0\n" \
    "v_mov_b64 v[178:179], 0\n" \
    "v_mov_b64 v[180:181], 0\n" \
    "v_mov_b64 v[182:183], 0\n" \
    "v_mov_b64 v[184:185], 0\n" \
    "v_mov_b64 v[186:187], 0\n" \
    "v_mov_b64 v[188:189], 0\n" \
    "v_mov_b64 v[190:191], 0\n" \
    "s_branch L%=_Tsc\n" \
    "L%=_Tvslow:\n" \
    "s_nop 7\n" \
    "s_nop 0\n" \
    "v_max_u32 v136, v168, v169\n" \
    "v_max3_u32 v136, v136, v170, v171\n" \
    "v_max3_u32 v136, v136, v172, v173\n" \
    "v_max3_u32 v136, v136, v174, v175\n" \
    "v_cmp_eq_u32 vcc, -1, v136\n" \
    "s_cbranch_vccz L%=_Tvok\n" \
    "s_cmp_eq_u32 s99, 0\n" \
    "s_cbranch_scc1 L%=_Tvok\n" \
    "s_sub_u32 s99, s99, 1\n" \
    "s_cmp_lg_u32 s99, 0\n" \
    "s_cbranch_scc1 L%=_Tu6\n" \
    "v_mov_b32 v136, 1\n" \
    "buffer_store_dword v136, off, s[44:47], 0 sc1\n" \
    "L%=_Tu6:\n" \
    "s_sleep 1\n" \
    "buffer_load_dwordx4 v[168:171], v225, s[40:43], s60 offen sc1\n" \
    "buffer_load_dwordx4 v[172:175], v225, s[40:43], s85 offen sc1\n" \
    "v_mov_b64 v[176:177], 0\n" \
    "v_mov_b64 v[178:179], 0\n" \
    "v_mov_b64 v[180:181], 0\n" \
    "v_mov_b64 v[182:183], 0\n" \
    "v_mov_b64 v[184:185], 0\n" \
    "v_mov_b64 v[186:187], 0\n" \
    "v_mov_b64 v[188:189], 0\n" \
    "v_mov_b64 v[190:191], 0\n" \
    "v_mov_b64 v[196:197], 0\n" \
    "v_mov_b64 v[198:199], 0\n" \
    "v_mov_b64 v[200:201], 0\n" \
    "v_mov_b64 v[202:203], 0\n" \
    "v_mov_b64 v[204:205], 0\n" \
    "v_mov_b64 v[206:207], 0\n" \
    "v_mov_b64 v[208:209], 0\n" \
    "v_mov_b64 v[210:211], 0\n" \
    "v_mov_b64 v[212:213], 0\n" \
    "v_mov_b64 v[214:215], 0\n" \
    "s_waitcnt vmcnt(0)\n" \
    "v_smfmac_f32_16x16x64_bf16 v[176:179], v[168:171], v[0:7], v224\n" \
    "v_smfmac_f32_16x16x64_bf16 v[180:183], v[168:171], v[8:15], v224\n" \
    "v_smfmac_f32_16x16x64_bf16 v[176:179], v[172:175], v[32:39], v224\n" \
    "s_nop 7\n" \
    "s_nop 0\n" \
    "s_branch L%=_Tvc\n" \
    "L%=_Tfin:\n" \
    "v_mov_b32 v143, 64\n" \
    "s_barrier\n" \
    "buffer_store_dword v143, v233, s[44:47], 0 offen sc1\n" \
    "s_branch L%=_end\n" \
    "L%=_Tu3s:\n" \
    "s_mov_b32 s77, s79\n" \
    "s_memtime s[38:39]\n" \
    "s_memrealtime vcc\n" \
    "s_nop 4\n" \
    "s_memrealtime s[32:33]\n" \
    "s_nop 4\n" \
    "s_memrealtime s[34:35]\n" \
    "s_nop 4\n" \
    "s_memrealtime s[36:37]\n" \
    "s_add_u32 s80, s77, s64\n" \
    "s_lshl_b32 s80, s80, 2\n" \
    "s_waitcnt lgkmcnt(0)\n" \
    "s_sub_u32 s80, s80, vcc_lo\n" \
    "s_sub_u32 s80, s80, s32\n" \
    "s_sub_u32 s80, s80, s34\n" \
    "s_sub_u32 s80, s80, s36\n" \
    "s_mul_i32 s80, s80, s71\n" \
    "s_ashr_i32 s80, s80, 6\n" \
    "s_add_u32 s78, s80, s38\n" \
    "s_branch L%=_Tu3k\n" \
    "L%=_Tu4s:\n" \
    "s_cmp_gt_u32 s79, 0x8000\n" \
    "s_cbranch_scc1 L%=_Tu4e\n" \
    "s_sleep 2\n" \
    "s_branch L%=_Tu4a\n" \
    "L%=_S:\n" \
    "s_mov_b32 s76, 0\n" \
    "s_mov_b32 s99, 0x20000\n" \
    "s_mov_b32 s84, 0\n" \
    "s_mov_b32 s81, 0\n" \
    "s_mov_b32 s86, 0\n" \
    "s_mov_b32 s87, 0\n" \
    "s_mov_b32 s88, 0\n" \
    "s_mov_b32 s89, 0\n" \
    "s_mov_b32 s90, 0\n" \
    "s_mov_b32 s91, 0\n" \
    "s_mov_b32 s92, 0\n" \
    "s_mov_b32 s93, 0\n" \
    "s_mov_b32 s94, 0\n" \
    "s_mov_b32 s95, 0\n" \
    "s_mov_b32 s96, 0\n" \
    "s_mov_b32 s97, 0\n" \
    "s_mov_b32 s98, 0\n" \
    "ds_read_b32 v216, v229\n" \
    "s_waitcnt lgkmcnt(0)\n" \
    "v_readfirstlane_b32 s94, v216\n" \
    "s_mul_i32 s79, s94, s70\n" \
    "s_add_u32 s79, s79, s81\n" \
    "s_mov_b32 s77, s79\n" \
    "s_mul_i32 s96, s65, s71\n" \
    "s_lshr_b32 s96, s96, 4\n" \
    "v_mov_b32 v96, 0x3f803f80\n" \
    "v_mov_b32 v97, 0x3f803f80\n" \
    "v_mov_b32 v98, 0x3f803f80\n" \
    "v_mov_b32 v99, 0x3f803f80\n" \
    "v_mov_b32 v100, 0x3f803f80\n" \
    "v_mov_b32 v101, 0x3f803f80\n" \
    "v_mov_b64 v[102:103], 0\n" \
    "v_mov_b64 v[64:65], 0\n" \
    "v_mov_b64 v[66:67], 0\n" \
    "v_mov_b64 v[68:69], 0\n" \
    "v_mov_b64 v[70:71], 0\n" \
    "v_mov_b64 v[72:73], 0\n" \
    "v_mov_b64 v[74:75], 0\n" \
    "v_mov_b64 v[76:77], 0\n" \
    "v_mov_b64 v[78:79], 0\n" \
    "v_mov_b64 v[80:81], 0\n" \
    "v_mov_b64 v[82:83], 0\n" \
    "v_mov_b64 v[84:85], 0\n" \
    "v_mov_b64 v[86:87], 0\n" \
    "v_mov_b64 v[88:89], 0\n" \
    "v_mov_b64 v[90:91], 0\n" \
    "v_mov_b64 v[92:93], 0\n" \
    "v_mov_b64 v[94:95], 0\n" \
    "v_mov_b64 v[192:193], 0\n" \
    "v_mov_b64 v[194:195], 0\n" \
    "L%=_Sloop:\n" \
    "ds_read_b64_tr_b16 v[104:105], v228 offset:16384\n" \
    "ds_read_b64_tr_b16 v[106:107], v228 offset:16416\n" \
    "ds_read_b64_tr_b16 v[108:109], v228 offset:18688\n" \
    "ds_read_b64_tr_b16 v[110:111], v228 offset:18720\n" \
    "ds_read_b64_tr_b16 v[112:113], v228 offset:20992\n" \
    "ds_read_b64_tr_b16 v[114:115], v228 offset:21024\n" \
    "ds_read_b64_tr_b16 v[116:117], v228 offset:23296\n" \
    "ds_read_b64_tr_b16 v[118:119], v228 offset:23328\n" \
    "ds_read_b64_tr_b16 v[120:121], v228 offset:16656\n" \
    "ds_read_b64_tr_b16 v[122:123], v228 offset:16688\n" \
    "ds_read_b64_tr_b16 v[124:125], v228 offset:18960\n" \
    "ds_read_b64_tr_b16 v[126:127], v228 offset:18992\n" \
    "ds_read_b64_tr_b16 v[128:129], v228 offset:21264\n" \
    "ds_read_b64_tr_b16 v[130:131], v228 offset:21296\n" \
    "ds_read_b64_tr_b16 v[132:133], v228 offset:23568\n" \
    "ds_read_b64_tr_b16 v[134:135], v228 offset:23600\n" \
    "s_waitcnt lgkmcnt(14)\n" \
    "v_mfma_f32_16x16x32_bf16 v[136:139], v[104:107], v[0:3], 0\n" \
    "v_mfma_f32_16x16x32_bf16 v[144:147], v[104:107], v[8:11], 0\n" \
    "v_mfma_f32_16x16x32_bf16 v[152:155], v[104:107], v[16:19], 0\n" \
    "v_mfma_f32_16x16x32_bf16 v[160:163], v[104:107], v[24:27], 0\n" \
    "s_waitcnt lgkmcnt(12)\n" \
    "v_mfma_f32_16x16x32_bf16 v[136:139], v[108:111], v[4:7], v[136:139]\n" \
    "v_mfma_f32_16x16x32_bf16 v[144:147], v[108:111], v[12:15], v[144:147]\n" \
    "v_mfma_f32_16x16x32_bf16 v[152:155], v[108:111], v[20:23], v[152:155]\n" \
    "v_mfma_f32_16x16x32_bf16 v[160:163], v[108:111], v[28:31], v[160:163]\n" \
    "s_waitcnt lgkmcnt(10)\n" \
    "v_mfma_f32_16x16x32_bf16 v[136:139], v[112:115], v[32:35], v[136:139]\n" \
    "v_mfma_f32_16x16x32_bf16 v[144:147], v[112:115], v[40:43], v[144:147]\n" \
    "v_mfma_f32_16x16x32_bf16 v[152:155], v[112:115], v[48:51], v[152:155]\n" \
    "v_mfma_f32_16x16x32_bf16 v[160:163], v[112:115], v[56:59], v[160:163]\n" \
    "s_waitcnt lgkmcnt(8)\n" \
    "v_mfma_f32_16x16x32_bf16 v[136:139], v[116:119], v[36:39], v[136:139]\n" \
    "v_mfma_f32_16x16x32_bf16 v[144:147], v[116:119], v[44:47], v[144:147]\n" \
    "v_mfma_f32_16x16x32_bf16 v[152:155], v[116:119], v[52:55], v[152:155]\n" \
    "v_mfma_f32_16x16x32_bf16 v[160:163], v[116:119], v[60:63], v[160:163]\n" \
    "s_waitcnt lgkmcnt(6)\n" \
    "v_mfma_f32_16x16x32_bf16 v[140:143], v[120:123], v[0:3], 0\n" \
    "v_mfma_f32_16x16x32_bf16 v[148:151], v[120:123], v[8:11], 0\n" \
    "v_mfma_f32_16x16x32_bf16 v[156:159], v[120:123], v[16:19], 0\n" \
    "v_mfma_f32_16x16x32_bf16 v[164:167], v[120:123], v[24:27], 0\n" \
    "s_waitcnt lgkmcnt(4)\n" \
    "v_mfma_f32_16x16x32_bf16 v[140:143], v[124:127], v[4:7], v[140:143]\n" \
    "v_mfma_f32_16x16x32_bf16 v[148:151], v[124:127], v[12:15], v[148:151]\n" \
    "v_mfma_f32_16x16x32_bf16 v[156:159], v[124:127], v[20:23], v[156:159]\n" \
    "v_mfma_f32_16x16x32_bf16 v[164:167], v[124:127], v[28:31], v[164:167]\n" \
    "s_waitcnt lgkmcnt(2)\n" \
    "v_mfma_f32_16x16x32_bf16 v[140:143], v[128:131], v[32:35], v[140:143]\n" \
    "v_mfma_f32_16x16x32_bf16 v[148:151], v[128:131], v[40:43], v[148:151]\n" \
    "v_mfma_f32_16x16x32_bf16 v[156:159], v[128:131], v[48:51], v[156:159]\n" \
    "v_mfma_f32_16x16x32_bf16 v[164:167], v[128:131], v[56:59], v[164:167]\n" \
    "s_waitcnt lgkmcnt(0)\n" \
    "v_mfma_f32_16x16x32_bf16 v[140:143], v[132:135], v[36:39], v[140:143]\n" \
    "v_mfma_f32_16x16x32_bf16 v[148:151], v[132:135], v[44:47], v[148:151]\n" \
    "v_mfma_f32_16x16x32_bf16 v[156:159], v[132:135], v[52:55], v[156:159]\n" \
    "v_mfma_f32_16x16x32_bf16 v[164:167], v[132:135], v[60:63], v[164:167]\n" \
    "v_cvt_pk_bf16_f32 v65, v136, v137\n" \
    "v_cvt_pk_bf16_f32 v68, v138, v139\n" \
    "v_cvt_pk_bf16_f32 v73, v144, v145\n" \
    "v_cvt_pk_bf16_f32 v76, v146, v147\n" \
    "v_cvt_pk_bf16_f32 v81, v152, v153\n" \
    "v_cvt_pk_bf16_f32 v84, v154, v155\n" \
    "v_cvt_pk_bf16_f32 v89, v160, v161\n" \
    "v_cvt_pk_bf16_f32 v92, v162, v163\n" \
    "v_cvt_pk_bf16_f32 v66, v142, v143\n" \
    "v_cvt_pk_bf16_f32 v67, v142, v143\n" \
    "v_cvt_pk_bf16_f32 v69, v140, v141\n" \
    "v_cvt_pk_bf16_f32 v74, v150, v151\n" \
    "v_cvt_pk_bf16_f32 v75, v150, v151\n" \
    "v_cvt_pk_bf16_f32 v77, v148, v149\n" \
    "v_cvt_pk_bf16_f32 v82, v158, v159\n" \
    "v_cvt_pk_bf16_f32 v83, v158, v159\n" \
    "v_cvt_pk_bf16_f32 v85, v156, v157\n" \
    "v_cvt_pk_bf16_f32 v90, v166, v167\n" \
    "v_cvt_pk_bf16_f32 v91, v166, v167\n" \
    "v_cvt_pk_bf16_f32 v93, v164, v165\n" \
    "v_permlane16_swap_b32 v66, v67\n" \
    "v_permlane16_swap_b32 v74, v75\n" \
    "v_permlane16_swap_b32 v82, v83\n" \
    "v_permlane16_swap_b32 v90, v91\n" \
    "ds_read_b128 v[240:243], v234 offset:25600\n" \
    "ds_read_b128 v[244:247], v234 offset:26624\n" \
    "v_mov_b64 v[176:177], 0\n" \
    "v_mov_b64 v[178:179], 0\n" \
    "v_mov_b64 v[180:181], 0\n" \
    "v_mov_b64 v[182:183], 0\n" \
    "v_mov_b64 v[184:185], 0\n" \
    "v_mov_b64 v[186:187], 0\n" \
    "v_mov_b64 v[188:189], 0\n" \
    "v_mov_b64 v[190:191], 0\n" \
    "v_mov_b64 v[196:197], 0\n" \
    "v_mov_b64 v[198:199], 0\n" \
    "v_mov_b64 v[200:201], 0\n" \
    "v_mov_b64 v[202:203], 0\n" \
    "v_mov_b64 v[204:205], 0\n" \
    "v_mov_b64 v[206:207], 0\n" \
    "v_mov_b64 v[208:209], 0\n" \
    "v_mov_b64 v[210:211], 0\n" \
    "v_mov_b64 v[212:213], 0\n" \
    "v_mov_b64 v[214:215], 0\n" \
    "s_add_u32 s85, s60, 0x1000\n" \
    "s_branch L%=_Su2b\n" \
    "L%=_Su2a:\n" \
    "s_sleep 4\n" \
    "L%=_Su2b:\n" \
    "s_memrealtime vcc\n" \
    "s_add_u32 s80, s77, s64\n" \
    "s_add_u32 s80, s80, s69\n" \
    "s_waitcnt lgkmcnt(0)\n" \
    "s_sub_i32 s79, s80, vcc_lo\n" \
    "s_sub_u32 s79, s79, 40\n" \
    "s_cmp_lt_u32 s79, 0x400\n" \
    "s_cbranch_scc1 L%=_Su2a\n" \
    "s_memtime s[38:39]\n" \
    "s_memrealtime vcc\n" \
    "s_nop 4\n" \
    "s_memrealtime s[32:33]\n" \
    "s_nop 4\n" \
    "s_memrealtime s[34:35]\n" \
    "s_nop 4\n" \
    "s_memrealtime s[36:37]\n" \
    "s_add_u32 s80, s77, s64\n" \
    "s_lshl_b32 s80, s80, 2\n" \
    "s_waitcnt lgkmcnt(0)\n" \
    "s_sub_u32 s80, s80, vcc_lo\n" \
    "s_sub_u32 s80, s80, s32\n" \
    "s_sub_u32 s80, s80, s34\n" \
    "s_sub_u32 s80, s80, s36\n" \
    "s_mul_i32 s80, s80, s71\n" \
    "s_ashr_i32 s80, s80, 6\n" \
    "s_add_u32 s78, s80, s38\n" \
    "L%=_Su3a:\n" \
    "s_memtime s[38:39]\n" \
    "s_mul_i32 s80, s69, 23\n" \
    "s_add_u32 s80, s80, s78\n" \
    "s_waitcnt lgkmcnt(0)\n" \
    "s_sub_i32 s79, s80, s38\n" \
    "s_max_i32 s79, s79, 0\n" \
    "s_cmp_gt_u32 s79, 320\n" \
    "s_cbranch_scc1 L%=_Su3s\n" \
    ".p2align 4\n" \
    "L%=_Su3d:\n" \
    "s_sub_u32 s79, s79, 28\n" \
    "s_cbranch_scc0 L%=_Su3d\n" \
    "L%=_Su3e:\n" \
    "s_sub_u32 s79, s84, 0x20000\n" \
    "s_cselect_b32 s79, 0x40000000, s79\n" \
    "buffer_store_dwordx4 v[236:239], v233, s[48:51], s79 offen sc1\n" \
    "s_setprio 3\n" \
    "s_barrier\n" \
    "L%=_Srd:\n" \
    "ds_read_b128 v[136:139], v229 offset:32\n" \
    "ds_read_b128 v[104:107], v230 offset:0\n" \
    "ds_read_b128 v[108:111], v230 offset:2176\n" \
    "ds_read_b128 v[112:115], v230 offset:4352\n" \
    "ds_read_b128 v[116:119], v230 offset:6528\n" \
    "ds_read_b128 v[120:123], v230 offset:1088\n" \
    "ds_read_b128 v[124:127], v230 offset:3264\n" \
    "ds_read_b128 v[128:131], v230 offset:5440\n" \
    "ds_read_b128 v[132:135], v230 offset:7616\n" \
    "s_waitcnt lgkmcnt(8)\n" \
    "v_min3_u32 v136, v136, v137, v138\n" \
    "v_min_u32 v136, v136, v139\n" \
    "v_cmp_ge_u32 vcc, s76, v136\n" \
    "s_cbranch_vccnz L%=_Srd\n" \
    "s_waitcnt lgkmcnt(6)\n" \
    "v_pk_add_f32 v[104:105], v[104:105], v[108:109]\n" \
    "v_pk_add_f32 v[106:107], v[106:107], v[110:111]\n" \
    "s_waitcnt lgkmcnt(4)\n" \
    "v_pk_add_f32 v[112:113], v[112:113], v[116:117]\n" \
    "v_pk_add_f32 v[114:115], v[114:115], v[118:119]\n" \
    "v_pk_add_f32 v[104:105], v[104:105], v[112:113]\n" \
    "v_pk_add_f32 v[106:107], v[106:107], v[114:115]\n" \
    "v_cvt_pk_bf16_f32 v236, v104, v105\n" \
    "v_cvt_pk_bf16_f32 v237, v106, v107\n" \
    "s_waitcnt lgkmcnt(2)\n" \
    "v_pk_add_f32 v[120:121], v[120:121], v[124:125]\n" \
    "v_pk_add_f32 v[122:123], v[122:123], v[126:127]\n" \
    "s_waitcnt lgkmcnt(0)\n" \
    "v_pk_add_f32 v[128:129], v[128:129], v[132:133]\n" \
    "v_pk_add_f32 v[130:131], v[130:131], v[134:135]\n" \
    "v_pk_add_f32 v[120:121], v[120:121], v[128:129]\n" \
    "v_pk_add_f32 v[122:123], v[122:123], v[130:131]\n" \
    "v_cvt_pk_bf16_f32 v238, v120, v121\n" \
    "v_cvt_pk_bf16_f32 v239, v122, v123\n" \
    "s_nop 1\n" \
    "v_mfma_f32_16x16x32_bf16 v[140:143], v[240:243], v[236:239], 0\n" \
    "v_mfma_f32_16x16x32_bf16 v[144:147], v[244:247], v[236:239], 0\n" \
    "v_mfma_f32_16x16x32_bf16 v[148:151], v[236:239], v[236:239], 0\n" \
    "buffer_store_dwordx4 v[140:143], v231, s[40:43], s60 offen sc1\n" \
    "s_nop 2\n" \
    "buffer_store_dwordx4 v[144:147], v231, s[40:43], s60 offen offset:512 sc1\n" \
    "s_nop 2\n" \
    "buffer_store_dwordx4 v[148:151], v232, s[40:43], s60 offen sc1\n" \
    "s_cmp_eq_u32 s76, 63\n" \
    "s_cbranch_scc1 L%=_Sfin\n" \
    "ds_read_b32 v216, v229\n" \
    "s_waitcnt lgkmcnt(0)\n" \
    "v_readfirstlane_b32 s94, v216\n" \
    "s_mul_i32 s79, s94, s70\n" \
    "s_add_u32 s79, s79, s81\n" \
    "s_cmp_lg_u32 s79, s77\n" \
    "s_cbranch_scc1 L%=_Su4s\n" \
    "L%=_Su4k:\n" \
    "L%=_Su5a:\n" \
    "s_memtime s[38:39]\n" \
    "s_mul_hi_u32 s80, s67, s96\n" \
    "s_add_u32 s80, s80, s78\n" \
    "s_waitcnt lgkmcnt(0)\n" \
    "s_sub_i32 s79, s80, s38\n" \
    "s_max_i32 s79, s79, 0\n" \
    "s_cmp_gt_u32 s79, 320\n" \
    "s_cbranch_scc1 L%=_Su5s\n" \
    ".p2align 4\n" \
    "L%=_Su5d:\n" \
    "s_sub_u32 s79, s79, 28\n" \
    "s_cbranch_scc0 L%=_Su5d\n" \
    "L%=_Su5e:\n" \
    "buffer_load_dwordx4 v[168:171], v225, s[40:43], s60 offen sc1\n" \
    "buffer_load_dwordx4 v[172:175], v225, s[40:43], s85 offen sc1\n" \
    "L%=_Svc:\n" \
    "s_waitcnt vmcnt(1)\n" \
    "v_smfmac_f32_16x16x64_bf16 v[176:179], v[168:171], v[0:7], v224\n" \
    "v_smfmac_f32_16x16x64_bf16 v[180:183], v[168:171], v[8:15], v224\n" \
    "s_waitcnt vmcnt(0)\n" \
    "v_smfmac_f32_16x16x64_bf16 v[176:179], v[172:175], v[32:39], v224\n" \
    "v_smfmac_f32_16x16x64_bf16 v[180:183], v[172:175], v[40:47], v224\n" \
    "v_smfmac_f32_16x16x64_bf16 v[184:187], v[168:171], v[16:23], v224\n" \
    "v_smfmac_f32_16x16x64_bf16 v[188:191], v[168:171], v[24:31], v224\n" \
    "v_add_f32 v176, v176, v178\n" \
    "v_smfmac_f32_16x16x64_bf16 v[184:187], v[172:175], v[48:55], v224\n" \
    "v_add_f32 v177, v177, v179\n" \
    "v_smfmac_f32_16x16x64_bf16 v[188:191], v[172:175], v[56:63], v224\n" \
    "v_cmp_u_f32 vcc, v176, v177\n" \
    "v_cvt_pk_bf16_f32 v64, v176, v177\n" \
    "v_add_f32 v180, v180, v182\n" \
    "v_add_f32 v181, v181, v183\n" \
    "v_cvt_pk_bf16_f32 v72, v180, v181\n" \
    "s_cbranch_vccnz L%=_Svslow\n" \
    "L%=_Svok:\n" \
    "v_add_f32 v184, v184, v186\n" \
    "v_add_f32 v185, v185, v187\n" \
    "v_cvt_pk_bf16_f32 v80, v184, v185\n" \
    "v_add_f32 v188, v188, v190\n" \
    "v_add_f32 v189, v189, v191\n" \
    "v_cvt_pk_bf16_f32 v88, v188, v189\n" \
    "s_barrier\n" \
    "L%=_Sfr:\n" \
    "ds_read_b128 v[192:195], v227\n" \
    "s_waitcnt lgkmcnt(0)\n" \
    "v_smfmac_f32_16x16x64_bf16 v[212:215], v[192:195], v[96:103], v224\n" \
    "v_cmp_ne_u32 vcc, s76, v195\n" \
    "v_smfmac_f32_16x16x64_bf16 v[196:199], v[192:195], v[64:71], v224\n" \
    "s_cbranch_vccnz L%=_Sfs\n" \
    "v_smfmac_f32_16x16x64_bf16 v[200:203], v[192:195], v[72:79], v224\n" \
    "v_smfmac_f32_16x16x64_bf16 v[204:207], v[192:195], v[80:87], v224\n" \
    "v_add_f32 v212, v212, v214\n" \
    "v_smfmac_f32_16x16x64_bf16 v[208:211], v[192:195], v[88:95], v224\n" \
    "v_add_f32 v213, v213, v215\n" \
    "v_rcp_f32 v220, v212\n" \
    "v_rcp_f32 v221, v213\n" \
    "v_add_f32 v196, v196, v198\n" \
    "v_add_f32 v197, v197, v199\n" \
    "v_pk_add_f32 v[200:201], v[200:201], v[202:203]\n" \
    "v_pk_add_f32 v[204:205], v[204:205], v[206:207]\n" \
    "s_nop 0\n" \
    "v_pk_add_f32 v[208:209], v[208:209], v[210:211]\n" \
    "v_pk_mul_f32 v[196:197], v[196:197], v[220:221]\n" \
    "v_pk_mul_f32 v[200:201], v[200:201], v[220:221]\n" \
    "v_pk_mul_f32 v[204:205], v[204:205], v[220:221]\n" \
    "v_pk_mul_f32 v[208:209], v[208:209], v[220:221]\n" \
    "v_cvt_pk_bf16_f32 v216, v196, v200\n" \
    "v_cvt_pk_bf16_f32 v217, v204, v208\n" \
    "v_cvt_pk_bf16_f32 v218, v197, v201\n" \
    "v_cvt_pk_bf16_f32 v219, v205, v209\n" \
    "buffer_store_dwordx4 v[216:219], v226, s[40:43], s60 offen sc1\n" \
    "s_setprio 0\n" \
    "ds_read_b64 v[220:221], v229 offset:64\n" \
    "s_mov_b32 s79, s60\n" \
    "s_mov_b32 s60, s61\n" \
    "s_mov_b32 s61, s62\n" \
    "s_mov_b32 s62, s63\n" \
    "s_mov_b32 s63, s79\n" \
    "s_add_u32 s76, s76, 1\n" \
    "s_add_u32 s77, s77, s65\n" \
    "s_add_u32 s84, s84, 0x20000\n" \
    "s_add_u32 s81, s81, s65\n" \
    "s_waitcnt lgkmcnt(0)\n" \
    "v_readfirstlane_b32 s65, v220\n" \
    "v_readfirstlane_b32 s71, v221\n" \
    "s_mul_i32 s96, s65, s71\n" \
    "s_lshr_b32 s96, s96, 4\n" \
    "s_branch L%=_Sloop\n" \
    "L%=_Sfin:\n" \
    "buffer_store_dwordx4 v[236:239], v233, s[48:51], s84 offen sc1\n" \
    "s_waitcnt vmcnt(0)\n" \
    "s_barrier\n" \
    "s_branch L%=_end\n" \
    "L%=_Su3s:\n" \
    "s_cmp_gt_u32 s79, 0x8000\n" \
    "s_cbranch_scc1 L%=_Su3e\n" \
    "s_sleep 2\n" \
    "s_branch L%=_Su3a\n" \
    "L%=_Su4s:\n" \
    "s_mov_b32 s77, s79\n" \
    "s_memtime s[38:39]\n" \
    "s_memrealtime vcc\n" \
    "s_nop 4\n" \
    "s_memrealtime s[32:33]\n" \
    "s_nop 4\n" \
    "s_memrealtime s[34:35]\n" \
    "s_nop 4\n" \
    "s_memrealtime s[36:37]\n" \
    "s_add_u32 s80, s77, s64\n" \
    "s_lshl_b32 s80, s80, 2\n" \
    "s_waitcnt lgkmcnt(0)\n" \
    "s_sub_u32 s80, s80, vcc_lo\n" \
    "s_sub_u32 s80, s80, s32\n" \
    "s_sub_u32 s80, s80, s34\n" \
    "s_sub_u32 s80, s80, s36\n" \
    "s_mul_i32 s80, s80, s71\n" \
    "s_ashr_i32 s80, s80, 6\n" \
    "s_add_u32 s78, s80, s38\n" \
    "s_branch L%=_Su4k\n" \
    "L%=_Su5s:\n" \
    "s_cmp_gt_u32 s79, 0x8000\n" \
    "s_cbranch_scc1 L%=_Su5e\n" \
    "s_sleep 2\n" \
    "s_branch L%=_Su5a\n" \
    "L%=_Svslow:\n" \
    "s_nop 7\n" \
    "s_nop 0\n" \
    "v_max_u32 v216, v168, v169\n" \
    "v_max3_u32 v216, v216, v170, v171\n" \
    "v_max3_u32 v216, v216, v172, v173\n" \
    "v_max3_u32 v216, v216, v174, v175\n" \
    "v_cmp_eq_u32 vcc, -1, v216\n" \
    "s_cbranch_vccz L%=_Svok\n" \
    "s_cmp_eq_u32 s99, 0\n" \
    "s_cbranch_scc1 L%=_Svok\n" \
    "s_sub_u32 s99, s99, 1\n" \
    "s_cmp_lg_u32 s99, 0\n" \
    "s_cbranch_scc1 L%=_Su6\n" \
    "v_mov_b32 v216, 1\n" \
    "buffer_store_dword v216, off, s[44:47], 0 sc1\n" \
    "L%=_Su6:\n" \
    "s_sleep 1\n" \
    "buffer_load_dwordx4 v[168:171], v225, s[40:43], s60 offen sc1\n" \
    "buffer_load_dwordx4 v[172:175], v225, s[40:43], s85 offen sc1\n" \
    "v_mov_b64 v[176:177], 0\n" \
    "v_mov_b64 v[178:179], 0\n" \
    "v_mov_b64 v[180:181], 0\n" \
    "v_mov_b64 v[182:183], 0\n" \
    "v_mov_b64 v[184:185], 0\n" \
    "v_mov_b64 v[186:187], 0\n" \
    "v_mov_b64 v[188:189], 0\n" \
    "v_mov_b64 v[190:191], 0\n" \
    "s_waitcnt vmcnt(0)\n" \
    "s_branch L%=_Svc\n" \
    "L%=_Sfs:\n" \
    "s_nop 7\n" \
    "s_nop 0\n" \
    "v_mov_b64 v[196:197], 0\n" \
    "v_mov_b64 v[198:199], 0\n" \
    "v_mov_b64 v[200:201], 0\n" \
    "v_mov_b64 v[202:203], 0\n" \
    "v_mov_b64 v[204:205], 0\n" \
    "v_mov_b64 v[206:207], 0\n" \
    "v_mov_b64 v[208:209], 0\n" \
    "v_mov_b64 v[210:211], 0\n" \
    "v_mov_b64 v[212:213], 0\n" \
    "v_mov_b64 v[214:215], 0\n" \
    "s_branch L%=_Sfr\n" \
    "L%=_R:\n" \
    "s_mov_b32 s76, 0\n" \
    "s_mov_b32 s99, 0x20000\n" \
    "s_mov_b32 s84, 0\n" \
    "s_mov_b32 s81, 0\n" \
    "s_mov_b32 s86, 0\n" \
    "s_mov_b32 s87, 0\n" \
    "s_mov_b32 s88, 0\n" \
    "s_mov_b32 s89, 0\n" \
    "s_mov_b32 s90, 0\n" \
    "s_mov_b32 s91, 0\n" \
    "s_mov_b32 s92, 0\n" \
    "s_mov_b32 s93, 0\n" \
    "s_mov_b32 s94, 0\n" \
    "s_mov_b32 s95, 0\n" \
    "s_mov_b32 s96, 0\n" \
    "s_mov_b32 s97, 0\n" \
    "s_mov_b32 s98, 0\n" \
    "ds_read_b32 v216, v229\n" \
    "s_waitcnt lgkmcnt(0)\n" \
    "v_readfirstlane_b32 s94, v216\n" \
    "s_mul_i32 s79, s94, s70\n" \
    "s_add_u32 s79, s79, s81\n" \
    "s_mov_b32 s77, s79\n" \
    "s_mul_i32 s96, s65, s71\n" \
    "s_lshr_b32 s96, s96, 4\n" \
    "v_mov_b32 v96, 0x3f803f80\n" \
    "v_mov_b32 v97, 0x3f803f80\n" \
    "v_mov_b32 v98, 0x3f803f80\n" \
    "v_mov_b32 v99, 0x3f803f80\n" \
    "v_mov_b32 v100, 0x3f803f80\n" \
    "v_mov_b32 v101, 0x3f803f80\n" \
    "v_mov_b64 v[102:103], 0\n" \
    "v_mov_b64 v[64:65], 0\n" \
    "v_mov_b64 v[66:67], 0\n" \
    "v_mov_b64 v[68:69], 0\n" \
    "v_mov_b64 v[70:71], 0\n" \
    "v_mov_b64 v[72:73], 0\n" \
    "v_mov_b64 v[74:75], 0\n" \
    "v_mov_b64 v[76:77], 0\n" \
    "v_mov_b64 v[78:79], 0\n" \
    "v_mov_b64 v[80:81], 0\n" \
    "v_mov_b64 v[82:83], 0\n" \
    "v_mov_b64 v[84:85], 0\n" \
    "v_mov_b64 v[86:87], 0\n" \
    "v_mov_b64 v[88:89], 0\n" \
    "v_mov_b64 v[90:91], 0\n" \
    "v_mov_b64 v[92:93], 0\n" \
    "v_mov_b64 v[94:95], 0\n" \
    "v_mov_b64 v[192:193], 0\n" \
    "v_mov_b64 v[194:195], 0\n" \
    "v_mov_b32 v222, v236\n" \
    "v_mov_b32 v223, 0\n" \
    "ds_read_b64_tr_b16 v[104:105], v228 offset:16384\n" \
    "ds_read_b64_tr_b16 v[106:107], v228 offset:16416\n" \
    "ds_read_b64_tr_b16 v[108:109], v228 offset:18688\n" \
    "ds_read_b64_tr_b16 v[110:111], v228 offset:18720\n" \
    "ds_read_b64_tr_b16 v[112:113], v228 offset:20992\n" \
    "ds_read_b64_tr_b16 v[114:115], v228 offset:21024\n" \
    "ds_read_b64_tr_b16 v[116:117], v228 offset:23296\n" \
    "ds_read_b64_tr_b16 v[118:119], v228 offset:23328\n" \
    "ds_read_b64_tr_b16 v[120:121], v228 offset:16656\n" \
    "ds_read_b64_tr_b16 v[122:123], v228 offset:16688\n" \
    "ds_read_b64_tr_b16 v[124:125], v228 offset:18960\n" \
    "ds_read_b64_tr_b16 v[126:127], v228 offset:18992\n" \
    "ds_read_b64_tr_b16 v[128:129], v228 offset:21264\n" \
    "ds_read_b64_tr_b16 v[130:131], v228 offset:21296\n" \
    "ds_read_b64_tr_b16 v[132:133], v228 offset:23568\n" \
    "ds_read_b64_tr_b16 v[134:135], v228 offset:23600\n" \
    "s_waitcnt lgkmcnt(14)\n" \
    "v_mfma_f32_16x16x32_bf16 v[136:139], v[104:107], v[0:3], 0\n" \
    "v_mfma_f32_16x16x32_bf16 v[144:147], v[104:107], v[8:11], 0\n" \
    "v_mfma_f32_16x16x32_bf16 v[152:155], v[104:107], v[16:19], 0\n" \
    "v_mfma_f32_16x16x32_bf16 v[160:163], v[104:107], v[24:27], 0\n" \
    "s_waitcnt lgkmcnt(12)\n" \
    "v_mfma_f32_16x16x32_bf16 v[136:139], v[108:111], v[4:7], v[136:139]\n" \
    "v_mfma_f32_16x16x32_bf16 v[144:147], v[108:111], v[12:15], v[144:147]\n" \
    "v_mfma_f32_16x16x32_bf16 v[152:155], v[108:111], v[20:23], v[152:155]\n" \
    "v_mfma_f32_16x16x32_bf16 v[160:163], v[108:111], v[28:31], v[160:163]\n" \
    "s_waitcnt lgkmcnt(10)\n" \
    "v_mfma_f32_16x16x32_bf16 v[136:139], v[112:115], v[32:35], v[136:139]\n" \
    "v_mfma_f32_16x16x32_bf16 v[144:147], v[112:115], v[40:43], v[144:147]\n" \
    "v_mfma_f32_16x16x32_bf16 v[152:155], v[112:115], v[48:51], v[152:155]\n" \
    "v_mfma_f32_16x16x32_bf16 v[160:163], v[112:115], v[56:59], v[160:163]\n" \
    "s_waitcnt lgkmcnt(8)\n" \
    "v_mfma_f32_16x16x32_bf16 v[136:139], v[116:119], v[36:39], v[136:139]\n" \
    "v_mfma_f32_16x16x32_bf16 v[144:147], v[116:119], v[44:47], v[144:147]\n" \
    "v_mfma_f32_16x16x32_bf16 v[152:155], v[116:119], v[52:55], v[152:155]\n" \
    "v_mfma_f32_16x16x32_bf16 v[160:163], v[116:119], v[60:63], v[160:163]\n" \
    "s_waitcnt lgkmcnt(6)\n" \
    "v_mfma_f32_16x16x32_bf16 v[140:143], v[120:123], v[0:3], 0\n" \
    "v_mfma_f32_16x16x32_bf16 v[148:151], v[120:123], v[8:11], 0\n" \
    "v_mfma_f32_16x16x32_bf16 v[156:159], v[120:123], v[16:19], 0\n" \
    "v_mfma_f32_16x16x32_bf16 v[164:167], v[120:123], v[24:27], 0\n" \
    "s_waitcnt lgkmcnt(4)\n" \
    "v_mfma_f32_16x16x32_bf16 v[140:143], v[124:127], v[4:7], v[140:143]\n" \
    "v_mfma_f32_16x16x32_bf16 v[148:151], v[124:127], v[12:15], v[148:151]\n" \
    "v_mfma_f32_16x16x32_bf16 v[156:159], v[124:127], v[20:23], v[156:159]\n" \
    "v_mfma_f32_16x16x32_bf16 v[164:167], v[124:127], v[28:31], v[164:167]\n" \
    "s_waitcnt lgkmcnt(2)\n" \
    "v_mfma_f32_16x16x32_bf16 v[140:143], v[128:131], v[32:35], v[140:143]\n" \
    "v_mfma_f32_16x16x32_bf16 v[148:151], v[128:131], v[40:43], v[148:151]\n" \
    "v_mfma_f32_16x16x32_bf16 v[156:159], v[128:131], v[48:51], v[156:159]\n" \
    "v_mfma_f32_16x16x32_bf16 v[164:167], v[128:131], v[56:59], v[164:167]\n" \
    "s_waitcnt lgkmcnt(0)\n" \
    "v_mfma_f32_16x16x32_bf16 v[140:143], v[132:135], v[36:39], v[140:143]\n" \
    "v_mfma_f32_16x16x32_bf16 v[148:151], v[132:135], v[44:47], v[148:151]\n" \
    "v_mfma_f32_16x16x32_bf16 v[156:159], v[132:135], v[52:55], v[156:159]\n" \
    "v_mfma_f32_16x16x32_bf16 v[164:167], v[132:135], v[60:63], v[164:167]\n" \
    "v_cvt_pk_bf16_f32 v65, v136, v137\n" \
    "v_cvt_pk_bf16_f32 v68, v138, v139\n" \
    "v_cvt_pk_bf16_f32 v73, v144, v145\n" \
    "v_cvt_pk_bf16_f32 v76, v146, v147\n" \
    "v_cvt_pk_bf16_f32 v81, v152, v153\n" \
    "v_cvt_pk_bf16_f32 v84, v154, v155\n" \
    "v_cvt_pk_bf16_f32 v89, v160, v161\n" \
    "v_cvt_pk_bf16_f32 v92, v162, v163\n" \
    "v_cvt_pk_bf16_f32 v66, v142, v143\n" \
    "v_cvt_pk_bf16_f32 v67, v142, v143\n" \
    "v_cvt_pk_bf16_f32 v69, v140, v141\n" \
    "v_cvt_pk_bf16_f32 v74, v150, v151\n" \
    "v_cvt_pk_bf16_f32 v75, v150, v151\n" \
    "v_cvt_pk_bf16_f32 v77, v148, v149\n" \
    "v_cvt_pk_bf16_f32 v82, v158, v159\n" \
    "v_cvt_pk_bf16_f32 v83, v158, v159\n" \
    "v_cvt_pk_bf16_f32 v85, v156, v157\n" \
    "v_cvt_pk_bf16_f32 v90, v166, v167\n" \
    "v_cvt_pk_bf16_f32 v91, v166, v167\n" \
    "v_cvt_pk_bf16_f32 v93, v164, v165\n" \
    "v_permlane16_swap_b32 v66, v67\n" \
    "v_permlane16_swap_b32 v74, v75\n" \
    "v_permlane16_swap_b32 v82, v83\n" \
    "v_permlane16_swap_b32 v90, v91\n" \
    "L%=_Rloop:\n" \
    "v_mov_b64 v[176:177], 0\n" \
    "v_mov_b64 v[178:179], 0\n" \
    "v_mov_b64 v[180:181], 0\n" \
    "v_mov_b64 v[182:183], 0\n" \
    "v_mov_b64 v[184:185], 0\n" \
    "v_mov_b64 v[186:187], 0\n" \
    "v_mov_b64 v[188:189], 0\n" \
    "v_mov_b64 v[190:191], 0\n" \
    "v_mov_b64 v[196:197], 0\n" \
    "v_mov_b64 v[198:199], 0\n" \
    "v_mov_b64 v[200:201], 0\n" \
    "v_mov_b64 v[202:203], 0\n" \
    "v_mov_b64 v[204:205], 0\n" \
    "v_mov_b64 v[206:207], 0\n" \
    "v_mov_b64 v[208:209], 0\n" \
    "v_mov_b64 v[210:211], 0\n" \
    "v_mov_b64 v[212:213], 0\n" \
    "v_mov_b64 v[214:215], 0\n" \
    "s_add_u32 s85, s60, 0x1000\n" \
    "s_branch L%=_Ru2b\n" \
    "L%=_Ru2a:\n" \
    "s_sleep 4\n" \
    "L%=_Ru2b:\n" \
    "s_memrealtime vcc\n" \
    "s_add_u32 s80, s77, s64\n" \
    "s_add_u32 s80, s80, s69\n" \
    "s_waitcnt lgkmcnt(0)\n" \
    "s_sub_i32 s79, s80, vcc_lo\n" \
    "s_sub_u32 s79, s79, 40\n" \
    "s_cmp_lt_u32 s79, 0x400\n" \
    "s_cbranch_scc1 L%=_Ru2a\n" \
    "s_memtime s[38:39]\n" \
    "s_memrealtime vcc\n" \
    "s_nop 4\n" \
    "s_memrealtime s[32:33]\n" \
    "s_nop 4\n" \
    "s_memrealtime s[34:35]\n" \
    "s_nop 4\n" \
    "s_memrealtime s[36:37]\n" \
    "s_add_u32 s80, s77, s64\n" \
    "s_lshl_b32 s80, s80, 2\n" \
    "s_waitcnt lgkmcnt(0)\n" \
    "s_sub_u32 s80, s80, vcc_lo\n" \
    "s_sub_u32 s80, s80, s32\n" \
    "s_sub_u32 s80, s80, s34\n" \
    "s_sub_u32 s80, s80, s36\n" \
    "s_mul_i32 s80, s80, s71\n" \
    "s_ashr_i32 s80, s80, 6\n" \
    "s_add_u32 s78, s80, s38\n" \
    "L%=_Ru3a:\n" \
    "s_memtime s[38:39]\n" \
    "s_mul_i32 s80, s69, 23\n" \
    "s_add_u32 s80, s80, s78\n" \
    "s_waitcnt lgkmcnt(0)\n" \
    "s_sub_i32 s79, s80, s38\n" \
    "s_max_i32 s79, s79, 0\n" \
    "s_cmp_gt_u32 s79, 320\n" \
    "s_cbranch_scc1 L%=_Ru3s\n" \
    ".p2align 4\n" \
    "L%=_Ru3d:\n" \
    "s_sub_u32 s79, s79, 28\n" \
    "s_cbranch_scc0 L%=_Ru3d\n" \
    "L%=_Ru3e:\n" \
    "s_sub_u32 s79, s84, 0x20000\n" \
    "s_cselect_b32 s79, 0x40000000, s79\n" \
    "buffer_store_dwordx4 v[236:239], v234, s[48:51], s79 offen sc1\n" \
    "s_setprio 3\n" \
    "s_barrier\n" \
    "s_nop 14\n" \
    "L%=_Rvs:\n" \
    "ds_read_b128 v[104:107], v229 offset:16\n" \
    "ds_read_b128 v[108:111], v230 offset:8704\n" \
    "ds_read_b128 v[112:115], v230 offset:9984\n" \
    "ds_read_b128 v[116:119], v230 offset:11264\n" \
    "ds_read_b128 v[120:123], v230 offset:12544\n" \
    "s_waitcnt lgkmcnt(4)\n" \
    "v_min3_u32 v104, v104, v105, v106\n" \
    "v_min_u32 v104, v104, v107\n" \
    "s_nop 0\n" \
    "v_readfirstlane_b32 s79, v104\n" \
    "s_cmp_le_u32 s79, s76\n" \
    "s_cbranch_scc1 L%=_Rvs\n" \
    "s_waitcnt lgkmcnt(2)\n" \
    "v_pk_add_f32 v[108:109], v[108:109], v[112:113]\n" \
    "v_pk_add_f32 v[110:111], v[110:111], v[114:115]\n" \
    "s_waitcnt lgkmcnt(0)\n" \
    "v_pk_add_f32 v[116:117], v[116:117], v[120:121]\n" \
    "v_pk_add_f32 v[118:119], v[118:119], v[122:123]\n" \
    "v_pk_add_f32 v[108:109], v[108:109], v[116:117]\n" \
    "v_pk_add_f32 v[110:111], v[110:111], v[118:119]\n" \
    "v_cvt_pk_bf16_f32 v124, v108, v109\n" \
    "v_cvt_pk_bf16_f32 v125, v110, v111\n" \
    "s_nop 0\n" \
    "v_mov_b32_dpp v126, v124 quad_perm:[1,0,3,2] row_mask:0xf bank_mask:0xf\n" \
    "v_mov_b32_dpp v127, v125 quad_perm:[1,0,3,2] row_mask:0xf bank_mask:0xf\n" \
    "buffer_store_dwordx4 v[124:127], v231, s[40:43], s60 offen sc1\n" \
    "ds_write_b64 v232, v[124:125]\n" \
    "ds_read_b64_tr_b16 v[236:237], v233\n" \
    "ds_read_b64_tr_b16 v[238:239], v233 offset:256\n" \
    "s_cmp_eq_u32 s76, 63\n" \
    "s_cbranch_scc1 L%=_Rfin\n" \
    "ds_read_b32 v216, v229\n" \
    "s_waitcnt lgkmcnt(0)\n" \
    "v_readfirstlane_b32 s94, v216\n" \
    "s_mul_i32 s79, s94, s70\n" \
    "s_add_u32 s79, s79, s81\n" \
    "s_cmp_lg_u32 s79, s77\n" \
    "s_cbranch_scc1 L%=_Ru4s\n" \
    "L%=_Ru4k:\n" \
    "L%=_Ru5a:\n" \
    "s_memtime s[38:39]\n" \
    "s_mul_hi_u32 s80, s67, s96\n" \
    "s_add_u32 s80, s80, s78\n" \
    "s_waitcnt lgkmcnt(0)\n" \
    "s_sub_i32 s79, s80, s38\n" \
    "s_max_i32 s79, s79, 0\n" \
    "s_cmp_gt_u32 s79, 320\n" \
    "s_cbranch_scc1 L%=_Ru5s\n" \
    ".p2align 4\n" \
    "L%=_Ru5d:\n" \
    "s_sub_u32 s79, s79, 28\n" \
    "s_cbranch_scc0 L%=_Ru5d\n" \
    "L%=_Ru5e:\n" \
    "buffer_load_dwordx4 v[168:171], v225, s[40:43], s60 offen sc1\n" \
    "buffer_load_dwordx4 v[172:175], v225, s[40:43], s85 offen sc1\n" \
    "s_memtime s[38:39]\n" \
    "s_memrealtime vcc\n" \
    "s_nop 4\n" \
    "s_memrealtime s[32:33]\n" \
    "s_nop 4\n" \
    "s_memrealtime s[34:35]\n" \
    "s_nop 4\n" \
    "s_memrealtime s[36:37]\n" \
    "s_add_u32 s80, s77, s64\n" \
    "s_lshl_b32 s80, s80, 2\n" \
    "s_waitcnt lgkmcnt(0)\n" \
    "s_sub_u32 s80, s80, vcc_lo\n" \
    "s_sub_u32 s80, s80, s32\n" \
    "s_sub_u32 s80, s80, s34\n" \
    "s_sub_u32 s80, s80, s36\n" \
    "s_mul_i32 s80, s80, s71\n" \
    "s_ashr_i32 s80, s80, 6\n" \
    "s_add_u32 s78, s80, s38\n" \
    "L%=_Rvc:\n" \
    "s_waitcnt vmcnt(1)\n" \
    "v_smfmac_f32_16x16x64_bf16 v[176:179], v[168:171], v[0:7], v224\n" \
    "v_smfmac_f32_16x16x64_bf16 v[180:183], v[168:171], v[8:15], v224\n" \
    "s_waitcnt vmcnt(0)\n" \
    "v_smfmac_f32_16x16x64_bf16 v[176:179], v[172:175], v[32:39], v224\n" \
    "v_smfmac_f32_16x16x64_bf16 v[180:183], v[172:175], v[40:47], v224\n" \
    "v_smfmac_f32_16x16x64_bf16 v[184:187], v[168:171], v[16:23], v224\n" \
    "v_smfmac_f32_16x16x64_bf16 v[188:191], v[168:171], v[24:31], v224\n" \
    "v_add_f32 v176, v176, v178\n" \
    "v_smfmac_f32_16x16x64_bf16 v[184:187], v[172:175], v[48:55], v224\n" \
    "v_add_f32 v177, v177, v179\n" \
    "v_smfmac_f32_16x16x64_bf16 v[188:191], v[172:175], v[56:63], v224\n" \
    "v_cmp_u_f32 vcc, v176, v177\n" \
    "v_cvt_pk_bf16_f32 v64, v176, v177\n" \
    "v_add_f32 v180, v180, v182\n" \
    "v_add_f32 v181, v181, v183\n" \
    "v_cvt_pk_bf16_f32 v72, v180, v181\n" \
    "s_cbranch_vccnz L%=_Rvslow\n" \
    "L%=_Rvok:\n" \
    "v_add_f32 v184, v184, v186\n" \
    "v_add_f32 v185, v185, v187\n" \
    "v_cvt_pk_bf16_f32 v80, v184, v185\n" \
    "v_add_f32 v188, v188, v190\n" \
    "v_add_f32 v189, v189, v191\n" \
    "v_cvt_pk_bf16_f32 v88, v188, v189\n" \
    "s_barrier\n" \
    "L%=_Rfr:\n" \
    "ds_read_b128 v[192:195], v227\n" \
    "s_waitcnt lgkmcnt(0)\n" \
    "v_smfmac_f32_16x16x64_bf16 v[212:215], v[192:195], v[96:103], v224\n" \
    "v_cmp_ne_u32 vcc, s76, v195\n" \
    "v_smfmac_f32_16x16x64_bf16 v[196:199], v[192:195], v[64:71], v224\n" \
    "s_cbranch_vccnz L%=_Rfs\n" \
    "v_smfmac_f32_16x16x64_bf16 v[200:203], v[192:195], v[72:79], v224\n" \
    "v_smfmac_f32_16x16x64_bf16 v[204:207], v[192:195], v[80:87], v224\n" \
    "v_add_f32 v212, v212, v214\n" \
    "v_smfmac_f32_16x16x64_bf16 v[208:211], v[192:195], v[88:95], v224\n" \
    "v_add_f32 v213, v213, v215\n" \
    "v_rcp_f32 v220, v212\n" \
    "v_rcp_f32 v221, v213\n" \
    "v_add_f32 v196, v196, v198\n" \
    "v_add_f32 v197, v197, v199\n" \
    "v_pk_add_f32 v[200:201], v[200:201], v[202:203]\n" \
    "v_pk_add_f32 v[204:205], v[204:205], v[206:207]\n" \
    "s_nop 0\n" \
    "v_pk_add_f32 v[208:209], v[208:209], v[210:211]\n" \
    "v_pk_mul_f32 v[196:197], v[196:197], v[220:221]\n" \
    "v_pk_mul_f32 v[200:201], v[200:201], v[220:221]\n" \
    "v_pk_mul_f32 v[204:205], v[204:205], v[220:221]\n" \
    "v_pk_mul_f32 v[208:209], v[208:209], v[220:221]\n" \
    "v_cvt_pk_bf16_f32 v216, v196, v200\n" \
    "v_cvt_pk_bf16_f32 v217, v204, v208\n" \
    "v_cvt_pk_bf16_f32 v218, v197, v201\n" \
    "v_cvt_pk_bf16_f32 v219, v205, v209\n" \
    "buffer_store_dwordx4 v[216:219], v226, s[40:43], s60 offen sc1\n" \
    "ds_read_b64_tr_b16 v[104:105], v228 offset:16384\n" \
    "ds_read_b64_tr_b16 v[106:107], v228 offset:16416\n" \
    "ds_read_b64_tr_b16 v[108:109], v228 offset:18688\n" \
    "ds_read_b64_tr_b16 v[110:111], v228 offset:18720\n" \
    "ds_read_b64_tr_b16 v[112:113], v228 offset:20992\n" \
    "ds_read_b64_tr_b16 v[114:115], v228 offset:21024\n" \
    "ds_read_b64_tr_b16 v[116:117], v228 offset:23296\n" \
    "ds_read_b64_tr_b16 v[118:119], v228 offset:23328\n" \
    "ds_read_b64_tr_b16 v[120:121], v228 offset:16656\n" \
    "ds_read_b64_tr_b16 v[122:123], v228 offset:16688\n" \
    "ds_read_b64_tr_b16 v[124:125], v228 offset:18960\n" \
    "ds_read_b64_tr_b16 v[126:127], v228 offset:18992\n" \
    "ds_read_b64_tr_b16 v[128:129], v228 offset:21264\n" \
    "ds_read_b64_tr_b16 v[130:131], v228 offset:21296\n" \
    "ds_read_b64_tr_b16 v[132:133], v228 offset:23568\n" \
    "ds_read_b64_tr_b16 v[134:135], v228 offset:23600\n" \
    "s_waitcnt lgkmcnt(14)\n" \
    "v_mfma_f32_16x16x32_bf16 v[136:139], v[104:107], v[0:3], 0\n" \
    "v_mfma_f32_16x16x32_bf16 v[144:147], v[104:107], v[8:11], 0\n" \
    "v_mfma_f32_16x16x32_bf16 v[152:155], v[104:107], v[16:19], 0\n" \
    "v_mfma_f32_16x16x32_bf16 v[160:163], v[104:107], v[24:27], 0\n" \
    "s_waitcnt lgkmcnt(12)\n" \
    "v_mfma_f32_16x16x32_bf16 v[136:139], v[108:111], v[4:7], v[136:139]\n" \
    "v_mfma_f32_16x16x32_bf16 v[144:147], v[108:111], v[12:15], v[144:147]\n" \
    "v_mfma_f32_16x16x32_bf16 v[152:155], v[108:111], v[20:23], v[152:155]\n" \
    "v_mfma_f32_16x16x32_bf16 v[160:163], v[108:111], v[28:31], v[160:163]\n" \
    "s_waitcnt lgkmcnt(10)\n" \
    "v_mfma_f32_16x16x32_bf16 v[136:139], v[112:115], v[32:35], v[136:139]\n" \
    "v_mfma_f32_16x16x32_bf16 v[144:147], v[112:115], v[40:43], v[144:147]\n" \
    "v_mfma_f32_16x16x32_bf16 v[152:155], v[112:115], v[48:51], v[152:155]\n" \
    "v_mfma_f32_16x16x32_bf16 v[160:163], v[112:115], v[56:59], v[160:163]\n" \
    "s_waitcnt lgkmcnt(8)\n" \
    "v_mfma_f32_16x16x32_bf16 v[136:139], v[116:119], v[36:39], v[136:139]\n" \
    "v_mfma_f32_16x16x32_bf16 v[144:147], v[116:119], v[44:47], v[144:147]\n" \
    "v_mfma_f32_16x16x32_bf16 v[152:155], v[116:119], v[52:55], v[152:155]\n" \
    "v_mfma_f32_16x16x32_bf16 v[160:163], v[116:119], v[60:63], v[160:163]\n" \
    "v_add_u32 v223, 1, v223\n" \
    "s_and_b32 s95, s94, 1\n" \
    "s_lshl_b32 s95, s95, 17\n" \
    "s_add_u32 s95, s95, s61\n" \
    "s_add_u32 s85, s95, 0x1000\n" \
    "s_xor_b32 s85, s95, 0x20000\n" \
    "L%=_Ru6a:\n" \
    "s_memtime s[38:39]\n" \
    "s_mul_hi_u32 s80, s68, s96\n" \
    "s_add_u32 s80, s80, s78\n" \
    "s_waitcnt lgkmcnt(0)\n" \
    "s_sub_i32 s79, s80, s38\n" \
    "s_max_i32 s79, s79, 0\n" \
    "s_cmp_gt_u32 s79, 320\n" \
    "s_cbranch_scc1 L%=_Ru6s\n" \
    ".p2align 4\n" \
    "L%=_Ru6d:\n" \
    "s_sub_u32 s79, s79, 28\n" \
    "s_cbranch_scc0 L%=_Ru6d\n" \
    "L%=_Ru6e:\n" \
    "buffer_load_dwordx4 v[176:179], v235, s[40:43], s60 offen offset:0 sc1\n" \
    "buffer_load_dwordx4 v[180:183], v235, s[40:43], s60 offen offset:1024 sc1\n" \
    "buffer_load_dwordx4 v[184:187], v235, s[40:43], s60 offen offset:2048 sc1\n" \
    "buffer_load_dwordx4 v[188:191], v235, s[40:43], s60 offen offset:3072 sc1\n" \
    "v_mfma_f32_16x16x32_bf16 v[140:143], v[120:123], v[0:3], 0\n" \
    "v_mfma_f32_16x16x32_bf16 v[148:151], v[120:123], v[8:11], 0\n" \
    "v_mfma_f32_16x16x32_bf16 v[156:159], v[120:123], v[16:19], 0\n" \
    "v_mfma_f32_16x16x32_bf16 v[164:167], v[120:123], v[24:27], 0\n" \
    "v_mfma_f32_16x16x32_bf16 v[140:143], v[124:127], v[4:7], v[140:143]\n" \
    "v_mfma_f32_16x16x32_bf16 v[148:151], v[124:127], v[12:15], v[148:151]\n" \
    "v_mfma_f32_16x16x32_bf16 v[156:159], v[124:127], v[20:23], v[156:159]\n" \
    "v_mfma_f32_16x16x32_bf16 v[164:167], v[124:127], v[28:31], v[164:167]\n" \
    "v_mfma_f32_16x16x32_bf16 v[140:143], v[128:131], v[32:35], v[140:143]\n" \
    "v_mfma_f32_16x16x32_bf16 v[148:151], v[128:131], v[40:43], v[148:151]\n" \
    "v_mfma_f32_16x16x32_bf16 v[156:159], v[128:131], v[48:51], v[156:159]\n" \
    "v_mfma_f32_16x16x32_bf16 v[164:167], v[128:131], v[56:59], v[164:167]\n" \
    "v_mfma_f32_16x16x32_bf16 v[140:143], v[132:135], v[36:39], v[140:143]\n" \
    "v_mfma_f32_16x16x32_bf16 v[148:151], v[132:135], v[44:47], v[148:151]\n" \
    "v_mfma_f32_16x16x32_bf16 v[156:159], v[132:135], v[52:55], v[156:159]\n" \
    "v_mfma_f32_16x16x32_bf16 v[164:167], v[132:135], v[60:63], v[164:167]\n" \
    "v_cvt_pk_bf16_f32 v65, v136, v137\n" \
    "v_cvt_pk_bf16_f32 v68, v138, v139\n" \
    "v_cvt_pk_bf16_f32 v73, v144, v145\n" \
    "v_cvt_pk_bf16_f32 v76, v146, v147\n" \
    "v_cvt_pk_bf16_f32 v81, v152, v153\n" \
    "v_cvt_pk_bf16_f32 v84, v154, v155\n" \
    "v_cvt_pk_bf16_f32 v89, v160, v161\n" \
    "v_cvt_pk_bf16_f32 v92, v162, v163\n" \
    "L%=_Rred:\n" \
    "s_waitcnt vmcnt(3)\n" \
    "v_mfma_f32_16x16x32_bf16 v[240:243], v[244:247], v[176:179], 0\n" \
    "s_waitcnt vmcnt(2)\n" \
    "v_mfma_f32_16x16x32_bf16 v[240:243], v[244:247], v[180:183], v[240:243]\n" \
    "s_waitcnt vmcnt(1)\n" \
    "v_mfma_f32_16x16x32_bf16 v[240:243], v[244:247], v[184:187], v[240:243]\n" \
    "s_waitcnt vmcnt(0)\n" \
    "v_mfma_f32_16x16x32_bf16 v[240:243], v[244:247], v[188:191], v[240:243]\n" \
    "s_nop 7\n" \
    "v_cmp_u_f32 vcc, v240, v240\n" \
    "v_add_f32_dpp v240, v240, v240 row_ror:8 row_mask:0xf bank_mask:0xf\n" \
    "v_add_f32_dpp v241, v241, v241 row_ror:8 row_mask:0xf bank_mask:0xf\n" \
    "v_add_f32_dpp v242, v242, v242 row_ror:8 row_mask:0xf bank_mask:0xf\n" \
    "v_add_f32_dpp v243, v243, v243 row_ror:8 row_mask:0xf bank_mask:0xf\n" \
    "v_cvt_pk_bf16_f32 v216, v240, v241\n" \
    "v_cvt_pk_bf16_f32 v217, v242, v243\n" \
    "s_cbranch_vccnz L%=_Rslow\n" \
    "L%=_Rok:\n" \
    "v_mov_b32_dpp v218, v216 quad_perm:[1,0,3,2] row_mask:0xf bank_mask:0xf\n" \
    "v_mov_b32_dpp v219, v217 quad_perm:[1,0,3,2] row_mask:0xf bank_mask:0xf\n" \
    "buffer_store_dwordx4 v[216:219], v222, s[40:43], s95 offen sc1\n" \
    "ds_write_b32 v229, v223 offset:4\n" \
    "buffer_store_dwordx4 v[216:219], v222, s[40:43], s85 offen sc1\n" \
    "s_setprio 0\n" \
    "v_cvt_pk_bf16_f32 v66, v142, v143\n" \
    "v_cvt_pk_bf16_f32 v67, v142, v143\n" \
    "v_cvt_pk_bf16_f32 v69, v140, v141\n" \
    "v_cvt_pk_bf16_f32 v74, v150, v151\n" \
    "v_cvt_pk_bf16_f32 v75, v150, v151\n" \
    "v_cvt_pk_bf16_f32 v77, v148, v149\n" \
    "v_cvt_pk_bf16_f32 v82, v158, v159\n" \
    "v_cvt_pk_bf16_f32 v83, v158, v159\n" \
    "v_cvt_pk_bf16_f32 v85, v156, v157\n" \
    "v_cvt_pk_bf16_f32 v90, v166, v167\n" \
    "v_cvt_pk_bf16_f32 v91, v166, v167\n" \
    "v_cvt_pk_bf16_f32 v93, v164, v165\n" \
    "v_permlane16_swap_b32 v66, v67\n" \
    "v_permlane16_swap_b32 v74, v75\n" \
    "v_permlane16_swap_b32 v82, v83\n" \
    "v_permlane16_swap_b32 v90, v91\n" \
    "ds_read_b64 v[220:221], v229 offset:64\n" \
    "s_mov_b32 s79, s60\n" \
    "s_mov_b32 s60, s61\n" \
    "s_mov_b32 s61, s62\n" \
    "s_mov_b32 s62, s63\n" \
    "s_mov_b32 s63, s79\n" \
    "s_add_u32 s76, s76, 1\n" \
    "s_add_u32 s77, s77, s65\n" \
    "s_add_u32 s84, s84, 0x20000\n" \
    "s_add_u32 s81, s81, s65\n" \
    "s_waitcnt lgkmcnt(0)\n" \
    "v_readfirstlane_b32 s65, v220\n" \
    "v_readfirstlane_b32 s71, v221\n" \
    "s_mul_i32 s96, s65, s71\n" \
    "s_lshr_b32 s96, s96, 4\n" \
    "s_branch L%=_Rloop\n" \
    "L%=_Rslow:\n" \
    "s_nop 7\n" \
    "s_nop 0\n" \
    "v_max_u32 v220, v176, v177\n" \
    "v_max3_u32 v220, v220, v178, v179\n" \
    "v_max3_u32 v220, v220, v180, v181\n" \
    "v_max3_u32 v220, v220, v182, v183\n" \
    "v_max3_u32 v220, v220, v184, v185\n" \
    "v_max3_u32 v220, v220, v186, v187\n" \
    "v_max3_u32 v220, v220, v188, v189\n" \
    "v_max3_u32 v220, v220, v190, v191\n" \
    "v_cmp_eq_u32 vcc, -1, v220\n" \
    "s_cbranch_vccz L%=_Rok\n" \
    "s_cmp_eq_u32 s99, 0\n" \
    "s_cbranch_scc1 L%=_Rok\n" \
    "s_sub_u32 s99, s99, 1\n" \
    "s_cmp_lg_u32 s99, 0\n" \
    "s_cbranch_scc1 L%=_Ru7\n" \
    "v_mov_b32 v220, 1\n" \
    "buffer_store_dword v220, off, s[44:47], 0 sc1\n" \
    "L%=_Ru7:\n" \
    "s_sleep 1\n" \
    "buffer_load_dwordx4 v[176:179], v235, s[40:43], s60 offen offset:0 sc1\n" \
    "buffer_load_dwordx4 v[180:183], v235, s[40:43], s60 offen offset:1024 sc1\n" \
    "buffer_load_dwordx4 v[184:187], v235, s[40:43], s60 offen offset:2048 sc1\n" \
    "buffer_load_dwordx4 v[188:191], v235, s[40:43], s60 offen offset:3072 sc1\n" \
    "s_branch L%=_Rred\n" \
    "L%=_Rfin:\n" \
    "s_waitcnt lgkmcnt(0)\n" \
    "buffer_store_dwordx4 v[236:239], v234, s[48:51], s84 offen sc1\n" \
    "s_waitcnt vmcnt(0)\n" \
    "s_barrier\n" \
    "s_branch L%=_end\n" \
    "L%=_Ru3s:\n" \
    "s_cmp_gt_u32 s79, 0x8000\n" \
    "s_cbranch_scc1 L%=_Ru3e\n" \
    "s_sleep 2\n" \
    "s_branch L%=_Ru3a\n" \
    "L%=_Ru4s:\n" \
    "s_mov_b32 s77, s79\n" \
    "s_memtime s[38:39]\n" \
    "s_memrealtime vcc\n" \
    "s_nop 4\n" \
    "s_memrealtime s[32:33]\n" \
    "s_nop 4\n" \
    "s_memrealtime s[34:35]\n" \
    "s_nop 4\n" \
    "s_memrealtime s[36:37]\n" \
    "s_add_u32 s80, s77, s64\n" \
    "s_lshl_b32 s80, s80, 2\n" \
    "s_waitcnt lgkmcnt(0)\n" \
    "s_sub_u32 s80, s80, vcc_lo\n" \
    "s_sub_u32 s80, s80, s32\n" \
    "s_sub_u32 s80, s80, s34\n" \
    "s_sub_u32 s80, s80, s36\n" \
    "s_mul_i32 s80, s80, s71\n" \
    "s_ashr_i32 s80, s80, 6\n" \
    "s_add_u32 s78, s80, s38\n" \
    "s_branch L%=_Ru4k\n" \
    "L%=_Ru5s:\n" \
    "s_cmp_gt_u32 s79, 0x8000\n" \
    "s_cbranch_scc1 L%=_Ru5e\n" \
    "s_sleep 2\n" \
    "s_branch L%=_Ru5a\n" \
    "L%=_Rvslow:\n" \
    "s_nop 7\n" \
    "s_nop 0\n" \
    "v_max_u32 v216, v168, v169\n" \
    "v_max3_u32 v216, v216, v170, v171\n" \
    "v_max3_u32 v216, v216, v172, v173\n" \
    "v_max3_u32 v216, v216, v174, v175\n" \
    "v_cmp_eq_u32 vcc, -1, v216\n" \
    "s_cbranch_vccz L%=_Rvok\n" \
    "s_cmp_eq_u32 s99, 0\n" \
    "s_cbranch_scc1 L%=_Rvok\n" \
    "s_sub_u32 s99, s99, 1\n" \
    "s_cmp_lg_u32 s99, 0\n" \
    "s_cbranch_scc1 L%=_Ru8\n" \
    "v_mov_b32 v216, 1\n" \
    "buffer_store_dword v216, off, s[44:47], 0 sc1\n" \
    "L%=_Ru8:\n" \
    "s_sleep 1\n" \
    "buffer_load_dwordx4 v[168:171], v225, s[40:43], s60 offen sc1\n" \
    "buffer_load_dwordx4 v[172:175], v225, s[40:43], s85 offen sc1\n" \
    "v_mov_b64 v[176:177], 0\n" \
    "v_mov_b64 v[178:179], 0\n" \
    "v_mov_b64 v[180:181], 0\n" \
    "v_mov_b64 v[182:183], 0\n" \
    "v_mov_b64 v[184:185], 0\n" \
    "v_mov_b64 v[186:187], 0\n" \
    "v_mov_b64 v[188:189], 0\n" \
    "v_mov_b64 v[190:191], 0\n" \
    "s_waitcnt vmcnt(0)\n" \
    "s_branch L%=_Rvc\n" \
    "L%=_Rfs:\n" \
    "s_nop 7\n" \
    "s_nop 0\n" \
    "v_mov_b64 v[196:197], 0\n" \
    "v_mov_b64 v[198:199], 0\n" \
    "v_mov_b64 v[200:201], 0\n" \
    "v_mov_b64 v[202:203], 0\n" \
    "v_mov_b64 v[204:205], 0\n" \
    "v_mov_b64 v[206:207], 0\n" \
    "v_mov_b64 v[208:209], 0\n" \
    "v_mov_b64 v[210:211], 0\n" \
    "v_mov_b64 v[212:213], 0\n" \
    "v_mov_b64 v[214:215], 0\n" \
    "s_branch L%=_Rfr\n" \
    "L%=_Ru6s:\n" \
    "s_cmp_gt_u32 s79, 0x8000\n" \
    "s_cbranch_scc1 L%=_Ru6e\n" \
    "s_sleep 2\n" \
    "s_branch L%=_Ru6a\n" \
    "L%=_P:\n" \
    "s_mov_b32 s76, 0\n" \
    "s_mov_b32 s99, 0x20000\n" \
    "s_mov_b32 s84, 0\n" \
    "s_mov_b32 s81, 0\n" \
    "s_mov_b32 s86, 0\n" \
    "s_mov_b32 s87, 0\n" \
    "s_mov_b32 s88, 0\n" \
    "s_mov_b32 s89, 0\n" \
    "s_mov_b32 s90, 0\n" \
    "s_mov_b32 s91, 0\n" \
    "s_mov_b32 s92, 0\n" \
    "s_mov_b32 s93, 0\n" \
    "s_mov_b32 s94, 0\n" \
    "s_mov_b32 s95, 0\n" \
    "s_mov_b32 s96, 0\n" \
    "s_mov_b32 s97, 0\n" \
    "s_mov_b32 s98, 0\n" \
    "ds_read_b32 v216, v229\n" \
    "s_waitcnt lgkmcnt(0)\n" \
    "v_readfirstlane_b32 s94, v216\n" \
    "s_mul_i32 s79, s94, s70\n" \
    "s_add_u32 s79, s79, s81\n" \
    "s_mov_b32 s77, s79\n" \
    "s_mul_i32 s96, s65, s71\n" \
    "s_lshr_b32 s96, s96, 4\n" \
    "v_mov_b32 v96, 0x3f803f80\n" \
    "v_mov_b32 v97, 0x3f803f80\n" \
    "v_mov_b32 v98, 0x3f803f80\n" \
    "v_mov_b32 v99, 0x3f803f80\n" \
    "v_mov_b32 v100, 0x3f803f80\n" \
    "v_mov_b32 v101, 0x3f803f80\n" \
    "v_mov_b64 v[102:103], 0\n" \
    "v_mov_b64 v[64:65], 0\n" \
    "v_mov_b64 v[66:67], 0\n" \
    "v_mov_b64 v[68:69], 0\n" \
    "v_mov_b64 v[70:71], 0\n" \
    "v_mov_b64 v[72:73], 0\n" \
    "v_mov_b64 v[74:75], 0\n" \
    "v_mov_b64 v[76:77], 0\n" \
    "v_mov_b64 v[78:79], 0\n" \
    "v_mov_b64 v[80:81], 0\n" \
    "v_mov_b64 v[82:83], 0\n" \
    "v_mov_b64 v[84:85], 0\n" \
    "v_mov_b64 v[86:87], 0\n" \
    "v_mov_b64 v[88:89], 0\n" \
    "v_mov_b64 v[90:91], 0\n" \
    "v_mov_b64 v[92:93], 0\n" \
    "v_mov_b64 v[94:95], 0\n" \
    "v_mov_b64 v[192:193], 0\n" \
    "v_mov_b64 v[194:195], 0\n" \
    "v_cvt_f32_u32 v240, s71\n" \
    "v_mov_b32 v241, 1.0\n" \
    "v_mov_b32 v242, 0\n" \
    "v_mov_b32 v243, s65\n" \
    "s_mov_b32 s74, s94\n" \
    "v_mov_b32 v244, -1\n" \
    "v_mov_b32 v245, -1\n" \
    "v_mov_b32 v246, -1\n" \
    "v_mov_b32 v247, -1\n" \
    "L%=_Ploop:\n" \
    "ds_read_b64_tr_b16 v[104:105], v228 offset:16384\n" \
    "ds_read_b64_tr_b16 v[106:107], v228 offset:16416\n" \
    "ds_read_b64_tr_b16 v[108:109], v228 offset:18688\n" \
    "ds_read_b64_tr_b16 v[110:111], v228 offset:18720\n" \
    "ds_read_b64_tr_b16 v[112:113], v228 offset:20992\n" \
    "ds_read_b64_tr_b16 v[114:115], v228 offset:21024\n" \
    "ds_read_b64_tr_b16 v[116:117], v228 offset:23296\n" \
    "ds_read_b64_tr_b16 v[118:119], v228 offset:23328\n" \
    "ds_read_b64_tr_b16 v[120:121], v228 offset:16656\n" \
    "ds_read_b64_tr_b16 v[122:123], v228 offset:16688\n" \
    "ds_read_b64_tr_b16 v[124:125], v228 offset:18960\n" \
    "ds_read_b64_tr_b16 v[126:127], v228 offset:18992\n" \
    "ds_read_b64_tr_b16 v[128:129], v228 offset:21264\n" \
    "ds_read_b64_tr_b16 v[130:131], v228 offset:21296\n" \
    "ds_read_b64_tr_b16 v[132:133], v228 offset:23568\n" \
    "ds_read_b64_tr_b16 v[134:135], v228 offset:23600\n" \
    "s_waitcnt lgkmcnt(14)\n" \
    "v_mfma_f32_16x16x32_bf16 v[136:139], v[104:107], v[0:3], 0\n" \
    "v_mfma_f32_16x16x32_bf16 v[144:147], v[104:107], v[8:11], 0\n" \
    "v_mfma_f32_16x16x32_bf16 v[152:155], v[104:107], v[16:19], 0\n" \
    "v_mfma_f32_16x16x32_bf16 v[160:163], v[104:107], v[24:27], 0\n" \
    "s_waitcnt lgkmcnt(12)\n" \
    "v_mfma_f32_16x16x32_bf16 v[136:139], v[108:111], v[4:7], v[136:139]\n" \
    "v_mfma_f32_16x16x32_bf16 v[144:147], v[108:111], v[12:15], v[144:147]\n" \
    "v_mfma_f32_16x16x32_bf16 v[152:155], v[108:111], v[20:23], v[152:155]\n" \
    "v_mfma_f32_16x16x32_bf16 v[160:163], v[108:111], v[28:31], v[160:163]\n" \
    "s_waitcnt lgkmcnt(10)\n" \
    "v_mfma_f32_16x16x32_bf16 v[136:139], v[112:115], v[32:35], v[136:139]\n" \
    "v_mfma_f32_16x16x32_bf16 v[144:147], v[112:115], v[40:43], v[144:147]\n" \
    "v_mfma_f32_16x16x32_bf16 v[152:155], v[112:115], v[48:51], v[152:155]\n" \
    "v_mfma_f32_16x16x32_bf16 v[160:163], v[112:115], v[56:59], v[160:163]\n" \
    "s_waitcnt lgkmcnt(8)\n" \
    "v_mfma_f32_16x16x32_bf16 v[136:139], v[116:119], v[36:39], v[136:139]\n" \
    "v_mfma_f32_16x16x32_bf16 v[144:147], v[116:119], v[44:47], v[144:147]\n" \
    "v_mfma_f32_16x16x32_bf16 v[152:155], v[116:119], v[52:55], v[152:155]\n" \
    "v_mfma_f32_16x16x32_bf16 v[160:163], v[116:119], v[60:63], v[160:163]\n" \
    "s_waitcnt lgkmcnt(6)\n" \
    "v_mfma_f32_16x16x32_bf16 v[140:143], v[120:123], v[0:3], 0\n" \
    "v_mfma_f32_16x16x32_bf16 v[148:151], v[120:123], v[8:11], 0\n" \
    "v_mfma_f32_16x16x32_bf16 v[156:159], v[120:123], v[16:19], 0\n" \
    "v_mfma_f32_16x16x32_bf16 v[164:167], v[120:123], v[24:27], 0\n" \
    "s_waitcnt lgkmcnt(4)\n" \
    "v_mfma_f32_16x16x32_bf16 v[140:143], v[124:127], v[4:7], v[140:143]\n" \
    "v_mfma_f32_16x16x32_bf16 v[148:151], v[124:127], v[12:15], v[148:151]\n" \
    "v_mfma_f32_16x16x32_bf16 v[156:159], v[124:127], v[20:23], v[156:159]\n" \
    "v_mfma_f32_16x16x32_bf16 v[164:167], v[124:127], v[28:31], v[164:167]\n" \
    "s_waitcnt lgkmcnt(2)\n" \
    "v_mfma_f32_16x16x32_bf16 v[140:143], v[128:131], v[32:35], v[140:143]\n" \
    "v_mfma_f32_16x16x32_bf16 v[148:151], v[128:131], v[40:43], v[148:151]\n" \
    "v_mfma_f32_16x16x32_bf16 v[156:159], v[128:131], v[48:51], v[156:159]\n" \
    "v_mfma_f32_16x16x32_bf16 v[164:167], v[128:131], v[56:59], v[164:167]\n" \
    "s_waitcnt lgkmcnt(0)\n" \
    "v_mfma_f32_16x16x32_bf16 v[140:143], v[132:135], v[36:39], v[140:143]\n" \
    "v_mfma_f32_16x16x32_bf16 v[148:151], v[132:135], v[44:47], v[148:151]\n" \
    "v_mfma_f32_16x16x32_bf16 v[156:159], v[132:135], v[52:55], v[156:159]\n" \
    "v_mfma_f32_16x16x32_bf16 v[164:167], v[132:135], v[60:63], v[164:167]\n" \
    "v_cvt_pk_bf16_f32 v65, v136, v137\n" \
    "v_cvt_pk_bf16_f32 v68, v138, v139\n" \
    "v_cvt_pk_bf16_f32 v73, v144, v145\n" \
    "v_cvt_pk_bf16_f32 v76, v146, v147\n" \
    "v_cvt_pk_bf16_f32 v81, v152, v153\n" \
    "v_cvt_pk_bf16_f32 v84, v154, v155\n" \
    "v_cvt_pk_bf16_f32 v89, v160, v161\n" \
    "v_cvt_pk_bf16_f32 v92, v162, v163\n" \
    "v_cvt_pk_bf16_f32 v66, v142, v143\n" \
    "v_cvt_pk_bf16_f32 v67, v142, v143\n" \
    "v_cvt_pk_bf16_f32 v69, v140, v141\n" \
    "v_cvt_pk_bf16_f32 v74, v150, v151\n" \
    "v_cvt_pk_bf16_f32 v75, v150, v151\n" \
    "v_cvt_pk_bf16_f32 v77, v148, v149\n" \
    "v_cvt_pk_bf16_f32 v82, v158, v159\n" \
    "v_cvt_pk_bf16_f32 v83, v158, v159\n" \
    "v_cvt_pk_bf16_f32 v85, v156, v157\n" \
    "v_cvt_pk_bf16_f32 v90, v166, v167\n" \
    "v_cvt_pk_bf16_f32 v91, v166, v167\n" \
    "v_cvt_pk_bf16_f32 v93, v164, v165\n" \
    "v_permlane16_swap_b32 v66, v67\n" \
    "v_permlane16_swap_b32 v74, v75\n" \
    "v_permlane16_swap_b32 v82, v83\n" \
    "v_permlane16_swap_b32 v90, v91\n" \
    "v_mov_b64 v[176:177], 0\n" \
    "v_mov_b64 v[178:179], 0\n" \
    "v_mov_b64 v[180:181], 0\n" \
    "v_mov_b64 v[182:183], 0\n" \
    "v_mov_b64 v[184:185], 0\n" \
    "v_mov_b64 v[186:187], 0\n" \
    "v_mov_b64 v[188:189], 0\n" \
    "v_mov_b64 v[190:191], 0\n" \
    "v_mov_b64 v[196:197], 0\n" \
    "v_mov_b64 v[198:199], 0\n" \
    "v_mov_b64 v[200:201], 0\n" \
    "v_mov_b64 v[202:203], 0\n" \
    "v_mov_b64 v[204:205], 0\n" \
    "v_mov_b64 v[206:207], 0\n" \
    "v_mov_b64 v[208:209], 0\n" \
    "v_mov_b64 v[210:211], 0\n" \
    "v_mov_b64 v[212:213], 0\n" \
    "v_mov_b64 v[214:215], 0\n" \
    "s_add_u32 s85, s60, 0x1000\n" \
    "s_add_u32 s82, s76, 1\n" \
    "s_min_u32 s82, s82, 63\n" \
    "s_lshl_b32 s83, s82, 19\n" \
    "s_add_u32 s83, s83, s72\n" \
    "s_lshl_b32 s79, s82, 11\n" \
    "s_and_b32 s79, s79, 0x2000\n" \
    "s_xor_b32 s83, s83, s79\n" \
    "v_cmp_ge_u32 vcc, s82, v237\n" \
    "s_cbranch_vccnz L%=_Pfslow\n" \
    "L%=_Pfok:\n" \
    "s_add_u32 s79, s82, 3\n" \
    "v_mov_b32 v150, 0x40000000\n" \
    "v_cmp_lt_u32 vcc, s79, v237\n" \
    "s_nop 1\n" \
    "v_cndmask_b32 v150, v235, v150, vcc\n" \
    "s_branch L%=_Pu2b\n" \
    "L%=_Pu2a:\n" \
    "s_sleep 4\n" \
    "L%=_Pu2b:\n" \
    "s_memrealtime vcc\n" \
    "s_add_u32 s80, s77, s64\n" \
    "s_add_u32 s80, s80, s69\n" \
    "s_waitcnt lgkmcnt(0)\n" \
    "s_sub_i32 s79, s80, vcc_lo\n" \
    "s_sub_u32 s79, s79, 40\n" \
    "s_cmp_lt_u32 s79, 0x400\n" \
    "s_cbranch_scc1 L%=_Pu2a\n" \
    "s_memtime s[38:39]\n" \
    "s_memrealtime vcc\n" \
    "s_nop 4\n" \
    "s_memrealtime s[32:33]\n" \
    "s_nop 4\n" \
    "s_memrealtime s[34:35]\n" \
    "s_nop 4\n" \
    "s_memrealtime s[36:37]\n" \
    "s_add_u32 s80, s77, s64\n" \
    "s_lshl_b32 s80, s80, 2\n" \
    "s_waitcnt lgkmcnt(0)\n" \
    "s_sub_u32 s80, s80, vcc_lo\n" \
    "s_sub_u32 s80, s80, s32\n" \
    "s_sub_u32 s80, s80, s34\n" \
    "s_sub_u32 s80, s80, s36\n" \
    "s_mul_i32 s80, s80, s71\n" \
    "s_ashr_i32 s80, s80, 6\n" \
    "s_add_u32 s78, s80, s38\n" \
    "L%=_Pu3a:\n" \
    "s_memtime s[38:39]\n" \
    "s_mul_i32 s80, s69, 23\n" \
    "s_add_u32 s80, s80, s78\n" \
    "s_waitcnt lgkmcnt(0)\n" \
    "s_sub_i32 s79, s80, s38\n" \
    "s_max_i32 s79, s79, 0\n" \
    "s_cmp_gt_u32 s79, 320\n" \
    "s_cbranch_scc1 L%=_Pu3s\n" \
    ".p2align 4\n" \
    "L%=_Pu3d:\n" \
    "s_sub_u32 s79, s79, 28\n" \
    "s_cbranch_scc0 L%=_Pu3d\n" \
    "L%=_Pu3e:\n" \
    "buffer_load_dwordx4 v[104:107], v230, s[56:59], s83 offen offset:0 sc1\n" \
    "buffer_load_dwordx4 v[108:111], v230, s[56:59], s83 offen offset:1024 sc1\n" \
    "buffer_load_dwordx4 v[112:115], v230, s[56:59], s83 offen offset:2048 sc1\n" \
    "buffer_load_dwordx4 v[116:119], v230, s[56:59], s83 offen offset:3072 sc1\n" \
    "s_add_u32 s79, s83, 0x1000\n" \
    "buffer_load_dwordx4 v[120:123], v230, s[56:59], s79 offen offset:0 sc1\n" \
    "buffer_load_dwordx4 v[124:127], v230, s[56:59], s79 offen offset:1024 sc1\n" \
    "buffer_load_dwordx4 v[128:131], v230, s[56:59], s79 offen offset:2048 sc1\n" \
    "buffer_load_dwordx4 v[132:135], v230, s[56:59], s79 offen offset:3072 sc1\n" \
    "s_xor_b32 s79, s83, s75\n" \
    "buffer_load_dwordx4 v[136:139], v230, s[52:55], s79 offen offset:0 sc1\n" \
    "buffer_load_dwordx4 v[140:143], v230, s[52:55], s79 offen offset:1024 sc1\n" \
    "buffer_load_dword v151, v150, s[44:47], 0 offen sc1\n" \
    "s_lshl_b32 s79, s76, 7\n" \
    "buffer_load_dwordx4 v[144:147], off, s[44:47], s79 offset:2176 sc1\n" \
    "buffer_store_dword v243, v239, s[44:47], s79 offen sc1\n" \
    "buffer_store_dwordx4 v[244:247], v232, s[40:43], s62 offen offset:0 sc1\n" \
    "buffer_store_dwordx4 v[244:247], v232, s[40:43], s62 offen offset:1024 sc1\n" \
    "buffer_store_dwordx4 v[244:247], v232, s[40:43], s62 offen offset:2048 sc1\n" \
    "buffer_store_dwordx4 v[244:247], v232, s[40:43], s62 offen offset:3072 sc1\n" \
    "buffer_store_dwordx4 v[244:247], v233, s[40:43], s62 offen offset:0 sc1\n" \
    "buffer_store_dwordx4 v[244:247], v233, s[40:43], s62 offen offset:1024 sc1\n" \
    "buffer_store_dwordx4 v[244:247], v234, s[40:43], s62 offen sc1\n" \
    "s_barrier\n" \
    "s_cmp_eq_u32 s76, 63\n" \
    "s_cbranch_scc1 L%=_Pfin\n" \
    "ds_read_b32 v216, v229\n" \
    "s_waitcnt lgkmcnt(0)\n" \
    "v_readfirstlane_b32 s94, v216\n" \
    "s_mul_i32 s79, s94, s70\n" \
    "s_add_u32 s79, s79, s81\n" \
    "s_cmp_lg_u32 s79, s77\n" \
    "s_cbranch_scc1 L%=_Pu4s\n" \
    "L%=_Pu4k:\n" \
    "s_sub_u32 s32, s78, s97\n" \
    "s_sub_u32 s33, s77, s98\n" \
    "s_mov_b32 s97, s78\n" \
    "s_mov_b32 s98, s77\n" \
    "s_lshl_b32 s32, s32, 4\n" \
    "s_cmp_eq_u32 s76, 0\n" \
    "s_cbranch_scc1 L%=_Pck\n" \
    "v_cvt_f32_u32 v148, s33\n" \
    "v_cvt_f32_u32 v149, s32\n" \
    "v_rcp_f32 v148, v148\n" \
    "s_cmp_lt_u32 s76, 4\n" \
    "s_cselect_b32 s34, 0, 0x3c449ba6\n" \
    "s_cmp_eq_u32 s94, s74\n" \
    "s_mov_b32 s74, s94\n" \
    "s_cselect_b32 s32, 0x7f000000, 0\n" \
    "s_cselect_b32 s33, 0.5, 1.0\n" \
    "s_cselect_b32 s34, 0, s34\n" \
    "v_fma_f32 v148, v148, v149, -v240\n" \
    "v_max_f32 v149, 0.5, v241\n" \
    "v_min_f32 v148, s32, v148\n" \
    "v_cmp_gt_f32 vcc, 0, v148\n" \
    "v_mul_f32 v242, 0x3f7851ec, v242\n" \
    "v_add_f32 v242, s34, v242\n" \
    "v_cndmask_b32 v149, v241, v149, vcc\n" \
    "v_fma_f32 v240, v148, v149, v240\n" \
    "v_mul_f32 v241, s33, v241\n" \
    "v_max_f32 v240, 0x43400000, v240\n" \
    "v_max_f32 v241, 0x3e000000, v241\n" \
    "v_min_f32 v240, 0x43e00000, v240\n" \
    "v_fma_f32 v148, v242, s73, s73\n" \
    "v_rcp_f32 v149, v240\n" \
    "s_nop 0\n" \
    "v_fma_f32 v148, v148, v149, 0.5\n" \
    "v_cvt_u32_f32 v243, v148\n" \
    "L%=_Pck:\n" \
    "v_add_f32 v149, 0.5, v240\n" \
    "v_mov_b32 v148, s65\n" \
    "v_cvt_u32_f32 v149, v149\n" \
    "s_waitcnt vmcnt(8)\n" \
    "ds_write_b128 v231, v[104:107] offset:16384\n" \
    "ds_write_b128 v231, v[108:111] offset:17536\n" \
    "ds_write_b128 v231, v[112:115] offset:18688\n" \
    "ds_write_b128 v231, v[116:119] offset:19840\n" \
    "ds_write_b128 v231, v[120:123] offset:20992\n" \
    "ds_write_b128 v231, v[124:127] offset:22144\n" \
    "ds_write_b128 v231, v[128:131] offset:23296\n" \
    "ds_write_b128 v231, v[132:135] offset:24448\n" \
    "ds_write_b128 v230, v[136:139] offset:25600\n" \
    "ds_write_b128 v230, v[140:143] offset:26624\n" \
    "v_max3_u32 v144, v144, v145, v146\n" \
    "v_max_u32 v144, v144, v147\n" \
    "v_cmp_eq_u32 vcc, 0, v144\n" \
    "s_nop 1\n" \
    "v_cndmask_b32 v148, v144, v148, vcc\n" \
    "ds_write_b64 v229, v[148:149] offset:64\n" \
    "v_max_u32 v237, v237, v151\n" \
    "s_setprio 3\n" \
    "L%=_Pu5a:\n" \
    "s_memtime s[38:39]\n" \
    "s_mul_hi_u32 s80, s67, s96\n" \
    "s_add_u32 s80, s80, s78\n" \
    "s_waitcnt lgkmcnt(0)\n" \
    "s_sub_i32 s79, s80, s38\n" \
    "s_max_i32 s79, s79, 0\n" \
    "s_cmp_gt_u32 s79, 320\n" \
    "s_cbranch_scc1 L%=_Pu5s\n" \
    ".p2align 4\n" \
    "L%=_Pu5d:\n" \
    "s_sub_u32 s79, s79, 28\n" \
    "s_cbranch_scc0 L%=_Pu5d\n" \
    "L%=_Pu5e:\n" \
    "buffer_load_dwordx4 v[168:171], v225, s[40:43], s60 offen sc1\n" \
    "buffer_load_dwordx4 v[172:175], v225, s[40:43], s85 offen sc1\n" \
    "L%=_Pvc:\n" \
    "s_waitcnt vmcnt(1)\n" \
    "v_smfmac_f32_16x16x64_bf16 v[176:179], v[168:171], v[0:7], v224\n" \
    "v_smfmac_f32_16x16x64_bf16 v[180:183], v[168:171], v[8:15], v224\n" \
    "s_waitcnt vmcnt(0)\n" \
    "v_smfmac_f32_16x16x64_bf16 v[176:179], v[172:175], v[32:39], v224\n" \
    "v_smfmac_f32_16x16x64_bf16 v[180:183], v[172:175], v[40:47], v224\n" \
    "v_smfmac_f32_16x16x64_bf16 v[184:187], v[168:171], v[16:23], v224\n" \
    "v_smfmac_f32_16x16x64_bf16 v[188:191], v[168:171], v[24:31], v224\n" \
    "v_add_f32 v176, v176, v178\n" \
    "v_smfmac_f32_16x16x64_bf16 v[184:187], v[172:175], v[48:55], v224\n" \
    "v_add_f32 v177, v177, v179\n" \
    "v_smfmac_f32_16x16x64_bf16 v[188:191], v[172:175], v[56:63], v224\n" \
    "v_cmp_u_f32 vcc, v176, v177\n" \
    "v_cvt_pk_bf16_f32 v64, v176, v177\n" \
    "v_add_f32 v180, v180, v182\n" \
    "v_add_f32 v181, v181, v183\n" \
    "v_cvt_pk_bf16_f32 v72, v180, v181\n" \
    "s_cbranch_vccnz L%=_Pvslow\n" \
    "L%=_Pvok:\n" \
    "v_add_f32 v184, v184, v186\n" \
    "v_add_f32 v185, v185, v187\n" \
    "v_cvt_pk_bf16_f32 v80, v184, v185\n" \
    "v_add_f32 v188, v188, v190\n" \
    "v_add_f32 v189, v189, v191\n" \
    "v_cvt_pk_bf16_f32 v88, v188, v189\n" \
    "s_barrier\n" \
    "L%=_Pfr:\n" \
    "ds_read_b128 v[192:195], v227\n" \
    "s_waitcnt lgkmcnt(0)\n" \
    "v_smfmac_f32_16x16x64_bf16 v[212:215], v[192:195], v[96:103], v224\n" \
    "v_cmp_ne_u32 vcc, s76, v195\n" \
    "v_smfmac_f32_16x16x64_bf16 v[196:199], v[192:195], v[64:71], v224\n" \
    "s_cbranch_vccnz L%=_Pfs\n" \
    "v_smfmac_f32_16x16x64_bf16 v[200:203], v[192:195], v[72:79], v224\n" \
    "v_smfmac_f32_16x16x64_bf16 v[204:207], v[192:195], v[80:87], v224\n" \
    "v_add_f32 v212, v212, v214\n" \
    "v_smfmac_f32_16x16x64_bf16 v[208:211], v[192:195], v[88:95], v224\n" \
    "v_add_f32 v213, v213, v215\n" \
    "v_rcp_f32 v220, v212\n" \
    "v_rcp_f32 v221, v213\n" \
    "v_add_f32 v196, v196, v198\n" \
    "v_add_f32 v197, v197, v199\n" \
    "v_pk_add_f32 v[200:201], v[200:201], v[202:203]\n" \
    "v_pk_add_f32 v[204:205], v[204:205], v[206:207]\n" \
    "s_nop 0\n" \
    "v_pk_add_f32 v[208:209], v[208:209], v[210:211]\n" \
    "v_pk_mul_f32 v[196:197], v[196:197], v[220:221]\n" \
    "v_pk_mul_f32 v[200:201], v[200:201], v[220:221]\n" \
    "v_pk_mul_f32 v[204:205], v[204:205], v[220:221]\n" \
    "v_pk_mul_f32 v[208:209], v[208:209], v[220:221]\n" \
    "v_cvt_pk_bf16_f32 v216, v196, v200\n" \
    "v_cvt_pk_bf16_f32 v217, v204, v208\n" \
    "v_cvt_pk_bf16_f32 v218, v197, v201\n" \
    "v_cvt_pk_bf16_f32 v219, v205, v209\n" \
    "buffer_store_dwordx4 v[216:219], v226, s[40:43], s60 offen sc1\n" \
    "s_setprio 0\n" \
    "ds_read_b64 v[220:221], v229 offset:64\n" \
    "s_mov_b32 s79, s60\n" \
    "s_mov_b32 s60, s61\n" \
    "s_mov_b32 s61, s62\n" \
    "s_mov_b32 s62, s63\n" \
    "s_mov_b32 s63, s79\n" \
    "s_add_u32 s76, s76, 1\n" \
    "s_add_u32 s77, s77, s65\n" \
    "s_add_u32 s84, s84, 0x20000\n" \
    "s_add_u32 s81, s81, s65\n" \
    "s_waitcnt lgkmcnt(0)\n" \
    "v_readfirstlane_b32 s65, v220\n" \
    "v_readfirstlane_b32 s71, v221\n" \
    "s_mul_i32 s96, s65, s71\n" \
    "s_lshr_b32 s96, s96, 4\n" \
    "s_branch L%=_Ploop\n" \
    "L%=_Pfslow:\n" \
    "s_sleep 4\n" \
    "buffer_load_dword v237, v235, s[44:47], 0 offen sc1\n" \
    "s_waitcnt vmcnt(0)\n" \
    "v_cmp_ge_u32 vcc, s82, v237\n" \
    "s_cbranch_vccz L%=_Pfok\n" \
    "s_cmp_eq_u32 s99, 0\n" \
    "s_cbranch_scc1 L%=_Pfok\n" \
    "s_sub_u32 s99, s99, 1\n" \
    "s_branch L%=_Pfslow\n" \
    "L%=_Pfin:\n" \
    "s_waitcnt vmcnt(0)\n" \
    "s_barrier\n" \
    "s_branch L%=_end\n" \
    "L%=_Pu3s:\n" \
    "s_cmp_gt_u32 s79, 0x8000\n" \
    "s_cbranch_scc1 L%=_Pu3e\n" \
    "s_sleep 2\n" \
    "s_branch L%=_Pu3a\n" \
    "L%=_Pu4s:\n" \
    "s_mov_b32 s77, s79\n" \
    "s_memtime s[38:39]\n" \
    "s_memrealtime vcc\n" \
    "s_nop 4\n" \
    "s_memrealtime s[32:33]\n" \
    "s_nop 4\n" \
    "s_memrealtime s[34:35]\n" \
    "s_nop 4\n" \
    "s_memrealtime s[36:37]\n" \
    "s_add_u32 s80, s77, s64\n" \
    "s_lshl_b32 s80, s80, 2\n" \
    "s_waitcnt lgkmcnt(0)\n" \
    "s_sub_u32 s80, s80, vcc_lo\n" \
    "s_sub_u32 s80, s80, s32\n" \
    "s_sub_u32 s80, s80, s34\n" \
    "s_sub_u32 s80, s80, s36\n" \
    "s_mul_i32 s80, s80, s71\n" \
    "s_ashr_i32 s80, s80, 6\n" \
    "s_add_u32 s78, s80, s38\n" \
    "s_branch L%=_Pu4k\n" \
    "L%=_Pu5s:\n" \
    "s_cmp_gt_u32 s79, 0x8000\n" \
    "s_cbranch_scc1 L%=_Pu5e\n" \
    "s_sleep 2\n" \
    "s_branch L%=_Pu5a\n" \
    "L%=_Pvslow:\n" \
    "s_nop 7\n" \
    "s_nop 0\n" \
    "v_max_u32 v216, v168, v169\n" \
    "v_max3_u32 v216, v216, v170, v171\n" \
    "v_max3_u32 v216, v216, v172, v173\n" \
    "v_max3_u32 v216, v216, v174, v175\n" \
    "v_cmp_eq_u32 vcc, -1, v216\n" \
    "s_cbranch_vccz L%=_Pvok\n" \
    "s_cmp_eq_u32 s99, 0\n" \
    "s_cbranch_scc1 L%=_Pvok\n" \
    "s_sub_u32 s99, s99, 1\n" \
    "s_cmp_lg_u32 s99, 0\n" \
    "s_cbranch_scc1 L%=_Pu6\n" \
    "v_mov_b32 v216, 1\n" \
    "buffer_store_dword v216, off, s[44:47], 0 sc1\n" \
    "L%=_Pu6:\n" \
    "s_sleep 1\n" \
    "buffer_load_dwordx4 v[168:171], v225, s[40:43], s60 offen sc1\n" \
    "buffer_load_dwordx4 v[172:175], v225, s[40:43], s85 offen sc1\n" \
    "v_mov_b64 v[176:177], 0\n" \
    "v_mov_b64 v[178:179], 0\n" \
    "v_mov_b64 v[180:181], 0\n" \
    "v_mov_b64 v[182:183], 0\n" \
    "v_mov_b64 v[184:185], 0\n" \
    "v_mov_b64 v[186:187], 0\n" \
    "v_mov_b64 v[188:189], 0\n" \
    "v_mov_b64 v[190:191], 0\n" \
    "s_waitcnt vmcnt(0)\n" \
    "s_branch L%=_Pvc\n" \
    "L%=_Pfs:\n" \
    "s_nop 7\n" \
    "s_nop 0\n" \
    "v_mov_b64 v[196:197], 0\n" \
    "v_mov_b64 v[198:199], 0\n" \
    "v_mov_b64 v[200:201], 0\n" \
    "v_mov_b64 v[202:203], 0\n" \
    "v_mov_b64 v[204:205], 0\n" \
    "v_mov_b64 v[206:207], 0\n" \
    "v_mov_b64 v[208:209], 0\n" \
    "v_mov_b64 v[210:211], 0\n" \
    "v_mov_b64 v[212:213], 0\n" \
    "v_mov_b64 v[214:215], 0\n" \
    "s_branch L%=_Pfr\n" \
    "L%=_end:\n"
#define V8(b) "v" #b "0", "v" #b "1", "v" #b "2", "v" #b "3", "v" #b "4", "v" #b "5", "v" #b "6", "v" #b "7", "v" #b "8", "v" #b "9"
#define S_IO "+{s[40:55]}"(sA), "+{s[56:71]}"(sB), "+{s[72:75]}"(sC), "+{s78}"(lwo), "={s[92:99]}"(so)
#define FIN2() do {} while (0)
#define S_CLOB "memory", "vcc", "scc", "m0", "s32", "s33", "s34", "s35", "s36", "s37", "s38", "s39", "s76", "s77", "s79", "s80", "s81", "s82", "s83", "s84", "s85", "s86", "s87", "s88", "s89", "s90", "s91"
#define B_CLOB S_CLOB, "v64", "v65", "v66", "v67", "v68", "v69", V8(7), V8(8), V8(9), V8(10), V8(11), V8(12), V8(13), V8(14), V8(15), V8(16), V8(17), V8(18), V8(19), V8(20), V8(21), \
    "v220", "v221", "v222", "v223", "v240", "v241", "v242", "v243"
#define FIN() do {} while (0)

#define MK_S() \
    a = args(); \
    v16u sA = cat8(cat4(mkrs(a->ring, NSLOT * SLOT), mkrs(a->ctl, CTL_SZ)), cat4(mkrs(a->mb, NSEG * NH * 4096), mkrs(a->qkv + QSZ, QSZ))); \
    v16u sB = cat8(cat4(mkrs(a->qkv + 2 * (size_t)QSZ, QSZ), v4u{0, 0, 0, 0}), \
                   v8u{0, (unsigned)a->per0, (unsigned)a->d1, (unsigned)a->dv, (unsigned)a->d2, (unsigned)a->dp, (unsigned)a->slack, (unsigned)a->thr}); \
    const v4u rsc = __builtin_shufflevector(sA, sA, 4, 5, 6, 7)
#define HALVES() do { const unsigned u_ = beacon(rsc, b0, b1, 16); \
_Pragma("unroll") \
    for (int k = 0; k < NSLOT; ++k) sB[4 + k] = k * (unsigned)SLOT + ((u_ >> k) & 1) * 8192u; } while (0)

#define START() __hip_atomic_fetch_max((LAS unsigned*)(lds + L_MISC), (rt32() - spare) / sB[14] + 1, __ATOMIC_RELAXED, __HIP_MEMORY_SCOPE_WORKGROUP)

DEV void chain(LAS char* lds, const int c) {
    KArgs a = args();
    const int w = __builtin_amdgcn_readfirstlane(threadIdx.x >> 6), h = c >> 2, j = c & 3;
    const unsigned lb = (unsigned)(size_t)lds, lbw = lb + L_STG + w * 16384;
#define LANE() int l = threadIdx.x & 63; asm volatile("" : "+v"(l)); const int l15 = l & 15, g = l >> 4, hb = (l >> 1) & 1, q4 = l15 >> 2; const unsigned idx = hb ? 0xEEEE : 0x4444
    v4u sC, stv; v2u stw; unsigned stx; v8u so;
    unsigned lwo = w & 3, b0, b1;
    if (w < 4) {
        LANE();
        v16u wt[12], m;
            const v4u rw[3] = {mkrs(a->Wk, DM * HD * 2), mkrs(a->Wv, DM * HD * 2), mkrs(a->Wq, DM * HD * 2)};
            const unsigned vo = (l >> 2) * (HD * 2) + (l & 3) * 16, sw = (256 * w * HD + h * DH + 32 * j) * 2;
            auto issue = [&](int p) {
#pragma unroll
                for (int i = 0; i < 8; ++i) DMA16N(rw[p >> 1], vo, sw + ((p & 1) * 128 + i * 16) * (HD * 2), lbw + (p & 1) * 8192 + i * 1024); };
            {
                const v4u rp = w & 1 ? rw[2] : rw[1];
                const unsigned ps = ((rp[0] >> 7) + (c >> 5) * 16) & 63, po = w < 2 ? ((3 - ps) & 7) * 128 + (l & 1) * 1024 : (63 - ps < 16 && !(l & 1) ? (63 - ps) * 128 : OOB);
                if (a->pf & 1) asm volatile("s_mov_b32 m0, %0\n\ts_nop 0\n\tbuffer_load_dword %1, %2, 0 offen lds" :: "s"(lb + L_PF + w * 256), "v"(((c & 31) * 32 + (l >> 1)) * (HD * 2) + (c >> 5) * 2048 + po), "s"(rp) : "memory", "m0");
            }
            unsigned mr[16];
#pragma unroll
            for (int k = 0; k < 16; ++k) mr[k] = *(const unsigned*)(a->mem0 + (2 * q4 + (l & 1)) * DM + 256 * w + 16 * k + 4 * g + 2 * hb);
            SCHED();
            issue(0); issue(1);
            MK_S();
            unsigned spare = a->spare; asm volatile("" : "+s"(sA), "+s"(sB), "+s"(spare));
            const LAS char* pw = lds + L_STG + w * 16384 + (4 * (g >> 1) + (g & 1) + 2 * (q4 >> 1) + 16 * (q4 & 1)) * 64 + (l & 3) * 8;
#pragma unroll
            for (int p = 0; p < 6; ++p) {
                if (p < 5) WAITV(8); else WAITV(0);
#pragma unroll
                for (int k = 0; k < 2; ++k) wt[(2 * p + k + 4) % 12] = cat8(rd_w<2048, 512, 2560>(pw + (p & 1) * 8192 + k * 4096), rd_w<2048, 512, 2560>(pw + (p & 1) * 8192 + k * 4096 + 32));
                WAITL(0);
                if (p < 4) issue(p + 2);
                if (p == 1) { RBAR(); b0 = ((volatile LAS unsigned*)(lds + L_MISC))[3]; }
                if (p == 3) BC_LD(b1, rsc);
            }
#pragma unroll
            for (int k = 0; k < 16; k += 4) {
                m[k] = __builtin_amdgcn_perm(mr[k + 1], mr[k], 0x05040100u); m[k + 1] = __builtin_amdgcn_perm(mr[k + 3], mr[k + 2], 0x05040100u);
                m[k + 2] = __builtin_amdgcn_perm(mr[k + 1], mr[k], 0x07060302u); m[k + 3] = __builtin_amdgcn_perm(mr[k + 3], mr[k + 2], 0x07060302u);
            }
        const unsigned pq = nearo(H3_OFF + (c >> 5) * 16384) + w * 1024 + l * 16;
        const v8u va = {idx, pq, 0, 0, lb + L_MISC + w * 4, lb + L_MISC, 0, pq + 16384};
        sC = v4u{lb + w * 2176u, lb + w * 1280u, 0, 0};
        HALVES();
        START();
        asm volatile(ASM_A : S_IO, "+{v[192:207]}"(m)
                     : "{v[0:31]}"(cat16(wt[0], wt[1])), "{v[32:63]}"(cat16(wt[2], wt[3])), "{v[64:95]}"(cat16(wt[4], wt[5])), "{v[96:127]}"(cat16(wt[6], wt[7])),
                       "{v[128:159]}"(cat16(wt[8], wt[9])), "{v[160:191]}"(cat16(wt[10], wt[11])), "{v[232:239]}"(va)
                     : S_CLOB, "v208", "v209", V8(21), V8(22), "v230", "v231", "v240", "v241", "v244", "v245", "v246", "v247");
        FIN();
    } else {
        LANE();
        const int bw = w - 4, l7 = l & 7;
        MK_S();
        BC_LD(b0, rsc);
        auto ldw = [&]() {
            const v4u rw = mkrs(a->Wo, HD * DM * 2);
            const unsigned vo = (l >> 3) * (DM * 2) + l7 * 16, sw = (h * DH * DM + 256 * j + 64 * bw) * 2;
#pragma unroll
            for (int i = 0; i < 16; ++i) DMA16N(rw, vo, sw + i * 8 * (DM * 2), lbw + i * 1024); };
        if (w != 7) ldw();
        else {
            asm volatile("s_waitcnt vmcnt(0)" : "+v"(b0) :: "memory");
            if (l < 16) ((LAS unsigned*)(lds + L_MISC))[l] = l ? (l == 3 ? b0 : 0) : rt32() / sB[14];
            WAITL(0);
            ldw();
        }
        RBAR();
        BC_LD(b1, rsc);
        WAITV(0);
        HALVES();
        const LAS char* pw = lds + L_STG + w * 16384 + (16 * (g & 1) + 8 * (g >> 1) + (q4 & 1) + 4 * (q4 >> 1)) * 128 + (l & 3) * 8;
#define RDO(o) rd_w<256, 4096, 4352>(pw + (o))
        const v32u wo0 = cat16(cat8(RDO(0), RDO(32)), cat8(RDO(64), RDO(96))), wo1 = cat16(cat8(RDO(8192), RDO(8224)), cat8(RDO(8256), RDO(8288)));
        const unsigned c3 = ((c >> 3) & 3) * 4096 + (c >> 5) * 1024 + (c & 7) * 128, h1 = nearo(H1_OFF + h * 8192), h1j = h1 + j * 2048;
        v16u va = {};
        va[0] = idx; va[1] = h1 + (g >> 1) * 2048 + 1280 + (l & 31) * 16;
        va[2] = nearo(H2_OFF + (32 * j + 8 * bw + q4 * 2 + (g >> 1)) * 4096) + h * 128 + ((g & 1) * 4 + (l & 3)) * 16;
        va[3] = lb + L_P + g * 320 + l15 * 16;
        va[4] = lb + (l & 1) * X_BLK + (l & 2) * X_FQ + (8 * (g >> 1) + (q4 & 1) + 4 * (q4 >> 1)) * 16 + (g & 1) * 8;
        va[5] = lb + L_MISC;
        v4u sel = {};
        if (w == 4) {
            const int tok = l & 7, u = l >> 5, z = (l >> 3) & 3;
            va[6] = h1 + (z == 0 ? 8 + u : (z == 1 ? 2 * u : (z == 2 ? 4 + 2 * u : 2 * u + 1))) * 128 + tok * 16;
            va[7] = h1 + (5 + 2 * u) * 128 + tok * 16 + (z & 1) * 8;
            va[8] = lb + L_P + ((z & 2) + u) * 320 + (4 * (tok >> 1) + 2 * (z & 1) + (tok & 1)) * 16;
            va[9] = l == 0 ? F_PG + c * 128 : OOB;
            *(LAS v4u*)(lds + va[8]) = v4u{POI, POI, POI, POI};
            sC = v4u{__float_as_uint(SCALE * LOG2E), __float_as_uint(SCALE * LOG2E), 0, 0};
        } else if (w == 5) {
            va[6] = lb + L_PQK + (l15 >> 3) * 256 + (l & 1) * 544 + (l7 >> 1) * 64 + g * 16;
            va[7] = (l & 8) ? OOB : h1j + g * 128 + l7 * 16;
            va[8] = ((l & 8) || g < 2) ? OOB : h1j + 1024 + ((g & 1) * 8 + l7) * 16;
            va[9] = (l & 8) ? h * 4096 + j * 512 + (g * 8 + l7) * 16 : OOB;
            va[10] = lb + l * 16;
            sC = v4u{0, 0, 0, 0};
        } else if (w == 6) {
            const int rho = (l >> 1) & 15, tok = 2 * (rho >> 2) + (rho & 1), c4 = 2 * (l >> 5) + ((rho >> 1) & 1);
            va[6] = lb + ((l & 1) * 160 + (tok & 1) * 72 + (tok >> 1) * 16 + c4 * 4) * 4;
            va[7] = (l & 1) ? OOB : h1j + 1280 + (l >> 1) * 16;
            va[8] = lb + L_VT + tok * 64 + (l & 1) * 32 + c4 * 8;
            va[9] = lb + L_VT + q4 * 64 + (g & 1) * 32 + (l & 3) * 8;
            va[10] = l < 32 ? h * 4096 + 2048 + (32 * j + l) * 16 : OOB;
            va[11] = nearo(H2_OFF + c * 4096) + l * 16;
            va[12] = (l & 1) ? OOB : nearo(H3_OFF + (2 * (g >> 1) + (l15 >> 3)) * 16384 + c3) + ((l7 & 6) + (g & 1)) * 16;
#pragma unroll
            for (int i = 0; i < 4; ++i) sel[i] = (l7 == 2 * i ? 0x3f80u : 0u) | (l7 == 2 * i + 1 ? 0x3f800000u : 0u);
            sC = v4u{0, 0, 0, 0};
        } else {
            const rsrc_t rk = mkrsrc(a->qkv + QSZ, QSZ), rv = mkrsrc(a->qkv + 2 * (size_t)QSZ, QSZ);
            wait_flags(mkrsrc(a->ctl, CTL_SZ), fw(l & 1, h) + 12, 1, a->ctl);
            v4u pf[10];
            const unsigned hb0 = h * 16384 + (KVS(a->ms >> 4, h) ^ sB[4] ^ 8192), kx = j * 2048 + (KVS(a->ms >> 4, h) ^ KVS(a->ms >> 2, h));
#pragma unroll
            for (int i = 0; i < 8; ++i) pf[i] = LDB(rv, i, hb0);
            pf[8] = LDB(rk, 0, hb0 ^ kx); pf[9] = LDB(rk, 1, hb0 ^ kx);
            va[13] = LD4(mkrsrc(a->ctl, CTL_SZ), fw(l & 1, h) + 12, 0);
#pragma unroll
            for (int i = 0; i < 8; ++i) *(LAS v4u*)(lds + L_X + i * X_BLK + g * X_FQ + l15 * 16) = pf[i];
            *(LAS v4u*)(lds + L_K + l * 16) = pf[8]; *(LAS v4u*)(lds + L_K + 1024 + l * 16) = pf[9];
            FENCE(); ((volatile LAS unsigned*)(lds + L_MISC))[2] = 1;
            va[6] = l * 16; va[7] = lb + g * X_FQ + l15 * 16;
            va[8] = nearo(H2_OFF + c * 4096) + l * 16; va[9] = h1j + l * 16;
            va[10] = nearo(H3_OFF + (l >> 3) * 16384 + c3) + l7 * 16;
            va[11] = fw(l & 1, h) + 12;
            va[15] = l == 0 && (c & 31) == 0 ? F_CK + 384 + (c >> 5) * 4 : OOB;
            sC = v4u{hb0, __float_as_uint(a->per), 0, kx};
        }
        while (((volatile LAS unsigned*)(lds + L_MISC))[2] == 0) __builtin_amdgcn_s_sleep(1);
        asm volatile(ASM_B : S_IO, "+{v[224:239]}"(va), "+{v[244:247]}"(sel) : "{v[0:31]}"(wo0), "{v[32:63]}"(wo1) : B_CLOB);
        FIN();
    }
}

#define RING_LD "buffer_load_dwordx4 %5, %10, %11, %12 offen offset:%13 sc1\n\t"
#define RING_M(C, W, Z) "v_mfma_f32_16x16x32_bf16 %" #C ", %" #W ", %4, " Z "\n\t"
#define RING_W(C, O) "ds_write_b128 %14, %" #C " offset:" #O "\n\t"
#define RING_OP(K, T) asm volatile(T : "+v"(c[0]), "+v"(c[1]), "+v"(c[2]), "+v"(c[3]), "+v"(ring[(K) & 15]), "+v"(ring[((K) + 15) & 15]) \
    : "v"(wo[(K) & 7][0]), "v"(wo[(K) & 7][1]), "v"(wo[(K) & 7][2]), "v"(wo[(K) & 7][3]), "v"(l16), "s"(rso), "s"(nb[(((K) + 15) >> 2) & 3]), "n"((((K) + 15) & 3) * 1024), "v"(rb) : "memory")
#define RING_1(K, B, O) "buffer_load_dwordx4 %" #K ", %16, %17, %" #B " offen offset:" #O " sc1\n\t"
#define RING_4(K0, K1, K2, K3, B) RING_1(K0, B, 0) RING_1(K1, B, 1024) RING_1(K2, B, 2048) RING_1(K3, B, 3072)
#define RING_ALL(P) P(ring[0]), P(ring[1]), P(ring[2]), P(ring[3]), P(ring[4]), P(ring[5]), P(ring[6]), P(ring[7]), P(ring[8]), P(ring[9]), P(ring[10]), P(ring[11]), P(ring[12]), P(ring[13]), P(ring[14]), P(ring[15])
#define NPROBE 4
static constexpr int RED_B = 65536;
#define OS(k) do {} while (0)

DEV void load_wo(LAS char* lds, v4u rw, unsigned o, int w, int l, v4u (&wo)[8][4]) {
    const unsigned vo = (l >> 3) * (DM * 2) + (((l & 7) ^ ((l >> 3) & 6)) << 4), lb = (unsigned)(size_t)lds + w * 16384;
    auto issue = [&](int p) {
#pragma unroll
        for (int k = 0; k < 8; ++k) DMA16N(rw, vo, o + (64 * p + 8 * k) * (DM * 2), lb + (p & 1) * 8192 + k * 1024); };
    issue(0); issue(1);
    const int row = l >> 2;
#pragma unroll
    for (int p = 0; p < 4; ++p) {
        if (p < 3) WAITV(8); else WAITV(0);
#pragma unroll
        for (int t = 0; t < 4; ++t) {
            const LAS char* q = lds + w * 16384 + (p & 1) * 8192 + row * 128 + (((2 * t + ((l >> 1) & 1)) ^ (row & 6)) << 4) + (l & 1) * 8;
            wo[2 * p][t] = cat(trd(q), trd(q + 2048)); wo[2 * p + 1][t] = cat(trd(q + 4096), trd(q + 6144));
        }
        WAITL(0);
        if (p < 2) issue(p + 2);
    }
}

DEV void worker(LAS char* lds, const int wk) {
    KArgs a = args();
    const int tid = threadIdx.x, w = __builtin_amdgcn_readfirstlane(tid >> 6), l = tid & 63, fr = l & 15, fq = l >> 4;
    const int g = wk >> 5, i = wk & 31;
    const rsrc_t rc = mkrsrc(a->ctl, CTL_SZ);
    unsigned* err = a->ctl;
    unsigned* dbg = a->dbg + 32768 + wk * 32;
    WSTAMP(0);
    const v4u rx = mkrs(a->x, S_TOK * DM * 2);
    const int role = i >> 3;
    const int h = 8 * g + (i & 7);
    const unsigned me = F_W + wk * 128;
    unsigned far = 0;
    v4u rsc = mkrs(a->ctl, CTL_SZ);
    if (role >= 2) {
        const unsigned t0 = rt32();
        const rsrc_t rr = mkrsrc(a->ring, NSLOT * SLOT);
        unsigned b0, ak = 1;
        BC_LD(b0, rsc);
        if (role == 2) {
#pragma unroll
            for (int k = 0; k < 14; ++k) ST16(rr, (h >> 4) * (unsigned)SLOT + ((h & 15) * 14 + k) * 8192 + tid * 16, 0, (v4u{POI, POI, POI, POI}));
        } else if (wk == 127) {
#pragma unroll
            for (int k = 0; k < 8; ++k) ST16(rc, k == 0 && (tid >> 3) == F_BC / 128 ? OOB : k * 8192 + tid * 16, 0, (v4u{0, 0, 0, 0}));
        } else if (i < 28 && w == 0) {
            unsigned t[2] = {~0u, ~0u};
            for (int it = 0; it < NPROBE; ++it) {
#pragma unroll
                for (int p = 0; p < 2; ++p) { const unsigned c0 = cyc32(); ST4(rr, (i & 3) * (unsigned)SLOT + p * 8192 + nearo(PR_OFF) + g * 256 + (l & 31) * 4, 0, c0); WAITV(0); t[p] = min(t[p], cyc32() - c0); }
            }
            ak = 256 + min(max((int)(t[1] - t[0]), -100), 100);
        }
        if (i >= 28 && i < 31 && (a->pf & 2)) {
            const unsigned u = (i & 3) * 8 + w, ro = (320 + (u >> 1) * 16 + (u & 1) * 512 + (l >> 2)) * (HD * 2) + g * 2048 + (l & 1) * 128 + (u < (a->pf >> 4) ? OOB : 0);
#pragma unroll
            for (int m = 0; m < 2; ++m) {
                const v4u rp = mkrs(m ? a->Wv : a->Wk, DM * HD * 2);
                const unsigned ps = ((rp[0] >> 7) + g * 16) & 63;
                asm volatile("s_mov_b32 m0, %0\n\ts_nop 0\n\tbuffer_load_dword %1, %3, 0 offen lds\n\tbuffer_load_dword %2, %3, 0 offen lds"
                             :: "s"((unsigned)(size_t)lds), "v"(ro + ((3 - ps) & 6) * 128 + (l & 2) * 512), "v"(63 - ps < 16 && !(l & 2) ? ro + ((63 - ps) & 14) * 128 : OOB), "s"(rp) : "memory", "m0");
            }
        }
        asm volatile("s_waitcnt vmcnt(0)\n\ts_barrier" : "+v"(b0) :: "memory");
        WSTAMP(1);
        if (w == 0) {
            if (wk != 127) beacon(rsc, b0, b0, 0);
            ST4(rc, l ? OOB : (wk == 127 ? F_BC : me), 0, ak);
            if (wk == 127) WAITV(0);
        }
        RBAR();
        const unsigned d = role == 2 ? a->wq : (wk == 127 ? 0 : a->wx);
        while (rt32() - t0 < d) __builtin_amdgcn_s_sleep(8);
#pragma unroll 1
        for (int k = role == 3 ? (wk == 127 && w < 2 ? 2 * w : 1) : 0; k > 0; --k) prep_rows(a->x, a->gw, a->w_r, rc, h * 64 + (w + 1 - k) * 8, l);
        if (w == 0 && wk == 127) {
            unsigned bc = 0, tl = rt32();
            for (unsigned tries = 0; tries < SPIN_CAP; ++tries) {
                FENCE();
                if (!(bc & 16)) {
                    unsigned v = LD4(rc, F_W + ((l >> 4) * 32 + 16 + fr) * 128, 0);
                    if (__builtin_amdgcn_ballot_w64(v == 0 && fr < 12) == 0) {
                        v += __shfl_xor((int)v, 16); v += __shfl_xor((int)v, 32);
                        const int d = (int)v - 1024;
                        int e = d - __shfl_xor(d, 1); e += __shfl_xor(e, 2);
                        bc |= 16 | ((unsigned)(__builtin_amdgcn_ballot_w64((abs(d) > 350 ? d : e) > 0) >> 8) & 15);
                        tl -= 4096;
                    }
                } else if (rt32() - t0 > 2400 && all_ge(LD4(rc, fr == 15 ? F_PG : F_W + ((fr >> 2) * 32 + 28 + (fr & 3)) * 128, 0), 1)) break;
                if (rt32() - tl > (bc & 16 ? 500 : 150)) { bc += 32; ST4(rc, l ? OOB : F_BC, 0, bc); tl = rt32(); }
                __builtin_amdgcn_s_sleep(8);
            }
            WSTAMP(5);
        }
        if (role == 3 && h >= NMOE) while (rt32() - t0 < 2600) __builtin_amdgcn_s_sleep(64);
    }
    if (role == 1) far = stream_qkv<true>(lds, rx, mkrs(a->Wv, DM * HD * 2), mkrsrc(a->qkv + 2 * (size_t)QSZ, QSZ), rc, rsc, true, me + 12, h, KVS(a->ms >> 4, h), a->pf, dbg);
    else if (role < 3) far = stream_qkv<false>(lds, rx, mkrs(role ? a->Wq : a->Wk, DM * HD * 2), mkrsrc(a->qkv + (role ? 0 : QSZ), QSZ), rc, rsc, role == 0, me + 12, h, role ? 0 : KVS(a->ms >> 2, h), a->pf, dbg);
    else if (h < NMOE) {
        const int sel = top8((LAS unsigned*)(lds + L_TOT - 256), rc, err, w, l);
        WSTAMP(26);
        if (h == 0 && tid < TOPC) ST4(mkrsrc(a->idx, 32), tid * 4, 0, sel);
        moe(lds, rx, sel, mkrs(a->wg, DM * FF * 2), mkrs(a->wu, DM * FF * 2), mkrs(a->wd, FF * DM * 2), mkrsrc(a->part, NMOE * TOPC * DM * 4), h);
        if (tid == 0) ST4(rc, me + 4, 0, 1u);
    }
    if (role == 3) far ^= FAR(beacon(rsc, 0, 0, 16));
    WSTAMP(2);
    a = args();
    const rsrc_t ri = mkrsrc(a->idx, 32), ru = mkrsrc(a->upd, TOPC * DM * 4), rt = mkrsrc(a->wt, HD * DM * 2);
    const int cg = role == 3 ? 4 * g + (i & 3) : (role == 2 ? h & 15 : i);
    const unsigned wb = side((cg * 64 + 8 * w) * 8192 + far);
    v4u wo[8][4];
    if (role == 3 && i < 28) {
        const rsrc_t rq = mkrsrc(a->qkv, QSZ), rk = mkrsrc(a->qkv + QSZ, QSZ), rv = mkrsrc(a->qkv + 2 * (size_t)QSZ, QSZ), rm = mkrsrc(a->mb, NSEG * NH * 4096);
        unsigned fl = 0;
#pragma unroll 1
        for (int xs = i & 3; xs < NSEG; xs += 4) {
            if ((xs & 60) == a->wi) {
                int l = tid & 63; asm volatile("" : "+v"(l));
                load_wo(lds, mkrs(a->Wo, HD * DM * 2), (256 * w * DM + 64 * cg) * 2, w, l, wo);
#pragma unroll
                for (int k = 0; k < 32; ++k) ST16(rt, wb + l * 16 + (k >> 3) * 16384 + (k & 7) * 1024, 0, wo[k >> 2][k & 3]);
            }
            fl = xattn(rq, rk, rv, rm, rc, err, xs, 8 * g + w, l, fl, far, a->ms, dbg); WSTAMP(9 + (xs >> 2));
        }
        WSTAMP(25);
        return;
    }
    const bool rcv = role == 2 || (role < 2 && g);
    const int k0 = (rcv ? 0 : 64) + 8 * w;
    const u64 mine = a->segs[role < 2 ? g : (role == 2 ? 4 + (g >> 1) : 6)];
    auto next = [&](int s) { return s < NSEG - 1 && mine >> (s + 1) ? s + 1 + __builtin_ctzll(mine >> (s + 1)) : NSEG; };
    auto fx = [&](int s, int l) { return fw(3, (rcv ? 0 : 16) + (l & 1) * 8 + (s & 3)) + 64 + (s & 60); };
    const unsigned fe = F_EX + cg * 256;
    volatile LAS unsigned* prog = (volatile LAS unsigned*)(lds + 2 * RED_B);
    LAS v4f* upd = (LAS v4f*)(lds + 2 * RED_B + 16384);
    u64 hit = 0;
    int sel = 0;
    if (rcv) {
        wait_flags(rc, fw(3, 2 * (cg & 12) + (cg & 3)) + (l & 1 ? 64 + a->wi : 512 + 8), 1, err);
        sel = LD4(ri, (l & 7) * 4, 0);
        const v4u u = LD16(ru, w < 2 ? (tid >> 4) * 4096 + cg * 256 + fr * 16 : OOB, 0);
#pragma unroll
        for (int k = 0; k < 32; ++k) wo[k >> 2][k & 3] = LDB(rt, k & 7, wb + (k >> 3) * 16384);
        if (w < 2) upd[tid] = __builtin_bit_cast(v4f, u);
#pragma unroll
        for (int q = 0; q < 8; ++q) hit |= 1ull << (__builtin_amdgcn_readlane(sel, q) >> 5);
    } else load_wo(lds, mkrs(a->Wo, HD * DM * 2), (32 * k0 * DM + 64 * cg) * 2, w, l, wo);
    WSTAMP(7);
    if (role == 3) {
        wait_flags<16>(rc, fw(3, min(l, NMOE - 1)) + 4, 1, err);
        const rsrc_t rp = mkrsrc(a->part, NMOE * TOPC * DM * 4);
        const unsigned t = LD4(ri, (tid & 112) >> 2, 0), o = (tid & 112) * 256 + cg * 256 + fr * 16;
        v4f p = {0, 0, 0, 0};
#pragma unroll
        for (int q = 0; q < 7; ++q) p += __builtin_bit_cast(v4f, LD16(rp, o, ((w >> 1) * 7 + q) * 32768));
        const float r = rbf(sigmoidf_(hi16(LD4(rc, F_GR + t * 8 + 4, 0))));
        upd[tid] = p;
        BAR();
        p += upd[tid ^ 128]; p += upd[tid ^ 256] + upd[tid ^ 384];
        ST16(ru, w < 2 ? o : OOB, 0, __builtin_bit_cast(v4u, (v4f{rbf(rbf(p[0]) * r), rbf(rbf(p[1]) * r), rbf(rbf(p[2]) * r), rbf(rbf(p[3]) * r)})));
        WAITV(0); RBAR();
        if (tid == 0) ST4(rc, me + 8, 0, 1u);
    }
    __builtin_amdgcn_s_waitcnt(0x0f70);
    bool pend = false;
    v4u ring[16];
    asm volatile("" : RING_ALL("=v"));
    const v4u rso = mkrs(a->qkv, QSZ), rse = mkrs(a->qkv + QSZ, QSZ);
    rsc = mkrs(a->ctl, CTL_SZ);
    const rsrc_t re = mkrsrc(a->qkv + QSZ, QSZ), rout = mkrsrc(a->out, S_TOK * DM * 2);
    BAR();
    const unsigned l16 = l * 16, lw = (unsigned)(size_t)lds + w * 1024, r0 = (w >> 2) * 16 + fr;
    const unsigned xo = rcv ? (r0 * DM + 64 * cg + (w & 3) * 16 + 4 * fq) * 2 : OOB, go = F_GR + r0 * 8, so = w ? OOB : (l < 2 ? fx(0, l) : (rcv ? fe : OOB));
    auto state = [&](int n) { return (unsigned)__builtin_amdgcn_ballot_w64(prog[(n & 1) * 2048 + l * 4] != 0); };
    auto exo = [&](int s) { return side((unsigned)(s * NH + 16 + cg) * 16384u + far) + w * 1024; };
    auto ask = [&](int o, int e) { unsigned u = o < NSEG ? (o & 3) * 128 + (o & 60) : OOB, v = e * 4; asm("" : "+s"(u), "+s"(v)); return so + (l16 < 32 ? u : v); };
    unsigned st;
    WSTAMP(8);
    v4f r[6];
    const LAS char* rp;
    auto get = [&](int n) {
        rp = lds + ((n & 1) * RED_B + w * 8192 + l16);
#pragma unroll
        for (int q = 0; q < 6; ++q) r[q] = *(const LAS v4f*)(rp + q * 1024); };
    auto fin = [&](int seg, bool got, v4f pv, v2u xv, float gt) {
        v4f acc = add4(r[0], r[1]); SCHED();
        r[0] = *(const LAS v4f*)(rp + 6144); r[1] = *(const LAS v4f*)(rp + 7168);
        acc = add4(add4(acc, add4(r[2], r[3])), add4(add4(r[4], r[5]), add4(r[0], r[1])));
        if (rcv) {
            if (!got) { wait_flags(rc, fe + seg * 4, 1, err); pv = __builtin_bit_cast(v4f, LD16(re, l16, exo(seg))); asm volatile("" : "+v"(pv)); }
            v4f b = {lo16(xv[0]), hi16(xv[0]), lo16(xv[1]), hi16(xv[1])};
            if ((hit >> seg) & 1) {
#pragma unroll 1
                for (int q = 0; q < TOPC; ++q) if (__builtin_amdgcn_readlane(sel, q) == seg * 32 + (int)(go - F_GR) / 8) {
                    b += upd[q * 16 + (w & 3) * 4 + (l16 >> 8)]; b = v4f{rbf(b[0]), rbf(b[1]), rbf(b[2]), rbf(b[3])}; }
            }
            acc = add4(acc, pv);
            gt = hi16(__float_as_uint(gt));
            const float omg = rbf(1.0f - gt);
#pragma unroll
            for (int e = 0; e < 4; ++e) acc[e] = add1(rbf(gt * rbf(acc[e])), rbf(omg * b[e]));
            __builtin_amdgcn_raw_buffer_store_b64(v2u{pk2(acc[0], acc[1]), pk2(acc[2], acc[3])}, rout, xo, seg * (32 * DM * 2), 0);
        } else ST16(re, l16, exo(seg), __builtin_bit_cast(v4u, acc));
        FENCE(); };
    unsigned sm; v2u xv; float gt; v4f pv;
    asm volatile("" : "=v"(sm), "=v"(pv), "=v"(xv), "=v"(gt));
    bool got = false;
#define SM_LD "buffer_load_dword %[sm], %[so], %[rc], 0 offen sc1\n\tbuffer_load_dwordx4 %[pv], %[l16], %[re], %[eo] offen sc1\n\tbuffer_load_dwordx2 %[xv], %[xo], %[rx], %[xs] offen\n\tbuffer_load_dword %[gt], %[go], %[rc], %[gs] offen sc1\n\t"
#define SM_O [sm] "+v"(sm), [pv] "+v"(pv), [xv] "+v"(xv), [gt] "+v"(gt)
#define SM_I(QO, QE) [rc] "s"(rsc), [re] "s"(rse), [rx] "s"(rx), [so] "v"(ask(QO, QE)), \
    [l16] "v"(l16), [eo] "s"(got ? exo(seg) : OOB), [xo] "v"(xo), [xs] "s"(seg * (32 * DM * 2)), [go] "v"(go), [gs] "s"(seg * 256)
#pragma unroll 1
    for (int n = 0, seg = next(-1), s1 = next(seg), s2 = next(s1), s3, p1 = OOB / 4, p2 = p1, p3 = p1; seg < NSEG; ++n, p3 = p2, p2 = p1, p1 = seg, seg = s1, s1 = s2, s2 = s3) {
        s3 = next(s2);
        if (!pend) {
            const unsigned ob = side((unsigned)(seg * 128 + k0) * 4096u + far), fo = l16 < 64 ? ask(s1, seg) : fx(seg, l16 >> 4);
            unsigned v;
            for (unsigned tries = 0;; ++tries) {
                FENCE(); v = LD4(rc, fo, 0);
                if ((__builtin_amdgcn_ballot_w64(v != 0) & 48) == 48) break;
                __builtin_amdgcn_s_sleep(6);
                if (spin_fail(tries, err)) break;
            }
            asm volatile(RING_4(0, 1, 2, 3, 18) RING_4(4, 5, 6, 7, 19) RING_4(8, 9, 10, 11, 20) RING_4(12, 13, 14, 15, 21)
                         : RING_ALL("+v") : "v"(l16), "s"(rso), "s"(ob), "s"(ob ^ 16384), "s"(ob ^ 4096), "s"(ob ^ 20480) : "memory");
            prog[(~n & 1) * 2048 + tid * 4] = v;
            BAR();
            st = state(n + 1); got = st & 4;
            asm volatile(SM_LD : SM_O : SM_I(s2, seg) : "memory");
            ST4(rc, OOB, 0, 0u);
        }
        const bool pre = (st & 3) == 3;
        unsigned nb[4], rb = lw + (n & 1) * RED_B + l16, pa = lw + 2 * RED_B + (n & 1) * 8192 + l16;
        nb[0] = pre ? side((unsigned)(s1 * 128 + k0) * 4096u + far) : OOB; nb[1] = nb[0] ^ 16384; nb[2] = nb[0] ^ 4096; nb[3] = nb[0] ^ 20480;
        v4f c[4];
        asm volatile("" : "+s"(nb[0]), "+s"(nb[1]), "+s"(nb[2]), "+s"(nb[3])); asm volatile("" : "+v"(rb), "+v"(pa), "=v"(c[0]), "=v"(c[1]), "=v"(c[2]), "=v"(c[3]));
        OS(0);
        if (pend || (pre && w >= 4)) BAR();
        OS(1);
        RING_OP(0, "s_setprio 0\n\ts_waitcnt vmcnt(20)\n\t" RING_M(0, 6, "0") RING_M(1, 7, "0") "s_waitcnt vmcnt(19)\n\t" RING_M(2, 8, "0") RING_M(3, 9, "0"));
#pragma unroll
        for (int k = 1; k < 16; ++k) {
            if (k == 8) RING_OP(k, RING_LD "s_nop 3\n\t" RING_W(0, 0) RING_M(0, 6, "0") RING_W(1, 8192) RING_M(1, 7, "0") RING_W(2, 16384) RING_M(2, 8, "0") RING_W(3, 24576) RING_M(3, 9, "0") "s_waitcnt vmcnt(19)");
            else if (k == 15) RING_OP(k, RING_M(0, 6, "%0") RING_LD RING_M(1, 7, "%1") RING_M(2, 8, "%2") RING_M(3, 9, "%3"));
            else RING_OP(k, RING_M(0, 6, "%0") RING_LD RING_M(1, 7, "%1") "s_waitcnt vmcnt(19)\n\t" RING_M(2, 8, "%2") RING_M(3, 9, "%3"));
            if (k == 13) asm volatile("s_waitcnt vmcnt(14)\n\tds_write_b32 %4, %0" : SM_O : "v"(pa) : "memory");
        }
        RING_OP(16, RING_LD "s_setprio 1");
        OS(2);
        if (pre || pend) BAR();
        OS(3);
        pa -= lw;
        asm volatile("ds_read_b32 %0, %0\n\ts_nop 3\n\tds_write_b128 %5, %1 offset:32768\n\tds_write_b128 %5, %2 offset:40960\n\tds_write_b128 %5, %3 offset:49152\n\tds_write_b128 %5, %4 offset:57344\n\ts_waitcnt lgkmcnt(4)"
                     : "+v"(pa) : "v"(c[0]), "v"(c[1]), "v"(c[2]), "v"(c[3]), "v"(rb) : "memory");
        OS(4);
        if (pre || pend) {
            get(n - 1);
            const bool got1 = got;
            st = __builtin_amdgcn_ballot_w64(pa != 0); got = st & 4;
            v4f pv1 = pv; v2u xv1 = xv; float gt1 = gt;
            asm volatile(SM_LD : SM_O, "+v"(pv1), "+v"(xv1), "+v"(gt1) : SM_I(s3, s1) : "memory");
            OS(5);
            if (pend) fin(p1, got1, pv1, xv1, gt1); else ST4(rc, OOB, 0, 0u);
            if (!rcv && tid == 256) ST4(rc, fe + p3 * 4, 0, 1u);
        }
        OS(6);
        if (!pre) {
            BAR();
            if (pend && w < 4) RBAR();
            get(n);
            asm volatile("s_waitcnt vmcnt(1)" : SM_O :: "memory");
            fin(seg, got, pv, xv, gt);
            if (!rcv) { WAITV(0); RBAR(); if (tid < 3) ST4(rc, fe + (tid ? (tid == 1 ? p1 : p2) : seg) * 4, 0, 1u); }
        }
        pend = pre;
        WSTAMP(9 + (seg >> 2));
    }
    asm volatile("s_waitcnt vmcnt(0)" :: RING_ALL("v") : "memory");
    WSTAMP(25);
}

__global__ void __launch_bounds__(512, 2) k_main(Args) {
    __shared__ __attribute__((aligned(16))) char lds[L_TOT];
    const int b = blockIdx.x, c = (b & 3) * 32 + (b >> 3);
    if ((b & 7) < 4) chain((LAS char*)lds, c); else worker((LAS char*)lds, c);
}

#define RING_MB 160
#define QKV_MB 0
#define TUNE_DEFAULT {184, 73, 77, 138, 8, 80, 336, 100, 900, 700, 7, 20, 0, 9, 38, 20}

extern "C" void kernel_launch(void* const* d_in, const int* in_sizes, int n_in, void* d_out, int out_size, void* d_ws, size_t ws_size, hipStream_t stream) {
    char* ring = (char*)(((size_t)d_ws + SLOT - 1) & ~(size_t)(SLOT - 1));
    char* ws = ring + NSLOT * SLOT; size_t off = 0;
    if (ws_size >= (256u << 20)) { ring += (size_t)RING_MB << 20; off = (size_t)QKV_MB << 20; }
    auto alloc = [&](size_t bytes) { void* p = ws + off; off += (bytes + 255) & ~(size_t)255; return p; };
    Args a;
    a.x = (const bf16_t*)d_in[0]; a.mem0 = (const bf16_t*)d_in[1]; a.Wq = (const bf16_t*)d_in[2]; a.Wk = (const bf16_t*)d_in[3]; a.Wv = (const bf16_t*)d_in[4]; a.Wo = (const bf16_t*)d_in[5];
    a.w_r = (const bf16_t*)d_in[8]; a.wg = (const bf16_t*)d_in[9]; a.wu = (const bf16_t*)d_in[10]; a.wd = (const bf16_t*)d_in[11]; a.gw = (const bf16_t*)d_in[12];
    a.qkv = (char*)alloc(3 * (size_t)QSZ); a.wt = (char*)alloc(HD * DM * 2); a.mb = (char*)alloc(NSEG * NH * 4096);
    a.idx = (int*)alloc(256); a.part = (float*)alloc(NMOE * TOPC * DM * 4); a.upd = (float*)alloc(TOPC * DM * 4); a.out = (bf16_t*)d_out;
    a.ring = ring; a.ctl = (unsigned*)alloc(CTL_SZ); a.dbg = (unsigned*)alloc((65536 + 1024 * 512) * 4);
    const int tn[16] = TUNE_DEFAULT;
    a.per = tn[0] * 368.0f; a.per0 = tn[0] * 368 / tn[6]; a.pf = tn[10]; a.ms = tn[11]; a.ws = tn[12]; a.wi = tn[15]; a.gap = 0;
    a.d1 = ((u64)tn[1] << 32) / tn[0]; a.dv = ((u64)tn[2] << 32) / tn[0]; a.d2 = ((u64)tn[3] << 32) / tn[0]; a.dp = tn[4]; a.slack = tn[5]; a.thr = tn[6]; a.spare = tn[7]; a.wq = tn[8]; a.wx = tn[9];
    for (int c = 0; c < 7; ++c) a.segs[c] = 0;
    for (int s = 0; s < NSEG; ++s) {
        a.segs[1 + (s < tn[13] ? s % 3 : (s - tn[13]) % 5)] |= 1ull << s;
        a.segs[s >= tn[14] && !((s - tn[14]) & 1) ? 0 : 6] |= 1ull << s;
    }
    k_main<<<256, 512, 0, stream>>>(a);
}
```

```cpp
#include <hip/hip_runtime.h>
#include <stdint.h>

typedef unsigned short bf16_t;
typedef unsigned v2u __attribute__((ext_vector_type(2)));
typedef unsigned v4u __attribute__((ext_vector_type(4)));
typedef unsigned v8u __attribute__((ext_vector_type(8)));
typedef float v2f __attribute__((ext_vector_type(2)));
typedef float v4f __attribute__((ext_vector_type(4)));
typedef short v4s __attribute__((ext_vector_type(4)));
typedef __bf16 v2b __attribute__((ext_vector_type(2)));
typedef __bf16 v8b __attribute__((ext_vector_type(8)));
typedef __bf16 v16b __attribute__((ext_vector_type(16)));
typedef unsigned long long u64;
typedef __amdgpu_buffer_rsrc_t rsrc_t;
#define LAS __attribute__((address_space(3)))
#define DEV __device__ __forceinline__

static constexpr int S_TOK = 2048, DM = 1024, NH = 32, DH = 128, SEG = 32, NSEG = 64;
static constexpr int HD = NH * DH;
static constexpr int FF = 3584, TOPC = 8;
static constexpr float SCALE = 0.08838834764831845f, LOG2E = 1.4426950408889634f, MASKV = -998244352.0f;

DEV unsigned pk2(float lo, float hi) { v2f f = {lo, hi}; return __builtin_bit_cast(unsigned, __builtin_convertvector(f, v2b)); }
DEV float lo16(unsigned u) { return __uint_as_float(u << 16); }
DEV float hi16(unsigned u) { return __uint_as_float(u & 0xffff0000u); }
DEV float rbf(float f) { return __uint_as_float(pk2(0.0f, f)); }
DEV v4f smf(v4u a, v8u b, v4f c, int idx) { return __builtin_amdgcn_smfmac_f32_16x16x64_bf16(__builtin_bit_cast(v8b, a), __builtin_bit_cast(v16b, b), c, idx, 0, 0); }
DEV v4f mf(v4u a, v4u b, v4f c) { return __builtin_amdgcn_mfma_f32_16x16x32_bf16(__builtin_bit_cast(v8b, a), __builtin_bit_cast(v8b, b), c, 0, 0, 0); }
DEV v2u trd(const LAS char* p) { return __builtin_bit_cast(v2u, __builtin_amdgcn_ds_read_tr16_b64_v4i16((LAS v4s*)p)); }
DEV v4u cat(v2u a, v2u b) { return __builtin_shufflevector(a, b, 0, 1, 2, 3); }
DEV v2u sw16(unsigned x, unsigned y) { return __builtin_amdgcn_permlane16_swap(x, y, false, false); }
DEV v2u sw32(unsigned x, unsigned y) { return __builtin_amdgcn_permlane32_swap(x, y, false, false); }
DEV float half_add(float x, float y) { v2u r = sw32(__float_as_uint(x), __float_as_uint(y)); return __uint_as_float(r[0]) + __uint_as_float(r[1]); }
DEV float ror8(float v) { return __int_as_float(__builtin_amdgcn_update_dpp(0, __float_as_int(v), 0x128, 0xf, 0xf, false)); }
template <bool MAX> DEV float red_g(float v) {
    auto op = [](float a, float b) { return MAX ? fmaxf(a, b) : a + b; };
    v2u r = sw16(__float_as_uint(v), __float_as_uint(v)); v = op(__uint_as_float(r[0]), __uint_as_float(r[1]));
    r = sw32(__float_as_uint(v), __float_as_uint(v)); return op(__uint_as_float(r[0]), __uint_as_float(r[1]));
}
DEV float add1(float a, float b) { float r; asm("v_add_f32 %0, %1, %2" : "=v"(r) : "v"(a), "v"(b)); return r; }
DEV float mul1(float a, float b) { float r; asm("v_mul_f32 %0, %1, %2" : "=v"(r) : "v"(a), "v"(b)); return r; }
DEV v4f add4(v4f a, v4f b) { return v4f{add1(a[0], b[0]), add1(a[1], b[1]), add1(a[2], b[2]), add1(a[3], b[3])}; }
DEV int lane() { int l; asm volatile("v_mbcnt_lo_u32_b32 %0, -1, 0\n\tv_mbcnt_hi_u32_b32 %0, -1, %0" : "=v"(l)); return l; }
DEV float sigmoidf_(float z) { return 1.0f / (1.0f + __expf(-z)); }
DEV v4u mkrs(const void* p, unsigned bytes) { const u64 a = (u64)p; return v4u{(unsigned)a, (unsigned)(a >> 32) & 0xffffu, bytes, 0x00020000u}; }
DEV rsrc_t mkrsrc(const void* p, unsigned bytes) { return __builtin_amdgcn_make_buffer_rsrc((void*)p, (short)0, (int)bytes, 0x00020000); }

#define ST16(RS, OFF, SOFF, V) __builtin_amdgcn_raw_buffer_store_b128(V, RS, (OFF) + (SOFF), 0, 16)
#define NTS 0
#define ST16X(RS, OFF, V, NT) __builtin_amdgcn_raw_buffer_store_b128(V, RS, OFF, 0, (NT) ? 18 : 16)
#define LD16X(RS, OFF, SOFF, NT) __builtin_amdgcn_raw_buffer_load_b128(RS, OFF, SOFF, (NT) ? 18 : 16)
#define ST4(RS, OFF, SOFF, V) __builtin_amdgcn_raw_buffer_store_b32(V, RS, OFF, SOFF, 16)
#define LD16(RS, OFF, SOFF) __builtin_amdgcn_raw_buffer_load_b128(RS, OFF, SOFF, 16)
#define LD8(RS, OFF, SOFF) __builtin_amdgcn_raw_buffer_load_b64(RS, OFF, SOFF, 16)
#define LD4(RS, OFF, SOFF) __builtin_amdgcn_raw_buffer_load_b32(RS, OFF, SOFF, 16)
#define DMA16(RS, VOFF, SOFF, LOFF) asm volatile("s_mov_b32 m0, %0\n\ts_nop 0\n\tbuffer_load_dwordx4 %1, %2, %3 offen lds" :: "s"(LOFF), "v"(VOFF), "s"(RS), "s"(SOFF) : "memory", "m0")
#define DMA16N(RS, VOFF, SOFF, LOFF) asm volatile("s_mov_b32 m0, %0\n\ts_nop 0\n\tbuffer_load_dwordx4 %1, %2, %3 offen nt lds" :: "s"(LOFF), "v"(VOFF), "s"(RS), "s"(SOFF) : "memory", "m0")
#define WSTAMP(k) do {} while (0)
#define TS(c, s, k) do {} while (0)
#define NPIPE 8
#define PIPE(k) ((k) < NPIPE)
#define XB 28
#define WSIDE 8192
#define EXP 0
#define XI 3
#define BK 9
#define FENCE() asm volatile("" ::: "memory")
#define WAITV(n) asm volatile("s_waitcnt vmcnt(" #n ")" ::: "memory")
#define WAITL(n) asm volatile("s_waitcnt lgkmcnt(" #n ")" ::: "memory")
#define RBAR() asm volatile("s_barrier" ::: "memory")
#define BAR() asm volatile("s_waitcnt lgkmcnt(0)\n\ts_barrier" ::: "memory")
#define SCHED() __builtin_amdgcn_sched_barrier(0)

#define KVS(m, h) ((((m) & 2 ? (h) : (m)) & 1u) << 13)
#define FAR(u) ((~(u) & 1u) << 13)
DEV unsigned side(unsigned o) { return o ^ ((o >> 8) & 8192u); }
static constexpr int XP = 1056, XS = 32 * XP, L_RED = 4 * XS;

DEV unsigned wt_src(int tid, unsigned ldw) { return (tid >> 4) * ldw + (((tid & 15) ^ ((((tid >> 4) & 3) << 2) | (((tid >> 7) & 1) << 1))) << 4); }
DEV int wt_rd(int w, int l, int n) { const int fr = l & 15, fq = l >> 4; return (w >> 2) * 16384 + (8 * fq + (fr >> 2)) * 256 + (((4 * (w & 3) + 2 * n + ((fr >> 1) & 1)) ^ (((fr >> 2) << 2) | ((fq & 1) << 1))) << 4) + (fr & 1) * 8; }
DEV v4u wt_frag(const LAS char* p) { return cat(trd(p), trd(p + 1024)); }
#define WT_ISSUE(R0, R1, VB, S0, S1, LDW, LB) do { DMA16N(R0, VB, S0, LB); DMA16N(R0, VB, (S0) + 32 * (LDW), (LB) + 8192); DMA16N(R1, VB, S1, (LB) + 16384); DMA16N(R1, VB, (S1) + 32 * (LDW), (LB) + 24576); } while (0)

static constexpr unsigned F_BC = 512, F_CK = 2048, F_EX = 12288, F_W = 16384, F_PG = 32768, F_GR = 49152, CTL_SZ = 65536;
DEV unsigned fw(int role, int h) { return F_W + ((h >> 3) * 32 + role * 8 + (h & 7)) * 128; }
static constexpr unsigned POI = 0xFFFFFFFFu, OOB = 0x40000000u, SPIN_CAP = 200000u;
DEV bool spin_fail(unsigned tries, unsigned* err) {
    __builtin_amdgcn_s_sleep(1);
    if ((tries & 255u) != 255u) return false;
    unsigned e; asm volatile("global_load_dword %0, %1, off sc1\n\ts_waitcnt vmcnt(0)" : "=&v"(e) : "v"(err) : "memory");
    if (__builtin_amdgcn_readfirstlane(e) == 1u) return true;
    if (tries > SPIN_CAP) { unsigned one = 1u; asm volatile("global_store_dword %0, %1, off sc1" :: "v"(err), "v"(one) : "memory"); return true; }
    return false;
}
DEV bool all_ge(unsigned v, unsigned t) { return __builtin_amdgcn_ballot_w64(v < t) == 0; }
template <int Z = 6> DEV void wait_flags(rsrc_t rc, unsigned off, unsigned t, unsigned* err) {
    for (unsigned tries = 0;; ++tries) {
        FENCE();
        if (all_ge(LD4(rc, off, 0), t)) break;
        __builtin_amdgcn_s_sleep(Z);
        if (spin_fail(tries, err)) break;
    }
    FENCE();
}

#define BC_LD(B, RS) asm volatile("buffer_load_dword %0, off, %1, 0 offset:512 sc1" : "=&v"(B) : "s"(RS) : "memory")
DEV unsigned beacon(v4u rs, unsigned b0, unsigned b1, unsigned need, bool seen = false) {
    asm volatile("" : "+v"(b0), "+v"(b1));
    for (unsigned tries = 0;; ++tries) {
        const unsigned u = __builtin_amdgcn_readfirstlane(b1);
        if (((seen || u != __builtin_amdgcn_readfirstlane(b0)) && (u & need) == need) || tries > SPIN_CAP) return u;
        __builtin_amdgcn_s_sleep(4);
        BC_LD(b1, rs); asm volatile("s_waitcnt vmcnt(0)" : "+v"(b1) :: "memory");
    }
}

template <bool VT> DEV unsigned stream_qkv(LAS char* lds, v4u rx, v4u rw, rsrc_t ro, rsrc_t rc, v4u rsc, bool hs, unsigned fo, int h, unsigned fx, unsigned pf, unsigned* dbg) {
    unsigned far = 0;
    const int tid = threadIdx.x, w = __builtin_amdgcn_readfirstlane(tid >> 6), l = tid & 63, kh = w >> 2, cg = w & 3;
    const unsigned lb = (unsigned)(size_t)lds, vb = wt_src(tid, HD * 2), l16 = l * 16;
    const unsigned x0 = (4 * cg + (VT ? 2 : 16) * (1 - kh)) * 2048 + kh * 1024, x1 = (4 * cg + (VT ? 2 : 16) * kh) * 2048 + kh * 1024;
#define STW(s) WT_ISSUE(rw, rw, vb, h * 256 + (s) * (64 * HD * 2), h * 256 + ((s) + 8) * (64 * HD * 2), HD * 2, lb + w * 1024 + ((s) & 3) * XS)
#define STX(u, q, e) DMA16(rx, l16, ((u) >> 1) * 65536 + ((q) & 1 ? x1 : x0) + (VT ? ((e) & 1) + 16 * ((e) >> 1) : (e)) * 2048, lb + (w * 4 + (e)) * XP + (q) * XS)
    const unsigned fl = tid == 0 ? fo : OOB;
    v4u W[16][2];
    unsigned b0, b1;
    BC_LD(b0, rsc);
    if (pf & 4) asm volatile("s_mov_b32 m0, %0\n\ts_nop 0\n\tbuffer_load_dword %1, %2, %3 offen lds"
                             :: "s"(lb + L_RED), "v"(hs && l < 16 ? l * 128 : OOB), "s"(rx), "s"((((h & 7) * 2 + VT) * 8 + w) * 2048) : "memory", "m0");
    STW(0); STW(1); STW(2); STW(3);
#pragma unroll
    for (int s = 0; s < 8; ++s) {
        WAITV(12); RBAR();
#pragma unroll
        for (int n = 0; n < 2; ++n) { const LAS char* p = lds + wt_rd(w, l, n) + (s & 3) * XS; W[2 * s][n] = wt_frag(p); W[2 * s + 1][n] = wt_frag(p + 8192); }
        BAR();
        if (s == 4) BC_LD(b1, rsc);
        if (s == 7) { far ^= FAR(beacon(rsc, b0, b1, 16, !hs)); ST4(rc, OOB, 0, 0u); FENCE(); }
        if (s < 4) STW(s + 4); else { STX(s - 4, s - 4, 0); STX(s - 4, s - 4, 1); if (s < 7) { STX(s - 4, s - 4, 2); STX(s - 4, s - 4, 3); } }
    }
    const LAS char* px = lds + (kh * 16 + (l & 15)) * XP + (l >> 4) * 16;
    LAS v4f* ps = (LAS v4f*)(lds + L_RED + w * 2048 + l16);
    const LAS v4f* pr = (const LAS v4f*)(lds + L_RED + (w ^ 4) * 2048 + l16);
    unsigned oo = OOB, on = (unsigned)h * 16384u + (far ^ fx) + cg * 2048u + kh * 1024u + l16;
    v4f c[2][2] = {}, r0 = {}, r1 = {};
    v4u f[8];
    auto fin = [&]() { r0 = add4(r0, c[1][0]); r1 = add4(r1, c[1][1]); ST16X(ro, oo, (v4u{pk2(r0[0], r0[1]), pk2(r0[2], r0[3]), pk2(r1[0], r1[1]), pk2(r1[2], r1[3])}), NTS & 1); oo = side(on); on += NH * 16384u; };
    WSTAMP(3);
    WAITV(10); RBAR();
    WSTAMP(4);
    const LAS char* px2 = px + 2 * XS; asm volatile("" : "+v"(px2));
#define RDX(q, k) f[(k) & 7] = *(const LAS v4u*)(((q) & 2 ? px2 : px) + ((q) & 1) * XS + (k) * 64)
#pragma unroll
    for (int k = 0; k < 8; ++k) RDX(0, k);
#pragma unroll 1
    for (int u0 = 0; u0 < 2 * NSEG; u0 += 4) {
        if (u0 == 4) WSTAMP(5);
        if (u0 == 64) WSTAMP(6);
#pragma unroll
        for (int q = 0; q < 4; ++q) {
#pragma unroll
            for (int k = 0; k < 16; ++k) {
                const v4u x = f[k & 7];
#pragma unroll
                for (int n = 0; n < 2; ++n) { const v4f z = k ? c[q & 1][n] : v4f{0, 0, 0, 0}; c[q & 1][n] = VT ? mf(x, W[k][n], z) : mf(W[k][n], x, z); }
                if (k < 8) RDX(q, k + 8);
                if (k == BK) { SCHED(); WAITV(10); BAR(); for (int e = 0; e < BK - 8; ++e) RDX(q + 1, e); }
                if (k >= BK) RDX(q + 1, k - 8);
                if (((k + 16 - BK) & 3) == 0) asm volatile("s_setprio %0" :: "n"(3 - (((k + 16 - BK) >> 2) & 3)));
                if (k == 2) { if (q & 1) { ps[0] = c[0][0]; ps[64] = c[0][1]; } else fin(); }
                if (k == 13) { if (q & 1) { r0 = pr[0]; r1 = pr[64]; } else ST4(rc, fl, 0, max((u0 + q - 2) >> 1, 0)); }
                if (!(EXP & 1) && !(k & 1) && kh == (((k >> 1) ^ ((BK + 1) >> 1)) & 1)) { if (k > BK) STX(u0 + q + 4, q, (k - BK) >> 2); else STX(u0 + q + 3, (q + 3) & 3, (k + 16 - BK) >> 2); }
                SCHED();
            }
        }
    }
    asm volatile("s_nop 7" : "+v"(c[1][0]), "+v"(c[1][1]));
    fin(); __builtin_amdgcn_s_setprio(0);
    WAITV(0); BAR();
    if (tid == 0) ST4(rc, fo, 0, NSEG);
    return far;
#undef STW
#undef STX
}

#define LDBX(RS, K, BASE, NT) LD16X(RS, l * 16 + ((K) & 3) * 1024, (BASE) + ((K) >> 2) * 4096, NT)
#define LDB(RS, K, BASE) LDBX(RS, K, BASE, 0)
DEV unsigned xattn(rsrc_t rq, rsrc_t rk, rsrc_t rv, rsrc_t rm, rsrc_t rc, unsigned* err, int seg, int h, unsigned fl, unsigned far, unsigned ms, LAS unsigned* cnt, unsigned* dbg) {
#define XST(k) do {} while (0)
    XST(3);
    const int l = lane(), fr = l & 15, g = l >> 4;
    const unsigned sb = side((unsigned)(seg * NH + h) * 16384u + far), mb = (unsigned)(seg * NH + h) * 4096u;
    const unsigned f0 = fw(min(l & 3, 2), h) + 12, f1 = F_PG + h * 512 + (l & 3) * 128;
    const unsigned old = __builtin_amdgcn_ballot_w64(fl <= (unsigned)seg);
    if (old & 15) wait_flags(rc, f0, seg + 1, err);
    XST(4);
    v4u q[4][2], k[4][2], km[4], v[8]; v2u vm[8];
    auto ldm = [&]() {
#pragma unroll
        for (int j = 0; j < 4; ++j) km[j] = LD16(rm, (g * 8 + (l & 7)) * 16 + j * 512, mb);
#pragma unroll
        for (int d = 0; d < 8; ++d) vm[d] = LD8(rm, 2048 + fr * 16 + (g & 1) * 8 + d * 256, mb); };
#pragma unroll
    for (int j = 0; j < 8; ++j) q[j >> 1][j & 1] = LDBX(rq, j, sb, 1);
    if (!(old & 240)) {
#pragma unroll
        for (int j = 0; j < 8; ++j) k[j >> 1][j & 1] = LDBX(rk, j, sb ^ KVS(ms >> 2, h), 1);
#pragma unroll
        for (int d = 0; d < 8; ++d) v[d] = LDBX(rv, d, sb ^ KVS(ms >> 4, h), 1);
        ldm();
    } else {
#pragma unroll
        for (int j = 0; j < 8; ++j) k[j >> 1][j & 1] = LDB(rk, j, sb ^ KVS(ms >> 2, h));
#pragma unroll
        for (int d = 0; d < 8; ++d) v[d] = LDB(rv, d, sb ^ KVS(ms >> 4, h));
    }
    v4f sx[3] = {}, o[2][8];
    float mx[2], zx[2];
    constexpr float C = SCALE * LOG2E, MR = MASKV / SCALE;
#pragma unroll
    for (int j = 0; j < 4; ++j) { sx[0] = mf(k[j][0], q[j][0], sx[0]); sx[1] = mf(k[j][0], q[j][1], sx[1]); sx[2] = mf(k[j][1], q[j][1], sx[2]); }
#pragma unroll
    for (int u = 0; u < 2; ++u) {
        float e[8];
#pragma unroll
        for (int r = 0; r < 4; ++r) { const bool vis = 4 * g + r <= fr; e[r] = (u || vis) ? sx[u][r] : MR; e[4 + r] = (u && vis) ? sx[2][r] : MR; }
        float m = e[0];
#pragma unroll
        for (int r = 1; r < 8; ++r) m = fmaxf(m, e[r]);
        mx[u] = m = red_g<true>(m); zx[u] = 0;
#pragma unroll
        for (int r = 0; r < 8; ++r) { e[r] = __builtin_amdgcn_exp2f((e[r] - m) * C); zx[u] += e[r]; }
        const v4u P = {pk2(e[0], e[1]), pk2(e[4], e[5]), pk2(e[2], e[3]), pk2(e[6], e[7])};
#pragma unroll
        for (int d = 0; d < 8; d += 2) {
            o[u][d] = mf(__builtin_shufflevector(v[d], v[d + 1], 0, 1, 4, 5), P, v4f{0, 0, 0, 0});
            o[u][d + 1] = mf(__builtin_shufflevector(v[d], v[d + 1], 2, 3, 6, 7), P, v4f{0, 0, 0, 0});
        }
    }
    asm volatile("" : "+v"(zx[0]), "+v"(zx[1]));
    SCHED(); XST(5);
    TS(threadIdx.x == 0, seg, 0);
    if (old & 240) { wait_flags<2>(rc, f1, seg + 1, err); TS(threadIdx.x == 0, seg, 1); ldm(); }
    XST(6);
    v4f sm[2] = {};
#pragma unroll
    for (int j = 0; j < 4; ++j) { sm[0] = mf(km[j], q[j][0], sm[0]); sm[1] = mf(km[j], q[j][1], sm[1]); }
    XST(28);
#pragma unroll
    for (int u = 0; u < 2; ++u) {
        const float m = red_g<true>(fmaxf(fmaxf(fmaxf(sm[u][0], sm[u][1]), fmaxf(sm[u][2], sm[u][3])), mx[u])), al = __builtin_amdgcn_exp2f((mx[u] - m) * C);
        float e[4];
#pragma unroll
        for (int r = 0; r < 4; ++r) e[r] = __builtin_amdgcn_exp2f((sm[u][r] - m) * C);
        const float inv = __builtin_amdgcn_rcpf(red_g<false>(al * zx[u] + 0.5f * (e[0] + e[1] + e[2] + e[3]))), f = al * inv, im = g < 2 ? inv : 0.0f;
        const unsigned pm0 = pk2(e[0] * im, e[1] * im), pm1 = pk2(e[2] * im, e[3] * im);
        const v4u Pa = {pm0, pm1, 0u, 0u}, Pb = {0u, 0u, pm0, pm1};
#pragma unroll
        for (int d = 0; d < 4; ++d) {
            const v4u vv = cat(vm[2 * d], vm[2 * d + 1]);
            const v4f o0 = mf(vv, Pa, o[u][2 * d] * f), o1 = mf(vv, Pb, o[u][2 * d + 1] * f);
            ST16(rq, l * 16 + d * 1024, sb + u * 4096, (v4u{pk2(o0[0], o0[1]), pk2(o0[2], o0[3]), pk2(o1[0], o1[1]), pk2(o1[2], o1[3])}));
        }
    }
    XST(29); TS(threadIdx.x == 0, seg, 2);
    FENCE(); const unsigned fn = LD4(rc, fl > seg + 4u ? OOB : (l & 4 ? f1 : f0), 0);
    WAITV(1); XST(30);
    if (l == 0 && __hip_atomic_fetch_add(cnt + (seg >> 2), 1u, __ATOMIC_RELAXED, __HIP_MEMORY_SCOPE_WORKGROUP) == 7) { ST4(rc, fw(3, (h & 24) + (seg & 3)) + 64 + (seg & 60), 0, 1u); TS(1, seg, 3); }
    if (seg >= XB) RBAR();
    XST(31);
    FENCE();
    return max(fl, fn);
}

template <int N> DEV unsigned ror(unsigned v) { return __builtin_amdgcn_update_dpp(0u, v, 0x120 + N, 0xf, 0xf, false); }
DEV void prep_rows(const bf16_t* x, const bf16_t* gw, const bf16_t* wr, rsrc_t rg, int row, int l) {
    v4u xv[16], gv[2], rv[2];
#pragma unroll
    for (int i = 0; i < 2; ++i) { gv[i] = *(const v4u*)(gw + i * 512 + l * 8); rv[i] = *(const v4u*)(wr + i * 512 + l * 8); }
#pragma unroll
    for (int i = 0; i < 16; ++i) xv[i] = *(const v4u*)(x + (size_t)row * DM + i * 512 + l * 8);
    float ga = 0, gb = 0;
#pragma unroll
    for (int k = 0; k < 8; ++k) {
        float a = 0, b = 0;
#pragma unroll
        for (int i = 0; i < 8; ++i) {
            const unsigned u = xv[2 * k + (i >> 2)][i & 3], g = gv[i >> 2][i & 3], r = rv[i >> 2][i & 3];
            a += rbf(lo16(u) * lo16(g)) + rbf(hi16(u) * hi16(g));
            b += lo16(u) * lo16(r) + hi16(u) * hi16(r);
        }
        a = half_add(a, b);
        a += __uint_as_float(ror<8>(__float_as_uint(a))); a += __uint_as_float(ror<4>(__float_as_uint(a)));
        a += __uint_as_float(ror<2>(__float_as_uint(a))); a += __uint_as_float(ror<1>(__float_as_uint(a)));
        const v2u s = sw16(__float_as_uint(a), __float_as_uint(a)); a = __uint_as_float(s[0]) + __uint_as_float(s[1]);
        if ((l & 31) == k) ga = a;
    }
    gb = rbf(ga);
    const unsigned u = __float_as_uint(l < 32 ? rbf(sigmoidf_(gb)) : gb) | 1u;
    const v2u e = sw32(u, u);
    ST16(rg, l < 8 && !(l & 1) ? F_GR + (row + l) * 8 : OOB, 0, (v4u{e[0], e[1], ror<15>(e[0]), ror<15>(e[1])}));
}
template <int N> DEV unsigned top_keys(unsigned* key, int l) {
    unsigned res = 0;
#pragma unroll 1
    for (int c = 0; c < TOPC; ++c) {
        unsigned m = key[0];
#pragma unroll
        for (int i = 1; i < N; ++i) m = max(m, key[i]);
        m = max(m, ror<8>(m)); m = max(m, ror<4>(m)); m = max(m, ror<2>(m)); m = max(m, ror<1>(m));
        v2u s = sw16(m, m); m = max(s[0], s[1]); s = sw32(m, m); m = max(s[0], s[1]);
#pragma unroll
        for (int i = 0; i < N; ++i) key[i] = key[i] == m ? 0u : key[i];
        if (l == c) res = m;
    }
    return res;
}
DEV int top8(LAS unsigned* cand, rsrc_t rc, unsigned* err, int w, int l) {
    v4u t[2];
    for (unsigned tries = 0;; ++tries) {
        FENCE();
        t[0] = LD16(rc, F_GR + w * 2048 + l * 16, 0); t[1] = LD16(rc, F_GR + w * 2048 + 1024 + l * 16, 0);
        const v4u v = t[0] & t[1];
        if (all_ge(v[0] & v[1] & v[2] & v[3] & 1u, 1u)) break;
        __builtin_amdgcn_s_sleep(8);
        if (spin_fail(tries, err)) break;
    }
    unsigned key[4];
#pragma unroll
    for (int i = 0; i < 4; ++i) { const unsigned r = t[i >> 1][(i & 1) * 2 + 1] - 1u; key[i] = ((int)r < 0 ? 0xffff0000u - r : r + 0x80000000u) + (0xffffu - (w * 256 + (i >> 1) * 128 + l * 2 + (i & 1))); }
    key[0] = top_keys<4>(key, l);
    if (l < TOPC) cand[w * TOPC + l] = key[0];
    BAR();
    key[0] = cand[l];
    return 0xffff - (int)(top_keys<1>(key, l) & 0xffffu);
}
static constexpr int NMOE = FF / 128, XR = 2080;
DEV void moe(LAS char* lds, v4u rx, int sel, v4u rg, v4u ru, v4u rd, rsrc_t rp, int cu) {
    const int tid = threadIdx.x, w = __builtin_amdgcn_readfirstlane(tid >> 6), l = tid & 63, kh = w >> 2, cg = w & 3, fr = l & 15, fq = l >> 4;
    const unsigned lb = (unsigned)(size_t)lds, v1 = wt_src(tid, FF * 2), v2 = wt_src(tid, DM * 2);
    const unsigned xo = __builtin_amdgcn_readlane(sel, w) * 2048;
    DMA16(rx, l * 16, xo, lb + L_RED + w * XR); DMA16(rx, l * 16, xo + 1024, lb + L_RED + w * XR + 1024);
    auto issue = [&](int s) {
        const unsigned d = lb + w * 1024 + (s & 3) * XS;
        if (s < 16) { const unsigned o = s * (64 * FF * 2) + cu * 256; WT_ISSUE(rg, ru, v1, o, o, FF * 2, d); }
        else { const unsigned o = s < 24 ? (cu * 128 + (s & 1) * 64) * (DM * 2) + (((s >> 1) + cu) & 3) * 512 : OOB; WT_ISSUE(rd, rd, v2, o, o + 256, DM * 2, d); } };
    issue(0); issue(1); issue(2); issue(3);
    const int p0 = wt_rd(w, l, 0), p1 = wt_rd(w, l, 1);
    v4f c[2] = {};
    auto stage = [&](int s, const LAS char* pa) {
        WAITV(12); RBAR();
        const LAS char* p = lds + (s & 3) * XS;
        const v4u a0 = *(const LAS v4u*)pa, a1 = *(const LAS v4u*)(pa + 64), b0 = wt_frag(p + p0), b1 = wt_frag(p + p1), b2 = wt_frag(p + p0 + 8192), b3 = wt_frag(p + p1 + 8192);
        BAR();
        issue(s + 4);
        c[0] = mf(b2, a1, mf(b0, a0, c[0])); c[1] = mf(b3, a1, mf(b1, a0, c[1])); };
#pragma unroll 1
    for (int s = 0; s < 16; ++s) stage(s, lds + L_RED + (l & 7) * XR + fq * 16 + s * 128);
    LAS v4f* pu = (LAS v4f*)(lds + L_RED + cg * 2048 + l * 16);
    if (kh) { pu[0] = c[0]; pu[64] = c[1]; }
    BAR();
    if (!kh && fr < 8) {
#pragma unroll
        for (int n = 0; n < 2; ++n) {
            const v4f u = pu[64 * n]; float hv[4];
#pragma unroll
            for (int e = 0; e < 4; ++e) { const float g = rbf(c[n][e]); hv[e] = rbf(g * sigmoidf_(g)) * rbf(u[e]); }
            *(LAS v2u*)(lds + L_RED + 8192 + fr * 288 + cg * 64 + n * 32 + fq * 8) = v2u{pk2(hv[0], hv[1]), pk2(hv[2], hv[3])};
        }
    }
    WAITL(0);
#pragma unroll 1
    for (int s = 16; s < 24; s += 2) {
        c[0] = v4f{0, 0, 0, 0}; c[1] = c[0];
        const LAS char* pa = lds + L_RED + 8192 + (l & 7) * 288 + fq * 16;
        stage(s, pa); stage(s + 1, pa + 128);
        ST16(rp, cu * 32768 + (l & 7) * 4096 + ((((s >> 1) + cu) & 3) * 256 + kh * 128 + cg * 32 + (fr >> 3) * 16 + fq * 4) * 4, 0, __builtin_bit_cast(v4u, fr < 8 ? c[0] : c[1]));
    }
    WAITV(0); RBAR();
}

static constexpr unsigned H2_OFF = 0, H1_OFF = 512 * 1024, H3_OFF = 768 * 1024, PR_OFF = 896 * 1024;
static constexpr size_t SLOT = 2u << 20;
static constexpr int NSLOT = 4;
struct Args {
    const bf16_t* x; const bf16_t* mem0; const bf16_t* Wq; const bf16_t* Wk; const bf16_t* Wv; const bf16_t* Wo;
    const bf16_t* w_r; const bf16_t* wg; const bf16_t* wu; const bf16_t* wd; const bf16_t* gw;
    char* qkv; char* mb; int* idx; float* part; float* upd; char* wt; bf16_t* out;
    char* ring; unsigned* ctl; unsigned* dbg;
    u64 segs;
    float per; unsigned per0, d1, dv, d2, dp, slack, thr, spare, wq, wx, pf, ms, ws, wi, gap;
};
typedef const __attribute__((address_space(4))) Args* KArgs;
DEV KArgs args() { KArgs p = (KArgs)__builtin_amdgcn_kernarg_segment_ptr(); asm volatile("" : "+s"(p)); return p; }
static constexpr unsigned QSZ = S_TOK * HD * 4;
typedef unsigned v16u __attribute__((ext_vector_type(16)));
typedef unsigned v32u __attribute__((ext_vector_type(32)));

DEV unsigned nearo(unsigned off) { return (off >> 13) * 16384u + (off & 8191u); }
DEV unsigned rt32() { u64 t; asm volatile("s_memrealtime %0\n\ts_waitcnt lgkmcnt(0)" : "=s"(t) :: "memory"); return (unsigned)t; }
DEV unsigned cyc32() { u64 t; asm volatile("s_memtime %0\n\ts_waitcnt lgkmcnt(0)" : "=s"(t) :: "memory"); return (unsigned)t; }
template <int R1, int R2, int R3> DEV v8u rd_w(const LAS char* p) {
    return __builtin_shufflevector(cat(trd(p), trd(p + R1)), cat(trd(p + R2), trd(p + R3)), 0, 1, 2, 3, 4, 5, 6, 7);
}
DEV v16u cat8(v8u a, v8u b) { return __builtin_shufflevector(a, b, 0, 1, 2, 3, 4, 5, 6, 7, 8, 9, 10, 11, 12, 13, 14, 15); }
DEV v32u cat16(v16u a, v16u b) { return __builtin_shufflevector(a, b, 0, 1, 2, 3, 4, 5, 6, 7, 8, 9, 10, 11, 12, 13, 14, 15, 16, 17, 18, 19, 20, 21, 22, 23, 24, 25, 26, 27, 28, 29, 30, 31); }
DEV v8u cat4(v4u a, v4u b) { return __builtin_shufflevector(a, b, 0, 1, 2, 3, 4, 5, 6, 7); }

static constexpr int L_PQK = 0, L_PV = 8704, L_P = 13824, L_VT = 15360, L_X = 16384, L_K = L_X + 9216, L_MISC = 28672, L_PF = 29696, L_STG = 32768, L_TOT = 163840;
static constexpr int X_FQ = 272, X_BLK = 1152;
static constexpr unsigned WO_DONE = 33;
#define ASM_A \
    "s_setprio 1\n" \
    "s_mov_b32 s76, 0\n" \
    "s_mov_b32 s99, 0x20000\n" \
    "s_mov_b32 s84, 0\n" \
    "s_mov_b32 s81, 0\n" \
    "s_mov_b32 s86, 0\n" \
    "s_mov_b32 s87, 0\n" \
    "s_mov_b32 s88, 0\n" \
    "s_mov_b32 s89, 0\n" \
    "s_mov_b32 s90, 0\n" \
    "s_mov_b32 s91, 0\n" \
    "s_mov_b32 s92, 0\n" \
    "s_mov_b32 s93, 0\n" \
    "s_mov_b32 s94, 0\n" \
    "s_mov_b32 s95, 0\n" \
    "s_mov_b32 s96, 0\n" \
    "s_mov_b32 s97, 0\n" \
    "s_mov_b32 s98, 0\n" \
    "ds_read_b32 v244, v237\n" \
    "s_waitcnt lgkmcnt(0)\n" \
    "v_readfirstlane_b32 s94, v244\n" \
    "s_mul_i32 s79, s94, s70\n" \
    "s_add_u32 s79, s79, s81\n" \
    "s_mov_b32 s77, s79\n" \
    "s_mul_i32 s96, s65, s71\n" \
    "s_lshr_b32 s96, s96, 4\n" \
    "v_mov_b64 v[208:209], 0\n" \
    "v_mov_b64 v[210:211], 0\n" \
    "v_mov_b64 v[212:213], 0\n" \
    "v_mov_b64 v[214:215], 0\n" \
    "v_mov_b64 v[216:217], 0\n" \
    "v_mov_b64 v[218:219], 0\n" \
    "v_mov_b64 v[220:221], 0\n" \
    "v_mov_b64 v[222:223], 0\n" \
    "v_mov_b64 v[224:225], 0\n" \
    "v_mov_b64 v[226:227], 0\n" \
    "v_mov_b64 v[228:229], 0\n" \
    "v_mov_b64 v[230:231], 0\n" \
    "v_mov_b32 v241, 1\n" \
    "v_mov_b32 v246, 0\n" \
    "s_mov_b32 s83, 0\n" \
    "s_branch L%=_Acomp\n" \
    "L%=_Aloop:\n" \
    "s_lshl_b32 s80, s84, 1\n" \
    "s_sub_u32 s80, s60, s80\n" \
    "v_mov_b32 v247, -1\n" \
    "s_sleep 64\n" \
    "L%=_Aloop2:\n" \
    "ds_read_b64 v[246:247], v237 offset:72\n" \
    "s_sleep 64\n" \
    "L%=_Aloop3:\n" \
    "v_readfirstlane_b32 s79, v247\n" \
    "s_add_u32 s95, s79, s80\n" \
    "s_sub_u32 s85, s95, s60\n" \
    "s_cmp_gt_u32 s85, 0x20000\n" \
    "s_cbranch_scc1 L%=_Aloop4\n" \
    "L%=_Ago:\n" \
    "buffer_load_dwordx4 v[192:195], v233, s[40:43], s95 offen sc1\n" \
    "s_add_u32 s85, s95, 0x1000\n" \
    "s_nop 4\n" \
    "buffer_load_dwordx4 v[196:199], v233, s[40:43], s85 offen sc1\n" \
    "s_nop 5\n" \
    "buffer_load_dwordx4 v[200:203], v239, s[40:43], s95 offen sc1\n" \
    "s_nop 5\n" \
    "buffer_load_dwordx4 v[204:207], v239, s[40:43], s85 offen sc1\n" \
    "L%=_Acomp:\n" \
    "s_mov_b32 m0, s72\n" \
    "s_waitcnt vmcnt(3)\n" \
    "v_smfmac_f32_16x16x64_bf16 v[208:211], v[192:195], v[0:7], v232\n" \
    "v_smfmac_f32_16x16x64_bf16 v[212:215], v[192:195], v[8:15], v232\n" \
    "v_smfmac_f32_16x16x64_bf16 v[216:219], v[192:195], v[64:71], v232\n" \
    "v_smfmac_f32_16x16x64_bf16 v[220:223], v[192:195], v[72:79], v232\n" \
    "v_smfmac_f32_16x16x64_bf16 v[224:227], v[192:195], v[128:135], v232\n" \
    "s_waitcnt vmcnt(2)\n" \
    "v_smfmac_f32_16x16x64_bf16 v[208:211], v[196:199], v[16:23], v232\n" \
    "v_smfmac_f32_16x16x64_bf16 v[212:215], v[196:199], v[24:31], v232\n" \
    "v_smfmac_f32_16x16x64_bf16 v[216:219], v[196:199], v[80:87], v232\n" \
    "v_smfmac_f32_16x16x64_bf16 v[220:223], v[196:199], v[88:95], v232\n" \
    "v_smfmac_f32_16x16x64_bf16 v[228:231], v[192:195], v[136:143], v232\n" \
    "s_waitcnt vmcnt(1)\n" \
    "v_smfmac_f32_16x16x64_bf16 v[208:211], v[200:203], v[32:39], v232\n" \
    "v_smfmac_f32_16x16x64_bf16 v[212:215], v[200:203], v[40:47], v232\n" \
    "v_smfmac_f32_16x16x64_bf16 v[216:219], v[200:203], v[96:103], v232\n" \
    "v_smfmac_f32_16x16x64_bf16 v[220:223], v[200:203], v[104:111], v232\n" \
    "v_smfmac_f32_16x16x64_bf16 v[224:227], v[196:199], v[144:151], v232\n" \
    "s_waitcnt vmcnt(0)\n" \
    "v_smfmac_f32_16x16x64_bf16 v[208:211], v[204:207], v[48:55], v232\n" \
    "v_smfmac_f32_16x16x64_bf16 v[212:215], v[204:207], v[56:63], v232\n" \
    "v_smfmac_f32_16x16x64_bf16 v[216:219], v[204:207], v[112:119], v232\n" \
    "v_add_f32 v208, v208, v210\n" \
    "v_smfmac_f32_16x16x64_bf16 v[220:223], v[204:207], v[120:127], v232\n" \
    "v_add_f32 v209, v209, v211\n" \
    "v_cmp_u_f32 vcc, v208, v209\n" \
    "v_add_f32 v212, v212, v214\n" \
    "v_add_f32 v213, v213, v215\n" \
    "v_add_f32 v216, v216, v218\n" \
    "v_add_f32 v217, v217, v219\n" \
    "s_nop 1\n" \
    "v_add_f32 v220, v220, v222\n" \
    "v_add_f32 v221, v221, v223\n" \
    "v_smfmac_f32_16x16x64_bf16 v[224:227], v[200:203], v[160:167], v232\n" \
    "s_cbranch_vccnz L%=_Aslow\n" \
    "L%=_Aok:\n" \
    "ds_write_addtid_b32 v208 offset:0\n" \
    "ds_write_addtid_b32 v209 offset:544\n" \
    "v_smfmac_f32_16x16x64_bf16 v[228:231], v[196:199], v[152:159], v232\n" \
    "ds_write_addtid_b32 v216 offset:256\n" \
    "ds_write_addtid_b32 v217 offset:800\n" \
    "v_smfmac_f32_16x16x64_bf16 v[224:227], v[204:207], v[176:183], v232\n" \
    "ds_write_addtid_b32 v212 offset:1088\n" \
    "ds_write_addtid_b32 v213 offset:1632\n" \
    "v_smfmac_f32_16x16x64_bf16 v[228:231], v[200:203], v[168:175], v232\n" \
    "ds_write_addtid_b32 v220 offset:1344\n" \
    "ds_write_addtid_b32 v221 offset:1888\n" \
    "ds_write_b32 v236, v241 offset:32\n" \
    "v_smfmac_f32_16x16x64_bf16 v[228:231], v[204:207], v[184:191], v232\n" \
    "s_barrier\n" \
    "s_mov_b32 m0, s73\n" \
    "v_add_f32 v224, v224, v226\n" \
    "v_add_f32 v225, v225, v227\n" \
    "ds_write_addtid_b32 v224 offset:8704\n" \
    "ds_write_addtid_b32 v225 offset:8992\n" \
    "s_nop 1\n" \
    "v_add_f32 v228, v228, v230\n" \
    "v_add_f32 v229, v229, v231\n" \
    "ds_write_addtid_b32 v228 offset:9344\n" \
    "ds_write_addtid_b32 v229 offset:9632\n" \
    "ds_write_b32 v236, v241 offset:16\n" \
    "s_setprio 0\n" \
    "v_mov_b64 v[208:209], 0\n" \
    "v_mov_b64 v[210:211], 0\n" \
    "v_mov_b64 v[212:213], 0\n" \
    "v_mov_b64 v[214:215], 0\n" \
    "v_mov_b64 v[216:217], 0\n" \
    "v_mov_b64 v[218:219], 0\n" \
    "v_mov_b64 v[220:221], 0\n" \
    "v_mov_b64 v[222:223], 0\n" \
    "v_mov_b64 v[224:225], 0\n" \
    "v_mov_b64 v[226:227], 0\n" \
    "v_mov_b64 v[228:229], 0\n" \
    "v_mov_b64 v[230:231], 0\n" \
    "v_add_u32 v241, 1, v241\n" \
    "v_readfirstlane_b32 s97, v246\n" \
    "s_lshl1_add_u32 s79, s76, 4\n" \
    "s_max_u32 s75, s75, s79\n" \
    "s_lshl_b32 s97, s97, 1\n" \
    "s_lshl1_add_u32 s79, s76, 7\n" \
    "s_min_u32 s79, s79, s97\n" \
    "s_cmp_eq_u32 s83, -1\n" \
    "s_cselect_b32 s79, 0, s79\n" \
    "s_cmp_ge_u32 s75, s79\n" \
    "s_cbranch_scc1 L%=_Ab1\n" \
    "s_lshr_b32 s80, s75, 1\n" \
    "s_lshl_b32 s98, s80, 19\n" \
    "s_add_u32 s98, s98, s74\n" \
    "s_lshl_b32 s79, s80, 11\n" \
    "s_and_b32 s79, s79, 0x2000\n" \
    "s_xor_b32 s98, s98, s79\n" \
    "s_lshl_b32 s79, s75, 9\n" \
    "s_and_b32 s79, s79, 0x200\n" \
    "s_add_u32 s98, s98, s79\n" \
    "s_add_u32 s75, s75, 1\n" \
    "s_mov_b32 s83, -1\n" \
    "s_buffer_load_dword s82, s[52:55], s98 offset:0\n" \
    "s_buffer_load_dword s82, s[52:55], s98 offset:64\n" \
    "s_buffer_load_dword s82, s[52:55], s98 offset:128\n" \
    "s_buffer_load_dword s82, s[52:55], s98 offset:192\n" \
    "s_buffer_load_dword s82, s[52:55], s98 offset:256\n" \
    "s_buffer_load_dword s82, s[52:55], s98 offset:320\n" \
    "s_buffer_load_dword s82, s[52:55], s98 offset:384\n" \
    "s_buffer_load_dword s83, s[52:55], s98 offset:448\n" \
    "L%=_Ab1:\n" \
    "s_sleep 12\n" \
    "s_lshl1_add_u32 s79, s76, 8\n" \
    "s_min_u32 s79, s79, s97\n" \
    "s_cmp_eq_u32 s83, -1\n" \
    "s_cselect_b32 s79, 0, s79\n" \
    "s_cmp_ge_u32 s75, s79\n" \
    "s_cbranch_scc1 L%=_Ab2\n" \
    "s_lshr_b32 s80, s75, 1\n" \
    "s_lshl_b32 s98, s80, 19\n" \
    "s_add_u32 s98, s98, s74\n" \
    "s_lshl_b32 s79, s80, 11\n" \
    "s_and_b32 s79, s79, 0x2000\n" \
    "s_xor_b32 s98, s98, s79\n" \
    "s_lshl_b32 s79, s75, 9\n" \
    "s_and_b32 s79, s79, 0x200\n" \
    "s_add_u32 s98, s98, s79\n" \
    "s_add_u32 s75, s75, 1\n" \
    "s_mov_b32 s83, -1\n" \
    "s_buffer_load_dword s82, s[52:55], s98 offset:0\n" \
    "s_buffer_load_dword s82, s[52:55], s98 offset:64\n" \
    "s_buffer_load_dword s82, s[52:55], s98 offset:128\n" \
    "s_buffer_load_dword s82, s[52:55], s98 offset:192\n" \
    "s_buffer_load_dword s82, s[52:55], s98 offset:256\n" \
    "s_buffer_load_dword s82, s[52:55], s98 offset:320\n" \
    "s_buffer_load_dword s82, s[52:55], s98 offset:384\n" \
    "s_buffer_load_dword s83, s[52:55], s98 offset:448\n" \
    "L%=_Ab2:\n" \
    "s_barrier\n" \
    "s_sleep 4\n" \
    "s_lshl1_add_u32 s79, s76, 8\n" \
    "s_min_u32 s79, s79, s97\n" \
    "s_cmp_eq_u32 s83, -1\n" \
    "s_cselect_b32 s79, 0, s79\n" \
    "s_cmp_ge_u32 s75, s79\n" \
    "s_cbranch_scc1 L%=_Ab3\n" \
    "s_lshr_b32 s80, s75, 1\n" \
    "s_lshl_b32 s98, s80, 19\n" \
    "s_add_u32 s98, s98, s74\n" \
    "s_lshl_b32 s79, s80, 11\n" \
    "s_and_b32 s79, s79, 0x2000\n" \
    "s_xor_b32 s98, s98, s79\n" \
    "s_lshl_b32 s79, s75, 9\n" \
    "s_and_b32 s79, s79, 0x200\n" \
    "s_add_u32 s98, s98, s79\n" \
    "s_add_u32 s75, s75, 1\n" \
    "s_mov_b32 s83, -1\n" \
    "s_buffer_load_dword s82, s[52:55], s98 offset:0\n" \
    "s_buffer_load_dword s82, s[52:55], s98 offset:64\n" \
    "s_buffer_load_dword s82, s[52:55], s98 offset:128\n" \
    "s_buffer_load_dword s82, s[52:55], s98 offset:192\n" \
    "s_buffer_load_dword s82, s[52:55], s98 offset:256\n" \
    "s_buffer_load_dword s82, s[52:55], s98 offset:320\n" \
    "s_buffer_load_dword s82, s[52:55], s98 offset:384\n" \
    "s_buffer_load_dword s83, s[52:55], s98 offset:448\n" \
    "L%=_Ab3:\n" \
    "s_mov_b32 s79, s60\n" \
    "s_mov_b32 s60, s61\n" \
    "s_mov_b32 s61, s62\n" \
    "s_mov_b32 s62, s63\n" \
    "s_mov_b32 s63, s79\n" \
    "s_add_u32 s76, s76, 1\n" \
    "s_add_u32 s84, s84, 0x20000\n" \
    "s_setprio 1\n" \
    "s_cmp_lt_u32 s76, 64\n" \
    "s_cbranch_scc1 L%=_Aloop\n" \
    "s_waitcnt lgkmcnt(0)\n" \
    "s_branch L%=_end\n" \
    "L%=_Aslow:\n" \
    "s_nop 7\n" \
    "s_nop 0\n" \
    "v_max_u32 v244, v192, v193\n" \
    "v_max3_u32 v244, v244, v194, v195\n" \
    "v_max3_u32 v244, v244, v196, v197\n" \
    "v_max3_u32 v244, v244, v198, v199\n" \
    "v_max3_u32 v244, v244, v200, v201\n" \
    "v_max3_u32 v244, v244, v202, v203\n" \
    "v_max3_u32 v244, v244, v204, v205\n" \
    "v_max3_u32 v244, v244, v206, v207\n" \
    "v_cmp_eq_u32 vcc, -1, v244\n" \
    "s_cbranch_vccz L%=_Aok\n" \
    "s_cmp_eq_u32 s99, 0\n" \
    "s_cbranch_scc1 L%=_Aok\n" \
    "s_sub_u32 s99, s99, 1\n" \
    "s_cmp_lg_u32 s99, 0\n" \
    "s_cbranch_scc1 L%=_Au2\n" \
    "v_mov_b32 v244, 1\n" \
    "buffer_store_dword v244, off, s[44:47], 0 sc1\n" \
    "L%=_Au2:\n" \
    "v_mov_b64 v[208:209], 0\n" \
    "v_mov_b64 v[210:211], 0\n" \
    "v_mov_b64 v[212:213], 0\n" \
    "v_mov_b64 v[214:215], 0\n" \
    "v_mov_b64 v[216:217], 0\n" \
    "v_mov_b64 v[218:219], 0\n" \
    "v_mov_b64 v[220:221], 0\n" \
    "v_mov_b64 v[222:223], 0\n" \
    "v_mov_b64 v[224:225], 0\n" \
    "v_mov_b64 v[226:227], 0\n" \
    "v_mov_b64 v[228:229], 0\n" \
    "v_mov_b64 v[230:231], 0\n" \
    "ds_read_b64 v[244:245], v237 offset:80\n" \
    "s_waitcnt lgkmcnt(0)\n" \
    "v_readfirstlane_b32 s81, v244\n" \
    "v_readfirstlane_b32 s71, v245\n" \
    "ds_read_b32 v244, v237\n" \
    "s_waitcnt lgkmcnt(0)\n" \
    "v_readfirstlane_b32 s94, v244\n" \
    "s_mul_i32 s79, s94, s70\n" \
    "s_add_u32 s79, s79, s81\n" \
    "s_mov_b32 s77, s79\n" \
    "s_memrealtime vcc\n" \
    "s_waitcnt lgkmcnt(0)\n" \
    "s_sub_u32 s80, vcc_lo, s64\n" \
    "s_add_u32 s80, s80, -4\n" \
    "s_branch L%=_Au4h\n" \
    "L%=_Au4g:\n" \
    "s_add_u32 s77, s77, s70\n" \
    "s_add_u32 s94, s94, 1\n" \
    "L%=_Au4h:\n" \
    "s_sub_u32 s79, s80, s77\n" \
    "s_cmp_lt_u32 s79, 0x1000000\n" \
    "s_cbranch_scc1 L%=_Au4g\n" \
    "v_mov_b32 v244, s94\n" \
    "ds_max_u32 v237, v244\n" \
    "s_branch L%=_Au5b\n" \
    "L%=_Au5a:\n" \
    "s_sleep 4\n" \
    "L%=_Au5b:\n" \
    "s_memrealtime vcc\n" \
    "s_add_u32 s80, s77, s64\n" \
    "s_add_u32 s80, s80, -1\n" \
    "s_waitcnt lgkmcnt(0)\n" \
    "s_sub_i32 s79, s80, vcc_lo\n" \
    "s_sub_u32 s79, s79, 40\n" \
    "s_cmp_lt_u32 s79, 0x400\n" \
    "s_cbranch_scc1 L%=_Au5a\n" \
    "s_memtime s[38:39]\n" \
    "s_memrealtime vcc\n" \
    "s_nop 4\n" \
    "s_memrealtime s[32:33]\n" \
    "s_nop 4\n" \
    "s_memrealtime s[34:35]\n" \
    "s_nop 4\n" \
    "s_memrealtime s[36:37]\n" \
    "s_add_u32 s80, s77, s64\n" \
    "s_lshl_b32 s80, s80, 2\n" \
    "s_waitcnt lgkmcnt(0)\n" \
    "s_sub_u32 s80, s80, vcc_lo\n" \
    "s_sub_u32 s80, s80, s32\n" \
    "s_sub_u32 s80, s80, s34\n" \
    "s_sub_u32 s80, s80, s36\n" \
    "s_mul_i32 s80, s80, s71\n" \
    "s_ashr_i32 s80, s80, 6\n" \
    "s_add_u32 s78, s80, s38\n" \
    "s_and_b32 s95, s94, 1\n" \
    "s_lshl_b32 s95, s95, 17\n" \
    "s_add_u32 s95, s95, s60\n" \
    "s_add_u32 s85, s95, 0x1000\n" \
    "L%=_Au6a:\n" \
    "s_memtime s[38:39]\n" \
    "s_mul_i32 s80, -1, 23\n" \
    "s_add_u32 s80, s80, s78\n" \
    "s_waitcnt lgkmcnt(0)\n" \
    "s_sub_i32 s79, s80, s38\n" \
    "s_max_i32 s79, s79, 0\n" \
    "s_cmp_gt_u32 s79, 320\n" \
    "s_cbranch_scc1 L%=_Au6s\n" \
    ".p2align 4\n" \
    "L%=_Au6d:\n" \
    "s_sub_u32 s79, s79, 28\n" \
    "s_cbranch_scc0 L%=_Au6d\n" \
    "L%=_Au6e:\n" \
    "s_branch L%=_Ago\n" \
    "L%=_Aloop4:\n" \
    "s_cmp_eq_u32 s79, -1\n" \
    "s_cbranch_scc1 L%=_Aloop3\n" \
    "v_mov_b32 v247, -1\n" \
    "s_branch L%=_Aloop2\n" \
    "L%=_Au6s:\n" \
    "s_cmp_gt_u32 s79, 0x8000\n" \
    "s_cbranch_scc1 L%=_Au6e\n" \
    "s_sleep 2\n" \
    "s_branch L%=_Au6a\n" \
    "L%=_end:\n"
#define ASM_B \
    "s_and_b32 s79, s78, 3\n" \
    "s_cmp_eq_u32 s79, 1\n" \
    "s_cbranch_scc1 L%=_S\n" \
    "s_cmp_eq_u32 s79, 2\n" \
    "s_cbranch_scc1 L%=_R\n" \
    "s_cmp_eq_u32 s79, 3\n" \
    "s_cbranch_scc1 L%=_P\n" \
    "L%=_T:\n" \
    "s_mov_b32 s76, 0\n" \
    "s_mov_b32 s99, 0x20000\n" \
    "s_mov_b32 s84, 0\n" \
    "s_mov_b32 s81, 0\n" \
    "s_mov_b32 s86, 0\n" \
    "s_mov_b32 s87, 0\n" \
    "s_mov_b32 s88, 0\n" \
    "s_mov_b32 s89, 0\n" \
    "s_mov_b32 s90, 0\n" \
    "s_mov_b32 s91, 0\n" \
    "s_mov_b32 s92, 0\n" \
    "s_mov_b32 s93, 0\n" \
    "s_mov_b32 s94, 0\n" \
    "s_mov_b32 s95, 0\n" \
    "s_mov_b32 s96, 0\n" \
    "s_mov_b32 s97, 0\n" \
    "s_mov_b32 s98, 0\n" \
    "ds_read_b32 v216, v229\n" \
    "s_waitcnt lgkmcnt(0)\n" \
    "v_readfirstlane_b32 s94, v216\n" \
    "s_mul_i32 s79, s94, s70\n" \
    "s_add_u32 s79, s79, s81\n" \
    "s_mov_b32 s77, s79\n" \
    "s_mul_i32 s96, s65, s71\n" \
    "s_lshr_b32 s96, s96, 4\n" \
    "v_mov_b32 v96, 0x3f803f80\n" \
    "v_mov_b32 v97, 0x3f803f80\n" \
    "v_mov_b32 v98, 0x3f803f80\n" \
    "v_mov_b32 v99, 0x3f803f80\n" \
    "v_mov_b32 v100, 0x3f803f80\n" \
    "v_mov_b32 v101, 0x3f803f80\n" \
    "v_mov_b64 v[102:103], 0\n" \
    "v_mov_b64 v[64:65], 0\n" \
    "v_mov_b64 v[66:67], 0\n" \
    "v_mov_b64 v[68:69], 0\n" \
    "v_mov_b64 v[70:71], 0\n" \
    "v_mov_b64 v[72:73], 0\n" \
    "v_mov_b64 v[74:75], 0\n" \
    "v_mov_b64 v[76:77], 0\n" \
    "v_mov_b64 v[78:79], 0\n" \
    "v_mov_b64 v[80:81], 0\n" \
    "v_mov_b64 v[82:83], 0\n" \
    "v_mov_b64 v[84:85], 0\n" \
    "v_mov_b64 v[86:87], 0\n" \
    "v_mov_b64 v[88:89], 0\n" \
    "v_mov_b64 v[90:91], 0\n" \
    "v_mov_b64 v[92:93], 0\n" \
    "v_mov_b64 v[94:95], 0\n" \
    "v_mov_b64 v[192:193], 0\n" \
    "v_mov_b64 v[194:195], 0\n" \
    "L%=_Tloop:\n" \
    "ds_read_b64_tr_b16 v[104:105], v228 offset:16384\n" \
    "ds_read_b64_tr_b16 v[106:107], v228 offset:16416\n" \
    "ds_read_b64_tr_b16 v[108:109], v228 offset:18688\n" \
    "ds_read_b64_tr_b16 v[110:111], v228 offset:18720\n" \
    "ds_read_b64_tr_b16 v[112:113], v228 offset:20992\n" \
    "ds_read_b64_tr_b16 v[114:115], v228 offset:21024\n" \
    "ds_read_b64_tr_b16 v[116:117], v228 offset:23296\n" \
    "ds_read_b64_tr_b16 v[118:119], v228 offset:23328\n" \
    "ds_read_b64_tr_b16 v[120:121], v228 offset:16656\n" \
    "ds_read_b64_tr_b16 v[122:123], v228 offset:16688\n" \
    "ds_read_b64_tr_b16 v[124:125], v228 offset:18960\n" \
    "ds_read_b64_tr_b16 v[126:127], v228 offset:18992\n" \
    "ds_read_b64_tr_b16 v[128:129], v228 offset:21264\n" \
    "ds_read_b64_tr_b16 v[130:131], v228 offset:21296\n" \
    "ds_read_b64_tr_b16 v[132:133], v228 offset:23568\n" \
    "ds_read_b64_tr_b16 v[134:135], v228 offset:23600\n" \
    "s_waitcnt lgkmcnt(14)\n" \
    "v_mfma_f32_16x16x32_bf16 v[136:139], v[104:107], v[0:3], 0\n" \
    "v_mfma_f32_16x16x32_bf16 v[144:147], v[104:107], v[8:11], 0\n" \
    "v_mfma_f32_16x16x32_bf16 v[152:155], v[104:107], v[16:19], 0\n" \
    "v_mfma_f32_16x16x32_bf16 v[160:163], v[104:107], v[24:27], 0\n" \
    "s_waitcnt lgkmcnt(12)\n" \
    "v_mfma_f32_16x16x32_bf16 v[136:139], v[108:111], v[4:7], v[136:139]\n" \
    "v_mfma_f32_16x16x32_bf16 v[144:147], v[108:111], v[12:15], v[144:147]\n" \
    "v_mfma_f32_16x16x32_bf16 v[152:155], v[108:111], v[20:23], v[152:155]\n" \
    "v_mfma_f32_16x16x32_bf16 v[160:163], v[108:111], v[28:31], v[160:163]\n" \
    "s_waitcnt lgkmcnt(10)\n" \
    "v_mfma_f32_16x16x32_bf16 v[136:139], v[112:115], v[32:35], v[136:139]\n" \
    "v_mfma_f32_16x16x32_bf16 v[144:147], v[112:115], v[40:43], v[144:147]\n" \
    "v_mfma_f32_16x16x32_bf16 v[152:155], v[112:115], v[48:51], v[152:155]\n" \
    "v_mfma_f32_16x16x32_bf16 v[160:163], v[112:115], v[56:59], v[160:163]\n" \
    "s_waitcnt lgkmcnt(8)\n" \
    "v_mfma_f32_16x16x32_bf16 v[136:139], v[116:119], v[36:39], v[136:139]\n" \
    "v_mfma_f32_16x16x32_bf16 v[144:147], v[116:119], v[44:47], v[144:147]\n" \
    "v_mfma_f32_16x16x32_bf16 v[152:155], v[116:119], v[52:55], v[152:155]\n" \
    "v_mfma_f32_16x16x32_bf16 v[160:163], v[116:119], v[60:63], v[160:163]\n" \
    "s_waitcnt lgkmcnt(6)\n" \
    "v_mfma_f32_16x16x32_bf16 v[140:143], v[120:123], v[0:3], 0\n" \
    "v_mfma_f32_16x16x32_bf16 v[148:151], v[120:123], v[8:11], 0\n" \
    "v_mfma_f32_16x16x32_bf16 v[156:159], v[120:123], v[16:19], 0\n" \
    "v_mfma_f32_16x16x32_bf16 v[164:167], v[120:123], v[24:27], 0\n" \
    "s_waitcnt lgkmcnt(4)\n" \
    "v_mfma_f32_16x16x32_bf16 v[140:143], v[124:127], v[4:7], v[140:143]\n" \
    "v_mfma_f32_16x16x32_bf16 v[148:151], v[124:127], v[12:15], v[148:151]\n" \
    "v_mfma_f32_16x16x32_bf16 v[156:159], v[124:127], v[20:23], v[156:159]\n" \
    "v_mfma_f32_16x16x32_bf16 v[164:167], v[124:127], v[28:31], v[164:167]\n" \
    "s_waitcnt lgkmcnt(2)\n" \
    "v_mfma_f32_16x16x32_bf16 v[140:143], v[128:131], v[32:35], v[140:143]\n" \
    "v_mfma_f32_16x16x32_bf16 v[148:151], v[128:131], v[40:43], v[148:151]\n" \
    "v_mfma_f32_16x16x32_bf16 v[156:159], v[128:131], v[48:51], v[156:159]\n" \
    "v_mfma_f32_16x16x32_bf16 v[164:167], v[128:131], v[56:59], v[164:167]\n" \
    "s_waitcnt lgkmcnt(0)\n" \
    "v_mfma_f32_16x16x32_bf16 v[140:143], v[132:135], v[36:39], v[140:143]\n" \
    "v_mfma_f32_16x16x32_bf16 v[148:151], v[132:135], v[44:47], v[148:151]\n" \
    "v_mfma_f32_16x16x32_bf16 v[156:159], v[132:135], v[52:55], v[156:159]\n" \
    "v_mfma_f32_16x16x32_bf16 v[164:167], v[132:135], v[60:63], v[164:167]\n" \
    "v_cvt_pk_bf16_f32 v65, v136, v137\n" \
    "v_cvt_pk_bf16_f32 v68, v138, v139\n" \
    "v_cvt_pk_bf16_f32 v73, v144, v145\n" \
    "v_cvt_pk_bf16_f32 v76, v146, v147\n" \
    "v_cvt_pk_bf16_f32 v81, v152, v153\n" \
    "v_cvt_pk_bf16_f32 v84, v154, v155\n" \
    "v_cvt_pk_bf16_f32 v89, v160, v161\n" \
    "v_cvt_pk_bf16_f32 v92, v162, v163\n" \
    "v_cvt_pk_bf16_f32 v66, v142, v143\n" \
    "v_cvt_pk_bf16_f32 v67, v142, v143\n" \
    "v_cvt_pk_bf16_f32 v69, v140, v141\n" \
    "v_cvt_pk_bf16_f32 v74, v150, v151\n" \
    "v_cvt_pk_bf16_f32 v75, v150, v151\n" \
    "v_cvt_pk_bf16_f32 v77, v148, v149\n" \
    "v_cvt_pk_bf16_f32 v82, v158, v159\n" \
    "v_cvt_pk_bf16_f32 v83, v158, v159\n" \
    "v_cvt_pk_bf16_f32 v85, v156, v157\n" \
    "v_cvt_pk_bf16_f32 v90, v166, v167\n" \
    "v_cvt_pk_bf16_f32 v91, v166, v167\n" \
    "v_cvt_pk_bf16_f32 v93, v164, v165\n" \
    "v_permlane16_swap_b32 v66, v67\n" \
    "v_permlane16_swap_b32 v74, v75\n" \
    "v_permlane16_swap_b32 v82, v83\n" \
    "v_permlane16_swap_b32 v90, v91\n" \
    "v_mov_b64 v[176:177], 0\n" \
    "v_mov_b64 v[178:179], 0\n" \
    "v_mov_b64 v[180:181], 0\n" \
    "v_mov_b64 v[182:183], 0\n" \
    "v_mov_b64 v[184:185], 0\n" \
    "v_mov_b64 v[186:187], 0\n" \
    "v_mov_b64 v[188:189], 0\n" \
    "v_mov_b64 v[190:191], 0\n" \
    "v_mov_b64 v[196:197], 0\n" \
    "v_mov_b64 v[198:199], 0\n" \
    "v_mov_b64 v[200:201], 0\n" \
    "v_mov_b64 v[202:203], 0\n" \
    "v_mov_b64 v[204:205], 0\n" \
    "v_mov_b64 v[206:207], 0\n" \
    "v_mov_b64 v[208:209], 0\n" \
    "v_mov_b64 v[210:211], 0\n" \
    "v_mov_b64 v[212:213], 0\n" \
    "v_mov_b64 v[214:215], 0\n" \
    "s_add_u32 s85, s60, 0x1000\n" \
    "s_sub_u32 s74, s76, 2\n" \
    "s_mul_i32 s74, s74, 2\n" \
    "s_cmp_lt_u32 s74, 64\n" \
    "s_cbranch_scc0 L%=_Tnd\n" \
    "s_lshr_b32 s79, s74, 2\n" \
    "s_lshl_b32 s79, s79, 7\n" \
    "s_and_b32 s75, s74, 3\n" \
    "s_add_u32 s79, s79, s75\n" \
    "s_lshl_b32 s79, s79, 5\n" \
    "v_add_u32 v160, s79, v234\n" \
    "ds_read_b64_tr_b16 v[144:145], v160 offset:0\n" \
    "ds_read_b64_tr_b16 v[146:147], v160 offset:2048\n" \
    "ds_read_b64_tr_b16 v[148:149], v160 offset:32\n" \
    "ds_read_b64_tr_b16 v[150:151], v160 offset:2080\n" \
    "s_and_b32 s75, s74, 48\n" \
    "s_mul_i32 s75, s75, 31\n" \
    "s_and_b32 s79, s74, 8\n" \
    "s_add_u32 s75, s75, s79\n" \
    "s_add_u32 s75, s75, s74\n" \
    "s_lshl_b32 s75, s75, 10\n" \
    "s_branch L%=_Tu2b\n" \
    "L%=_Tu2a:\n" \
    "s_sleep 4\n" \
    "L%=_Tu2b:\n" \
    "s_memrealtime vcc\n" \
    "s_add_u32 s80, s77, s64\n" \
    "s_add_u32 s80, s80, s69\n" \
    "s_waitcnt lgkmcnt(0)\n" \
    "s_sub_i32 s79, s80, vcc_lo\n" \
    "s_sub_u32 s79, s79, 40\n" \
    "s_cmp_lt_u32 s79, 0x400\n" \
    "s_cbranch_scc1 L%=_Tu2a\n" \
    "s_memtime s[38:39]\n" \
    "s_memrealtime vcc\n" \
    "s_nop 4\n" \
    "s_memrealtime s[32:33]\n" \
    "s_nop 4\n" \
    "s_memrealtime s[34:35]\n" \
    "s_nop 4\n" \
    "s_memrealtime s[36:37]\n" \
    "s_add_u32 s80, s77, s64\n" \
    "s_lshl_b32 s80, s80, 2\n" \
    "s_waitcnt lgkmcnt(0)\n" \
    "s_sub_u32 s80, s80, vcc_lo\n" \
    "s_sub_u32 s80, s80, s32\n" \
    "s_sub_u32 s80, s80, s34\n" \
    "s_sub_u32 s80, s80, s36\n" \
    "s_mul_i32 s80, s80, s71\n" \
    "s_ashr_i32 s80, s80, 6\n" \
    "s_add_u32 s78, s80, s38\n" \
    "L%=_Tu3a:\n" \
    "s_memtime s[38:39]\n" \
    "s_mul_i32 s80, s69, 23\n" \
    "s_add_u32 s80, s80, s78\n" \
    "s_waitcnt lgkmcnt(0)\n" \
    "s_sub_i32 s79, s80, s38\n" \
    "s_max_i32 s79, s79, 0\n" \
    "s_cmp_gt_u32 s79, 320\n" \
    "s_cbranch_scc1 L%=_Tu3s\n" \
    ".p2align 4\n" \
    "L%=_Tu3d:\n" \
    "s_sub_u32 s79, s79, 28\n" \
    "s_cbranch_scc0 L%=_Tu3d\n" \
    "L%=_Tu3e:\n" \
    "buffer_store_dwordx4 v[144:147], v235, s[48:51], s75 offen offset:0 sc1\n" \
    "buffer_store_dwordx4 v[148:151], v235, s[48:51], s75 offen offset:1024 sc1\n" \
    "s_branch L%=_Tdd\n" \
    "L%=_Tnd:\n" \
    "s_branch L%=_Tu4b\n" \
    "L%=_Tu4a:\n" \
    "s_sleep 4\n" \
    "L%=_Tu4b:\n" \
    "s_memrealtime vcc\n" \
    "s_add_u32 s80, s77, s64\n" \
    "s_add_u32 s80, s80, s69\n" \
    "s_waitcnt lgkmcnt(0)\n" \
    "s_sub_i32 s79, s80, vcc_lo\n" \
    "s_sub_u32 s79, s79, 40\n" \
    "s_cmp_lt_u32 s79, 0x400\n" \
    "s_cbranch_scc1 L%=_Tu4a\n" \
    "s_memtime s[38:39]\n" \
    "s_memrealtime vcc\n" \
    "s_nop 4\n" \
    "s_memrealtime s[32:33]\n" \
    "s_nop 4\n" \
    "s_memrealtime s[34:35]\n" \
    "s_nop 4\n" \
    "s_memrealtime s[36:37]\n" \
    "s_add_u32 s80, s77, s64\n" \
    "s_lshl_b32 s80, s80, 2\n" \
    "s_waitcnt lgkmcnt(0)\n" \
    "s_sub_u32 s80, s80, vcc_lo\n" \
    "s_sub_u32 s80, s80, s32\n" \
    "s_sub_u32 s80, s80, s34\n" \
    "s_sub_u32 s80, s80, s36\n" \
    "s_mul_i32 s80, s80, s71\n" \
    "s_ashr_i32 s80, s80, 6\n" \
    "s_add_u32 s78, s80, s38\n" \
    "L%=_Tdd:\n" \
    "v_mov_b32 v143, s76\n" \
    "s_barrier\n" \
    "s_cmp_eq_u32 s76, 63\n" \
    "s_cbranch_scc1 L%=_Tfin\n" \
    "ds_read_b32 v216, v229\n" \
    "s_waitcnt lgkmcnt(0)\n" \
    "v_readfirstlane_b32 s94, v216\n" \
    "s_mul_i32 s79, s94, s70\n" \
    "s_add_u32 s79, s79, s81\n" \
    "s_cmp_lg_u32 s79, s77\n" \
    "s_cbranch_scc1 L%=_Tu5s\n" \
    "L%=_Tu5k:\n" \
    "s_setprio 1\n" \
    "L%=_Tu6a:\n" \
    "s_memtime s[38:39]\n" \
    "s_mul_hi_u32 s80, s66, s96\n" \
    "s_add_u32 s80, s80, s78\n" \
    "s_waitcnt lgkmcnt(0)\n" \
    "s_sub_i32 s79, s80, s38\n" \
    "s_max_i32 s79, s79, 0\n" \
    "s_cmp_gt_u32 s79, 320\n" \
    "s_cbranch_scc1 L%=_Tu6s\n" \
    ".p2align 4\n" \
    "L%=_Tu6d:\n" \
    "s_sub_u32 s79, s79, 28\n" \
    "s_cbranch_scc0 L%=_Tu6d\n" \
    "L%=_Tu6e:\n" \
    "buffer_load_dwordx4 v[104:107], v230, s[40:43], s60 offen offset:0 sc1\n" \
    "buffer_load_dwordx2 v[120:121], v231, s[40:43], s60 offen offset:0 sc1\n" \
    "buffer_load_dwordx4 v[108:111], v230, s[40:43], s60 offen offset:2048 sc1\n" \
    "buffer_load_dwordx2 v[122:123], v231, s[40:43], s60 offen offset:2048 sc1\n" \
    "buffer_load_dwordx4 v[112:115], v230, s[40:43], s85 offen offset:0 sc1\n" \
    "buffer_load_dwordx2 v[124:125], v231, s[40:43], s85 offen offset:0 sc1\n" \
    "buffer_load_dwordx4 v[116:119], v230, s[40:43], s85 offen offset:2048 sc1\n" \
    "buffer_load_dwordx2 v[126:127], v231, s[40:43], s85 offen offset:2048 sc1\n" \
    "s_setprio 3\n" \
    "buffer_load_dwordx4 v[168:171], v225, s[40:43], s60 offen sc1\n" \
    "buffer_load_dwordx4 v[172:175], v225, s[40:43], s85 offen sc1\n" \
    "L%=_Tsc:\n" \
    "s_waitcnt vmcnt(6)\n" \
    "v_pk_add_f32 v[128:129], v[104:105], v[108:109]\n" \
    "v_pk_add_f32 v[130:131], v[106:107], v[110:111]\n" \
    "v_pk_add_f32 v[132:133], v[120:121], v[122:123]\n" \
    "s_waitcnt vmcnt(4)\n" \
    "v_pk_add_f32 v[128:129], v[128:129], v[112:113]\n" \
    "v_pk_add_f32 v[130:131], v[130:131], v[114:115]\n" \
    "v_pk_add_f32 v[132:133], v[132:133], v[124:125]\n" \
    "s_waitcnt vmcnt(2)\n" \
    "v_pk_add_f32 v[128:129], v[128:129], v[116:117]\n" \
    "v_pk_add_f32 v[130:131], v[130:131], v[118:119]\n" \
    "v_pk_add_f32 v[132:133], v[132:133], v[126:127]\n" \
    "v_max3_f32 v136, v128, v129, v130\n" \
    "v_max3_f32 v137, v131, v132, v133\n" \
    "v_max_f32 v136, v136, v137\n" \
    "v_fma_f32 v141, v131, v132, v133\n" \
    "v_fma_f32 v140, v128, v129, v130\n" \
    "v_max_f32_dpp v138, v136, v136 row_ror:8 row_mask:0xf bank_mask:0xf\n" \
    "v_max_f32_dpp v139, v136, v136 row_ror:8 row_mask:0xf bank_mask:0xf\n" \
    "s_waitcnt vmcnt(0)\n" \
    "v_smfmac_f32_16x16x64_bf16 v[176:179], v[168:171], v[0:7], v224\n" \
    "v_cmp_u_f32 vcc, v140, v141\n" \
    "v_permlane16_swap_b32 v138, v139\n" \
    "v_max_f32 v136, v138, v139\n" \
    "v_max_f32 v137, v138, v139\n" \
    "v_smfmac_f32_16x16x64_bf16 v[180:183], v[168:171], v[8:15], v224\n" \
    "s_cbranch_vccnz L%=_Tslow\n" \
    "L%=_Tok:\n" \
    "v_permlane32_swap_b32 v136, v137\n" \
    "v_max_f32 v136, v136, v137\n" \
    "v_mul_f32 v136, s72, v136\n" \
    "v_pk_fma_f32 v[128:129], v[128:129], s[72:73], v[136:137] op_sel_hi:[1,1,0] neg_lo:[0,0,1] neg_hi:[0,0,1]\n" \
    "v_pk_fma_f32 v[132:133], v[132:133], s[72:73], v[136:137] op_sel_hi:[1,1,0] neg_lo:[0,0,1] neg_hi:[0,0,1]\n" \
    "v_pk_fma_f32 v[130:131], v[130:131], s[72:73], v[136:137] op_sel_hi:[1,1,0] neg_lo:[0,0,1] neg_hi:[0,0,1]\n" \
    "v_exp_f32 v128, v128\n" \
    "v_exp_f32 v129, v129\n" \
    "v_exp_f32 v132, v132\n" \
    "v_exp_f32 v133, v133\n" \
    "v_exp_f32 v130, v130\n" \
    "v_exp_f32 v131, v131\n" \
    "v_cvt_pk_bf16_f32 v140, v128, v129\n" \
    "v_cvt_pk_bf16_f32 v141, v132, v133\n" \
    "v_cvt_pk_bf16_f32 v142, v130, v131\n" \
    "ds_write_b128 v232, v[140:143]\n" \
    "s_barrier\n" \
    "ds_read_b128 v[192:195], v227\n" \
    "s_waitcnt vmcnt(0)\n" \
    "v_smfmac_f32_16x16x64_bf16 v[176:179], v[172:175], v[32:39], v224\n" \
    "L%=_Tvc:\n" \
    "v_smfmac_f32_16x16x64_bf16 v[180:183], v[172:175], v[40:47], v224\n" \
    "v_smfmac_f32_16x16x64_bf16 v[184:187], v[168:171], v[16:23], v224\n" \
    "v_add_f32 v176, v176, v178\n" \
    "v_smfmac_f32_16x16x64_bf16 v[188:191], v[168:171], v[24:31], v224\n" \
    "v_add_f32 v177, v177, v179\n" \
    "v_smfmac_f32_16x16x64_bf16 v[184:187], v[172:175], v[48:55], v224\n" \
    "v_cmp_u_f32 vcc, v176, v177\n" \
    "v_smfmac_f32_16x16x64_bf16 v[188:191], v[172:175], v[56:63], v224\n" \
    "s_cbranch_vccnz L%=_Tvslow\n" \
    "L%=_Tvok:\n" \
    "v_cvt_pk_bf16_f32 v64, v176, v177\n" \
    "v_add_f32 v180, v180, v182\n" \
    "s_waitcnt lgkmcnt(0)\n" \
    "v_smfmac_f32_16x16x64_bf16 v[212:215], v[192:195], v[96:103], v224\n" \
    "v_add_f32 v181, v181, v183\n" \
    "v_smfmac_f32_16x16x64_bf16 v[196:199], v[192:195], v[64:71], v224\n" \
    "v_cvt_pk_bf16_f32 v72, v180, v181\n" \
    "v_add_f32 v184, v184, v186\n" \
    "v_add_f32 v185, v185, v187\n" \
    "v_smfmac_f32_16x16x64_bf16 v[200:203], v[192:195], v[72:79], v224\n" \
    "v_cvt_pk_bf16_f32 v80, v184, v185\n" \
    "v_add_f32 v188, v188, v190\n" \
    "v_add_f32 v189, v189, v191\n" \
    "v_smfmac_f32_16x16x64_bf16 v[204:207], v[192:195], v[80:87], v224\n" \
    "v_cvt_pk_bf16_f32 v88, v188, v189\n" \
    "v_add_f32 v212, v212, v214\n" \
    "v_add_f32 v213, v213, v215\n" \
    "v_smfmac_f32_16x16x64_bf16 v[208:211], v[192:195], v[88:95], v224\n" \
    "v_rcp_f32 v220, v212\n" \
    "v_rcp_f32 v221, v213\n" \
    "v_add_f32 v196, v196, v198\n" \
    "v_add_f32 v197, v197, v199\n" \
    "v_pk_add_f32 v[200:201], v[200:201], v[202:203]\n" \
    "v_pk_add_f32 v[204:205], v[204:205], v[206:207]\n" \
    "s_nop 1\n" \
    "v_pk_add_f32 v[208:209], v[208:209], v[210:211]\n" \
    "v_pk_mul_f32 v[196:197], v[196:197], v[220:221]\n" \
    "v_pk_mul_f32 v[200:201], v[200:201], v[220:221]\n" \
    "v_pk_mul_f32 v[204:205], v[204:205], v[220:221]\n" \
    "v_pk_mul_f32 v[208:209], v[208:209], v[220:221]\n" \
    "v_cvt_pk_bf16_f32 v216, v196, v200\n" \
    "v_cvt_pk_bf16_f32 v217, v204, v208\n" \
    "v_cvt_pk_bf16_f32 v218, v197, v201\n" \
    "v_cvt_pk_bf16_f32 v219, v205, v209\n" \
    "buffer_store_dwordx4 v[216:219], v226, s[40:43], s60 offen sc1\n" \
    "s_setprio 0\n" \
    "s_cmp_ge_u32 s76, 63\n" \
    "s_addc_u32 s79, s76, 0\n" \
    "v_mov_b32 v143, s79\n" \
    "buffer_store_dword v143, v233, s[44:47], 0 offen sc1\n" \
    "ds_read_b64 v[220:221], v229 offset:64\n" \
    "s_mov_b32 s79, s60\n" \
    "s_mov_b32 s60, s61\n" \
    "s_mov_b32 s61, s62\n" \
    "s_mov_b32 s62, s63\n" \
    "s_mov_b32 s63, s79\n" \
    "s_add_u32 s76, s76, 1\n" \
    "s_add_u32 s84, s84, 0x20000\n" \
    "s_add_u32 s77, s77, s65\n" \
    "s_add_u32 s81, s81, s65\n" \
    "s_waitcnt lgkmcnt(0)\n" \
    "v_readfirstlane_b32 s65, v220\n" \
    "v_readfirstlane_b32 s71, v221\n" \
    "s_mul_i32 s96, s65, s71\n" \
    "s_lshr_b32 s96, s96, 4\n" \
    "s_branch L%=_Tloop\n" \
    "L%=_Tslow:\n" \
    "s_nop 7\n" \
    "s_nop 0\n" \
    "v_max_u32 v138, v104, v105\n" \
    "v_max3_u32 v138, v138, v106, v107\n" \
    "v_max3_u32 v138, v138, v108, v109\n" \
    "v_max3_u32 v138, v138, v110, v111\n" \
    "v_max3_u32 v138, v138, v112, v113\n" \
    "v_max3_u32 v138, v138, v114, v115\n" \
    "v_max3_u32 v138, v138, v116, v117\n" \
    "v_max3_u32 v138, v138, v118, v119\n" \
    "v_max3_u32 v138, v138, v120, v121\n" \
    "v_max3_u32 v138, v138, v122, v123\n" \
    "v_max3_u32 v138, v138, v124, v125\n" \
    "v_max3_u32 v138, v138, v126, v127\n" \
    "v_cmp_eq_u32 vcc, -1, v138\n" \
    "s_cbranch_vccz L%=_Tok\n" \
    "s_cmp_eq_u32 s99, 0\n" \
    "s_cbranch_scc1 L%=_Tok\n" \
    "s_sub_u32 s99, s99, 1\n" \
    "s_cmp_lg_u32 s99, 0\n" \
    "s_cbranch_scc1 L%=_Tu7\n" \
    "v_mov_b32 v138, 1\n" \
    "buffer_store_dword v138, off, s[44:47], 0 sc1\n" \
    "L%=_Tu7:\n" \
    "s_sleep 1\n" \
    "buffer_load_dwordx4 v[104:107], v230, s[40:43], s60 offen offset:0 sc1\n" \
    "buffer_load_dwordx2 v[120:121], v231, s[40:43], s60 offen offset:0 sc1\n" \
    "buffer_load_dwordx4 v[108:111], v230, s[40:43], s60 offen offset:2048 sc1\n" \
    "buffer_load_dwordx2 v[122:123], v231, s[40:43], s60 offen offset:2048 sc1\n" \
    "buffer_load_dwordx4 v[112:115], v230, s[40:43], s85 offen offset:0 sc1\n" \
    "buffer_load_dwordx2 v[124:125], v231, s[40:43], s85 offen offset:0 sc1\n" \
    "buffer_load_dwordx4 v[116:119], v230, s[40:43], s85 offen offset:2048 sc1\n" \
    "buffer_load_dwordx2 v[126:127], v231, s[40:43], s85 offen offset:2048 sc1\n" \
    "buffer_load_dwordx4 v[168:171], v225, s[40:43], s60 offen sc1\n" \
    "buffer_load_dwordx4 v[172:175], v225, s[40:43], s85 offen sc1\n" \
    "v_mov_b64 v[176:177], 0\n" \
    "v_mov_b64 v[178:179], 0\n" \
    "v_mov_b64 v[180:181], 0\n" \
    "v_mov_b64 v[182:183], 0\n" \
    "v_mov_b64 v[184:185], 0\n" \
    "v_mov_b64 v[186:187], 0\n" \
    "v_mov_b64 v[188:189], 0\n" \
    "v_mov_b64 v[190:191], 0\n" \
    "s_branch L%=_Tsc\n" \
    "L%=_Tvslow:\n" \
    "s_nop 7\n" \
    "s_nop 0\n" \
    "v_max_u32 v136, v168, v169\n" \
    "v_max3_u32 v136, v136, v170, v171\n" \
    "v_max3_u32 v136, v136, v172, v173\n" \
    "v_max3_u32 v136, v136, v174, v175\n" \
    "v_cmp_eq_u32 vcc, -1, v136\n" \
    "s_cbranch_vccz L%=_Tvok\n" \
    "s_cmp_eq_u32 s99, 0\n" \
    "s_cbranch_scc1 L%=_Tvok\n" \
    "s_sub_u32 s99, s99, 1\n" \
    "s_cmp_lg_u32 s99, 0\n" \
    "s_cbranch_scc1 L%=_Tu8\n" \
    "v_mov_b32 v136, 1\n" \
    "buffer_store_dword v136, off, s[44:47], 0 sc1\n" \
    "L%=_Tu8:\n" \
    "s_sleep 1\n" \
    "buffer_load_dwordx4 v[168:171], v225, s[40:43], s60 offen sc1\n" \
    "buffer_load_dwordx4 v[172:175], v225, s[40:43], s85 offen sc1\n" \
    "v_mov_b64 v[176:177], 0\n" \
    "v_mov_b64 v[178:179], 0\n" \
    "v_mov_b64 v[180:181], 0\n" \
    "v_mov_b64 v[182:183], 0\n" \
    "v_mov_b64 v[184:185], 0\n" \
    "v_mov_b64 v[186:187], 0\n" \
    "v_mov_b64 v[188:189], 0\n" \
    "v_mov_b64 v[190:191], 0\n" \
    "v_mov_b64 v[196:197], 0\n" \
    "v_mov_b64 v[198:199], 0\n" \
    "v_mov_b64 v[200:201], 0\n" \
    "v_mov_b64 v[202:203], 0\n" \
    "v_mov_b64 v[204:205], 0\n" \
    "v_mov_b64 v[206:207], 0\n" \
    "v_mov_b64 v[208:209], 0\n" \
    "v_mov_b64 v[210:211], 0\n" \
    "v_mov_b64 v[212:213], 0\n" \
    "v_mov_b64 v[214:215], 0\n" \
    "s_waitcnt vmcnt(0)\n" \
    "v_smfmac_f32_16x16x64_bf16 v[176:179], v[168:171], v[0:7], v224\n" \
    "v_smfmac_f32_16x16x64_bf16 v[180:183], v[168:171], v[8:15], v224\n" \
    "v_smfmac_f32_16x16x64_bf16 v[176:179], v[172:175], v[32:39], v224\n" \
    "s_nop 7\n" \
    "s_nop 0\n" \
    "s_branch L%=_Tvc\n" \
    "L%=_Tfin:\n" \
    "v_mov_b32 v143, 64\n" \
    "s_mov_b32 s98, s81\n" \
    "s_barrier\n" \
    "buffer_store_dword v143, v233, s[44:47], 0 offen sc1\n" \
    "s_branch L%=_end\n" \
    "L%=_Tu3s:\n" \
    "s_cmp_gt_u32 s79, 0x8000\n" \
    "s_cbranch_scc1 L%=_Tu3e\n" \
    "s_sleep 2\n" \
    "s_branch L%=_Tu3a\n" \
    "L%=_Tu5s:\n" \
    "s_mov_b32 s77, s79\n" \
    "s_memtime s[38:39]\n" \
    "s_memrealtime vcc\n" \
    "s_nop 4\n" \
    "s_memrealtime s[32:33]\n" \
    "s_nop 4\n" \
    "s_memrealtime s[34:35]\n" \
    "s_nop 4\n" \
    "s_memrealtime s[36:37]\n" \
    "s_add_u32 s80, s77, s64\n" \
    "s_lshl_b32 s80, s80, 2\n" \
    "s_waitcnt lgkmcnt(0)\n" \
    "s_sub_u32 s80, s80, vcc_lo\n" \
    "s_sub_u32 s80, s80, s32\n" \
    "s_sub_u32 s80, s80, s34\n" \
    "s_sub_u32 s80, s80, s36\n" \
    "s_mul_i32 s80, s80, s71\n" \
    "s_ashr_i32 s80, s80, 6\n" \
    "s_add_u32 s78, s80, s38\n" \
    "s_branch L%=_Tu5k\n" \
    "L%=_Tu6s:\n" \
    "s_cmp_gt_u32 s79, 0x8000\n" \
    "s_cbranch_scc1 L%=_Tu6e\n" \
    "s_sleep 2\n" \
    "s_branch L%=_Tu6a\n" \
    "L%=_S:\n" \
    "s_mov_b32 s76, 0\n" \
    "s_mov_b32 s99, 0x20000\n" \
    "s_mov_b32 s84, 0\n" \
    "s_mov_b32 s81, 0\n" \
    "s_mov_b32 s86, 0\n" \
    "s_mov_b32 s87, 0\n" \
    "s_mov_b32 s88, 0\n" \
    "s_mov_b32 s89, 0\n" \
    "s_mov_b32 s90, 0\n" \
    "s_mov_b32 s91, 0\n" \
    "s_mov_b32 s92, 0\n" \
    "s_mov_b32 s93, 0\n" \
    "s_mov_b32 s94, 0\n" \
    "s_mov_b32 s95, 0\n" \
    "s_mov_b32 s96, 0\n" \
    "s_mov_b32 s97, 0\n" \
    "s_mov_b32 s98, 0\n" \
    "ds_read_b32 v216, v229\n" \
    "s_waitcnt lgkmcnt(0)\n" \
    "v_readfirstlane_b32 s94, v216\n" \
    "s_mul_i32 s79, s94, s70\n" \
    "s_add_u32 s79, s79, s81\n" \
    "s_mov_b32 s77, s79\n" \
    "s_mul_i32 s96, s65, s71\n" \
    "s_lshr_b32 s96, s96, 4\n" \
    "v_mov_b32 v96, 0x3f803f80\n" \
    "v_mov_b32 v97, 0x3f803f80\n" \
    "v_mov_b32 v98, 0x3f803f80\n" \
    "v_mov_b32 v99, 0x3f803f80\n" \
    "v_mov_b32 v100, 0x3f803f80\n" \
    "v_mov_b32 v101, 0x3f803f80\n" \
    "v_mov_b64 v[102:103], 0\n" \
    "v_mov_b64 v[64:65], 0\n" \
    "v_mov_b64 v[66:67], 0\n" \
    "v_mov_b64 v[68:69], 0\n" \
    "v_mov_b64 v[70:71], 0\n" \
    "v_mov_b64 v[72:73], 0\n" \
    "v_mov_b64 v[74:75], 0\n" \
    "v_mov_b64 v[76:77], 0\n" \
    "v_mov_b64 v[78:79], 0\n" \
    "v_mov_b64 v[80:81], 0\n" \
    "v_mov_b64 v[82:83], 0\n" \
    "v_mov_b64 v[84:85], 0\n" \
    "v_mov_b64 v[86:87], 0\n" \
    "v_mov_b64 v[88:89], 0\n" \
    "v_mov_b64 v[90:91], 0\n" \
    "v_mov_b64 v[92:93], 0\n" \
    "v_mov_b64 v[94:95], 0\n" \
    "v_mov_b64 v[192:193], 0\n" \
    "v_mov_b64 v[194:195], 0\n" \
    "L%=_Sloop:\n" \
    "ds_read_b64_tr_b16 v[104:105], v228 offset:16384\n" \
    "ds_read_b64_tr_b16 v[106:107], v228 offset:16416\n" \
    "ds_read_b64_tr_b16 v[108:109], v228 offset:18688\n" \
    "ds_read_b64_tr_b16 v[110:111], v228 offset:18720\n" \
    "ds_read_b64_tr_b16 v[112:113], v228 offset:20992\n" \
    "ds_read_b64_tr_b16 v[114:115], v228 offset:21024\n" \
    "ds_read_b64_tr_b16 v[116:117], v228 offset:23296\n" \
    "ds_read_b64_tr_b16 v[118:119], v228 offset:23328\n" \
    "ds_read_b64_tr_b16 v[120:121], v228 offset:16656\n" \
    "ds_read_b64_tr_b16 v[122:123], v228 offset:16688\n" \
    "ds_read_b64_tr_b16 v[124:125], v228 offset:18960\n" \
    "ds_read_b64_tr_b16 v[126:127], v228 offset:18992\n" \
    "ds_read_b64_tr_b16 v[128:129], v228 offset:21264\n" \
    "ds_read_b64_tr_b16 v[130:131], v228 offset:21296\n" \
    "ds_read_b64_tr_b16 v[132:133], v228 offset:23568\n" \
    "ds_read_b64_tr_b16 v[134:135], v228 offset:23600\n" \
    "s_waitcnt lgkmcnt(14)\n" \
    "v_mfma_f32_16x16x32_bf16 v[136:139], v[104:107], v[0:3], 0\n" \
    "v_mfma_f32_16x16x32_bf16 v[144:147], v[104:107], v[8:11], 0\n" \
    "v_mfma_f32_16x16x32_bf16 v[152:155], v[104:107], v[16:19], 0\n" \
    "v_mfma_f32_16x16x32_bf16 v[160:163], v[104:107], v[24:27], 0\n" \
    "s_waitcnt lgkmcnt(12)\n" \
    "v_mfma_f32_16x16x32_bf16 v[136:139], v[108:111], v[4:7], v[136:139]\n" \
    "v_mfma_f32_16x16x32_bf16 v[144:147], v[108:111], v[12:15], v[144:147]\n" \
    "v_mfma_f32_16x16x32_bf16 v[152:155], v[108:111], v[20:23], v[152:155]\n" \
    "v_mfma_f32_16x16x32_bf16 v[160:163], v[108:111], v[28:31], v[160:163]\n" \
    "s_waitcnt lgkmcnt(10)\n" \
    "v_mfma_f32_16x16x32_bf16 v[136:139], v[112:115], v[32:35], v[136:139]\n" \
    "v_mfma_f32_16x16x32_bf16 v[144:147], v[112:115], v[40:43], v[144:147]\n" \
    "v_mfma_f32_16x16x32_bf16 v[152:155], v[112:115], v[48:51], v[152:155]\n" \
    "v_mfma_f32_16x16x32_bf16 v[160:163], v[112:115], v[56:59], v[160:163]\n" \
    "s_waitcnt lgkmcnt(8)\n" \
    "v_mfma_f32_16x16x32_bf16 v[136:139], v[116:119], v[36:39], v[136:139]\n" \
    "v_mfma_f32_16x16x32_bf16 v[144:147], v[116:119], v[44:47], v[144:147]\n" \
    "v_mfma_f32_16x16x32_bf16 v[152:155], v[116:119], v[52:55], v[152:155]\n" \
    "v_mfma_f32_16x16x32_bf16 v[160:163], v[116:119], v[60:63], v[160:163]\n" \
    "s_waitcnt lgkmcnt(6)\n" \
    "v_mfma_f32_16x16x32_bf16 v[140:143], v[120:123], v[0:3], 0\n" \
    "v_mfma_f32_16x16x32_bf16 v[148:151], v[120:123], v[8:11], 0\n" \
    "v_mfma_f32_16x16x32_bf16 v[156:159], v[120:123], v[16:19], 0\n" \
    "v_mfma_f32_16x16x32_bf16 v[164:167], v[120:123], v[24:27], 0\n" \
    "s_waitcnt lgkmcnt(4)\n" \
    "v_mfma_f32_16x16x32_bf16 v[140:143], v[124:127], v[4:7], v[140:143]\n" \
    "v_mfma_f32_16x16x32_bf16 v[148:151], v[124:127], v[12:15], v[148:151]\n" \
    "v_mfma_f32_16x16x32_bf16 v[156:159], v[124:127], v[20:23], v[156:159]\n" \
    "v_mfma_f32_16x16x32_bf16 v[164:167], v[124:127], v[28:31], v[164:167]\n" \
    "s_waitcnt lgkmcnt(2)\n" \
    "v_mfma_f32_16x16x32_bf16 v[140:143], v[128:131], v[32:35], v[140:143]\n" \
    "v_mfma_f32_16x16x32_bf16 v[148:151], v[128:131], v[40:43], v[148:151]\n" \
    "v_mfma_f32_16x16x32_bf16 v[156:159], v[128:131], v[48:51], v[156:159]\n" \
    "v_mfma_f32_16x16x32_bf16 v[164:167], v[128:131], v[56:59], v[164:167]\n" \
    "s_waitcnt lgkmcnt(0)\n" \
    "v_mfma_f32_16x16x32_bf16 v[140:143], v[132:135], v[36:39], v[140:143]\n" \
    "v_mfma_f32_16x16x32_bf16 v[148:151], v[132:135], v[44:47], v[148:151]\n" \
    "v_mfma_f32_16x16x32_bf16 v[156:159], v[132:135], v[52:55], v[156:159]\n" \
    "v_mfma_f32_16x16x32_bf16 v[164:167], v[132:135], v[60:63], v[164:167]\n" \
    "v_cvt_pk_bf16_f32 v65, v136, v137\n" \
    "v_cvt_pk_bf16_f32 v68, v138, v139\n" \
    "v_cvt_pk_bf16_f32 v73, v144, v145\n" \
    "v_cvt_pk_bf16_f32 v76, v146, v147\n" \
    "v_cvt_pk_bf16_f32 v81, v152, v153\n" \
    "v_cvt_pk_bf16_f32 v84, v154, v155\n" \
    "v_cvt_pk_bf16_f32 v89, v160, v161\n" \
    "v_cvt_pk_bf16_f32 v92, v162, v163\n" \
    "v_cvt_pk_bf16_f32 v66, v142, v143\n" \
    "v_cvt_pk_bf16_f32 v67, v142, v143\n" \
    "v_cvt_pk_bf16_f32 v69, v140, v141\n" \
    "v_cvt_pk_bf16_f32 v74, v150, v151\n" \
    "v_cvt_pk_bf16_f32 v75, v150, v151\n" \
    "v_cvt_pk_bf16_f32 v77, v148, v149\n" \
    "v_cvt_pk_bf16_f32 v82, v158, v159\n" \
    "v_cvt_pk_bf16_f32 v83, v158, v159\n" \
    "v_cvt_pk_bf16_f32 v85, v156, v157\n" \
    "v_cvt_pk_bf16_f32 v90, v166, v167\n" \
    "v_cvt_pk_bf16_f32 v91, v166, v167\n" \
    "v_cvt_pk_bf16_f32 v93, v164, v165\n" \
    "v_permlane16_swap_b32 v66, v67\n" \
    "v_permlane16_swap_b32 v74, v75\n" \
    "v_permlane16_swap_b32 v82, v83\n" \
    "v_permlane16_swap_b32 v90, v91\n" \
    "ds_read_b128 v[240:243], v234 offset:25600\n" \
    "ds_read_b128 v[244:247], v234 offset:26624\n" \
    "v_mov_b64 v[176:177], 0\n" \
    "v_mov_b64 v[178:179], 0\n" \
    "v_mov_b64 v[180:181], 0\n" \
    "v_mov_b64 v[182:183], 0\n" \
    "v_mov_b64 v[184:185], 0\n" \
    "v_mov_b64 v[186:187], 0\n" \
    "v_mov_b64 v[188:189], 0\n" \
    "v_mov_b64 v[190:191], 0\n" \
    "v_mov_b64 v[196:197], 0\n" \
    "v_mov_b64 v[198:199], 0\n" \
    "v_mov_b64 v[200:201], 0\n" \
    "v_mov_b64 v[202:203], 0\n" \
    "v_mov_b64 v[204:205], 0\n" \
    "v_mov_b64 v[206:207], 0\n" \
    "v_mov_b64 v[208:209], 0\n" \
    "v_mov_b64 v[210:211], 0\n" \
    "v_mov_b64 v[212:213], 0\n" \
    "v_mov_b64 v[214:215], 0\n" \
    "s_add_u32 s85, s60, 0x1000\n" \
    "s_branch L%=_Su2b\n" \
    "L%=_Su2a:\n" \
    "s_sleep 4\n" \
    "L%=_Su2b:\n" \
    "s_memrealtime vcc\n" \
    "s_add_u32 s80, s77, s64\n" \
    "s_add_u32 s80, s80, s69\n" \
    "s_waitcnt lgkmcnt(0)\n" \
    "s_sub_i32 s79, s80, vcc_lo\n" \
    "s_sub_u32 s79, s79, 40\n" \
    "s_cmp_lt_u32 s79, 0x400\n" \
    "s_cbranch_scc1 L%=_Su2a\n" \
    "s_memtime s[38:39]\n" \
    "s_memrealtime vcc\n" \
    "s_nop 4\n" \
    "s_memrealtime s[32:33]\n" \
    "s_nop 4\n" \
    "s_memrealtime s[34:35]\n" \
    "s_nop 4\n" \
    "s_memrealtime s[36:37]\n" \
    "s_add_u32 s80, s77, s64\n" \
    "s_lshl_b32 s80, s80, 2\n" \
    "s_waitcnt lgkmcnt(0)\n" \
    "s_sub_u32 s80, s80, vcc_lo\n" \
    "s_sub_u32 s80, s80, s32\n" \
    "s_sub_u32 s80, s80, s34\n" \
    "s_sub_u32 s80, s80, s36\n" \
    "s_mul_i32 s80, s80, s71\n" \
    "s_ashr_i32 s80, s80, 6\n" \
    "s_add_u32 s78, s80, s38\n" \
    "L%=_Su3a:\n" \
    "s_memtime s[38:39]\n" \
    "s_mul_i32 s80, s69, 23\n" \
    "s_add_u32 s80, s80, s78\n" \
    "s_waitcnt lgkmcnt(0)\n" \
    "s_sub_i32 s79, s80, s38\n" \
    "s_max_i32 s79, s79, 0\n" \
    "s_cmp_gt_u32 s79, 320\n" \
    "s_cbranch_scc1 L%=_Su3s\n" \
    ".p2align 4\n" \
    "L%=_Su3d:\n" \
    "s_sub_u32 s79, s79, 28\n" \
    "s_cbranch_scc0 L%=_Su3d\n" \
    "L%=_Su3e:\n" \
    "s_sub_u32 s79, s76, 1\n" \
    "s_cmp_lt_u32 s79, 63\n" \
    "s_cselect_b32 s79, s84, 0x40020000\n" \
    "s_sub_u32 s79, s79, 0x20000\n" \
    "buffer_store_dwordx4 v[236:239], v233, s[48:51], s79 offen sc1\n" \
    "s_setprio 3\n" \
    "s_barrier\n" \
    "L%=_Srd:\n" \
    "ds_read_b128 v[136:139], v229 offset:32\n" \
    "ds_read_b128 v[104:107], v230 offset:0\n" \
    "ds_read_b128 v[108:111], v230 offset:2176\n" \
    "ds_read_b128 v[112:115], v230 offset:4352\n" \
    "ds_read_b128 v[116:119], v230 offset:6528\n" \
    "ds_read_b128 v[120:123], v230 offset:1088\n" \
    "ds_read_b128 v[124:127], v230 offset:3264\n" \
    "ds_read_b128 v[128:131], v230 offset:5440\n" \
    "ds_read_b128 v[132:135], v230 offset:7616\n" \
    "s_waitcnt lgkmcnt(8)\n" \
    "v_min3_u32 v136, v136, v137, v138\n" \
    "v_min_u32 v136, v136, v139\n" \
    "v_cmp_ge_u32 vcc, s76, v136\n" \
    "s_cbranch_vccnz L%=_Srd\n" \
    "s_waitcnt lgkmcnt(6)\n" \
    "v_pk_add_f32 v[104:105], v[104:105], v[108:109]\n" \
    "v_pk_add_f32 v[106:107], v[106:107], v[110:111]\n" \
    "s_waitcnt lgkmcnt(4)\n" \
    "v_pk_add_f32 v[112:113], v[112:113], v[116:117]\n" \
    "v_pk_add_f32 v[114:115], v[114:115], v[118:119]\n" \
    "v_pk_add_f32 v[104:105], v[104:105], v[112:113]\n" \
    "v_pk_add_f32 v[106:107], v[106:107], v[114:115]\n" \
    "v_cvt_pk_bf16_f32 v236, v104, v105\n" \
    "v_cvt_pk_bf16_f32 v237, v106, v107\n" \
    "s_waitcnt lgkmcnt(2)\n" \
    "v_pk_add_f32 v[120:121], v[120:121], v[124:125]\n" \
    "v_pk_add_f32 v[122:123], v[122:123], v[126:127]\n" \
    "s_waitcnt lgkmcnt(0)\n" \
    "v_pk_add_f32 v[128:129], v[128:129], v[132:133]\n" \
    "v_pk_add_f32 v[130:131], v[130:131], v[134:135]\n" \
    "v_pk_add_f32 v[120:121], v[120:121], v[128:129]\n" \
    "v_pk_add_f32 v[122:123], v[122:123], v[130:131]\n" \
    "v_cvt_pk_bf16_f32 v238, v120, v121\n" \
    "v_cvt_pk_bf16_f32 v239, v122, v123\n" \
    "s_nop 1\n" \
    "v_mfma_f32_16x16x32_bf16 v[140:143], v[240:243], v[236:239], 0\n" \
    "v_mfma_f32_16x16x32_bf16 v[144:147], v[244:247], v[236:239], 0\n" \
    "v_mfma_f32_16x16x32_bf16 v[148:151], v[236:239], v[236:239], 0\n" \
    "buffer_store_dwordx4 v[140:143], v231, s[40:43], s60 offen sc1\n" \
    "s_nop 2\n" \
    "buffer_store_dwordx4 v[144:147], v231, s[40:43], s60 offen offset:512 sc1\n" \
    "s_nop 2\n" \
    "buffer_store_dwordx4 v[148:151], v232, s[40:43], s60 offen sc1\n" \
    "s_cmp_eq_u32 s76, 63\n" \
    "s_cbranch_scc1 L%=_Sfin\n" \
    "s_cmp_lt_u32 s76, 63\n" \
    "s_cbranch_scc1 L%=_Sne\n" \
    "s_waitcnt lgkmcnt(0)\n" \
    "buffer_store_dwordx4 v[236:239], v233, s[48:51], s84 offen sc1\n" \
    "L%=_Sne:\n" \
    "ds_read_b32 v216, v229\n" \
    "s_waitcnt lgkmcnt(0)\n" \
    "v_readfirstlane_b32 s94, v216\n" \
    "s_mul_i32 s79, s94, s70\n" \
    "s_add_u32 s79, s79, s81\n" \
    "s_cmp_lg_u32 s79, s77\n" \
    "s_cbranch_scc1 L%=_Su4s\n" \
    "L%=_Su4k:\n" \
    "L%=_Su5a:\n" \
    "s_memtime s[38:39]\n" \
    "s_mul_hi_u32 s80, s67, s96\n" \
    "s_add_u32 s80, s80, s78\n" \
    "s_waitcnt lgkmcnt(0)\n" \
    "s_sub_i32 s79, s80, s38\n" \
    "s_max_i32 s79, s79, 0\n" \
    "s_cmp_gt_u32 s79, 320\n" \
    "s_cbranch_scc1 L%=_Su5s\n" \
    ".p2align 4\n" \
    "L%=_Su5d:\n" \
    "s_sub_u32 s79, s79, 28\n" \
    "s_cbranch_scc0 L%=_Su5d\n" \
    "L%=_Su5e:\n" \
    "buffer_load_dwordx4 v[168:171], v225, s[40:43], s60 offen sc1\n" \
    "buffer_load_dwordx4 v[172:175], v225, s[40:43], s85 offen sc1\n" \
    "L%=_Svc:\n" \
    "s_waitcnt vmcnt(1)\n" \
    "v_smfmac_f32_16x16x64_bf16 v[176:179], v[168:171], v[0:7], v224\n" \
    "v_smfmac_f32_16x16x64_bf16 v[180:183], v[168:171], v[8:15], v224\n" \
    "s_waitcnt vmcnt(0)\n" \
    "v_smfmac_f32_16x16x64_bf16 v[176:179], v[172:175], v[32:39], v224\n" \
    "v_smfmac_f32_16x16x64_bf16 v[180:183], v[172:175], v[40:47], v224\n" \
    "v_smfmac_f32_16x16x64_bf16 v[184:187], v[168:171], v[16:23], v224\n" \
    "v_smfmac_f32_16x16x64_bf16 v[188:191], v[168:171], v[24:31], v224\n" \
    "v_add_f32 v176, v176, v178\n" \
    "v_smfmac_f32_16x16x64_bf16 v[184:187], v[172:175], v[48:55], v224\n" \
    "v_add_f32 v177, v177, v179\n" \
    "v_smfmac_f32_16x16x64_bf16 v[188:191], v[172:175], v[56:63], v224\n" \
    "v_cmp_u_f32 vcc, v176, v177\n" \
    "v_cvt_pk_bf16_f32 v64, v176, v177\n" \
    "v_add_f32 v180, v180, v182\n" \
    "v_add_f32 v181, v181, v183\n" \
    "v_cvt_pk_bf16_f32 v72, v180, v181\n" \
    "s_cbranch_vccnz L%=_Svslow\n" \
    "L%=_Svok:\n" \
    "v_add_f32 v184, v184, v186\n" \
    "v_add_f32 v185, v185, v187\n" \
    "v_cvt_pk_bf16_f32 v80, v184, v185\n" \
    "v_add_f32 v188, v188, v190\n" \
    "v_add_f32 v189, v189, v191\n" \
    "v_cvt_pk_bf16_f32 v88, v188, v189\n" \
    "s_barrier\n" \
    "L%=_Sfr:\n" \
    "ds_read_b128 v[192:195], v227\n" \
    "s_waitcnt lgkmcnt(0)\n" \
    "v_smfmac_f32_16x16x64_bf16 v[212:215], v[192:195], v[96:103], v224\n" \
    "v_cmp_ne_u32 vcc, s76, v195\n" \
    "v_smfmac_f32_16x16x64_bf16 v[196:199], v[192:195], v[64:71], v224\n" \
    "s_cbranch_vccnz L%=_Sfs\n" \
    "v_smfmac_f32_16x16x64_bf16 v[200:203], v[192:195], v[72:79], v224\n" \
    "v_smfmac_f32_16x16x64_bf16 v[204:207], v[192:195], v[80:87], v224\n" \
    "v_add_f32 v212, v212, v214\n" \
    "v_smfmac_f32_16x16x64_bf16 v[208:211], v[192:195], v[88:95], v224\n" \
    "v_add_f32 v213, v213, v215\n" \
    "v_rcp_f32 v220, v212\n" \
    "v_rcp_f32 v221, v213\n" \
    "v_add_f32 v196, v196, v198\n" \
    "v_add_f32 v197, v197, v199\n" \
    "v_pk_add_f32 v[200:201], v[200:201], v[202:203]\n" \
    "v_pk_add_f32 v[204:205], v[204:205], v[206:207]\n" \
    "s_nop 0\n" \
    "v_pk_add_f32 v[208:209], v[208:209], v[210:211]\n" \
    "v_pk_mul_f32 v[196:197], v[196:197], v[220:221]\n" \
    "v_pk_mul_f32 v[200:201], v[200:201], v[220:221]\n" \
    "v_pk_mul_f32 v[204:205], v[204:205], v[220:221]\n" \
    "v_pk_mul_f32 v[208:209], v[208:209], v[220:221]\n" \
    "v_cvt_pk_bf16_f32 v216, v196, v200\n" \
    "v_cvt_pk_bf16_f32 v217, v204, v208\n" \
    "v_cvt_pk_bf16_f32 v218, v197, v201\n" \
    "v_cvt_pk_bf16_f32 v219, v205, v209\n" \
    "buffer_store_dwordx4 v[216:219], v226, s[40:43], s60 offen sc1\n" \
    "s_setprio 0\n" \
    "ds_read_b64 v[220:221], v229 offset:64\n" \
    "s_mov_b32 s79, s60\n" \
    "s_mov_b32 s60, s61\n" \
    "s_mov_b32 s61, s62\n" \
    "s_mov_b32 s62, s63\n" \
    "s_mov_b32 s63, s79\n" \
    "s_add_u32 s76, s76, 1\n" \
    "s_add_u32 s84, s84, 0x20000\n" \
    "s_add_u32 s77, s77, s65\n" \
    "s_add_u32 s81, s81, s65\n" \
    "s_waitcnt lgkmcnt(0)\n" \
    "v_readfirstlane_b32 s65, v220\n" \
    "v_readfirstlane_b32 s71, v221\n" \
    "s_mul_i32 s96, s65, s71\n" \
    "s_lshr_b32 s96, s96, 4\n" \
    "s_branch L%=_Sloop\n" \
    "L%=_Sfin:\n" \
    "buffer_store_dwordx4 v[236:239], v233, s[48:51], s84 offen sc1\n" \
    "s_waitcnt vmcnt(0)\n" \
    "s_barrier\n" \
    "s_branch L%=_end\n" \
    "L%=_Su3s:\n" \
    "s_cmp_gt_u32 s79, 0x8000\n" \
    "s_cbranch_scc1 L%=_Su3e\n" \
    "s_sleep 2\n" \
    "s_branch L%=_Su3a\n" \
    "L%=_Su4s:\n" \
    "s_mov_b32 s77, s79\n" \
    "s_memtime s[38:39]\n" \
    "s_memrealtime vcc\n" \
    "s_nop 4\n" \
    "s_memrealtime s[32:33]\n" \
    "s_nop 4\n" \
    "s_memrealtime s[34:35]\n" \
    "s_nop 4\n" \
    "s_memrealtime s[36:37]\n" \
    "s_add_u32 s80, s77, s64\n" \
    "s_lshl_b32 s80, s80, 2\n" \
    "s_waitcnt lgkmcnt(0)\n" \
    "s_sub_u32 s80, s80, vcc_lo\n" \
    "s_sub_u32 s80, s80, s32\n" \
    "s_sub_u32 s80, s80, s34\n" \
    "s_sub_u32 s80, s80, s36\n" \
    "s_mul_i32 s80, s80, s71\n" \
    "s_ashr_i32 s80, s80, 6\n" \
    "s_add_u32 s78, s80, s38\n" \
    "s_branch L%=_Su4k\n" \
    "L%=_Su5s:\n" \
    "s_cmp_gt_u32 s79, 0x8000\n" \
    "s_cbranch_scc1 L%=_Su5e\n" \
    "s_sleep 2\n" \
    "s_branch L%=_Su5a\n" \
    "L%=_Svslow:\n" \
    "s_nop 7\n" \
    "s_nop 0\n" \
    "v_max_u32 v216, v168, v169\n" \
    "v_max3_u32 v216, v216, v170, v171\n" \
    "v_max3_u32 v216, v216, v172, v173\n" \
    "v_max3_u32 v216, v216, v174, v175\n" \
    "v_cmp_eq_u32 vcc, -1, v216\n" \
    "s_cbranch_vccz L%=_Svok\n" \
    "s_cmp_eq_u32 s99, 0\n" \
    "s_cbranch_scc1 L%=_Svok\n" \
    "s_sub_u32 s99, s99, 1\n" \
    "s_cmp_lg_u32 s99, 0\n" \
    "s_cbranch_scc1 L%=_Su6\n" \
    "v_mov_b32 v216, 1\n" \
    "buffer_store_dword v216, off, s[44:47], 0 sc1\n" \
    "L%=_Su6:\n" \
    "s_sleep 1\n" \
    "buffer_load_dwordx4 v[168:171], v225, s[40:43], s60 offen sc1\n" \
    "buffer_load_dwordx4 v[172:175], v225, s[40:43], s85 offen sc1\n" \
    "v_mov_b64 v[176:177], 0\n" \
    "v_mov_b64 v[178:179], 0\n" \
    "v_mov_b64 v[180:181], 0\n" \
    "v_mov_b64 v[182:183], 0\n" \
    "v_mov_b64 v[184:185], 0\n" \
    "v_mov_b64 v[186:187], 0\n" \
    "v_mov_b64 v[188:189], 0\n" \
    "v_mov_b64 v[190:191], 0\n" \
    "s_waitcnt vmcnt(0)\n" \
    "s_branch L%=_Svc\n" \
    "L%=_Sfs:\n" \
    "s_nop 7\n" \
    "s_nop 0\n" \
    "v_mov_b64 v[196:197], 0\n" \
    "v_mov_b64 v[198:199], 0\n" \
    "v_mov_b64 v[200:201], 0\n" \
    "v_mov_b64 v[202:203], 0\n" \
    "v_mov_b64 v[204:205], 0\n" \
    "v_mov_b64 v[206:207], 0\n" \
    "v_mov_b64 v[208:209], 0\n" \
    "v_mov_b64 v[210:211], 0\n" \
    "v_mov_b64 v[212:213], 0\n" \
    "v_mov_b64 v[214:215], 0\n" \
    "s_branch L%=_Sfr\n" \
    "L%=_R:\n" \
    "s_mov_b32 s76, 0\n" \
    "s_mov_b32 s99, 0x20000\n" \
    "s_mov_b32 s84, 0\n" \
    "s_mov_b32 s81, 0\n" \
    "s_mov_b32 s86, 0\n" \
    "s_mov_b32 s87, 0\n" \
    "s_mov_b32 s88, 0\n" \
    "s_mov_b32 s89, 0\n" \
    "s_mov_b32 s90, 0\n" \
    "s_mov_b32 s91, 0\n" \
    "s_mov_b32 s92, 0\n" \
    "s_mov_b32 s93, 0\n" \
    "s_mov_b32 s94, 0\n" \
    "s_mov_b32 s95, 0\n" \
    "s_mov_b32 s96, 0\n" \
    "s_mov_b32 s97, 0\n" \
    "s_mov_b32 s98, 0\n" \
    "ds_read_b32 v216, v229\n" \
    "s_waitcnt lgkmcnt(0)\n" \
    "v_readfirstlane_b32 s94, v216\n" \
    "s_mul_i32 s79, s94, s70\n" \
    "s_add_u32 s79, s79, s81\n" \
    "s_mov_b32 s77, s79\n" \
    "s_mul_i32 s96, s65, s71\n" \
    "s_lshr_b32 s96, s96, 4\n" \
    "v_mov_b32 v96, 0x3f803f80\n" \
    "v_mov_b32 v97, 0x3f803f80\n" \
    "v_mov_b32 v98, 0x3f803f80\n" \
    "v_mov_b32 v99, 0x3f803f80\n" \
    "v_mov_b32 v100, 0x3f803f80\n" \
    "v_mov_b32 v101, 0x3f803f80\n" \
    "v_mov_b64 v[102:103], 0\n" \
    "v_mov_b64 v[64:65], 0\n" \
    "v_mov_b64 v[66:67], 0\n" \
    "v_mov_b64 v[68:69], 0\n" \
    "v_mov_b64 v[70:71], 0\n" \
    "v_mov_b64 v[72:73], 0\n" \
    "v_mov_b64 v[74:75], 0\n" \
    "v_mov_b64 v[76:77], 0\n" \
    "v_mov_b64 v[78:79], 0\n" \
    "v_mov_b64 v[80:81], 0\n" \
    "v_mov_b64 v[82:83], 0\n" \
    "v_mov_b64 v[84:85], 0\n" \
    "v_mov_b64 v[86:87], 0\n" \
    "v_mov_b64 v[88:89], 0\n" \
    "v_mov_b64 v[90:91], 0\n" \
    "v_mov_b64 v[92:93], 0\n" \
    "v_mov_b64 v[94:95], 0\n" \
    "v_mov_b64 v[192:193], 0\n" \
    "v_mov_b64 v[194:195], 0\n" \
    "v_mov_b32 v222, v236\n" \
    "ds_read_b64_tr_b16 v[104:105], v228 offset:16384\n" \
    "ds_read_b64_tr_b16 v[106:107], v228 offset:16416\n" \
    "ds_read_b64_tr_b16 v[108:109], v228 offset:18688\n" \
    "ds_read_b64_tr_b16 v[110:111], v228 offset:18720\n" \
    "ds_read_b64_tr_b16 v[112:113], v228 offset:20992\n" \
    "ds_read_b64_tr_b16 v[114:115], v228 offset:21024\n" \
    "ds_read_b64_tr_b16 v[116:117], v228 offset:23296\n" \
    "ds_read_b64_tr_b16 v[118:119], v228 offset:23328\n" \
    "ds_read_b64_tr_b16 v[120:121], v228 offset:16656\n" \
    "ds_read_b64_tr_b16 v[122:123], v228 offset:16688\n" \
    "ds_read_b64_tr_b16 v[124:125], v228 offset:18960\n" \
    "ds_read_b64_tr_b16 v[126:127], v228 offset:18992\n" \
    "ds_read_b64_tr_b16 v[128:129], v228 offset:21264\n" \
    "ds_read_b64_tr_b16 v[130:131], v228 offset:21296\n" \
    "ds_read_b64_tr_b16 v[132:133], v228 offset:23568\n" \
    "ds_read_b64_tr_b16 v[134:135], v228 offset:23600\n" \
    "s_waitcnt lgkmcnt(14)\n" \
    "v_mfma_f32_16x16x32_bf16 v[136:139], v[104:107], v[0:3], 0\n" \
    "v_mfma_f32_16x16x32_bf16 v[144:147], v[104:107], v[8:11], 0\n" \
    "v_mfma_f32_16x16x32_bf16 v[152:155], v[104:107], v[16:19], 0\n" \
    "v_mfma_f32_16x16x32_bf16 v[160:163], v[104:107], v[24:27], 0\n" \
    "s_waitcnt lgkmcnt(12)\n" \
    "v_mfma_f32_16x16x32_bf16 v[136:139], v[108:111], v[4:7], v[136:139]\n" \
    "v_mfma_f32_16x16x32_bf16 v[144:147], v[108:111], v[12:15], v[144:147]\n" \
    "v_mfma_f32_16x16x32_bf16 v[152:155], v[108:111], v[20:23], v[152:155]\n" \
    "v_mfma_f32_16x16x32_bf16 v[160:163], v[108:111], v[28:31], v[160:163]\n" \
    "s_waitcnt lgkmcnt(10)\n" \
    "v_mfma_f32_16x16x32_bf16 v[136:139], v[112:115], v[32:35], v[136:139]\n" \
    "v_mfma_f32_16x16x32_bf16 v[144:147], v[112:115], v[40:43], v[144:147]\n" \
    "v_mfma_f32_16x16x32_bf16 v[152:155], v[112:115], v[48:51], v[152:155]\n" \
    "v_mfma_f32_16x16x32_bf16 v[160:163], v[112:115], v[56:59], v[160:163]\n" \
    "s_waitcnt lgkmcnt(8)\n" \
    "v_mfma_f32_16x16x32_bf16 v[136:139], v[116:119], v[36:39], v[136:139]\n" \
    "v_mfma_f32_16x16x32_bf16 v[144:147], v[116:119], v[44:47], v[144:147]\n" \
    "v_mfma_f32_16x16x32_bf16 v[152:155], v[116:119], v[52:55], v[152:155]\n" \
    "v_mfma_f32_16x16x32_bf16 v[160:163], v[116:119], v[60:63], v[160:163]\n" \
    "s_waitcnt lgkmcnt(6)\n" \
    "v_mfma_f32_16x16x32_bf16 v[140:143], v[120:123], v[0:3], 0\n" \
    "v_mfma_f32_16x16x32_bf16 v[148:151], v[120:123], v[8:11], 0\n" \
    "v_mfma_f32_16x16x32_bf16 v[156:159], v[120:123], v[16:19], 0\n" \
    "v_mfma_f32_16x16x32_bf16 v[164:167], v[120:123], v[24:27], 0\n" \
    "s_waitcnt lgkmcnt(4)\n" \
    "v_mfma_f32_16x16x32_bf16 v[140:143], v[124:127], v[4:7], v[140:143]\n" \
    "v_mfma_f32_16x16x32_bf16 v[148:151], v[124:127], v[12:15], v[148:151]\n" \
    "v_mfma_f32_16x16x32_bf16 v[156:159], v[124:127], v[20:23], v[156:159]\n" \
    "v_mfma_f32_16x16x32_bf16 v[164:167], v[124:127], v[28:31], v[164:167]\n" \
    "s_waitcnt lgkmcnt(2)\n" \
    "v_mfma_f32_16x16x32_bf16 v[140:143], v[128:131], v[32:35], v[140:143]\n" \
    "v_mfma_f32_16x16x32_bf16 v[148:151], v[128:131], v[40:43], v[148:151]\n" \
    "v_mfma_f32_16x16x32_bf16 v[156:159], v[128:131], v[48:51], v[156:159]\n" \
    "v_mfma_f32_16x16x32_bf16 v[164:167], v[128:131], v[56:59], v[164:167]\n" \
    "s_waitcnt lgkmcnt(0)\n" \
    "v_mfma_f32_16x16x32_bf16 v[140:143], v[132:135], v[36:39], v[140:143]\n" \
    "v_mfma_f32_16x16x32_bf16 v[148:151], v[132:135], v[44:47], v[148:151]\n" \
    "v_mfma_f32_16x16x32_bf16 v[156:159], v[132:135], v[52:55], v[156:159]\n" \
    "v_mfma_f32_16x16x32_bf16 v[164:167], v[132:135], v[60:63], v[164:167]\n" \
    "v_cvt_pk_bf16_f32 v65, v136, v137\n" \
    "v_cvt_pk_bf16_f32 v68, v138, v139\n" \
    "v_cvt_pk_bf16_f32 v73, v144, v145\n" \
    "v_cvt_pk_bf16_f32 v76, v146, v147\n" \
    "v_cvt_pk_bf16_f32 v81, v152, v153\n" \
    "v_cvt_pk_bf16_f32 v84, v154, v155\n" \
    "v_cvt_pk_bf16_f32 v89, v160, v161\n" \
    "v_cvt_pk_bf16_f32 v92, v162, v163\n" \
    "v_cvt_pk_bf16_f32 v66, v142, v143\n" \
    "v_cvt_pk_bf16_f32 v67, v142, v143\n" \
    "v_cvt_pk_bf16_f32 v69, v140, v141\n" \
    "v_cvt_pk_bf16_f32 v74, v150, v151\n" \
    "v_cvt_pk_bf16_f32 v75, v150, v151\n" \
    "v_cvt_pk_bf16_f32 v77, v148, v149\n" \
    "v_cvt_pk_bf16_f32 v82, v158, v159\n" \
    "v_cvt_pk_bf16_f32 v83, v158, v159\n" \
    "v_cvt_pk_bf16_f32 v85, v156, v157\n" \
    "v_cvt_pk_bf16_f32 v90, v166, v167\n" \
    "v_cvt_pk_bf16_f32 v91, v166, v167\n" \
    "v_cvt_pk_bf16_f32 v93, v164, v165\n" \
    "v_permlane16_swap_b32 v66, v67\n" \
    "v_permlane16_swap_b32 v74, v75\n" \
    "v_permlane16_swap_b32 v82, v83\n" \
    "v_permlane16_swap_b32 v90, v91\n" \
    "L%=_Rloop:\n" \
    "v_mov_b64 v[176:177], 0\n" \
    "v_mov_b64 v[178:179], 0\n" \
    "v_mov_b64 v[180:181], 0\n" \
    "v_mov_b64 v[182:183], 0\n" \
    "v_mov_b64 v[184:185], 0\n" \
    "v_mov_b64 v[186:187], 0\n" \
    "v_mov_b64 v[188:189], 0\n" \
    "v_mov_b64 v[190:191], 0\n" \
    "v_mov_b64 v[196:197], 0\n" \
    "v_mov_b64 v[198:199], 0\n" \
    "v_mov_b64 v[200:201], 0\n" \
    "v_mov_b64 v[202:203], 0\n" \
    "v_mov_b64 v[204:205], 0\n" \
    "v_mov_b64 v[206:207], 0\n" \
    "v_mov_b64 v[208:209], 0\n" \
    "v_mov_b64 v[210:211], 0\n" \
    "v_mov_b64 v[212:213], 0\n" \
    "v_mov_b64 v[214:215], 0\n" \
    "s_add_u32 s85, s60, 0x1000\n" \
    "s_branch L%=_Ru2b\n" \
    "L%=_Ru2a:\n" \
    "s_sleep 4\n" \
    "L%=_Ru2b:\n" \
    "s_memrealtime vcc\n" \
    "s_add_u32 s80, s77, s64\n" \
    "s_add_u32 s80, s80, s69\n" \
    "s_waitcnt lgkmcnt(0)\n" \
    "s_sub_i32 s79, s80, vcc_lo\n" \
    "s_sub_u32 s79, s79, 40\n" \
    "s_cmp_lt_u32 s79, 0x400\n" \
    "s_cbranch_scc1 L%=_Ru2a\n" \
    "s_memtime s[38:39]\n" \
    "s_memrealtime vcc\n" \
    "s_nop 4\n" \
    "s_memrealtime s[32:33]\n" \
    "s_nop 4\n" \
    "s_memrealtime s[34:35]\n" \
    "s_nop 4\n" \
    "s_memrealtime s[36:37]\n" \
    "s_add_u32 s80, s77, s64\n" \
    "s_lshl_b32 s80, s80, 2\n" \
    "s_waitcnt lgkmcnt(0)\n" \
    "s_sub_u32 s80, s80, vcc_lo\n" \
    "s_sub_u32 s80, s80, s32\n" \
    "s_sub_u32 s80, s80, s34\n" \
    "s_sub_u32 s80, s80, s36\n" \
    "s_mul_i32 s80, s80, s71\n" \
    "s_ashr_i32 s80, s80, 6\n" \
    "s_add_u32 s78, s80, s38\n" \
    "L%=_Ru3a:\n" \
    "s_memtime s[38:39]\n" \
    "s_mul_i32 s80, s69, 23\n" \
    "s_add_u32 s80, s80, s78\n" \
    "s_waitcnt lgkmcnt(0)\n" \
    "s_sub_i32 s79, s80, s38\n" \
    "s_max_i32 s79, s79, 0\n" \
    "s_cmp_gt_u32 s79, 320\n" \
    "s_cbranch_scc1 L%=_Ru3s\n" \
    ".p2align 4\n" \
    "L%=_Ru3d:\n" \
    "s_sub_u32 s79, s79, 28\n" \
    "s_cbranch_scc0 L%=_Ru3d\n" \
    "L%=_Ru3e:\n" \
    "s_sub_u32 s79, s76, 1\n" \
    "s_cmp_lt_u32 s79, 63\n" \
    "s_cselect_b32 s79, s84, 0x40020000\n" \
    "s_sub_u32 s79, s79, 0x20000\n" \
    "buffer_store_dwordx4 v[236:239], v234, s[48:51], s79 offen sc1\n" \
    "s_setprio 3\n" \
    "s_barrier\n" \
    "s_nop 14\n" \
    "L%=_Rvs:\n" \
    "ds_read_b128 v[104:107], v229 offset:16\n" \
    "ds_read_b128 v[108:111], v230 offset:8704\n" \
    "ds_read_b128 v[112:115], v230 offset:9984\n" \
    "ds_read_b128 v[116:119], v230 offset:11264\n" \
    "ds_read_b128 v[120:123], v230 offset:12544\n" \
    "s_waitcnt lgkmcnt(4)\n" \
    "v_min3_u32 v104, v104, v105, v106\n" \
    "v_min_u32 v104, v104, v107\n" \
    "s_nop 0\n" \
    "v_readfirstlane_b32 s79, v104\n" \
    "s_cmp_le_u32 s79, s76\n" \
    "s_cbranch_scc1 L%=_Rvs\n" \
    "s_waitcnt lgkmcnt(2)\n" \
    "v_pk_add_f32 v[108:109], v[108:109], v[112:113]\n" \
    "v_pk_add_f32 v[110:111], v[110:111], v[114:115]\n" \
    "s_waitcnt lgkmcnt(0)\n" \
    "v_pk_add_f32 v[116:117], v[116:117], v[120:121]\n" \
    "v_pk_add_f32 v[118:119], v[118:119], v[122:123]\n" \
    "v_pk_add_f32 v[108:109], v[108:109], v[116:117]\n" \
    "v_pk_add_f32 v[110:111], v[110:111], v[118:119]\n" \
    "v_cvt_pk_bf16_f32 v124, v108, v109\n" \
    "v_cvt_pk_bf16_f32 v125, v110, v111\n" \
    "s_nop 0\n" \
    "v_mov_b32_dpp v126, v124 quad_perm:[1,0,3,2] row_mask:0xf bank_mask:0xf\n" \
    "v_mov_b32_dpp v127, v125 quad_perm:[1,0,3,2] row_mask:0xf bank_mask:0xf\n" \
    "buffer_store_dwordx4 v[124:127], v231, s[40:43], s60 offen sc1\n" \
    "ds_write_b64 v232, v[124:125]\n" \
    "ds_read_b64_tr_b16 v[236:237], v233\n" \
    "ds_read_b64_tr_b16 v[238:239], v233 offset:256\n" \
    "s_cmp_eq_u32 s76, 63\n" \
    "s_cbranch_scc1 L%=_Rfin\n" \
    "s_cmp_lt_u32 s76, 63\n" \
    "s_cbranch_scc1 L%=_Rne\n" \
    "s_waitcnt lgkmcnt(0)\n" \
    "buffer_store_dwordx4 v[236:239], v234, s[48:51], s84 offen sc1\n" \
    "L%=_Rne:\n" \
    "ds_read_b32 v216, v229\n" \
    "s_waitcnt lgkmcnt(0)\n" \
    "v_readfirstlane_b32 s94, v216\n" \
    "s_mul_i32 s79, s94, s70\n" \
    "s_add_u32 s79, s79, s81\n" \
    "s_cmp_lg_u32 s79, s77\n" \
    "s_cbranch_scc1 L%=_Ru4s\n" \
    "L%=_Ru4k:\n" \
    "L%=_Ru5a:\n" \
    "s_memtime s[38:39]\n" \
    "s_mul_hi_u32 s80, s67, s96\n" \
    "s_add_u32 s80, s80, s78\n" \
    "s_waitcnt lgkmcnt(0)\n" \
    "s_sub_i32 s79, s80, s38\n" \
    "s_max_i32 s79, s79, 0\n" \
    "s_cmp_gt_u32 s79, 320\n" \
    "s_cbranch_scc1 L%=_Ru5s\n" \
    ".p2align 4\n" \
    "L%=_Ru5d:\n" \
    "s_sub_u32 s79, s79, 28\n" \
    "s_cbranch_scc0 L%=_Ru5d\n" \
    "L%=_Ru5e:\n" \
    "buffer_load_dwordx4 v[168:171], v225, s[40:43], s60 offen sc1\n" \
    "buffer_load_dwordx4 v[172:175], v225, s[40:43], s85 offen sc1\n" \
    "s_memtime s[38:39]\n" \
    "s_memrealtime vcc\n" \
    "s_nop 4\n" \
    "s_memrealtime s[32:33]\n" \
    "s_nop 4\n" \
    "s_memrealtime s[34:35]\n" \
    "s_nop 4\n" \
    "s_memrealtime s[36:37]\n" \
    "s_add_u32 s80, s77, s64\n" \
    "s_lshl_b32 s80, s80, 2\n" \
    "s_waitcnt lgkmcnt(0)\n" \
    "s_sub_u32 s80, s80, vcc_lo\n" \
    "s_sub_u32 s80, s80, s32\n" \
    "s_sub_u32 s80, s80, s34\n" \
    "s_sub_u32 s80, s80, s36\n" \
    "s_mul_i32 s80, s80, s71\n" \
    "s_ashr_i32 s80, s80, 6\n" \
    "s_add_u32 s78, s80, s38\n" \
    "L%=_Rvc:\n" \
    "s_waitcnt vmcnt(1)\n" \
    "v_smfmac_f32_16x16x64_bf16 v[176:179], v[168:171], v[0:7], v224\n" \
    "v_smfmac_f32_16x16x64_bf16 v[180:183], v[168:171], v[8:15], v224\n" \
    "s_waitcnt vmcnt(0)\n" \
    "v_smfmac_f32_16x16x64_bf16 v[176:179], v[172:175], v[32:39], v224\n" \
    "v_smfmac_f32_16x16x64_bf16 v[180:183], v[172:175], v[40:47], v224\n" \
    "v_smfmac_f32_16x16x64_bf16 v[184:187], v[168:171], v[16:23], v224\n" \
    "v_smfmac_f32_16x16x64_bf16 v[188:191], v[168:171], v[24:31], v224\n" \
    "v_add_f32 v176, v176, v178\n" \
    "v_smfmac_f32_16x16x64_bf16 v[184:187], v[172:175], v[48:55], v224\n" \
    "v_add_f32 v177, v177, v179\n" \
    "v_smfmac_f32_16x16x64_bf16 v[188:191], v[172:175], v[56:63], v224\n" \
    "v_cmp_u_f32 vcc, v176, v177\n" \
    "v_cvt_pk_bf16_f32 v64, v176, v177\n" \
    "v_add_f32 v180, v180, v182\n" \
    "v_add_f32 v181, v181, v183\n" \
    "v_cvt_pk_bf16_f32 v72, v180, v181\n" \
    "s_cbranch_vccnz L%=_Rvslow\n" \
    "L%=_Rvok:\n" \
    "v_add_f32 v184, v184, v186\n" \
    "v_add_f32 v185, v185, v187\n" \
    "v_cvt_pk_bf16_f32 v80, v184, v185\n" \
    "v_add_f32 v188, v188, v190\n" \
    "v_add_f32 v189, v189, v191\n" \
    "v_cvt_pk_bf16_f32 v88, v188, v189\n" \
    "s_barrier\n" \
    "L%=_Rfr:\n" \
    "ds_read_b128 v[192:195], v227\n" \
    "s_waitcnt lgkmcnt(0)\n" \
    "v_smfmac_f32_16x16x64_bf16 v[212:215], v[192:195], v[96:103], v224\n" \
    "v_cmp_ne_u32 vcc, s76, v195\n" \
    "v_smfmac_f32_16x16x64_bf16 v[196:199], v[192:195], v[64:71], v224\n" \
    "s_cbranch_vccnz L%=_Rfs\n" \
    "v_smfmac_f32_16x16x64_bf16 v[200:203], v[192:195], v[72:79], v224\n" \
    "v_smfmac_f32_16x16x64_bf16 v[204:207], v[192:195], v[80:87], v224\n" \
    "v_add_f32 v212, v212, v214\n" \
    "v_smfmac_f32_16x16x64_bf16 v[208:211], v[192:195], v[88:95], v224\n" \
    "v_add_f32 v213, v213, v215\n" \
    "v_rcp_f32 v220, v212\n" \
    "v_rcp_f32 v221, v213\n" \
    "v_add_f32 v196, v196, v198\n" \
    "v_add_f32 v197, v197, v199\n" \
    "v_pk_add_f32 v[200:201], v[200:201], v[202:203]\n" \
    "v_pk_add_f32 v[204:205], v[204:205], v[206:207]\n" \
    "s_nop 0\n" \
    "v_pk_add_f32 v[208:209], v[208:209], v[210:211]\n" \
    "v_pk_mul_f32 v[196:197], v[196:197], v[220:221]\n" \
    "v_pk_mul_f32 v[200:201], v[200:201], v[220:221]\n" \
    "v_pk_mul_f32 v[204:205], v[204:205], v[220:221]\n" \
    "v_pk_mul_f32 v[208:209], v[208:209], v[220:221]\n" \
    "v_cvt_pk_bf16_f32 v216, v196, v200\n" \
    "v_cvt_pk_bf16_f32 v217, v204, v208\n" \
    "v_cvt_pk_bf16_f32 v218, v197, v201\n" \
    "v_cvt_pk_bf16_f32 v219, v205, v209\n" \
    "buffer_store_dwordx4 v[216:219], v226, s[40:43], s60 offen sc1\n" \
    "ds_read_b64_tr_b16 v[104:105], v228 offset:16384\n" \
    "ds_read_b64_tr_b16 v[106:107], v228 offset:16416\n" \
    "ds_read_b64_tr_b16 v[108:109], v228 offset:18688\n" \
    "ds_read_b64_tr_b16 v[110:111], v228 offset:18720\n" \
    "ds_read_b64_tr_b16 v[112:113], v228 offset:20992\n" \
    "ds_read_b64_tr_b16 v[114:115], v228 offset:21024\n" \
    "ds_read_b64_tr_b16 v[116:117], v228 offset:23296\n" \
    "ds_read_b64_tr_b16 v[118:119], v228 offset:23328\n" \
    "ds_read_b64_tr_b16 v[120:121], v228 offset:16656\n" \
    "ds_read_b64_tr_b16 v[122:123], v228 offset:16688\n" \
    "ds_read_b64_tr_b16 v[124:125], v228 offset:18960\n" \
    "ds_read_b64_tr_b16 v[126:127], v228 offset:18992\n" \
    "ds_read_b64_tr_b16 v[128:129], v228 offset:21264\n" \
    "ds_read_b64_tr_b16 v[130:131], v228 offset:21296\n" \
    "ds_read_b64_tr_b16 v[132:133], v228 offset:23568\n" \
    "ds_read_b64_tr_b16 v[134:135], v228 offset:23600\n" \
    "s_waitcnt lgkmcnt(14)\n" \
    "v_mfma_f32_16x16x32_bf16 v[136:139], v[104:107], v[0:3], 0\n" \
    "v_mfma_f32_16x16x32_bf16 v[144:147], v[104:107], v[8:11], 0\n" \
    "v_mfma_f32_16x16x32_bf16 v[152:155], v[104:107], v[16:19], 0\n" \
    "v_mfma_f32_16x16x32_bf16 v[160:163], v[104:107], v[24:27], 0\n" \
    "s_waitcnt lgkmcnt(12)\n" \
    "v_mfma_f32_16x16x32_bf16 v[136:139], v[108:111], v[4:7], v[136:139]\n" \
    "v_mfma_f32_16x16x32_bf16 v[144:147], v[108:111], v[12:15], v[144:147]\n" \
    "v_mfma_f32_16x16x32_bf16 v[152:155], v[108:111], v[20:23], v[152:155]\n" \
    "v_mfma_f32_16x16x32_bf16 v[160:163], v[108:111], v[28:31], v[160:163]\n" \
    "s_waitcnt lgkmcnt(10)\n" \
    "v_mfma_f32_16x16x32_bf16 v[136:139], v[112:115], v[32:35], v[136:139]\n" \
    "v_mfma_f32_16x16x32_bf16 v[144:147], v[112:115], v[40:43], v[144:147]\n" \
    "v_mfma_f32_16x16x32_bf16 v[152:155], v[112:115], v[48:51], v[152:155]\n" \
    "v_mfma_f32_16x16x32_bf16 v[160:163], v[112:115], v[56:59], v[160:163]\n" \
    "s_waitcnt lgkmcnt(8)\n" \
    "v_mfma_f32_16x16x32_bf16 v[136:139], v[116:119], v[36:39], v[136:139]\n" \
    "v_mfma_f32_16x16x32_bf16 v[144:147], v[116:119], v[44:47], v[144:147]\n" \
    "v_mfma_f32_16x16x32_bf16 v[152:155], v[116:119], v[52:55], v[152:155]\n" \
    "v_mfma_f32_16x16x32_bf16 v[160:163], v[116:119], v[60:63], v[160:163]\n" \
    "L%=_Ru6a:\n" \
    "s_memtime s[38:39]\n" \
    "s_mul_hi_u32 s80, s68, s96\n" \
    "s_add_u32 s80, s80, s78\n" \
    "s_waitcnt lgkmcnt(0)\n" \
    "s_sub_i32 s79, s80, s38\n" \
    "s_cmp_lt_i32 s79, -2000\n" \
    "s_cbranch_scc1 L%=_Rmiss\n" \
    "s_max_i32 s79, s79, 0\n" \
    "s_cmp_gt_u32 s79, 320\n" \
    "s_cbranch_scc1 L%=_Ru6s\n" \
    ".p2align 4\n" \
    "L%=_Ru6d:\n" \
    "s_sub_u32 s79, s79, 28\n" \
    "s_cbranch_scc0 L%=_Ru6d\n" \
    "L%=_Ru6e:\n" \
    "buffer_load_dwordx4 v[176:179], v235, s[40:43], s60 offen offset:0 sc1\n" \
    "buffer_load_dwordx4 v[180:183], v235, s[40:43], s60 offen offset:1024 sc1\n" \
    "buffer_load_dwordx4 v[184:187], v235, s[40:43], s60 offen offset:2048 sc1\n" \
    "buffer_load_dwordx4 v[188:191], v235, s[40:43], s60 offen offset:3072 sc1\n" \
    "v_mfma_f32_16x16x32_bf16 v[140:143], v[120:123], v[0:3], 0\n" \
    "s_add_u32 s77, s77, s65\n" \
    "s_add_u32 s81, s81, s65\n" \
    "v_mov_b32 v220, s81\n" \
    "v_mfma_f32_16x16x32_bf16 v[148:151], v[120:123], v[8:11], 0\n" \
    "v_mov_b32 v221, s71\n" \
    "ds_write_b64 v229, v[220:221] offset:80\n" \
    "s_memtime s[38:39]\n" \
    "v_mfma_f32_16x16x32_bf16 v[156:159], v[120:123], v[16:19], 0\n" \
    "s_memrealtime vcc\n" \
    "v_cvt_pk_bf16_f32 v65, v136, v137\n" \
    "v_cvt_pk_bf16_f32 v68, v138, v139\n" \
    "v_mfma_f32_16x16x32_bf16 v[164:167], v[120:123], v[24:27], 0\n" \
    "v_cvt_pk_bf16_f32 v73, v144, v145\n" \
    "v_cvt_pk_bf16_f32 v76, v146, v147\n" \
    "v_cvt_pk_bf16_f32 v81, v152, v153\n" \
    "v_mfma_f32_16x16x32_bf16 v[140:143], v[124:127], v[4:7], v[140:143]\n" \
    "s_memrealtime s[32:33]\n" \
    "v_cvt_pk_bf16_f32 v84, v154, v155\n" \
    "v_cvt_pk_bf16_f32 v89, v160, v161\n" \
    "v_mfma_f32_16x16x32_bf16 v[148:151], v[124:127], v[12:15], v[148:151]\n" \
    "s_memrealtime s[34:35]\n" \
    "v_cvt_pk_bf16_f32 v92, v162, v163\n" \
    "s_add_u32 s80, s77, s64\n" \
    "v_mfma_f32_16x16x32_bf16 v[156:159], v[124:127], v[20:23], v[156:159]\n" \
    "s_lshl_b32 s80, s80, 2\n" \
    "s_and_b32 s95, s94, 1\n" \
    "s_lshl_b32 s95, s95, 17\n" \
    "v_mfma_f32_16x16x32_bf16 v[164:167], v[124:127], v[28:31], v[164:167]\n" \
    "s_memrealtime s[36:37]\n" \
    "s_add_u32 s95, s95, s61\n" \
    "s_add_u32 s85, s95, 0x1000\n" \
    "v_mfma_f32_16x16x32_bf16 v[140:143], v[128:131], v[32:35], v[140:143]\n" \
    "s_xor_b32 s85, s95, 0x20000\n" \
    "s_and_b32 s79, s94, 1\n" \
    "s_lshl1_add_u32 s79, s76, s79\n" \
    "v_mfma_f32_16x16x32_bf16 v[148:151], v[128:131], v[40:43], v[148:151]\n" \
    "s_add_u32 s79, s79, 2\n" \
    "s_lshl_b32 s79, s79, 17\n" \
    "v_mov_b32 v223, s79\n" \
    "v_mfma_f32_16x16x32_bf16 v[156:159], v[128:131], v[48:51], v[156:159]\n" \
    "s_waitcnt lgkmcnt(0)\n" \
    "s_sub_u32 s80, s80, vcc_lo\n" \
    "s_sub_u32 s80, s80, s32\n" \
    "v_mfma_f32_16x16x32_bf16 v[164:167], v[128:131], v[56:59], v[164:167]\n" \
    "s_sub_u32 s80, s80, s34\n" \
    "s_sub_u32 s80, s80, s36\n" \
    "s_mul_i32 s80, s80, s71\n" \
    "v_mfma_f32_16x16x32_bf16 v[140:143], v[132:135], v[36:39], v[140:143]\n" \
    "s_ashr_i32 s80, s80, 6\n" \
    "s_add_u32 s78, s80, s38\n" \
    "s_add_u32 s72, s78, -183\n" \
    "v_mfma_f32_16x16x32_bf16 v[148:151], v[132:135], v[44:47], v[148:151]\n" \
    "s_sub_u32 s79, s72, s38\n" \
    "s_cmp_lt_i32 s79, 0\n" \
    "v_mfma_f32_16x16x32_bf16 v[156:159], v[132:135], v[52:55], v[156:159]\n" \
    "v_mfma_f32_16x16x32_bf16 v[164:167], v[132:135], v[60:63], v[164:167]\n" \
    "s_cbranch_scc1 L%=_Rpast\n" \
    "ds_write_b32 v229, v223 offset:76\n" \
    "s_waitcnt vmcnt(3)\n" \
    "v_mfma_f32_16x16x32_bf16 v[240:243], v[244:247], v[176:179], 0\n" \
    "s_waitcnt vmcnt(2)\n" \
    "v_mfma_f32_16x16x32_bf16 v[240:243], v[244:247], v[180:183], v[240:243]\n" \
    "s_waitcnt vmcnt(1)\n" \
    "v_mfma_f32_16x16x32_bf16 v[240:243], v[244:247], v[184:187], v[240:243]\n" \
    "s_memtime s[38:39]\n" \
    "s_waitcnt vmcnt(0)\n" \
    "v_mfma_f32_16x16x32_bf16 v[240:243], v[244:247], v[188:191], v[240:243]\n" \
    "s_nop 4\n" \
    "s_waitcnt lgkmcnt(0)\n" \
    "s_cmp_lt_u32 s72, s38\n" \
    "s_cselect_b32 s95, s85, s95\n" \
    "v_cmp_u_f32 vcc, v240, v240\n" \
    "v_add_f32_dpp v240, v240, v240 row_ror:8 row_mask:0xf bank_mask:0xf\n" \
    "v_add_f32_dpp v241, v241, v241 row_ror:8 row_mask:0xf bank_mask:0xf\n" \
    "v_add_f32_dpp v242, v242, v242 row_ror:8 row_mask:0xf bank_mask:0xf\n" \
    "v_add_f32_dpp v243, v243, v243 row_ror:8 row_mask:0xf bank_mask:0xf\n" \
    "v_cvt_pk_bf16_f32 v216, v240, v241\n" \
    "v_cvt_pk_bf16_f32 v217, v242, v243\n" \
    "s_cbranch_vccnz L%=_Rslow\n" \
    "v_mov_b32_dpp v218, v216 quad_perm:[1,0,3,2] row_mask:0xf bank_mask:0xf\n" \
    "v_mov_b32_dpp v219, v217 quad_perm:[1,0,3,2] row_mask:0xf bank_mask:0xf\n" \
    "buffer_store_dwordx4 v[216:219], v222, s[40:43], s95 offen sc1\n" \
    "s_cbranch_scc1 L%=_Rlate\n" \
    "s_wakeup\n" \
    "buffer_store_dwordx4 v[216:219], v222, s[40:43], s85 offen sc1\n" \
    "L%=_Rcall:\n" \
    "L%=_Ru7a:\n" \
    "s_memtime s[38:39]\n" \
    "s_mul_i32 s80, -3, 23\n" \
    "s_add_u32 s80, s80, s78\n" \
    "s_waitcnt lgkmcnt(0)\n" \
    "s_sub_i32 s79, s80, s38\n" \
    "s_cmp_lt_i32 s79, -100\n" \
    "s_cbranch_scc1 L%=_Rover\n" \
    "s_max_i32 s79, s79, 0\n" \
    "s_cmp_gt_u32 s79, 320\n" \
    "s_cbranch_scc1 L%=_Ru7s\n" \
    ".p2align 4\n" \
    "L%=_Ru7d:\n" \
    "s_sub_u32 s79, s79, 28\n" \
    "s_cbranch_scc0 L%=_Ru7d\n" \
    "L%=_Ru7e:\n" \
    "s_wakeup\n" \
    "L%=_Rdone:\n" \
    "s_setprio 0\n" \
    "v_cvt_pk_bf16_f32 v66, v142, v143\n" \
    "v_cvt_pk_bf16_f32 v67, v142, v143\n" \
    "v_cvt_pk_bf16_f32 v69, v140, v141\n" \
    "v_cvt_pk_bf16_f32 v74, v150, v151\n" \
    "v_cvt_pk_bf16_f32 v75, v150, v151\n" \
    "v_cvt_pk_bf16_f32 v77, v148, v149\n" \
    "v_cvt_pk_bf16_f32 v82, v158, v159\n" \
    "v_cvt_pk_bf16_f32 v83, v158, v159\n" \
    "v_cvt_pk_bf16_f32 v85, v156, v157\n" \
    "v_cvt_pk_bf16_f32 v90, v166, v167\n" \
    "v_cvt_pk_bf16_f32 v91, v166, v167\n" \
    "v_cvt_pk_bf16_f32 v93, v164, v165\n" \
    "v_permlane16_swap_b32 v66, v67\n" \
    "v_permlane16_swap_b32 v74, v75\n" \
    "v_permlane16_swap_b32 v82, v83\n" \
    "v_permlane16_swap_b32 v90, v91\n" \
    "ds_read_b64 v[220:221], v229 offset:64\n" \
    "s_mov_b32 s79, s60\n" \
    "s_mov_b32 s60, s61\n" \
    "s_mov_b32 s61, s62\n" \
    "s_mov_b32 s62, s63\n" \
    "s_mov_b32 s63, s79\n" \
    "s_add_u32 s76, s76, 1\n" \
    "s_add_u32 s84, s84, 0x20000\n" \
    "s_waitcnt lgkmcnt(0)\n" \
    "v_readfirstlane_b32 s65, v220\n" \
    "v_readfirstlane_b32 s71, v221\n" \
    "s_mul_i32 s96, s65, s71\n" \
    "s_lshr_b32 s96, s96, 4\n" \
    "s_branch L%=_Rloop\n" \
    "L%=_Rslow:\n" \
    "s_nop 7\n" \
    "s_nop 0\n" \
    "v_max_u32 v220, v176, v177\n" \
    "v_max3_u32 v220, v220, v178, v179\n" \
    "v_max3_u32 v220, v220, v180, v181\n" \
    "v_max3_u32 v220, v220, v182, v183\n" \
    "v_max3_u32 v220, v220, v184, v185\n" \
    "v_max3_u32 v220, v220, v186, v187\n" \
    "v_max3_u32 v220, v220, v188, v189\n" \
    "v_max3_u32 v220, v220, v190, v191\n" \
    "v_cmp_eq_u32 vcc, -1, v220\n" \
    "s_cbranch_vccz L%=_Rok\n" \
    "s_cmp_eq_u32 s99, 0\n" \
    "s_cbranch_scc1 L%=_Rok\n" \
    "s_sub_u32 s99, s99, 1\n" \
    "s_cmp_lg_u32 s99, 0\n" \
    "s_cbranch_scc1 L%=_Ru8\n" \
    "v_mov_b32 v220, 1\n" \
    "buffer_store_dword v220, off, s[44:47], 0 sc1\n" \
    "L%=_Ru8:\n" \
    "ds_write_b32 v229, v102 offset:76\n" \
    "L%=_Rgrid:\n" \
    "s_mul_hi_u32 s73, s68, s65\n" \
    "s_sub_u32 s73, s73, s65\n" \
    "s_add_u32 s73, s73, -18\n" \
    "s_sub_u32 s74, 8, s73\n" \
    "s_memrealtime vcc\n" \
    "s_waitcnt lgkmcnt(0)\n" \
    "s_sub_u32 s80, vcc_lo, s64\n" \
    "s_add_u32 s80, s80, s74\n" \
    "s_branch L%=_Ru9h\n" \
    "L%=_Ru9g:\n" \
    "s_add_u32 s77, s77, s70\n" \
    "s_add_u32 s94, s94, 1\n" \
    "L%=_Ru9h:\n" \
    "s_sub_u32 s79, s80, s77\n" \
    "s_cmp_lt_u32 s79, 0x1000000\n" \
    "s_cbranch_scc1 L%=_Ru9g\n" \
    "v_mov_b32 v220, s94\n" \
    "ds_max_u32 v229, v220\n" \
    "s_cmp_lt_i32 s79, -991\n" \
    "s_cbranch_scc1 L%=_Rnow\n" \
    "s_branch L%=_Ru10b\n" \
    "L%=_Ru10a:\n" \
    "s_sleep 4\n" \
    "L%=_Ru10b:\n" \
    "s_memrealtime vcc\n" \
    "s_add_u32 s80, s77, s64\n" \
    "s_add_u32 s80, s80, s73\n" \
    "s_waitcnt lgkmcnt(0)\n" \
    "s_sub_i32 s79, s80, vcc_lo\n" \
    "s_sub_u32 s79, s79, 40\n" \
    "s_cmp_lt_u32 s79, 0x400\n" \
    "s_cbranch_scc1 L%=_Ru10a\n" \
    "s_memtime s[38:39]\n" \
    "s_memrealtime vcc\n" \
    "s_nop 4\n" \
    "s_memrealtime s[32:33]\n" \
    "s_nop 4\n" \
    "s_memrealtime s[34:35]\n" \
    "s_nop 4\n" \
    "s_memrealtime s[36:37]\n" \
    "s_add_u32 s80, s77, s64\n" \
    "s_lshl_b32 s80, s80, 2\n" \
    "s_waitcnt lgkmcnt(0)\n" \
    "s_sub_u32 s80, s80, vcc_lo\n" \
    "s_sub_u32 s80, s80, s32\n" \
    "s_sub_u32 s80, s80, s34\n" \
    "s_sub_u32 s80, s80, s36\n" \
    "s_mul_i32 s80, s80, s71\n" \
    "s_ashr_i32 s80, s80, 6\n" \
    "s_add_u32 s78, s80, s38\n" \
    "L%=_Ru11a:\n" \
    "s_memtime s[38:39]\n" \
    "s_mul_i32 s80, s73, s71\n" \
    "s_ashr_i32 s80, s80, 4\n" \
    "s_add_u32 s80, s80, s78\n" \
    "s_waitcnt lgkmcnt(0)\n" \
    "s_sub_i32 s79, s80, s38\n" \
    "s_max_i32 s79, s79, 0\n" \
    "s_cmp_gt_u32 s79, 320\n" \
    "s_cbranch_scc1 L%=_Ru11s\n" \
    ".p2align 4\n" \
    "L%=_Ru11d:\n" \
    "s_sub_u32 s79, s79, 28\n" \
    "s_cbranch_scc0 L%=_Ru11d\n" \
    "L%=_Ru11e:\n" \
    "L%=_Rnow:\n" \
    "buffer_load_dwordx4 v[176:179], v235, s[40:43], s60 offen offset:0 sc1\n" \
    "buffer_load_dwordx4 v[180:183], v235, s[40:43], s60 offen offset:1024 sc1\n" \
    "buffer_load_dwordx4 v[184:187], v235, s[40:43], s60 offen offset:2048 sc1\n" \
    "buffer_load_dwordx4 v[188:191], v235, s[40:43], s60 offen offset:3072 sc1\n" \
    "L%=_Rarm:\n" \
    "s_memtime s[38:39]\n" \
    "s_memrealtime vcc\n" \
    "s_nop 4\n" \
    "s_memrealtime s[32:33]\n" \
    "s_nop 4\n" \
    "s_memrealtime s[34:35]\n" \
    "s_nop 4\n" \
    "s_memrealtime s[36:37]\n" \
    "s_add_u32 s80, s77, s64\n" \
    "s_lshl_b32 s80, s80, 2\n" \
    "s_waitcnt lgkmcnt(0)\n" \
    "s_sub_u32 s80, s80, vcc_lo\n" \
    "s_sub_u32 s80, s80, s32\n" \
    "s_sub_u32 s80, s80, s34\n" \
    "s_sub_u32 s80, s80, s36\n" \
    "s_mul_i32 s80, s80, s71\n" \
    "s_ashr_i32 s80, s80, 6\n" \
    "s_add_u32 s78, s80, s38\n" \
    "s_and_b32 s95, s94, 1\n" \
    "s_lshl_b32 s95, s95, 17\n" \
    "s_add_u32 s95, s95, s61\n" \
    "s_add_u32 s85, s95, 0x1000\n" \
    "s_xor_b32 s85, s95, 0x20000\n" \
    "s_and_b32 s79, s94, 1\n" \
    "s_lshl1_add_u32 s79, s76, s79\n" \
    "s_add_u32 s79, s79, 2\n" \
    "s_lshl_b32 s79, s79, 17\n" \
    "v_mov_b32 v223, s79\n" \
    "s_add_u32 s72, s78, -183\n" \
    "s_sub_u32 s79, s72, s38\n" \
    "s_cmp_lt_i32 s79, 0\n" \
    "s_cbranch_scc1 L%=_Rpast\n" \
    "s_waitcnt vmcnt(3)\n" \
    "v_mfma_f32_16x16x32_bf16 v[240:243], v[244:247], v[176:179], 0\n" \
    "s_waitcnt vmcnt(2)\n" \
    "v_mfma_f32_16x16x32_bf16 v[240:243], v[244:247], v[180:183], v[240:243]\n" \
    "s_waitcnt vmcnt(1)\n" \
    "v_mfma_f32_16x16x32_bf16 v[240:243], v[244:247], v[184:187], v[240:243]\n" \
    "s_waitcnt vmcnt(0)\n" \
    "v_mfma_f32_16x16x32_bf16 v[240:243], v[244:247], v[188:191], v[240:243]\n" \
    "s_memtime s[38:39]\n" \
    "s_nop 3\n" \
    "s_waitcnt lgkmcnt(0)\n" \
    "s_cmp_lt_u32 s72, s38\n" \
    "s_cselect_b32 s95, s85, s95\n" \
    "v_cmp_u_f32 vcc, v240, v240\n" \
    "v_add_f32_dpp v240, v240, v240 row_ror:8 row_mask:0xf bank_mask:0xf\n" \
    "v_add_f32_dpp v241, v241, v241 row_ror:8 row_mask:0xf bank_mask:0xf\n" \
    "v_add_f32_dpp v242, v242, v242 row_ror:8 row_mask:0xf bank_mask:0xf\n" \
    "v_add_f32_dpp v243, v243, v243 row_ror:8 row_mask:0xf bank_mask:0xf\n" \
    "v_cvt_pk_bf16_f32 v216, v240, v241\n" \
    "v_cvt_pk_bf16_f32 v217, v242, v243\n" \
    "s_cbranch_vccnz L%=_Rslow\n" \
    "L%=_Rok:\n" \
    "ds_write_b32 v229, v223 offset:76\n" \
    "v_mov_b32_dpp v218, v216 quad_perm:[1,0,3,2] row_mask:0xf bank_mask:0xf\n" \
    "v_mov_b32_dpp v219, v217 quad_perm:[1,0,3,2] row_mask:0xf bank_mask:0xf\n" \
    "buffer_store_dwordx4 v[216:219], v222, s[40:43], s95 offen sc1\n" \
    "s_cbranch_scc1 L%=_Rlate\n" \
    "buffer_store_dwordx4 v[216:219], v222, s[40:43], s85 offen sc1\n" \
    "s_waitcnt lgkmcnt(0)\n" \
    "s_wakeup\n" \
    "s_branch L%=_Rcall\n" \
    "L%=_Rpast:\n" \
    "s_memrealtime vcc\n" \
    "s_waitcnt lgkmcnt(0)\n" \
    "s_sub_u32 s80, vcc_lo, s64\n" \
    "s_add_u32 s80, s80, 12\n" \
    "s_branch L%=_Ru12h\n" \
    "L%=_Ru12g:\n" \
    "s_add_u32 s77, s77, s70\n" \
    "s_add_u32 s94, s94, 1\n" \
    "L%=_Ru12h:\n" \
    "s_sub_u32 s79, s80, s77\n" \
    "s_cmp_lt_u32 s79, 0x1000000\n" \
    "s_cbranch_scc1 L%=_Ru12g\n" \
    "v_mov_b32 v220, s94\n" \
    "ds_max_u32 v229, v220\n" \
    "s_branch L%=_Rarm\n" \
    "L%=_Rmiss:\n" \
    "s_add_u32 s77, s77, s65\n" \
    "s_add_u32 s81, s81, s65\n" \
    "v_mov_b32 v220, s81\n" \
    "v_mov_b32 v221, s71\n" \
    "ds_write_b64 v229, v[220:221] offset:80\n" \
    "v_mfma_f32_16x16x32_bf16 v[140:143], v[120:123], v[0:3], 0\n" \
    "v_mfma_f32_16x16x32_bf16 v[148:151], v[120:123], v[8:11], 0\n" \
    "v_mfma_f32_16x16x32_bf16 v[156:159], v[120:123], v[16:19], 0\n" \
    "v_mfma_f32_16x16x32_bf16 v[164:167], v[120:123], v[24:27], 0\n" \
    "v_mfma_f32_16x16x32_bf16 v[140:143], v[124:127], v[4:7], v[140:143]\n" \
    "v_mfma_f32_16x16x32_bf16 v[148:151], v[124:127], v[12:15], v[148:151]\n" \
    "v_mfma_f32_16x16x32_bf16 v[156:159], v[124:127], v[20:23], v[156:159]\n" \
    "v_mfma_f32_16x16x32_bf16 v[164:167], v[124:127], v[28:31], v[164:167]\n" \
    "v_mfma_f32_16x16x32_bf16 v[140:143], v[128:131], v[32:35], v[140:143]\n" \
    "v_mfma_f32_16x16x32_bf16 v[148:151], v[128:131], v[40:43], v[148:151]\n" \
    "v_mfma_f32_16x16x32_bf16 v[156:159], v[128:131], v[48:51], v[156:159]\n" \
    "v_mfma_f32_16x16x32_bf16 v[164:167], v[128:131], v[56:59], v[164:167]\n" \
    "v_mfma_f32_16x16x32_bf16 v[140:143], v[132:135], v[36:39], v[140:143]\n" \
    "v_mfma_f32_16x16x32_bf16 v[148:151], v[132:135], v[44:47], v[148:151]\n" \
    "v_mfma_f32_16x16x32_bf16 v[156:159], v[132:135], v[52:55], v[156:159]\n" \
    "v_mfma_f32_16x16x32_bf16 v[164:167], v[132:135], v[60:63], v[164:167]\n" \
    "v_cvt_pk_bf16_f32 v65, v136, v137\n" \
    "v_cvt_pk_bf16_f32 v68, v138, v139\n" \
    "v_cvt_pk_bf16_f32 v73, v144, v145\n" \
    "v_cvt_pk_bf16_f32 v76, v146, v147\n" \
    "v_cvt_pk_bf16_f32 v81, v152, v153\n" \
    "v_cvt_pk_bf16_f32 v84, v154, v155\n" \
    "v_cvt_pk_bf16_f32 v89, v160, v161\n" \
    "v_cvt_pk_bf16_f32 v92, v162, v163\n" \
    "s_branch L%=_Rgrid\n" \
    "L%=_Rover:\n" \
    "s_memrealtime vcc\n" \
    "s_waitcnt lgkmcnt(0)\n" \
    "s_sub_u32 s80, vcc_lo, s64\n" \
    "s_add_u32 s80, s80, 12\n" \
    "s_branch L%=_Ru13h\n" \
    "L%=_Ru13g:\n" \
    "s_add_u32 s77, s77, s70\n" \
    "s_add_u32 s94, s94, 1\n" \
    "L%=_Ru13h:\n" \
    "s_sub_u32 s79, s80, s77\n" \
    "s_cmp_lt_u32 s79, 0x1000000\n" \
    "s_cbranch_scc1 L%=_Ru13g\n" \
    "v_mov_b32 v220, s94\n" \
    "ds_max_u32 v229, v220\n" \
    "s_branch L%=_Ru14b\n" \
    "L%=_Ru14a:\n" \
    "s_sleep 4\n" \
    "L%=_Ru14b:\n" \
    "s_memrealtime vcc\n" \
    "s_add_u32 s80, s77, s64\n" \
    "s_add_u32 s80, s80, -3\n" \
    "s_waitcnt lgkmcnt(0)\n" \
    "s_sub_i32 s79, s80, vcc_lo\n" \
    "s_sub_u32 s79, s79, 40\n" \
    "s_cmp_lt_u32 s79, 0x400\n" \
    "s_cbranch_scc1 L%=_Ru14a\n" \
    "s_memtime s[38:39]\n" \
    "s_memrealtime vcc\n" \
    "s_nop 4\n" \
    "s_memrealtime s[32:33]\n" \
    "s_nop 4\n" \
    "s_memrealtime s[34:35]\n" \
    "s_nop 4\n" \
    "s_memrealtime s[36:37]\n" \
    "s_add_u32 s80, s77, s64\n" \
    "s_lshl_b32 s80, s80, 2\n" \
    "s_waitcnt lgkmcnt(0)\n" \
    "s_sub_u32 s80, s80, vcc_lo\n" \
    "s_sub_u32 s80, s80, s32\n" \
    "s_sub_u32 s80, s80, s34\n" \
    "s_sub_u32 s80, s80, s36\n" \
    "s_mul_i32 s80, s80, s71\n" \
    "s_ashr_i32 s80, s80, 6\n" \
    "s_add_u32 s78, s80, s38\n" \
    "s_branch L%=_Rcall\n" \
    "L%=_Rlate:\n" \
    "ds_write_b32 v229, v102 offset:76\n" \
    "s_memrealtime vcc\n" \
    "s_waitcnt lgkmcnt(0)\n" \
    "s_sub_u32 s80, vcc_lo, s64\n" \
    "s_add_u32 s80, s80, 12\n" \
    "s_branch L%=_Ru15h\n" \
    "L%=_Ru15g:\n" \
    "s_add_u32 s77, s77, s70\n" \
    "s_add_u32 s94, s94, 1\n" \
    "L%=_Ru15h:\n" \
    "s_sub_u32 s79, s80, s77\n" \
    "s_cmp_lt_u32 s79, 0x1000000\n" \
    "s_cbranch_scc1 L%=_Ru15g\n" \
    "v_mov_b32 v220, s94\n" \
    "ds_max_u32 v229, v220\n" \
    "s_branch L%=_Ru16b\n" \
    "L%=_Ru16a:\n" \
    "s_sleep 4\n" \
    "L%=_Ru16b:\n" \
    "s_memrealtime vcc\n" \
    "s_add_u32 s80, s77, s64\n" \
    "s_add_u32 s80, s80, -3\n" \
    "s_waitcnt lgkmcnt(0)\n" \
    "s_sub_i32 s79, s80, vcc_lo\n" \
    "s_sub_u32 s79, s79, 40\n" \
    "s_cmp_lt_u32 s79, 0x400\n" \
    "s_cbranch_scc1 L%=_Ru16a\n" \
    "s_memtime s[38:39]\n" \
    "s_memrealtime vcc\n" \
    "s_nop 4\n" \
    "s_memrealtime s[32:33]\n" \
    "s_nop 4\n" \
    "s_memrealtime s[34:35]\n" \
    "s_nop 4\n" \
    "s_memrealtime s[36:37]\n" \
    "s_add_u32 s80, s77, s64\n" \
    "s_lshl_b32 s80, s80, 2\n" \
    "s_waitcnt lgkmcnt(0)\n" \
    "s_sub_u32 s80, s80, vcc_lo\n" \
    "s_sub_u32 s80, s80, s32\n" \
    "s_sub_u32 s80, s80, s34\n" \
    "s_sub_u32 s80, s80, s36\n" \
    "s_mul_i32 s80, s80, s71\n" \
    "s_ashr_i32 s80, s80, 6\n" \
    "s_add_u32 s78, s80, s38\n" \
    "s_and_b32 s79, s94, 1\n" \
    "s_lshl1_add_u32 s79, s76, s79\n" \
    "s_add_u32 s79, s79, 2\n" \
    "s_lshl_b32 s79, s79, 17\n" \
    "v_mov_b32 v223, s79\n" \
    "ds_write_b32 v229, v223 offset:76\n" \
    "s_waitcnt lgkmcnt(0)\n" \
    "s_wakeup\n" \
    "L%=_Ru17a:\n" \
    "s_memtime s[38:39]\n" \
    "s_mul_i32 s80, -3, 23\n" \
    "s_add_u32 s80, s80, s78\n" \
    "s_waitcnt lgkmcnt(0)\n" \
    "s_sub_i32 s79, s80, s38\n" \
    "s_max_i32 s79, s79, 0\n" \
    "s_cmp_gt_u32 s79, 320\n" \
    "s_cbranch_scc1 L%=_Ru17s\n" \
    ".p2align 4\n" \
    "L%=_Ru17d:\n" \
    "s_sub_u32 s79, s79, 28\n" \
    "s_cbranch_scc0 L%=_Ru17d\n" \
    "L%=_Ru17e:\n" \
    "s_wakeup\n" \
    "s_xor_b32 s85, s95, 0x20000\n" \
    "buffer_store_dwordx4 v[216:219], v222, s[40:43], s85 offen sc1\n" \
    "s_branch L%=_Rdone\n" \
    "L%=_Rfin:\n" \
    "s_waitcnt lgkmcnt(0)\n" \
    "buffer_store_dwordx4 v[236:239], v234, s[48:51], s84 offen sc1\n" \
    "s_waitcnt vmcnt(0)\n" \
    "s_barrier\n" \
    "s_branch L%=_end\n" \
    "L%=_Ru3s:\n" \
    "s_cmp_gt_u32 s79, 0x8000\n" \
    "s_cbranch_scc1 L%=_Ru3e\n" \
    "s_sleep 2\n" \
    "s_branch L%=_Ru3a\n" \
    "L%=_Ru4s:\n" \
    "s_mov_b32 s77, s79\n" \
    "s_memtime s[38:39]\n" \
    "s_memrealtime vcc\n" \
    "s_nop 4\n" \
    "s_memrealtime s[32:33]\n" \
    "s_nop 4\n" \
    "s_memrealtime s[34:35]\n" \
    "s_nop 4\n" \
    "s_memrealtime s[36:37]\n" \
    "s_add_u32 s80, s77, s64\n" \
    "s_lshl_b32 s80, s80, 2\n" \
    "s_waitcnt lgkmcnt(0)\n" \
    "s_sub_u32 s80, s80, vcc_lo\n" \
    "s_sub_u32 s80, s80, s32\n" \
    "s_sub_u32 s80, s80, s34\n" \
    "s_sub_u32 s80, s80, s36\n" \
    "s_mul_i32 s80, s80, s71\n" \
    "s_ashr_i32 s80, s80, 6\n" \
    "s_add_u32 s78, s80, s38\n" \
    "s_branch L%=_Ru4k\n" \
    "L%=_Ru5s:\n" \
    "s_cmp_gt_u32 s79, 0x8000\n" \
    "s_cbranch_scc1 L%=_Ru5e\n" \
    "s_sleep 2\n" \
    "s_branch L%=_Ru5a\n" \
    "L%=_Rvslow:\n" \
    "s_nop 7\n" \
    "s_nop 0\n" \
    "v_max_u32 v216, v168, v169\n" \
    "v_max3_u32 v216, v216, v170, v171\n" \
    "v_max3_u32 v216, v216, v172, v173\n" \
    "v_max3_u32 v216, v216, v174, v175\n" \
    "v_cmp_eq_u32 vcc, -1, v216\n" \
    "s_cbranch_vccz L%=_Rvok\n" \
    "s_cmp_eq_u32 s99, 0\n" \
    "s_cbranch_scc1 L%=_Rvok\n" \
    "s_sub_u32 s99, s99, 1\n" \
    "s_cmp_lg_u32 s99, 0\n" \
    "s_cbranch_scc1 L%=_Ru18\n" \
    "v_mov_b32 v216, 1\n" \
    "buffer_store_dword v216, off, s[44:47], 0 sc1\n" \
    "L%=_Ru18:\n" \
    "s_sleep 1\n" \
    "buffer_load_dwordx4 v[168:171], v225, s[40:43], s60 offen sc1\n" \
    "buffer_load_dwordx4 v[172:175], v225, s[40:43], s85 offen sc1\n" \
    "v_mov_b64 v[176:177], 0\n" \
    "v_mov_b64 v[178:179], 0\n" \
    "v_mov_b64 v[180:181], 0\n" \
    "v_mov_b64 v[182:183], 0\n" \
    "v_mov_b64 v[184:185], 0\n" \
    "v_mov_b64 v[186:187], 0\n" \
    "v_mov_b64 v[188:189], 0\n" \
    "v_mov_b64 v[190:191], 0\n" \
    "s_waitcnt vmcnt(0)\n" \
    "s_branch L%=_Rvc\n" \
    "L%=_Rfs:\n" \
    "s_nop 7\n" \
    "s_nop 0\n" \
    "v_mov_b64 v[196:197], 0\n" \
    "v_mov_b64 v[198:199], 0\n" \
    "v_mov_b64 v[200:201], 0\n" \
    "v_mov_b64 v[202:203], 0\n" \
    "v_mov_b64 v[204:205], 0\n" \
    "v_mov_b64 v[206:207], 0\n" \
    "v_mov_b64 v[208:209], 0\n" \
    "v_mov_b64 v[210:211], 0\n" \
    "v_mov_b64 v[212:213], 0\n" \
    "v_mov_b64 v[214:215], 0\n" \
    "s_branch L%=_Rfr\n" \
    "L%=_Ru6s:\n" \
    "s_cmp_gt_u32 s79, 0x8000\n" \
    "s_cbranch_scc1 L%=_Ru6e\n" \
    "s_sleep 2\n" \
    "s_branch L%=_Ru6a\n" \
    "L%=_Ru7s:\n" \
    "s_cmp_gt_u32 s79, 0x8000\n" \
    "s_cbranch_scc1 L%=_Ru7e\n" \
    "s_sleep 2\n" \
    "s_branch L%=_Ru7a\n" \
    "L%=_Ru11s:\n" \
    "s_cmp_gt_u32 s79, 0x8000\n" \
    "s_cbranch_scc1 L%=_Ru11e\n" \
    "s_sleep 2\n" \
    "s_branch L%=_Ru11a\n" \
    "L%=_Ru17s:\n" \
    "s_cmp_gt_u32 s79, 0x8000\n" \
    "s_cbranch_scc1 L%=_Ru17e\n" \
    "s_sleep 2\n" \
    "s_branch L%=_Ru17a\n" \
    "L%=_P:\n" \
    "s_mov_b32 s76, 0\n" \
    "s_mov_b32 s99, 0x20000\n" \
    "s_mov_b32 s84, 0\n" \
    "s_mov_b32 s81, 0\n" \
    "s_mov_b32 s86, 0\n" \
    "s_mov_b32 s87, 0\n" \
    "s_mov_b32 s88, 0\n" \
    "s_mov_b32 s89, 0\n" \
    "s_mov_b32 s90, 0\n" \
    "s_mov_b32 s91, 0\n" \
    "s_mov_b32 s92, 0\n" \
    "s_mov_b32 s93, 0\n" \
    "s_mov_b32 s94, 0\n" \
    "s_mov_b32 s95, 0\n" \
    "s_mov_b32 s96, 0\n" \
    "s_mov_b32 s97, 0\n" \
    "s_mov_b32 s98, 0\n" \
    "ds_read_b32 v216, v229\n" \
    "s_waitcnt lgkmcnt(0)\n" \
    "v_readfirstlane_b32 s94, v216\n" \
    "s_mul_i32 s79, s94, s70\n" \
    "s_add_u32 s79, s79, s81\n" \
    "s_mov_b32 s77, s79\n" \
    "s_mul_i32 s96, s65, s71\n" \
    "s_lshr_b32 s96, s96, 4\n" \
    "v_mov_b32 v96, 0x3f803f80\n" \
    "v_mov_b32 v97, 0x3f803f80\n" \
    "v_mov_b32 v98, 0x3f803f80\n" \
    "v_mov_b32 v99, 0x3f803f80\n" \
    "v_mov_b32 v100, 0x3f803f80\n" \
    "v_mov_b32 v101, 0x3f803f80\n" \
    "v_mov_b64 v[102:103], 0\n" \
    "v_mov_b64 v[64:65], 0\n" \
    "v_mov_b64 v[66:67], 0\n" \
    "v_mov_b64 v[68:69], 0\n" \
    "v_mov_b64 v[70:71], 0\n" \
    "v_mov_b64 v[72:73], 0\n" \
    "v_mov_b64 v[74:75], 0\n" \
    "v_mov_b64 v[76:77], 0\n" \
    "v_mov_b64 v[78:79], 0\n" \
    "v_mov_b64 v[80:81], 0\n" \
    "v_mov_b64 v[82:83], 0\n" \
    "v_mov_b64 v[84:85], 0\n" \
    "v_mov_b64 v[86:87], 0\n" \
    "v_mov_b64 v[88:89], 0\n" \
    "v_mov_b64 v[90:91], 0\n" \
    "v_mov_b64 v[92:93], 0\n" \
    "v_mov_b64 v[94:95], 0\n" \
    "v_mov_b64 v[192:193], 0\n" \
    "v_mov_b64 v[194:195], 0\n" \
    "v_cvt_f32_u32 v240, s71\n" \
    "v_mov_b32 v241, 1.0\n" \
    "v_mov_b32 v242, 0\n" \
    "v_mov_b32 v243, s65\n" \
    "s_mov_b32 s74, s94\n" \
    "v_mov_b32 v244, -1\n" \
    "v_mov_b32 v245, -1\n" \
    "v_mov_b32 v246, -1\n" \
    "v_mov_b32 v247, -1\n" \
    "L%=_Ploop:\n" \
    "ds_read_b64_tr_b16 v[104:105], v228 offset:16384\n" \
    "ds_read_b64_tr_b16 v[106:107], v228 offset:16416\n" \
    "ds_read_b64_tr_b16 v[108:109], v228 offset:18688\n" \
    "ds_read_b64_tr_b16 v[110:111], v228 offset:18720\n" \
    "ds_read_b64_tr_b16 v[112:113], v228 offset:20992\n" \
    "ds_read_b64_tr_b16 v[114:115], v228 offset:21024\n" \
    "ds_read_b64_tr_b16 v[116:117], v228 offset:23296\n" \
    "ds_read_b64_tr_b16 v[118:119], v228 offset:23328\n" \
    "ds_read_b64_tr_b16 v[120:121], v228 offset:16656\n" \
    "ds_read_b64_tr_b16 v[122:123], v228 offset:16688\n" \
    "ds_read_b64_tr_b16 v[124:125], v228 offset:18960\n" \
    "ds_read_b64_tr_b16 v[126:127], v228 offset:18992\n" \
    "ds_read_b64_tr_b16 v[128:129], v228 offset:21264\n" \
    "ds_read_b64_tr_b16 v[130:131], v228 offset:21296\n" \
    "ds_read_b64_tr_b16 v[132:133], v228 offset:23568\n" \
    "ds_read_b64_tr_b16 v[134:135], v228 offset:23600\n" \
    "s_waitcnt lgkmcnt(14)\n" \
    "v_mfma_f32_16x16x32_bf16 v[136:139], v[104:107], v[0:3], 0\n" \
    "v_mfma_f32_16x16x32_bf16 v[144:147], v[104:107], v[8:11], 0\n" \
    "v_mfma_f32_16x16x32_bf16 v[152:155], v[104:107], v[16:19], 0\n" \
    "v_mfma_f32_16x16x32_bf16 v[160:163], v[104:107], v[24:27], 0\n" \
    "s_waitcnt lgkmcnt(12)\n" \
    "v_mfma_f32_16x16x32_bf16 v[136:139], v[108:111], v[4:7], v[136:139]\n" \
    "v_mfma_f32_16x16x32_bf16 v[144:147], v[108:111], v[12:15], v[144:147]\n" \
    "v_mfma_f32_16x16x32_bf16 v[152:155], v[108:111], v[20:23], v[152:155]\n" \
    "v_mfma_f32_16x16x32_bf16 v[160:163], v[108:111], v[28:31], v[160:163]\n" \
    "s_waitcnt lgkmcnt(10)\n" \
    "v_mfma_f32_16x16x32_bf16 v[136:139], v[112:115], v[32:35], v[136:139]\n" \
    "v_mfma_f32_16x16x32_bf16 v[144:147], v[112:115], v[40:43], v[144:147]\n" \
    "v_mfma_f32_16x16x32_bf16 v[152:155], v[112:115], v[48:51], v[152:155]\n" \
    "v_mfma_f32_16x16x32_bf16 v[160:163], v[112:115], v[56:59], v[160:163]\n" \
    "s_waitcnt lgkmcnt(8)\n" \
    "v_mfma_f32_16x16x32_bf16 v[136:139], v[116:119], v[36:39], v[136:139]\n" \
    "v_mfma_f32_16x16x32_bf16 v[144:147], v[116:119], v[44:47], v[144:147]\n" \
    "v_mfma_f32_16x16x32_bf16 v[152:155], v[116:119], v[52:55], v[152:155]\n" \
    "v_mfma_f32_16x16x32_bf16 v[160:163], v[116:119], v[60:63], v[160:163]\n" \
    "s_waitcnt lgkmcnt(6)\n" \
    "v_mfma_f32_16x16x32_bf16 v[140:143], v[120:123], v[0:3], 0\n" \
    "v_mfma_f32_16x16x32_bf16 v[148:151], v[120:123], v[8:11], 0\n" \
    "v_mfma_f32_16x16x32_bf16 v[156:159], v[120:123], v[16:19], 0\n" \
    "v_mfma_f32_16x16x32_bf16 v[164:167], v[120:123], v[24:27], 0\n" \
    "s_waitcnt lgkmcnt(4)\n" \
    "v_mfma_f32_16x16x32_bf16 v[140:143], v[124:127], v[4:7], v[140:143]\n" \
    "v_mfma_f32_16x16x32_bf16 v[148:151], v[124:127], v[12:15], v[148:151]\n" \
    "v_mfma_f32_16x16x32_bf16 v[156:159], v[124:127], v[20:23], v[156:159]\n" \
    "v_mfma_f32_16x16x32_bf16 v[164:167], v[124:127], v[28:31], v[164:167]\n" \
    "s_waitcnt lgkmcnt(2)\n" \
    "v_mfma_f32_16x16x32_bf16 v[140:143], v[128:131], v[32:35], v[140:143]\n" \
    "v_mfma_f32_16x16x32_bf16 v[148:151], v[128:131], v[40:43], v[148:151]\n" \
    "v_mfma_f32_16x16x32_bf16 v[156:159], v[128:131], v[48:51], v[156:159]\n" \
    "v_mfma_f32_16x16x32_bf16 v[164:167], v[128:131], v[56:59], v[164:167]\n" \
    "s_waitcnt lgkmcnt(0)\n" \
    "v_mfma_f32_16x16x32_bf16 v[140:143], v[132:135], v[36:39], v[140:143]\n" \
    "v_mfma_f32_16x16x32_bf16 v[148:151], v[132:135], v[44:47], v[148:151]\n" \
    "v_mfma_f32_16x16x32_bf16 v[156:159], v[132:135], v[52:55], v[156:159]\n" \
    "v_mfma_f32_16x16x32_bf16 v[164:167], v[132:135], v[60:63], v[164:167]\n" \
    "v_cvt_pk_bf16_f32 v65, v136, v137\n" \
    "v_cvt_pk_bf16_f32 v68, v138, v139\n" \
    "v_cvt_pk_bf16_f32 v73, v144, v145\n" \
    "v_cvt_pk_bf16_f32 v76, v146, v147\n" \
    "v_cvt_pk_bf16_f32 v81, v152, v153\n" \
    "v_cvt_pk_bf16_f32 v84, v154, v155\n" \
    "v_cvt_pk_bf16_f32 v89, v160, v161\n" \
    "v_cvt_pk_bf16_f32 v92, v162, v163\n" \
    "v_cvt_pk_bf16_f32 v66, v142, v143\n" \
    "v_cvt_pk_bf16_f32 v67, v142, v143\n" \
    "v_cvt_pk_bf16_f32 v69, v140, v141\n" \
    "v_cvt_pk_bf16_f32 v74, v150, v151\n" \
    "v_cvt_pk_bf16_f32 v75, v150, v151\n" \
    "v_cvt_pk_bf16_f32 v77, v148, v149\n" \
    "v_cvt_pk_bf16_f32 v82, v158, v159\n" \
    "v_cvt_pk_bf16_f32 v83, v158, v159\n" \
    "v_cvt_pk_bf16_f32 v85, v156, v157\n" \
    "v_cvt_pk_bf16_f32 v90, v166, v167\n" \
    "v_cvt_pk_bf16_f32 v91, v166, v167\n" \
    "v_cvt_pk_bf16_f32 v93, v164, v165\n" \
    "v_permlane16_swap_b32 v66, v67\n" \
    "v_permlane16_swap_b32 v74, v75\n" \
    "v_permlane16_swap_b32 v82, v83\n" \
    "v_permlane16_swap_b32 v90, v91\n" \
    "v_mov_b64 v[176:177], 0\n" \
    "v_mov_b64 v[178:179], 0\n" \
    "v_mov_b64 v[180:181], 0\n" \
    "v_mov_b64 v[182:183], 0\n" \
    "v_mov_b64 v[184:185], 0\n" \
    "v_mov_b64 v[186:187], 0\n" \
    "v_mov_b64 v[188:189], 0\n" \
    "v_mov_b64 v[190:191], 0\n" \
    "v_mov_b64 v[196:197], 0\n" \
    "v_mov_b64 v[198:199], 0\n" \
    "v_mov_b64 v[200:201], 0\n" \
    "v_mov_b64 v[202:203], 0\n" \
    "v_mov_b64 v[204:205], 0\n" \
    "v_mov_b64 v[206:207], 0\n" \
    "v_mov_b64 v[208:209], 0\n" \
    "v_mov_b64 v[210:211], 0\n" \
    "v_mov_b64 v[212:213], 0\n" \
    "v_mov_b64 v[214:215], 0\n" \
    "s_add_u32 s85, s60, 0x1000\n" \
    "s_add_u32 s82, s76, 1\n" \
    "s_min_u32 s82, s82, 63\n" \
    "s_lshl_b32 s83, s82, 19\n" \
    "s_add_u32 s83, s83, s72\n" \
    "s_lshl_b32 s79, s82, 11\n" \
    "s_and_b32 s79, s79, 0x2000\n" \
    "s_xor_b32 s83, s83, s79\n" \
    "v_cmp_ge_u32 vcc, s82, v237\n" \
    "s_cbranch_vccnz L%=_Pfslow\n" \
    "L%=_Pfok:\n" \
    "s_add_u32 s79, s82, 4\n" \
    "v_mov_b32 v150, 0x40000000\n" \
    "v_cmp_lt_u32 vcc, s79, v237\n" \
    "s_nop 1\n" \
    "v_cndmask_b32 v150, v235, v150, vcc\n" \
    "s_branch L%=_Pu2b\n" \
    "L%=_Pu2a:\n" \
    "s_sleep 4\n" \
    "L%=_Pu2b:\n" \
    "s_memrealtime vcc\n" \
    "s_add_u32 s80, s77, s64\n" \
    "s_add_u32 s80, s80, s69\n" \
    "s_waitcnt lgkmcnt(0)\n" \
    "s_sub_i32 s79, s80, vcc_lo\n" \
    "s_sub_u32 s79, s79, 40\n" \
    "s_cmp_lt_u32 s79, 0x400\n" \
    "s_cbranch_scc1 L%=_Pu2a\n" \
    "s_memtime s[38:39]\n" \
    "s_memrealtime vcc\n" \
    "s_nop 4\n" \
    "s_memrealtime s[32:33]\n" \
    "s_nop 4\n" \
    "s_memrealtime s[34:35]\n" \
    "s_nop 4\n" \
    "s_memrealtime s[36:37]\n" \
    "s_add_u32 s80, s77, s64\n" \
    "s_lshl_b32 s80, s80, 2\n" \
    "s_waitcnt lgkmcnt(0)\n" \
    "s_sub_u32 s80, s80, vcc_lo\n" \
    "s_sub_u32 s80, s80, s32\n" \
    "s_sub_u32 s80, s80, s34\n" \
    "s_sub_u32 s80, s80, s36\n" \
    "s_mul_i32 s80, s80, s71\n" \
    "s_ashr_i32 s80, s80, 6\n" \
    "s_add_u32 s78, s80, s38\n" \
    "L%=_Pu3a:\n" \
    "s_memtime s[38:39]\n" \
    "s_mul_i32 s80, s69, 23\n" \
    "s_add_u32 s80, s80, s78\n" \
    "s_waitcnt lgkmcnt(0)\n" \
    "s_sub_i32 s79, s80, s38\n" \
    "s_max_i32 s79, s79, 0\n" \
    "s_cmp_gt_u32 s79, 320\n" \
    "s_cbranch_scc1 L%=_Pu3s\n" \
    ".p2align 4\n" \
    "L%=_Pu3d:\n" \
    "s_sub_u32 s79, s79, 28\n" \
    "s_cbranch_scc0 L%=_Pu3d\n" \
    "L%=_Pu3e:\n" \
    "buffer_load_dwordx4 v[104:107], v230, s[56:59], s83 offen offset:0 sc1\n" \
    "buffer_load_dwordx4 v[108:111], v230, s[56:59], s83 offen offset:1024 sc1\n" \
    "buffer_load_dwordx4 v[112:115], v230, s[56:59], s83 offen offset:2048 sc1\n" \
    "buffer_load_dwordx4 v[116:119], v230, s[56:59], s83 offen offset:3072 sc1\n" \
    "s_add_u32 s79, s83, 0x1000\n" \
    "buffer_load_dwordx4 v[120:123], v230, s[56:59], s79 offen offset:0 sc1\n" \
    "buffer_load_dwordx4 v[124:127], v230, s[56:59], s79 offen offset:1024 sc1\n" \
    "buffer_load_dwordx4 v[128:131], v230, s[56:59], s79 offen offset:2048 sc1\n" \
    "buffer_load_dwordx4 v[132:135], v230, s[56:59], s79 offen offset:3072 sc1\n" \
    "s_xor_b32 s79, s83, s75\n" \
    "buffer_load_dwordx4 v[136:139], v230, s[52:55], s79 offen offset:0 sc1\n" \
    "buffer_load_dwordx4 v[140:143], v230, s[52:55], s79 offen offset:1024 sc1\n" \
    "buffer_load_dword v151, v150, s[44:47], 0 offen sc1\n" \
    "s_lshl_b32 s79, s76, 7\n" \
    "buffer_load_dwordx4 v[144:147], off, s[44:47], s79 offset:2176 sc1\n" \
    "buffer_store_dword v243, v239, s[44:47], s79 offen sc1\n" \
    "buffer_store_dwordx4 v[244:247], v232, s[40:43], s62 offen offset:0 sc1\n" \
    "buffer_store_dwordx4 v[244:247], v232, s[40:43], s62 offen offset:1024 sc1\n" \
    "buffer_store_dwordx4 v[244:247], v232, s[40:43], s62 offen offset:2048 sc1\n" \
    "buffer_store_dwordx4 v[244:247], v232, s[40:43], s62 offen offset:3072 sc1\n" \
    "buffer_store_dwordx4 v[244:247], v233, s[40:43], s62 offen offset:0 sc1\n" \
    "buffer_store_dwordx4 v[244:247], v233, s[40:43], s62 offen offset:1024 sc1\n" \
    "buffer_store_dwordx4 v[244:247], v234, s[40:43], s62 offen sc1\n" \
    "s_barrier\n" \
    "s_cmp_eq_u32 s76, 63\n" \
    "s_cbranch_scc1 L%=_Pfin\n" \
    "ds_read_b32 v216, v229\n" \
    "s_waitcnt lgkmcnt(0)\n" \
    "v_readfirstlane_b32 s94, v216\n" \
    "s_mul_i32 s79, s94, s70\n" \
    "s_add_u32 s79, s79, s81\n" \
    "s_cmp_lg_u32 s79, s77\n" \
    "s_cbranch_scc1 L%=_Pu4s\n" \
    "L%=_Pu4k:\n" \
    "s_sub_u32 s32, s78, s97\n" \
    "s_sub_u32 s33, s77, s98\n" \
    "s_mov_b32 s97, s78\n" \
    "s_mov_b32 s98, s77\n" \
    "s_lshl_b32 s32, s32, 4\n" \
    "s_cmp_eq_u32 s76, 0\n" \
    "s_cbranch_scc1 L%=_Pck\n" \
    "v_cvt_f32_u32 v148, s33\n" \
    "v_cvt_f32_u32 v149, s32\n" \
    "v_rcp_f32 v148, v148\n" \
    "s_cmp_lt_u32 s76, 4\n" \
    "s_cselect_b32 s34, 0, 0x3c449ba6\n" \
    "s_cmp_eq_u32 s94, s74\n" \
    "s_mov_b32 s74, s94\n" \
    "s_cselect_b32 s32, 0x7f000000, 0\n" \
    "s_cselect_b32 s33, 0.5, 1.0\n" \
    "s_cselect_b32 s34, 0, s34\n" \
    "s_cmp_eq_u32 s76, 1\n" \
    "s_cselect_b32 s32, 0x7f000000, s32\n" \
    "s_cselect_b32 s33, 1.0, s33\n" \
    "s_cselect_b32 s35, 0x3f7851ec, 1.0\n" \
    "v_mul_f32 v149, s35, v149\n" \
    "v_fma_f32 v148, v148, v149, -v240\n" \
    "v_max_f32 v149, 0.5, v241\n" \
    "v_min_f32 v148, s32, v148\n" \
    "v_cmp_gt_f32 vcc, 0, v148\n" \
    "v_mul_f32 v242, 0x3f7851ec, v242\n" \
    "v_add_f32 v242, s34, v242\n" \
    "v_cndmask_b32 v149, v241, v149, vcc\n" \
    "v_fma_f32 v240, v148, v149, v240\n" \
    "v_mul_f32 v241, s33, v241\n" \
    "v_max_f32 v240, 0x43400000, v240\n" \
    "v_max_f32 v241, 0x3e000000, v241\n" \
    "v_min_f32 v240, 0x43e00000, v240\n" \
    "v_fma_f32 v148, v242, s73, s73\n" \
    "v_rcp_f32 v149, v240\n" \
    "s_nop 0\n" \
    "v_fma_f32 v148, v148, v149, 0.5\n" \
    "v_cvt_u32_f32 v243, v148\n" \
    "L%=_Pck:\n" \
    "v_add_f32 v149, 0.5, v240\n" \
    "v_mov_b32 v148, s65\n" \
    "v_cvt_u32_f32 v149, v149\n" \
    "s_waitcnt vmcnt(8)\n" \
    "ds_write_b128 v231, v[104:107] offset:16384\n" \
    "ds_write_b128 v231, v[108:111] offset:17536\n" \
    "ds_write_b128 v231, v[112:115] offset:18688\n" \
    "ds_write_b128 v231, v[116:119] offset:19840\n" \
    "ds_write_b128 v231, v[120:123] offset:20992\n" \
    "ds_write_b128 v231, v[124:127] offset:22144\n" \
    "ds_write_b128 v231, v[128:131] offset:23296\n" \
    "ds_write_b128 v231, v[132:135] offset:24448\n" \
    "ds_write_b128 v230, v[136:139] offset:25600\n" \
    "ds_write_b128 v230, v[140:143] offset:26624\n" \
    "v_max3_u32 v144, v144, v145, v146\n" \
    "v_max_u32 v144, v144, v147\n" \
    "v_cmp_eq_u32 vcc, 0, v144\n" \
    "s_nop 1\n" \
    "v_cndmask_b32 v148, v144, v148, vcc\n" \
    "ds_write_b64 v229, v[148:149] offset:64\n" \
    "v_max_u32 v237, v237, v151\n" \
    "s_setprio 3\n" \
    "s_nop 0\n" \
    "v_min_u32_dpp v150, v237, v237 quad_perm:[1,0,3,2] row_mask:0xf bank_mask:0xf\n" \
    "ds_write_b32 v229, v150 offset:72\n" \
    "L%=_Pu5a:\n" \
    "s_memtime s[38:39]\n" \
    "s_mul_hi_u32 s80, s67, s96\n" \
    "s_add_u32 s80, s80, s78\n" \
    "s_waitcnt lgkmcnt(0)\n" \
    "s_sub_i32 s79, s80, s38\n" \
    "s_max_i32 s79, s79, 0\n" \
    "s_cmp_gt_u32 s79, 320\n" \
    "s_cbranch_scc1 L%=_Pu5s\n" \
    ".p2align 4\n" \
    "L%=_Pu5d:\n" \
    "s_sub_u32 s79, s79, 28\n" \
    "s_cbranch_scc0 L%=_Pu5d\n" \
    "L%=_Pu5e:\n" \
    "buffer_load_dwordx4 v[168:171], v225, s[40:43], s60 offen sc1\n" \
    "buffer_load_dwordx4 v[172:175], v225, s[40:43], s85 offen sc1\n" \
    "L%=_Pvc:\n" \
    "s_waitcnt vmcnt(1)\n" \
    "v_smfmac_f32_16x16x64_bf16 v[176:179], v[168:171], v[0:7], v224\n" \
    "v_smfmac_f32_16x16x64_bf16 v[180:183], v[168:171], v[8:15], v224\n" \
    "s_waitcnt vmcnt(0)\n" \
    "v_smfmac_f32_16x16x64_bf16 v[176:179], v[172:175], v[32:39], v224\n" \
    "v_smfmac_f32_16x16x64_bf16 v[180:183], v[172:175], v[40:47], v224\n" \
    "v_smfmac_f32_16x16x64_bf16 v[184:187], v[168:171], v[16:23], v224\n" \
    "v_smfmac_f32_16x16x64_bf16 v[188:191], v[168:171], v[24:31], v224\n" \
    "v_add_f32 v176, v176, v178\n" \
    "v_smfmac_f32_16x16x64_bf16 v[184:187], v[172:175], v[48:55], v224\n" \
    "v_add_f32 v177, v177, v179\n" \
    "v_smfmac_f32_16x16x64_bf16 v[188:191], v[172:175], v[56:63], v224\n" \
    "v_cmp_u_f32 vcc, v176, v177\n" \
    "v_cvt_pk_bf16_f32 v64, v176, v177\n" \
    "v_add_f32 v180, v180, v182\n" \
    "v_add_f32 v181, v181, v183\n" \
    "v_cvt_pk_bf16_f32 v72, v180, v181\n" \
    "s_cbranch_vccnz L%=_Pvslow\n" \
    "L%=_Pvok:\n" \
    "v_add_f32 v184, v184, v186\n" \
    "v_add_f32 v185, v185, v187\n" \
    "v_cvt_pk_bf16_f32 v80, v184, v185\n" \
    "v_add_f32 v188, v188, v190\n" \
    "v_add_f32 v189, v189, v191\n" \
    "v_cvt_pk_bf16_f32 v88, v188, v189\n" \
    "s_barrier\n" \
    "L%=_Pfr:\n" \
    "ds_read_b128 v[192:195], v227\n" \
    "s_waitcnt lgkmcnt(0)\n" \
    "v_smfmac_f32_16x16x64_bf16 v[212:215], v[192:195], v[96:103], v224\n" \
    "v_cmp_ne_u32 vcc, s76, v195\n" \
    "v_smfmac_f32_16x16x64_bf16 v[196:199], v[192:195], v[64:71], v224\n" \
    "s_cbranch_vccnz L%=_Pfs\n" \
    "v_smfmac_f32_16x16x64_bf16 v[200:203], v[192:195], v[72:79], v224\n" \
    "v_smfmac_f32_16x16x64_bf16 v[204:207], v[192:195], v[80:87], v224\n" \
    "v_add_f32 v212, v212, v214\n" \
    "v_smfmac_f32_16x16x64_bf16 v[208:211], v[192:195], v[88:95], v224\n" \
    "v_add_f32 v213, v213, v215\n" \
    "v_rcp_f32 v220, v212\n" \
    "v_rcp_f32 v221, v213\n" \
    "v_add_f32 v196, v196, v198\n" \
    "v_add_f32 v197, v197, v199\n" \
    "v_pk_add_f32 v[200:201], v[200:201], v[202:203]\n" \
    "v_pk_add_f32 v[204:205], v[204:205], v[206:207]\n" \
    "s_nop 0\n" \
    "v_pk_add_f32 v[208:209], v[208:209], v[210:211]\n" \
    "v_pk_mul_f32 v[196:197], v[196:197], v[220:221]\n" \
    "v_pk_mul_f32 v[200:201], v[200:201], v[220:221]\n" \
    "v_pk_mul_f32 v[204:205], v[204:205], v[220:221]\n" \
    "v_pk_mul_f32 v[208:209], v[208:209], v[220:221]\n" \
    "v_cvt_pk_bf16_f32 v216, v196, v200\n" \
    "v_cvt_pk_bf16_f32 v217, v204, v208\n" \
    "v_cvt_pk_bf16_f32 v218, v197, v201\n" \
    "v_cvt_pk_bf16_f32 v219, v205, v209\n" \
    "buffer_store_dwordx4 v[216:219], v226, s[40:43], s60 offen sc1\n" \
    "s_setprio 0\n" \
    "ds_read_b64 v[220:221], v229 offset:64\n" \
    "s_mov_b32 s79, s60\n" \
    "s_mov_b32 s60, s61\n" \
    "s_mov_b32 s61, s62\n" \
    "s_mov_b32 s62, s63\n" \
    "s_mov_b32 s63, s79\n" \
    "s_add_u32 s76, s76, 1\n" \
    "s_add_u32 s84, s84, 0x20000\n" \
    "s_add_u32 s77, s77, s65\n" \
    "s_add_u32 s81, s81, s65\n" \
    "s_waitcnt lgkmcnt(0)\n" \
    "v_readfirstlane_b32 s65, v220\n" \
    "v_readfirstlane_b32 s71, v221\n" \
    "s_mul_i32 s96, s65, s71\n" \
    "s_lshr_b32 s96, s96, 4\n" \
    "s_branch L%=_Ploop\n" \
    "L%=_Pfslow:\n" \
    "s_sleep 4\n" \
    "buffer_load_dword v237, v235, s[44:47], 0 offen sc1\n" \
    "s_waitcnt vmcnt(0)\n" \
    "v_cmp_ge_u32 vcc, s82, v237\n" \
    "s_cbranch_vccz L%=_Pfok\n" \
    "s_cmp_eq_u32 s99, 0\n" \
    "s_cbranch_scc1 L%=_Pfok\n" \
    "s_sub_u32 s99, s99, 1\n" \
    "s_branch L%=_Pfslow\n" \
    "L%=_Pfin:\n" \
    "s_waitcnt vmcnt(0)\n" \
    "s_barrier\n" \
    "s_branch L%=_end\n" \
    "L%=_Pu3s:\n" \
    "s_cmp_gt_u32 s79, 0x8000\n" \
    "s_cbranch_scc1 L%=_Pu3e\n" \
    "s_sleep 2\n" \
    "s_branch L%=_Pu3a\n" \
    "L%=_Pu4s:\n" \
    "s_mov_b32 s77, s79\n" \
    "s_memtime s[38:39]\n" \
    "s_memrealtime vcc\n" \
    "s_nop 4\n" \
    "s_memrealtime s[32:33]\n" \
    "s_nop 4\n" \
    "s_memrealtime s[34:35]\n" \
    "s_nop 4\n" \
    "s_memrealtime s[36:37]\n" \
    "s_add_u32 s80, s77, s64\n" \
    "s_lshl_b32 s80, s80, 2\n" \
    "s_waitcnt lgkmcnt(0)\n" \
    "s_sub_u32 s80, s80, vcc_lo\n" \
    "s_sub_u32 s80, s80, s32\n" \
    "s_sub_u32 s80, s80, s34\n" \
    "s_sub_u32 s80, s80, s36\n" \
    "s_mul_i32 s80, s80, s71\n" \
    "s_ashr_i32 s80, s80, 6\n" \
    "s_add_u32 s78, s80, s38\n" \
    "s_branch L%=_Pu4k\n" \
    "L%=_Pu5s:\n" \
    "s_cmp_gt_u32 s79, 0x8000\n" \
    "s_cbranch_scc1 L%=_Pu5e\n" \
    "s_sleep 2\n" \
    "s_branch L%=_Pu5a\n" \
    "L%=_Pvslow:\n" \
    "s_nop 7\n" \
    "s_nop 0\n" \
    "v_max_u32 v216, v168, v169\n" \
    "v_max3_u32 v216, v216, v170, v171\n" \
    "v_max3_u32 v216, v216, v172, v173\n" \
    "v_max3_u32 v216, v216, v174, v175\n" \
    "v_cmp_eq_u32 vcc, -1, v216\n" \
    "s_cbranch_vccz L%=_Pvok\n" \
    "s_cmp_eq_u32 s99, 0\n" \
    "s_cbranch_scc1 L%=_Pvok\n" \
    "s_sub_u32 s99, s99, 1\n" \
    "s_cmp_lg_u32 s99, 0\n" \
    "s_cbranch_scc1 L%=_Pu6\n" \
    "v_mov_b32 v216, 1\n" \
    "buffer_store_dword v216, off, s[44:47], 0 sc1\n" \
    "L%=_Pu6:\n" \
    "s_sleep 1\n" \
    "buffer_load_dwordx4 v[168:171], v225, s[40:43], s60 offen sc1\n" \
    "buffer_load_dwordx4 v[172:175], v225, s[40:43], s85 offen sc1\n" \
    "v_mov_b64 v[176:177], 0\n" \
    "v_mov_b64 v[178:179], 0\n" \
    "v_mov_b64 v[180:181], 0\n" \
    "v_mov_b64 v[182:183], 0\n" \
    "v_mov_b64 v[184:185], 0\n" \
    "v_mov_b64 v[186:187], 0\n" \
    "v_mov_b64 v[188:189], 0\n" \
    "v_mov_b64 v[190:191], 0\n" \
    "s_waitcnt vmcnt(0)\n" \
    "s_branch L%=_Pvc\n" \
    "L%=_Pfs:\n" \
    "s_nop 7\n" \
    "s_nop 0\n" \
    "v_mov_b64 v[196:197], 0\n" \
    "v_mov_b64 v[198:199], 0\n" \
    "v_mov_b64 v[200:201], 0\n" \
    "v_mov_b64 v[202:203], 0\n" \
    "v_mov_b64 v[204:205], 0\n" \
    "v_mov_b64 v[206:207], 0\n" \
    "v_mov_b64 v[208:209], 0\n" \
    "v_mov_b64 v[210:211], 0\n" \
    "v_mov_b64 v[212:213], 0\n" \
    "v_mov_b64 v[214:215], 0\n" \
    "s_branch L%=_Pfr\n" \
    "L%=_end:\n"
#define V8(b) "v" #b "0", "v" #b "1", "v" #b "2", "v" #b "3", "v" #b "4", "v" #b "5", "v" #b "6", "v" #b "7", "v" #b "8", "v" #b "9"
#define S_IO "+{s[40:55]}"(sA), "+{s[56:71]}"(sB), "+{s[72:75]}"(sC), "+{s78}"(lwo), "={s[92:99]}"(so)
#define FIN2() do {} while (0)
#define S_CLOB "memory", "vcc", "scc", "m0", "s32", "s33", "s34", "s35", "s36", "s37", "s38", "s39", "s76", "s77", "s79", "s80", "s81", "s82", "s83", "s84", "s85", "s86", "s87", "s88", "s89", "s90", "s91"
#define B_CLOB S_CLOB, "v64", "v65", "v66", "v67", "v68", "v69", V8(7), V8(8), V8(9), V8(10), V8(11), V8(12), V8(13), V8(14), V8(15), V8(16), V8(17), V8(18), V8(19), V8(20), V8(21), \
    "v220", "v221", "v222", "v223", "v240", "v241", "v242", "v243"
#define FIN() do {} while (0)

#define MK_S() \
    a = args(); \
    v16u sA = cat8(cat4(mkrs(a->ring, NSLOT * SLOT), mkrs(a->ctl, CTL_SZ)), cat4(mkrs(a->mb, NSEG * NH * 4096), mkrs(a->qkv + QSZ, QSZ))); \
    v16u sB = cat8(cat4(mkrs(a->qkv + 2 * (size_t)QSZ, QSZ), v4u{0, 0, 0, 0}), \
                   v8u{0, (unsigned)a->per0, (unsigned)a->d1, (unsigned)a->dv, (unsigned)a->d2, (unsigned)a->dp, (unsigned)a->slack, (unsigned)a->thr}); \
    const v4u rsc = __builtin_shufflevector(sA, sA, 4, 5, 6, 7)
#define HALVES() do { const unsigned u_ = beacon(rsc, b0, b1, 16); \
_Pragma("unroll") \
    for (int k = 0; k < NSLOT; ++k) sB[4 + k] = k * (unsigned)SLOT + ((u_ >> k) & 1) * 8192u; } while (0)

#define START() __hip_atomic_fetch_max((LAS unsigned*)(lds + L_MISC), (rt32() - spare) / sB[14] + 1, __ATOMIC_RELAXED, __HIP_MEMORY_SCOPE_WORKGROUP)

DEV void chain(LAS char* lds, const int c, const int w) {
    KArgs a = args();
    const int h = c >> 2, j = c & 3;
    const unsigned lb = (unsigned)(size_t)lds, lbw = lb + L_STG + w * 16384;
#define LANE() const int l = lane(), l15 = l & 15, g = l >> 4, hb = (l >> 1) & 1, q4 = l15 >> 2; const unsigned idx = hb ? 0xEEEE : 0x4444
    v4u sC, stv; v2u stw; unsigned stx; v8u so;
    unsigned lwo = w & 3, b0, b1;
    if (w < 4) {
        LANE();
        v16u wt[12], m;
            const v4u rw[3] = {mkrs(a->Wk, DM * HD * 2), mkrs(a->Wv, DM * HD * 2), mkrs(a->Wq, DM * HD * 2)};
            const unsigned vo = (l >> 2) * (HD * 2) + (l & 3) * 16, sw = (256 * w * HD + h * DH + 32 * j) * 2;
            auto issue = [&](int p) {
#pragma unroll
                for (int i = 0; i < 8; ++i) DMA16N(rw[p >> 1], vo, sw + ((p & 1) * 128 + i * 16) * (HD * 2), lbw + (p & 1) * 8192 + i * 1024); };
            {
                const v4u rp = w & 1 ? rw[2] : rw[1];
                const unsigned ps = ((rp[0] >> 7) + (c >> 5) * 16) & 63, po = w < 2 ? ((3 - ps) & 7) * 128 + (l & 1) * 1024 : (63 - ps < 16 && !(l & 1) ? (63 - ps) * 128 : OOB);
                if (a->pf & 1) asm volatile("s_mov_b32 m0, %0\n\ts_nop 0\n\tbuffer_load_dword %1, %2, 0 offen lds" :: "s"(lb + L_PF + w * 256), "v"(((c & 31) * 32 + (l >> 1)) * (HD * 2) + (c >> 5) * 2048 + po), "s"(rp) : "memory", "m0");
            }
            unsigned mr[16];
#pragma unroll
            for (int k = 0; k < 16; ++k) mr[k] = *(const unsigned*)(a->mem0 + (2 * q4 + (l & 1)) * DM + 256 * w + 16 * k + 4 * g + 2 * hb);
            SCHED();
            issue(0); issue(1);
            MK_S();
            unsigned spare = a->spare; asm volatile("" : "+s"(sA), "+s"(sB), "+s"(spare));
            const LAS char* pw = lds + L_STG + w * 16384 + (4 * (g >> 1) + (g & 1) + 2 * (q4 >> 1) + 16 * (q4 & 1)) * 64 + (l & 3) * 8;
#pragma unroll
            for (int p = 0; p < 6; ++p) {
                if (p < 5) WAITV(8); else WAITV(0);
#pragma unroll
                for (int k = 0; k < 2; ++k) wt[(2 * p + k + 4) % 12] = cat8(rd_w<2048, 512, 2560>(pw + (p & 1) * 8192 + k * 4096), rd_w<2048, 512, 2560>(pw + (p & 1) * 8192 + k * 4096 + 32));
                WAITL(0);
                if (p < 4) issue(p + 2);
                if (p == 1) { RBAR(); b0 = ((volatile LAS unsigned*)(lds + L_MISC))[3]; }
                if (p == 3) BC_LD(b1, rsc);
            }
#pragma unroll
            for (int k = 0; k < 16; k += 4) {
                m[k] = __builtin_amdgcn_perm(mr[k + 1], mr[k], 0x05040100u); m[k + 1] = __builtin_amdgcn_perm(mr[k + 3], mr[k + 2], 0x05040100u);
                m[k + 2] = __builtin_amdgcn_perm(mr[k + 1], mr[k], 0x07060302u); m[k + 3] = __builtin_amdgcn_perm(mr[k + 3], mr[k + 2], 0x07060302u);
            }
        const unsigned pq = nearo(H3_OFF + (c >> 5) * 16384) + w * 1024 + l * 16;
        const v8u va = {idx, pq, 0, 0, lb + L_MISC + w * 4, lb + L_MISC, 0, pq + 16384};
        sC = v4u{lb + w * 2176u, lb + w * 1280u, 0, 0};
        HALVES();
        sC[2] = h * 16384 + (KVS(a->ms >> (j < 2 ? 2 : 4), h) ^ sB[4] ^ 8192) + ((j ^ h) & 1) * 4096 + w * 1024;
        if (j >= 2) { sA[12] = sB[0]; sA[13] = sB[1]; }
        START();
        asm volatile(ASM_A : S_IO, "+{v[192:207]}"(m)
                     : "{v[0:31]}"(cat16(wt[0], wt[1])), "{v[32:63]}"(cat16(wt[2], wt[3])), "{v[64:95]}"(cat16(wt[4], wt[5])), "{v[96:127]}"(cat16(wt[6], wt[7])),
                       "{v[128:159]}"(cat16(wt[8], wt[9])), "{v[160:191]}"(cat16(wt[10], wt[11])), "{v[232:239]}"(va)
                     : S_CLOB, "v208", "v209", V8(21), V8(22), "v230", "v231", "v240", "v241", "v244", "v245", "v246", "v247");
        FIN();
    } else {
        LANE();
        const int bw = w - 4, l7 = l & 7;
        MK_S();
        BC_LD(b0, rsc);
        auto ldw = [&]() {
            const v4u rw = mkrs(a->Wo, HD * DM * 2);
            const unsigned vo = (l >> 3) * (DM * 2) + l7 * 16, sw = (h * DH * DM + 256 * j + 64 * bw) * 2;
#pragma unroll
            for (int i = 0; i < 16; ++i) DMA16N(rw, vo, sw + i * 8 * (DM * 2), lbw + i * 1024); };
        if (w != 7) ldw();
        else {
            asm volatile("s_waitcnt vmcnt(0)" : "+v"(b0) :: "memory");
            if (l < 24) ((LAS unsigned*)(lds + L_MISC))[l] = l ? (l == 3 ? b0 : 0) : rt32() / sB[14];
            WAITL(0);
            ldw();
        }
        RBAR();
        BC_LD(b1, rsc);
        WAITV(0);
        HALVES();
        const LAS char* pw = lds + L_STG + w * 16384 + (16 * (g & 1) + 8 * (g >> 1) + (q4 & 1) + 4 * (q4 >> 1)) * 128 + (l & 3) * 8;
#define RDO(o) rd_w<256, 4096, 4352>(pw + (o))
        const v32u wo0 = cat16(cat8(RDO(0), RDO(32)), cat8(RDO(64), RDO(96))), wo1 = cat16(cat8(RDO(8192), RDO(8224)), cat8(RDO(8256), RDO(8288)));
        const unsigned c3 = ((c >> 3) & 3) * 4096 + (c >> 5) * 1024 + (c & 7) * 128, h1 = nearo(H1_OFF + h * 8192), h1j = h1 + j * 2048;
        v16u va = {};
        va[0] = idx; va[1] = h1 + (g >> 1) * 2048 + 1280 + (l & 31) * 16;
        va[2] = nearo(H2_OFF + (32 * j + 8 * bw + q4 * 2 + (g >> 1)) * 4096) + h * 128 + ((g & 1) * 4 + (l & 3)) * 16;
        va[3] = lb + L_P + g * 320 + l15 * 16;
        va[4] = lb + (l & 1) * X_BLK + (l & 2) * X_FQ + (8 * (g >> 1) + (q4 & 1) + 4 * (q4 >> 1)) * 16 + (g & 1) * 8;
        va[5] = lb + L_MISC;
        v4u sel = {};
        if (w == 4) {
            const int tok = l & 7, u = l >> 5, z = (l >> 3) & 3;
            va[6] = h1 + (z == 0 ? 8 + u : (z == 1 ? 2 * u : (z == 2 ? 4 + 2 * u : 2 * u + 1))) * 128 + tok * 16;
            va[7] = h1 + (5 + 2 * u) * 128 + tok * 16 + (z & 1) * 8;
            va[8] = lb + L_P + ((z & 2) + u) * 320 + (4 * (tok >> 1) + 2 * (z & 1) + (tok & 1)) * 16;
            va[9] = l == 0 ? F_PG + c * 128 : OOB;
            va[10] = lb + L_STG + 65536 + (l >> 2) * 128 + (l & 3) * 8;
            va[11] = side((j * 256 + (h & 15) * 4) * 8192 + (sB[4] ^ WSIDE)) + (h >> 4) * (HD * DM * 2) + l * 16;
            const v4u rt = mkrs(a->wt, HD * DM * 4);
            sA[8] = rt[0]; sA[9] = rt[1]; sA[10] = rt[2];
            *(LAS v4u*)(lds + va[8]) = v4u{POI, POI, POI, POI};
            sC = v4u{__float_as_uint(SCALE * LOG2E), __float_as_uint(SCALE * LOG2E), 0, 0};
        } else if (w == 5) {
            va[6] = lb + L_PQK + (l15 >> 3) * 256 + (l & 1) * 544 + (l7 >> 1) * 64 + g * 16;
            va[7] = (l & 8) ? OOB : h1j + g * 128 + l7 * 16;
            va[8] = ((l & 8) || g < 2) ? OOB : h1j + 1024 + ((g & 1) * 8 + l7) * 16;
            va[9] = (l & 8) ? h * 4096 + j * 512 + (g * 8 + l7) * 16 : OOB;
            va[10] = lb + l * 16;
            sC = v4u{0, 0, 0, 0};
        } else if (w == 6) {
            const int rho = (l >> 1) & 15, tok = 2 * (rho >> 2) + (rho & 1), c4 = 2 * (l >> 5) + ((rho >> 1) & 1);
            va[6] = lb + ((l & 1) * 160 + (tok & 1) * 72 + (tok >> 1) * 16 + c4 * 4) * 4;
            va[7] = (l & 1) ? OOB : h1j + 1280 + (l >> 1) * 16;
            va[8] = lb + L_VT + tok * 64 + (l & 1) * 32 + c4 * 8;
            va[9] = lb + L_VT + q4 * 64 + (g & 1) * 32 + (l & 3) * 8;
            va[10] = l < 32 ? h * 4096 + 2048 + (32 * j + l) * 16 : OOB;
            va[11] = nearo(H2_OFF + c * 4096) + l * 16;
            va[12] = (l & 1) ? OOB : nearo(H3_OFF + (2 * (g >> 1) + (l15 >> 3)) * 16384 + c3) + ((l7 & 6) + (g & 1)) * 16;
#pragma unroll
            for (int i = 0; i < 4; ++i) sel[i] = (l7 == 2 * i ? 0x3f80u : 0u) | (l7 == 2 * i + 1 ? 0x3f800000u : 0u);
            sC = v4u{0, 0, 0, 0};
        } else {
            const rsrc_t rk = mkrsrc(a->qkv + QSZ, QSZ), rv = mkrsrc(a->qkv + 2 * (size_t)QSZ, QSZ);
            wait_flags(mkrsrc(a->ctl, CTL_SZ), fw(l & 1, h) + 12, 1, a->ctl);
            v4u pf[10];
            const unsigned hb0 = h * 16384 + (KVS(a->ms >> 4, h) ^ sB[4] ^ 8192), kx = j * 2048 + (KVS(a->ms >> 4, h) ^ KVS(a->ms >> 2, h));
#pragma unroll
            for (int i = 0; i < 8; ++i) pf[i] = LDB(rv, i, hb0);
            pf[8] = LDB(rk, 0, hb0 ^ kx); pf[9] = LDB(rk, 1, hb0 ^ kx);
            va[13] = LD4(mkrsrc(a->ctl, CTL_SZ), fw(l & 1, h) + 12, 0);
#pragma unroll
            for (int i = 0; i < 8; ++i) *(LAS v4u*)(lds + L_X + i * X_BLK + g * X_FQ + l15 * 16) = pf[i];
            *(LAS v4u*)(lds + L_K + l * 16) = pf[8]; *(LAS v4u*)(lds + L_K + 1024 + l * 16) = pf[9];
            FENCE(); ((volatile LAS unsigned*)(lds + L_MISC))[2] = 1;
            va[6] = l * 16; va[7] = lb + g * X_FQ + l15 * 16;
            va[8] = nearo(H2_OFF + c * 4096) + l * 16; va[9] = h1j + l * 16;
            va[10] = nearo(H3_OFF + (l >> 3) * 16384 + c3) + l7 * 16;
            va[11] = fw(l & 1, h) + 12;
            va[15] = l == 0 && (c & 31) == 0 ? F_CK + 384 + (c >> 5) * 4 : OOB;
            sC = v4u{hb0, __float_as_uint(a->per), 0, kx};
        }
        while (((volatile LAS unsigned*)(lds + L_MISC))[2] == 0) __builtin_amdgcn_s_sleep(1);
        asm volatile(ASM_B : S_IO, "+{v[224:239]}"(va), "+{v[244:247]}"(sel) : "{v[0:31]}"(wo0), "{v[32:63]}"(wo1) : B_CLOB);
        FIN();
    }
}

#define RING_LD "buffer_load_dwordx4 %5, %10, %11, %12 offen offset:%13 sc1\n\t"
#define RING_M(C, W, Z) "v_mfma_f32_16x16x32_bf16 %" #C ", %" #W ", %4, " Z "\n\t"
#define RING_W(C, O) "ds_write_b128 %14, %" #C " offset:" #O "\n\t"
#define RING_OP(K, T) asm volatile(T : "+v"(c[0]), "+v"(c[1]), "+v"(c[2]), "+v"(c[3]), "+v"(ring[(K) & 15]), "+v"(ring[((K) + 15) & 15]) \
    : "v"(wo[(K) & 7][0]), "v"(wo[(K) & 7][1]), "v"(wo[(K) & 7][2]), "v"(wo[(K) & 7][3]), "v"(l16), "s"(rso), "s"(nb[(((K) + 15) >> 2) & 3]), "n"((((K) + 15) & 3) * 1024), "v"(rb) : "memory")
#define RING_1(K, B, O) "buffer_load_dwordx4 %" #K ", %16, %17, %" #B " offen offset:" #O " sc1\n\t"
#define RING_4(K0, K1, K2, K3, B) RING_1(K0, B, 0) RING_1(K1, B, 1024) RING_1(K2, B, 2048) RING_1(K3, B, 3072)
#define RING_ALL(P) P(ring[0]), P(ring[1]), P(ring[2]), P(ring[3]), P(ring[4]), P(ring[5]), P(ring[6]), P(ring[7]), P(ring[8]), P(ring[9]), P(ring[10]), P(ring[11]), P(ring[12]), P(ring[13]), P(ring[14]), P(ring[15])
#define NPROBE 4
static constexpr int RED_B = 65536;
#define OS(k) do {} while (0)

DEV void load_wo(LAS char* lds, v4u rw, unsigned o, int w, int l, v4u (&wo)[8][4]) {
    const unsigned vo = (l >> 3) * (DM * 2) + (((l & 7) ^ ((l >> 3) & 6)) << 4), lb = (unsigned)(size_t)lds + w * 16384;
    auto issue = [&](int p) {
#pragma unroll
        for (int k = 0; k < 8; ++k) DMA16N(rw, vo, o + (64 * p + 8 * k) * (DM * 2), lb + (p & 1) * 8192 + k * 1024); };
    issue(0); issue(1);
    const int row = l >> 2;
#pragma unroll
    for (int p = 0; p < 4; ++p) {
        if (p < 3) WAITV(8); else WAITV(0);
#pragma unroll
        for (int t = 0; t < 4; ++t) {
            const LAS char* q = lds + w * 16384 + (p & 1) * 8192 + row * 128 + (((2 * t + ((l >> 1) & 1)) ^ (row & 6)) << 4) + (l & 1) * 8;
            wo[2 * p][t] = cat(trd(q), trd(q + 2048)); wo[2 * p + 1][t] = cat(trd(q + 4096), trd(q + 6144));
        }
        WAITL(0);
        if (p < 2) issue(p + 2);
    }
}

DEV void worker(LAS char* lds, const int wk, const int w) {
    KArgs a = args();
    const int tid = threadIdx.x, l = tid & 63, fr = l & 15;
    const int g = wk >> 5, i = wk & 31;
    const rsrc_t rc = mkrsrc(a->ctl, CTL_SZ);
    unsigned* err = a->ctl;
    unsigned* dbg = a->dbg + 32768 + wk * 32;
    WSTAMP(0);
    const v4u rx = mkrs(a->x, S_TOK * DM * 2);
    const int role = i >> 3;
    const int h = 8 * g + (i & 7);
    const unsigned me = F_W + wk * 128;
    unsigned far = 0;
    v4u rsc = mkrs(a->ctl, CTL_SZ);
    const unsigned t0 = rt32();
    if (role >= 2) {
        const rsrc_t rr = mkrsrc(a->ring, NSLOT * SLOT);
        unsigned b0, ak = 1;
        BC_LD(b0, rsc);
        if (role == 2) {
#pragma unroll
            for (int k = 0; k < 14; ++k) ST16(rr, (h >> 4) * (unsigned)SLOT + ((h & 15) * 14 + k) * 8192 + tid * 16, 0, (v4u{POI, POI, POI, POI}));
#pragma unroll
            for (int k = 0; k < 2; ++k) ST16(mkrsrc(a->mb, NSEG * NH * 4096), 60 * NH * 4096 + (h * 2 + k) * 8192 + tid * 16, 0, (v4u{POI, POI, POI, POI}));
        } else if (wk == 127) {
#pragma unroll
            for (int k = 0; k < 8; ++k) ST16(rc, k == 0 && (tid >> 3) == F_BC / 128 ? OOB : k * 8192 + tid * 16, 0, (v4u{0, 0, 0, 0}));
        } else if (i < 28 && w == 0) {
            unsigned t[2] = {~0u, ~0u};
            for (int it = 0; it < NPROBE; ++it) {
#pragma unroll
                for (int p = 0; p < 2; ++p) { const unsigned c0 = cyc32(); ST4(rr, (i & 3) * (unsigned)SLOT + p * 8192 + nearo(PR_OFF) + g * 256 + (l & 31) * 4, 0, c0); WAITV(0); t[p] = min(t[p], cyc32() - c0); }
            }
            ak = 256 + min(max((int)(t[1] - t[0]), -100), 100);
        }
        if (i >= 28 && i < 31 && (a->pf & 2)) {
            const unsigned u = (i & 3) * 8 + w, ro = (320 + (u >> 1) * 16 + (u & 1) * 512 + (l >> 2)) * (HD * 2) + g * 2048 + (l & 1) * 128 + (u < (a->pf >> 4) ? OOB : 0);
#pragma unroll
            for (int m = 0; m < 2; ++m) {
                const v4u rp = mkrs(m ? a->Wv : a->Wk, DM * HD * 2);
                const unsigned ps = ((rp[0] >> 7) + g * 16) & 63;
                asm volatile("s_mov_b32 m0, %0\n\ts_nop 0\n\tbuffer_load_dword %1, %3, 0 offen lds\n\tbuffer_load_dword %2, %3, 0 offen lds"
                             :: "s"((unsigned)(size_t)lds), "v"(ro + ((3 - ps) & 6) * 128 + (l & 2) * 512), "v"(63 - ps < 16 && !(l & 2) ? ro + ((63 - ps) & 14) * 128 : OOB), "s"(rp) : "memory", "m0");
            }
        }
        asm volatile("s_waitcnt vmcnt(0)\n\ts_barrier" : "+v"(b0) :: "memory");
        WSTAMP(1);
        if (w == 0) {
            if (wk != 127) beacon(rsc, b0, b0, 0);
            ST4(rc, l ? OOB : (wk == 127 ? F_BC : me), 0, ak);
            if (wk == 127) WAITV(0);
        }
        RBAR();
        const unsigned d = role == 2 ? a->wq : (wk == 127 ? 0 : a->wx);
        while (rt32() - t0 < d) __builtin_amdgcn_s_sleep(8);
#pragma unroll 1
        for (int k = role == 3 ? (wk == 127 && w < 2 ? 2 * w : 1) : 0; k > 0; --k) prep_rows(a->x, a->gw, a->w_r, rc, h * 64 + (w + 1 - k) * 8, l);
        if (w == 0 && wk == 127) {
            unsigned bc = 0, tl = rt32();
            for (unsigned tries = 0; tries < SPIN_CAP; ++tries) {
                FENCE();
                if (!(bc & 16)) {
                    unsigned v = LD4(rc, F_W + ((l >> 4) * 32 + 16 + fr) * 128, 0);
                    if (__builtin_amdgcn_ballot_w64(v == 0 && fr < 12) == 0) {
                        v += __shfl_xor((int)v, 16); v += __shfl_xor((int)v, 32);
                        const int d = (int)v - 1024;
                        int e = d - __shfl_xor(d, 1); e += __shfl_xor(e, 2);
                        bc |= 16 | ((unsigned)(__builtin_amdgcn_ballot_w64((abs(d) > 350 ? d : e) > 0) >> 8) & 15);
                        tl -= 4096;
                    }
                } else if (rt32() - t0 > 2400 && all_ge(LD4(rc, fr == 15 ? F_PG : F_W + ((fr >> 2) * 32 + 28 + (fr & 3)) * 128, 0), 1)) break;
                if (rt32() - tl > (bc & 16 ? 500 : 150)) { bc += 32; ST4(rc, l ? OOB : F_BC, 0, bc); tl = rt32(); }
                __builtin_amdgcn_s_sleep(8);
            }
            WSTAMP(5);
        }
        if (role == 3 && h >= NMOE) while (rt32() - t0 < 2600) __builtin_amdgcn_s_sleep(64);
    }
    if (role == 1) far = stream_qkv<true>(lds, rx, mkrs(a->Wv, DM * HD * 2), mkrsrc(a->qkv + 2 * (size_t)QSZ, QSZ), rc, rsc, true, me + 12, h, KVS(a->ms >> 4, h), a->pf, dbg);
    else if (role < 3) far = stream_qkv<false>(lds, rx, mkrs(role ? a->Wq : a->Wk, DM * HD * 2), mkrsrc(a->qkv + (role ? 0 : QSZ), QSZ), rc, rsc, role == 0, me + 12, h, role ? 0 : KVS(a->ms >> 2, h), a->pf, dbg);
    else if (h < NMOE) {
        const int sel = top8((LAS unsigned*)(lds + L_TOT - 256), rc, err, w, l);
        WSTAMP(26);
        if (h == 0 && tid < TOPC) ST4(mkrsrc(a->idx, 32), tid * 4, 0, sel);
        moe(lds, rx, sel, mkrs(a->wg, DM * FF * 2), mkrs(a->wu, DM * FF * 2), mkrs(a->wd, FF * DM * 2), mkrsrc(a->part, NMOE * TOPC * DM * 4), h);
        if (tid == 0) ST4(rc, me + 4, 0, 1u);
    }
    if (role == 3) far ^= FAR(beacon(rsc, 0, 0, 16));
    WSTAMP(2);
    a = args();
    const rsrc_t ri = mkrsrc(a->idx, 32), ru = mkrsrc(a->upd, TOPC * DM * 4);
    const int cg = role == 3 ? 4 * g + (i & 3) : (role == 2 ? h & 15 : i);
    if (role == 3 && i < 28) {
        const rsrc_t rq = mkrsrc(a->qkv, QSZ), rk = mkrsrc(a->qkv + QSZ, QSZ), rv = mkrsrc(a->qkv + 2 * (size_t)QSZ, QSZ), rm = mkrsrc(a->mb, NSEG * NH * 4096);
        unsigned fl = 0;
        LAS unsigned* cnt = (LAS unsigned*)(lds + L_TOT - 512);
        cnt[lane() & 15] = 0;
        BAR();
#pragma unroll 1
        for (int xs = i & 3; xs < NSEG; xs += 4) {
            if ((xs & 60) == (a->wi & 60)) {
                const int l = lane();
                wait_flags<16>(rc, fw(3, min(l, NMOE - 1)) + 4, 1, err);
                const rsrc_t rp = mkrsrc(a->part, NMOE * TOPC * DM * 4);
                const unsigned t = LD4(ri, w * 4, 0), o = w * 4096 + cg * 256 + (l & 15) * 16;
                v4f p = {0, 0, 0, 0};
#pragma unroll
                for (int q = 0; q < 7; ++q) p += __builtin_bit_cast(v4f, LD16(rp, o + (l >> 4) * 7 * 32768, q * 32768));
                const float r = rbf(sigmoidf_(hi16(LD4(rc, F_GR + t * 8 + 4, 0))));
#pragma unroll
                for (int e = 0; e < 4; ++e) p[e] = rbf(rbf(red_g<false>(p[e])) * r);
                ST16(ru, l < 16 ? o : OOB, 0, __builtin_bit_cast(v4u, p));
            }
            if ((xs & 60) == a->wi >> 8 && w == 7) {
                const int l = lane();
                const unsigned p = min(LD4(rc, l < 3 ? F_W + (l * 32 + 32 + cg) * 128 + 12 : (l < 5 ? fw(2, 16 * l - 48 + cg) + 12 : OOB), 0), 63u);
                const float tau = (float)((rt32() - t0 - (l < 3 ? 1100 : a->ws)) & 0xffffu) * 8.0f / (p + 1);
                unsigned key = l < 5 ? (unsigned)(tau * (63 - p)) * 8 + l : ~0u;
                const unsigned inc = (unsigned)(tau * (0.01f * (a->gap & 255))) * 8;
                u64 m = l ? (0x11ull << 55) << l : 0;
#pragma unroll 1
                for (int s = 0; s < 56; ++s) {
                    if (s == (a->gap >> 8) && !l) key = ~0u;
                    unsigned u = min(key, ror<4>(key)); u = min(u, ror<2>(u)); u = min(u, ror<1>(u));
                    if (key == __builtin_amdgcn_readlane(u, 7)) { key += inc; m |= 1ull << s; }
                }
                __builtin_amdgcn_raw_buffer_store_b64(__builtin_bit_cast(v2u, m), rc, l < 5 ? me + 16 + l * 8 : OOB, 0, 16);
            }
            fl = xattn(rq, rk, rv, rm, rc, err, xs, 8 * g + w, fl, far, a->ms, cnt, dbg); WSTAMP(9 + (xs >> 2));
        }
        WSTAMP(25);
        return;
    }
    auto proj = [&](const bool rcv) __attribute__((always_inline)) {
    const int l = lane(), tid = w * 64 + l, fr = l & 15, fq = l >> 4;
    a = args();
    const rsrc_t ri = mkrsrc(a->idx, 32), ru = mkrsrc(a->upd, TOPC * DM * 4), rt = mkrsrc(a->wt, HD * DM * 4);
    const int k0 = (rcv ? 0 : 64) + 8 * w;
    u64 mine = role == 3 ? ~(a->segs | 9ull << 60) : (g ? 9ull << 60 : a->segs);
    auto next = [&]() { int s; asm volatile("s_ff1_i32_b64 %0, %1\n\ts_bitset0_b64 %1, %0\n\ts_min_u32 %0, %0, 64" : "=&s"(s), "+s"(mine) :: "scc"); return s; };
    auto fx = [&](int s, int l) { return fw(3, (rcv ? 0 : 16) + (l & 1) * 8 + (s & 3)) + 64 + (s & 60); };
    const unsigned fe = F_EX + cg * 256;
    volatile LAS unsigned* prog = (volatile LAS unsigned*)(lds + 2 * RED_B);
    LAS v4f* upd = (LAS v4f*)(lds + 2 * RED_B + 16384);
    v4u wo[8][4];
    u64 hit = 0;
    int sel = 0;
    if (rcv || role < 3) {
        const unsigned fs = fw(3, 2 * (cg & 12) + (cg & 3));
        const unsigned wb = side((cg * 64 + 8 * w) * 8192 + (far ^ WSIDE ^ 8192)) + (rcv ? 0 : HD * DM * 2);
        wait_flags(rc, l & 2 ? F_PG + (k0 + (l & 1) * 4 + (cg >> 2)) * 128 : fs + 64 + (l & 1 ? a->wi & 60 : a->wi >> 8), l & 2 ? WO_DONE : rcv, err);
        sel = LD4(ri, rcv ? (l & 7) * 4 : OOB, 0);
        const v2u m = LD8(rc, fs + 8 + 8 * (role < 2 ? g : 4 + (g >> 1)), 0);
        const v4u u = LD16(ru, w < 2 && rcv ? (tid >> 4) * 4096 + cg * 256 + fr * 16 : OOB, 0);
#pragma unroll
        for (int k = 0; k < 32; ++k) wo[k >> 2][k & 3] = LDB(rt, k & 7, wb + (k >> 3) * 16384);
        if (w < 2) upd[tid] = __builtin_bit_cast(v4f, u);
#pragma unroll
        for (int q = 0; q < 8; ++q) hit |= 1ull << (__builtin_amdgcn_readlane(sel, q) >> 5);
        if (rcv) mine = (u64)__builtin_amdgcn_readfirstlane(m[1]) << 32 | (unsigned)__builtin_amdgcn_readfirstlane(m[0]);
    } else load_wo(lds, mkrs(a->Wo, HD * DM * 2), (32 * k0 * DM + 64 * cg) * 2, w, l, wo);
    WSTAMP(7);
    __builtin_amdgcn_s_waitcnt(0x0f70);
    bool pend = false;
    v4u ring[16];
    asm volatile("" : RING_ALL("=v"));
    const v4u rso = mkrs(a->qkv, QSZ), rse = mkrs(a->qkv + QSZ, QSZ);
    rsc = mkrs(a->ctl, CTL_SZ);
    const rsrc_t re = mkrsrc(a->qkv + QSZ, QSZ), rout = mkrsrc(a->out, S_TOK * DM * 2);
    BAR();
    const unsigned l16 = l * 16, lw = (unsigned)(size_t)lds + w * 1024, r0 = (w >> 2) * 16 + fr;
    const unsigned xo = rcv ? (r0 * DM + 64 * cg + (w & 3) * 16 + 4 * fq) * 2 : OOB, go = F_GR + r0 * 8, so = w ? OOB : (l < 2 ? fx(0, l) : (rcv ? fe : OOB));
    auto state = [&](int n) { return (unsigned)__builtin_amdgcn_ballot_w64(prog[(n & 1) * 2048 + l * 4] != 0); };
    auto sgo = [&](int s) { return s * 0x80800u & 0xfff82000u; };
    const unsigned co = k0 * 4096u + far, ce = (16 + cg) * 16384u + far + w * 1024;
    auto ask1 = [&](int o) { return o * 0x1000081u & (OOB | 0x1bcu); };
    auto ask2 = [&](unsigned u, int e) { unsigned v; asm("s_lshl_b32 %1, %2, 2" : "+s"(u), "=s"(v) : "s"(e) : "scc"); return l16 < 32 ? u : v; };
    auto ask = [&](int o, int e) { return so + ask2(ask1(o), e); };
    auto both = [&](int s, int o) { unsigned t = l16; asm volatile("" : "+v"(t)); const unsigned q = ask(o, s), f = fx(s, t >> 4); return l16 < 64 ? q : f; };
    unsigned st;
    WSTAMP(8);
    v4f r[5];
    const LAS char* rp;
    auto get = [&]() {
#pragma unroll
        for (int q = 0; q < 5; ++q) r[q] = *(const LAS v4f*)(rp + q * 1024); };
    unsigned sm; v2u xv; float gt; v4f pv;
    v4f pw; float tb[4], gw, omg;
#define PIN(...) do { asm volatile("" : __VA_ARGS__); SCHED(); } while (0)
    auto prep = [&](int k, int seg) {
        if (!rcv) return;
        if (k == 0) { gw = hi16(__float_as_uint(gt)); omg = rbf(1.0f - gw); PIN("+v"(gw), "+v"(omg)); }
        if (k == 1) { tb[0] = lo16(xv[0]); tb[1] = hi16(xv[0]); PIN("+v"(tb[0]), "+v"(tb[1])); }
        if (k == 2) { tb[2] = lo16(xv[1]); tb[3] = hi16(xv[1]); PIN("+v"(tb[2]), "+v"(tb[3])); }
        if (k == 3 && seg >= 0) {
            unsigned t = l16, g8 = go; asm volatile("" : "+v"(t), "+v"(g8));
#pragma unroll 1
            for (int q = 0; q < TOPC; ++q) if (__builtin_amdgcn_readlane(sel, q) == seg * 32 + (int)(g8 - F_GR) / 8) {
                const v4f u = upd[q * 16 + (w & 3) * 4 + (t >> 8)];
#pragma unroll
                for (int e = 0; e < 4; ++e) tb[e] = rbf(tb[e] + u[e]); }
        }
        if (k == 4) { tb[0] = mul1(omg, tb[0]); tb[1] = mul1(omg, tb[1]); tb[2] = mul1(omg, tb[2]); PIN("+v"(tb[0]), "+v"(tb[1]), "+v"(tb[2])); }
        if (k == 5) { tb[3] = mul1(omg, tb[3]); tb[0] = rbf(tb[0]); tb[1] = rbf(tb[1]); PIN("+v"(tb[0]), "+v"(tb[1]), "+v"(tb[3])); }
        if (k == 6) { tb[2] = rbf(tb[2]); tb[3] = rbf(tb[3]); PIN("+v"(tb[2]), "+v"(tb[3])); }
        if (k == 7) { pw = pv; PIN("+v"(pw)); } };
    auto fin = [&](int seg, bool got, unsigned eo) {
        v4f acc = r[0];
#pragma unroll
        for (int q = 1; q < 8; ++q) { acc = add4(acc, r[q < 5 ? q : q - 4]); SCHED(); if (q < 4) r[q] = *(const LAS v4f*)(rp + (q + 4) * 1024); }
        if (rcv) {
            if (!got) { wait_flags(rc, fe + seg * 4, 1, err); pw = __builtin_bit_cast(v4f, LD16(re, l16, eo)); asm volatile("" : "+v"(pw)); TS(tid == 0, seg, 2); }
            acc = add4(acc, pw);
#pragma unroll
            for (int e = 0; e < 4; ++e) acc[e] = add1(rbf(mul1(gw, rbf(acc[e]))), tb[e]);
            __builtin_amdgcn_raw_buffer_store_b64(v2u{pk2(acc[0], acc[1]), pk2(acc[2], acc[3])}, rout, xo, seg << 16, 0);
        } else ST16(re, l16, eo, __builtin_bit_cast(v4u, acc));
        FENCE(); };
    asm volatile("" : "=v"(sm), "=v"(pv), "=v"(xv), "=v"(gt));
    bool got = false;
#define SM_LD "buffer_load_dword %[sm], %[so], %[rc], 0 offen sc1\n\tbuffer_load_dwordx4 %[pv], %[l16], %[re], %[eo] offen sc1\n\tbuffer_load_dwordx2 %[xv], %[xo], %[rx], %[xs] offen\n\tbuffer_load_dword %[gt], %[go], %[rc], %[gs] offen sc1\n\t"
#define SM_O [sm] "+v"(sm), [pv] "+v"(pv), [xv] "+v"(xv), [gt] "+v"(gt)
#define SM_I(QA) [rc] "s"(rsc), [re] "s"(rse), [rx] "s"(rx), [so] "v"(QA), [l16] "v"(l16), [eo] "s"(got ? eo : OOB), [xo] "v"(xo), [xs] "s"(seg << 16), [go] "v"(go), [gs] "s"(seg << 8)
    int seg = next(), s1 = next(), s2 = next(), s3;
    unsigned o1 = sgo(s1) ^ co, eo = sgo(seg) ^ ce, e1 = 0, qa;
#pragma unroll 1
    for (int n = 0, p1 = OOB / 4, p2 = p1, p3 = p1; seg < NSEG; ++n, p3 = p2, p2 = p1, p1 = seg, seg = s1, s1 = s2, s2 = s3) {
        if (!pend) {
            const unsigned ob = eo ^ ce ^ co;
            unsigned v = ring[0][0];
            asm volatile("" : "+v"(v));
            const bool now = n && rcv && (__builtin_amdgcn_ballot_w64(v != 0) & 48) == 48;
#define RING_GO(T) asm volatile(RING_4(0, 1, 2, 3, 18) RING_4(4, 5, 6, 7, 19) RING_4(8, 9, 10, 11, 20) RING_4(12, 13, 14, 15, 21) T \
                         : RING_ALL("+v") : "v"(l16), "s"(rso), "s"(ob), "s"(ob ^ 16384), "s"(ob ^ 4096), "s"(ob ^ 20480) : "memory")
            for (unsigned tries = 0; !now; ++tries) {
                FENCE(); v = LD4(rc, both(seg, s1), 0); __builtin_amdgcn_s_waitcnt(0x0f70); FENCE();
                if (!rcv && !tries && n) { RBAR(); if (tid < 3) ST4(rc, fe + (tid ? (tid == 1 ? p2 : p3) : p1) * 4, 0, 1u); TS(tid == 0, p1, 2); }
                if ((__builtin_amdgcn_ballot_w64(v != 0) & 48) == 48) break;
                __builtin_amdgcn_s_sleep(6);
                if (spin_fail(tries, err)) break;
            }
            TS(tid == 0, seg, 0); RING_GO("");
            prog[(~n & 1) * 2048 + tid * 4] = v;
            BAR();
            st = state(n + 1); got = st & 4;
            asm volatile(SM_LD : SM_O : SM_I(ask(s2, seg)) : "memory");
            ST4(rc, OOB, 0, 0u);
        }
        const bool pre = (st & 3) == 3;
        unsigned nb[4], rb = lw + (n & 1) * RED_B + l16, pa = lw + 2 * RED_B + (n & 1) * 8192 + l16;
        nb[0] = pre ? o1 : OOB; nb[1] = nb[0] ^ 16384; nb[2] = nb[0] ^ 4096; nb[3] = nb[0] ^ 20480;
        v4f c[4];
        unsigned en = o1 ^ co ^ ce, u;
        int sp;
        asm volatile("" : "+s"(nb[0]), "+s"(nb[1]), "+s"(nb[2]), "+s"(nb[3])); asm volatile("" : "+v"(rb), "+v"(pa), "=v"(c[0]), "=v"(c[1]), "=v"(c[2]), "=v"(c[3]));
        OS(0);
        if (pend || (pre && w >= 4)) BAR();
        OS(1);
        RING_OP(0, "s_setprio 0\n\ts_waitcnt vmcnt(20)\n\t" RING_M(0, 6, "0") RING_M(1, 7, "0") "s_waitcnt vmcnt(19)\n\t" RING_M(2, 8, "0") RING_M(3, 9, "0"));
#pragma unroll
        for (int k = 1; k < 16; ++k) {
            if (k == 8) RING_OP(k, RING_LD "s_nop 3\n\t" RING_W(0, 0) RING_M(0, 6, "0") RING_W(1, 8192) RING_M(1, 7, "0") RING_W(2, 16384) RING_M(2, 8, "0") RING_W(3, 24576) RING_M(3, 9, "0") "s_waitcnt vmcnt(19)");
            else if (k > 5 && (k < 13 || (k == 14 && !rcv))) RING_OP(k, RING_M(0, 6, "%0") RING_LD RING_M(1, 7, "%1") "s_waitcnt vmcnt(19)\n\t" RING_M(2, 8, "%2") RING_M(3, 9, "%3"));
            else {
                if (k == 15) { RING_OP(k, RING_M(0, 6, "%0") RING_LD RING_M(1, 7, "%1")); } else RING_OP(k, RING_M(0, 6, "%0") RING_LD RING_M(1, 7, "%1") "s_waitcnt vmcnt(19)");
                SCHED();
                if (k == 13) { asm volatile("s_waitcnt vmcnt(14)\n\tds_write_b32 %4, %0" : SM_O : "v"(pa) : "memory"); SCHED(); }
                if (k >= 14 && PIPE(3 * k - 40)) prep(3 * k - 40, sp);
                RING_OP(k, RING_M(2, 8, "%2")); SCHED();
                if (k == 1) s3 = next();
                if (k == 2) { qa = ask2(u, s1); PIN("+v"(qa)); }
                if (k == 3) { u = s2; PIN("+s"(u)); o1 = sgo(u) ^ co; PIN("+s"(o1)); }
                if (k == 4) { u = n; PIN("+s"(u)); u = (~u & 1) * RED_B; PIN("+s"(u)); }
                if (k == 5 && rcv) asm volatile("s_bitcmp1_b64 %1, %0\n\ts_cselect_b32 %0, %0, -1" : "+s"(sp) : "s"(hit) : "scc");
                if (k >= 13 && PIPE(3 * k - 39)) prep(3 * k - 39, sp);
                RING_OP(k, RING_M(3, 9, "%3")); SCHED();
                if (k == 1) { u = ask1(s3); PIN("+s"(u)); }
                if (k == 2) { qa += so; PIN("+v"(qa)); }
                if (k == 3 && rcv) { sp = pend ? p1 : seg; PIN("+s"(sp)); }
                if (k == 4) { rp = lds + (u + w * 8192 + l16); PIN("+v"(rp)); }
                if (k >= 13 && PIPE(3 * k - 38)) prep(3 * k - 38, sp);
            }
            if (k == 8 && !pre && rcv) asm volatile("buffer_load_dwordx4 %0, %1, %2, 0 offen sc1" : "+v"(ring[0]) : "v"(both(s1, s2)), "s"(rsc) : "memory");
        }
        RING_OP(16, RING_LD "s_setprio 1");
        OS(2); TS(tid == 256 && !pre, seg, 1);
        if (pre || pend) BAR();
        OS(3);
        pa -= lw;
        asm volatile("ds_read_b32 %0, %0\n\ts_nop 3\n\tds_write_b128 %5, %1 offset:32768\n\tds_write_b128 %5, %2 offset:40960\n\tds_write_b128 %5, %3 offset:49152\n\tds_write_b128 %5, %4 offset:57344\n\ts_waitcnt lgkmcnt(4)"
                     : "+v"(pa) : "v"(c[0]), "v"(c[1]), "v"(c[2]), "v"(c[3]), "v"(rb) : "memory");
        OS(4);
#pragma unroll
        for (int k = 0; k < 8; ++k) if (!PIPE(k)) prep(k, sp);
        if (pre || pend) {
            get();
            const bool got1 = got;
            st = __builtin_amdgcn_ballot_w64(pa != 0); got = st & 4;
            asm volatile(SM_LD : SM_O : SM_I(qa) : "memory");
            OS(5);
            if (pend) fin(p1, got1, e1); else ST4(rc, OOB, 0, 0u);
            if (!rcv && tid == 256) ST4(rc, fe + p3 * 4, 0, 1u);
        }
        OS(6);
        if (!pre) {
            BAR();
            if (pend && w < 4) RBAR();
            rp = lds + ((n & 1) * RED_B + w * 8192 + l16);
            get();
            asm volatile("s_waitcnt vmcnt(1)" : SM_O, "+v"(ring[0]) :: "memory");
            if (pend) {
#pragma unroll
                for (int k = 0; k < 8; ++k) prep(k, (hit >> seg) & 1 ? seg : -1);
            }
            fin(seg, got, eo);
            if (!rcv && s1 == NSEG) { WAITV(0); RBAR(); if (tid < 3) ST4(rc, fe + (tid ? (tid == 1 ? p1 : p2) : seg) * 4, 0, 1u); }
        }
        TS(tid == 0 && !pre, seg, 3);
        pend = pre; e1 = eo; eo = en;
        if (!rcv && (seg & 12) == 12) WSTAMP(9 + (seg >> 2));
    }
    asm volatile("s_waitcnt vmcnt(0)" :: RING_ALL("v") : "memory");
    };
    if (role == 2 || (role < 2 && g)) proj(true);
    if (role == 3 || (role < 2 && g < 2)) proj(false);
    WSTAMP(25);
}

__global__ void __launch_bounds__(512, 2) k_main(Args) {
    __shared__ __attribute__((aligned(16))) char lds[L_TOT];
    const int b = blockIdx.x, c = (b & 3) * 32 + (b >> 3), w = __builtin_amdgcn_readfirstlane(threadIdx.x >> 6);
    if ((b & 7) < 4) chain((LAS char*)lds, c, w); else worker((LAS char*)lds, c, w);
}

#define RING_MB 160
#define QKV_MB 0
#define TUNE_DEFAULT {184, 73, 77, 138, 11, 80, 344, 100, 900, 700, 7, 20, 214, 32, 32, 20}

extern "C" void kernel_launch(void* const* d_in, const int* in_sizes, int n_in, void* d_out, int out_size, void* d_ws, size_t ws_size, hipStream_t stream) {
    char* ring = (char*)(((size_t)d_ws + SLOT - 1) & ~(size_t)(SLOT - 1));
    char* ws = ring + NSLOT * SLOT; size_t off = 0;
    if (ws_size >= (256u << 20)) { ring += (size_t)RING_MB << 20; off = (size_t)QKV_MB << 20; }
    auto alloc = [&](size_t bytes) { void* p = ws + off; off += (bytes + 255) & ~(size_t)255; return p; };
    Args a;
    a.x = (const bf16_t*)d_in[0]; a.mem0 = (const bf16_t*)d_in[1]; a.Wq = (const bf16_t*)d_in[2]; a.Wk = (const bf16_t*)d_in[3]; a.Wv = (const bf16_t*)d_in[4]; a.Wo = (const bf16_t*)d_in[5];
    a.w_r = (const bf16_t*)d_in[8]; a.wg = (const bf16_t*)d_in[9]; a.wu = (const bf16_t*)d_in[10]; a.wd = (const bf16_t*)d_in[11]; a.gw = (const bf16_t*)d_in[12];
    a.qkv = (char*)alloc(3 * (size_t)QSZ); a.wt = (char*)alloc(HD * DM * 4); a.mb = (char*)alloc(NSEG * NH * 4096);
    a.idx = (int*)alloc(256); a.part = (float*)alloc(NMOE * TOPC * DM * 4); a.upd = (float*)alloc(TOPC * DM * 4); a.out = (bf16_t*)d_out;
    a.ring = ring; a.ctl = (unsigned*)alloc(CTL_SZ); a.dbg = (unsigned*)alloc((65536 + 1024 * 512) * 4);
    const int tn[16] = TUNE_DEFAULT;
    a.per = tn[0] * 368.0f; a.per0 = tn[12]; a.pf = tn[10]; a.ms = tn[11]; a.ws = tn[8] + 950; a.wi = tn[15] | tn[13] << 8; a.gap = 135 | 48 << 8;
    a.d1 = ((u64)tn[1] << 32) / tn[0]; a.dv = ((u64)tn[2] << 32) / tn[0]; a.d2 = ((u64)tn[3] << 32) / tn[0]; a.dp = tn[4]; a.slack = tn[5]; a.thr = tn[6]; a.spare = tn[7]; a.wq = tn[8]; a.wx = tn[9];
    a.segs = 0x2555555555555555ull >> tn[14] << tn[14];
    k_main<<<256, 512, 0, stream>>>(a);
}
```

```cpp
#include <hip/hip_runtime.h>
#include <stdint.h>

typedef unsigned short bf16_t;
typedef unsigned v2u __attribute__((ext_vector_type(2)));
typedef unsigned v4u __attribute__((ext_vector_type(4)));
typedef unsigned v8u __attribute__((ext_vector_type(8)));
typedef float v2f __attribute__((ext_vector_type(2)));
typedef float v4f __attribute__((ext_vector_type(4)));
typedef short v4s __attribute__((ext_vector_type(4)));
typedef __bf16 v2b __attribute__((ext_vector_type(2)));
typedef __bf16 v8b __attribute__((ext_vector_type(8)));
typedef __bf16 v16b __attribute__((ext_vector_type(16)));
typedef unsigned long long u64;
typedef __amdgpu_buffer_rsrc_t rsrc_t;
#define LAS __attribute__((address_space(3)))
#define DEV __device__ __forceinline__

static constexpr int S_TOK = 2048, DM = 1024, NH = 32, DH = 128, SEG = 32, NSEG = 64;
static constexpr int HD = NH * DH;
static constexpr int FF = 3584, TOPC = 8;
static constexpr float SCALE = 0.08838834764831845f, LOG2E = 1.4426950408889634f, MASKV = -998244352.0f;

DEV unsigned pk2(float lo, float hi) { v2f f = {lo, hi}; return __builtin_bit_cast(unsigned, __builtin_convertvector(f, v2b)); }
DEV float lo16(unsigned u) { return __uint_as_float(u << 16); }
DEV float hi16(unsigned u) { return __uint_as_float(u & 0xffff0000u); }
DEV float rbf(float f) { return __uint_as_float(pk2(0.0f, f)); }
DEV v4f smf(v4u a, v8u b, v4f c, int idx) { return __builtin_amdgcn_smfmac_f32_16x16x64_bf16(__builtin_bit_cast(v8b, a), __builtin_bit_cast(v16b, b), c, idx, 0, 0); }
DEV v4f mf(v4u a, v4u b, v4f c) { return __builtin_amdgcn_mfma_f32_16x16x32_bf16(__builtin_bit_cast(v8b, a), __builtin_bit_cast(v8b, b), c, 0, 0, 0); }
DEV v2u trd(const LAS char* p) { return __builtin_bit_cast(v2u, __builtin_amdgcn_ds_read_tr16_b64_v4i16((LAS v4s*)p)); }
DEV v4u cat(v2u a, v2u b) { return __builtin_shufflevector(a, b, 0, 1, 2, 3); }
DEV v2u sw16(unsigned x, unsigned y) { return __builtin_amdgcn_permlane16_swap(x, y, false, false); }
DEV v2u sw32(unsigned x, unsigned y) { return __builtin_amdgcn_permlane32_swap(x, y, false, false); }
DEV float half_add(float x, float y) { v2u r = sw32(__float_as_uint(x), __float_as_uint(y)); return __uint_as_float(r[0]) + __uint_as_float(r[1]); }
DEV float ror8(float v) { return __int_as_float(__builtin_amdgcn_update_dpp(0, __float_as_int(v), 0x128, 0xf, 0xf, false)); }
template <bool MAX> DEV float red_g(float v) {
    auto op = [](float a, float b) { return MAX ? fmaxf(a, b) : a + b; };
    v2u r = sw16(__float_as_uint(v), __float_as_uint(v)); v = op(__uint_as_float(r[0]), __uint_as_float(r[1]));
    r = sw32(__float_as_uint(v), __float_as_uint(v)); return op(__uint_as_float(r[0]), __uint_as_float(r[1]));
}
DEV float add1(float a, float b) { float r; asm("v_add_f32 %0, %1, %2" : "=v"(r) : "v"(a), "v"(b)); return r; }
DEV float mul1(float a, float b) { float r; asm("v_mul_f32 %0, %1, %2" : "=v"(r) : "v"(a), "v"(b)); return r; }
DEV v4f add4(v4f a, v4f b) { return v4f{add1(a[0], b[0]), add1(a[1], b[1]), add1(a[2], b[2]), add1(a[3], b[3])}; }
DEV int lane() { int l; asm volatile("v_mbcnt_lo_u32_b32 %0, -1, 0\n\tv_mbcnt_hi_u32_b32 %0, -1, %0" : "=v"(l)); return l; }
DEV float sigmoidf_(float z) { return 1.0f / (1.0f + __expf(-z)); }
DEV v4u mkrs(const void* p, unsigned bytes) { const u64 a = (u64)p; return v4u{(unsigned)a, (unsigned)(a >> 32) & 0xffffu, bytes, 0x00020000u}; }
DEV rsrc_t mkrsrc(const void* p, unsigned bytes) { return __builtin_amdgcn_make_buffer_rsrc((void*)p, (short)0, (int)bytes, 0x00020000); }

#define ST16(RS, OFF, SOFF, V) __builtin_amdgcn_raw_buffer_store_b128(V, RS, (OFF) + (SOFF), 0, 16)
#define NTS 0
#define ST16X(RS, OFF, V, NT) __builtin_amdgcn_raw_buffer_store_b128(V, RS, OFF, 0, (NT) ? 18 : 16)
#define LD16X(RS, OFF, SOFF, NT) __builtin_amdgcn_raw_buffer_load_b128(RS, OFF, SOFF, (NT) ? 18 : 16)
#define ST4(RS, OFF, SOFF, V) __builtin_amdgcn_raw_buffer_store_b32(V, RS, OFF, SOFF, 16)
#define LD16(RS, OFF, SOFF) __builtin_amdgcn_raw_buffer_load_b128(RS, OFF, SOFF, 16)
#define LD8(RS, OFF, SOFF) __builtin_amdgcn_raw_buffer_load_b64(RS, OFF, SOFF, 16)
#define LD4(RS, OFF, SOFF) __builtin_amdgcn_raw_buffer_load_b32(RS, OFF, SOFF, 16)
#define DMA16(RS, VOFF, SOFF, LOFF) asm volatile("s_mov_b32 m0, %0\n\ts_nop 0\n\tbuffer_load_dwordx4 %1, %2, %3 offen lds" :: "s"(LOFF), "v"(VOFF), "s"(RS), "s"(SOFF) : "memory", "m0")
#define DMA16N(RS, VOFF, SOFF, LOFF) asm volatile("s_mov_b32 m0, %0\n\ts_nop 0\n\tbuffer_load_dwordx4 %1, %2, %3 offen nt lds" :: "s"(LOFF), "v"(VOFF), "s"(RS), "s"(SOFF) : "memory", "m0")
#define WSTAMP(k) do {} while (0)
#define TS(c, s, k) do {} while (0)
#define NPIPE 8
#define PIPE(k) ((k) < NPIPE)
#define Z62 12
#define Z63 24
#define ZP 3
#define XE 59
#define CPF 1
#define CM 9ull
#define CSD 48
#define WQ 900
#define XB 28
#define WSIDE 8192
#define EXP 0
#define XI 3
#define BK 9
#define FENCE() asm volatile("" ::: "memory")
#define WAITV(n) asm volatile("s_waitcnt vmcnt(" #n ")" ::: "memory")
#define WAITL(n) asm volatile("s_waitcnt lgkmcnt(" #n ")" ::: "memory")
#define RBAR() asm volatile("s_barrier" ::: "memory")
#define BAR() asm volatile("s_waitcnt lgkmcnt(0)\n\ts_barrier" ::: "memory")
#define SCHED() __builtin_amdgcn_sched_barrier(0)

#define KVS(m, h) 8192u
#define FAR(u) ((~(u) & 1u) << 13)
DEV unsigned side(unsigned o) { return o ^ ((o >> 8) & 8192u); }
static constexpr int XP = 1056, XS = 32 * XP, L_RED = 4 * XS;

DEV unsigned wt_src(int tid, unsigned ldw) { return (tid >> 4) * ldw + (((tid & 15) ^ ((((tid >> 4) & 3) << 2) | (((tid >> 7) & 1) << 1))) << 4); }
DEV int wt_rd(int w, int l, int n) { const int fr = l & 15, fq = l >> 4; return (w >> 2) * 16384 + (8 * fq + (fr >> 2)) * 256 + (((4 * (w & 3) + 2 * n + ((fr >> 1) & 1)) ^ (((fr >> 2) << 2) | ((fq & 1) << 1))) << 4) + (fr & 1) * 8; }
DEV v4u wt_frag(const LAS char* p) { return cat(trd(p), trd(p + 1024)); }
#define WT_ISSUE(R0, R1, VB, S0, S1, LDW, LB) do { DMA16N(R0, VB, S0, LB); DMA16N(R0, VB, (S0) + 32 * (LDW), (LB) + 8192); DMA16N(R1, VB, S1, (LB) + 16384); DMA16N(R1, VB, (S1) + 32 * (LDW), (LB) + 24576); } while (0)

static constexpr unsigned F_BC = 512, F_CK = 2048, F_EX = 12288, F_W = 16384, F_PG = 32768, F_GR = 49152, CTL_SZ = 65536;
DEV unsigned fw(int role, int h) { return F_W + ((h >> 3) * 32 + role * 8 + (h & 7)) * 128; }
static constexpr unsigned POI = 0xFFFFFFFFu, OOB = 0x40000000u, SPIN_CAP = 200000u;
DEV bool spin_fail(unsigned tries, unsigned* err) {
    __builtin_amdgcn_s_sleep(1);
    if ((tries & 255u) != 255u) return false;
    unsigned e; asm volatile("global_load_dword %0, %1, off sc1\n\ts_waitcnt vmcnt(0)" : "=&v"(e) : "v"(err) : "memory");
    if (__builtin_amdgcn_readfirstlane(e) == 1u) return true;
    if (tries > SPIN_CAP) { unsigned one = 1u; asm volatile("global_store_dword %0, %1, off sc1" :: "v"(err), "v"(one) : "memory"); return true; }
    return false;
}
DEV bool all_ge(unsigned v, unsigned t) { return __builtin_amdgcn_ballot_w64(v < t) == 0; }
template <int Z = 6> DEV void wait_flags(rsrc_t rc, unsigned off, unsigned t, unsigned* err) {
    for (unsigned tries = 0;; ++tries) {
        FENCE();
        if (all_ge(LD4(rc, off, 0), t)) break;
        __builtin_amdgcn_s_sleep(Z);
        if (spin_fail(tries, err)) break;
    }
    FENCE();
}

#define BC_LD(B, RS) asm volatile("buffer_load_dword %0, off, %1, 0 offset:512 sc1" : "=&v"(B) : "s"(RS) : "memory")
DEV unsigned beacon(v4u rs, unsigned b0, unsigned b1, unsigned need, bool seen = false) {
    asm volatile("" : "+v"(b0), "+v"(b1));
    for (unsigned tries = 0;; ++tries) {
        const unsigned u = __builtin_amdgcn_readfirstlane(b1);
        if (((seen || u != __builtin_amdgcn_readfirstlane(b0)) && (u & need) == need) || tries > SPIN_CAP) return u;
        __builtin_amdgcn_s_sleep(4);
        BC_LD(b1, rs); asm volatile("s_waitcnt vmcnt(0)" : "+v"(b1) :: "memory");
    }
}

template <bool VT> DEV unsigned stream_qkv(LAS char* lds, v4u rx, v4u rw, rsrc_t ro, rsrc_t rc, v4u rsc, bool hs, unsigned fo, int h, unsigned fx, unsigned pf, unsigned* dbg) {
    unsigned far = 0;
    const int tid = threadIdx.x, w = __builtin_amdgcn_readfirstlane(tid >> 6), l = tid & 63, kh = w >> 2, cg = w & 3;
    const unsigned lb = (unsigned)(size_t)lds, vb = wt_src(tid, HD * 2), l16 = l * 16;
    const unsigned x0 = (4 * cg + (VT ? 2 : 16) * (1 - kh)) * 2048 + kh * 1024, x1 = (4 * cg + (VT ? 2 : 16) * kh) * 2048 + kh * 1024;
#define STW(s) WT_ISSUE(rw, rw, vb, h * 256 + (s) * (64 * HD * 2), h * 256 + ((s) + 8) * (64 * HD * 2), HD * 2, lb + w * 1024 + ((s) & 3) * XS)
#define STX(u, q, e) DMA16(rx, l16, ((u) >> 1) * 65536 + ((q) & 1 ? x1 : x0) + (VT ? ((e) & 1) + 16 * ((e) >> 1) : (e)) * 2048, lb + (w * 4 + (e)) * XP + (q) * XS)
    const unsigned fl = tid == 0 ? fo : OOB;
    v4u W[16][2];
    unsigned b0, b1;
    BC_LD(b0, rsc);
    if (pf & 4) asm volatile("s_mov_b32 m0, %0\n\ts_nop 0\n\tbuffer_load_dword %1, %2, %3 offen lds"
                             :: "s"(lb + L_RED), "v"(hs && l < 16 ? l * 128 : OOB), "s"(rx), "s"((((h & 7) * 2 + VT) * 8 + w) * 2048) : "memory", "m0");
    STW(0); STW(1); STW(2); STW(3);
#pragma unroll
    for (int s = 0; s < 8; ++s) {
        WAITV(12); RBAR();
#pragma unroll
        for (int n = 0; n < 2; ++n) { const LAS char* p = lds + wt_rd(w, l, n) + (s & 3) * XS; W[2 * s][n] = wt_frag(p); W[2 * s + 1][n] = wt_frag(p + 8192); }
        BAR();
        if (s == 4) BC_LD(b1, rsc);
        if (s == 7) { far ^= FAR(beacon(rsc, b0, b1, 16, !hs)); ST4(rc, OOB, 0, 0u); FENCE(); }
        if (s < 4) STW(s + 4); else { STX(s - 4, s - 4, 0); STX(s - 4, s - 4, 1); if (s < 7) { STX(s - 4, s - 4, 2); STX(s - 4, s - 4, 3); } }
    }
    const LAS char* px = lds + (kh * 16 + (l & 15)) * XP + (l >> 4) * 16;
    LAS v4f* ps = (LAS v4f*)(lds + L_RED + w * 2048 + l16);
    const LAS v4f* pr = (const LAS v4f*)(lds + L_RED + (w ^ 4) * 2048 + l16);
    unsigned oo = OOB, on = (unsigned)h * 16384u + (far ^ fx) + cg * 2048u + kh * 1024u + l16;
    v4f c[2][2] = {}, r0 = {}, r1 = {};
    v4u f[8];
    auto fin = [&]() { r0 = add4(r0, c[1][0]); r1 = add4(r1, c[1][1]); ST16X(ro, oo, (v4u{pk2(r0[0], r0[1]), pk2(r0[2], r0[3]), pk2(r1[0], r1[1]), pk2(r1[2], r1[3])}), NTS & 1); oo = side(on); on += NH * 16384u; };
    WSTAMP(3);
    WAITV(10); RBAR();
    WSTAMP(4);
    const LAS char* px2 = px + 2 * XS; asm volatile("" : "+v"(px2));
#define RDX(q, k) f[(k) & 7] = *(const LAS v4u*)(((q) & 2 ? px2 : px) + ((q) & 1) * XS + (k) * 64)
#pragma unroll
    for (int k = 0; k < 8; ++k) RDX(0, k);
#pragma unroll 1
    for (int u0 = 0; u0 < 2 * NSEG; u0 += 4) {
        if (u0 == 4) WSTAMP(5);
        if (u0 == 64) WSTAMP(6);
#pragma unroll
        for (int q = 0; q < 4; ++q) {
#pragma unroll
            for (int k = 0; k < 16; ++k) {
                const v4u x = f[k & 7];
#pragma unroll
                for (int n = 0; n < 2; ++n) { const v4f z = k ? c[q & 1][n] : v4f{0, 0, 0, 0}; c[q & 1][n] = VT ? mf(x, W[k][n], z) : mf(W[k][n], x, z); }
                if (k < 8) RDX(q, k + 8);
                if (k == BK) { SCHED(); WAITV(10); BAR(); for (int e = 0; e < BK - 8; ++e) RDX(q + 1, e); }
                if (k >= BK) RDX(q + 1, k - 8);
                if (((k + 16 - BK) & 3) == 0) asm volatile("s_setprio %0" :: "n"(3 - (((k + 16 - BK) >> 2) & 3)));
                if (k == 2) { if (q & 1) { ps[0] = c[0][0]; ps[64] = c[0][1]; } else fin(); }
                if (k == 13) { if (q & 1) { r0 = pr[0]; r1 = pr[64]; } else ST4(rc, fl, 0, max((u0 + q - 2) >> 1, 0)); }
                if (!(EXP & 1) && !(k & 1) && kh == (((k >> 1) ^ ((BK + 1) >> 1)) & 1)) { if (k > BK) STX(u0 + q + 4, q, (k - BK) >> 2); else STX(u0 + q + 3, (q + 3) & 3, (k + 16 - BK) >> 2); }
                SCHED();
            }
        }
    }
    asm volatile("s_nop 7" : "+v"(c[1][0]), "+v"(c[1][1]));
    fin(); __builtin_amdgcn_s_setprio(0);
    return far;
#undef STW
#undef STX
}

#define LDBX(RS, K, BASE, NT) LD16X(RS, l * 16 + ((K) & 3) * 1024, (BASE) + ((K) >> 2) * 4096, NT)
#define LDB(RS, K, BASE) LDBX(RS, K, BASE, 0)
DEV unsigned xattn(rsrc_t rq, rsrc_t rk, rsrc_t rv, rsrc_t rm, rsrc_t rc, unsigned* err, int seg, int h, unsigned fl, unsigned far, unsigned ms, LAS unsigned* cnt, unsigned* dbg) {
#define XST(k) do {} while (0)
    XST(3);
    const int l = lane(), fr = l & 15, g = l >> 4;
    const unsigned sb = side((unsigned)(seg * NH + h) * 16384u + far), mb = (unsigned)(seg * NH + h) * 4096u;
    const unsigned f0 = fw(min(l & 3, 2), h) + 12, f1 = F_PG + h * 512 + (l & 3) * 128;
    const unsigned old = __builtin_amdgcn_ballot_w64(fl <= (unsigned)seg);
    if (old & 15) wait_flags(rc, f0, seg + 1, err);
    XST(4);
    v4u q[4][2], k[4][2], km[4], v[8]; v2u vm[8];
    auto ldm = [&]() {
#pragma unroll
        for (int j = 0; j < 4; ++j) km[j] = LD16(rm, (g * 8 + (l & 7)) * 16 + j * 512, mb);
#pragma unroll
        for (int d = 0; d < 8; ++d) vm[d] = LD8(rm, 2048 + fr * 16 + (g & 1) * 8 + d * 256, mb); };
#pragma unroll
    for (int j = 0; j < 8; ++j) q[j >> 1][j & 1] = LDBX(rq, j, sb, 1);
    if (!(old & 240)) {
#pragma unroll
        for (int j = 0; j < 8; ++j) k[j >> 1][j & 1] = LDBX(rk, j, sb ^ KVS(ms >> 2, h), 1);
#pragma unroll
        for (int d = 0; d < 8; ++d) v[d] = LDBX(rv, d, sb ^ KVS(ms >> 4, h), 1);
        ldm();
    } else {
#pragma unroll
        for (int j = 0; j < 8; ++j) k[j >> 1][j & 1] = LDB(rk, j, sb ^ KVS(ms >> 2, h));
#pragma unroll
        for (int d = 0; d < 8; ++d) v[d] = LDB(rv, d, sb ^ KVS(ms >> 4, h));
    }
    v4f sx[3] = {}, o[2][8];
    float mx[2], zx[2];
    constexpr float C = SCALE * LOG2E, MR = MASKV / SCALE;
#pragma unroll
    for (int j = 0; j < 4; ++j) { sx[0] = mf(k[j][0], q[j][0], sx[0]); sx[1] = mf(k[j][0], q[j][1], sx[1]); sx[2] = mf(k[j][1], q[j][1], sx[2]); }
#pragma unroll
    for (int u = 0; u < 2; ++u) {
        float e[8];
#pragma unroll
        for (int r = 0; r < 4; ++r) { const bool vis = 4 * g + r <= fr; e[r] = (u || vis) ? sx[u][r] : MR; e[4 + r] = (u && vis) ? sx[2][r] : MR; }
        float m = e[0];
#pragma unroll
        for (int r = 1; r < 8; ++r) m = fmaxf(m, e[r]);
        mx[u] = m = red_g<true>(m); zx[u] = 0;
#pragma unroll
        for (int r = 0; r < 8; ++r) { e[r] = __builtin_amdgcn_exp2f((e[r] - m) * C); zx[u] += e[r]; }
        const v4u P = {pk2(e[0], e[1]), pk2(e[4], e[5]), pk2(e[2], e[3]), pk2(e[6], e[7])};
#pragma unroll
        for (int d = 0; d < 8; d += 2) {
            o[u][d] = mf(__builtin_shufflevector(v[d], v[d + 1], 0, 1, 4, 5), P, v4f{0, 0, 0, 0});
            o[u][d + 1] = mf(__builtin_shufflevector(v[d], v[d + 1], 2, 3, 6, 7), P, v4f{0, 0, 0, 0});
        }
    }
    asm volatile("" : "+v"(zx[0]), "+v"(zx[1]));
    SCHED(); XST(5);
    TS(threadIdx.x == 0, seg, 0);
    if (old & 240) {
        if (!all_ge(LD4(rc, f1, 0), min(seg + 1, 62))) {
            wait_flags<2>(rc, f1, min(seg + 1, 62), err); TS(threadIdx.x == 0, seg, 1);
            if (seg == 62) __builtin_amdgcn_s_sleep(Z62); else if (seg == 63) __builtin_amdgcn_s_sleep(Z63);
        }
        for (unsigned tries = 0, f = seg < 62;; ++tries) {
            FENCE(); const unsigned p = LD4(rc, f ? OOB : f1, 0); ldm();
            unsigned m = 0;
#pragma unroll
            for (int j = 0; j < 8; ++j) m = max(max(m, max(km[j >> 1][j & 1], km[j >> 1][2 + (j & 1)])), max(vm[j][0], vm[j][1]));
            if ((all_ge(~m, 1) | f) || spin_fail(tries, err)) break;
            f = all_ge(p, NSEG);
        }
    }
    XST(6);
    v4f sm[2] = {};
#pragma unroll
    for (int j = 0; j < 4; ++j) { sm[0] = mf(km[j], q[j][0], sm[0]); sm[1] = mf(km[j], q[j][1], sm[1]); }
    XST(28);
#pragma unroll
    for (int u = 0; u < 2; ++u) {
        const float m = red_g<true>(fmaxf(fmaxf(fmaxf(sm[u][0], sm[u][1]), fmaxf(sm[u][2], sm[u][3])), mx[u])), al = __builtin_amdgcn_exp2f((mx[u] - m) * C);
        float e[4];
#pragma unroll
        for (int r = 0; r < 4; ++r) e[r] = __builtin_amdgcn_exp2f((sm[u][r] - m) * C);
        const float inv = __builtin_amdgcn_rcpf(red_g<false>(al * zx[u] + 0.5f * (e[0] + e[1] + e[2] + e[3]))), f = al * inv, im = g < 2 ? inv : 0.0f;
        const unsigned pm0 = pk2(e[0] * im, e[1] * im), pm1 = pk2(e[2] * im, e[3] * im);
        const v4u Pa = {pm0, pm1, 0u, 0u}, Pb = {0u, 0u, pm0, pm1};
#pragma unroll
        for (int d = 0; d < 4; ++d) {
            const v4u vv = cat(vm[2 * d], vm[2 * d + 1]);
            const v4f o0 = mf(vv, Pa, o[u][2 * d] * f), o1 = mf(vv, Pb, o[u][2 * d + 1] * f);
            ST16(rq, l * 16 + d * 1024, sb + u * 4096, (v4u{pk2(o0[0], o0[1]), pk2(o0[2], o0[3]), pk2(o1[0], o1[1]), pk2(o1[2], o1[3])}));
        }
    }
    XST(29); TS(threadIdx.x == 0, seg, 2);
    FENCE(); const unsigned fn = LD4(rc, fl > seg + 4u ? OOB : (l & 4 ? f1 : f0), 0);
    WAITV(1); XST(30);
    if (l == 0 && __hip_atomic_fetch_add(cnt + (seg >> 2), 1u, __ATOMIC_RELAXED, __HIP_MEMORY_SCOPE_WORKGROUP) == 7) { ST4(rc, fw(3, (h & 24) + (seg & 3)) + 64 + (seg & 60), 0, 1u); TS(1, seg, 3); }
    if (seg >= XB && seg < XE) RBAR();
    XST(31);
    FENCE();
    return max(fl, fn);
}

template <int N> DEV unsigned ror(unsigned v) { return __builtin_amdgcn_update_dpp(0u, v, 0x120 + N, 0xf, 0xf, false); }
DEV void prep_rows(const bf16_t* x, const bf16_t* gw, const bf16_t* wr, rsrc_t rg, int row, int l) {
    v4u xv[16], gv[2], rv[2];
#pragma unroll
    for (int i = 0; i < 2; ++i) { gv[i] = *(const v4u*)(gw + i * 512 + l * 8); rv[i] = *(const v4u*)(wr + i * 512 + l * 8); }
#pragma unroll
    for (int i = 0; i < 16; ++i) xv[i] = *(const v4u*)(x + (size_t)row * DM + i * 512 + l * 8);
    float ga = 0, gb = 0;
#pragma unroll
    for (int k = 0; k < 8; ++k) {
        float a = 0, b = 0;
#pragma unroll
        for (int i = 0; i < 8; ++i) {
            const unsigned u = xv[2 * k + (i >> 2)][i & 3], g = gv[i >> 2][i & 3], r = rv[i >> 2][i & 3];
            a += rbf(lo16(u) * lo16(g)) + rbf(hi16(u) * hi16(g));
            b += lo16(u) * lo16(r) + hi16(u) * hi16(r);
        }
        a = half_add(a, b);
        a += __uint_as_float(ror<8>(__float_as_uint(a))); a += __uint_as_float(ror<4>(__float_as_uint(a)));
        a += __uint_as_float(ror<2>(__float_as_uint(a))); a += __uint_as_float(ror<1>(__float_as_uint(a)));
        const v2u s = sw16(__float_as_uint(a), __float_as_uint(a)); a = __uint_as_float(s[0]) + __uint_as_float(s[1]);
        if ((l & 31) == k) ga = a;
    }
    gb = rbf(ga);
    const unsigned u = __float_as_uint(l < 32 ? rbf(sigmoidf_(gb)) : gb) | 1u;
    const v2u e = sw32(u, u);
    ST16(rg, l < 8 && !(l & 1) ? F_GR + (row + l) * 8 : OOB, 0, (v4u{e[0], e[1], ror<15>(e[0]), ror<15>(e[1])}));
}
template <int N> DEV unsigned top_keys(unsigned* key, int l) {
    unsigned res = 0;
#pragma unroll 1
    for (int c = 0; c < TOPC; ++c) {
        unsigned m = key[0];
#pragma unroll
        for (int i = 1; i < N; ++i) m = max(m, key[i]);
        m = max(m, ror<8>(m)); m = max(m, ror<4>(m)); m = max(m, ror<2>(m)); m = max(m, ror<1>(m));
        v2u s = sw16(m, m); m = max(s[0], s[1]); s = sw32(m, m); m = max(s[0], s[1]);
#pragma unroll
        for (int i = 0; i < N; ++i) key[i] = key[i] == m ? 0u : key[i];
        if (l == c) res = m;
    }
    return res;
}
DEV int top8(LAS unsigned* cand, rsrc_t rc, unsigned* err, int w, int l) {
    v4u t[2];
    for (unsigned tries = 0;; ++tries) {
        FENCE();
        t[0] = LD16(rc, F_GR + w * 2048 + l * 16, 0); t[1] = LD16(rc, F_GR + w * 2048 + 1024 + l * 16, 0);
        const v4u v = t[0] & t[1];
        if (all_ge(v[0] & v[1] & v[2] & v[3] & 1u, 1u)) break;
        __builtin_amdgcn_s_sleep(8);
        if (spin_fail(tries, err)) break;
    }
    unsigned key[4];
#pragma unroll
    for (int i = 0; i < 4; ++i) { const unsigned r = t[i >> 1][(i & 1) * 2 + 1] - 1u; key[i] = ((int)r < 0 ? 0xffff0000u - r : r + 0x80000000u) + (0xffffu - (w * 256 + (i >> 1) * 128 + l * 2 + (i & 1))); }
    key[0] = top_keys<4>(key, l);
    if (l < TOPC) cand[w * TOPC + l] = key[0];
    BAR();
    key[0] = cand[l];
    return 0xffff - (int)(top_keys<1>(key, l) & 0xffffu);
}
static constexpr int NMOE = FF / 128, XR = 2080;
DEV void moe(LAS char* lds, v4u rx, int sel, v4u rg, v4u ru, v4u rd, rsrc_t rp, int cu) {
    const int tid = threadIdx.x, w = __builtin_amdgcn_readfirstlane(tid >> 6), l = tid & 63, kh = w >> 2, cg = w & 3, fr = l & 15, fq = l >> 4;
    const unsigned lb = (unsigned)(size_t)lds, v1 = wt_src(tid, FF * 2), v2 = wt_src(tid, DM * 2);
    const unsigned xo = __builtin_amdgcn_readlane(sel, w) * 2048;
    DMA16(rx, l * 16, xo, lb + L_RED + w * XR); DMA16(rx, l * 16, xo + 1024, lb + L_RED + w * XR + 1024);
    auto issue = [&](int s) {
        const unsigned d = lb + w * 1024 + (s & 3) * XS;
        if (s < 16) { const unsigned o = s * (64 * FF * 2) + cu * 256; WT_ISSUE(rg, ru, v1, o, o, FF * 2, d); }
        else { const unsigned o = s < 24 ? (cu * 128 + (s & 1) * 64) * (DM * 2) + (((s >> 1) + cu) & 3) * 512 : OOB; WT_ISSUE(rd, rd, v2, o, o + 256, DM * 2, d); } };
    issue(0); issue(1); issue(2); issue(3);
    const int p0 = wt_rd(w, l, 0), p1 = wt_rd(w, l, 1);
    v4f c[2] = {};
    auto stage = [&](int s, const LAS char* pa) {
        WAITV(12); RBAR();
        const LAS char* p = lds + (s & 3) * XS;
        const v4u a0 = *(const LAS v4u*)pa, a1 = *(const LAS v4u*)(pa + 64), b0 = wt_frag(p + p0), b1 = wt_frag(p + p1), b2 = wt_frag(p + p0 + 8192), b3 = wt_frag(p + p1 + 8192);
        BAR();
        issue(s + 4);
        c[0] = mf(b2, a1, mf(b0, a0, c[0])); c[1] = mf(b3, a1, mf(b1, a0, c[1])); };
#pragma unroll 1
    for (int s = 0; s < 16; ++s) stage(s, lds + L_RED + (l & 7) * XR + fq * 16 + s * 128);
    LAS v4f* pu = (LAS v4f*)(lds + L_RED + cg * 2048 + l * 16);
    if (kh) { pu[0] = c[0]; pu[64] = c[1]; }
    BAR();
    if (!kh && fr < 8) {
#pragma unroll
        for (int n = 0; n < 2; ++n) {
            const v4f u = pu[64 * n]; float hv[4];
#pragma unroll
            for (int e = 0; e < 4; ++e) { const float g = rbf(c[n][e]); hv[e] = rbf(g * sigmoidf_(g)) * rbf(u[e]); }
            *(LAS v2u*)(lds + L_RED + 8192 + fr * 288 + cg * 64 + n * 32 + fq * 8) = v2u{pk2(hv[0], hv[1]), pk2(hv[2], hv[3])};
        }
    }
    WAITL(0);
#pragma unroll 1
    for (int s = 16; s < 24; s += 2) {
        c[0] = v4f{0, 0, 0, 0}; c[1] = c[0];
        const LAS char* pa = lds + L_RED + 8192 + (l & 7) * 288 + fq * 16;
        stage(s, pa); stage(s + 1, pa + 128);
        ST16(rp, cu * 32768 + (l & 7) * 4096 + ((((s >> 1) + cu) & 3) * 256 + kh * 128 + cg * 32 + (fr >> 3) * 16 + fq * 4) * 4, 0, __builtin_bit_cast(v4u, fr < 8 ? c[0] : c[1]));
    }
    WAITV(0); RBAR();
}

static constexpr unsigned H2_OFF = 0, H1_OFF = 512 * 1024, H3_OFF = 768 * 1024, PR_OFF = 896 * 1024;
static constexpr size_t SLOT = 2u << 20;
static constexpr int NSLOT = 4;
struct Args {
    const bf16_t* x; const bf16_t* mem0; const bf16_t* Wq; const bf16_t* Wk; const bf16_t* Wv; const bf16_t* Wo;
    const bf16_t* w_r; const bf16_t* wg; const bf16_t* wu; const bf16_t* wd; const bf16_t* gw;
    char* qkv; char* mb; int* idx; float* part; float* upd; char* wt; bf16_t* out;
    char* ring; unsigned* ctl; unsigned* dbg;
    u64 segs;
    float per; unsigned per0, d1, dv, d2, dp, slack, thr, spare, wq, wx, pf, ms, ws, wi, gap;
};
typedef const __attribute__((address_space(4))) Args* KArgs;
DEV KArgs args() { KArgs p = (KArgs)__builtin_amdgcn_kernarg_segment_ptr(); asm volatile("" : "+s"(p)); return p; }
static constexpr unsigned QSZ = S_TOK * HD * 4;
typedef unsigned v16u __attribute__((ext_vector_type(16)));
typedef unsigned v32u __attribute__((ext_vector_type(32)));

DEV unsigned nearo(unsigned off) { return (off >> 13) * 16384u + (off & 8191u); }
DEV unsigned rt32() { u64 t; asm volatile("s_memrealtime %0\n\ts_waitcnt lgkmcnt(0)" : "=s"(t) :: "memory"); return (unsigned)t; }
DEV unsigned cyc32() { u64 t; asm volatile("s_memtime %0\n\ts_waitcnt lgkmcnt(0)" : "=s"(t) :: "memory"); return (unsigned)t; }
template <int R1, int R2, int R3> DEV v8u rd_w(const LAS char* p) {
    return __builtin_shufflevector(cat(trd(p), trd(p + R1)), cat(trd(p + R2), trd(p + R3)), 0, 1, 2, 3, 4, 5, 6, 7);
}
DEV v16u cat8(v8u a, v8u b) { return __builtin_shufflevector(a, b, 0, 1, 2, 3, 4, 5, 6, 7, 8, 9, 10, 11, 12, 13, 14, 15); }
DEV v32u cat16(v16u a, v16u b) { return __builtin_shufflevector(a, b, 0, 1, 2, 3, 4, 5, 6, 7, 8, 9, 10, 11, 12, 13, 14, 15, 16, 17, 18, 19, 20, 21, 22, 23, 24, 25, 26, 27, 28, 29, 30, 31); }
DEV v8u cat4(v4u a, v4u b) { return __builtin_shufflevector(a, b, 0, 1, 2, 3, 4, 5, 6, 7); }

static constexpr int L_PQK = 0, L_PV = 8704, L_P = 13824, L_VT = 15360, L_X = 16384, L_K = L_X + 9216, L_MISC = 28672, L_PF = 29696, L_STG = 32768, L_TOT = 163840;
static constexpr int X_FQ = 272, X_BLK = 1152;
DEV void code_pf(unsigned lo, unsigned tid) {
    if (!(CPF & 1)) return;
    unsigned p0, p1, n;
    asm volatile("s_getpc_b64 vcc\n.Lpc%=:\n\ts_sub_u32 %0, vcc_lo, .Lpc%=-.Lfunc_beg\n\ts_subb_u32 %1, vcc_hi, 0\n\ts_mov_b32 %2, .Lfunc_fin-.Lfunc_beg" : "=s"(p0), "=s"(p1), "=s"(n) :: "vcc", "scc");
    asm volatile("s_mov_b32 m0, %0\n\ts_nop 0\n\tbuffer_load_dword %1, %2, 0 offen lds\n\tbuffer_load_dword %1, %2, %3 offen lds" :: "s"(lo), "v"(tid * 128), "s"(v4u{p0, p1 & 0xffffu, n, 0x00020000u}), "s"(65536) : "memory", "m0");
}
static constexpr unsigned WO_DONE = 33;
#define ASM_A \
    "s_setprio 1\n" \
    "s_mov_b32 s76, 0\n" \
    "s_mov_b32 s99, 0x20000\n" \
    "s_mov_b32 s84, 0\n" \
    "s_mov_b32 s81, 0\n" \
    "s_mov_b32 s86, 0\n" \
    "s_mov_b32 s87, 0\n" \
    "s_mov_b32 s88, 0\n" \
    "s_mov_b32 s89, 0\n" \
    "s_mov_b32 s90, 0\n" \
    "s_mov_b32 s91, 0\n" \
    "s_mov_b32 s92, 0\n" \
    "s_mov_b32 s93, 0\n" \
    "s_mov_b32 s94, 0\n" \
    "s_mov_b32 s95, 0\n" \
    "s_mov_b32 s96, 0\n" \
    "s_mov_b32 s97, 0\n" \
    "s_mov_b32 s98, 0\n" \
    "ds_read_b32 v244, v237\n" \
    "s_waitcnt lgkmcnt(0)\n" \
    "v_readfirstlane_b32 s94, v244\n" \
    "s_mul_i32 s79, s94, s70\n" \
    "s_add_u32 s79, s79, s81\n" \
    "s_mov_b32 s77, s79\n" \
    "s_mul_i32 s96, s65, s71\n" \
    "s_lshr_b32 s96, s96, 4\n" \
    "v_mov_b64 v[208:209], 0\n" \
    "v_mov_b64 v[210:211], 0\n" \
    "v_mov_b64 v[212:213], 0\n" \
    "v_mov_b64 v[214:215], 0\n" \
    "v_mov_b64 v[216:217], 0\n" \
    "v_mov_b64 v[218:219], 0\n" \
    "v_mov_b64 v[220:221], 0\n" \
    "v_mov_b64 v[222:223], 0\n" \
    "v_mov_b64 v[224:225], 0\n" \
    "v_mov_b64 v[226:227], 0\n" \
    "v_mov_b64 v[228:229], 0\n" \
    "v_mov_b64 v[230:231], 0\n" \
    "v_mov_b32 v241, 1\n" \
    "v_mov_b32 v246, 0\n" \
    "s_mov_b32 s83, 0\n" \
    "s_branch L%=_Acomp\n" \
    "L%=_Aloop:\n" \
    "s_lshl_b32 s80, s84, 1\n" \
    "s_sub_u32 s80, s60, s80\n" \
    "v_mov_b32 v247, -1\n" \
    "s_sleep 64\n" \
    "L%=_Aloop2:\n" \
    "ds_read_b64 v[246:247], v237 offset:72\n" \
    "s_sleep 64\n" \
    "L%=_Aloop3:\n" \
    "v_readfirstlane_b32 s79, v247\n" \
    "s_add_u32 s95, s79, s80\n" \
    "s_sub_u32 s85, s95, s60\n" \
    "s_cmp_gt_u32 s85, 0x20000\n" \
    "s_cbranch_scc1 L%=_Aloop4\n" \
    "L%=_Ago:\n" \
    "buffer_load_dwordx4 v[192:195], v233, s[40:43], s95 offen sc1\n" \
    "s_add_u32 s85, s95, 0x1000\n" \
    "s_nop 4\n" \
    "buffer_load_dwordx4 v[196:199], v233, s[40:43], s85 offen sc1\n" \
    "s_nop 5\n" \
    "buffer_load_dwordx4 v[200:203], v239, s[40:43], s95 offen sc1\n" \
    "s_nop 5\n" \
    "buffer_load_dwordx4 v[204:207], v239, s[40:43], s85 offen sc1\n" \
    "L%=_Acomp:\n" \
    "s_mov_b32 m0, s72\n" \
    "s_waitcnt vmcnt(3)\n" \
    "v_smfmac_f32_16x16x64_bf16 v[208:211], v[192:195], v[0:7], v232\n" \
    "v_smfmac_f32_16x16x64_bf16 v[212:215], v[192:195], v[8:15], v232\n" \
    "v_smfmac_f32_16x16x64_bf16 v[216:219], v[192:195], v[64:71], v232\n" \
    "v_smfmac_f32_16x16x64_bf16 v[220:223], v[192:195], v[72:79], v232\n" \
    "v_smfmac_f32_16x16x64_bf16 v[224:227], v[192:195], v[128:135], v232\n" \
    "s_waitcnt vmcnt(2)\n" \
    "v_smfmac_f32_16x16x64_bf16 v[208:211], v[196:199], v[16:23], v232\n" \
    "v_smfmac_f32_16x16x64_bf16 v[212:215], v[196:199], v[24:31], v232\n" \
    "v_smfmac_f32_16x16x64_bf16 v[216:219], v[196:199], v[80:87], v232\n" \
    "v_smfmac_f32_16x16x64_bf16 v[220:223], v[196:199], v[88:95], v232\n" \
    "v_smfmac_f32_16x16x64_bf16 v[228:231], v[192:195], v[136:143], v232\n" \
    "s_waitcnt vmcnt(1)\n" \
    "v_smfmac_f32_16x16x64_bf16 v[208:211], v[200:203], v[32:39], v232\n" \
    "v_smfmac_f32_16x16x64_bf16 v[212:215], v[200:203], v[40:47], v232\n" \
    "v_smfmac_f32_16x16x64_bf16 v[216:219], v[200:203], v[96:103], v232\n" \
    "v_smfmac_f32_16x16x64_bf16 v[220:223], v[200:203], v[104:111], v232\n" \
    "v_smfmac_f32_16x16x64_bf16 v[224:227], v[196:199], v[144:151], v232\n" \
    "s_waitcnt vmcnt(0)\n" \
    "v_smfmac_f32_16x16x64_bf16 v[208:211], v[204:207], v[48:55], v232\n" \
    "v_smfmac_f32_16x16x64_bf16 v[212:215], v[204:207], v[56:63], v232\n" \
    "v_smfmac_f32_16x16x64_bf16 v[216:219], v[204:207], v[112:119], v232\n" \
    "v_add_f32 v208, v208, v210\n" \
    "v_smfmac_f32_16x16x64_bf16 v[220:223], v[204:207], v[120:127], v232\n" \
    "v_add_f32 v209, v209, v211\n" \
    "v_cmp_u_f32 vcc, v208, v209\n" \
    "v_add_f32 v212, v212, v214\n" \
    "v_add_f32 v213, v213, v215\n" \
    "v_add_f32 v216, v216, v218\n" \
    "v_add_f32 v217, v217, v219\n" \
    "s_nop 1\n" \
    "v_add_f32 v220, v220, v222\n" \
    "v_add_f32 v221, v221, v223\n" \
    "v_smfmac_f32_16x16x64_bf16 v[224:227], v[200:203], v[160:167], v232\n" \
    "s_cbranch_vccnz L%=_Aslow\n" \
    "L%=_Aok:\n" \
    "ds_write_addtid_b32 v208 offset:0\n" \
    "ds_write_addtid_b32 v209 offset:544\n" \
    "v_smfmac_f32_16x16x64_bf16 v[228:231], v[196:199], v[152:159], v232\n" \
    "ds_write_addtid_b32 v216 offset:256\n" \
    "ds_write_addtid_b32 v217 offset:800\n" \
    "v_smfmac_f32_16x16x64_bf16 v[224:227], v[204:207], v[176:183], v232\n" \
    "ds_write_addtid_b32 v212 offset:1088\n" \
    "ds_write_addtid_b32 v213 offset:1632\n" \
    "v_smfmac_f32_16x16x64_bf16 v[228:231], v[200:203], v[168:175], v232\n" \
    "ds_write_addtid_b32 v220 offset:1344\n" \
    "ds_write_addtid_b32 v221 offset:1888\n" \
    "ds_write_b32 v236, v241 offset:32\n" \
    "v_smfmac_f32_16x16x64_bf16 v[228:231], v[204:207], v[184:191], v232\n" \
    "s_barrier\n" \
    "s_mov_b32 m0, s73\n" \
    "v_add_f32 v224, v224, v226\n" \
    "v_add_f32 v225, v225, v227\n" \
    "ds_write_addtid_b32 v224 offset:8704\n" \
    "ds_write_addtid_b32 v225 offset:8992\n" \
    "s_nop 1\n" \
    "v_add_f32 v228, v228, v230\n" \
    "v_add_f32 v229, v229, v231\n" \
    "ds_write_addtid_b32 v228 offset:9344\n" \
    "ds_write_addtid_b32 v229 offset:9632\n" \
    "ds_write_b32 v236, v241 offset:16\n" \
    "s_setprio 0\n" \
    "v_mov_b64 v[208:209], 0\n" \
    "v_mov_b64 v[210:211], 0\n" \
    "v_mov_b64 v[212:213], 0\n" \
    "v_mov_b64 v[214:215], 0\n" \
    "v_mov_b64 v[216:217], 0\n" \
    "v_mov_b64 v[218:219], 0\n" \
    "v_mov_b64 v[220:221], 0\n" \
    "v_mov_b64 v[222:223], 0\n" \
    "v_mov_b64 v[224:225], 0\n" \
    "v_mov_b64 v[226:227], 0\n" \
    "v_mov_b64 v[228:229], 0\n" \
    "v_mov_b64 v[230:231], 0\n" \
    "v_add_u32 v241, 1, v241\n" \
    "v_readfirstlane_b32 s97, v246\n" \
    "s_lshl1_add_u32 s79, s76, 4\n" \
    "s_max_u32 s75, s75, s79\n" \
    "s_lshl_b32 s97, s97, 1\n" \
    "s_lshl1_add_u32 s79, s76, 7\n" \
    "s_min_u32 s79, s79, s97\n" \
    "s_cmp_eq_u32 s83, -1\n" \
    "s_cselect_b32 s79, 0, s79\n" \
    "s_cmp_ge_u32 s75, s79\n" \
    "s_cbranch_scc1 L%=_Ab1\n" \
    "s_lshr_b32 s80, s75, 1\n" \
    "s_lshl_b32 s98, s80, 19\n" \
    "s_add_u32 s98, s98, s74\n" \
    "s_lshl_b32 s79, s80, 11\n" \
    "s_and_b32 s79, s79, 0x2000\n" \
    "s_xor_b32 s98, s98, s79\n" \
    "s_lshl_b32 s79, s75, 9\n" \
    "s_and_b32 s79, s79, 0x200\n" \
    "s_add_u32 s98, s98, s79\n" \
    "s_add_u32 s75, s75, 1\n" \
    "s_mov_b32 s83, -1\n" \
    "s_buffer_load_dword s82, s[52:55], s98 offset:0\n" \
    "s_buffer_load_dword s82, s[52:55], s98 offset:64\n" \
    "s_buffer_load_dword s82, s[52:55], s98 offset:128\n" \
    "s_buffer_load_dword s82, s[52:55], s98 offset:192\n" \
    "s_buffer_load_dword s82, s[52:55], s98 offset:256\n" \
    "s_buffer_load_dword s82, s[52:55], s98 offset:320\n" \
    "s_buffer_load_dword s82, s[52:55], s98 offset:384\n" \
    "s_buffer_load_dword s83, s[52:55], s98 offset:448\n" \
    "L%=_Ab1:\n" \
    "s_sleep 12\n" \
    "s_lshl1_add_u32 s79, s76, 8\n" \
    "s_min_u32 s79, s79, s97\n" \
    "s_cmp_eq_u32 s83, -1\n" \
    "s_cselect_b32 s79, 0, s79\n" \
    "s_cmp_ge_u32 s75, s79\n" \
    "s_cbranch_scc1 L%=_Ab2\n" \
    "s_lshr_b32 s80, s75, 1\n" \
    "s_lshl_b32 s98, s80, 19\n" \
    "s_add_u32 s98, s98, s74\n" \
    "s_lshl_b32 s79, s80, 11\n" \
    "s_and_b32 s79, s79, 0x2000\n" \
    "s_xor_b32 s98, s98, s79\n" \
    "s_lshl_b32 s79, s75, 9\n" \
    "s_and_b32 s79, s79, 0x200\n" \
    "s_add_u32 s98, s98, s79\n" \
    "s_add_u32 s75, s75, 1\n" \
    "s_mov_b32 s83, -1\n" \
    "s_buffer_load_dword s82, s[52:55], s98 offset:0\n" \
    "s_buffer_load_dword s82, s[52:55], s98 offset:64\n" \
    "s_buffer_load_dword s82, s[52:55], s98 offset:128\n" \
    "s_buffer_load_dword s82, s[52:55], s98 offset:192\n" \
    "s_buffer_load_dword s82, s[52:55], s98 offset:256\n" \
    "s_buffer_load_dword s82, s[52:55], s98 offset:320\n" \
    "s_buffer_load_dword s82, s[52:55], s98 offset:384\n" \
    "s_buffer_load_dword s83, s[52:55], s98 offset:448\n" \
    "L%=_Ab2:\n" \
    "s_barrier\n" \
    "s_sleep 4\n" \
    "s_lshl1_add_u32 s79, s76, 8\n" \
    "s_min_u32 s79, s79, s97\n" \
    "s_cmp_eq_u32 s83, -1\n" \
    "s_cselect_b32 s79, 0, s79\n" \
    "s_cmp_ge_u32 s75, s79\n" \
    "s_cbranch_scc1 L%=_Ab3\n" \
    "s_lshr_b32 s80, s75, 1\n" \
    "s_lshl_b32 s98, s80, 19\n" \
    "s_add_u32 s98, s98, s74\n" \
    "s_lshl_b32 s79, s80, 11\n" \
    "s_and_b32 s79, s79, 0x2000\n" \
    "s_xor_b32 s98, s98, s79\n" \
    "s_lshl_b32 s79, s75, 9\n" \
    "s_and_b32 s79, s79, 0x200\n" \
    "s_add_u32 s98, s98, s79\n" \
    "s_add_u32 s75, s75, 1\n" \
    "s_mov_b32 s83, -1\n" \
    "s_buffer_load_dword s82, s[52:55], s98 offset:0\n" \
    "s_buffer_load_dword s82, s[52:55], s98 offset:64\n" \
    "s_buffer_load_dword s82, s[52:55], s98 offset:128\n" \
    "s_buffer_load_dword s82, s[52:55], s98 offset:192\n" \
    "s_buffer_load_dword s82, s[52:55], s98 offset:256\n" \
    "s_buffer_load_dword s82, s[52:55], s98 offset:320\n" \
    "s_buffer_load_dword s82, s[52:55], s98 offset:384\n" \
    "s_buffer_load_dword s83, s[52:55], s98 offset:448\n" \
    "L%=_Ab3:\n" \
    "s_mov_b32 s79, s60\n" \
    "s_mov_b32 s60, s61\n" \
    "s_mov_b32 s61, s62\n" \
    "s_mov_b32 s62, s63\n" \
    "s_mov_b32 s63, s79\n" \
    "s_add_u32 s76, s76, 1\n" \
    "s_add_u32 s84, s84, 0x20000\n" \
    "s_setprio 1\n" \
    "s_cmp_lt_u32 s76, 64\n" \
    "s_cbranch_scc1 L%=_Aloop\n" \
    "s_waitcnt lgkmcnt(0)\n" \
    "s_branch L%=_end\n" \
    "L%=_Aslow:\n" \
    "s_nop 7\n" \
    "s_nop 0\n" \
    "v_max_u32 v244, v192, v193\n" \
    "v_max3_u32 v244, v244, v194, v195\n" \
    "v_max3_u32 v244, v244, v196, v197\n" \
    "v_max3_u32 v244, v244, v198, v199\n" \
    "v_max3_u32 v244, v244, v200, v201\n" \
    "v_max3_u32 v244, v244, v202, v203\n" \
    "v_max3_u32 v244, v244, v204, v205\n" \
    "v_max3_u32 v244, v244, v206, v207\n" \
    "v_cmp_eq_u32 vcc, -1, v244\n" \
    "s_cbranch_vccz L%=_Aok\n" \
    "s_cmp_eq_u32 s99, 0\n" \
    "s_cbranch_scc1 L%=_Aok\n" \
    "s_sub_u32 s99, s99, 1\n" \
    "s_cmp_lg_u32 s99, 0\n" \
    "s_cbranch_scc1 L%=_Au2\n" \
    "v_mov_b32 v244, 1\n" \
    "buffer_store_dword v244, off, s[44:47], 0 sc1\n" \
    "L%=_Au2:\n" \
    "v_mov_b64 v[208:209], 0\n" \
    "v_mov_b64 v[210:211], 0\n" \
    "v_mov_b64 v[212:213], 0\n" \
    "v_mov_b64 v[214:215], 0\n" \
    "v_mov_b64 v[216:217], 0\n" \
    "v_mov_b64 v[218:219], 0\n" \
    "v_mov_b64 v[220:221], 0\n" \
    "v_mov_b64 v[222:223], 0\n" \
    "v_mov_b64 v[224:225], 0\n" \
    "v_mov_b64 v[226:227], 0\n" \
    "v_mov_b64 v[228:229], 0\n" \
    "v_mov_b64 v[230:231], 0\n" \
    "ds_read_b64 v[244:245], v237 offset:80\n" \
    "s_waitcnt lgkmcnt(0)\n" \
    "v_readfirstlane_b32 s81, v244\n" \
    "v_readfirstlane_b32 s71, v245\n" \
    "ds_read_b32 v244, v237\n" \
    "s_waitcnt lgkmcnt(0)\n" \
    "v_readfirstlane_b32 s94, v244\n" \
    "s_mul_i32 s79, s94, s70\n" \
    "s_add_u32 s79, s79, s81\n" \
    "s_mov_b32 s77, s79\n" \
    "s_memrealtime vcc\n" \
    "s_waitcnt lgkmcnt(0)\n" \
    "s_sub_u32 s80, vcc_lo, s64\n" \
    "s_add_u32 s80, s80, -4\n" \
    "s_branch L%=_Au4h\n" \
    "L%=_Au4g:\n" \
    "s_add_u32 s77, s77, s70\n" \
    "s_add_u32 s94, s94, 1\n" \
    "L%=_Au4h:\n" \
    "s_sub_u32 s79, s80, s77\n" \
    "s_cmp_lt_u32 s79, 0x1000000\n" \
    "s_cbranch_scc1 L%=_Au4g\n" \
    "v_mov_b32 v244, s94\n" \
    "ds_max_u32 v237, v244\n" \
    "s_branch L%=_Au5b\n" \
    "L%=_Au5a:\n" \
    "s_sleep 4\n" \
    "L%=_Au5b:\n" \
    "s_memrealtime vcc\n" \
    "s_add_u32 s80, s77, s64\n" \
    "s_add_u32 s80, s80, -1\n" \
    "s_waitcnt lgkmcnt(0)\n" \
    "s_sub_i32 s79, s80, vcc_lo\n" \
    "s_sub_u32 s79, s79, 40\n" \
    "s_cmp_lt_u32 s79, 0x400\n" \
    "s_cbranch_scc1 L%=_Au5a\n" \
    "s_memtime s[38:39]\n" \
    "s_memrealtime vcc\n" \
    "s_nop 4\n" \
    "s_memrealtime s[32:33]\n" \
    "s_nop 4\n" \
    "s_memrealtime s[34:35]\n" \
    "s_nop 4\n" \
    "s_memrealtime s[36:37]\n" \
    "s_add_u32 s80, s77, s64\n" \
    "s_lshl_b32 s80, s80, 2\n" \
    "s_waitcnt lgkmcnt(0)\n" \
    "s_sub_u32 s80, s80, vcc_lo\n" \
    "s_sub_u32 s80, s80, s32\n" \
    "s_sub_u32 s80, s80, s34\n" \
    "s_sub_u32 s80, s80, s36\n" \
    "s_mul_i32 s80, s80, s71\n" \
    "s_ashr_i32 s80, s80, 6\n" \
    "s_add_u32 s78, s80, s38\n" \
    "s_and_b32 s95, s94, 1\n" \
    "s_lshl_b32 s95, s95, 17\n" \
    "s_add_u32 s95, s95, s60\n" \
    "s_add_u32 s85, s95, 0x1000\n" \
    "L%=_Au6a:\n" \
    "s_memtime s[38:39]\n" \
    "s_mul_i32 s80, -1, 23\n" \
    "s_add_u32 s80, s80, s78\n" \
    "s_waitcnt lgkmcnt(0)\n" \
    "s_sub_i32 s79, s80, s38\n" \
    "s_max_i32 s79, s79, 0\n" \
    "s_cmp_gt_u32 s79, 320\n" \
    "s_cbranch_scc1 L%=_Au6s\n" \
    ".p2align 4\n" \
    "L%=_Au6d:\n" \
    "s_sub_u32 s79, s79, 28\n" \
    "s_cbranch_scc0 L%=_Au6d\n" \
    "L%=_Au6e:\n" \
    "s_branch L%=_Ago\n" \
    "L%=_Aloop4:\n" \
    "s_cmp_eq_u32 s79, -1\n" \
    "s_cbranch_scc1 L%=_Aloop3\n" \
    "v_mov_b32 v247, -1\n" \
    "s_branch L%=_Aloop2\n" \
    "L%=_Au6s:\n" \
    "s_cmp_gt_u32 s79, 0x8000\n" \
    "s_cbranch_scc1 L%=_Au6e\n" \
    "s_sleep 2\n" \
    "s_branch L%=_Au6a\n" \
    "L%=_end:\n"
#define ASM_B \
    "s_and_b32 s79, s78, 3\n" \
    "s_cmp_eq_u32 s79, 1\n" \
    "s_cbranch_scc1 L%=_S\n" \
    "s_cmp_eq_u32 s79, 2\n" \
    "s_cbranch_scc1 L%=_R\n" \
    "s_cmp_eq_u32 s79, 3\n" \
    "s_cbranch_scc1 L%=_P\n" \
    "L%=_T:\n" \
    "s_mov_b32 s76, 0\n" \
    "s_mov_b32 s99, 0x20000\n" \
    "s_mov_b32 s84, 0\n" \
    "s_mov_b32 s81, 0\n" \
    "s_mov_b32 s86, 0\n" \
    "s_mov_b32 s87, 0\n" \
    "s_mov_b32 s88, 0\n" \
    "s_mov_b32 s89, 0\n" \
    "s_mov_b32 s90, 0\n" \
    "s_mov_b32 s91, 0\n" \
    "s_mov_b32 s92, 0\n" \
    "s_mov_b32 s93, 0\n" \
    "s_mov_b32 s94, 0\n" \
    "s_mov_b32 s95, 0\n" \
    "s_mov_b32 s96, 0\n" \
    "s_mov_b32 s97, 0\n" \
    "s_mov_b32 s98, 0\n" \
    "ds_read_b32 v216, v229\n" \
    "s_waitcnt lgkmcnt(0)\n" \
    "v_readfirstlane_b32 s94, v216\n" \
    "s_mul_i32 s79, s94, s70\n" \
    "s_add_u32 s79, s79, s81\n" \
    "s_mov_b32 s77, s79\n" \
    "s_mul_i32 s96, s65, s71\n" \
    "s_lshr_b32 s96, s96, 4\n" \
    "v_mov_b32 v96, 0x3f803f80\n" \
    "v_mov_b32 v97, 0x3f803f80\n" \
    "v_mov_b32 v98, 0x3f803f80\n" \
    "v_mov_b32 v99, 0x3f803f80\n" \
    "v_mov_b32 v100, 0x3f803f80\n" \
    "v_mov_b32 v101, 0x3f803f80\n" \
    "v_mov_b64 v[102:103], 0\n" \
    "v_mov_b64 v[64:65], 0\n" \
    "v_mov_b64 v[66:67], 0\n" \
    "v_mov_b64 v[68:69], 0\n" \
    "v_mov_b64 v[70:71], 0\n" \
    "v_mov_b64 v[72:73], 0\n" \
    "v_mov_b64 v[74:75], 0\n" \
    "v_mov_b64 v[76:77], 0\n" \
    "v_mov_b64 v[78:79], 0\n" \
    "v_mov_b64 v[80:81], 0\n" \
    "v_mov_b64 v[82:83], 0\n" \
    "v_mov_b64 v[84:85], 0\n" \
    "v_mov_b64 v[86:87], 0\n" \
    "v_mov_b64 v[88:89], 0\n" \
    "v_mov_b64 v[90:91], 0\n" \
    "v_mov_b64 v[92:93], 0\n" \
    "v_mov_b64 v[94:95], 0\n" \
    "v_mov_b64 v[192:193], 0\n" \
    "v_mov_b64 v[194:195], 0\n" \
    "L%=_Tloop:\n" \
    "ds_read_b64_tr_b16 v[104:105], v228 offset:16384\n" \
    "ds_read_b64_tr_b16 v[106:107], v228 offset:16416\n" \
    "ds_read_b64_tr_b16 v[108:109], v228 offset:18688\n" \
    "ds_read_b64_tr_b16 v[110:111], v228 offset:18720\n" \
    "ds_read_b64_tr_b16 v[112:113], v228 offset:20992\n" \
    "ds_read_b64_tr_b16 v[114:115], v228 offset:21024\n" \
    "ds_read_b64_tr_b16 v[116:117], v228 offset:23296\n" \
    "ds_read_b64_tr_b16 v[118:119], v228 offset:23328\n" \
    "ds_read_b64_tr_b16 v[120:121], v228 offset:16656\n" \
    "ds_read_b64_tr_b16 v[122:123], v228 offset:16688\n" \
    "ds_read_b64_tr_b16 v[124:125], v228 offset:18960\n" \
    "ds_read_b64_tr_b16 v[126:127], v228 offset:18992\n" \
    "ds_read_b64_tr_b16 v[128:129], v228 offset:21264\n" \
    "ds_read_b64_tr_b16 v[130:131], v228 offset:21296\n" \
    "ds_read_b64_tr_b16 v[132:133], v228 offset:23568\n" \
    "ds_read_b64_tr_b16 v[134:135], v228 offset:23600\n" \
    "s_waitcnt lgkmcnt(14)\n" \
    "v_mfma_f32_16x16x32_bf16 v[136:139], v[104:107], v[0:3], 0\n" \
    "v_mfma_f32_16x16x32_bf16 v[144:147], v[104:107], v[8:11], 0\n" \
    "v_mfma_f32_16x16x32_bf16 v[152:155], v[104:107], v[16:19], 0\n" \
    "v_mfma_f32_16x16x32_bf16 v[160:163], v[104:107], v[24:27], 0\n" \
    "s_waitcnt lgkmcnt(12)\n" \
    "v_mfma_f32_16x16x32_bf16 v[136:139], v[108:111], v[4:7], v[136:139]\n" \
    "v_mfma_f32_16x16x32_bf16 v[144:147], v[108:111], v[12:15], v[144:147]\n" \
    "v_mfma_f32_16x16x32_bf16 v[152:155], v[108:111], v[20:23], v[152:155]\n" \
    "v_mfma_f32_16x16x32_bf16 v[160:163], v[108:111], v[28:31], v[160:163]\n" \
    "s_waitcnt lgkmcnt(10)\n" \
    "v_mfma_f32_16x16x32_bf16 v[136:139], v[112:115], v[32:35], v[136:139]\n" \
    "v_mfma_f32_16x16x32_bf16 v[144:147], v[112:115], v[40:43], v[144:147]\n" \
    "v_mfma_f32_16x16x32_bf16 v[152:155], v[112:115], v[48:51], v[152:155]\n" \
    "v_mfma_f32_16x16x32_bf16 v[160:163], v[112:115], v[56:59], v[160:163]\n" \
    "s_waitcnt lgkmcnt(8)\n" \
    "v_mfma_f32_16x16x32_bf16 v[136:139], v[116:119], v[36:39], v[136:139]\n" \
    "v_mfma_f32_16x16x32_bf16 v[144:147], v[116:119], v[44:47], v[144:147]\n" \
    "v_mfma_f32_16x16x32_bf16 v[152:155], v[116:119], v[52:55], v[152:155]\n" \
    "v_mfma_f32_16x16x32_bf16 v[160:163], v[116:119], v[60:63], v[160:163]\n" \
    "s_waitcnt lgkmcnt(6)\n" \
    "v_mfma_f32_16x16x32_bf16 v[140:143], v[120:123], v[0:3], 0\n" \
    "v_mfma_f32_16x16x32_bf16 v[148:151], v[120:123], v[8:11], 0\n" \
    "v_mfma_f32_16x16x32_bf16 v[156:159], v[120:123], v[16:19], 0\n" \
    "v_mfma_f32_16x16x32_bf16 v[164:167], v[120:123], v[24:27], 0\n" \
    "s_waitcnt lgkmcnt(4)\n" \
    "v_mfma_f32_16x16x32_bf16 v[140:143], v[124:127], v[4:7], v[140:143]\n" \
    "v_mfma_f32_16x16x32_bf16 v[148:151], v[124:127], v[12:15], v[148:151]\n" \
    "v_mfma_f32_16x16x32_bf16 v[156:159], v[124:127], v[20:23], v[156:159]\n" \
    "v_mfma_f32_16x16x32_bf16 v[164:167], v[124:127], v[28:31], v[164:167]\n" \
    "s_waitcnt lgkmcnt(2)\n" \
    "v_mfma_f32_16x16x32_bf16 v[140:143], v[128:131], v[32:35], v[140:143]\n" \
    "v_mfma_f32_16x16x32_bf16 v[148:151], v[128:131], v[40:43], v[148:151]\n" \
    "v_mfma_f32_16x16x32_bf16 v[156:159], v[128:131], v[48:51], v[156:159]\n" \
    "v_mfma_f32_16x16x32_bf16 v[164:167], v[128:131], v[56:59], v[164:167]\n" \
    "s_waitcnt lgkmcnt(0)\n" \
    "v_mfma_f32_16x16x32_bf16 v[140:143], v[132:135], v[36:39], v[140:143]\n" \
    "v_mfma_f32_16x16x32_bf16 v[148:151], v[132:135], v[44:47], v[148:151]\n" \
    "v_mfma_f32_16x16x32_bf16 v[156:159], v[132:135], v[52:55], v[156:159]\n" \
    "v_mfma_f32_16x16x32_bf16 v[164:167], v[132:135], v[60:63], v[164:167]\n" \
    "v_cvt_pk_bf16_f32 v65, v136, v137\n" \
    "v_cvt_pk_bf16_f32 v68, v138, v139\n" \
    "v_cvt_pk_bf16_f32 v73, v144, v145\n" \
    "v_cvt_pk_bf16_f32 v76, v146, v147\n" \
    "v_cvt_pk_bf16_f32 v81, v152, v153\n" \
    "v_cvt_pk_bf16_f32 v84, v154, v155\n" \
    "v_cvt_pk_bf16_f32 v89, v160, v161\n" \
    "v_cvt_pk_bf16_f32 v92, v162, v163\n" \
    "v_cvt_pk_bf16_f32 v66, v142, v143\n" \
    "v_cvt_pk_bf16_f32 v67, v142, v143\n" \
    "v_cvt_pk_bf16_f32 v69, v140, v141\n" \
    "v_cvt_pk_bf16_f32 v74, v150, v151\n" \
    "v_cvt_pk_bf16_f32 v75, v150, v151\n" \
    "v_cvt_pk_bf16_f32 v77, v148, v149\n" \
    "v_cvt_pk_bf16_f32 v82, v158, v159\n" \
    "v_cvt_pk_bf16_f32 v83, v158, v159\n" \
    "v_cvt_pk_bf16_f32 v85, v156, v157\n" \
    "v_cvt_pk_bf16_f32 v90, v166, v167\n" \
    "v_cvt_pk_bf16_f32 v91, v166, v167\n" \
    "v_cvt_pk_bf16_f32 v93, v164, v165\n" \
    "v_permlane16_swap_b32 v66, v67\n" \
    "v_permlane16_swap_b32 v74, v75\n" \
    "v_permlane16_swap_b32 v82, v83\n" \
    "v_permlane16_swap_b32 v90, v91\n" \
    "v_mov_b64 v[176:177], 0\n" \
    "v_mov_b64 v[178:179], 0\n" \
    "v_mov_b64 v[180:181], 0\n" \
    "v_mov_b64 v[182:183], 0\n" \
    "v_mov_b64 v[184:185], 0\n" \
    "v_mov_b64 v[186:187], 0\n" \
    "v_mov_b64 v[188:189], 0\n" \
    "v_mov_b64 v[190:191], 0\n" \
    "v_mov_b64 v[196:197], 0\n" \
    "v_mov_b64 v[198:199], 0\n" \
    "v_mov_b64 v[200:201], 0\n" \
    "v_mov_b64 v[202:203], 0\n" \
    "v_mov_b64 v[204:205], 0\n" \
    "v_mov_b64 v[206:207], 0\n" \
    "v_mov_b64 v[208:209], 0\n" \
    "v_mov_b64 v[210:211], 0\n" \
    "v_mov_b64 v[212:213], 0\n" \
    "v_mov_b64 v[214:215], 0\n" \
    "s_add_u32 s85, s60, 0x1000\n" \
    "s_sub_u32 s74, s76, 2\n" \
    "s_mul_i32 s74, s74, 2\n" \
    "s_cmp_lt_u32 s74, 64\n" \
    "s_cbranch_scc0 L%=_Tnd\n" \
    "s_lshr_b32 s79, s74, 2\n" \
    "s_lshl_b32 s79, s79, 7\n" \
    "s_and_b32 s75, s74, 3\n" \
    "s_add_u32 s79, s79, s75\n" \
    "s_lshl_b32 s79, s79, 5\n" \
    "v_add_u32 v160, s79, v234\n" \
    "ds_read_b64_tr_b16 v[144:145], v160 offset:0\n" \
    "ds_read_b64_tr_b16 v[146:147], v160 offset:2048\n" \
    "ds_read_b64_tr_b16 v[148:149], v160 offset:32\n" \
    "ds_read_b64_tr_b16 v[150:151], v160 offset:2080\n" \
    "s_and_b32 s75, s74, 48\n" \
    "s_mul_i32 s75, s75, 31\n" \
    "s_and_b32 s79, s74, 8\n" \
    "s_add_u32 s75, s75, s79\n" \
    "s_add_u32 s75, s75, s74\n" \
    "s_lshl_b32 s75, s75, 10\n" \
    "s_branch L%=_Tu2b\n" \
    "L%=_Tu2a:\n" \
    "s_sleep 4\n" \
    "L%=_Tu2b:\n" \
    "s_memrealtime vcc\n" \
    "s_add_u32 s80, s77, s64\n" \
    "s_add_u32 s80, s80, s69\n" \
    "s_waitcnt lgkmcnt(0)\n" \
    "s_sub_i32 s79, s80, vcc_lo\n" \
    "s_sub_u32 s79, s79, 40\n" \
    "s_cmp_lt_u32 s79, 0x400\n" \
    "s_cbranch_scc1 L%=_Tu2a\n" \
    "s_memtime s[38:39]\n" \
    "s_memrealtime vcc\n" \
    "s_nop 4\n" \
    "s_memrealtime s[32:33]\n" \
    "s_nop 4\n" \
    "s_memrealtime s[34:35]\n" \
    "s_nop 4\n" \
    "s_memrealtime s[36:37]\n" \
    "s_add_u32 s80, s77, s64\n" \
    "s_lshl_b32 s80, s80, 2\n" \
    "s_waitcnt lgkmcnt(0)\n" \
    "s_sub_u32 s80, s80, vcc_lo\n" \
    "s_sub_u32 s80, s80, s32\n" \
    "s_sub_u32 s80, s80, s34\n" \
    "s_sub_u32 s80, s80, s36\n" \
    "s_mul_i32 s80, s80, s71\n" \
    "s_ashr_i32 s80, s80, 6\n" \
    "s_add_u32 s78, s80, s38\n" \
    "L%=_Tu3a:\n" \
    "s_memtime s[38:39]\n" \
    "s_mul_i32 s80, s69, 23\n" \
    "s_add_u32 s80, s80, s78\n" \
    "s_waitcnt lgkmcnt(0)\n" \
    "s_sub_i32 s79, s80, s38\n" \
    "s_max_i32 s79, s79, 0\n" \
    "s_cmp_gt_u32 s79, 320\n" \
    "s_cbranch_scc1 L%=_Tu3s\n" \
    ".p2align 4\n" \
    "L%=_Tu3d:\n" \
    "s_sub_u32 s79, s79, 28\n" \
    "s_cbranch_scc0 L%=_Tu3d\n" \
    "L%=_Tu3e:\n" \
    "buffer_store_dwordx4 v[144:147], v235, s[48:51], s75 offen offset:0 sc1\n" \
    "buffer_store_dwordx4 v[148:151], v235, s[48:51], s75 offen offset:1024 sc1\n" \
    "s_branch L%=_Tdd\n" \
    "L%=_Tnd:\n" \
    "s_branch L%=_Tu4b\n" \
    "L%=_Tu4a:\n" \
    "s_sleep 4\n" \
    "L%=_Tu4b:\n" \
    "s_memrealtime vcc\n" \
    "s_add_u32 s80, s77, s64\n" \
    "s_add_u32 s80, s80, s69\n" \
    "s_waitcnt lgkmcnt(0)\n" \
    "s_sub_i32 s79, s80, vcc_lo\n" \
    "s_sub_u32 s79, s79, 40\n" \
    "s_cmp_lt_u32 s79, 0x400\n" \
    "s_cbranch_scc1 L%=_Tu4a\n" \
    "s_memtime s[38:39]\n" \
    "s_memrealtime vcc\n" \
    "s_nop 4\n" \
    "s_memrealtime s[32:33]\n" \
    "s_nop 4\n" \
    "s_memrealtime s[34:35]\n" \
    "s_nop 4\n" \
    "s_memrealtime s[36:37]\n" \
    "s_add_u32 s80, s77, s64\n" \
    "s_lshl_b32 s80, s80, 2\n" \
    "s_waitcnt lgkmcnt(0)\n" \
    "s_sub_u32 s80, s80, vcc_lo\n" \
    "s_sub_u32 s80, s80, s32\n" \
    "s_sub_u32 s80, s80, s34\n" \
    "s_sub_u32 s80, s80, s36\n" \
    "s_mul_i32 s80, s80, s71\n" \
    "s_ashr_i32 s80, s80, 6\n" \
    "s_add_u32 s78, s80, s38\n" \
    "L%=_Tdd:\n" \
    "v_mov_b32 v143, s76\n" \
    "s_barrier\n" \
    "s_cmp_eq_u32 s76, 63\n" \
    "s_cbranch_scc1 L%=_Tfin\n" \
    "ds_read_b32 v216, v229\n" \
    "s_waitcnt lgkmcnt(0)\n" \
    "v_readfirstlane_b32 s94, v216\n" \
    "s_mul_i32 s79, s94, s70\n" \
    "s_add_u32 s79, s79, s81\n" \
    "s_cmp_lg_u32 s79, s77\n" \
    "s_cbranch_scc1 L%=_Tu5s\n" \
    "L%=_Tu5k:\n" \
    "s_setprio 1\n" \
    "L%=_Tu6a:\n" \
    "s_memtime s[38:39]\n" \
    "s_mul_hi_u32 s80, s66, s96\n" \
    "s_add_u32 s80, s80, s78\n" \
    "s_waitcnt lgkmcnt(0)\n" \
    "s_sub_i32 s79, s80, s38\n" \
    "s_max_i32 s79, s79, 0\n" \
    "s_cmp_gt_u32 s79, 320\n" \
    "s_cbranch_scc1 L%=_Tu6s\n" \
    ".p2align 4\n" \
    "L%=_Tu6d:\n" \
    "s_sub_u32 s79, s79, 28\n" \
    "s_cbranch_scc0 L%=_Tu6d\n" \
    "L%=_Tu6e:\n" \
    "buffer_load_dwordx4 v[104:107], v230, s[40:43], s60 offen offset:0 sc1\n" \
    "buffer_load_dwordx2 v[120:121], v231, s[40:43], s60 offen offset:0 sc1\n" \
    "buffer_load_dwordx4 v[108:111], v230, s[40:43], s60 offen offset:2048 sc1\n" \
    "buffer_load_dwordx2 v[122:123], v231, s[40:43], s60 offen offset:2048 sc1\n" \
    "buffer_load_dwordx4 v[112:115], v230, s[40:43], s85 offen offset:0 sc1\n" \
    "buffer_load_dwordx2 v[124:125], v231, s[40:43], s85 offen offset:0 sc1\n" \
    "buffer_load_dwordx4 v[116:119], v230, s[40:43], s85 offen offset:2048 sc1\n" \
    "buffer_load_dwordx2 v[126:127], v231, s[40:43], s85 offen offset:2048 sc1\n" \
    "s_setprio 3\n" \
    "buffer_load_dwordx4 v[168:171], v225, s[40:43], s60 offen sc1\n" \
    "buffer_load_dwordx4 v[172:175], v225, s[40:43], s85 offen sc1\n" \
    "L%=_Tsc:\n" \
    "s_waitcnt vmcnt(6)\n" \
    "v_pk_add_f32 v[128:129], v[104:105], v[108:109]\n" \
    "v_pk_add_f32 v[130:131], v[106:107], v[110:111]\n" \
    "v_pk_add_f32 v[132:133], v[120:121], v[122:123]\n" \
    "s_waitcnt vmcnt(4)\n" \
    "v_pk_add_f32 v[128:129], v[128:129], v[112:113]\n" \
    "v_pk_add_f32 v[130:131], v[130:131], v[114:115]\n" \
    "v_pk_add_f32 v[132:133], v[132:133], v[124:125]\n" \
    "s_waitcnt vmcnt(2)\n" \
    "v_pk_add_f32 v[128:129], v[128:129], v[116:117]\n" \
    "v_pk_add_f32 v[130:131], v[130:131], v[118:119]\n" \
    "v_pk_add_f32 v[132:133], v[132:133], v[126:127]\n" \
    "v_max3_f32 v136, v128, v129, v130\n" \
    "v_max3_f32 v137, v131, v132, v133\n" \
    "v_max_f32 v136, v136, v137\n" \
    "v_fma_f32 v141, v131, v132, v133\n" \
    "v_fma_f32 v140, v128, v129, v130\n" \
    "v_max_f32_dpp v138, v136, v136 row_ror:8 row_mask:0xf bank_mask:0xf\n" \
    "v_max_f32_dpp v139, v136, v136 row_ror:8 row_mask:0xf bank_mask:0xf\n" \
    "s_waitcnt vmcnt(0)\n" \
    "v_smfmac_f32_16x16x64_bf16 v[176:179], v[168:171], v[0:7], v224\n" \
    "v_cmp_u_f32 vcc, v140, v141\n" \
    "v_permlane16_swap_b32 v138, v139\n" \
    "v_max_f32 v136, v138, v139\n" \
    "v_max_f32 v137, v138, v139\n" \
    "v_smfmac_f32_16x16x64_bf16 v[180:183], v[168:171], v[8:15], v224\n" \
    "s_cbranch_vccnz L%=_Tslow\n" \
    "L%=_Tok:\n" \
    "v_permlane32_swap_b32 v136, v137\n" \
    "v_max_f32 v136, v136, v137\n" \
    "v_mul_f32 v136, s72, v136\n" \
    "v_pk_fma_f32 v[128:129], v[128:129], s[72:73], v[136:137] op_sel_hi:[1,1,0] neg_lo:[0,0,1] neg_hi:[0,0,1]\n" \
    "v_pk_fma_f32 v[132:133], v[132:133], s[72:73], v[136:137] op_sel_hi:[1,1,0] neg_lo:[0,0,1] neg_hi:[0,0,1]\n" \
    "v_pk_fma_f32 v[130:131], v[130:131], s[72:73], v[136:137] op_sel_hi:[1,1,0] neg_lo:[0,0,1] neg_hi:[0,0,1]\n" \
    "v_exp_f32 v128, v128\n" \
    "v_exp_f32 v129, v129\n" \
    "v_exp_f32 v132, v132\n" \
    "v_exp_f32 v133, v133\n" \
    "v_exp_f32 v130, v130\n" \
    "v_exp_f32 v131, v131\n" \
    "v_cvt_pk_bf16_f32 v140, v128, v129\n" \
    "v_cvt_pk_bf16_f32 v141, v132, v133\n" \
    "v_cvt_pk_bf16_f32 v142, v130, v131\n" \
    "ds_write_b128 v232, v[140:143]\n" \
    "s_barrier\n" \
    "ds_read_b128 v[192:195], v227\n" \
    "s_waitcnt vmcnt(0)\n" \
    "v_smfmac_f32_16x16x64_bf16 v[176:179], v[172:175], v[32:39], v224\n" \
    "L%=_Tvc:\n" \
    "v_smfmac_f32_16x16x64_bf16 v[180:183], v[172:175], v[40:47], v224\n" \
    "v_smfmac_f32_16x16x64_bf16 v[184:187], v[168:171], v[16:23], v224\n" \
    "v_add_f32 v176, v176, v178\n" \
    "v_smfmac_f32_16x16x64_bf16 v[188:191], v[168:171], v[24:31], v224\n" \
    "v_add_f32 v177, v177, v179\n" \
    "v_smfmac_f32_16x16x64_bf16 v[184:187], v[172:175], v[48:55], v224\n" \
    "v_cmp_u_f32 vcc, v176, v177\n" \
    "v_smfmac_f32_16x16x64_bf16 v[188:191], v[172:175], v[56:63], v224\n" \
    "s_cbranch_vccnz L%=_Tvslow\n" \
    "L%=_Tvok:\n" \
    "v_cvt_pk_bf16_f32 v64, v176, v177\n" \
    "v_add_f32 v180, v180, v182\n" \
    "s_waitcnt lgkmcnt(0)\n" \
    "v_smfmac_f32_16x16x64_bf16 v[212:215], v[192:195], v[96:103], v224\n" \
    "v_add_f32 v181, v181, v183\n" \
    "v_smfmac_f32_16x16x64_bf16 v[196:199], v[192:195], v[64:71], v224\n" \
    "v_cvt_pk_bf16_f32 v72, v180, v181\n" \
    "v_add_f32 v184, v184, v186\n" \
    "v_add_f32 v185, v185, v187\n" \
    "v_smfmac_f32_16x16x64_bf16 v[200:203], v[192:195], v[72:79], v224\n" \
    "v_cvt_pk_bf16_f32 v80, v184, v185\n" \
    "v_add_f32 v188, v188, v190\n" \
    "v_add_f32 v189, v189, v191\n" \
    "v_smfmac_f32_16x16x64_bf16 v[204:207], v[192:195], v[80:87], v224\n" \
    "v_cvt_pk_bf16_f32 v88, v188, v189\n" \
    "v_add_f32 v212, v212, v214\n" \
    "v_add_f32 v213, v213, v215\n" \
    "v_smfmac_f32_16x16x64_bf16 v[208:211], v[192:195], v[88:95], v224\n" \
    "v_rcp_f32 v220, v212\n" \
    "v_rcp_f32 v221, v213\n" \
    "v_add_f32 v196, v196, v198\n" \
    "v_add_f32 v197, v197, v199\n" \
    "v_pk_add_f32 v[200:201], v[200:201], v[202:203]\n" \
    "v_pk_add_f32 v[204:205], v[204:205], v[206:207]\n" \
    "s_nop 1\n" \
    "v_pk_add_f32 v[208:209], v[208:209], v[210:211]\n" \
    "v_pk_mul_f32 v[196:197], v[196:197], v[220:221]\n" \
    "v_pk_mul_f32 v[200:201], v[200:201], v[220:221]\n" \
    "v_pk_mul_f32 v[204:205], v[204:205], v[220:221]\n" \
    "v_pk_mul_f32 v[208:209], v[208:209], v[220:221]\n" \
    "v_cvt_pk_bf16_f32 v216, v196, v200\n" \
    "v_cvt_pk_bf16_f32 v217, v204, v208\n" \
    "v_cvt_pk_bf16_f32 v218, v197, v201\n" \
    "v_cvt_pk_bf16_f32 v219, v205, v209\n" \
    "buffer_store_dwordx4 v[216:219], v226, s[40:43], s60 offen sc1\n" \
    "s_setprio 0\n" \
    "s_cmp_ge_u32 s76, 63\n" \
    "s_addc_u32 s79, s76, 0\n" \
    "v_mov_b32 v143, s79\n" \
    "buffer_store_dword v143, v233, s[44:47], 0 offen sc1\n" \
    "ds_read_b64 v[220:221], v229 offset:64\n" \
    "s_mov_b32 s79, s60\n" \
    "s_mov_b32 s60, s61\n" \
    "s_mov_b32 s61, s62\n" \
    "s_mov_b32 s62, s63\n" \
    "s_mov_b32 s63, s79\n" \
    "s_add_u32 s76, s76, 1\n" \
    "s_add_u32 s84, s84, 0x20000\n" \
    "s_add_u32 s77, s77, s65\n" \
    "s_add_u32 s81, s81, s65\n" \
    "s_waitcnt lgkmcnt(0)\n" \
    "v_readfirstlane_b32 s65, v220\n" \
    "v_readfirstlane_b32 s71, v221\n" \
    "s_mul_i32 s96, s65, s71\n" \
    "s_lshr_b32 s96, s96, 4\n" \
    "s_branch L%=_Tloop\n" \
    "L%=_Tslow:\n" \
    "s_nop 7\n" \
    "s_nop 0\n" \
    "v_max_u32 v138, v104, v105\n" \
    "v_max3_u32 v138, v138, v106, v107\n" \
    "v_max3_u32 v138, v138, v108, v109\n" \
    "v_max3_u32 v138, v138, v110, v111\n" \
    "v_max3_u32 v138, v138, v112, v113\n" \
    "v_max3_u32 v138, v138, v114, v115\n" \
    "v_max3_u32 v138, v138, v116, v117\n" \
    "v_max3_u32 v138, v138, v118, v119\n" \
    "v_max3_u32 v138, v138, v120, v121\n" \
    "v_max3_u32 v138, v138, v122, v123\n" \
    "v_max3_u32 v138, v138, v124, v125\n" \
    "v_max3_u32 v138, v138, v126, v127\n" \
    "v_cmp_eq_u32 vcc, -1, v138\n" \
    "s_cbranch_vccz L%=_Tok\n" \
    "s_cmp_eq_u32 s99, 0\n" \
    "s_cbranch_scc1 L%=_Tok\n" \
    "s_sub_u32 s99, s99, 1\n" \
    "s_cmp_lg_u32 s99, 0\n" \
    "s_cbranch_scc1 L%=_Tu7\n" \
    "v_mov_b32 v138, 1\n" \
    "buffer_store_dword v138, off, s[44:47], 0 sc1\n" \
    "L%=_Tu7:\n" \
    "s_sleep 1\n" \
    "buffer_load_dwordx4 v[104:107], v230, s[40:43], s60 offen offset:0 sc1\n" \
    "buffer_load_dwordx2 v[120:121], v231, s[40:43], s60 offen offset:0 sc1\n" \
    "buffer_load_dwordx4 v[108:111], v230, s[40:43], s60 offen offset:2048 sc1\n" \
    "buffer_load_dwordx2 v[122:123], v231, s[40:43], s60 offen offset:2048 sc1\n" \
    "buffer_load_dwordx4 v[112:115], v230, s[40:43], s85 offen offset:0 sc1\n" \
    "buffer_load_dwordx2 v[124:125], v231, s[40:43], s85 offen offset:0 sc1\n" \
    "buffer_load_dwordx4 v[116:119], v230, s[40:43], s85 offen offset:2048 sc1\n" \
    "buffer_load_dwordx2 v[126:127], v231, s[40:43], s85 offen offset:2048 sc1\n" \
    "buffer_load_dwordx4 v[168:171], v225, s[40:43], s60 offen sc1\n" \
    "buffer_load_dwordx4 v[172:175], v225, s[40:43], s85 offen sc1\n" \
    "v_mov_b64 v[176:177], 0\n" \
    "v_mov_b64 v[178:179], 0\n" \
    "v_mov_b64 v[180:181], 0\n" \
    "v_mov_b64 v[182:183], 0\n" \
    "v_mov_b64 v[184:185], 0\n" \
    "v_mov_b64 v[186:187], 0\n" \
    "v_mov_b64 v[188:189], 0\n" \
    "v_mov_b64 v[190:191], 0\n" \
    "s_branch L%=_Tsc\n" \
    "L%=_Tvslow:\n" \
    "s_nop 7\n" \
    "s_nop 0\n" \
    "v_max_u32 v136, v168, v169\n" \
    "v_max3_u32 v136, v136, v170, v171\n" \
    "v_max3_u32 v136, v136, v172, v173\n" \
    "v_max3_u32 v136, v136, v174, v175\n" \
    "v_cmp_eq_u32 vcc, -1, v136\n" \
    "s_cbranch_vccz L%=_Tvok\n" \
    "s_cmp_eq_u32 s99, 0\n" \
    "s_cbranch_scc1 L%=_Tvok\n" \
    "s_sub_u32 s99, s99, 1\n" \
    "s_cmp_lg_u32 s99, 0\n" \
    "s_cbranch_scc1 L%=_Tu8\n" \
    "v_mov_b32 v136, 1\n" \
    "buffer_store_dword v136, off, s[44:47], 0 sc1\n" \
    "L%=_Tu8:\n" \
    "s_sleep 1\n" \
    "buffer_load_dwordx4 v[168:171], v225, s[40:43], s60 offen sc1\n" \
    "buffer_load_dwordx4 v[172:175], v225, s[40:43], s85 offen sc1\n" \
    "v_mov_b64 v[176:177], 0\n" \
    "v_mov_b64 v[178:179], 0\n" \
    "v_mov_b64 v[180:181], 0\n" \
    "v_mov_b64 v[182:183], 0\n" \
    "v_mov_b64 v[184:185], 0\n" \
    "v_mov_b64 v[186:187], 0\n" \
    "v_mov_b64 v[188:189], 0\n" \
    "v_mov_b64 v[190:191], 0\n" \
    "v_mov_b64 v[196:197], 0\n" \
    "v_mov_b64 v[198:199], 0\n" \
    "v_mov_b64 v[200:201], 0\n" \
    "v_mov_b64 v[202:203], 0\n" \
    "v_mov_b64 v[204:205], 0\n" \
    "v_mov_b64 v[206:207], 0\n" \
    "v_mov_b64 v[208:209], 0\n" \
    "v_mov_b64 v[210:211], 0\n" \
    "v_mov_b64 v[212:213], 0\n" \
    "v_mov_b64 v[214:215], 0\n" \
    "s_waitcnt vmcnt(0)\n" \
    "v_smfmac_f32_16x16x64_bf16 v[176:179], v[168:171], v[0:7], v224\n" \
    "v_smfmac_f32_16x16x64_bf16 v[180:183], v[168:171], v[8:15], v224\n" \
    "v_smfmac_f32_16x16x64_bf16 v[176:179], v[172:175], v[32:39], v224\n" \
    "s_nop 7\n" \
    "s_nop 0\n" \
    "s_branch L%=_Tvc\n" \
    "L%=_Tfin:\n" \
    "v_mov_b32 v143, 64\n" \
    "s_mov_b32 s98, s81\n" \
    "s_barrier\n" \
    "buffer_store_dword v143, v233, s[44:47], 0 offen sc1\n" \
    "s_branch L%=_end\n" \
    "L%=_Tu3s:\n" \
    "s_cmp_gt_u32 s79, 0x8000\n" \
    "s_cbranch_scc1 L%=_Tu3e\n" \
    "s_sleep 2\n" \
    "s_branch L%=_Tu3a\n" \
    "L%=_Tu5s:\n" \
    "s_mov_b32 s77, s79\n" \
    "s_memtime s[38:39]\n" \
    "s_memrealtime vcc\n" \
    "s_nop 4\n" \
    "s_memrealtime s[32:33]\n" \
    "s_nop 4\n" \
    "s_memrealtime s[34:35]\n" \
    "s_nop 4\n" \
    "s_memrealtime s[36:37]\n" \
    "s_add_u32 s80, s77, s64\n" \
    "s_lshl_b32 s80, s80, 2\n" \
    "s_waitcnt lgkmcnt(0)\n" \
    "s_sub_u32 s80, s80, vcc_lo\n" \
    "s_sub_u32 s80, s80, s32\n" \
    "s_sub_u32 s80, s80, s34\n" \
    "s_sub_u32 s80, s80, s36\n" \
    "s_mul_i32 s80, s80, s71\n" \
    "s_ashr_i32 s80, s80, 6\n" \
    "s_add_u32 s78, s80, s38\n" \
    "s_branch L%=_Tu5k\n" \
    "L%=_Tu6s:\n" \
    "s_cmp_gt_u32 s79, 0x8000\n" \
    "s_cbranch_scc1 L%=_Tu6e\n" \
    "s_sleep 2\n" \
    "s_branch L%=_Tu6a\n" \
    "L%=_S:\n" \
    "s_mov_b32 s76, 0\n" \
    "s_mov_b32 s99, 0x20000\n" \
    "s_mov_b32 s84, 0\n" \
    "s_mov_b32 s81, 0\n" \
    "s_mov_b32 s86, 0\n" \
    "s_mov_b32 s87, 0\n" \
    "s_mov_b32 s88, 0\n" \
    "s_mov_b32 s89, 0\n" \
    "s_mov_b32 s90, 0\n" \
    "s_mov_b32 s91, 0\n" \
    "s_mov_b32 s92, 0\n" \
    "s_mov_b32 s93, 0\n" \
    "s_mov_b32 s94, 0\n" \
    "s_mov_b32 s95, 0\n" \
    "s_mov_b32 s96, 0\n" \
    "s_mov_b32 s97, 0\n" \
    "s_mov_b32 s98, 0\n" \
    "ds_read_b32 v216, v229\n" \
    "s_waitcnt lgkmcnt(0)\n" \
    "v_readfirstlane_b32 s94, v216\n" \
    "s_mul_i32 s79, s94, s70\n" \
    "s_add_u32 s79, s79, s81\n" \
    "s_mov_b32 s77, s79\n" \
    "s_mul_i32 s96, s65, s71\n" \
    "s_lshr_b32 s96, s96, 4\n" \
    "v_mov_b32 v96, 0x3f803f80\n" \
    "v_mov_b32 v97, 0x3f803f80\n" \
    "v_mov_b32 v98, 0x3f803f80\n" \
    "v_mov_b32 v99, 0x3f803f80\n" \
    "v_mov_b32 v100, 0x3f803f80\n" \
    "v_mov_b32 v101, 0x3f803f80\n" \
    "v_mov_b64 v[102:103], 0\n" \
    "v_mov_b64 v[64:65], 0\n" \
    "v_mov_b64 v[66:67], 0\n" \
    "v_mov_b64 v[68:69], 0\n" \
    "v_mov_b64 v[70:71], 0\n" \
    "v_mov_b64 v[72:73], 0\n" \
    "v_mov_b64 v[74:75], 0\n" \
    "v_mov_b64 v[76:77], 0\n" \
    "v_mov_b64 v[78:79], 0\n" \
    "v_mov_b64 v[80:81], 0\n" \
    "v_mov_b64 v[82:83], 0\n" \
    "v_mov_b64 v[84:85], 0\n" \
    "v_mov_b64 v[86:87], 0\n" \
    "v_mov_b64 v[88:89], 0\n" \
    "v_mov_b64 v[90:91], 0\n" \
    "v_mov_b64 v[92:93], 0\n" \
    "v_mov_b64 v[94:95], 0\n" \
    "v_mov_b64 v[192:193], 0\n" \
    "v_mov_b64 v[194:195], 0\n" \
    "L%=_Sloop:\n" \
    "ds_read_b64_tr_b16 v[104:105], v228 offset:16384\n" \
    "ds_read_b64_tr_b16 v[106:107], v228 offset:16416\n" \
    "ds_read_b64_tr_b16 v[108:109], v228 offset:18688\n" \
    "ds_read_b64_tr_b16 v[110:111], v228 offset:18720\n" \
    "ds_read_b64_tr_b16 v[112:113], v228 offset:20992\n" \
    "ds_read_b64_tr_b16 v[114:115], v228 offset:21024\n" \
    "ds_read_b64_tr_b16 v[116:117], v228 offset:23296\n" \
    "ds_read_b64_tr_b16 v[118:119], v228 offset:23328\n" \
    "ds_read_b64_tr_b16 v[120:121], v228 offset:16656\n" \
    "ds_read_b64_tr_b16 v[122:123], v228 offset:16688\n" \
    "ds_read_b64_tr_b16 v[124:125], v228 offset:18960\n" \
    "ds_read_b64_tr_b16 v[126:127], v228 offset:18992\n" \
    "ds_read_b64_tr_b16 v[128:129], v228 offset:21264\n" \
    "ds_read_b64_tr_b16 v[130:131], v228 offset:21296\n" \
    "ds_read_b64_tr_b16 v[132:133], v228 offset:23568\n" \
    "ds_read_b64_tr_b16 v[134:135], v228 offset:23600\n" \
    "s_waitcnt lgkmcnt(14)\n" \
    "v_mfma_f32_16x16x32_bf16 v[136:139], v[104:107], v[0:3], 0\n" \
    "v_mfma_f32_16x16x32_bf16 v[144:147], v[104:107], v[8:11], 0\n" \
    "v_mfma_f32_16x16x32_bf16 v[152:155], v[104:107], v[16:19], 0\n" \
    "v_mfma_f32_16x16x32_bf16 v[160:163], v[104:107], v[24:27], 0\n" \
    "s_waitcnt lgkmcnt(12)\n" \
    "v_mfma_f32_16x16x32_bf16 v[136:139], v[108:111], v[4:7], v[136:139]\n" \
    "v_mfma_f32_16x16x32_bf16 v[144:147], v[108:111], v[12:15], v[144:147]\n" \
    "v_mfma_f32_16x16x32_bf16 v[152:155], v[108:111], v[20:23], v[152:155]\n" \
    "v_mfma_f32_16x16x32_bf16 v[160:163], v[108:111], v[28:31], v[160:163]\n" \
    "s_waitcnt lgkmcnt(10)\n" \
    "v_mfma_f32_16x16x32_bf16 v[136:139], v[112:115], v[32:35], v[136:139]\n" \
    "v_mfma_f32_16x16x32_bf16 v[144:147], v[112:115], v[40:43], v[144:147]\n" \
    "v_mfma_f32_16x16x32_bf16 v[152:155], v[112:115], v[48:51], v[152:155]\n" \
    "v_mfma_f32_16x16x32_bf16 v[160:163], v[112:115], v[56:59], v[160:163]\n" \
    "s_waitcnt lgkmcnt(8)\n" \
    "v_mfma_f32_16x16x32_bf16 v[136:139], v[116:119], v[36:39], v[136:139]\n" \
    "v_mfma_f32_16x16x32_bf16 v[144:147], v[116:119], v[44:47], v[144:147]\n" \
    "v_mfma_f32_16x16x32_bf16 v[152:155], v[116:119], v[52:55], v[152:155]\n" \
    "v_mfma_f32_16x16x32_bf16 v[160:163], v[116:119], v[60:63], v[160:163]\n" \
    "s_waitcnt lgkmcnt(6)\n" \
    "v_mfma_f32_16x16x32_bf16 v[140:143], v[120:123], v[0:3], 0\n" \
    "v_mfma_f32_16x16x32_bf16 v[148:151], v[120:123], v[8:11], 0\n" \
    "v_mfma_f32_16x16x32_bf16 v[156:159], v[120:123], v[16:19], 0\n" \
    "v_mfma_f32_16x16x32_bf16 v[164:167], v[120:123], v[24:27], 0\n" \
    "s_waitcnt lgkmcnt(4)\n" \
    "v_mfma_f32_16x16x32_bf16 v[140:143], v[124:127], v[4:7], v[140:143]\n" \
    "v_mfma_f32_16x16x32_bf16 v[148:151], v[124:127], v[12:15], v[148:151]\n" \
    "v_mfma_f32_16x16x32_bf16 v[156:159], v[124:127], v[20:23], v[156:159]\n" \
    "v_mfma_f32_16x16x32_bf16 v[164:167], v[124:127], v[28:31], v[164:167]\n" \
    "s_waitcnt lgkmcnt(2)\n" \
    "v_mfma_f32_16x16x32_bf16 v[140:143], v[128:131], v[32:35], v[140:143]\n" \
    "v_mfma_f32_16x16x32_bf16 v[148:151], v[128:131], v[40:43], v[148:151]\n" \
    "v_mfma_f32_16x16x32_bf16 v[156:159], v[128:131], v[48:51], v[156:159]\n" \
    "v_mfma_f32_16x16x32_bf16 v[164:167], v[128:131], v[56:59], v[164:167]\n" \
    "s_waitcnt lgkmcnt(0)\n" \
    "v_mfma_f32_16x16x32_bf16 v[140:143], v[132:135], v[36:39], v[140:143]\n" \
    "v_mfma_f32_16x16x32_bf16 v[148:151], v[132:135], v[44:47], v[148:151]\n" \
    "v_mfma_f32_16x16x32_bf16 v[156:159], v[132:135], v[52:55], v[156:159]\n" \
    "v_mfma_f32_16x16x32_bf16 v[164:167], v[132:135], v[60:63], v[164:167]\n" \
    "v_cvt_pk_bf16_f32 v65, v136, v137\n" \
    "v_cvt_pk_bf16_f32 v68, v138, v139\n" \
    "v_cvt_pk_bf16_f32 v73, v144, v145\n" \
    "v_cvt_pk_bf16_f32 v76, v146, v147\n" \
    "v_cvt_pk_bf16_f32 v81, v152, v153\n" \
    "v_cvt_pk_bf16_f32 v84, v154, v155\n" \
    "v_cvt_pk_bf16_f32 v89, v160, v161\n" \
    "v_cvt_pk_bf16_f32 v92, v162, v163\n" \
    "v_cvt_pk_bf16_f32 v66, v142, v143\n" \
    "v_cvt_pk_bf16_f32 v67, v142, v143\n" \
    "v_cvt_pk_bf16_f32 v69, v140, v141\n" \
    "v_cvt_pk_bf16_f32 v74, v150, v151\n" \
    "v_cvt_pk_bf16_f32 v75, v150, v151\n" \
    "v_cvt_pk_bf16_f32 v77, v148, v149\n" \
    "v_cvt_pk_bf16_f32 v82, v158, v159\n" \
    "v_cvt_pk_bf16_f32 v83, v158, v159\n" \
    "v_cvt_pk_bf16_f32 v85, v156, v157\n" \
    "v_cvt_pk_bf16_f32 v90, v166, v167\n" \
    "v_cvt_pk_bf16_f32 v91, v166, v167\n" \
    "v_cvt_pk_bf16_f32 v93, v164, v165\n" \
    "v_permlane16_swap_b32 v66, v67\n" \
    "v_permlane16_swap_b32 v74, v75\n" \
    "v_permlane16_swap_b32 v82, v83\n" \
    "v_permlane16_swap_b32 v90, v91\n" \
    "ds_read_b128 v[240:243], v234 offset:25600\n" \
    "ds_read_b128 v[244:247], v234 offset:26624\n" \
    "v_mov_b64 v[176:177], 0\n" \
    "v_mov_b64 v[178:179], 0\n" \
    "v_mov_b64 v[180:181], 0\n" \
    "v_mov_b64 v[182:183], 0\n" \
    "v_mov_b64 v[184:185], 0\n" \
    "v_mov_b64 v[186:187], 0\n" \
    "v_mov_b64 v[188:189], 0\n" \
    "v_mov_b64 v[190:191], 0\n" \
    "v_mov_b64 v[196:197], 0\n" \
    "v_mov_b64 v[198:199], 0\n" \
    "v_mov_b64 v[200:201], 0\n" \
    "v_mov_b64 v[202:203], 0\n" \
    "v_mov_b64 v[204:205], 0\n" \
    "v_mov_b64 v[206:207], 0\n" \
    "v_mov_b64 v[208:209], 0\n" \
    "v_mov_b64 v[210:211], 0\n" \
    "v_mov_b64 v[212:213], 0\n" \
    "v_mov_b64 v[214:215], 0\n" \
    "s_add_u32 s85, s60, 0x1000\n" \
    "s_branch L%=_Su2b\n" \
    "L%=_Su2a:\n" \
    "s_sleep 4\n" \
    "L%=_Su2b:\n" \
    "s_memrealtime vcc\n" \
    "s_add_u32 s80, s77, s64\n" \
    "s_add_u32 s80, s80, s69\n" \
    "s_waitcnt lgkmcnt(0)\n" \
    "s_sub_i32 s79, s80, vcc_lo\n" \
    "s_sub_u32 s79, s79, 40\n" \
    "s_cmp_lt_u32 s79, 0x400\n" \
    "s_cbranch_scc1 L%=_Su2a\n" \
    "s_memtime s[38:39]\n" \
    "s_memrealtime vcc\n" \
    "s_nop 4\n" \
    "s_memrealtime s[32:33]\n" \
    "s_nop 4\n" \
    "s_memrealtime s[34:35]\n" \
    "s_nop 4\n" \
    "s_memrealtime s[36:37]\n" \
    "s_add_u32 s80, s77, s64\n" \
    "s_lshl_b32 s80, s80, 2\n" \
    "s_waitcnt lgkmcnt(0)\n" \
    "s_sub_u32 s80, s80, vcc_lo\n" \
    "s_sub_u32 s80, s80, s32\n" \
    "s_sub_u32 s80, s80, s34\n" \
    "s_sub_u32 s80, s80, s36\n" \
    "s_mul_i32 s80, s80, s71\n" \
    "s_ashr_i32 s80, s80, 6\n" \
    "s_add_u32 s78, s80, s38\n" \
    "L%=_Su3a:\n" \
    "s_memtime s[38:39]\n" \
    "s_mul_i32 s80, s69, 23\n" \
    "s_add_u32 s80, s80, s78\n" \
    "s_waitcnt lgkmcnt(0)\n" \
    "s_sub_i32 s79, s80, s38\n" \
    "s_max_i32 s79, s79, 0\n" \
    "s_cmp_gt_u32 s79, 320\n" \
    "s_cbranch_scc1 L%=_Su3s\n" \
    ".p2align 4\n" \
    "L%=_Su3d:\n" \
    "s_sub_u32 s79, s79, 28\n" \
    "s_cbranch_scc0 L%=_Su3d\n" \
    "L%=_Su3e:\n" \
    "s_sub_u32 s79, s76, 1\n" \
    "s_cmp_lt_u32 s79, 63\n" \
    "s_cselect_b32 s79, s84, 0x40020000\n" \
    "s_sub_u32 s79, s79, 0x20000\n" \
    "buffer_store_dwordx4 v[236:239], v233, s[48:51], s79 offen sc1\n" \
    "s_setprio 3\n" \
    "s_barrier\n" \
    "L%=_Srd:\n" \
    "ds_read_b128 v[136:139], v229 offset:32\n" \
    "ds_read_b128 v[104:107], v230 offset:0\n" \
    "ds_read_b128 v[108:111], v230 offset:2176\n" \
    "ds_read_b128 v[112:115], v230 offset:4352\n" \
    "ds_read_b128 v[116:119], v230 offset:6528\n" \
    "ds_read_b128 v[120:123], v230 offset:1088\n" \
    "ds_read_b128 v[124:127], v230 offset:3264\n" \
    "ds_read_b128 v[128:131], v230 offset:5440\n" \
    "ds_read_b128 v[132:135], v230 offset:7616\n" \
    "s_waitcnt lgkmcnt(8)\n" \
    "v_min3_u32 v136, v136, v137, v138\n" \
    "v_min_u32 v136, v136, v139\n" \
    "v_cmp_ge_u32 vcc, s76, v136\n" \
    "s_cbranch_vccnz L%=_Srd\n" \
    "s_waitcnt lgkmcnt(6)\n" \
    "v_pk_add_f32 v[104:105], v[104:105], v[108:109]\n" \
    "v_pk_add_f32 v[106:107], v[106:107], v[110:111]\n" \
    "s_waitcnt lgkmcnt(4)\n" \
    "v_pk_add_f32 v[112:113], v[112:113], v[116:117]\n" \
    "v_pk_add_f32 v[114:115], v[114:115], v[118:119]\n" \
    "v_pk_add_f32 v[104:105], v[104:105], v[112:113]\n" \
    "v_pk_add_f32 v[106:107], v[106:107], v[114:115]\n" \
    "v_cvt_pk_bf16_f32 v236, v104, v105\n" \
    "v_cvt_pk_bf16_f32 v237, v106, v107\n" \
    "s_waitcnt lgkmcnt(2)\n" \
    "v_pk_add_f32 v[120:121], v[120:121], v[124:125]\n" \
    "v_pk_add_f32 v[122:123], v[122:123], v[126:127]\n" \
    "s_waitcnt lgkmcnt(0)\n" \
    "v_pk_add_f32 v[128:129], v[128:129], v[132:133]\n" \
    "v_pk_add_f32 v[130:131], v[130:131], v[134:135]\n" \
    "v_pk_add_f32 v[120:121], v[120:121], v[128:129]\n" \
    "v_pk_add_f32 v[122:123], v[122:123], v[130:131]\n" \
    "v_cvt_pk_bf16_f32 v238, v120, v121\n" \
    "v_cvt_pk_bf16_f32 v239, v122, v123\n" \
    "s_nop 1\n" \
    "v_mfma_f32_16x16x32_bf16 v[140:143], v[240:243], v[236:239], 0\n" \
    "v_mfma_f32_16x16x32_bf16 v[144:147], v[244:247], v[236:239], 0\n" \
    "v_mfma_f32_16x16x32_bf16 v[148:151], v[236:239], v[236:239], 0\n" \
    "buffer_store_dwordx4 v[140:143], v231, s[40:43], s60 offen sc1\n" \
    "s_nop 2\n" \
    "buffer_store_dwordx4 v[144:147], v231, s[40:43], s60 offen offset:512 sc1\n" \
    "s_nop 2\n" \
    "buffer_store_dwordx4 v[148:151], v232, s[40:43], s60 offen sc1\n" \
    "s_cmp_lt_u32 s76, 63\n" \
    "s_cbranch_scc1 L%=_Sne\n" \
    "s_waitcnt lgkmcnt(0)\n" \
    "buffer_store_dwordx4 v[236:239], v233, s[48:51], s84 offen sc1\n" \
    "L%=_Sne:\n" \
    "s_cmp_eq_u32 s76, 63\n" \
    "s_cbranch_scc1 L%=_Sfin\n" \
    "ds_read_b32 v216, v229\n" \
    "s_waitcnt lgkmcnt(0)\n" \
    "v_readfirstlane_b32 s94, v216\n" \
    "s_mul_i32 s79, s94, s70\n" \
    "s_add_u32 s79, s79, s81\n" \
    "s_cmp_lg_u32 s79, s77\n" \
    "s_cbranch_scc1 L%=_Su4s\n" \
    "L%=_Su4k:\n" \
    "L%=_Su5a:\n" \
    "s_memtime s[38:39]\n" \
    "s_mul_hi_u32 s80, s67, s96\n" \
    "s_add_u32 s80, s80, s78\n" \
    "s_waitcnt lgkmcnt(0)\n" \
    "s_sub_i32 s79, s80, s38\n" \
    "s_max_i32 s79, s79, 0\n" \
    "s_cmp_gt_u32 s79, 320\n" \
    "s_cbranch_scc1 L%=_Su5s\n" \
    ".p2align 4\n" \
    "L%=_Su5d:\n" \
    "s_sub_u32 s79, s79, 28\n" \
    "s_cbranch_scc0 L%=_Su5d\n" \
    "L%=_Su5e:\n" \
    "buffer_load_dwordx4 v[168:171], v225, s[40:43], s60 offen sc1\n" \
    "buffer_load_dwordx4 v[172:175], v225, s[40:43], s85 offen sc1\n" \
    "L%=_Svc:\n" \
    "s_waitcnt vmcnt(1)\n" \
    "v_smfmac_f32_16x16x64_bf16 v[176:179], v[168:171], v[0:7], v224\n" \
    "v_smfmac_f32_16x16x64_bf16 v[180:183], v[168:171], v[8:15], v224\n" \
    "s_waitcnt vmcnt(0)\n" \
    "v_smfmac_f32_16x16x64_bf16 v[176:179], v[172:175], v[32:39], v224\n" \
    "v_smfmac_f32_16x16x64_bf16 v[180:183], v[172:175], v[40:47], v224\n" \
    "v_smfmac_f32_16x16x64_bf16 v[184:187], v[168:171], v[16:23], v224\n" \
    "v_smfmac_f32_16x16x64_bf16 v[188:191], v[168:171], v[24:31], v224\n" \
    "v_add_f32 v176, v176, v178\n" \
    "v_smfmac_f32_16x16x64_bf16 v[184:187], v[172:175], v[48:55], v224\n" \
    "v_add_f32 v177, v177, v179\n" \
    "v_smfmac_f32_16x16x64_bf16 v[188:191], v[172:175], v[56:63], v224\n" \
    "v_cmp_u_f32 vcc, v176, v177\n" \
    "v_cvt_pk_bf16_f32 v64, v176, v177\n" \
    "v_add_f32 v180, v180, v182\n" \
    "v_add_f32 v181, v181, v183\n" \
    "v_cvt_pk_bf16_f32 v72, v180, v181\n" \
    "s_cbranch_vccnz L%=_Svslow\n" \
    "L%=_Svok:\n" \
    "v_add_f32 v184, v184, v186\n" \
    "v_add_f32 v185, v185, v187\n" \
    "v_cvt_pk_bf16_f32 v80, v184, v185\n" \
    "v_add_f32 v188, v188, v190\n" \
    "v_add_f32 v189, v189, v191\n" \
    "v_cvt_pk_bf16_f32 v88, v188, v189\n" \
    "s_barrier\n" \
    "L%=_Sfr:\n" \
    "ds_read_b128 v[192:195], v227\n" \
    "s_waitcnt lgkmcnt(0)\n" \
    "v_smfmac_f32_16x16x64_bf16 v[212:215], v[192:195], v[96:103], v224\n" \
    "v_cmp_ne_u32 vcc, s76, v195\n" \
    "v_smfmac_f32_16x16x64_bf16 v[196:199], v[192:195], v[64:71], v224\n" \
    "s_cbranch_vccnz L%=_Sfs\n" \
    "v_smfmac_f32_16x16x64_bf16 v[200:203], v[192:195], v[72:79], v224\n" \
    "v_smfmac_f32_16x16x64_bf16 v[204:207], v[192:195], v[80:87], v224\n" \
    "v_add_f32 v212, v212, v214\n" \
    "v_smfmac_f32_16x16x64_bf16 v[208:211], v[192:195], v[88:95], v224\n" \
    "v_add_f32 v213, v213, v215\n" \
    "v_rcp_f32 v220, v212\n" \
    "v_rcp_f32 v221, v213\n" \
    "v_add_f32 v196, v196, v198\n" \
    "v_add_f32 v197, v197, v199\n" \
    "v_pk_add_f32 v[200:201], v[200:201], v[202:203]\n" \
    "v_pk_add_f32 v[204:205], v[204:205], v[206:207]\n" \
    "s_nop 0\n" \
    "v_pk_add_f32 v[208:209], v[208:209], v[210:211]\n" \
    "v_pk_mul_f32 v[196:197], v[196:197], v[220:221]\n" \
    "v_pk_mul_f32 v[200:201], v[200:201], v[220:221]\n" \
    "v_pk_mul_f32 v[204:205], v[204:205], v[220:221]\n" \
    "v_pk_mul_f32 v[208:209], v[208:209], v[220:221]\n" \
    "v_cvt_pk_bf16_f32 v216, v196, v200\n" \
    "v_cvt_pk_bf16_f32 v217, v204, v208\n" \
    "v_cvt_pk_bf16_f32 v218, v197, v201\n" \
    "v_cvt_pk_bf16_f32 v219, v205, v209\n" \
    "buffer_store_dwordx4 v[216:219], v226, s[40:43], s60 offen sc1\n" \
    "s_setprio 0\n" \
    "ds_read_b64 v[220:221], v229 offset:64\n" \
    "s_mov_b32 s79, s60\n" \
    "s_mov_b32 s60, s61\n" \
    "s_mov_b32 s61, s62\n" \
    "s_mov_b32 s62, s63\n" \
    "s_mov_b32 s63, s79\n" \
    "s_add_u32 s76, s76, 1\n" \
    "s_add_u32 s84, s84, 0x20000\n" \
    "s_add_u32 s77, s77, s65\n" \
    "s_add_u32 s81, s81, s65\n" \
    "s_waitcnt lgkmcnt(0)\n" \
    "v_readfirstlane_b32 s65, v220\n" \
    "v_readfirstlane_b32 s71, v221\n" \
    "s_mul_i32 s96, s65, s71\n" \
    "s_lshr_b32 s96, s96, 4\n" \
    "s_branch L%=_Sloop\n" \
    "L%=_Sfin:\n" \
    "s_waitcnt vmcnt(0)\n" \
    "s_barrier\n" \
    "s_branch L%=_end\n" \
    "L%=_Su3s:\n" \
    "s_cmp_gt_u32 s79, 0x8000\n" \
    "s_cbranch_scc1 L%=_Su3e\n" \
    "s_sleep 2\n" \
    "s_branch L%=_Su3a\n" \
    "L%=_Su4s:\n" \
    "s_mov_b32 s77, s79\n" \
    "s_memtime s[38:39]\n" \
    "s_memrealtime vcc\n" \
    "s_nop 4\n" \
    "s_memrealtime s[32:33]\n" \
    "s_nop 4\n" \
    "s_memrealtime s[34:35]\n" \
    "s_nop 4\n" \
    "s_memrealtime s[36:37]\n" \
    "s_add_u32 s80, s77, s64\n" \
    "s_lshl_b32 s80, s80, 2\n" \
    "s_waitcnt lgkmcnt(0)\n" \
    "s_sub_u32 s80, s80, vcc_lo\n" \
    "s_sub_u32 s80, s80, s32\n" \
    "s_sub_u32 s80, s80, s34\n" \
    "s_sub_u32 s80, s80, s36\n" \
    "s_mul_i32 s80, s80, s71\n" \
    "s_ashr_i32 s80, s80, 6\n" \
    "s_add_u32 s78, s80, s38\n" \
    "s_branch L%=_Su4k\n" \
    "L%=_Su5s:\n" \
    "s_cmp_gt_u32 s79, 0x8000\n" \
    "s_cbranch_scc1 L%=_Su5e\n" \
    "s_sleep 2\n" \
    "s_branch L%=_Su5a\n" \
    "L%=_Svslow:\n" \
    "s_nop 7\n" \
    "s_nop 0\n" \
    "v_max_u32 v216, v168, v169\n" \
    "v_max3_u32 v216, v216, v170, v171\n" \
    "v_max3_u32 v216, v216, v172, v173\n" \
    "v_max3_u32 v216, v216, v174, v175\n" \
    "v_cmp_eq_u32 vcc, -1, v216\n" \
    "s_cbranch_vccz L%=_Svok\n" \
    "s_cmp_eq_u32 s99, 0\n" \
    "s_cbranch_scc1 L%=_Svok\n" \
    "s_sub_u32 s99, s99, 1\n" \
    "s_cmp_lg_u32 s99, 0\n" \
    "s_cbranch_scc1 L%=_Su6\n" \
    "v_mov_b32 v216, 1\n" \
    "buffer_store_dword v216, off, s[44:47], 0 sc1\n" \
    "L%=_Su6:\n" \
    "s_sleep 1\n" \
    "buffer_load_dwordx4 v[168:171], v225, s[40:43], s60 offen sc1\n" \
    "buffer_load_dwordx4 v[172:175], v225, s[40:43], s85 offen sc1\n" \
    "v_mov_b64 v[176:177], 0\n" \
    "v_mov_b64 v[178:179], 0\n" \
    "v_mov_b64 v[180:181], 0\n" \
    "v_mov_b64 v[182:183], 0\n" \
    "v_mov_b64 v[184:185], 0\n" \
    "v_mov_b64 v[186:187], 0\n" \
    "v_mov_b64 v[188:189], 0\n" \
    "v_mov_b64 v[190:191], 0\n" \
    "s_waitcnt vmcnt(0)\n" \
    "s_branch L%=_Svc\n" \
    "L%=_Sfs:\n" \
    "s_nop 7\n" \
    "s_nop 0\n" \
    "v_mov_b64 v[196:197], 0\n" \
    "v_mov_b64 v[198:199], 0\n" \
    "v_mov_b64 v[200:201], 0\n" \
    "v_mov_b64 v[202:203], 0\n" \
    "v_mov_b64 v[204:205], 0\n" \
    "v_mov_b64 v[206:207], 0\n" \
    "v_mov_b64 v[208:209], 0\n" \
    "v_mov_b64 v[210:211], 0\n" \
    "v_mov_b64 v[212:213], 0\n" \
    "v_mov_b64 v[214:215], 0\n" \
    "s_branch L%=_Sfr\n" \
    "L%=_R:\n" \
    "s_mov_b32 s76, 0\n" \
    "s_mov_b32 s99, 0x20000\n" \
    "s_mov_b32 s84, 0\n" \
    "s_mov_b32 s81, 0\n" \
    "s_mov_b32 s86, 0\n" \
    "s_mov_b32 s87, 0\n" \
    "s_mov_b32 s88, 0\n" \
    "s_mov_b32 s89, 0\n" \
    "s_mov_b32 s90, 0\n" \
    "s_mov_b32 s91, 0\n" \
    "s_mov_b32 s92, 0\n" \
    "s_mov_b32 s93, 0\n" \
    "s_mov_b32 s94, 0\n" \
    "s_mov_b32 s95, 0\n" \
    "s_mov_b32 s96, 0\n" \
    "s_mov_b32 s97, 0\n" \
    "s_mov_b32 s98, 0\n" \
    "ds_read_b32 v216, v229\n" \
    "s_waitcnt lgkmcnt(0)\n" \
    "v_readfirstlane_b32 s94, v216\n" \
    "s_mul_i32 s79, s94, s70\n" \
    "s_add_u32 s79, s79, s81\n" \
    "s_mov_b32 s77, s79\n" \
    "s_mul_i32 s96, s65, s71\n" \
    "s_lshr_b32 s96, s96, 4\n" \
    "v_mov_b32 v96, 0x3f803f80\n" \
    "v_mov_b32 v97, 0x3f803f80\n" \
    "v_mov_b32 v98, 0x3f803f80\n" \
    "v_mov_b32 v99, 0x3f803f80\n" \
    "v_mov_b32 v100, 0x3f803f80\n" \
    "v_mov_b32 v101, 0x3f803f80\n" \
    "v_mov_b64 v[102:103], 0\n" \
    "v_mov_b64 v[64:65], 0\n" \
    "v_mov_b64 v[66:67], 0\n" \
    "v_mov_b64 v[68:69], 0\n" \
    "v_mov_b64 v[70:71], 0\n" \
    "v_mov_b64 v[72:73], 0\n" \
    "v_mov_b64 v[74:75], 0\n" \
    "v_mov_b64 v[76:77], 0\n" \
    "v_mov_b64 v[78:79], 0\n" \
    "v_mov_b64 v[80:81], 0\n" \
    "v_mov_b64 v[82:83], 0\n" \
    "v_mov_b64 v[84:85], 0\n" \
    "v_mov_b64 v[86:87], 0\n" \
    "v_mov_b64 v[88:89], 0\n" \
    "v_mov_b64 v[90:91], 0\n" \
    "v_mov_b64 v[92:93], 0\n" \
    "v_mov_b64 v[94:95], 0\n" \
    "v_mov_b64 v[192:193], 0\n" \
    "v_mov_b64 v[194:195], 0\n" \
    "v_mov_b32 v222, v236\n" \
    "ds_read_b64_tr_b16 v[104:105], v228 offset:16384\n" \
    "ds_read_b64_tr_b16 v[106:107], v228 offset:16416\n" \
    "ds_read_b64_tr_b16 v[108:109], v228 offset:18688\n" \
    "ds_read_b64_tr_b16 v[110:111], v228 offset:18720\n" \
    "ds_read_b64_tr_b16 v[112:113], v228 offset:20992\n" \
    "ds_read_b64_tr_b16 v[114:115], v228 offset:21024\n" \
    "ds_read_b64_tr_b16 v[116:117], v228 offset:23296\n" \
    "ds_read_b64_tr_b16 v[118:119], v228 offset:23328\n" \
    "ds_read_b64_tr_b16 v[120:121], v228 offset:16656\n" \
    "ds_read_b64_tr_b16 v[122:123], v228 offset:16688\n" \
    "ds_read_b64_tr_b16 v[124:125], v228 offset:18960\n" \
    "ds_read_b64_tr_b16 v[126:127], v228 offset:18992\n" \
    "ds_read_b64_tr_b16 v[128:129], v228 offset:21264\n" \
    "ds_read_b64_tr_b16 v[130:131], v228 offset:21296\n" \
    "ds_read_b64_tr_b16 v[132:133], v228 offset:23568\n" \
    "ds_read_b64_tr_b16 v[134:135], v228 offset:23600\n" \
    "s_waitcnt lgkmcnt(14)\n" \
    "v_mfma_f32_16x16x32_bf16 v[136:139], v[104:107], v[0:3], 0\n" \
    "v_mfma_f32_16x16x32_bf16 v[144:147], v[104:107], v[8:11], 0\n" \
    "v_mfma_f32_16x16x32_bf16 v[152:155], v[104:107], v[16:19], 0\n" \
    "v_mfma_f32_16x16x32_bf16 v[160:163], v[104:107], v[24:27], 0\n" \
    "s_waitcnt lgkmcnt(12)\n" \
    "v_mfma_f32_16x16x32_bf16 v[136:139], v[108:111], v[4:7], v[136:139]\n" \
    "v_mfma_f32_16x16x32_bf16 v[144:147], v[108:111], v[12:15], v[144:147]\n" \
    "v_mfma_f32_16x16x32_bf16 v[152:155], v[108:111], v[20:23], v[152:155]\n" \
    "v_mfma_f32_16x16x32_bf16 v[160:163], v[108:111], v[28:31], v[160:163]\n" \
    "s_waitcnt lgkmcnt(10)\n" \
    "v_mfma_f32_16x16x32_bf16 v[136:139], v[112:115], v[32:35], v[136:139]\n" \
    "v_mfma_f32_16x16x32_bf16 v[144:147], v[112:115], v[40:43], v[144:147]\n" \
    "v_mfma_f32_16x16x32_bf16 v[152:155], v[112:115], v[48:51], v[152:155]\n" \
    "v_mfma_f32_16x16x32_bf16 v[160:163], v[112:115], v[56:59], v[160:163]\n" \
    "s_waitcnt lgkmcnt(8)\n" \
    "v_mfma_f32_16x16x32_bf16 v[136:139], v[116:119], v[36:39], v[136:139]\n" \
    "v_mfma_f32_16x16x32_bf16 v[144:147], v[116:119], v[44:47], v[144:147]\n" \
    "v_mfma_f32_16x16x32_bf16 v[152:155], v[116:119], v[52:55], v[152:155]\n" \
    "v_mfma_f32_16x16x32_bf16 v[160:163], v[116:119], v[60:63], v[160:163]\n" \
    "s_waitcnt lgkmcnt(6)\n" \
    "v_mfma_f32_16x16x32_bf16 v[140:143], v[120:123], v[0:3], 0\n" \
    "v_mfma_f32_16x16x32_bf16 v[148:151], v[120:123], v[8:11], 0\n" \
    "v_mfma_f32_16x16x32_bf16 v[156:159], v[120:123], v[16:19], 0\n" \
    "v_mfma_f32_16x16x32_bf16 v[164:167], v[120:123], v[24:27], 0\n" \
    "s_waitcnt lgkmcnt(4)\n" \
    "v_mfma_f32_16x16x32_bf16 v[140:143], v[124:127], v[4:7], v[140:143]\n" \
    "v_mfma_f32_16x16x32_bf16 v[148:151], v[124:127], v[12:15], v[148:151]\n" \
    "v_mfma_f32_16x16x32_bf16 v[156:159], v[124:127], v[20:23], v[156:159]\n" \
    "v_mfma_f32_16x16x32_bf16 v[164:167], v[124:127], v[28:31], v[164:167]\n" \
    "s_waitcnt lgkmcnt(2)\n" \
    "v_mfma_f32_16x16x32_bf16 v[140:143], v[128:131], v[32:35], v[140:143]\n" \
    "v_mfma_f32_16x16x32_bf16 v[148:151], v[128:131], v[40:43], v[148:151]\n" \
    "v_mfma_f32_16x16x32_bf16 v[156:159], v[128:131], v[48:51], v[156:159]\n" \
    "v_mfma_f32_16x16x32_bf16 v[164:167], v[128:131], v[56:59], v[164:167]\n" \
    "s_waitcnt lgkmcnt(0)\n" \
    "v_mfma_f32_16x16x32_bf16 v[140:143], v[132:135], v[36:39], v[140:143]\n" \
    "v_mfma_f32_16x16x32_bf16 v[148:151], v[132:135], v[44:47], v[148:151]\n" \
    "v_mfma_f32_16x16x32_bf16 v[156:159], v[132:135], v[52:55], v[156:159]\n" \
    "v_mfma_f32_16x16x32_bf16 v[164:167], v[132:135], v[60:63], v[164:167]\n" \
    "v_cvt_pk_bf16_f32 v65, v136, v137\n" \
    "v_cvt_pk_bf16_f32 v68, v138, v139\n" \
    "v_cvt_pk_bf16_f32 v73, v144, v145\n" \
    "v_cvt_pk_bf16_f32 v76, v146, v147\n" \
    "v_cvt_pk_bf16_f32 v81, v152, v153\n" \
    "v_cvt_pk_bf16_f32 v84, v154, v155\n" \
    "v_cvt_pk_bf16_f32 v89, v160, v161\n" \
    "v_cvt_pk_bf16_f32 v92, v162, v163\n" \
    "v_cvt_pk_bf16_f32 v66, v142, v143\n" \
    "v_cvt_pk_bf16_f32 v67, v142, v143\n" \
    "v_cvt_pk_bf16_f32 v69, v140, v141\n" \
    "v_cvt_pk_bf16_f32 v74, v150, v151\n" \
    "v_cvt_pk_bf16_f32 v75, v150, v151\n" \
    "v_cvt_pk_bf16_f32 v77, v148, v149\n" \
    "v_cvt_pk_bf16_f32 v82, v158, v159\n" \
    "v_cvt_pk_bf16_f32 v83, v158, v159\n" \
    "v_cvt_pk_bf16_f32 v85, v156, v157\n" \
    "v_cvt_pk_bf16_f32 v90, v166, v167\n" \
    "v_cvt_pk_bf16_f32 v91, v166, v167\n" \
    "v_cvt_pk_bf16_f32 v93, v164, v165\n" \
    "v_permlane16_swap_b32 v66, v67\n" \
    "v_permlane16_swap_b32 v74, v75\n" \
    "v_permlane16_swap_b32 v82, v83\n" \
    "v_permlane16_swap_b32 v90, v91\n" \
    "L%=_Rloop:\n" \
    "v_mov_b64 v[176:177], 0\n" \
    "v_mov_b64 v[178:179], 0\n" \
    "v_mov_b64 v[180:181], 0\n" \
    "v_mov_b64 v[182:183], 0\n" \
    "v_mov_b64 v[184:185], 0\n" \
    "v_mov_b64 v[186:187], 0\n" \
    "v_mov_b64 v[188:189], 0\n" \
    "v_mov_b64 v[190:191], 0\n" \
    "v_mov_b64 v[196:197], 0\n" \
    "v_mov_b64 v[198:199], 0\n" \
    "v_mov_b64 v[200:201], 0\n" \
    "v_mov_b64 v[202:203], 0\n" \
    "v_mov_b64 v[204:205], 0\n" \
    "v_mov_b64 v[206:207], 0\n" \
    "v_mov_b64 v[208:209], 0\n" \
    "v_mov_b64 v[210:211], 0\n" \
    "v_mov_b64 v[212:213], 0\n" \
    "v_mov_b64 v[214:215], 0\n" \
    "s_add_u32 s85, s60, 0x1000\n" \
    "s_branch L%=_Ru2b\n" \
    "L%=_Ru2a:\n" \
    "s_sleep 4\n" \
    "L%=_Ru2b:\n" \
    "s_memrealtime vcc\n" \
    "s_add_u32 s80, s77, s64\n" \
    "s_add_u32 s80, s80, s69\n" \
    "s_waitcnt lgkmcnt(0)\n" \
    "s_sub_i32 s79, s80, vcc_lo\n" \
    "s_sub_u32 s79, s79, 40\n" \
    "s_cmp_lt_u32 s79, 0x400\n" \
    "s_cbranch_scc1 L%=_Ru2a\n" \
    "s_memtime s[38:39]\n" \
    "s_memrealtime vcc\n" \
    "s_nop 4\n" \
    "s_memrealtime s[32:33]\n" \
    "s_nop 4\n" \
    "s_memrealtime s[34:35]\n" \
    "s_nop 4\n" \
    "s_memrealtime s[36:37]\n" \
    "s_add_u32 s80, s77, s64\n" \
    "s_lshl_b32 s80, s80, 2\n" \
    "s_waitcnt lgkmcnt(0)\n" \
    "s_sub_u32 s80, s80, vcc_lo\n" \
    "s_sub_u32 s80, s80, s32\n" \
    "s_sub_u32 s80, s80, s34\n" \
    "s_sub_u32 s80, s80, s36\n" \
    "s_mul_i32 s80, s80, s71\n" \
    "s_ashr_i32 s80, s80, 6\n" \
    "s_add_u32 s78, s80, s38\n" \
    "L%=_Ru3a:\n" \
    "s_memtime s[38:39]\n" \
    "s_mul_i32 s80, s69, 23\n" \
    "s_add_u32 s80, s80, s78\n" \
    "s_waitcnt lgkmcnt(0)\n" \
    "s_sub_i32 s79, s80, s38\n" \
    "s_max_i32 s79, s79, 0\n" \
    "s_cmp_gt_u32 s79, 320\n" \
    "s_cbranch_scc1 L%=_Ru3s\n" \
    ".p2align 4\n" \
    "L%=_Ru3d:\n" \
    "s_sub_u32 s79, s79, 28\n" \
    "s_cbranch_scc0 L%=_Ru3d\n" \
    "L%=_Ru3e:\n" \
    "s_sub_u32 s79, s76, 1\n" \
    "s_cmp_lt_u32 s79, 63\n" \
    "s_cselect_b32 s79, s84, 0x40020000\n" \
    "s_sub_u32 s79, s79, 0x20000\n" \
    "buffer_store_dwordx4 v[236:239], v234, s[48:51], s79 offen sc1\n" \
    "s_setprio 3\n" \
    "s_barrier\n" \
    "s_nop 14\n" \
    "L%=_Rvs:\n" \
    "ds_read_b128 v[104:107], v229 offset:16\n" \
    "ds_read_b128 v[108:111], v230 offset:8704\n" \
    "ds_read_b128 v[112:115], v230 offset:9984\n" \
    "ds_read_b128 v[116:119], v230 offset:11264\n" \
    "ds_read_b128 v[120:123], v230 offset:12544\n" \
    "s_waitcnt lgkmcnt(4)\n" \
    "v_min3_u32 v104, v104, v105, v106\n" \
    "v_min_u32 v104, v104, v107\n" \
    "s_nop 0\n" \
    "v_readfirstlane_b32 s79, v104\n" \
    "s_cmp_le_u32 s79, s76\n" \
    "s_cbranch_scc1 L%=_Rvs\n" \
    "s_waitcnt lgkmcnt(2)\n" \
    "v_pk_add_f32 v[108:109], v[108:109], v[112:113]\n" \
    "v_pk_add_f32 v[110:111], v[110:111], v[114:115]\n" \
    "s_waitcnt lgkmcnt(0)\n" \
    "v_pk_add_f32 v[116:117], v[116:117], v[120:121]\n" \
    "v_pk_add_f32 v[118:119], v[118:119], v[122:123]\n" \
    "v_pk_add_f32 v[108:109], v[108:109], v[116:117]\n" \
    "v_pk_add_f32 v[110:111], v[110:111], v[118:119]\n" \
    "v_cvt_pk_bf16_f32 v124, v108, v109\n" \
    "v_cvt_pk_bf16_f32 v125, v110, v111\n" \
    "s_nop 0\n" \
    "v_mov_b32_dpp v126, v124 quad_perm:[1,0,3,2] row_mask:0xf bank_mask:0xf\n" \
    "v_mov_b32_dpp v127, v125 quad_perm:[1,0,3,2] row_mask:0xf bank_mask:0xf\n" \
    "buffer_store_dwordx4 v[124:127], v231, s[40:43], s60 offen sc1\n" \
    "ds_write_b64 v232, v[124:125]\n" \
    "ds_read_b64_tr_b16 v[236:237], v233\n" \
    "ds_read_b64_tr_b16 v[238:239], v233 offset:256\n" \
    "s_cmp_lt_u32 s76, 63\n" \
    "s_cbranch_scc1 L%=_Rne\n" \
    "s_waitcnt lgkmcnt(0)\n" \
    "buffer_store_dwordx4 v[236:239], v234, s[48:51], s84 offen sc1\n" \
    "L%=_Rne:\n" \
    "s_cmp_eq_u32 s76, 63\n" \
    "s_cbranch_scc1 L%=_Rfin\n" \
    "ds_read_b32 v216, v229\n" \
    "s_waitcnt lgkmcnt(0)\n" \
    "v_readfirstlane_b32 s94, v216\n" \
    "s_mul_i32 s79, s94, s70\n" \
    "s_add_u32 s79, s79, s81\n" \
    "s_cmp_lg_u32 s79, s77\n" \
    "s_cbranch_scc1 L%=_Ru4s\n" \
    "L%=_Ru4k:\n" \
    "L%=_Ru5a:\n" \
    "s_memtime s[38:39]\n" \
    "s_mul_hi_u32 s80, s67, s96\n" \
    "s_add_u32 s80, s80, s78\n" \
    "s_waitcnt lgkmcnt(0)\n" \
    "s_sub_i32 s79, s80, s38\n" \
    "s_max_i32 s79, s79, 0\n" \
    "s_cmp_gt_u32 s79, 320\n" \
    "s_cbranch_scc1 L%=_Ru5s\n" \
    ".p2align 4\n" \
    "L%=_Ru5d:\n" \
    "s_sub_u32 s79, s79, 28\n" \
    "s_cbranch_scc0 L%=_Ru5d\n" \
    "L%=_Ru5e:\n" \
    "buffer_load_dwordx4 v[168:171], v225, s[40:43], s60 offen sc1\n" \
    "buffer_load_dwordx4 v[172:175], v225, s[40:43], s85 offen sc1\n" \
    "s_memtime s[38:39]\n" \
    "s_memrealtime vcc\n" \
    "s_nop 4\n" \
    "s_memrealtime s[32:33]\n" \
    "s_nop 4\n" \
    "s_memrealtime s[34:35]\n" \
    "s_nop 4\n" \
    "s_memrealtime s[36:37]\n" \
    "s_add_u32 s80, s77, s64\n" \
    "s_lshl_b32 s80, s80, 2\n" \
    "s_waitcnt lgkmcnt(0)\n" \
    "s_sub_u32 s80, s80, vcc_lo\n" \
    "s_sub_u32 s80, s80, s32\n" \
    "s_sub_u32 s80, s80, s34\n" \
    "s_sub_u32 s80, s80, s36\n" \
    "s_mul_i32 s80, s80, s71\n" \
    "s_ashr_i32 s80, s80, 6\n" \
    "s_add_u32 s78, s80, s38\n" \
    "L%=_Rvc:\n" \
    "s_waitcnt vmcnt(1)\n" \
    "v_smfmac_f32_16x16x64_bf16 v[176:179], v[168:171], v[0:7], v224\n" \
    "v_smfmac_f32_16x16x64_bf16 v[180:183], v[168:171], v[8:15], v224\n" \
    "s_waitcnt vmcnt(0)\n" \
    "v_smfmac_f32_16x16x64_bf16 v[176:179], v[172:175], v[32:39], v224\n" \
    "v_smfmac_f32_16x16x64_bf16 v[180:183], v[172:175], v[40:47], v224\n" \
    "v_smfmac_f32_16x16x64_bf16 v[184:187], v[168:171], v[16:23], v224\n" \
    "v_smfmac_f32_16x16x64_bf16 v[188:191], v[168:171], v[24:31], v224\n" \
    "v_add_f32 v176, v176, v178\n" \
    "v_smfmac_f32_16x16x64_bf16 v[184:187], v[172:175], v[48:55], v224\n" \
    "v_add_f32 v177, v177, v179\n" \
    "v_smfmac_f32_16x16x64_bf16 v[188:191], v[172:175], v[56:63], v224\n" \
    "v_cmp_u_f32 vcc, v176, v177\n" \
    "v_cvt_pk_bf16_f32 v64, v176, v177\n" \
    "v_add_f32 v180, v180, v182\n" \
    "v_add_f32 v181, v181, v183\n" \
    "v_cvt_pk_bf16_f32 v72, v180, v181\n" \
    "s_cbranch_vccnz L%=_Rvslow\n" \
    "L%=_Rvok:\n" \
    "v_add_f32 v184, v184, v186\n" \
    "v_add_f32 v185, v185, v187\n" \
    "v_cvt_pk_bf16_f32 v80, v184, v185\n" \
    "v_add_f32 v188, v188, v190\n" \
    "v_add_f32 v189, v189, v191\n" \
    "v_cvt_pk_bf16_f32 v88, v188, v189\n" \
    "s_barrier\n" \
    "L%=_Rfr:\n" \
    "ds_read_b128 v[192:195], v227\n" \
    "s_waitcnt lgkmcnt(0)\n" \
    "v_smfmac_f32_16x16x64_bf16 v[212:215], v[192:195], v[96:103], v224\n" \
    "v_cmp_ne_u32 vcc, s76, v195\n" \
    "v_smfmac_f32_16x16x64_bf16 v[196:199], v[192:195], v[64:71], v224\n" \
    "s_cbranch_vccnz L%=_Rfs\n" \
    "v_smfmac_f32_16x16x64_bf16 v[200:203], v[192:195], v[72:79], v224\n" \
    "v_smfmac_f32_16x16x64_bf16 v[204:207], v[192:195], v[80:87], v224\n" \
    "v_add_f32 v212, v212, v214\n" \
    "v_smfmac_f32_16x16x64_bf16 v[208:211], v[192:195], v[88:95], v224\n" \
    "v_add_f32 v213, v213, v215\n" \
    "v_rcp_f32 v220, v212\n" \
    "v_rcp_f32 v221, v213\n" \
    "v_add_f32 v196, v196, v198\n" \
    "v_add_f32 v197, v197, v199\n" \
    "v_pk_add_f32 v[200:201], v[200:201], v[202:203]\n" \
    "v_pk_add_f32 v[204:205], v[204:205], v[206:207]\n" \
    "s_nop 0\n" \
    "v_pk_add_f32 v[208:209], v[208:209], v[210:211]\n" \
    "v_pk_mul_f32 v[196:197], v[196:197], v[220:221]\n" \
    "v_pk_mul_f32 v[200:201], v[200:201], v[220:221]\n" \
    "v_pk_mul_f32 v[204:205], v[204:205], v[220:221]\n" \
    "v_pk_mul_f32 v[208:209], v[208:209], v[220:221]\n" \
    "v_cvt_pk_bf16_f32 v216, v196, v200\n" \
    "v_cvt_pk_bf16_f32 v217, v204, v208\n" \
    "v_cvt_pk_bf16_f32 v218, v197, v201\n" \
    "v_cvt_pk_bf16_f32 v219, v205, v209\n" \
    "buffer_store_dwordx4 v[216:219], v226, s[40:43], s60 offen sc1\n" \
    "ds_read_b64_tr_b16 v[104:105], v228 offset:16384\n" \
    "ds_read_b64_tr_b16 v[106:107], v228 offset:16416\n" \
    "ds_read_b64_tr_b16 v[108:109], v228 offset:18688\n" \
    "ds_read_b64_tr_b16 v[110:111], v228 offset:18720\n" \
    "ds_read_b64_tr_b16 v[112:113], v228 offset:20992\n" \
    "ds_read_b64_tr_b16 v[114:115], v228 offset:21024\n" \
    "ds_read_b64_tr_b16 v[116:117], v228 offset:23296\n" \
    "ds_read_b64_tr_b16 v[118:119], v228 offset:23328\n" \
    "ds_read_b64_tr_b16 v[120:121], v228 offset:16656\n" \
    "ds_read_b64_tr_b16 v[122:123], v228 offset:16688\n" \
    "ds_read_b64_tr_b16 v[124:125], v228 offset:18960\n" \
    "ds_read_b64_tr_b16 v[126:127], v228 offset:18992\n" \
    "ds_read_b64_tr_b16 v[128:129], v228 offset:21264\n" \
    "ds_read_b64_tr_b16 v[130:131], v228 offset:21296\n" \
    "ds_read_b64_tr_b16 v[132:133], v228 offset:23568\n" \
    "ds_read_b64_tr_b16 v[134:135], v228 offset:23600\n" \
    "s_waitcnt lgkmcnt(14)\n" \
    "v_mfma_f32_16x16x32_bf16 v[136:139], v[104:107], v[0:3], 0\n" \
    "v_mfma_f32_16x16x32_bf16 v[144:147], v[104:107], v[8:11], 0\n" \
    "v_mfma_f32_16x16x32_bf16 v[152:155], v[104:107], v[16:19], 0\n" \
    "v_mfma_f32_16x16x32_bf16 v[160:163], v[104:107], v[24:27], 0\n" \
    "s_waitcnt lgkmcnt(12)\n" \
    "v_mfma_f32_16x16x32_bf16 v[136:139], v[108:111], v[4:7], v[136:139]\n" \
    "v_mfma_f32_16x16x32_bf16 v[144:147], v[108:111], v[12:15], v[144:147]\n" \
    "v_mfma_f32_16x16x32_bf16 v[152:155], v[108:111], v[20:23], v[152:155]\n" \
    "v_mfma_f32_16x16x32_bf16 v[160:163], v[108:111], v[28:31], v[160:163]\n" \
    "s_waitcnt lgkmcnt(10)\n" \
    "v_mfma_f32_16x16x32_bf16 v[136:139], v[112:115], v[32:35], v[136:139]\n" \
    "v_mfma_f32_16x16x32_bf16 v[144:147], v[112:115], v[40:43], v[144:147]\n" \
    "v_mfma_f32_16x16x32_bf16 v[152:155], v[112:115], v[48:51], v[152:155]\n" \
    "v_mfma_f32_16x16x32_bf16 v[160:163], v[112:115], v[56:59], v[160:163]\n" \
    "s_waitcnt lgkmcnt(8)\n" \
    "v_mfma_f32_16x16x32_bf16 v[136:139], v[116:119], v[36:39], v[136:139]\n" \
    "v_mfma_f32_16x16x32_bf16 v[144:147], v[116:119], v[44:47], v[144:147]\n" \
    "v_mfma_f32_16x16x32_bf16 v[152:155], v[116:119], v[52:55], v[152:155]\n" \
    "v_mfma_f32_16x16x32_bf16 v[160:163], v[116:119], v[60:63], v[160:163]\n" \
    "L%=_Ru6a:\n" \
    "s_memtime s[38:39]\n" \
    "s_mul_hi_u32 s80, s68, s96\n" \
    "s_add_u32 s80, s80, s78\n" \
    "s_waitcnt lgkmcnt(0)\n" \
    "s_sub_i32 s79, s80, s38\n" \
    "s_cmp_lt_i32 s79, -2000\n" \
    "s_cbranch_scc1 L%=_Rmiss\n" \
    "s_max_i32 s79, s79, 0\n" \
    "s_cmp_gt_u32 s79, 320\n" \
    "s_cbranch_scc1 L%=_Ru6s\n" \
    ".p2align 4\n" \
    "L%=_Ru6d:\n" \
    "s_sub_u32 s79, s79, 28\n" \
    "s_cbranch_scc0 L%=_Ru6d\n" \
    "L%=_Ru6e:\n" \
    "buffer_load_dwordx4 v[176:179], v235, s[40:43], s60 offen offset:0 sc1\n" \
    "buffer_load_dwordx4 v[180:183], v235, s[40:43], s60 offen offset:1024 sc1\n" \
    "buffer_load_dwordx4 v[184:187], v235, s[40:43], s60 offen offset:2048 sc1\n" \
    "buffer_load_dwordx4 v[188:191], v235, s[40:43], s60 offen offset:3072 sc1\n" \
    "v_mfma_f32_16x16x32_bf16 v[140:143], v[120:123], v[0:3], 0\n" \
    "s_add_u32 s77, s77, s65\n" \
    "s_add_u32 s81, s81, s65\n" \
    "v_mov_b32 v220, s81\n" \
    "v_mfma_f32_16x16x32_bf16 v[148:151], v[120:123], v[8:11], 0\n" \
    "v_mov_b32 v221, s71\n" \
    "ds_write_b64 v229, v[220:221] offset:80\n" \
    "s_memtime s[38:39]\n" \
    "v_mfma_f32_16x16x32_bf16 v[156:159], v[120:123], v[16:19], 0\n" \
    "s_memrealtime vcc\n" \
    "v_cvt_pk_bf16_f32 v65, v136, v137\n" \
    "v_cvt_pk_bf16_f32 v68, v138, v139\n" \
    "v_mfma_f32_16x16x32_bf16 v[164:167], v[120:123], v[24:27], 0\n" \
    "v_cvt_pk_bf16_f32 v73, v144, v145\n" \
    "v_cvt_pk_bf16_f32 v76, v146, v147\n" \
    "v_cvt_pk_bf16_f32 v81, v152, v153\n" \
    "v_mfma_f32_16x16x32_bf16 v[140:143], v[124:127], v[4:7], v[140:143]\n" \
    "s_memrealtime s[32:33]\n" \
    "v_cvt_pk_bf16_f32 v84, v154, v155\n" \
    "v_cvt_pk_bf16_f32 v89, v160, v161\n" \
    "v_mfma_f32_16x16x32_bf16 v[148:151], v[124:127], v[12:15], v[148:151]\n" \
    "s_memrealtime s[34:35]\n" \
    "v_cvt_pk_bf16_f32 v92, v162, v163\n" \
    "s_add_u32 s80, s77, s64\n" \
    "v_mfma_f32_16x16x32_bf16 v[156:159], v[124:127], v[20:23], v[156:159]\n" \
    "s_lshl_b32 s80, s80, 2\n" \
    "s_and_b32 s95, s94, 1\n" \
    "s_lshl_b32 s95, s95, 17\n" \
    "v_mfma_f32_16x16x32_bf16 v[164:167], v[124:127], v[28:31], v[164:167]\n" \
    "s_memrealtime s[36:37]\n" \
    "s_add_u32 s95, s95, s61\n" \
    "s_add_u32 s85, s95, 0x1000\n" \
    "v_mfma_f32_16x16x32_bf16 v[140:143], v[128:131], v[32:35], v[140:143]\n" \
    "s_xor_b32 s85, s95, 0x20000\n" \
    "s_and_b32 s79, s94, 1\n" \
    "s_lshl1_add_u32 s79, s76, s79\n" \
    "v_mfma_f32_16x16x32_bf16 v[148:151], v[128:131], v[40:43], v[148:151]\n" \
    "s_add_u32 s79, s79, 2\n" \
    "s_lshl_b32 s79, s79, 17\n" \
    "v_mov_b32 v223, s79\n" \
    "v_mfma_f32_16x16x32_bf16 v[156:159], v[128:131], v[48:51], v[156:159]\n" \
    "s_waitcnt lgkmcnt(0)\n" \
    "s_sub_u32 s80, s80, vcc_lo\n" \
    "s_sub_u32 s80, s80, s32\n" \
    "v_mfma_f32_16x16x32_bf16 v[164:167], v[128:131], v[56:59], v[164:167]\n" \
    "s_sub_u32 s80, s80, s34\n" \
    "s_sub_u32 s80, s80, s36\n" \
    "s_mul_i32 s80, s80, s71\n" \
    "v_mfma_f32_16x16x32_bf16 v[140:143], v[132:135], v[36:39], v[140:143]\n" \
    "s_ashr_i32 s80, s80, 6\n" \
    "s_add_u32 s78, s80, s38\n" \
    "s_add_u32 s72, s78, -183\n" \
    "v_mfma_f32_16x16x32_bf16 v[148:151], v[132:135], v[44:47], v[148:151]\n" \
    "s_sub_u32 s79, s72, s38\n" \
    "s_cmp_lt_i32 s79, 0\n" \
    "v_mfma_f32_16x16x32_bf16 v[156:159], v[132:135], v[52:55], v[156:159]\n" \
    "v_mfma_f32_16x16x32_bf16 v[164:167], v[132:135], v[60:63], v[164:167]\n" \
    "s_cbranch_scc1 L%=_Rpast\n" \
    "ds_write_b32 v229, v223 offset:76\n" \
    "s_waitcnt vmcnt(3)\n" \
    "v_mfma_f32_16x16x32_bf16 v[240:243], v[244:247], v[176:179], 0\n" \
    "s_waitcnt vmcnt(2)\n" \
    "v_mfma_f32_16x16x32_bf16 v[240:243], v[244:247], v[180:183], v[240:243]\n" \
    "s_waitcnt vmcnt(1)\n" \
    "v_mfma_f32_16x16x32_bf16 v[240:243], v[244:247], v[184:187], v[240:243]\n" \
    "s_memtime s[38:39]\n" \
    "s_waitcnt vmcnt(0)\n" \
    "v_mfma_f32_16x16x32_bf16 v[240:243], v[244:247], v[188:191], v[240:243]\n" \
    "s_nop 4\n" \
    "s_waitcnt lgkmcnt(0)\n" \
    "s_cmp_lt_u32 s72, s38\n" \
    "s_cselect_b32 s95, s85, s95\n" \
    "v_cmp_u_f32 vcc, v240, v240\n" \
    "v_add_f32_dpp v240, v240, v240 row_ror:8 row_mask:0xf bank_mask:0xf\n" \
    "v_add_f32_dpp v241, v241, v241 row_ror:8 row_mask:0xf bank_mask:0xf\n" \
    "v_add_f32_dpp v242, v242, v242 row_ror:8 row_mask:0xf bank_mask:0xf\n" \
    "v_add_f32_dpp v243, v243, v243 row_ror:8 row_mask:0xf bank_mask:0xf\n" \
    "v_cvt_pk_bf16_f32 v216, v240, v241\n" \
    "v_cvt_pk_bf16_f32 v217, v242, v243\n" \
    "s_cbranch_vccnz L%=_Rslow\n" \
    "v_mov_b32_dpp v218, v216 quad_perm:[1,0,3,2] row_mask:0xf bank_mask:0xf\n" \
    "v_mov_b32_dpp v219, v217 quad_perm:[1,0,3,2] row_mask:0xf bank_mask:0xf\n" \
    "buffer_store_dwordx4 v[216:219], v222, s[40:43], s95 offen sc1\n" \
    "s_cbranch_scc1 L%=_Rlate\n" \
    "s_wakeup\n" \
    "buffer_store_dwordx4 v[216:219], v222, s[40:43], s85 offen sc1\n" \
    "L%=_Rcall:\n" \
    "L%=_Ru7a:\n" \
    "s_memtime s[38:39]\n" \
    "s_mul_i32 s80, -3, 23\n" \
    "s_add_u32 s80, s80, s78\n" \
    "s_waitcnt lgkmcnt(0)\n" \
    "s_sub_i32 s79, s80, s38\n" \
    "s_cmp_lt_i32 s79, -100\n" \
    "s_cbranch_scc1 L%=_Rover\n" \
    "s_max_i32 s79, s79, 0\n" \
    "s_cmp_gt_u32 s79, 320\n" \
    "s_cbranch_scc1 L%=_Ru7s\n" \
    ".p2align 4\n" \
    "L%=_Ru7d:\n" \
    "s_sub_u32 s79, s79, 28\n" \
    "s_cbranch_scc0 L%=_Ru7d\n" \
    "L%=_Ru7e:\n" \
    "s_wakeup\n" \
    "L%=_Rdone:\n" \
    "s_setprio 0\n" \
    "v_cvt_pk_bf16_f32 v66, v142, v143\n" \
    "v_cvt_pk_bf16_f32 v67, v142, v143\n" \
    "v_cvt_pk_bf16_f32 v69, v140, v141\n" \
    "v_cvt_pk_bf16_f32 v74, v150, v151\n" \
    "v_cvt_pk_bf16_f32 v75, v150, v151\n" \
    "v_cvt_pk_bf16_f32 v77, v148, v149\n" \
    "v_cvt_pk_bf16_f32 v82, v158, v159\n" \
    "v_cvt_pk_bf16_f32 v83, v158, v159\n" \
    "v_cvt_pk_bf16_f32 v85, v156, v157\n" \
    "v_cvt_pk_bf16_f32 v90, v166, v167\n" \
    "v_cvt_pk_bf16_f32 v91, v166, v167\n" \
    "v_cvt_pk_bf16_f32 v93, v164, v165\n" \
    "v_permlane16_swap_b32 v66, v67\n" \
    "v_permlane16_swap_b32 v74, v75\n" \
    "v_permlane16_swap_b32 v82, v83\n" \
    "v_permlane16_swap_b32 v90, v91\n" \
    "ds_read_b64 v[220:221], v229 offset:64\n" \
    "s_mov_b32 s79, s60\n" \
    "s_mov_b32 s60, s61\n" \
    "s_mov_b32 s61, s62\n" \
    "s_mov_b32 s62, s63\n" \
    "s_mov_b32 s63, s79\n" \
    "s_add_u32 s76, s76, 1\n" \
    "s_add_u32 s84, s84, 0x20000\n" \
    "s_waitcnt lgkmcnt(0)\n" \
    "v_readfirstlane_b32 s65, v220\n" \
    "v_readfirstlane_b32 s71, v221\n" \
    "s_mul_i32 s96, s65, s71\n" \
    "s_lshr_b32 s96, s96, 4\n" \
    "s_branch L%=_Rloop\n" \
    "L%=_Rslow:\n" \
    "s_nop 7\n" \
    "s_nop 0\n" \
    "v_max_u32 v220, v176, v177\n" \
    "v_max3_u32 v220, v220, v178, v179\n" \
    "v_max3_u32 v220, v220, v180, v181\n" \
    "v_max3_u32 v220, v220, v182, v183\n" \
    "v_max3_u32 v220, v220, v184, v185\n" \
    "v_max3_u32 v220, v220, v186, v187\n" \
    "v_max3_u32 v220, v220, v188, v189\n" \
    "v_max3_u32 v220, v220, v190, v191\n" \
    "v_cmp_eq_u32 vcc, -1, v220\n" \
    "s_cbranch_vccz L%=_Rok\n" \
    "s_cmp_eq_u32 s99, 0\n" \
    "s_cbranch_scc1 L%=_Rok\n" \
    "s_sub_u32 s99, s99, 1\n" \
    "s_cmp_lg_u32 s99, 0\n" \
    "s_cbranch_scc1 L%=_Ru8\n" \
    "v_mov_b32 v220, 1\n" \
    "buffer_store_dword v220, off, s[44:47], 0 sc1\n" \
    "L%=_Ru8:\n" \
    "ds_write_b32 v229, v102 offset:76\n" \
    "L%=_Rgrid:\n" \
    "s_mul_hi_u32 s73, s68, s65\n" \
    "s_sub_u32 s73, s73, s65\n" \
    "s_add_u32 s73, s73, -18\n" \
    "s_sub_u32 s74, 8, s73\n" \
    "s_memrealtime vcc\n" \
    "s_waitcnt lgkmcnt(0)\n" \
    "s_sub_u32 s80, vcc_lo, s64\n" \
    "s_add_u32 s80, s80, s74\n" \
    "s_branch L%=_Ru9h\n" \
    "L%=_Ru9g:\n" \
    "s_add_u32 s77, s77, s70\n" \
    "s_add_u32 s94, s94, 1\n" \
    "L%=_Ru9h:\n" \
    "s_sub_u32 s79, s80, s77\n" \
    "s_cmp_lt_u32 s79, 0x1000000\n" \
    "s_cbranch_scc1 L%=_Ru9g\n" \
    "v_mov_b32 v220, s94\n" \
    "ds_max_u32 v229, v220\n" \
    "s_cmp_lt_i32 s79, -991\n" \
    "s_cbranch_scc1 L%=_Rnow\n" \
    "s_branch L%=_Ru10b\n" \
    "L%=_Ru10a:\n" \
    "s_sleep 4\n" \
    "L%=_Ru10b:\n" \
    "s_memrealtime vcc\n" \
    "s_add_u32 s80, s77, s64\n" \
    "s_add_u32 s80, s80, s73\n" \
    "s_waitcnt lgkmcnt(0)\n" \
    "s_sub_i32 s79, s80, vcc_lo\n" \
    "s_sub_u32 s79, s79, 40\n" \
    "s_cmp_lt_u32 s79, 0x400\n" \
    "s_cbranch_scc1 L%=_Ru10a\n" \
    "s_memtime s[38:39]\n" \
    "s_memrealtime vcc\n" \
    "s_nop 4\n" \
    "s_memrealtime s[32:33]\n" \
    "s_nop 4\n" \
    "s_memrealtime s[34:35]\n" \
    "s_nop 4\n" \
    "s_memrealtime s[36:37]\n" \
    "s_add_u32 s80, s77, s64\n" \
    "s_lshl_b32 s80, s80, 2\n" \
    "s_waitcnt lgkmcnt(0)\n" \
    "s_sub_u32 s80, s80, vcc_lo\n" \
    "s_sub_u32 s80, s80, s32\n" \
    "s_sub_u32 s80, s80, s34\n" \
    "s_sub_u32 s80, s80, s36\n" \
    "s_mul_i32 s80, s80, s71\n" \
    "s_ashr_i32 s80, s80, 6\n" \
    "s_add_u32 s78, s80, s38\n" \
    "L%=_Ru11a:\n" \
    "s_memtime s[38:39]\n" \
    "s_mul_i32 s80, s73, s71\n" \
    "s_ashr_i32 s80, s80, 4\n" \
    "s_add_u32 s80, s80, s78\n" \
    "s_waitcnt lgkmcnt(0)\n" \
    "s_sub_i32 s79, s80, s38\n" \
    "s_max_i32 s79, s79, 0\n" \
    "s_cmp_gt_u32 s79, 320\n" \
    "s_cbranch_scc1 L%=_Ru11s\n" \
    ".p2align 4\n" \
    "L%=_Ru11d:\n" \
    "s_sub_u32 s79, s79, 28\n" \
    "s_cbranch_scc0 L%=_Ru11d\n" \
    "L%=_Ru11e:\n" \
    "L%=_Rnow:\n" \
    "buffer_load_dwordx4 v[176:179], v235, s[40:43], s60 offen offset:0 sc1\n" \
    "buffer_load_dwordx4 v[180:183], v235, s[40:43], s60 offen offset:1024 sc1\n" \
    "buffer_load_dwordx4 v[184:187], v235, s[40:43], s60 offen offset:2048 sc1\n" \
    "buffer_load_dwordx4 v[188:191], v235, s[40:43], s60 offen offset:3072 sc1\n" \
    "L%=_Rarm:\n" \
    "s_memtime s[38:39]\n" \
    "s_memrealtime vcc\n" \
    "s_nop 4\n" \
    "s_memrealtime s[32:33]\n" \
    "s_nop 4\n" \
    "s_memrealtime s[34:35]\n" \
    "s_nop 4\n" \
    "s_memrealtime s[36:37]\n" \
    "s_add_u32 s80, s77, s64\n" \
    "s_lshl_b32 s80, s80, 2\n" \
    "s_waitcnt lgkmcnt(0)\n" \
    "s_sub_u32 s80, s80, vcc_lo\n" \
    "s_sub_u32 s80, s80, s32\n" \
    "s_sub_u32 s80, s80, s34\n" \
    "s_sub_u32 s80, s80, s36\n" \
    "s_mul_i32 s80, s80, s71\n" \
    "s_ashr_i32 s80, s80, 6\n" \
    "s_add_u32 s78, s80, s38\n" \
    "s_and_b32 s95, s94, 1\n" \
    "s_lshl_b32 s95, s95, 17\n" \
    "s_add_u32 s95, s95, s61\n" \
    "s_add_u32 s85, s95, 0x1000\n" \
    "s_xor_b32 s85, s95, 0x20000\n" \
    "s_and_b32 s79, s94, 1\n" \
    "s_lshl1_add_u32 s79, s76, s79\n" \
    "s_add_u32 s79, s79, 2\n" \
    "s_lshl_b32 s79, s79, 17\n" \
    "v_mov_b32 v223, s79\n" \
    "s_add_u32 s72, s78, -183\n" \
    "s_sub_u32 s79, s72, s38\n" \
    "s_cmp_lt_i32 s79, 0\n" \
    "s_cbranch_scc1 L%=_Rpast\n" \
    "s_waitcnt vmcnt(3)\n" \
    "v_mfma_f32_16x16x32_bf16 v[240:243], v[244:247], v[176:179], 0\n" \
    "s_waitcnt vmcnt(2)\n" \
    "v_mfma_f32_16x16x32_bf16 v[240:243], v[244:247], v[180:183], v[240:243]\n" \
    "s_waitcnt vmcnt(1)\n" \
    "v_mfma_f32_16x16x32_bf16 v[240:243], v[244:247], v[184:187], v[240:243]\n" \
    "s_waitcnt vmcnt(0)\n" \
    "v_mfma_f32_16x16x32_bf16 v[240:243], v[244:247], v[188:191], v[240:243]\n" \
    "s_memtime s[38:39]\n" \
    "s_nop 3\n" \
    "s_waitcnt lgkmcnt(0)\n" \
    "s_cmp_lt_u32 s72, s38\n" \
    "s_cselect_b32 s95, s85, s95\n" \
    "v_cmp_u_f32 vcc, v240, v240\n" \
    "v_add_f32_dpp v240, v240, v240 row_ror:8 row_mask:0xf bank_mask:0xf\n" \
    "v_add_f32_dpp v241, v241, v241 row_ror:8 row_mask:0xf bank_mask:0xf\n" \
    "v_add_f32_dpp v242, v242, v242 row_ror:8 row_mask:0xf bank_mask:0xf\n" \
    "v_add_f32_dpp v243, v243, v243 row_ror:8 row_mask:0xf bank_mask:0xf\n" \
    "v_cvt_pk_bf16_f32 v216, v240, v241\n" \
    "v_cvt_pk_bf16_f32 v217, v242, v243\n" \
    "s_cbranch_vccnz L%=_Rslow\n" \
    "L%=_Rok:\n" \
    "ds_write_b32 v229, v223 offset:76\n" \
    "v_mov_b32_dpp v218, v216 quad_perm:[1,0,3,2] row_mask:0xf bank_mask:0xf\n" \
    "v_mov_b32_dpp v219, v217 quad_perm:[1,0,3,2] row_mask:0xf bank_mask:0xf\n" \
    "buffer_store_dwordx4 v[216:219], v222, s[40:43], s95 offen sc1\n" \
    "s_cbranch_scc1 L%=_Rlate\n" \
    "buffer_store_dwordx4 v[216:219], v222, s[40:43], s85 offen sc1\n" \
    "s_waitcnt lgkmcnt(0)\n" \
    "s_wakeup\n" \
    "s_branch L%=_Rcall\n" \
    "L%=_Rpast:\n" \
    "s_memrealtime vcc\n" \
    "s_waitcnt lgkmcnt(0)\n" \
    "s_sub_u32 s80, vcc_lo, s64\n" \
    "s_add_u32 s80, s80, 12\n" \
    "s_branch L%=_Ru12h\n" \
    "L%=_Ru12g:\n" \
    "s_add_u32 s77, s77, s70\n" \
    "s_add_u32 s94, s94, 1\n" \
    "L%=_Ru12h:\n" \
    "s_sub_u32 s79, s80, s77\n" \
    "s_cmp_lt_u32 s79, 0x1000000\n" \
    "s_cbranch_scc1 L%=_Ru12g\n" \
    "v_mov_b32 v220, s94\n" \
    "ds_max_u32 v229, v220\n" \
    "s_branch L%=_Rarm\n" \
    "L%=_Rmiss:\n" \
    "s_add_u32 s77, s77, s65\n" \
    "s_add_u32 s81, s81, s65\n" \
    "v_mov_b32 v220, s81\n" \
    "v_mov_b32 v221, s71\n" \
    "ds_write_b64 v229, v[220:221] offset:80\n" \
    "v_mfma_f32_16x16x32_bf16 v[140:143], v[120:123], v[0:3], 0\n" \
    "v_mfma_f32_16x16x32_bf16 v[148:151], v[120:123], v[8:11], 0\n" \
    "v_mfma_f32_16x16x32_bf16 v[156:159], v[120:123], v[16:19], 0\n" \
    "v_mfma_f32_16x16x32_bf16 v[164:167], v[120:123], v[24:27], 0\n" \
    "v_mfma_f32_16x16x32_bf16 v[140:143], v[124:127], v[4:7], v[140:143]\n" \
    "v_mfma_f32_16x16x32_bf16 v[148:151], v[124:127], v[12:15], v[148:151]\n" \
    "v_mfma_f32_16x16x32_bf16 v[156:159], v[124:127], v[20:23], v[156:159]\n" \
    "v_mfma_f32_16x16x32_bf16 v[164:167], v[124:127], v[28:31], v[164:167]\n" \
    "v_mfma_f32_16x16x32_bf16 v[140:143], v[128:131], v[32:35], v[140:143]\n" \
    "v_mfma_f32_16x16x32_bf16 v[148:151], v[128:131], v[40:43], v[148:151]\n" \
    "v_mfma_f32_16x16x32_bf16 v[156:159], v[128:131], v[48:51], v[156:159]\n" \
    "v_mfma_f32_16x16x32_bf16 v[164:167], v[128:131], v[56:59], v[164:167]\n" \
    "v_mfma_f32_16x16x32_bf16 v[140:143], v[132:135], v[36:39], v[140:143]\n" \
    "v_mfma_f32_16x16x32_bf16 v[148:151], v[132:135], v[44:47], v[148:151]\n" \
    "v_mfma_f32_16x16x32_bf16 v[156:159], v[132:135], v[52:55], v[156:159]\n" \
    "v_mfma_f32_16x16x32_bf16 v[164:167], v[132:135], v[60:63], v[164:167]\n" \
    "v_cvt_pk_bf16_f32 v65, v136, v137\n" \
    "v_cvt_pk_bf16_f32 v68, v138, v139\n" \
    "v_cvt_pk_bf16_f32 v73, v144, v145\n" \
    "v_cvt_pk_bf16_f32 v76, v146, v147\n" \
    "v_cvt_pk_bf16_f32 v81, v152, v153\n" \
    "v_cvt_pk_bf16_f32 v84, v154, v155\n" \
    "v_cvt_pk_bf16_f32 v89, v160, v161\n" \
    "v_cvt_pk_bf16_f32 v92, v162, v163\n" \
    "s_branch L%=_Rgrid\n" \
    "L%=_Rover:\n" \
    "s_memrealtime vcc\n" \
    "s_waitcnt lgkmcnt(0)\n" \
    "s_sub_u32 s80, vcc_lo, s64\n" \
    "s_add_u32 s80, s80, 12\n" \
    "s_branch L%=_Ru13h\n" \
    "L%=_Ru13g:\n" \
    "s_add_u32 s77, s77, s70\n" \
    "s_add_u32 s94, s94, 1\n" \
    "L%=_Ru13h:\n" \
    "s_sub_u32 s79, s80, s77\n" \
    "s_cmp_lt_u32 s79, 0x1000000\n" \
    "s_cbranch_scc1 L%=_Ru13g\n" \
    "v_mov_b32 v220, s94\n" \
    "ds_max_u32 v229, v220\n" \
    "s_branch L%=_Ru14b\n" \
    "L%=_Ru14a:\n" \
    "s_sleep 4\n" \
    "L%=_Ru14b:\n" \
    "s_memrealtime vcc\n" \
    "s_add_u32 s80, s77, s64\n" \
    "s_add_u32 s80, s80, -3\n" \
    "s_waitcnt lgkmcnt(0)\n" \
    "s_sub_i32 s79, s80, vcc_lo\n" \
    "s_sub_u32 s79, s79, 40\n" \
    "s_cmp_lt_u32 s79, 0x400\n" \
    "s_cbranch_scc1 L%=_Ru14a\n" \
    "s_memtime s[38:39]\n" \
    "s_memrealtime vcc\n" \
    "s_nop 4\n" \
    "s_memrealtime s[32:33]\n" \
    "s_nop 4\n" \
    "s_memrealtime s[34:35]\n" \
    "s_nop 4\n" \
    "s_memrealtime s[36:37]\n" \
    "s_add_u32 s80, s77, s64\n" \
    "s_lshl_b32 s80, s80, 2\n" \
    "s_waitcnt lgkmcnt(0)\n" \
    "s_sub_u32 s80, s80, vcc_lo\n" \
    "s_sub_u32 s80, s80, s32\n" \
    "s_sub_u32 s80, s80, s34\n" \
    "s_sub_u32 s80, s80, s36\n" \
    "s_mul_i32 s80, s80, s71\n" \
    "s_ashr_i32 s80, s80, 6\n" \
    "s_add_u32 s78, s80, s38\n" \
    "s_branch L%=_Rcall\n" \
    "L%=_Rlate:\n" \
    "ds_write_b32 v229, v102 offset:76\n" \
    "s_memrealtime vcc\n" \
    "s_waitcnt lgkmcnt(0)\n" \
    "s_sub_u32 s80, vcc_lo, s64\n" \
    "s_add_u32 s80, s80, 12\n" \
    "s_branch L%=_Ru15h\n" \
    "L%=_Ru15g:\n" \
    "s_add_u32 s77, s77, s70\n" \
    "s_add_u32 s94, s94, 1\n" \
    "L%=_Ru15h:\n" \
    "s_sub_u32 s79, s80, s77\n" \
    "s_cmp_lt_u32 s79, 0x1000000\n" \
    "s_cbranch_scc1 L%=_Ru15g\n" \
    "v_mov_b32 v220, s94\n" \
    "ds_max_u32 v229, v220\n" \
    "s_branch L%=_Ru16b\n" \
    "L%=_Ru16a:\n" \
    "s_sleep 4\n" \
    "L%=_Ru16b:\n" \
    "s_memrealtime vcc\n" \
    "s_add_u32 s80, s77, s64\n" \
    "s_add_u32 s80, s80, -3\n" \
    "s_waitcnt lgkmcnt(0)\n" \
    "s_sub_i32 s79, s80, vcc_lo\n" \
    "s_sub_u32 s79, s79, 40\n" \
    "s_cmp_lt_u32 s79, 0x400\n" \
    "s_cbranch_scc1 L%=_Ru16a\n" \
    "s_memtime s[38:39]\n" \
    "s_memrealtime vcc\n" \
    "s_nop 4\n" \
    "s_memrealtime s[32:33]\n" \
    "s_nop 4\n" \
    "s_memrealtime s[34:35]\n" \
    "s_nop 4\n" \
    "s_memrealtime s[36:37]\n" \
    "s_add_u32 s80, s77, s64\n" \
    "s_lshl_b32 s80, s80, 2\n" \
    "s_waitcnt lgkmcnt(0)\n" \
    "s_sub_u32 s80, s80, vcc_lo\n" \
    "s_sub_u32 s80, s80, s32\n" \
    "s_sub_u32 s80, s80, s34\n" \
    "s_sub_u32 s80, s80, s36\n" \
    "s_mul_i32 s80, s80, s71\n" \
    "s_ashr_i32 s80, s80, 6\n" \
    "s_add_u32 s78, s80, s38\n" \
    "s_and_b32 s79, s94, 1\n" \
    "s_lshl1_add_u32 s79, s76, s79\n" \
    "s_add_u32 s79, s79, 2\n" \
    "s_lshl_b32 s79, s79, 17\n" \
    "v_mov_b32 v223, s79\n" \
    "ds_write_b32 v229, v223 offset:76\n" \
    "s_waitcnt lgkmcnt(0)\n" \
    "s_wakeup\n" \
    "L%=_Ru17a:\n" \
    "s_memtime s[38:39]\n" \
    "s_mul_i32 s80, -3, 23\n" \
    "s_add_u32 s80, s80, s78\n" \
    "s_waitcnt lgkmcnt(0)\n" \
    "s_sub_i32 s79, s80, s38\n" \
    "s_max_i32 s79, s79, 0\n" \
    "s_cmp_gt_u32 s79, 320\n" \
    "s_cbranch_scc1 L%=_Ru17s\n" \
    ".p2align 4\n" \
    "L%=_Ru17d:\n" \
    "s_sub_u32 s79, s79, 28\n" \
    "s_cbranch_scc0 L%=_Ru17d\n" \
    "L%=_Ru17e:\n" \
    "s_wakeup\n" \
    "s_xor_b32 s85, s95, 0x20000\n" \
    "buffer_store_dwordx4 v[216:219], v222, s[40:43], s85 offen sc1\n" \
    "s_branch L%=_Rdone\n" \
    "L%=_Rfin:\n" \
    "s_waitcnt vmcnt(0)\n" \
    "s_barrier\n" \
    "s_branch L%=_end\n" \
    "L%=_Ru3s:\n" \
    "s_cmp_gt_u32 s79, 0x8000\n" \
    "s_cbranch_scc1 L%=_Ru3e\n" \
    "s_sleep 2\n" \
    "s_branch L%=_Ru3a\n" \
    "L%=_Ru4s:\n" \
    "s_mov_b32 s77, s79\n" \
    "s_memtime s[38:39]\n" \
    "s_memrealtime vcc\n" \
    "s_nop 4\n" \
    "s_memrealtime s[32:33]\n" \
    "s_nop 4\n" \
    "s_memrealtime s[34:35]\n" \
    "s_nop 4\n" \
    "s_memrealtime s[36:37]\n" \
    "s_add_u32 s80, s77, s64\n" \
    "s_lshl_b32 s80, s80, 2\n" \
    "s_waitcnt lgkmcnt(0)\n" \
    "s_sub_u32 s80, s80, vcc_lo\n" \
    "s_sub_u32 s80, s80, s32\n" \
    "s_sub_u32 s80, s80, s34\n" \
    "s_sub_u32 s80, s80, s36\n" \
    "s_mul_i32 s80, s80, s71\n" \
    "s_ashr_i32 s80, s80, 6\n" \
    "s_add_u32 s78, s80, s38\n" \
    "s_branch L%=_Ru4k\n" \
    "L%=_Ru5s:\n" \
    "s_cmp_gt_u32 s79, 0x8000\n" \
    "s_cbranch_scc1 L%=_Ru5e\n" \
    "s_sleep 2\n" \
    "s_branch L%=_Ru5a\n" \
    "L%=_Rvslow:\n" \
    "s_nop 7\n" \
    "s_nop 0\n" \
    "v_max_u32 v216, v168, v169\n" \
    "v_max3_u32 v216, v216, v170, v171\n" \
    "v_max3_u32 v216, v216, v172, v173\n" \
    "v_max3_u32 v216, v216, v174, v175\n" \
    "v_cmp_eq_u32 vcc, -1, v216\n" \
    "s_cbranch_vccz L%=_Rvok\n" \
    "s_cmp_eq_u32 s99, 0\n" \
    "s_cbranch_scc1 L%=_Rvok\n" \
    "s_sub_u32 s99, s99, 1\n" \
    "s_cmp_lg_u32 s99, 0\n" \
    "s_cbranch_scc1 L%=_Ru18\n" \
    "v_mov_b32 v216, 1\n" \
    "buffer_store_dword v216, off, s[44:47], 0 sc1\n" \
    "L%=_Ru18:\n" \
    "s_sleep 1\n" \
    "buffer_load_dwordx4 v[168:171], v225, s[40:43], s60 offen sc1\n" \
    "buffer_load_dwordx4 v[172:175], v225, s[40:43], s85 offen sc1\n" \
    "v_mov_b64 v[176:177], 0\n" \
    "v_mov_b64 v[178:179], 0\n" \
    "v_mov_b64 v[180:181], 0\n" \
    "v_mov_b64 v[182:183], 0\n" \
    "v_mov_b64 v[184:185], 0\n" \
    "v_mov_b64 v[186:187], 0\n" \
    "v_mov_b64 v[188:189], 0\n" \
    "v_mov_b64 v[190:191], 0\n" \
    "s_waitcnt vmcnt(0)\n" \
    "s_branch L%=_Rvc\n" \
    "L%=_Rfs:\n" \
    "s_nop 7\n" \
    "s_nop 0\n" \
    "v_mov_b64 v[196:197], 0\n" \
    "v_mov_b64 v[198:199], 0\n" \
    "v_mov_b64 v[200:201], 0\n" \
    "v_mov_b64 v[202:203], 0\n" \
    "v_mov_b64 v[204:205], 0\n" \
    "v_mov_b64 v[206:207], 0\n" \
    "v_mov_b64 v[208:209], 0\n" \
    "v_mov_b64 v[210:211], 0\n" \
    "v_mov_b64 v[212:213], 0\n" \
    "v_mov_b64 v[214:215], 0\n" \
    "s_branch L%=_Rfr\n" \
    "L%=_Ru6s:\n" \
    "s_cmp_gt_u32 s79, 0x8000\n" \
    "s_cbranch_scc1 L%=_Ru6e\n" \
    "s_sleep 2\n" \
    "s_branch L%=_Ru6a\n" \
    "L%=_Ru7s:\n" \
    "s_cmp_gt_u32 s79, 0x8000\n" \
    "s_cbranch_scc1 L%=_Ru7e\n" \
    "s_sleep 2\n" \
    "s_branch L%=_Ru7a\n" \
    "L%=_Ru11s:\n" \
    "s_cmp_gt_u32 s79, 0x8000\n" \
    "s_cbranch_scc1 L%=_Ru11e\n" \
    "s_sleep 2\n" \
    "s_branch L%=_Ru11a\n" \
    "L%=_Ru17s:\n" \
    "s_cmp_gt_u32 s79, 0x8000\n" \
    "s_cbranch_scc1 L%=_Ru17e\n" \
    "s_sleep 2\n" \
    "s_branch L%=_Ru17a\n" \
    "L%=_P:\n" \
    "s_mov_b32 s76, 0\n" \
    "s_mov_b32 s99, 0x20000\n" \
    "s_mov_b32 s84, 0\n" \
    "s_mov_b32 s81, 0\n" \
    "s_mov_b32 s86, 0\n" \
    "s_mov_b32 s87, 0\n" \
    "s_mov_b32 s88, 0\n" \
    "s_mov_b32 s89, 0\n" \
    "s_mov_b32 s90, 0\n" \
    "s_mov_b32 s91, 0\n" \
    "s_mov_b32 s92, 0\n" \
    "s_mov_b32 s93, 0\n" \
    "s_mov_b32 s94, 0\n" \
    "s_mov_b32 s95, 0\n" \
    "s_mov_b32 s96, 0\n" \
    "s_mov_b32 s97, 0\n" \
    "s_mov_b32 s98, 0\n" \
    "ds_read_b32 v216, v229\n" \
    "s_waitcnt lgkmcnt(0)\n" \
    "v_readfirstlane_b32 s94, v216\n" \
    "s_mul_i32 s79, s94, s70\n" \
    "s_add_u32 s79, s79, s81\n" \
    "s_mov_b32 s77, s79\n" \
    "s_mul_i32 s96, s65, s71\n" \
    "s_lshr_b32 s96, s96, 4\n" \
    "v_mov_b32 v96, 0x3f803f80\n" \
    "v_mov_b32 v97, 0x3f803f80\n" \
    "v_mov_b32 v98, 0x3f803f80\n" \
    "v_mov_b32 v99, 0x3f803f80\n" \
    "v_mov_b32 v100, 0x3f803f80\n" \
    "v_mov_b32 v101, 0x3f803f80\n" \
    "v_mov_b64 v[102:103], 0\n" \
    "v_mov_b64 v[64:65], 0\n" \
    "v_mov_b64 v[66:67], 0\n" \
    "v_mov_b64 v[68:69], 0\n" \
    "v_mov_b64 v[70:71], 0\n" \
    "v_mov_b64 v[72:73], 0\n" \
    "v_mov_b64 v[74:75], 0\n" \
    "v_mov_b64 v[76:77], 0\n" \
    "v_mov_b64 v[78:79], 0\n" \
    "v_mov_b64 v[80:81], 0\n" \
    "v_mov_b64 v[82:83], 0\n" \
    "v_mov_b64 v[84:85], 0\n" \
    "v_mov_b64 v[86:87], 0\n" \
    "v_mov_b64 v[88:89], 0\n" \
    "v_mov_b64 v[90:91], 0\n" \
    "v_mov_b64 v[92:93], 0\n" \
    "v_mov_b64 v[94:95], 0\n" \
    "v_mov_b64 v[192:193], 0\n" \
    "v_mov_b64 v[194:195], 0\n" \
    "v_cvt_f32_u32 v240, s71\n" \
    "v_mov_b32 v241, 1.0\n" \
    "v_mov_b32 v242, 0\n" \
    "v_mov_b32 v243, s65\n" \
    "s_mov_b32 s74, s94\n" \
    "v_mov_b32 v244, -1\n" \
    "v_mov_b32 v245, -1\n" \
    "v_mov_b32 v246, -1\n" \
    "v_mov_b32 v247, -1\n" \
    "L%=_Ploop:\n" \
    "ds_read_b64_tr_b16 v[104:105], v228 offset:16384\n" \
    "ds_read_b64_tr_b16 v[106:107], v228 offset:16416\n" \
    "ds_read_b64_tr_b16 v[108:109], v228 offset:18688\n" \
    "ds_read_b64_tr_b16 v[110:111], v228 offset:18720\n" \
    "ds_read_b64_tr_b16 v[112:113], v228 offset:20992\n" \
    "ds_read_b64_tr_b16 v[114:115], v228 offset:21024\n" \
    "ds_read_b64_tr_b16 v[116:117], v228 offset:23296\n" \
    "ds_read_b64_tr_b16 v[118:119], v228 offset:23328\n" \
    "ds_read_b64_tr_b16 v[120:121], v228 offset:16656\n" \
    "ds_read_b64_tr_b16 v[122:123], v228 offset:16688\n" \
    "ds_read_b64_tr_b16 v[124:125], v228 offset:18960\n" \
    "ds_read_b64_tr_b16 v[126:127], v228 offset:18992\n" \
    "ds_read_b64_tr_b16 v[128:129], v228 offset:21264\n" \
    "ds_read_b64_tr_b16 v[130:131], v228 offset:21296\n" \
    "ds_read_b64_tr_b16 v[132:133], v228 offset:23568\n" \
    "ds_read_b64_tr_b16 v[134:135], v228 offset:23600\n" \
    "s_waitcnt lgkmcnt(14)\n" \
    "v_mfma_f32_16x16x32_bf16 v[136:139], v[104:107], v[0:3], 0\n" \
    "v_mfma_f32_16x16x32_bf16 v[144:147], v[104:107], v[8:11], 0\n" \
    "v_mfma_f32_16x16x32_bf16 v[152:155], v[104:107], v[16:19], 0\n" \
    "v_mfma_f32_16x16x32_bf16 v[160:163], v[104:107], v[24:27], 0\n" \
    "s_waitcnt lgkmcnt(12)\n" \
    "v_mfma_f32_16x16x32_bf16 v[136:139], v[108:111], v[4:7], v[136:139]\n" \
    "v_mfma_f32_16x16x32_bf16 v[144:147], v[108:111], v[12:15], v[144:147]\n" \
    "v_mfma_f32_16x16x32_bf16 v[152:155], v[108:111], v[20:23], v[152:155]\n" \
    "v_mfma_f32_16x16x32_bf16 v[160:163], v[108:111], v[28:31], v[160:163]\n" \
    "s_waitcnt lgkmcnt(10)\n" \
    "v_mfma_f32_16x16x32_bf16 v[136:139], v[112:115], v[32:35], v[136:139]\n" \
    "v_mfma_f32_16x16x32_bf16 v[144:147], v[112:115], v[40:43], v[144:147]\n" \
    "v_mfma_f32_16x16x32_bf16 v[152:155], v[112:115], v[48:51], v[152:155]\n" \
    "v_mfma_f32_16x16x32_bf16 v[160:163], v[112:115], v[56:59], v[160:163]\n" \
    "s_waitcnt lgkmcnt(8)\n" \
    "v_mfma_f32_16x16x32_bf16 v[136:139], v[116:119], v[36:39], v[136:139]\n" \
    "v_mfma_f32_16x16x32_bf16 v[144:147], v[116:119], v[44:47], v[144:147]\n" \
    "v_mfma_f32_16x16x32_bf16 v[152:155], v[116:119], v[52:55], v[152:155]\n" \
    "v_mfma_f32_16x16x32_bf16 v[160:163], v[116:119], v[60:63], v[160:163]\n" \
    "s_waitcnt lgkmcnt(6)\n" \
    "v_mfma_f32_16x16x32_bf16 v[140:143], v[120:123], v[0:3], 0\n" \
    "v_mfma_f32_16x16x32_bf16 v[148:151], v[120:123], v[8:11], 0\n" \
    "v_mfma_f32_16x16x32_bf16 v[156:159], v[120:123], v[16:19], 0\n" \
    "v_mfma_f32_16x16x32_bf16 v[164:167], v[120:123], v[24:27], 0\n" \
    "s_waitcnt lgkmcnt(4)\n" \
    "v_mfma_f32_16x16x32_bf16 v[140:143], v[124:127], v[4:7], v[140:143]\n" \
    "v_mfma_f32_16x16x32_bf16 v[148:151], v[124:127], v[12:15], v[148:151]\n" \
    "v_mfma_f32_16x16x32_bf16 v[156:159], v[124:127], v[20:23], v[156:159]\n" \
    "v_mfma_f32_16x16x32_bf16 v[164:167], v[124:127], v[28:31], v[164:167]\n" \
    "s_waitcnt lgkmcnt(2)\n" \
    "v_mfma_f32_16x16x32_bf16 v[140:143], v[128:131], v[32:35], v[140:143]\n" \
    "v_mfma_f32_16x16x32_bf16 v[148:151], v[128:131], v[40:43], v[148:151]\n" \
    "v_mfma_f32_16x16x32_bf16 v[156:159], v[128:131], v[48:51], v[156:159]\n" \
    "v_mfma_f32_16x16x32_bf16 v[164:167], v[128:131], v[56:59], v[164:167]\n" \
    "s_waitcnt lgkmcnt(0)\n" \
    "v_mfma_f32_16x16x32_bf16 v[140:143], v[132:135], v[36:39], v[140:143]\n" \
    "v_mfma_f32_16x16x32_bf16 v[148:151], v[132:135], v[44:47], v[148:151]\n" \
    "v_mfma_f32_16x16x32_bf16 v[156:159], v[132:135], v[52:55], v[156:159]\n" \
    "v_mfma_f32_16x16x32_bf16 v[164:167], v[132:135], v[60:63], v[164:167]\n" \
    "v_cvt_pk_bf16_f32 v65, v136, v137\n" \
    "v_cvt_pk_bf16_f32 v68, v138, v139\n" \
    "v_cvt_pk_bf16_f32 v73, v144, v145\n" \
    "v_cvt_pk_bf16_f32 v76, v146, v147\n" \
    "v_cvt_pk_bf16_f32 v81, v152, v153\n" \
    "v_cvt_pk_bf16_f32 v84, v154, v155\n" \
    "v_cvt_pk_bf16_f32 v89, v160, v161\n" \
    "v_cvt_pk_bf16_f32 v92, v162, v163\n" \
    "v_cvt_pk_bf16_f32 v66, v142, v143\n" \
    "v_cvt_pk_bf16_f32 v67, v142, v143\n" \
    "v_cvt_pk_bf16_f32 v69, v140, v141\n" \
    "v_cvt_pk_bf16_f32 v74, v150, v151\n" \
    "v_cvt_pk_bf16_f32 v75, v150, v151\n" \
    "v_cvt_pk_bf16_f32 v77, v148, v149\n" \
    "v_cvt_pk_bf16_f32 v82, v158, v159\n" \
    "v_cvt_pk_bf16_f32 v83, v158, v159\n" \
    "v_cvt_pk_bf16_f32 v85, v156, v157\n" \
    "v_cvt_pk_bf16_f32 v90, v166, v167\n" \
    "v_cvt_pk_bf16_f32 v91, v166, v167\n" \
    "v_cvt_pk_bf16_f32 v93, v164, v165\n" \
    "v_permlane16_swap_b32 v66, v67\n" \
    "v_permlane16_swap_b32 v74, v75\n" \
    "v_permlane16_swap_b32 v82, v83\n" \
    "v_permlane16_swap_b32 v90, v91\n" \
    "v_mov_b64 v[176:177], 0\n" \
    "v_mov_b64 v[178:179], 0\n" \
    "v_mov_b64 v[180:181], 0\n" \
    "v_mov_b64 v[182:183], 0\n" \
    "v_mov_b64 v[184:185], 0\n" \
    "v_mov_b64 v[186:187], 0\n" \
    "v_mov_b64 v[188:189], 0\n" \
    "v_mov_b64 v[190:191], 0\n" \
    "v_mov_b64 v[196:197], 0\n" \
    "v_mov_b64 v[198:199], 0\n" \
    "v_mov_b64 v[200:201], 0\n" \
    "v_mov_b64 v[202:203], 0\n" \
    "v_mov_b64 v[204:205], 0\n" \
    "v_mov_b64 v[206:207], 0\n" \
    "v_mov_b64 v[208:209], 0\n" \
    "v_mov_b64 v[210:211], 0\n" \
    "v_mov_b64 v[212:213], 0\n" \
    "v_mov_b64 v[214:215], 0\n" \
    "s_add_u32 s85, s60, 0x1000\n" \
    "s_add_u32 s82, s76, 1\n" \
    "s_min_u32 s82, s82, 63\n" \
    "s_lshl_b32 s83, s82, 19\n" \
    "s_add_u32 s83, s83, s72\n" \
    "s_lshl_b32 s79, s82, 11\n" \
    "s_and_b32 s79, s79, 0x2000\n" \
    "s_xor_b32 s83, s83, s79\n" \
    "v_cmp_ge_u32 vcc, s82, v237\n" \
    "s_cbranch_vccnz L%=_Pfslow\n" \
    "L%=_Pfok:\n" \
    "s_add_u32 s79, s82, 4\n" \
    "v_mov_b32 v150, 0x40000000\n" \
    "v_cmp_lt_u32 vcc, s79, v237\n" \
    "s_nop 1\n" \
    "v_cndmask_b32 v150, v235, v150, vcc\n" \
    "s_branch L%=_Pu2b\n" \
    "L%=_Pu2a:\n" \
    "s_sleep 4\n" \
    "L%=_Pu2b:\n" \
    "s_memrealtime vcc\n" \
    "s_add_u32 s80, s77, s64\n" \
    "s_add_u32 s80, s80, s69\n" \
    "s_waitcnt lgkmcnt(0)\n" \
    "s_sub_i32 s79, s80, vcc_lo\n" \
    "s_sub_u32 s79, s79, 40\n" \
    "s_cmp_lt_u32 s79, 0x400\n" \
    "s_cbranch_scc1 L%=_Pu2a\n" \
    "s_memtime s[38:39]\n" \
    "s_memrealtime vcc\n" \
    "s_nop 4\n" \
    "s_memrealtime s[32:33]\n" \
    "s_nop 4\n" \
    "s_memrealtime s[34:35]\n" \
    "s_nop 4\n" \
    "s_memrealtime s[36:37]\n" \
    "s_add_u32 s80, s77, s64\n" \
    "s_lshl_b32 s80, s80, 2\n" \
    "s_waitcnt lgkmcnt(0)\n" \
    "s_sub_u32 s80, s80, vcc_lo\n" \
    "s_sub_u32 s80, s80, s32\n" \
    "s_sub_u32 s80, s80, s34\n" \
    "s_sub_u32 s80, s80, s36\n" \
    "s_mul_i32 s80, s80, s71\n" \
    "s_ashr_i32 s80, s80, 6\n" \
    "s_add_u32 s78, s80, s38\n" \
    "L%=_Pu3a:\n" \
    "s_memtime s[38:39]\n" \
    "s_mul_i32 s80, s69, 23\n" \
    "s_add_u32 s80, s80, s78\n" \
    "s_waitcnt lgkmcnt(0)\n" \
    "s_sub_i32 s79, s80, s38\n" \
    "s_max_i32 s79, s79, 0\n" \
    "s_cmp_gt_u32 s79, 320\n" \
    "s_cbranch_scc1 L%=_Pu3s\n" \
    ".p2align 4\n" \
    "L%=_Pu3d:\n" \
    "s_sub_u32 s79, s79, 28\n" \
    "s_cbranch_scc0 L%=_Pu3d\n" \
    "L%=_Pu3e:\n" \
    "buffer_load_dwordx4 v[104:107], v230, s[56:59], s83 offen offset:0 sc1\n" \
    "buffer_load_dwordx4 v[108:111], v230, s[56:59], s83 offen offset:1024 sc1\n" \
    "buffer_load_dwordx4 v[112:115], v230, s[56:59], s83 offen offset:2048 sc1\n" \
    "buffer_load_dwordx4 v[116:119], v230, s[56:59], s83 offen offset:3072 sc1\n" \
    "s_add_u32 s79, s83, 0x1000\n" \
    "buffer_load_dwordx4 v[120:123], v230, s[56:59], s79 offen offset:0 sc1\n" \
    "buffer_load_dwordx4 v[124:127], v230, s[56:59], s79 offen offset:1024 sc1\n" \
    "buffer_load_dwordx4 v[128:131], v230, s[56:59], s79 offen offset:2048 sc1\n" \
    "buffer_load_dwordx4 v[132:135], v230, s[56:59], s79 offen offset:3072 sc1\n" \
    "s_xor_b32 s79, s83, s75\n" \
    "buffer_load_dwordx4 v[136:139], v230, s[52:55], s79 offen offset:0 sc1\n" \
    "buffer_load_dwordx4 v[140:143], v230, s[52:55], s79 offen offset:1024 sc1\n" \
    "buffer_load_dword v151, v150, s[44:47], 0 offen sc1\n" \
    "s_lshl_b32 s79, s76, 7\n" \
    "buffer_load_dwordx4 v[144:147], off, s[44:47], s79 offset:2176 sc1\n" \
    "buffer_store_dword v243, v239, s[44:47], s79 offen sc1\n" \
    "buffer_store_dwordx4 v[244:247], v232, s[40:43], s62 offen offset:0 sc1\n" \
    "buffer_store_dwordx4 v[244:247], v232, s[40:43], s62 offen offset:1024 sc1\n" \
    "buffer_store_dwordx4 v[244:247], v232, s[40:43], s62 offen offset:2048 sc1\n" \
    "buffer_store_dwordx4 v[244:247], v232, s[40:43], s62 offen offset:3072 sc1\n" \
    "buffer_store_dwordx4 v[244:247], v233, s[40:43], s62 offen offset:0 sc1\n" \
    "buffer_store_dwordx4 v[244:247], v233, s[40:43], s62 offen offset:1024 sc1\n" \
    "buffer_store_dwordx4 v[244:247], v234, s[40:43], s62 offen sc1\n" \
    "s_barrier\n" \
    "s_cmp_eq_u32 s76, 63\n" \
    "s_cbranch_scc1 L%=_Pfin\n" \
    "ds_read_b32 v216, v229\n" \
    "s_waitcnt lgkmcnt(0)\n" \
    "v_readfirstlane_b32 s94, v216\n" \
    "s_mul_i32 s79, s94, s70\n" \
    "s_add_u32 s79, s79, s81\n" \
    "s_cmp_lg_u32 s79, s77\n" \
    "s_cbranch_scc1 L%=_Pu4s\n" \
    "L%=_Pu4k:\n" \
    "s_sub_u32 s32, s78, s97\n" \
    "s_sub_u32 s33, s77, s98\n" \
    "s_mov_b32 s97, s78\n" \
    "s_mov_b32 s98, s77\n" \
    "s_lshl_b32 s32, s32, 4\n" \
    "s_cmp_eq_u32 s76, 0\n" \
    "s_cbranch_scc1 L%=_Pck\n" \
    "v_cvt_f32_u32 v148, s33\n" \
    "v_cvt_f32_u32 v149, s32\n" \
    "v_rcp_f32 v148, v148\n" \
    "s_cmp_lt_u32 s76, 4\n" \
    "s_cselect_b32 s34, 0, 0x3c449ba6\n" \
    "s_cmp_eq_u32 s94, s74\n" \
    "s_mov_b32 s74, s94\n" \
    "s_cselect_b32 s32, 0x7f000000, 0\n" \
    "s_cselect_b32 s33, 0.5, 1.0\n" \
    "s_cselect_b32 s34, 0, s34\n" \
    "s_cmp_eq_u32 s76, 1\n" \
    "s_cselect_b32 s32, 0x7f000000, s32\n" \
    "s_cselect_b32 s33, 1.0, s33\n" \
    "s_cselect_b32 s35, 0x3f7851ec, 1.0\n" \
    "v_mul_f32 v149, s35, v149\n" \
    "v_fma_f32 v148, v148, v149, -v240\n" \
    "v_max_f32 v149, 0.5, v241\n" \
    "v_min_f32 v148, s32, v148\n" \
    "v_cmp_gt_f32 vcc, 0, v148\n" \
    "v_mul_f32 v242, 0x3f7851ec, v242\n" \
    "v_add_f32 v242, s34, v242\n" \
    "v_cndmask_b32 v149, v241, v149, vcc\n" \
    "v_fma_f32 v240, v148, v149, v240\n" \
    "v_mul_f32 v241, s33, v241\n" \
    "v_max_f32 v240, 0x43400000, v240\n" \
    "v_max_f32 v241, 0x3e000000, v241\n" \
    "v_min_f32 v240, 0x43e00000, v240\n" \
    "v_fma_f32 v148, v242, s73, s73\n" \
    "v_rcp_f32 v149, v240\n" \
    "s_nop 0\n" \
    "v_fma_f32 v148, v148, v149, 0.5\n" \
    "v_cvt_u32_f32 v243, v148\n" \
    "L%=_Pck:\n" \
    "v_add_f32 v149, 0.5, v240\n" \
    "v_mov_b32 v148, s65\n" \
    "v_cvt_u32_f32 v149, v149\n" \
    "s_waitcnt vmcnt(8)\n" \
    "ds_write_b128 v231, v[104:107] offset:16384\n" \
    "ds_write_b128 v231, v[108:111] offset:17536\n" \
    "ds_write_b128 v231, v[112:115] offset:18688\n" \
    "ds_write_b128 v231, v[116:119] offset:19840\n" \
    "ds_write_b128 v231, v[120:123] offset:20992\n" \
    "ds_write_b128 v231, v[124:127] offset:22144\n" \
    "ds_write_b128 v231, v[128:131] offset:23296\n" \
    "ds_write_b128 v231, v[132:135] offset:24448\n" \
    "ds_write_b128 v230, v[136:139] offset:25600\n" \
    "ds_write_b128 v230, v[140:143] offset:26624\n" \
    "v_max3_u32 v144, v144, v145, v146\n" \
    "v_max_u32 v144, v144, v147\n" \
    "v_cmp_eq_u32 vcc, 0, v144\n" \
    "s_nop 1\n" \
    "v_cndmask_b32 v148, v144, v148, vcc\n" \
    "ds_write_b64 v229, v[148:149] offset:64\n" \
    "v_max_u32 v237, v237, v151\n" \
    "s_setprio 3\n" \
    "s_nop 0\n" \
    "v_min_u32_dpp v150, v237, v237 quad_perm:[1,0,3,2] row_mask:0xf bank_mask:0xf\n" \
    "ds_write_b32 v229, v150 offset:72\n" \
    "L%=_Pu5a:\n" \
    "s_memtime s[38:39]\n" \
    "s_mul_hi_u32 s80, s67, s96\n" \
    "s_add_u32 s80, s80, s78\n" \
    "s_waitcnt lgkmcnt(0)\n" \
    "s_sub_i32 s79, s80, s38\n" \
    "s_max_i32 s79, s79, 0\n" \
    "s_cmp_gt_u32 s79, 320\n" \
    "s_cbranch_scc1 L%=_Pu5s\n" \
    ".p2align 4\n" \
    "L%=_Pu5d:\n" \
    "s_sub_u32 s79, s79, 28\n" \
    "s_cbranch_scc0 L%=_Pu5d\n" \
    "L%=_Pu5e:\n" \
    "buffer_load_dwordx4 v[168:171], v225, s[40:43], s60 offen sc1\n" \
    "buffer_load_dwordx4 v[172:175], v225, s[40:43], s85 offen sc1\n" \
    "L%=_Pvc:\n" \
    "s_waitcnt vmcnt(1)\n" \
    "v_smfmac_f32_16x16x64_bf16 v[176:179], v[168:171], v[0:7], v224\n" \
    "v_smfmac_f32_16x16x64_bf16 v[180:183], v[168:171], v[8:15], v224\n" \
    "s_waitcnt vmcnt(0)\n" \
    "v_smfmac_f32_16x16x64_bf16 v[176:179], v[172:175], v[32:39], v224\n" \
    "v_smfmac_f32_16x16x64_bf16 v[180:183], v[172:175], v[40:47], v224\n" \
    "v_smfmac_f32_16x16x64_bf16 v[184:187], v[168:171], v[16:23], v224\n" \
    "v_smfmac_f32_16x16x64_bf16 v[188:191], v[168:171], v[24:31], v224\n" \
    "v_add_f32 v176, v176, v178\n" \
    "v_smfmac_f32_16x16x64_bf16 v[184:187], v[172:175], v[48:55], v224\n" \
    "v_add_f32 v177, v177, v179\n" \
    "v_smfmac_f32_16x16x64_bf16 v[188:191], v[172:175], v[56:63], v224\n" \
    "v_cmp_u_f32 vcc, v176, v177\n" \
    "v_cvt_pk_bf16_f32 v64, v176, v177\n" \
    "v_add_f32 v180, v180, v182\n" \
    "v_add_f32 v181, v181, v183\n" \
    "v_cvt_pk_bf16_f32 v72, v180, v181\n" \
    "s_cbranch_vccnz L%=_Pvslow\n" \
    "L%=_Pvok:\n" \
    "v_add_f32 v184, v184, v186\n" \
    "v_add_f32 v185, v185, v187\n" \
    "v_cvt_pk_bf16_f32 v80, v184, v185\n" \
    "v_add_f32 v188, v188, v190\n" \
    "v_add_f32 v189, v189, v191\n" \
    "v_cvt_pk_bf16_f32 v88, v188, v189\n" \
    "s_barrier\n" \
    "L%=_Pfr:\n" \
    "ds_read_b128 v[192:195], v227\n" \
    "s_waitcnt lgkmcnt(0)\n" \
    "v_smfmac_f32_16x16x64_bf16 v[212:215], v[192:195], v[96:103], v224\n" \
    "v_cmp_ne_u32 vcc, s76, v195\n" \
    "v_smfmac_f32_16x16x64_bf16 v[196:199], v[192:195], v[64:71], v224\n" \
    "s_cbranch_vccnz L%=_Pfs\n" \
    "v_smfmac_f32_16x16x64_bf16 v[200:203], v[192:195], v[72:79], v224\n" \
    "v_smfmac_f32_16x16x64_bf16 v[204:207], v[192:195], v[80:87], v224\n" \
    "v_add_f32 v212, v212, v214\n" \
    "v_smfmac_f32_16x16x64_bf16 v[208:211], v[192:195], v[88:95], v224\n" \
    "v_add_f32 v213, v213, v215\n" \
    "v_rcp_f32 v220, v212\n" \
    "v_rcp_f32 v221, v213\n" \
    "v_add_f32 v196, v196, v198\n" \
    "v_add_f32 v197, v197, v199\n" \
    "v_pk_add_f32 v[200:201], v[200:201], v[202:203]\n" \
    "v_pk_add_f32 v[204:205], v[204:205], v[206:207]\n" \
    "s_nop 0\n" \
    "v_pk_add_f32 v[208:209], v[208:209], v[210:211]\n" \
    "v_pk_mul_f32 v[196:197], v[196:197], v[220:221]\n" \
    "v_pk_mul_f32 v[200:201], v[200:201], v[220:221]\n" \
    "v_pk_mul_f32 v[204:205], v[204:205], v[220:221]\n" \
    "v_pk_mul_f32 v[208:209], v[208:209], v[220:221]\n" \
    "v_cvt_pk_bf16_f32 v216, v196, v200\n" \
    "v_cvt_pk_bf16_f32 v217, v204, v208\n" \
    "v_cvt_pk_bf16_f32 v218, v197, v201\n" \
    "v_cvt_pk_bf16_f32 v219, v205, v209\n" \
    "buffer_store_dwordx4 v[216:219], v226, s[40:43], s60 offen sc1\n" \
    "s_setprio 0\n" \
    "ds_read_b64 v[220:221], v229 offset:64\n" \
    "s_mov_b32 s79, s60\n" \
    "s_mov_b32 s60, s61\n" \
    "s_mov_b32 s61, s62\n" \
    "s_mov_b32 s62, s63\n" \
    "s_mov_b32 s63, s79\n" \
    "s_add_u32 s76, s76, 1\n" \
    "s_add_u32 s84, s84, 0x20000\n" \
    "s_add_u32 s77, s77, s65\n" \
    "s_add_u32 s81, s81, s65\n" \
    "s_waitcnt lgkmcnt(0)\n" \
    "v_readfirstlane_b32 s65, v220\n" \
    "v_readfirstlane_b32 s71, v221\n" \
    "s_mul_i32 s96, s65, s71\n" \
    "s_lshr_b32 s96, s96, 4\n" \
    "s_branch L%=_Ploop\n" \
    "L%=_Pfslow:\n" \
    "s_sleep 4\n" \
    "buffer_load_dword v237, v235, s[44:47], 0 offen sc1\n" \
    "s_waitcnt vmcnt(0)\n" \
    "v_cmp_ge_u32 vcc, s82, v237\n" \
    "s_cbranch_vccz L%=_Pfok\n" \
    "s_cmp_eq_u32 s99, 0\n" \
    "s_cbranch_scc1 L%=_Pfok\n" \
    "s_sub_u32 s99, s99, 1\n" \
    "s_branch L%=_Pfslow\n" \
    "L%=_Pfin:\n" \
    "s_waitcnt vmcnt(0)\n" \
    "s_barrier\n" \
    "s_branch L%=_end\n" \
    "L%=_Pu3s:\n" \
    "s_cmp_gt_u32 s79, 0x8000\n" \
    "s_cbranch_scc1 L%=_Pu3e\n" \
    "s_sleep 2\n" \
    "s_branch L%=_Pu3a\n" \
    "L%=_Pu4s:\n" \
    "s_mov_b32 s77, s79\n" \
    "s_memtime s[38:39]\n" \
    "s_memrealtime vcc\n" \
    "s_nop 4\n" \
    "s_memrealtime s[32:33]\n" \
    "s_nop 4\n" \
    "s_memrealtime s[34:35]\n" \
    "s_nop 4\n" \
    "s_memrealtime s[36:37]\n" \
    "s_add_u32 s80, s77, s64\n" \
    "s_lshl_b32 s80, s80, 2\n" \
    "s_waitcnt lgkmcnt(0)\n" \
    "s_sub_u32 s80, s80, vcc_lo\n" \
    "s_sub_u32 s80, s80, s32\n" \
    "s_sub_u32 s80, s80, s34\n" \
    "s_sub_u32 s80, s80, s36\n" \
    "s_mul_i32 s80, s80, s71\n" \
    "s_ashr_i32 s80, s80, 6\n" \
    "s_add_u32 s78, s80, s38\n" \
    "s_branch L%=_Pu4k\n" \
    "L%=_Pu5s:\n" \
    "s_cmp_gt_u32 s79, 0x8000\n" \
    "s_cbranch_scc1 L%=_Pu5e\n" \
    "s_sleep 2\n" \
    "s_branch L%=_Pu5a\n" \
    "L%=_Pvslow:\n" \
    "s_nop 7\n" \
    "s_nop 0\n" \
    "v_max_u32 v216, v168, v169\n" \
    "v_max3_u32 v216, v216, v170, v171\n" \
    "v_max3_u32 v216, v216, v172, v173\n" \
    "v_max3_u32 v216, v216, v174, v175\n" \
    "v_cmp_eq_u32 vcc, -1, v216\n" \
    "s_cbranch_vccz L%=_Pvok\n" \
    "s_cmp_eq_u32 s99, 0\n" \
    "s_cbranch_scc1 L%=_Pvok\n" \
    "s_sub_u32 s99, s99, 1\n" \
    "s_cmp_lg_u32 s99, 0\n" \
    "s_cbranch_scc1 L%=_Pu6\n" \
    "v_mov_b32 v216, 1\n" \
    "buffer_store_dword v216, off, s[44:47], 0 sc1\n" \
    "L%=_Pu6:\n" \
    "s_sleep 1\n" \
    "buffer_load_dwordx4 v[168:171], v225, s[40:43], s60 offen sc1\n" \
    "buffer_load_dwordx4 v[172:175], v225, s[40:43], s85 offen sc1\n" \
    "v_mov_b64 v[176:177], 0\n" \
    "v_mov_b64 v[178:179], 0\n" \
    "v_mov_b64 v[180:181], 0\n" \
    "v_mov_b64 v[182:183], 0\n" \
    "v_mov_b64 v[184:185], 0\n" \
    "v_mov_b64 v[186:187], 0\n" \
    "v_mov_b64 v[188:189], 0\n" \
    "v_mov_b64 v[190:191], 0\n" \
    "s_waitcnt vmcnt(0)\n" \
    "s_branch L%=_Pvc\n" \
    "L%=_Pfs:\n" \
    "s_nop 7\n" \
    "s_nop 0\n" \
    "v_mov_b64 v[196:197], 0\n" \
    "v_mov_b64 v[198:199], 0\n" \
    "v_mov_b64 v[200:201], 0\n" \
    "v_mov_b64 v[202:203], 0\n" \
    "v_mov_b64 v[204:205], 0\n" \
    "v_mov_b64 v[206:207], 0\n" \
    "v_mov_b64 v[208:209], 0\n" \
    "v_mov_b64 v[210:211], 0\n" \
    "v_mov_b64 v[212:213], 0\n" \
    "v_mov_b64 v[214:215], 0\n" \
    "s_branch L%=_Pfr\n" \
    "L%=_end:\n"
#define V8(b) "v" #b "0", "v" #b "1", "v" #b "2", "v" #b "3", "v" #b "4", "v" #b "5", "v" #b "6", "v" #b "7", "v" #b "8", "v" #b "9"
#define S_IO "+{s[40:55]}"(sA), "+{s[56:71]}"(sB), "+{s[72:75]}"(sC), "+{s78}"(lwo), "={s[92:99]}"(so)
#define FIN2() do {} while (0)
#define S_CLOB "memory", "vcc", "scc", "m0", "s32", "s33", "s34", "s35", "s36", "s37", "s38", "s39", "s76", "s77", "s79", "s80", "s81", "s82", "s83", "s84", "s85", "s86", "s87", "s88", "s89", "s90", "s91"
#define B_CLOB S_CLOB, "v64", "v65", "v66", "v67", "v68", "v69", V8(7), V8(8), V8(9), V8(10), V8(11), V8(12), V8(13), V8(14), V8(15), V8(16), V8(17), V8(18), V8(19), V8(20), V8(21), \
    "v220", "v221", "v222", "v223", "v240", "v241", "v242", "v243"
#define FIN() do {} while (0)

#define MK_S() \
    a = args(); \
    v16u sA = cat8(cat4(mkrs(a->ring, NSLOT * SLOT), mkrs(a->ctl, CTL_SZ)), cat4(mkrs(a->mb, NSEG * NH * 4096), mkrs(a->qkv + QSZ, QSZ))); \
    v16u sB = cat8(cat4(mkrs(a->qkv + 2 * (size_t)QSZ, QSZ), v4u{0, 0, 0, 0}), \
                   v8u{0, (unsigned)a->per0, (unsigned)a->d1, (unsigned)a->dv, (unsigned)a->d2, (unsigned)a->dp, (unsigned)a->slack, (unsigned)a->thr}); \
    const v4u rsc = __builtin_shufflevector(sA, sA, 4, 5, 6, 7)
#define HALVES() do { const unsigned u_ = beacon(rsc, b0, b1, 16); \
_Pragma("unroll") \
    for (int k = 0; k < NSLOT; ++k) sB[4 + k] = k * (unsigned)SLOT + ((u_ >> k) & 1) * 8192u; } while (0)

#define START() __hip_atomic_fetch_max((LAS unsigned*)(lds + L_MISC), (rt32() - spare) / sB[14] + 1, __ATOMIC_RELAXED, __HIP_MEMORY_SCOPE_WORKGROUP)

DEV void chain(LAS char* lds, const int c, const int w) {
    KArgs a = args();
    const int h = c >> 2, j = c & 3;
    const unsigned lb = (unsigned)(size_t)lds, lbw = lb + L_STG + w * 16384;
#define LANE() const int l = lane(), l15 = l & 15, g = l >> 4, hb = (l >> 1) & 1, q4 = l15 >> 2; const unsigned idx = hb ? 0xEEEE : 0x4444
    v4u sC, stv; v2u stw; unsigned stx; v8u so;
    unsigned lwo = w & 3, b0, b1;
    if (w < 4) {
        LANE();
        v16u wt[12], m;
            const v4u rw[3] = {mkrs(a->Wk, DM * HD * 2), mkrs(a->Wv, DM * HD * 2), mkrs(a->Wq, DM * HD * 2)};
            const unsigned vo = (l >> 2) * (HD * 2) + (l & 3) * 16, sw = (256 * w * HD + h * DH + 32 * j) * 2;
            auto issue = [&](int p) {
#pragma unroll
                for (int i = 0; i < 8; ++i) DMA16N(rw[p >> 1], vo, sw + ((p & 1) * 128 + i * 16) * (HD * 2), lbw + (p & 1) * 8192 + i * 1024); };
            {
                const v4u rp = w & 1 ? rw[2] : rw[1];
                const unsigned ps = ((rp[0] >> 7) + (c >> 5) * 16) & 63, po = w < 2 ? ((3 - ps) & 7) * 128 + (l & 1) * 1024 : (63 - ps < 16 && !(l & 1) ? (63 - ps) * 128 : OOB);
                if (a->pf & 1) asm volatile("s_mov_b32 m0, %0\n\ts_nop 0\n\tbuffer_load_dword %1, %2, 0 offen lds" :: "s"(lb + L_PF + w * 256), "v"(((c & 31) * 32 + (l >> 1)) * (HD * 2) + (c >> 5) * 2048 + po), "s"(rp) : "memory", "m0");
            }
            unsigned mr[16];
#pragma unroll
            for (int k = 0; k < 16; ++k) mr[k] = *(const unsigned*)(a->mem0 + (2 * q4 + (l & 1)) * DM + 256 * w + 16 * k + 4 * g + 2 * hb);
            SCHED();
            issue(0); issue(1);
            MK_S();
            unsigned spare = a->spare; asm volatile("" : "+s"(sA), "+s"(sB), "+s"(spare));
            const LAS char* pw = lds + L_STG + w * 16384 + (4 * (g >> 1) + (g & 1) + 2 * (q4 >> 1) + 16 * (q4 & 1)) * 64 + (l & 3) * 8;
#pragma unroll
            for (int p = 0; p < 6; ++p) {
                if (p < 5) WAITV(8); else WAITV(0);
#pragma unroll
                for (int k = 0; k < 2; ++k) wt[(2 * p + k + 4) % 12] = cat8(rd_w<2048, 512, 2560>(pw + (p & 1) * 8192 + k * 4096), rd_w<2048, 512, 2560>(pw + (p & 1) * 8192 + k * 4096 + 32));
                WAITL(0);
                if (p < 4) issue(p + 2);
                if (p == 1) { RBAR(); b0 = ((volatile LAS unsigned*)(lds + L_MISC))[3]; }
                if (p == 3) BC_LD(b1, rsc);
            }
#pragma unroll
            for (int k = 0; k < 16; k += 4) {
                m[k] = __builtin_amdgcn_perm(mr[k + 1], mr[k], 0x05040100u); m[k + 1] = __builtin_amdgcn_perm(mr[k + 3], mr[k + 2], 0x05040100u);
                m[k + 2] = __builtin_amdgcn_perm(mr[k + 1], mr[k], 0x07060302u); m[k + 3] = __builtin_amdgcn_perm(mr[k + 3], mr[k + 2], 0x07060302u);
            }
        const unsigned pq = nearo(H3_OFF + (c >> 5) * 16384) + w * 1024 + l * 16;
        const v8u va = {idx, pq, 0, 0, lb + L_MISC + w * 4, lb + L_MISC, 0, pq + 16384};
        sC = v4u{lb + w * 2176u, lb + w * 1280u, 0, 0};
        HALVES();
        sC[2] = h * 16384 + (KVS(a->ms >> (j < 2 ? 2 : 4), h) ^ sB[4] ^ 8192) + ((j ^ h) & 1) * 4096 + w * 1024;
        if (j >= 2) { sA[12] = sB[0]; sA[13] = sB[1]; }
        START();
        asm volatile(ASM_A ".Lfunc_fin:\n" : S_IO, "+{v[192:207]}"(m)
                     : "{v[0:31]}"(cat16(wt[0], wt[1])), "{v[32:63]}"(cat16(wt[2], wt[3])), "{v[64:95]}"(cat16(wt[4], wt[5])), "{v[96:127]}"(cat16(wt[6], wt[7])),
                       "{v[128:159]}"(cat16(wt[8], wt[9])), "{v[160:191]}"(cat16(wt[10], wt[11])), "{v[232:239]}"(va)
                     : S_CLOB, "v208", "v209", V8(21), V8(22), "v230", "v231", "v240", "v241", "v244", "v245", "v246", "v247");
        FIN();
    } else {
        LANE();
        const int bw = w - 4, l7 = l & 7;
        MK_S();
        BC_LD(b0, rsc);
        auto ldw = [&]() {
            const v4u rw = mkrs(a->Wo, HD * DM * 2);
            const unsigned vo = (l >> 3) * (DM * 2) + l7 * 16, sw = (h * DH * DM + 256 * j + 64 * bw) * 2;
#pragma unroll
            for (int i = 0; i < 16; ++i) DMA16N(rw, vo, sw + i * 8 * (DM * 2), lbw + i * 1024); };
        if (w != 7) ldw();
        else {
            asm volatile("s_waitcnt vmcnt(0)" : "+v"(b0) :: "memory");
            if (l < 24) ((LAS unsigned*)(lds + L_MISC))[l] = l ? (l == 3 ? b0 : 0) : rt32() / sB[14];
            WAITL(0);
            ldw();
        }
        RBAR();
        BC_LD(b1, rsc);
        WAITV(0);
        HALVES();
        const LAS char* pw = lds + L_STG + w * 16384 + (16 * (g & 1) + 8 * (g >> 1) + (q4 & 1) + 4 * (q4 >> 1)) * 128 + (l & 3) * 8;
#define RDO(o) rd_w<256, 4096, 4352>(pw + (o))
        const v32u wo0 = cat16(cat8(RDO(0), RDO(32)), cat8(RDO(64), RDO(96))), wo1 = cat16(cat8(RDO(8192), RDO(8224)), cat8(RDO(8256), RDO(8288)));
        const unsigned c3 = ((c >> 3) & 3) * 4096 + (c >> 5) * 1024 + (c & 7) * 128, h1 = nearo(H1_OFF + h * 8192), h1j = h1 + j * 2048;
        v16u va = {};
        va[0] = idx; va[1] = h1 + (g >> 1) * 2048 + 1280 + (l & 31) * 16;
        va[2] = nearo(H2_OFF + (32 * j + 8 * bw + q4 * 2 + (g >> 1)) * 4096) + h * 128 + ((g & 1) * 4 + (l & 3)) * 16;
        va[3] = lb + L_P + g * 320 + l15 * 16;
        va[4] = lb + (l & 1) * X_BLK + (l & 2) * X_FQ + (8 * (g >> 1) + (q4 & 1) + 4 * (q4 >> 1)) * 16 + (g & 1) * 8;
        va[5] = lb + L_MISC;
        v4u sel = {};
        if (w == 4) {
            const int tok = l & 7, u = l >> 5, z = (l >> 3) & 3;
            va[6] = h1 + (z == 0 ? 8 + u : (z == 1 ? 2 * u : (z == 2 ? 4 + 2 * u : 2 * u + 1))) * 128 + tok * 16;
            va[7] = h1 + (5 + 2 * u) * 128 + tok * 16 + (z & 1) * 8;
            va[8] = lb + L_P + ((z & 2) + u) * 320 + (4 * (tok >> 1) + 2 * (z & 1) + (tok & 1)) * 16;
            va[9] = l == 0 ? F_PG + c * 128 : OOB;
            va[10] = lb + L_STG + 65536 + (l >> 2) * 128 + (l & 3) * 8;
            va[11] = side((j * 256 + (h & 15) * 4) * 8192 + (sB[4] ^ WSIDE)) + (h >> 4) * (HD * DM * 2) + l * 16;
            const v4u rt = mkrs(a->wt, HD * DM * 4);
            sA[8] = rt[0]; sA[9] = rt[1]; sA[10] = rt[2];
            *(LAS v4u*)(lds + va[8]) = v4u{POI, POI, POI, POI};
            sC = v4u{__float_as_uint(SCALE * LOG2E), __float_as_uint(SCALE * LOG2E), 0, 0};
        } else if (w == 5) {
            va[6] = lb + L_PQK + (l15 >> 3) * 256 + (l & 1) * 544 + (l7 >> 1) * 64 + g * 16;
            va[7] = (l & 8) ? OOB : h1j + g * 128 + l7 * 16;
            va[8] = ((l & 8) || g < 2) ? OOB : h1j + 1024 + ((g & 1) * 8 + l7) * 16;
            va[9] = (l & 8) ? h * 4096 + j * 512 + (g * 8 + l7) * 16 : OOB;
            va[10] = lb + l * 16;
            sC = v4u{0, 0, 0, 0};
        } else if (w == 6) {
            const int rho = (l >> 1) & 15, tok = 2 * (rho >> 2) + (rho & 1), c4 = 2 * (l >> 5) + ((rho >> 1) & 1);
            va[6] = lb + ((l & 1) * 160 + (tok & 1) * 72 + (tok >> 1) * 16 + c4 * 4) * 4;
            va[7] = (l & 1) ? OOB : h1j + 1280 + (l >> 1) * 16;
            va[8] = lb + L_VT + tok * 64 + (l & 1) * 32 + c4 * 8;
            va[9] = lb + L_VT + q4 * 64 + (g & 1) * 32 + (l & 3) * 8;
            va[10] = l < 32 ? h * 4096 + 2048 + (32 * j + l) * 16 : OOB;
            va[11] = nearo(H2_OFF + c * 4096) + l * 16;
            va[12] = (l & 1) ? OOB : nearo(H3_OFF + (2 * (g >> 1) + (l15 >> 3)) * 16384 + c3) + ((l7 & 6) + (g & 1)) * 16;
#pragma unroll
            for (int i = 0; i < 4; ++i) sel[i] = (l7 == 2 * i ? 0x3f80u : 0u) | (l7 == 2 * i + 1 ? 0x3f800000u : 0u);
            sC = v4u{0, 0, 0, 0};
        } else {
            const rsrc_t rk = mkrsrc(a->qkv + QSZ, QSZ), rv = mkrsrc(a->qkv + 2 * (size_t)QSZ, QSZ);
            wait_flags(mkrsrc(a->ctl, CTL_SZ), fw(l & 1, h) + 12, 1, a->ctl);
            v4u pf[10];
            const unsigned hb0 = h * 16384 + (KVS(a->ms >> 4, h) ^ sB[4] ^ 8192), kx = j * 2048 + (KVS(a->ms >> 4, h) ^ KVS(a->ms >> 2, h));
#pragma unroll
            for (int i = 0; i < 8; ++i) pf[i] = LDB(rv, i, hb0);
            pf[8] = LDB(rk, 0, hb0 ^ kx); pf[9] = LDB(rk, 1, hb0 ^ kx);
            va[13] = LD4(mkrsrc(a->ctl, CTL_SZ), fw(l & 1, h) + 12, 0);
#pragma unroll
            for (int i = 0; i < 8; ++i) *(LAS v4u*)(lds + L_X + i * X_BLK + g * X_FQ + l15 * 16) = pf[i];
            *(LAS v4u*)(lds + L_K + l * 16) = pf[8]; *(LAS v4u*)(lds + L_K + 1024 + l * 16) = pf[9];
            FENCE(); ((volatile LAS unsigned*)(lds + L_MISC))[2] = 1;
            va[6] = l * 16; va[7] = lb + g * X_FQ + l15 * 16;
            va[8] = nearo(H2_OFF + c * 4096) + l * 16; va[9] = h1j + l * 16;
            va[10] = nearo(H3_OFF + (l >> 3) * 16384 + c3) + l7 * 16;
            va[11] = fw(l & 1, h) + 12;
            va[15] = l == 0 && (c & 31) == 0 ? F_CK + 384 + (c >> 5) * 4 : OOB;
            sC = v4u{hb0, __float_as_uint(a->per), 0, kx};
        }
        while (((volatile LAS unsigned*)(lds + L_MISC))[2] == 0) __builtin_amdgcn_s_sleep(1);
        asm volatile(ASM_B : S_IO, "+{v[224:239]}"(va), "+{v[244:247]}"(sel) : "{v[0:31]}"(wo0), "{v[32:63]}"(wo1) : B_CLOB);
        FIN();
    }
}

#define RING_LD "buffer_load_dwordx4 %5, %10, %11, %12 offen offset:%13 sc1\n\t"
#define RING_M(C, W, Z) "v_mfma_f32_16x16x32_bf16 %" #C ", %" #W ", %4, " Z "\n\t"
#define RING_W(C, O) "ds_write_b128 %14, %" #C " offset:" #O "\n\t"
#define RING_OP(K, T) asm volatile(T : "+v"(c[0]), "+v"(c[1]), "+v"(c[2]), "+v"(c[3]), "+v"(ring[(K) & 15]), "+v"(ring[((K) + 15) & 15]) \
    : "v"(wo[(K) & 7][0]), "v"(wo[(K) & 7][1]), "v"(wo[(K) & 7][2]), "v"(wo[(K) & 7][3]), "v"(l16), "s"(rso), "s"(nb[(((K) + 15) >> 2) & 3]), "n"((((K) + 15) & 3) * 1024), "v"(rb) : "memory")
#define RING_1(K, B, O) "buffer_load_dwordx4 %" #K ", %16, %17, %" #B " offen offset:" #O " sc1\n\t"
#define RING_4(K0, K1, K2, K3, B) RING_1(K0, B, 0) RING_1(K1, B, 1024) RING_1(K2, B, 2048) RING_1(K3, B, 3072)
#define RING_ALL(P) P(ring[0]), P(ring[1]), P(ring[2]), P(ring[3]), P(ring[4]), P(ring[5]), P(ring[6]), P(ring[7]), P(ring[8]), P(ring[9]), P(ring[10]), P(ring[11]), P(ring[12]), P(ring[13]), P(ring[14]), P(ring[15])
#define NPROBE 4
static constexpr int RED_B = 65536;
#define OS(k) do {} while (0)

DEV void load_wo(LAS char* lds, v4u rw, unsigned o, int w, int l, v4u (&wo)[8][4]) {
    const unsigned vo = (l >> 3) * (DM * 2) + (((l & 7) ^ ((l >> 3) & 6)) << 4), lb = (unsigned)(size_t)lds + w * 16384;
    auto issue = [&](int p) {
#pragma unroll
        for (int k = 0; k < 8; ++k) DMA16N(rw, vo, o + (64 * p + 8 * k) * (DM * 2), lb + (p & 1) * 8192 + k * 1024); };
    issue(0); issue(1);
    const int row = l >> 2;
#pragma unroll
    for (int p = 0; p < 4; ++p) {
        if (p < 3) WAITV(8); else WAITV(0);
#pragma unroll
        for (int t = 0; t < 4; ++t) {
            const LAS char* q = lds + w * 16384 + (p & 1) * 8192 + row * 128 + (((2 * t + ((l >> 1) & 1)) ^ (row & 6)) << 4) + (l & 1) * 8;
            wo[2 * p][t] = cat(trd(q), trd(q + 2048)); wo[2 * p + 1][t] = cat(trd(q + 4096), trd(q + 6144));
        }
        WAITL(0);
        if (p < 2) issue(p + 2);
    }
}

DEV void worker(LAS char* lds, const int wk, const int w) {
    KArgs a = args();
    const int tid = threadIdx.x, l = tid & 63, fr = l & 15;
    const int g = wk >> 5, i = wk & 31;
    const rsrc_t rc = mkrsrc(a->ctl, CTL_SZ);
    unsigned* err = a->ctl;
    unsigned* dbg = a->dbg + 32768 + wk * 32;
    WSTAMP(0);
    const v4u rx = mkrs(a->x, S_TOK * DM * 2);
    const int role = i >> 3;
    const int h = 8 * g + (i & 7);
    const unsigned me = F_W + wk * 128;
    unsigned far = 0;
    v4u rsc = mkrs(a->ctl, CTL_SZ);
    const unsigned t0 = rt32();
    if (role >= 2) {
        const rsrc_t rr = mkrsrc(a->ring, NSLOT * SLOT);
        unsigned b0, ak = 1;
        BC_LD(b0, rsc);
        if (role == 2) {
#pragma unroll
            for (int k = 0; k < 14; ++k) ST16(rr, (h >> 4) * (unsigned)SLOT + ((h & 15) * 14 + k) * 8192 + tid * 16, 0, (v4u{POI, POI, POI, POI}));
#pragma unroll
            for (int k = 0; k < 4; ++k) ST16(mkrsrc(a->qkv + QSZ, QSZ), ((60 + k) * NH + 16) * 16384 + h * 8192 + tid * 16, 0, (v4u{POI, POI, POI, POI}));
            ST16(mkrsrc(a->mb, NSEG * NH * 4096), 62 * NH * 4096 + h * 8192 + tid * 16, 0, (v4u{POI, POI, POI, POI}));
        } else if (wk == 127) {
#pragma unroll
            for (int k = 0; k < 8; ++k) ST16(rc, k == 0 && (tid >> 3) == F_BC / 128 ? OOB : k * 8192 + tid * 16, 0, (v4u{0, 0, 0, 0}));
        } else if (i < 28 && w == 0) {
            unsigned t[2] = {~0u, ~0u};
            for (int it = 0; it < NPROBE; ++it) {
#pragma unroll
                for (int p = 0; p < 2; ++p) { const unsigned c0 = cyc32(); ST4(rr, (i & 3) * (unsigned)SLOT + p * 8192 + nearo(PR_OFF) + g * 256 + (l & 31) * 4, 0, c0); WAITV(0); t[p] = min(t[p], cyc32() - c0); }
            }
            ak = 256 + min(max((int)(t[1] - t[0]), -100), 100);
        }
        if (i >= 28 && i < 31 && (a->pf & 2)) {
            const unsigned u = (i & 3) * 8 + w, ro = (320 + (u >> 1) * 16 + (u & 1) * 512 + (l >> 2)) * (HD * 2) + g * 2048 + (l & 1) * 128 + (u < (a->pf >> 4) ? OOB : 0);
#pragma unroll
            for (int m = 0; m < 2; ++m) {
                const v4u rp = mkrs(m ? a->Wv : a->Wk, DM * HD * 2);
                const unsigned ps = ((rp[0] >> 7) + g * 16) & 63;
                asm volatile("s_mov_b32 m0, %0\n\ts_nop 0\n\tbuffer_load_dword %1, %3, 0 offen lds\n\tbuffer_load_dword %2, %3, 0 offen lds"
                             :: "s"((unsigned)(size_t)lds), "v"(ro + ((3 - ps) & 6) * 128 + (l & 2) * 512), "v"(63 - ps < 16 && !(l & 2) ? ro + ((63 - ps) & 14) * 128 : OOB), "s"(rp) : "memory", "m0");
            }
        }
        asm volatile("s_waitcnt vmcnt(0)\n\ts_barrier" : "+v"(b0) :: "memory");
        WSTAMP(1);
        if (w == 0) {
            if (wk != 127) beacon(rsc, b0, b0, 0);
            ST4(rc, l ? OOB : (wk == 127 ? F_BC : me), 0, ak);
            if (wk == 127) WAITV(0);
        }
        RBAR();
        const unsigned d = role == 2 ? a->wq : (wk == 127 ? 0 : a->wx);
        while (rt32() - t0 < d) __builtin_amdgcn_s_sleep(8);
#pragma unroll 1
        for (int k = role == 3 ? (wk == 127 && w < 2 ? 2 * w : 1) : 0; k > 0; --k) prep_rows(a->x, a->gw, a->w_r, rc, h * 64 + (w + 1 - k) * 8, l);
        if (w == 0 && wk == 127) {
            unsigned bc = 0, tl = rt32();
            for (unsigned tries = 0; tries < SPIN_CAP; ++tries) {
                FENCE();
                if (!(bc & 16)) {
                    unsigned v = LD4(rc, F_W + ((l >> 4) * 32 + 16 + fr) * 128, 0);
                    if (__builtin_amdgcn_ballot_w64(v == 0 && fr < 12) == 0) {
                        v += __shfl_xor((int)v, 16); v += __shfl_xor((int)v, 32);
                        const int d = (int)v - 1024;
                        int e = d - __shfl_xor(d, 1); e += __shfl_xor(e, 2);
                        bc |= 16 | ((unsigned)(__builtin_amdgcn_ballot_w64((abs(d) > 350 ? d : e) > 0) >> 8) & 15);
                        tl -= 4096;
                    }
                } else if (rt32() - t0 > 2400 && all_ge(LD4(rc, fr == 15 ? F_PG : F_W + ((fr >> 2) * 32 + 28 + (fr & 3)) * 128, 0), 1)) break;
                if (rt32() - tl > (bc & 16 ? 500 : 150)) { bc += 32; ST4(rc, l ? OOB : F_BC, 0, bc); tl = rt32(); }
                __builtin_amdgcn_s_sleep(8);
            }
            WSTAMP(5);
        }
        if (role == 3 && h >= NMOE) while (rt32() - t0 < 2600) __builtin_amdgcn_s_sleep(64);
    }
    if (role == 1) far = stream_qkv<true>(lds, rx, mkrs(a->Wv, DM * HD * 2), mkrsrc(a->qkv + 2 * (size_t)QSZ, QSZ), rc, rsc, true, me + 12, h, KVS(a->ms >> 4, h), a->pf, dbg);
    else if (role < 3) far = stream_qkv<false>(lds, rx, mkrs(role ? a->Wq : a->Wk, DM * HD * 2), mkrsrc(a->qkv + (role ? 0 : QSZ), QSZ), rc, rsc, role == 0, me + 12, h, role ? 0 : KVS(a->ms >> 2, h), a->pf, dbg);
    else if (h < NMOE) {
        const int sel = top8((LAS unsigned*)(lds + L_TOT - 256), rc, err, w, l);
        WSTAMP(26);
        if (h == 0 && tid < TOPC) ST4(mkrsrc(a->idx, 32), tid * 4, 0, sel);
        moe(lds, rx, sel, mkrs(a->wg, DM * FF * 2), mkrs(a->wu, DM * FF * 2), mkrs(a->wd, FF * DM * 2), mkrsrc(a->part, NMOE * TOPC * DM * 4), h);
        if (tid == 0) ST4(rc, me + 4, 0, 1u);
    }
    if (role == 3) far ^= FAR(beacon(rsc, 0, 0, 16));
    WSTAMP(2);
    a = args();
    const rsrc_t ri = mkrsrc(a->idx, 32), ru = mkrsrc(a->upd, TOPC * DM * 4);
    const int cg = role == 3 ? 4 * g + (i & 3) : (role == 2 ? h & 15 : i);
    if (role == 3 && i < 28) {
        const rsrc_t rq = mkrsrc(a->qkv, QSZ), rk = mkrsrc(a->qkv + QSZ, QSZ), rv = mkrsrc(a->qkv + 2 * (size_t)QSZ, QSZ), rm = mkrsrc(a->mb, NSEG * NH * 4096);
        unsigned fl = 0;
        LAS unsigned* cnt = (LAS unsigned*)(lds + L_TOT - 512);
        cnt[lane() & 15] = 0;
        BAR();
#pragma unroll 1
        for (int xs = i & 3; xs < NSEG; xs += 4) {
            if ((xs & 60) == (a->wi & 60)) {
                const int l = lane();
                wait_flags<16>(rc, fw(3, min(l, NMOE - 1)) + 4, 1, err);
                const rsrc_t rp = mkrsrc(a->part, NMOE * TOPC * DM * 4);
                const unsigned t = LD4(ri, w * 4, 0), o = w * 4096 + cg * 256 + (l & 15) * 16;
                v4f p = {0, 0, 0, 0};
#pragma unroll
                for (int q = 0; q < 7; ++q) p += __builtin_bit_cast(v4f, LD16(rp, o + (l >> 4) * 7 * 32768, q * 32768));
                const float r = rbf(sigmoidf_(hi16(LD4(rc, F_GR + t * 8 + 4, 0))));
#pragma unroll
                for (int e = 0; e < 4; ++e) p[e] = rbf(rbf(red_g<false>(p[e])) * r);
                ST16(ru, l < 16 ? o : OOB, 0, __builtin_bit_cast(v4u, p));
            }
            if ((CPF & 2) && (xs == 36 || xs == 57)) code_pf(L_TOT - 1024, w * 64 + lane());
            if ((xs & 60) == a->wi >> 8 && w == 7) {
                const int l = lane();
                const unsigned p = min(LD4(rc, l < 3 ? F_W + (l * 32 + 32 + cg) * 128 + 12 : (l < 5 ? fw(2, 16 * l - 48 + cg) + 12 : OOB), 0), 63u);
                const float tau = (float)((rt32() - t0 - (l < 3 ? 1100 : a->ws)) & 0xffffu) * 8.0f / (p + 1);
                unsigned key = l < 5 ? (unsigned)(tau * (63 - p)) * 8 + l : ~0u;
                const unsigned inc = (unsigned)(tau * (0.01f * (a->gap & 255))) * 8;
                u64 m = l ? 1ull << (59 + l) | 1ull << (56 + ((l + 1) & 3)) : 0;
#pragma unroll 1
                for (int s = 0; s < 56; ++s) {
                    if (s == (a->gap >> 8) && !l) key = ~0u;
                    unsigned u = min(key, ror<4>(key)); u = min(u, ror<2>(u)); u = min(u, ror<1>(u));
                    if (key == __builtin_amdgcn_readlane(u, 7)) { key += inc; m |= 1ull << s; }
                }
                __builtin_amdgcn_raw_buffer_store_b64(__builtin_bit_cast(v2u, m), rc, l < 5 ? me + 16 + l * 8 : OOB, 0, 16);
            }
            fl = xattn(rq, rk, rv, rm, rc, err, xs, 8 * g + w, fl, far, a->ms, cnt, dbg); WSTAMP(9 + (xs >> 2));
        }
        WSTAMP(25);
        return;
    }
    auto proj = [&](const bool rcv) __attribute__((always_inline)) {
    const int l = lane(), tid = w * 64 + l, fr = l & 15, fq = l >> 4;
    a = args();
    const rsrc_t rt = mkrsrc(a->wt, HD * DM * 4);
    const int lim = (a->wi >> 8) + 4;
    const int k0 = (rcv ? 0 : 64) + 8 * w;
    u64 mine = role == 3 ? ~(a->segs | CM << 60) : (g ? CM << 60 : a->segs);
    auto next = [&]() { int s; asm volatile("s_ff1_i32_b64 %0, %1\n\ts_bitset0_b64 %1, %0\n\ts_min_u32 %0, %0, 64" : "=&s"(s), "+s"(mine) :: "scc"); return s; };
    auto fx = [&](int s, int l) { return fw(3, (rcv ? 0 : 16) + (l & 1) * 8 + (s & 3)) + 64 + (s & 60); };
    const unsigned fe = F_EX + cg * 256;
    volatile LAS unsigned* prog = (volatile LAS unsigned*)(lds + 2 * RED_B);
    LAS v4f* upd = (LAS v4f*)(lds + 2 * RED_B + 16384);
    v4u wo[8][4];
    u64 hit = 0;
    int sel = 0;
    if (rcv || role < 3) {
        const unsigned fs = fw(3, 2 * (cg & 12) + (cg & 3));
        const unsigned wb = side((cg * 64 + 8 * w) * 8192 + (far ^ WSIDE ^ 8192)) + (rcv ? 0 : HD * DM * 2);
        const unsigned fo = l & 8 ? fw(3, (l & 4) * 2 + (l & 3)) + 60 + lim : (l & 2 ? F_PG + (k0 + (l & 1) * 4 + (cg >> 2)) * 128 : fs + 64 + (l & 1 ? a->wi & 60 : lim - 4));
        const unsigned ft = (l & 10) == 2 ? WO_DONE : rcv, f0 = LD4(rc, fo, 0);
        if (rcv || !g) { __builtin_amdgcn_s_waitcnt(0x0f70); FENCE(); BAR(); if (tid == 0) ST4(rc, me + 12, 0, NSEG); }
        if (!all_ge(f0, ft)) wait_flags(rc, fo, ft, err);
        FENCE();
        if (rcv) {
            v2u m; v4u u;
            asm volatile("buffer_load_dwordx2 %0, off, %3, %4 sc1\n\tbuffer_load_dword %1, %5, %6, 0 offen sc1\n\tbuffer_load_dwordx4 %2, %7, %8, 0 offen sc1" : "=&v"(m), "=&v"(sel), "=&v"(u)
                         : "s"(mkrs(a->ctl, CTL_SZ)), "s"(fs + 8 + 8 * (role < 2 ? g : 4 + (g >> 1))), "v"((l & 7) * 4), "s"(mkrs(a->idx, 32)),
                           "v"(w < 2 ? (tid >> 4) * 4096 + cg * 256 + fr * 16 : OOB), "s"(mkrs(a->upd, TOPC * DM * 4)) : "memory");
#define WO_1(K, O) "buffer_load_dwordx4 %" #K ", %4, %5, %6 offen offset:" #O " sc1\n\t"
#pragma unroll
            for (int k = 0; k < 8; ++k) asm volatile(WO_1(0, 0) WO_1(1, 1024) WO_1(2, 2048) WO_1(3, 3072) : "=&v"(wo[k][0]), "=&v"(wo[k][1]), "=&v"(wo[k][2]), "=&v"(wo[k][3])
                                                     : "v"(l * 16), "s"(mkrs(a->wt, HD * DM * 4)), "s"(wb + (k >> 1) * 16384 + (k & 1) * 4096) : "memory");
            asm volatile("s_waitcnt vmcnt(32)" : "+v"(m), "+v"(sel), "+v"(u) :: "memory");
            if (w < 2) upd[tid] = __builtin_bit_cast(v4f, u);
#pragma unroll
            for (int q = 0; q < 8; ++q) hit |= 1ull << (__builtin_amdgcn_readlane(sel, q) >> 5);
            mine = (u64)__builtin_amdgcn_readfirstlane(m[1]) << 32 | (unsigned)__builtin_amdgcn_readfirstlane(m[0]);
        } else {
#pragma unroll
            for (int k = 0; k < 32; ++k) wo[k >> 2][k & 3] = LDB(rt, k & 7, wb + (k >> 3) * 16384);
        }
    } else load_wo(lds, mkrs(a->Wo, HD * DM * 2), (32 * k0 * DM + 64 * cg) * 2, w, l, wo);
    WSTAMP(7);
    if (!rcv) __builtin_amdgcn_s_waitcnt(0x0f70);
    bool pend = false;
    v4u ring[16];
    asm volatile("" : RING_ALL("=v"));
    const v4u rso = mkrs(a->qkv, QSZ), rse = mkrs(a->qkv + QSZ, QSZ);
    rsc = mkrs(a->ctl, CTL_SZ);
    const rsrc_t re = mkrsrc(a->qkv + QSZ, QSZ), rout = mkrsrc(a->out, S_TOK * DM * 2);
    if (!rcv) BAR();
    const unsigned l16 = l * 16, lw = (unsigned)(size_t)lds + w * 1024, r0 = (w >> 2) * 16 + fr;
    const unsigned xo = rcv ? (r0 * DM + 64 * cg + (w & 3) * 16 + 4 * fq) * 2 : OOB, go = F_GR + r0 * 8, so = w ? OOB : (l < 2 ? fx(0, l) : (rcv ? fe : OOB));
    auto state = [&](int n) { return (unsigned)__builtin_amdgcn_ballot_w64(prog[(n & 1) * 2048 + l * 4] != 0); };
    auto sgo = [&](int s) { return s * 0x80800u & 0xfff82000u; };
    const unsigned co = k0 * 4096u + far, ce = (16 + cg) * 16384u + far + w * 1024;
    auto ask1 = [&](int o) { return o * 0x1000081u & (OOB | 0x1bcu); };
    auto ask2 = [&](unsigned u, int e) { unsigned v; asm("s_lshl_b32 %1, %2, 2" : "+s"(u), "=s"(v) : "s"(e) : "scc"); return l16 < 32 ? u : v; };
    auto ask = [&](int o, int e) { return so + ask2(ask1(o), e); };
    auto both = [&](int s, int o) { unsigned t = l16; asm volatile("" : "+v"(t)); const unsigned q = ask(o, s), f = fx(s, t >> 4); return l16 < 64 ? q : f; };
    unsigned st;
    WSTAMP(8);
    v4f r[5];
    const LAS char* rp;
    auto get = [&]() {
#pragma unroll
        for (int q = 0; q < 5; ++q) r[q] = *(const LAS v4f*)(rp + q * 1024); };
    unsigned sm; v2u xv; float gt; v4f pv;
    v4f pw; float tb[4], gw, omg;
#define PIN(...) do { asm volatile("" : __VA_ARGS__); SCHED(); } while (0)
    auto prep = [&](int k, int seg) {
        if (!rcv) return;
        if (k == 0) { gw = hi16(__float_as_uint(gt)); omg = rbf(1.0f - gw); PIN("+v"(gw), "+v"(omg)); }
        if (k == 1) { tb[0] = lo16(xv[0]); tb[1] = hi16(xv[0]); PIN("+v"(tb[0]), "+v"(tb[1])); }
        if (k == 2) { tb[2] = lo16(xv[1]); tb[3] = hi16(xv[1]); PIN("+v"(tb[2]), "+v"(tb[3])); }
        if (k == 3 && seg >= 0) {
            unsigned t = l16, g8 = go; asm volatile("" : "+v"(t), "+v"(g8));
#pragma unroll 1
            for (int q = 0; q < TOPC; ++q) if (__builtin_amdgcn_readlane(sel, q) == seg * 32 + (int)(g8 - F_GR) / 8) {
                const v4f u = upd[q * 16 + (w & 3) * 4 + (t >> 8)];
#pragma unroll
                for (int e = 0; e < 4; ++e) tb[e] = rbf(tb[e] + u[e]); }
        }
        if (k == 4) { tb[0] = mul1(omg, tb[0]); tb[1] = mul1(omg, tb[1]); tb[2] = mul1(omg, tb[2]); PIN("+v"(tb[0]), "+v"(tb[1]), "+v"(tb[2])); }
        if (k == 5) { tb[3] = mul1(omg, tb[3]); tb[0] = rbf(tb[0]); tb[1] = rbf(tb[1]); PIN("+v"(tb[0]), "+v"(tb[1]), "+v"(tb[3])); }
        if (k == 6) { tb[2] = rbf(tb[2]); tb[3] = rbf(tb[3]); PIN("+v"(tb[2]), "+v"(tb[3])); }
        if (k == 7) { pw = pv; PIN("+v"(pw)); } };
    auto fin = [&](int seg, bool got, unsigned eo, unsigned sf) {
        v4f acc = r[0];
#pragma unroll
        for (int q = 1; q < 8; ++q) { acc = add4(acc, r[q < 5 ? q : q - 4]); SCHED(); if (q < 4) r[q] = *(const LAS v4f*)(rp + (q + 4) * 1024); }
        if (rcv) {
            if (!pend) asm volatile("s_waitcnt vmcnt(0)" : "+v"(pw) :: "memory");
            if (!got) {
                for (unsigned tries = 0, f = 0;; ++tries) {
                    const v4u u = __builtin_bit_cast(v4u, pw);
                    if (f || (seg >= 60 && all_ge(~max(max(u[0], u[1]), max(u[2], u[3])), 1))) break;
                    if (tries && spin_fail(tries, err)) break;
                    FENCE(); f = __builtin_amdgcn_readfirstlane(sf);
                    if (tries && !f) __builtin_amdgcn_s_sleep(3);
                    sf = LD4(rc, fe + seg * 4, 0); pw = __builtin_bit_cast(v4f, LD16(re, l16, f || seg >= 60 ? eo : OOB));
                }
                asm volatile("" : "+v"(pw)); TS(tid == 0, seg, 2);
            }
            acc = add4(acc, pw);
#pragma unroll
            for (int e = 0; e < 4; ++e) acc[e] = add1(rbf(mul1(gw, rbf(acc[e]))), tb[e]);
            __builtin_amdgcn_raw_buffer_store_b64(v2u{pk2(acc[0], acc[1]), pk2(acc[2], acc[3])}, rout, xo, seg << 16, 0);
        } else ST16(re, l16, eo, __builtin_bit_cast(v4u, acc));
        FENCE(); };
    asm volatile("" : "=v"(sm), "=v"(pv), "=v"(xv), "=v"(gt));
    bool got = false;
#define SM_LD "buffer_load_dword %[sm], %[so], %[rc], 0 offen sc1\n\tbuffer_load_dwordx4 %[pv], %[l16], %[re], %[eo] offen sc1\n\tbuffer_load_dwordx2 %[xv], %[xo], %[rx], %[xs] offen\n\tbuffer_load_dword %[gt], %[go], %[rc], %[gs] offen sc1\n\t"
#define SM_O [sm] "+v"(sm), [pv] "+v"(pv), [xv] "+v"(xv), [gt] "+v"(gt)
#define SM_I(QA) [rc] "s"(rsc), [re] "s"(rse), [rx] "s"(rx), [so] "v"(QA), [l16] "v"(l16), [eo] "s"(got || seg >= 60 ? eo : OOB), [xo] "v"(xo), [xs] "s"(seg << 16), [go] "v"(go), [gs] "s"(seg << 8)
    int seg = next(), s1 = next(), s2 = next(), s3;
    unsigned o1 = sgo(s1) ^ co, eo = sgo(seg) ^ ce, e1 = 0, qa;
    ring[0][0] = l16 == 32 ? 0 : (l16 < 32 ? s1 : seg) < lim;
#pragma unroll 1
    for (int n = 0, p1 = OOB / 4, p2 = p1, p3 = p1; seg < NSEG; ++n, p3 = p2, p2 = p1, p1 = seg, seg = s1, s1 = s2, s2 = s3) {
        if (!pend) {
            const unsigned ob = eo ^ ce ^ co;
            asm volatile("" : "+v"(ring[0]) :: "memory");
            unsigned v = ring[0][0];
            asm volatile("" : "+v"(v));
            const bool now = rcv && (__builtin_amdgcn_ballot_w64(v != 0) & 48) == 48;
#define RING_GO(T) asm volatile(RING_4(0, 1, 2, 3, 18) RING_4(4, 5, 6, 7, 19) RING_4(8, 9, 10, 11, 20) RING_4(12, 13, 14, 15, 21) T \
                         : RING_ALL("+v") : "v"(l16), "s"(rso), "s"(ob), "s"(ob ^ 16384), "s"(ob ^ 4096), "s"(ob ^ 20480) : "memory")
            for (unsigned tries = 0; !now; ++tries) {
                FENCE(); v = LD4(rc, both(seg, s1), 0); __builtin_amdgcn_s_waitcnt(0x0f70); FENCE();
                if (!rcv && !tries && n) { RBAR(); if (tid < 3) ST4(rc, fe + (tid ? (tid == 1 ? p2 : p3) : p1) * 4, 0, 1u); TS(tid == 0, p1, 2); }
                if ((__builtin_amdgcn_ballot_w64(v != 0) & 48) == 48) break;
                __builtin_amdgcn_s_sleep(ZP);
                if (spin_fail(tries, err)) break;
            }
            TS(tid == 0, seg, 0); RING_GO("");
            prog[(~n & 1) * 2048 + tid * 4] = v;
            BAR();
            st = state(n + 1); got = st & 4;
            asm volatile(SM_LD : SM_O : SM_I(ask(s2, seg)) : "memory");
            ST4(rc, OOB, 0, 0u);
        }
        const bool pre = (st & 3) == 3;
        unsigned nb[4], rb = lw + (n & 1) * RED_B + l16, pa = lw + 2 * RED_B + (n & 1) * 8192 + l16;
        nb[0] = pre ? o1 : OOB; nb[1] = nb[0] ^ 16384; nb[2] = nb[0] ^ 4096; nb[3] = nb[0] ^ 20480;
        v4f c[4];
        unsigned en = o1 ^ co ^ ce, u;
        int sp;
        asm volatile("" : "+s"(nb[0]), "+s"(nb[1]), "+s"(nb[2]), "+s"(nb[3])); asm volatile("" : "+v"(rb), "+v"(pa), "=v"(c[0]), "=v"(c[1]), "=v"(c[2]), "=v"(c[3]));
        OS(0);
        if (pend || (pre && w >= 4)) BAR();
        OS(1);
        RING_OP(0, "s_setprio 0\n\ts_waitcnt vmcnt(20)\n\t" RING_M(0, 6, "0") RING_M(1, 7, "0") "s_waitcnt vmcnt(19)\n\t" RING_M(2, 8, "0") RING_M(3, 9, "0"));
#pragma unroll
        for (int k = 1; k < 16; ++k) {
            if (k == 8) RING_OP(k, RING_LD "s_nop 3\n\t" RING_W(0, 0) RING_M(0, 6, "0") RING_W(1, 8192) RING_M(1, 7, "0") RING_W(2, 16384) RING_M(2, 8, "0") RING_W(3, 24576) RING_M(3, 9, "0") "s_waitcnt vmcnt(19)");
            else if (k > 5 && (k < 13 || (k == 14 && !rcv))) RING_OP(k, RING_M(0, 6, "%0") RING_LD RING_M(1, 7, "%1") "s_waitcnt vmcnt(19)\n\t" RING_M(2, 8, "%2") RING_M(3, 9, "%3"));
            else {
                if (k == 15) { RING_OP(k, RING_M(0, 6, "%0") RING_LD RING_M(1, 7, "%1")); } else RING_OP(k, RING_M(0, 6, "%0") RING_LD RING_M(1, 7, "%1") "s_waitcnt vmcnt(19)");
                SCHED();
                if (k == 13) { asm volatile("s_waitcnt vmcnt(14)\n\tds_write_b32 %4, %0" : SM_O : "v"(pa) : "memory"); SCHED(); }
                if (k >= 14 && PIPE(3 * k - 40)) prep(3 * k - 40, sp);
                RING_OP(k, RING_M(2, 8, "%2")); SCHED();
                if (k == 1) s3 = next();
                if (k == 2) { qa = ask2(u, s1); PIN("+v"(qa)); }
                if (k == 3) { u = s2; PIN("+s"(u)); o1 = sgo(u) ^ co; PIN("+s"(o1)); }
                if (k == 4) { u = n; PIN("+s"(u)); u = (~u & 1) * RED_B; PIN("+s"(u)); }
                if (k == 5 && rcv) asm volatile("s_bitcmp1_b64 %1, %0\n\ts_cselect_b32 %0, %0, -1" : "+s"(sp) : "s"(hit) : "scc");
                if (k >= 13 && PIPE(3 * k - 39)) prep(3 * k - 39, sp);
                RING_OP(k, RING_M(3, 9, "%3")); SCHED();
                if (k == 1) { u = ask1(s3); PIN("+s"(u)); }
                if (k == 2) { qa += so; PIN("+v"(qa)); }
                if (k == 3 && rcv) { sp = pend ? p1 : seg; PIN("+s"(sp)); }
                if (k == 4) { rp = lds + (u + w * 8192 + l16); PIN("+v"(rp)); }
                if (k >= 13 && PIPE(3 * k - 38)) prep(3 * k - 38, sp);
            }
            if (k == 8 && !pre && rcv) asm volatile("buffer_load_dwordx4 %0, %1, %2, 0 offen sc1" : "+v"(ring[0]) : "v"(both(s1, s2)), "s"(rsc) : "memory");
        }
        RING_OP(16, RING_LD "s_setprio 1");
        OS(2); TS(tid == 256 && !pre, seg, 1);
        if (pre || pend) BAR();
        OS(3);
        pa -= lw;
        asm volatile("ds_read_b32 %0, %0\n\ts_nop 3\n\tds_write_b128 %5, %1 offset:32768\n\tds_write_b128 %5, %2 offset:40960\n\tds_write_b128 %5, %3 offset:49152\n\tds_write_b128 %5, %4 offset:57344\n\ts_waitcnt lgkmcnt(4)"
                     : "+v"(pa) : "v"(c[0]), "v"(c[1]), "v"(c[2]), "v"(c[3]), "v"(rb) : "memory");
        OS(4);
#pragma unroll
        for (int k = 0; k < 8; ++k) if (!PIPE(k)) prep(k, sp);
        if (pre || pend) {
            get();
            const bool got1 = got;
            st = __builtin_amdgcn_ballot_w64(pa != 0); got = st & 4;
            asm volatile(SM_LD : SM_O : SM_I(qa) : "memory");
            OS(5);
            if (pend) fin(p1, got1, e1, 0); else ST4(rc, OOB, 0, 0u);
            if (!rcv && tid == 256) ST4(rc, fe + p3 * 4, 0, 1u);
        }
        OS(6);
        if (!pre) {
#define PW_LD() asm volatile("buffer_load_dwordx4 %0, %1, %2, %3 offen sc1" : "+v"(pw) : "v"(l16), "s"(rse), "s"(eo) : "memory")
            const bool again = rcv && !got && !pend;
            if (again && seg >= 60) PW_LD();
            BAR();
            if (again && seg < 60) { got = state(n) & 4; if (got) PW_LD(); }
            if (pend && w < 4) RBAR();
            rp = lds + ((n & 1) * RED_B + w * 8192 + l16);
            get();
            if (pend) {
                asm volatile("s_waitcnt vmcnt(1)" : SM_O :: "memory");
#pragma unroll
                for (int k = 0; k < 8; ++k) prep(k, (hit >> seg) & 1 ? seg : -1);
            }
            fin(seg, got, eo, 0);
            if (!rcv && s1 == NSEG) { WAITV(0); RBAR(); if (tid < 3) ST4(rc, fe + (tid ? (tid == 1 ? p1 : p2) : seg) * 4, 0, 1u); }
        }
        TS(tid == 0 && !pre, seg, 3);
        pend = pre; e1 = eo; eo = en;
        if (!rcv && (seg & 12) == 12) WSTAMP(9 + (seg >> 2));
    }
    asm volatile("s_waitcnt vmcnt(0)" :: RING_ALL("v") : "memory");
#pragma unroll
    for (int k = 0; k < 8; ++k) asm volatile("" :: "v"(wo[k][0]), "v"(wo[k][1]), "v"(wo[k][2]), "v"(wo[k][3]));
    };
    if (role == 2 || (role < 2 && g)) proj(true);
    if (role == 3 || (role < 2 && g < 2)) proj(false);
    WSTAMP(25);
}

__global__ void __launch_bounds__(512, 2) k_main(Args) {
    __shared__ __attribute__((aligned(16))) char lds[L_TOT];
    asm volatile(".Lfunc_beg:");
    const int b = blockIdx.x, c = (b & 3) * 32 + (b >> 3), w = __builtin_amdgcn_readfirstlane(threadIdx.x >> 6);
    code_pf(L_PF + ((b & 7) < 4 ? 0 : L_TOT - 1024 - L_PF), threadIdx.x);
    if ((b & 7) < 4) chain((LAS char*)lds, c, w); else worker((LAS char*)lds, c, w);
}

#define RING_MB 160
#define QKV_MB 0
#define TUNE_DEFAULT {184, 73, 77, 138, 11, 80, 344, 100, WQ, 600, 7, 20, 214, 32, 32, 20}

extern "C" void kernel_launch(void* const* d_in, const int* in_sizes, int n_in, void* d_out, int out_size, void* d_ws, size_t ws_size, hipStream_t stream) {
    char* ring = (char*)(((size_t)d_ws + SLOT - 1) & ~(size_t)(SLOT - 1));
    char* ws = ring + NSLOT * SLOT; size_t off = 0;
    if (ws_size >= (256u << 20)) { ring += (size_t)RING_MB << 20; off = (size_t)QKV_MB << 20; }
    auto alloc = [&](size_t bytes) { void* p = ws + off; off += (bytes + 255) & ~(size_t)255; return p; };
    Args a;
    a.x = (const bf16_t*)d_in[0]; a.mem0 = (const bf16_t*)d_in[1]; a.Wq = (const bf16_t*)d_in[2]; a.Wk = (const bf16_t*)d_in[3]; a.Wv = (const bf16_t*)d_in[4]; a.Wo = (const bf16_t*)d_in[5];
    a.w_r = (const bf16_t*)d_in[8]; a.wg = (const bf16_t*)d_in[9]; a.wu = (const bf16_t*)d_in[10]; a.wd = (const bf16_t*)d_in[11]; a.gw = (const bf16_t*)d_in[12];
    a.qkv = (char*)alloc(3 * (size_t)QSZ); a.wt = (char*)alloc(HD * DM * 4); a.mb = (char*)alloc(NSEG * NH * 4096);
    a.idx = (int*)alloc(256); a.part = (float*)alloc(NMOE * TOPC * DM * 4); a.upd = (float*)alloc(TOPC * DM * 4); a.out = (bf16_t*)d_out;
    a.ring = ring; a.ctl = (unsigned*)alloc(CTL_SZ); a.dbg = (unsigned*)alloc((65536 + 1024 * 512) * 4);
    const int tn[16] = TUNE_DEFAULT;
    a.per = tn[0] * 368.0f; a.per0 = tn[12]; a.pf = tn[10]; a.ms = tn[11]; a.ws = tn[8] + 950; a.wi = tn[15] | tn[13] << 8; a.gap = 135 | CSD << 8;
    a.d1 = ((u64)tn[1] << 32) / tn[0]; a.dv = ((u64)tn[2] << 32) / tn[0]; a.d2 = ((u64)tn[3] << 32) / tn[0]; a.dp = tn[4]; a.slack = tn[5]; a.thr = tn[6]; a.spare = tn[7]; a.wq = tn[8]; a.wx = tn[9];
    a.segs = (CM & 1 ? 0x2555555555555555ull : 0x5555555555555555ull) >> tn[14] << tn[14];
    k_main<<<256, 512, 0, stream>>>(a);
}
```
